```python
import torch

def _softmax(x, axis=-1):
    return torch.softmax(x, dim=axis)

def _gelu(x):
    return torch.nn.functional.gelu(x, approximate='tanh')

def _silu(x):
    return torch.nn.functional.silu(x)

def _sigmoid(x):
    return torch.sigmoid(x)

def _layernorm(x, axis=-1, eps=1e-05):
    m = x.mean(dim=axis, keepdim=True)
    v = x.var(dim=axis, keepdim=True, unbiased=False)
    return (x - m) / torch.sqrt(v + eps)

def _scatter_add(tgt, idx, src, axis=0):
    return tgt.clone().index_add_(axis, idx, src)

def _segment_sum(src, seg, n):
    out = torch.zeros((n,) + src.shape[1:], dtype=src.dtype)
    return out.index_add_(0, seg, src)

def _pad(x, pw):
    flat = tuple((v for pair in reversed(pw) for v in pair))
    return torch.nn.functional.pad(x, flat)
kernel_name = 'sequential_preLN__memory_aug_recurrent_mem_tr__moe_arch_mod__prompt'

def setup_inputs():

    _w = lambda a: (lambda t: t.to(torch.bfloat16) if t.is_floating_point() else t)(torch.as_tensor(a))

    def _A_setup():
        B, S, Dm = (1, 2048, 512)
        H, Dh = (32, 128)
        M, seg = (8, 32)
        seg = min(seg, S)
        S = S // seg * seg
        return {'x': _w((lambda *s: torch.randn(*s, dtype=torch.bfloat16) * 0.05)(B, S, Dm)), 'mem0': _w((lambda *s: torch.randn(*s, dtype=torch.bfloat16) * 0.2)(B, M, Dm)), 'Wq': _w((lambda *s: torch.randn(*s, dtype=torch.bfloat16) * 1.6)(Dm, H, Dh) * Dm ** (-0.5)), 'Wk': _w((lambda *s: torch.randn(*s, dtype=torch.bfloat16) * 1.6)(Dm, H, Dh) * Dm ** (-0.5)), 'Wv': _w((lambda *s: torch.randn(*s, dtype=torch.bfloat16) * 0.2)(Dm, H, Dh) * Dm ** (-0.5)), 'Wo': _w((lambda *s: torch.randn(*s, dtype=torch.bfloat16) * 0.2)(H, Dh, Dm) * (H * Dh) ** (-0.5)), 'seg_len': seg}
    _A_in = _A_setup()

    def _B_setup():
        B, S, D, F, C = (1, 2048, 512, 1792, 2)
        return {'x': _w((lambda *s: torch.randn(*s, dtype=torch.bfloat16) * 0.05)(B, S, D)), 'w_r': _w((lambda *s: torch.randn(*s, dtype=torch.bfloat16) * 0.05)(D)), 'w_gate': _w((lambda *s: torch.randn(*s, dtype=torch.bfloat16) * 0.05)(D, F)), 'w_up': _w((lambda *s: torch.randn(*s, dtype=torch.bfloat16) * 0.05)(D, F)), 'w_down': _w((lambda *s: torch.randn(*s, dtype=torch.bfloat16) * 0.05)(F, D))}
    _B_in = _B_setup()
    return {'A_x': _A_in['x'], 'A_mem0': _A_in['mem0'], 'A_Wq': _A_in['Wq'], 'A_Wk': _A_in['Wk'], 'A_Wv': _A_in['Wv'], 'A_Wo': _A_in['Wo'], 'A_seg_len': _A_in['seg_len'], 'B_x': _B_in['x'], 'B_w_r': _B_in['w_r'], 'B_w_gate': _B_in['w_gate'], 'B_w_up': _B_in['w_up'], 'B_w_down': _B_in['w_down']}

def reference(A_x, A_mem0, A_Wq, A_Wk, A_Wv, A_Wo, A_seg_len, B_x, B_w_r, B_w_gate, B_w_up, B_w_down):

    def _A_ref(x, mem0, Wq, Wk, Wv, Wo, seg_len):
        B, S, Dm = x.shape
        H, Dh = (Wq.shape[1], Wq.shape[2])
        M = mem0.shape[1]
        scale = Dh ** (-0.5)
        n_seg = S // seg_len
        L = M + seg_len
        idx = torch.arange(L)
        mask = (idx[:, None] < M) | (idx[:, None] >= idx[None, :])
        mem = mem0
        outs = []
        for s in range(n_seg):
            xs = x[:, s * seg_len:(s + 1) * seg_len, :].clone()
            h = torch.concatenate([mem, xs], axis=1)
            q = torch.einsum('btd,dhe->bhte', h, Wq)
            k = torch.einsum('btd,dhe->bhte', h, Wk)
            v = torch.einsum('btd,dhe->bhte', h, Wv)
            scores = torch.einsum('bhqe,bhke->bhqk', q, k) * scale
            scores = torch.where(mask[None, None], scores, -1000000000.0)
            p = _softmax(scores, axis=-1)
            o = torch.einsum('bhqk,bhke->bhqe', p, v)
            o = torch.einsum('bhte,hed->btd', o, Wo)
            mem = o[:, :M, :].clone()
            outs.append(o[:, M:, :])
        return torch.concatenate(outs, axis=1)

    def _B_ref(x, w_r, w_gate, w_up, w_down):
        B, S, D = x.shape
        C = 2
        r = x @ w_r
        idx = torch.argsort(-r, axis=-1)[:, :C]
        idx_e = (lambda x, n, axis: torch.repeat_interleave(x, n, dim=axis))(idx.reshape(B, C, 1), D, axis=2)
        x_sel = (lambda x, i, axis: torch.take_along_dim(x, i, dim=axis))(x, idx_e, axis=1)
        g = _silu(x_sel @ w_gate)
        u = x_sel @ w_up
        y = g * u @ w_down
        r_sel = _sigmoid((lambda x, i, axis: torch.take_along_dim(x, i, dim=axis))(r, idx, axis=1))
        y = y * r_sel.reshape(B, C, 1)
        b_off = (torch.arange(B) * S).reshape(B, 1)
        flat_idx = (idx + b_off).reshape(B * C)
        zero = (x * 0.0).reshape(B * S, D)
        upd = _scatter_add(zero, flat_idx, y.reshape(B * C, D), axis=0)
        return x + upd.reshape(B, S, D)
    x = A_x
    h = x + _A_ref(_layernorm(x, axis=-1), A_mem0, A_Wq, A_Wk, A_Wv, A_Wo, A_seg_len)
    h2 = h + _B_ref(_layernorm(h, axis=-1), B_w_r, B_w_gate, B_w_up, B_w_down)
    return h2
```

```cpp
#include <hip/hip_runtime.h>
#include <hip/hip_bf16.h>

typedef unsigned short u16;
typedef unsigned long long u64;
typedef __attribute__((ext_vector_type(8))) short bf16x8;
typedef __attribute__((ext_vector_type(4))) float f32x4;
typedef __attribute__((ext_vector_type(4))) unsigned u32x4;
#define GAS __attribute__((address_space(1)))
typedef GAS u64 gu64;
typedef GAS unsigned gu32;
#define RLX_AGENT __ATOMIC_RELAXED, __HIP_MEMORY_SCOPE_AGENT

constexpr int S_ = 2048, DM = 512, NH = 32, DH = 128, MM = 8, SEG = 32, NSEG = 64;
constexpr int HD = NH * DH;
constexpr int QKVN = 3 * HD;
constexpr int FF = 1792;

__device__ __forceinline__ float bf2f(u16 v) { return __builtin_bit_cast(float, (unsigned)v << 16); }
__device__ __forceinline__ float rbf(float f) { return (float)(__bf16)f; }

template <int CTRL> __device__ __forceinline__ float dpp_add(float v) {
  return v + __builtin_bit_cast(float, __builtin_amdgcn_update_dpp(0, __builtin_bit_cast(int, v), CTRL, 0xf, 0xf, false));
}
__device__ __forceinline__ float wave_sum(float v) {
  v = dpp_add<0x121>(v); v = dpp_add<0x122>(v); v = dpp_add<0x124>(v); v = dpp_add<0x128>(v);
  auto r = __builtin_amdgcn_permlane16_swap(__builtin_bit_cast(unsigned, v), __builtin_bit_cast(unsigned, v), false, false);
  const unsigned r0 = r[0], r1 = r[1];
  v = __builtin_bit_cast(float, r0) + __builtin_bit_cast(float, r1);
  auto q = __builtin_amdgcn_permlane32_swap(__builtin_bit_cast(unsigned, v), __builtin_bit_cast(unsigned, v), false, false);
  const unsigned q0 = q[0], q1 = q[1];
  return __builtin_bit_cast(float, q0) + __builtin_bit_cast(float, q1);
}
typedef __bf16 bf16x2_t __attribute__((ext_vector_type(2)));
typedef float f32x2_t __attribute__((ext_vector_type(2)));
__device__ __forceinline__ unsigned pk2(float a, float b) {
  f32x2_t v; v.x = a; v.y = b;
  return __builtin_bit_cast(unsigned, __builtin_convertvector(v, bf16x2_t));
}
__device__ __forceinline__ void rbf2(float& a, float& b) {
  const unsigned p = pk2(a, b);
  a = __builtin_bit_cast(float, p << 16); b = __builtin_bit_cast(float, p & 0xffff0000u);
}
__device__ __forceinline__ void ln_row(const float (&x)[8], float (&y)[8]) {
  float s = ((x[0] + x[1]) + (x[2] + x[3])) + ((x[4] + x[5]) + (x[6] + x[7]));
  s = wave_sum(s);
  const float mean = s * (1.0f / DM);
  float q = 0.f;
#pragma unroll
  for (int i = 0; i < 8; ++i) { float d = x[i] - mean; q += d * d; }
  q = wave_sum(q);
  float var = q * (1.0f / DM), mb = mean;
  rbf2(var, mb);
  const float sd = rbf(__builtin_amdgcn_sqrtf(rbf(var + 1e-5f)));
  const float inv = __builtin_amdgcn_rcpf(sd);
#pragma unroll
  for (int i = 0; i < 8; i += 2) {
    float a = x[i] - mb, b = x[i + 1] - mb;
    rbf2(a, b);
    a *= inv; b *= inv;
    rbf2(a, b);
    y[i] = a; y[i + 1] = b;
  }
}

constexpr size_t MBY = 1u << 20;
constexpr size_t OFF_G3 = 0, OFF_SYNC = 2 * MBY, OFF_G2 = 116 * MBY;
constexpr size_t OFF_EP = OFF_SYNC, OFF_ERR = OFF_SYNC + 512, OFF_RDY = OFF_SYNC + 0x1000, OFF_ACT = OFF_SYNC + 0x140000, OFF_RC2 = OFF_SYNC + 0x128000;
constexpr size_t OFF_CLM = OFF_SYNC + 0x10000, OFF_TAB = OFF_SYNC + 0xC000, OFF_XFL = OFF_SYNC + 0xE000;
constexpr size_t OFF_PIL = OFF_G3 + 0x80000, OFF_FLAGS = OFF_SYNC + 0x22000, OFF_OFL = OFF_SYNC + 0xA2000, OFF_HFL = OFF_SYNC + 0xE2000;
constexpr size_t OFF_RC = OFF_SYNC + 0x102000, OFF_PAR = OFF_SYNC + 0x112000;
constexpr unsigned SENT = 0xFFBFFFBFu;
constexpr size_t OFF_KF = 21 * MBY, OFF_VF = 37 * MBY, OFF_KM = 100 * MBY, OFF_VM = 108 * MBY;
constexpr size_t OFF_QF = 61 * MBY, OFF_XN = 77 * MBY, OFF_O = 79 * MBY, OFF_HP = 148 * MBY;

__device__ __forceinline__ unsigned tl_idx() { unsigned xcc; asm volatile("s_getreg_b32 %0, hwreg(HW_REG_XCC_ID)" : "=s"(xcc)); return (blockIdx.x & ~7u) | (xcc & 7u); }
#define GS(i) do {} while (0)
#define TS2(i) do {} while (0)
#define TSD() do {} while (0)
#define TL(i) do {} while (0)
#define TLP() do {} while (0)
__device__ __forceinline__ u64 now_clk() { u64 t; asm volatile("s_memtime %0\n\ts_waitcnt lgkmcnt(0)" : "=s"(t) :: "memory"); return t; }

constexpr int NREC = 64;

__device__ __forceinline__ bool spin_bail(unsigned& spins, char* ws, u64 T) {
  ++spins;
  if ((spins & 255u) == 0u) {
    gu64* err = (gu64*)(ws + OFF_ERR);
    if (__hip_atomic_load(err, RLX_AGENT) == T) return true;
    if (spins > (1u << 20)) { __hip_atomic_store(err, T, RLX_AGENT); return true; }
  }
  return false;
}
__device__ __forceinline__ void set_flag(char* ws, size_t off, u64 T, unsigned v) { __hip_atomic_store((gu64*)(ws + off), T + v, RLX_AGENT); }
__device__ __forceinline__ bf16x8 mk8(unsigned a, unsigned b, unsigned c, unsigned d) {
  u32x4 v; v.x = a; v.y = b; v.z = c; v.w = d; return __builtin_bit_cast(bf16x8, v);
}
__device__ __forceinline__ void store16_sc1(const void* base, unsigned off, u32x4 v) {
  asm volatile("global_store_dwordx4 %0, %1, %2 sc1\n\ts_nop 1" :: "v"(off), "v"(v), "s"(base) : "memory");
}

#define NSPEC (59 * 32)
#define ASL 24
#define QSL 1
#define NSIX 42
#define XROT 0
#define NPROBE 6
#define PSL 4
#define HSL 10
#define LAS __attribute__((address_space(3)))
__device__ __forceinline__ void unpack8(const u32x4 v, float (&f)[8]) {
  f[0] = bf2f(v.x & 0xffff); f[1] = bf2f(v.x >> 16); f[2] = bf2f(v.y & 0xffff); f[3] = bf2f(v.y >> 16);
  f[4] = bf2f(v.z & 0xffff); f[5] = bf2f(v.z >> 16); f[6] = bf2f(v.w & 0xffff); f[7] = bf2f(v.w >> 16);
}
template <int SL>
__device__ __forceinline__ void wait_flag(char* ws, unsigned off, u64 T, unsigned target) {
  gu64* p = (gu64*)(ws + off);
  unsigned spins = 0;
  while (!__all(__hip_atomic_load(p, RLX_AGENT) - T - target < 64u - target)) {
    __builtin_amdgcn_s_sleep(SL);
    if (spin_bail(spins, ws, T)) break;
  }
  __builtin_amdgcn_fence(__ATOMIC_ACQUIRE, "wavefront");
}
typedef __attribute__((ext_vector_type(4))) short s16x4;
__device__ __forceinline__ bf16x8 tr_frag(const char* p0, const char* p1) {
  const s16x4 a = __builtin_amdgcn_ds_read_tr16_b64_v4i16((LAS s16x4*)p0), b = __builtin_amdgcn_ds_read_tr16_b64_v4i16((LAS s16x4*)p1);
  return __builtin_shufflevector(a, b, 0, 1, 2, 3, 4, 5, 6, 7);
}

__device__ __forceinline__ float sum_g(float v) {
  auto r = __builtin_amdgcn_permlane16_swap(__builtin_bit_cast(unsigned, v), __builtin_bit_cast(unsigned, v), false, false);
  const unsigned r0 = r[0], r1 = r[1];
  v = __builtin_bit_cast(float, r0) + __builtin_bit_cast(float, r1);
  auto q = __builtin_amdgcn_permlane32_swap(__builtin_bit_cast(unsigned, v), __builtin_bit_cast(unsigned, v), false, false);
  const unsigned q0 = q[0], q1 = q[1];
  return __builtin_bit_cast(float, q0) + __builtin_bit_cast(float, q1);
}
__device__ __forceinline__ float max_g(float v) {
  auto r = __builtin_amdgcn_permlane16_swap(__builtin_bit_cast(unsigned, v), __builtin_bit_cast(unsigned, v), false, false);
  const unsigned r0 = r[0], r1 = r[1];
  v = fmaxf(__builtin_bit_cast(float, r0), __builtin_bit_cast(float, r1));
  auto q = __builtin_amdgcn_permlane32_swap(__builtin_bit_cast(unsigned, v), __builtin_bit_cast(unsigned, v), false, false);
  const unsigned q0 = q[0], q1 = q[1];
  return fmaxf(__builtin_bit_cast(float, q0), __builtin_bit_cast(float, q1));
}
template <class F>
__device__ __forceinline__ void ln_tile(u32x4 (&xr)[16], F hook) {
  auto up = [](unsigned u) { f32x2_t v; v.x = __builtin_bit_cast(float, u << 16); v.y = __builtin_bit_cast(float, u & 0xffff0000u); return v; };
  f32x2_t f[16][4], sb[4] = {{0.f, 0.f}, {0.f, 0.f}, {0.f, 0.f}, {0.f, 0.f}};
#pragma unroll
  for (int k = 0; k < 16; ++k) {
#pragma unroll
    for (int e = 0; e < 4; ++e) { const unsigned u = xr[k][e]; f[k][e] = up(u); sb[e] += f[k][e]; }
    if (k & 1) hook(k >> 1);
  }
  const f32x2_t sa = (sb[0] + sb[1]) + (sb[2] + sb[3]);
  const float mean = sum_g(sa.x + sa.y) * (1.0f / DM);
  f32x2_t qb[4] = {{0.f, 0.f}, {0.f, 0.f}, {0.f, 0.f}, {0.f, 0.f}};
#pragma unroll
  for (int k = 0; k < 16; ++k) {
#pragma unroll
    for (int e = 0; e < 4; ++e) { const f32x2_t d = f[k][e] - mean; qb[e] += d * d; }
    if (k & 1) hook(8 + (k >> 1));
  }
  const f32x2_t qa = (qb[0] + qb[1]) + (qb[2] + qb[3]);
  float var = sum_g(qa.x + qa.y) * (1.0f / DM), mb = mean;
  rbf2(var, mb);
  const float inv = __builtin_amdgcn_rcpf(rbf(__builtin_amdgcn_sqrtf(rbf(var + 1e-5f))));
#pragma unroll
  for (int k = 0; k < 16; ++k)
#pragma unroll
    for (int e = 0; e < 4; ++e) {
      const f32x2_t d = f[k][e] - mb;
      const f32x2_t y = up(pk2(d.x, d.y)) * inv;
      xr[k][e] = pk2(y.x, y.y);
    }
}
__device__ __forceinline__ void probe(char* ws, int q, u64 T) {
      unsigned best[2] = {~0u, ~0u};
#pragma unroll 1
      for (int i = 0; i < NPROBE; ++i) {
        gu32* p = (gu32*)(ws + (q < 16 ? OFF_G2 + q * 2 * MBY + MBY : OFF_G3 + 0xC0000) + (i & 1) * 8192);
        const u64 t0 = now_clk();
        __hip_atomic_store(p, SENT, RLX_AGENT);
        asm volatile("s_waitcnt vmcnt(0)" ::: "memory");
        const unsigned d = (unsigned)(now_clk() - t0);
        if (d < best[i & 1]) best[i & 1] = d;
      }
      set_flag(ws, OFF_PAR + q * 128, T, best[1] < best[0] ? 2u : 1u);
}
__device__ __forceinline__ u64 tag_of(u64 ep) { u64 T = (ep + 1) << 6; asm volatile("" : "+s"(T)); return T; }
__device__ __forceinline__ u64 ln1_phase(char* ws, const u16* __restrict__ x, int q, u64 ep0, char* lds, int tid) {
  const int lane = tid & 63, wv = tid >> 6;
  const int q2 = q;
  if (wv < 2) {
    u32x4 xr[16];
#pragma unroll
    for (int k = 0; k < 16; ++k) xr[k] = *(const u32x4*)(x + (size_t)(q2 * 32 + wv * 16 + (lane & 15)) * DM + k * 32 + (lane >> 4) * 8);
    ln_tile(xr, [](int) {});
#pragma unroll
    for (int k = 0; k < 16; ++k) store16_sc1(ws + OFF_XN, (unsigned)((q2 * 2 + wv) * 16384 + k * 1024 + lane * 16), xr[k]);
  } else {
    const u32x4 z = (u32x4){0u, 0u, 0u, 0u};
    store16_sc1(ws, (unsigned)((wv == 2 ? OFF_RC : OFF_RC2) + q * 1024 + lane * 16), z);
    const u32x4 p = (u32x4){SENT, SENT, SENT, SENT};
    store16_sc1(ws + OFF_ACT, (unsigned)(q * 4096 + (wv - 2) * 2048 + lane * 16), p);
    store16_sc1(ws + OFF_ACT, (unsigned)(q * 4096 + (wv - 2) * 2048 + 1024 + lane * 16), p);
  }
  asm volatile("s_waitcnt vmcnt(0)" ::: "memory");
  __syncthreads();
  const u64 T = tag_of(ep0);
  if (tid == 0) set_flag(ws, OFF_XFL + q * 128, T, 1);
  TL(6);
  return T;
}

__device__ __forceinline__ void load_h(const __amdgpu_buffer_rsrc_t rs, const u32x4 xv, int row, int lane, float (&f)[8]) {
  f32x4 p[4][2];
#pragma unroll
  for (int k = 0; k < 4; ++k)
#pragma unroll
    for (int u = 0; u < 2; ++u)
      p[k][u] = __builtin_bit_cast(f32x4, __builtin_amdgcn_raw_buffer_load_b128(rs, (int)OFF_HP + (((row >> 5) * 4 + k) * 4 + (lane >> 4)) * 16384 + (row & 31) * 512 + (lane & 15) * 32 + u * 16, 0, 16));
  float xf[8];
  unpack8(xv, xf);
#pragma unroll
  for (int i = 0; i < 8; ++i) f[i] = rbf(xf[i] + rbf((p[0][i >> 2][i & 3] + p[1][i >> 2][i & 3]) + (p[2][i >> 2][i & 3] + p[3][i >> 2][i & 3])));
}

__device__ __forceinline__ unsigned wait_par(char* ws, u64 T, int lane) {
  unsigned spins = 0; u64 p;
  for (;;) {
    p = __hip_atomic_load((gu64*)(ws + OFF_PAR + min(lane, 16) * 128), RLX_AGENT) - T;
    if (__all(p - 1 < 2)) break;
    __builtin_amdgcn_s_sleep(8);
    if (spin_bail(spins, ws, T)) break;
  }
  return (unsigned)__ballot(p == 2);
}
template <int MAT>
__device__ __forceinline__ u64 qkv_phase(char* ws, const u16* __restrict__ x, const u16* __restrict__ W, int h, u64 ep0, char* lds, int tid) {
  const int lane = tid & 63, wv = __builtin_amdgcn_readfirstlane(tid >> 6), l15 = lane & 15, g = lane >> 4;
  constexpr int TS = 16384 + 32, BS = 4 * TS;
  bf16x8 wf[4][16];
  const int wr = wv >> 1, wc = wv & 1;
  u32x4 xr[16];
#pragma unroll
  for (int i = 0; i < 16; ++i)
    if (MAT != 0) asm volatile("global_load_dwordx4 %0, %1, off" : "=v"(xr[i]) : "v"(x + (size_t)(wv * 16 + l15) * DM + i * 32 + g * 8) : "memory");
  auto wdma = [&](int i) {
    const int r = (i >> 2) * 64 + wv * 16 + (i & 3) * 4 + g, f = (r & 3) | ((r >> 1) & 4);
    __builtin_amdgcn_sched_barrier(0);
    __builtin_amdgcn_global_load_lds((const GAS void*)(W + (unsigned)(r * HD + h * DH + ((l15 ^ (f << 1)) * 8))),
                                     (LAS void*)(lds + (i >> 4) * BS + ((i >> 2) & 3) * 16384 + (wv * 4 + (i & 3)) * 1024), 16, 0, 0);
    __builtin_amdgcn_sched_barrier(0);
  };
#pragma unroll
  for (int i = 0; i < 16; ++i) wdma(i);
  if (MAT != 0) {
  asm volatile("s_waitcnt vmcnt(16)" : "+v"(xr[0]), "+v"(xr[1]), "+v"(xr[2]), "+v"(xr[3]), "+v"(xr[4]), "+v"(xr[5]), "+v"(xr[6]), "+v"(xr[7]),
               "+v"(xr[8]), "+v"(xr[9]), "+v"(xr[10]), "+v"(xr[11]), "+v"(xr[12]), "+v"(xr[13]), "+v"(xr[14]), "+v"(xr[15]) :: "memory");
  TS2(3);
  ln_tile(xr, [&](int i) { wdma(16 + i); });
#pragma unroll
  for (int i = 0; i < 16; ++i) asm volatile("" : "+v"(xr[i]));
  } else {
#pragma unroll
    for (int i = 16; i < 32; ++i) wdma(i);
  }
  TS2(4);
  {
    const int qq = l15 >> 2, f = qq | ((g & 1) << 2);
    const char* rp = lds + (g * 8 + qq) * 256 + (l15 & 3) * 8;
    asm volatile("s_waitcnt vmcnt(0)" ::: "memory");
    __builtin_amdgcn_s_barrier();
    asm volatile("" ::: "memory");
    TS2(5);
#pragma unroll
    for (int j = 0; j < 4; ++j) {
#pragma unroll
      for (int ks = 0; ks < 16; ++ks) {
        const char* p = rp + (ks >> 3) * BS + (ks & 7) * 8192 + (((wc * 4 + j) ^ f) * 32);
        wf[j][ks] = tr_frag(p, p + 1024);
      }
#pragma unroll
      for (int ks = 0; ks < 16; ++ks) asm volatile("" : "+a"(wf[j][ks]));
    }
    asm volatile("s_waitcnt lgkmcnt(0)" ::: "memory");
    __builtin_amdgcn_s_barrier();
    asm volatile("" ::: "memory");
    if (MAT != 0) {
#pragma unroll
      for (int i = 0; i < 16; ++i) *(u32x4*)(lds + wv * TS + i * 1024 + lane * 16) = xr[i];
    } else {
      wait_flag<4>(ws, (unsigned)OFF_XFL + lane * 128, tag_of(ep0), 1u);
#pragma unroll
      for (int i = 0; i < 16; ++i)
        __builtin_amdgcn_global_load_lds((const GAS void*)(ws + OFF_XN + wv * 16384 + i * 1024 + lane * 16), (LAS void*)(lds + wv * TS + i * 1024), 16, 0, 16);
    }
  }
  const int aoff = lane * 16;
  constexpr int ASTEP = TS;
  const u64 T = tag_of(ep0);
  const unsigned rdy = (unsigned)OFF_RDY + (MAT * 32 + h) * 128;
  constexpr int NST = MAT == 0 ? 4 : 6;
  const unsigned par = MAT != 0 ? wait_par(ws, T, lane) : 0u;
  asm volatile("s_waitcnt lgkmcnt(0)" ::: "memory");
  TS2(6);
#pragma unroll 1
  for (int k = 0; k < 32; ++k) {
    const int c = k, buf = k & 1;
    GS(0);
    if (k <= 2) asm volatile("s_waitcnt vmcnt(0)" ::: "memory");
    else if (NST == 4) asm volatile("s_waitcnt vmcnt(4)" ::: "memory");
    else asm volatile("s_waitcnt vmcnt(6)" ::: "memory");
    __builtin_amdgcn_s_barrier();
    asm volatile("" ::: "memory");
    if (k == 0) TS2(7);
    if (k >= 2 && tid == 0) set_flag(ws, rdy, T, k == 2 ? 2 : k - 1);
    GS(1);
    f32x4 acc[2][4];
    const unsigned ab = (unsigned)(size_t)(LAS char*)lds + buf * BS + wr * 2 * TS + aoff;
    const unsigned dl = __builtin_amdgcn_readfirstlane((unsigned)(size_t)(LAS char*)lds + (buf ^ 1) * BS + wv * TS);
    const unsigned dv = (unsigned)OFF_XN + (min(c + 1, 31) * 4 + wv) * 16384 + lane * 16;
    bf16x8 af[4][2];
#define RDA(d, ks) asm volatile("ds_read_b128 %0, %2 offset:%3\n\tds_read_b128 %1, %2 offset:%4" : "=&v"(af[d][0]), "=&v"(af[d][1]) : "v"(ab), "i"((ks) * 1024), "i"((ks) * 1024 + ASTEP) : "memory")
    RDA(0, 0); RDA(1, 1); RDA(2, 2);
#pragma unroll
    for (int ks = 0; ks < 16; ++ks) {
      if (ks + 3 < 16) {
        switch (ks) {
          case 0: RDA(3, 3); break; case 1: RDA(0, 4); break; case 2: RDA(1, 5); break; case 3: RDA(2, 6); break; case 4: RDA(3, 7); break;
          case 5: RDA(0, 8); break; case 6: RDA(1, 9); break; case 7: RDA(2, 10); break; case 8: RDA(3, 11); break; case 9: RDA(0, 12); break;
          case 10: RDA(1, 13); break; case 11: RDA(2, 14); break; default: RDA(3, 15); break;
        }
      }
      if (k > 0 && ks < 8) asm volatile("s_add_u32 m0, %0, %2\n\ts_nop 0\n\tglobal_load_lds_dwordx4 %1, %3 sc1\n\tglobal_load_lds_dwordx4 %1, %3 offset:1024 sc1"
                                        :: "s"(dl), "v"(dv + ks * 2048), "i"(ks * 2048), "s"(ws) : "memory", "scc");
      if (ks < 13) asm volatile("s_waitcnt lgkmcnt(6)" : "+v"(af[ks & 3][0]), "+v"(af[ks & 3][1]));
      else if (ks == 13) asm volatile("s_waitcnt lgkmcnt(4)" : "+v"(af[1][0]), "+v"(af[1][1]));
      else if (ks == 14) asm volatile("s_waitcnt lgkmcnt(2)" : "+v"(af[2][0]), "+v"(af[2][1]));
      else asm volatile("s_waitcnt lgkmcnt(0)" : "+v"(af[3][0]), "+v"(af[3][1]));
#pragma unroll
      for (int i = 0; i < 2; ++i)
#pragma unroll
        for (int j = 0; j < 4; ++j) {
          if (ks == 0) {
            if (MAT == 2) asm volatile("v_mfma_f32_16x16x32_bf16 %0, %1, %2, 0" : "=&v"(acc[i][j]) : "v"(af[0][i]), "a"(wf[j][0]));
            else asm volatile("v_mfma_f32_16x16x32_bf16 %0, %2, %1, 0" : "=&v"(acc[i][j]) : "v"(af[0][i]), "a"(wf[j][0]));
          } else {
            if (MAT == 2) asm volatile("v_mfma_f32_16x16x32_bf16 %0, %1, %2, %0" : "+v"(acc[i][j]) : "v"(af[ks & 3][i]), "a"(wf[j][ks]));
            else asm volatile("v_mfma_f32_16x16x32_bf16 %0, %2, %1, %0" : "+v"(acc[i][j]) : "v"(af[ks & 3][i]), "a"(wf[j][ks]));
          }
        }
    }
#undef RDA
    GS(2);
    asm volatile("s_nop 7\n\ts_nop 3" : "+v"(acc[0][0]), "+v"(acc[0][1]), "+v"(acc[0][2]), "+v"(acc[0][3]), "+v"(acc[1][0]), "+v"(acc[1][1]), "+v"(acc[1][2]), "+v"(acc[1][3]));
    u32x4 pv[4];
#pragma unroll
    for (int q = 0; q < 4; ++q) {
      const f32x4 a = MAT == 2 ? acc[0][q] : acc[q & 1][(q >> 1) * 2], b = MAT == 2 ? acc[1][q] : acc[q & 1][(q >> 1) * 2 + 1];
      pv[q].x = pk2(a[0], a[1]); pv[q].y = pk2(a[2], a[3]); pv[q].z = pk2(b[0], b[1]); pv[q].w = pk2(b[2], b[3]);
    }
    if (MAT == 1) {
      u32x4 o[4];
#pragma unroll
      for (int kt = 0; kt < 2; ++kt)
#pragma unroll
        for (int e = 0; e < 4; ++e) {
          const unsigned x0 = pv[kt][e], x1 = pv[2 + kt][e];
          const unsigned d1 = (unsigned)__builtin_amdgcn_update_dpp(0, (int)x1, 0x44, 0xf, 0xf, false), d0 = (unsigned)__builtin_amdgcn_update_dpp(0, (int)x0, 0xee, 0xf, 0xf, false);
          o[kt * 2][e] = (lane & 2) ? d1 : x0; o[kt * 2 + 1][e] = (lane & 2) ? x1 : d0;
        }
#pragma unroll
      for (int q = 0; q < 4; ++q) pv[q] = o[q];
    }
#pragma unroll
    for (int q = 0; q < 4; ++q)
      store16_sc1(ws + (MAT == 0 ? OFF_QF : (MAT == 1 ? OFF_KF : OFF_VF)), (unsigned)(((c * 2 + wr) * 32 + h) * 8192 + wc * 4096 + q * 1024 + lane * 16), pv[q]);
    if (MAT != 0) {
      const u32x4 p = (u32x4){SENT, SENT, SENT, SENT};
#pragma unroll
      for (int i = 0; i < 2; ++i) store16_sc1(ws + OFF_G2, (unsigned)((c * 2 + MAT - 1) * 524288 + (i * 16 + (tid >> 4)) * 16384 + ((par >> (c >> 1)) & 1u) * 8192 + ((tid >> 3) & 1) * 4096 + (h ^ (i * 16 + (tid >> 4))) * 128 + (tid & 7) * 16), p);
    }
    GS(3);
    if (k == 0) {
      asm volatile("s_waitcnt vmcnt(0)" ::: "memory");
      __builtin_amdgcn_s_barrier();
      if (tid == 0) set_flag(ws, rdy, T, 1);
      TS2(1); TSD();
      {
      wait_flag<4>(ws, (unsigned)OFF_XFL + lane * 128, T, 1u);
#pragma unroll
      for (int ks = 0; ks < 16; ++ks)
        asm volatile("s_add_u32 m0, %0, %2\n\ts_nop 0\n\tglobal_load_lds_dwordx4 %1, %3 sc1" :: "s"(dl), "v"(dv + ks * 1024), "i"(ks * 1024), "s"(ws) : "memory", "scc");
      }
    }
  }
  asm volatile("s_waitcnt vmcnt(0)" ::: "memory");
  __builtin_amdgcn_s_barrier();
  if (tid == 0) set_flag(ws, rdy, T, 32);
  return T;
}

__device__ __forceinline__ void xa_phase(char* ws, int grp, int h, u64 T, int lane) {
  const int l15 = lane & 15, g = lane >> 4;
  const float scale = 0.08838834764831845f;
  const __amdgpu_buffer_rsrc_t rs = __builtin_amdgcn_make_buffer_rsrc(ws, 0, (int)(164 * MBY), 0x00020000);
  auto ld16 = [&](int off) { return __builtin_bit_cast(bf16x8, __builtin_amdgcn_raw_buffer_load_b128(rs, off, 0, 16)); };
#pragma unroll 1
  for (int s = grp; s < NSEG; s += 4) {
    wait_flag<8>(ws, (unsigned)OFF_RDY + ((lane < 3 ? lane : 0) * 32 + h) * 128, T, (unsigned)(s >> 1) + 1u);
    const int qp = (int)OFF_QF + (s * 32 + h) * 8192 + lane * 16;
    const int kp = (int)OFF_KF + (s * 32 + h) * 8192 + ((l15 >> 1) & 1) * 1024 + (g * 16 + (l15 & 13)) * 16;
    const int vp = (int)OFF_VF + (s * 32 + h) * 8192 + lane * 16;
    const int km = (int)OFF_KM + (s * 32 + h) * 4096;
    const int vm = (int)OFF_VM + (s * 32 + h) * 4096;
    bf16x8 qf[4][2], vf[8];
    f32x4 sc[2][3];
#pragma unroll
    for (int nt = 0; nt < 2; ++nt)
#pragma unroll
      for (int t = 0; t < 3; ++t) sc[nt][t] = (f32x4){0.f, 0.f, 0.f, 0.f};
#pragma unroll
    for (int ks = 0; ks < 4; ++ks) {
      bf16x8 kf[2];
#pragma unroll
      for (int nt = 0; nt < 2; ++nt) qf[ks][nt] = ld16(qp + ks * 2048 + nt * 1024);
#pragma unroll
      for (int t = 0; t < 2; ++t) kf[t] = ld16(kp + (ks >> 1) * 4096 + (ks & 1) * 32 + t * 2048);
#pragma unroll
      for (int nt = 0; nt < 2; ++nt)
#pragma unroll
        for (int t = 0; t < 2; ++t) sc[nt][t] = __builtin_amdgcn_mfma_f32_16x16x32_bf16(kf[t], qf[ks][nt], sc[nt][t], 0, 0, 0);
    }
#pragma unroll
    for (int e = 0; e < 8; ++e) vf[e] = ld16(vp + e * 1024);
    float mt[2], lt[2];
    f32x4 acc[2][8];
#pragma unroll
    for (int nt = 0; nt < 2; ++nt) {
      const int qi = nt * 16 + l15;
      float v[2][4], m = -3.0e38f;
#pragma unroll
      for (int t = 0; t < 2; ++t)
#pragma unroll
        for (int r = 0; r < 4; ++r) {
          v[t][r] = (t * 16 + g * 4 + r <= qi) ? rbf(rbf(sc[nt][t][r]) * scale) : -3.0e38f;
          m = fmaxf(m, v[t][r]);
        }
      m = max_g(m);
#pragma unroll
      for (int t = 0; t < 2; ++t)
#pragma unroll
        for (int r = 0; r < 4; r += 2) {
          v[t][r] = (t * 16 + g * 4 + r <= qi) ? __expf(v[t][r] - m) : 0.f;
          v[t][r + 1] = (t * 16 + g * 4 + r + 1 <= qi) ? __expf(v[t][r + 1] - m) : 0.f;
          rbf2(v[t][r], v[t][r + 1]);
        }
      mt[nt] = m;
      lt[nt] = sum_g(((v[0][0] + v[0][1]) + (v[0][2] + v[0][3])) + ((v[1][0] + v[1][1]) + (v[1][2] + v[1][3])));
      const bf16x8 pf = mk8(pk2(v[0][0], v[0][1]), pk2(v[0][2], v[0][3]), pk2(v[1][0], v[1][1]), pk2(v[1][2], v[1][3]));
#pragma unroll
      for (int e = 0; e < 8; ++e) acc[nt][e] = __builtin_amdgcn_mfma_f32_16x16x32_bf16(vf[e], pf, (f32x4){0.f, 0.f, 0.f, 0.f}, 0, 0, 0);
    }
#pragma unroll
    for (int e = 0; e < 8; ++e) asm volatile("" : "+v"(acc[0][e]), "+v"(acc[1][e]));
    asm volatile("" : "+v"(mt[0]), "+v"(mt[1]), "+v"(lt[0]), "+v"(lt[1]));
    wait_flag<PSL>(ws, (unsigned)OFF_FLAGS + (s * 64 + h + (lane & 1) * 32) * 128, T, 1u);
    if (s == 63) TL(4);
    bf16x8 k2[4];
    unsigned mv[4][2];
#pragma unroll
    for (int ks = 0; ks < 4; ++ks) k2[ks] = ld16(km + (ks >> 1) * 1024 + (g * 16 + ((l15 >> 3) | ((l15 >> 1) & 2)) * 4 + (ks & 1) * 2 + (l15 & 1)) * 16);
#pragma unroll
    for (int w = 0; w < 4; ++w) { const auto t = __builtin_amdgcn_raw_buffer_load_b64(rs, vm + w * 512 + lane * 8, 0, 16); mv[w][0] = t[0]; mv[w][1] = t[1]; }
#pragma unroll
    for (int ks = 0; ks < 4; ++ks)
#pragma unroll
      for (int nt = 0; nt < 2; ++nt) sc[nt][2] = __builtin_amdgcn_mfma_f32_16x16x32_bf16(k2[ks], qf[ks][nt], sc[nt][2], 0, 0, 0);
#pragma unroll
    for (int nt = 0; nt < 2; ++nt) {
      float a = sc[nt][2][0], b = sc[nt][2][1];
      rbf2(a, b); a *= scale; b *= scale; rbf2(a, b);
      const float m = fmaxf(mt[nt], max_g(fmaxf(a, b)));
      const float al = __expf(mt[nt] - m);
      a = __expf(a - m); b = __expf(b - m);
      const float inv = __builtin_amdgcn_rcpf(al * lt[nt] + sum_g(a + b));
      const float sa = al * inv;
      const bf16x8 pf1 = mk8(pk2(a * inv, b * inv), 0u, 0u, 0u);
#pragma unroll
      for (int e = 0; e < 8; ++e) acc[nt][e] = __builtin_amdgcn_mfma_f32_16x16x32_bf16(mk8(mv[e >> 1][e & 1], 0u, 0u, 0u), pf1, acc[nt][e] * sa, 0, 0, 0);
#pragma unroll
      for (int w = 0; w < 4; ++w) {
        const f32x4 a0 = acc[nt][2 * w], a1 = acc[nt][2 * w + 1];
        u32x4 o; o.x = pk2(a0[0], a0[1]); o.y = pk2(a1[0], a1[1]); o.z = pk2(a0[2], a0[3]); o.w = pk2(a1[2], a1[3]);
        store16_sc1(ws + OFF_O, (unsigned)(((s * 32 + h) * 8 + nt * 4 + w) * 1024 + lane * 16), o);
      }
    }
    asm volatile("s_waitcnt vmcnt(0)" ::: "memory");
    if (lane == 0) set_flag(ws, OFF_OFL + (s * 32 + h) * 128, T, 1);
  }
}

__device__ __forceinline__ void op_phase(char* ws, const u16* __restrict__ x, const u16* __restrict__ w_r, u16* out, const u16* __restrict__ Wo, int grp, int ks4, int cs, u64 T, char* lds, int tid) {
  const int lane = tid & 63, wv = __builtin_amdgcn_readfirstlane(tid >> 6), l15 = lane & 15, g = lane >> 4;
  const __amdgpu_buffer_rsrc_t rs = __builtin_amdgcn_make_buffer_rsrc(ws, 0, (int)(164 * MBY), 0x00020000);
  const int h0 = ks4 * 8 + wv * 2;
  bf16x8 wo[2][32];
#pragma unroll
  for (int hh = 0; hh < 2; ++hh) {
#pragma unroll
    for (int i = 0; i < 32; ++i) {
      const int r = i * 4 + g, f = (r & 1) | ((r >> 1) & 2) | ((r >> 2) & 4);
      __builtin_amdgcn_global_load_lds((const GAS void*)(Wo + (size_t)((h0 + hh) * DH + r) * DM + cs * 128 + ((l15 ^ (f << 1)) * 8)),
                                       (LAS void*)(lds + wv * 32768 + i * 1024), 16, 0, 0);
    }
    asm volatile("s_waitcnt vmcnt(0)" ::: "memory");
    const int qq = l15 >> 2, f = (qq & 1) | ((g & 1) << 1) | ((qq >> 1) << 2);
    const char* rp = lds + wv * 32768 + (g * 4 + (qq & 1) + (qq >> 1) * 16) * 256 + (l15 & 3) * 8;
#pragma unroll
    for (int j = 0; j < 8; ++j)
#pragma unroll
      for (int ks = 0; ks < 4; ++ks) {
        const char* p = rp + ks * 8192 + ((j ^ f) * 32);
        wo[hh][j * 4 + ks] = tr_frag(p, p + 512);
      }
#pragma unroll
    for (int f2 = 0; f2 < 32; ++f2) asm volatile("" : "+a"(wo[hh][f2]));
    asm volatile("s_waitcnt lgkmcnt(0)" ::: "memory");
  }
  __syncthreads();
  constexpr int RS = 528, RW = 32 * RS;
  auto ln2 = [&](int s, bool fresh) {
      const int row = s * 32 + (ks4 * 4 + cs) * 2 + wv;
      const u32x4 xv = *(const u32x4*)(x + (size_t)row * DM + lane * 8);
      float wr[8];
      unpack8(*(const u32x4*)(w_r + lane * 8), wr);
      if (fresh) __builtin_amdgcn_s_sleep(HSL);
      wait_flag<PSL>(ws, (unsigned)OFF_HFL + s * 2048 + l15 * 128, T, 1u);
      if (s == 63) TL(7);
      float f[8], y[8];
      load_h(rs, xv, row, lane, f);
      ln_row(f, y);
      float r = 0.f;
#pragma unroll
      for (int i = 0; i < 8; ++i) r += y[i] * wr[i];
      r = wave_sum(r);
      u32x4 z;
      z.x = pk2(f[0] + y[0], f[1] + y[1]); z.y = pk2(f[2] + y[2], f[3] + y[3]); z.z = pk2(f[4] + y[4], f[5] + y[5]); z.w = pk2(f[6] + y[6], f[7] + y[7]);
      const unsigned rw = (pk2(r, r) & 0xffff0000u) | 1u;
      if (lane < 8 && row >= NSPEC) __hip_atomic_store((gu32*)(ws + OFF_RC2 + (lane * 64 + ((row - NSPEC) >> 2)) * 128 + (row & 3) * 4), rw, RLX_AGENT);
      store16_sc1(out, (unsigned)(row * 1024 + lane * 16), z);
      asm volatile("s_waitcnt vmcnt(0)" ::: "memory");
      if (lane == 0) __hip_atomic_store((gu32*)(ws + OFF_RC + (row >> 2) * 128 + (row & 3) * 4), rw, RLX_AGENT);
  };
  int pend = -1;
#pragma unroll 1
  for (int s = grp; s < NSEG; s = s + 6 < NSIX ? s + 6 : (s < NSIX ? (grp < 4 ? NSIX + grp : NSEG) : s + 4)) {
    f32x4 acc[2][8];
#pragma unroll
    for (int i = 0; i < 2; ++i)
#pragma unroll
      for (int j = 0; j < 8; ++j) { acc[i][j] = (f32x4){0.f, 0.f, 0.f, 0.f}; asm volatile("" : "+v"(acc[i][j])); }
    if (pend >= 0 && !__all(__hip_atomic_load((gu64*)(ws + OFF_OFL + (s * 32 + h0 + (lane & 1)) * 128), RLX_AGENT) == T + 1)) { ln2(pend, true); pend = -1; }
    wait_flag<PSL>(ws, (unsigned)OFF_OFL + (s * 32 + h0 + (lane & 1)) * 128, T, 1u);
    if (s == 63) TL(1);
    const int ob = (int)OFF_O + (s * 32 + h0) * 8192 + lane * 16;
    bf16x8 of[16];
#pragma unroll
    for (int f = 0; f < 16; ++f) of[f] = __builtin_bit_cast(bf16x8, __builtin_amdgcn_raw_buffer_load_b128(rs, ob + f * 1024, 0, 16));
    __builtin_amdgcn_sched_barrier(0);
#pragma unroll
    for (int f = 0; f < 16; ++f)
#pragma unroll
      for (int j = 0; j < 8; ++j)
        asm volatile("v_mfma_f32_16x16x32_bf16 %0, %1, %2, %0" : "+v"(acc[(f >> 2) & 1][j]) : "a"(wo[f >> 3][j * 4 + (f & 3)]), "v"(of[f]));
    asm volatile("s_nop 7\n\ts_nop 3" : "+v"(acc[1][4]), "+v"(acc[1][5]), "+v"(acc[1][6]), "+v"(acc[1][7]), "+v"(acc[1][0]), "+v"(acc[1][1]), "+v"(acc[1][2]), "+v"(acc[1][3]));
    __builtin_amdgcn_sched_barrier(0);
    if (s == 63) TL(3);
#pragma unroll
    for (int tt = 0; tt < 2; ++tt)
#pragma unroll
      for (int j = 0; j < 8; ++j) *(f32x4*)(lds + wv * RW + (tt * 16 + l15) * RS + j * 64 + g * 16) = acc[tt][j];
    __syncthreads();
#pragma unroll
    for (int i = 0; i < 4; ++i) {
      const int row = i * 8 + (tid >> 5);
      const char* p = lds + row * RS + (tid & 31) * 16;
      const f32x4 a = (*(const f32x4*)p + *(const f32x4*)(p + RW)) + (*(const f32x4*)(p + 2 * RW) + *(const f32x4*)(p + 3 * RW));
      store16_sc1(ws + OFF_HP, (unsigned)(((s * 4 + ks4) * 4 + cs) * 16384 + i * 4096 + tid * 16), __builtin_bit_cast(u32x4, a));
    }
    asm volatile("s_waitcnt vmcnt(0)" ::: "memory");
    __syncthreads();
    if (tid == 0) set_flag(ws, OFF_HFL + (s * 16 + ks4 * 4 + cs) * 128, T, 1);
    if (s == 63) TL(6);
    if (pend >= 0) ln2(pend, false);
    pend = wv < 2 ? s : -1;
  }
  if (pend >= 0) ln2(pend, true);
}

__device__ __forceinline__ unsigned rkey(unsigned w, int idx) { return ((w ^ (((int)w >> 31) | 0x80000000u)) & 0xffff0000u) | (unsigned)(0xffff - idx); }
__device__ __forceinline__ int key_row(unsigned k) { return (0xffff - (int)(k & 0xffffu)) & 2047; }
__device__ __forceinline__ float key_val(unsigned k) { k &= 0xffff0000u; return __builtin_bit_cast(float, (k & 0x80000000u) ? k ^ 0x80000000u : (~k) & 0xffff0000u); }
__device__ __forceinline__ unsigned wave_umax(unsigned v) {
  v = max(v, (unsigned)__builtin_amdgcn_update_dpp(0, (int)v, 0x121, 0xf, 0xf, false)); v = max(v, (unsigned)__builtin_amdgcn_update_dpp(0, (int)v, 0x122, 0xf, 0xf, false));
  v = max(v, (unsigned)__builtin_amdgcn_update_dpp(0, (int)v, 0x124, 0xf, 0xf, false)); v = max(v, (unsigned)__builtin_amdgcn_update_dpp(0, (int)v, 0x128, 0xf, 0xf, false));
  return max(max((unsigned)__builtin_amdgcn_readlane((int)v, 0), (unsigned)__builtin_amdgcn_readlane((int)v, 16)),
             max((unsigned)__builtin_amdgcn_readlane((int)v, 32), (unsigned)__builtin_amdgcn_readlane((int)v, 48)));
}
__device__ __forceinline__ void push2(unsigned& a, unsigned& b, unsigned k) { b = max(b, min(a, k)); a = max(a, k); }
__device__ __forceinline__ void wave_top2(unsigned& a, unsigned& b) {
  const unsigned m = wave_umax(a);
  b = wave_umax(a == m ? b : a); a = m;
}
__device__ __forceinline__ void top2(char* ws, u64 T, int nrows, int tid, unsigned& k0, unsigned& k1, unsigned* sk) {
  const __amdgpu_buffer_rsrc_t rs = __builtin_amdgcn_make_buffer_rsrc(ws, 0, (int)(164 * MBY), 0x00020000);
  const int lane = tid & 63, wv = tid >> 6;
  const bool use0 = tid * 4 < nrows, use1 = (256 + tid) * 4 < nrows;
  u32x4 c[2];
  unsigned spins = 0;
  if (wv == 0) { wait_flag<12>(ws, (unsigned)OFF_HFL + ((nrows >> 5) - 1) * 2048, T, 1u); __builtin_amdgcn_s_sleep(48); }
  __syncthreads();
  for (;;) {
    c[0] = __builtin_bit_cast(u32x4, __builtin_amdgcn_raw_buffer_load_b128(rs, (int)OFF_RC + (use0 ? tid : 0) * 128, 0, 16));
    c[1] = __builtin_bit_cast(u32x4, __builtin_amdgcn_raw_buffer_load_b128(rs, (int)OFF_RC + (use1 ? 256 + tid : 0) * 128, 0, 16));
    if (__all(c[0].x & c[0].y & c[0].z & c[0].w & c[1].x & c[1].y & c[1].z & c[1].w & 1u)) break;
    __builtin_amdgcn_s_sleep(8);
    if (spin_bail(spins, ws, T)) break;
    asm volatile("" ::: "memory");
  }
  unsigned a = 0, b = 0;
#pragma unroll
  for (int k = 0; k < 2; ++k)
#pragma unroll
    for (int e = 0; e < 4; ++e) { const unsigned w = c[k][e]; push2(a, b, (k == 0 ? use0 : use1) ? rkey(w, (k * 256 + tid) * 4 + e) : 0u); }
  wave_top2(a, b);
  if (lane == 0) { sk[wv * 2] = a; sk[wv * 2 + 1] = b; }
  __syncthreads();
  a = 0; b = 0;
#pragma unroll
  for (int i = 0; i < 8; ++i) push2(a, b, sk[i]);
  k0 = a; k1 = b;
}
__device__ __forceinline__ bool spec_holds(char* ws, u64 T, unsigned xcc, unsigned& k0, unsigned& k1, unsigned* sk, int tid) {
  static_assert(S_ - NSPEC <= 256, "one line per lane");
  if (tid < 64) {
    const __amdgpu_buffer_rsrc_t rs = __builtin_amdgcn_make_buffer_rsrc(ws, 0, (int)(164 * MBY), 0x00020000);
    const bool use = tid < (S_ - NSPEC) / 4;
    u32x4 c;
    unsigned spins = 0;
    for (;;) {
      c = __builtin_bit_cast(u32x4, __builtin_amdgcn_raw_buffer_load_b128(rs, (int)OFF_RC2 + (xcc * 64 + (use ? tid : 0)) * 128, 0, 16));
      if (__all(c.x & c.y & c.z & c.w & 1u)) break;
      __builtin_amdgcn_s_sleep(2);
      if (spin_bail(spins, ws, T)) break;
      asm volatile("" ::: "memory");
    }
    unsigned a = 0, b = 0;
#pragma unroll
    for (int e = 0; e < 4; ++e) { const unsigned w = c[e]; push2(a, b, use ? rkey(w, NSPEC + tid * 4 + e) : 0u); }
    wave_top2(a, b);
    push2(a, b, k0); push2(a, b, k1);
    if (tid == 0) { sk[8] = a; sk[9] = b; }
  }
  __syncthreads();
  const bool same = sk[8] == k0 && sk[9] == k1;
  k0 = sk[8]; k1 = sk[9];
  return same;
}
__device__ __forceinline__ void sel_rows(char* ws, const u16* __restrict__ x, u64 T, int tid, int i0, int i1, float* hs, float* ys, char* yb) {
  const __amdgpu_buffer_rsrc_t rs = __builtin_amdgcn_make_buffer_rsrc(ws, 0, (int)(164 * MBY), 0x00020000);
  const int lane = tid & 63, wv = tid >> 6;
  if (wv < 2) {
    const int row = wv ? i1 : i0;
    for (unsigned spins = 0; __hip_atomic_load((gu32*)(ws + OFF_RC + (row >> 2) * 128 + (row & 3) * 4), RLX_AGENT) == 0u;)
      if (spin_bail(spins, ws, T)) break;
    float f[8], y[8];
    load_h(rs, *(const u32x4*)(x + (size_t)row * DM + lane * 8), row, lane, f);
    ln_row(f, y);
#pragma unroll
    for (int i = 0; i < 8; ++i) { hs[wv * DM + lane * 8 + i] = f[i]; ys[wv * DM + lane * 8 + i] = y[i]; }
    u32x4 o; o.x = pk2(y[0], y[1]); o.y = pk2(y[2], y[3]); o.z = pk2(y[4], y[5]); o.w = pk2(y[6], y[7]);
    *(u32x4*)(yb + wv * 1040 + lane * 16) = o;
  }
  __syncthreads();
}
template <int NF>
__device__ __forceinline__ void load_frags(bf16x8 (&w)[NF], const u16* __restrict__ src, int pitch, char* st, int lane) {
  const int l15 = lane & 15, g = lane >> 4, qq = l15 >> 2;
#pragma unroll
  for (int b = 0; b < NF / 2; b += 16) {
    constexpr int NK = 16;
#pragma unroll
    for (int i = 0; i < NK * 2; ++i) {
      if (b * 2 + i < NF) {
        const int r = i * 16 + (lane >> 2);
        __builtin_amdgcn_global_load_lds((const GAS void*)(src + (size_t)(b * 32 + r) * pitch + (((lane & 3) ^ ((r >> 2) & 2)) * 8)), (LAS void*)(st + i * 1024), 16, 0, 0);
      }
    }
    asm volatile("s_waitcnt vmcnt(0)" ::: "memory");
#pragma unroll
    for (int ks = 0; ks < NK; ++ks)
#pragma unroll
      for (int j = 0; j < 2; ++j)
        if ((b + ks) * 2 < NF) {
          const char* p = st + (ks * 32 + g * 8 + qq) * 64 + ((j ^ (g & 1)) * 32) + (l15 & 3) * 8;
          w[(b + ks) * 2 + j] = tr_frag(p, p + 256);
        }
#pragma unroll
    for (int f = 0; f < NK * 2; ++f) if (b * 2 + f < NF) asm volatile("" : "+a"(w[b * 2 + f]));
    asm volatile("s_waitcnt lgkmcnt(0)" ::: "memory");
  }
}

__device__ __forceinline__ void mlp1_phase(char* ws, const u16* __restrict__ x, const u16* __restrict__ wg, const u16* __restrict__ wu, int m, u64 T, char* lds, int tid) {
  unsigned* sk = (unsigned*)lds; float* hs = (float*)(lds + 128); float* xs = hs + 2 * DM; char* yb = (char*)(xs + 2 * DM);
  const int lane = tid & 63, wv = __builtin_amdgcn_readfirstlane(tid >> 6), l15 = lane & 15, g = lane >> 4;
  bf16x8 a[32], b[32];
  load_frags<32>(a, wg + m * 128 + wv * 32, FF, lds + 16384 + wv * 32768, lane);
  load_frags<32>(b, wu + m * 128 + wv * 32, FF, lds + 16384 + wv * 32768, lane);
  unsigned xcc, k0, k1;
  asm volatile("s_getreg_b32 %0, hwreg(HW_REG_XCC_ID)" : "=s"(xcc));
  xcc &= 7u;
#pragma unroll 1
  for (int round = 1; round < 3; ++round) {
    if (round < 2) top2(ws, T, NSPEC, tid, k0, k1, sk);
    const int i0 = key_row(k0), i1 = key_row(k1);
    TL(1);
    sel_rows(ws, x, T, tid, i0, i1, hs, xs, yb);
    TL(3);
    bf16x8 xf[16];
#pragma unroll
    for (int ks = 0; ks < 16; ++ks) xf[ks] = *(const bf16x8*)(yb + (l15 & 1) * 1040 + ks * 64 + g * 16);
    f32x4 acc[4];
#pragma unroll
    for (int j = 0; j < 4; ++j) { acc[j] = (f32x4){0.f, 0.f, 0.f, 0.f}; asm volatile("" : "+v"(acc[j])); }
    asm volatile("s_nop 1" ::: "memory");
#pragma unroll
    for (int ks = 0; ks < 16; ++ks)
#pragma unroll
      for (int j = 0; j < 2; ++j) {
        asm volatile("v_mfma_f32_16x16x32_bf16 %0, %1, %2, %0" : "+v"(acc[j]) : "v"(xf[ks]), "a"(a[ks * 2 + j]));
        asm volatile("v_mfma_f32_16x16x32_bf16 %0, %1, %2, %0" : "+v"(acc[2 + j]) : "v"(xf[ks]), "a"(b[ks * 2 + j]));
      }
    asm volatile("s_nop 7\n\ts_nop 3" : "+v"(acc[0]), "+v"(acc[1]), "+v"(acc[2]), "+v"(acc[3]));
    {
      const int t = g >> 1, j = g & 1;
      const float g0 = j ? acc[1][0] : acc[0][0], g1 = j ? acc[1][1] : acc[0][1], u0 = j ? acc[3][0] : acc[2][0], u1 = j ? acc[3][1] : acc[2][1];
      const float gt = rbf(t ? g1 : g0), u = rbf(t ? u1 : u0);
      const float sg = rbf(gt / (1.0f + __expf(-gt)));
      const float av = sg * u;
      u16* ab = (u16*)(lds + 12288) + wv * 64;
      ab[lane] = (u16)pk2(av, av);
      __builtin_amdgcn_fence(__ATOMIC_RELEASE, "wavefront"); __builtin_amdgcn_wave_barrier(); __builtin_amdgcn_fence(__ATOMIC_ACQUIRE, "wavefront");
      store16_sc1(ws + OFF_ACT, (unsigned)(round * 65536 + (lane >> 3) * 8192 + (m * 4 + wv) * 128 + (lane & 7) * 16), *(const u32x4*)(ab + (lane & 7) * 8));
    }
    TL(6);
    if (round == 1 && spec_holds(ws, T, xcc, k0, k1, sk, tid)) break;
  }
}

__device__ __forceinline__ void mlp2_phase(char* ws, const u16* __restrict__ x, const u16* __restrict__ wd, u16* out, int m, u64 T, char* lds, int tid) {
  unsigned* sk = (unsigned*)lds; float* hs = (float*)(lds + 128); float* xs = hs + 2 * DM; char* yb = (char*)(xs + 2 * DM);
  float* part = (float*)(lds + 12288);
  const __amdgpu_buffer_rsrc_t rs = __builtin_amdgcn_make_buffer_rsrc(ws, 0, (int)(164 * MBY), 0x00020000);
  const int lane = tid & 63, wv = __builtin_amdgcn_readfirstlane(tid >> 6), l15 = lane & 15, g = lane >> 4;
  bf16x8 a[28];
  load_frags<28>(a, wd + (size_t)wv * 448 * DM + m * 32, DM, lds + 16384 + wv * 32768, lane);
  unsigned xcc, k0, k1;
  asm volatile("s_getreg_b32 %0, hwreg(HW_REG_XCC_ID)" : "=s"(xcc));
  xcc &= 7u;
#pragma unroll 1
  for (int round = 1; round < 3; ++round) {
    if (round < 2) top2(ws, T, NSPEC, tid, k0, k1, sk);
    const int i0 = key_row(k0), i1 = key_row(k1);
    TL(1);
    sel_rows(ws, x, T, tid, i0, i1, hs, xs, yb);
    TL(3);
    u32x4 xf[14];
    unsigned spins = 0;
    __builtin_amdgcn_s_sleep(ASL);
    for (;;) {
      bool ok = true;
#pragma unroll
      for (int ks = 0; ks < 14; ++ks) {
        xf[ks] = __builtin_bit_cast(u32x4, __builtin_amdgcn_raw_buffer_load_b128(rs, (int)OFF_ACT + round * 65536 + xcc * 8192 + (wv * 14 + ks) * 128 + (l15 & 1) * 64 + g * 16, 0, 16));
        ok = ok && xf[ks].x != SENT && xf[ks].y != SENT && xf[ks].z != SENT && xf[ks].w != SENT;
      }
      if (__all(ok)) break;
      __builtin_amdgcn_s_sleep(2);
      if (spin_bail(spins, ws, T)) break;
      asm volatile("" ::: "memory");
    }
    TL(7);
    f32x4 acc[2];
#pragma unroll
    for (int j = 0; j < 2; ++j) { acc[j] = (f32x4){0.f, 0.f, 0.f, 0.f}; asm volatile("" : "+v"(acc[j])); }
    asm volatile("s_nop 1" ::: "memory");
#pragma unroll
    for (int ks = 0; ks < 14; ++ks)
#pragma unroll
      for (int j = 0; j < 2; ++j) asm volatile("v_mfma_f32_16x16x32_bf16 %0, %1, %2, %0" : "+v"(acc[j]) : "v"(xf[ks]), "a"(a[ks * 2 + j]));
    asm volatile("s_nop 7\n\ts_nop 3" : "+v"(acc[0]), "+v"(acc[1]));
    {
      const float e0 = (g & 1) ? acc[1][0] : acc[0][0], e1 = (g & 1) ? acc[1][1] : acc[0][1];
      part[tid] = (g >> 1) ? e1 : e0;
    }
    __syncthreads();
    const int t = (tid >> 4) & 1, cc = (tid & 15) * 2, idx = t ? i1 : i0, col = m * 32 + cc;
    const float r = key_val(t ? k1 : k0);
    const float sg = rbf(1.0f / (1.0f + __expf(-r)));
    float o[2];
#pragma unroll
    for (int e = 0; e < 2; ++e) {
      const int q = t * 32 + cc + e;
      const float y = (part[q] + part[64 + q]) + (part[128 + q] + part[192 + q]);
      const float upd = rbf(rbf(y) * sg);
      o[e] = hs[t * DM + col + e] + rbf(xs[t * DM + col + e] + upd);
    }
    TL(4);
    gu32* op = (gu32*)(out + (size_t)idx * DM + col);
    if (tid < 32) __hip_atomic_store(op, pk2(o[0], o[1]), RLX_AGENT);
    if (round == 2 || spec_holds(ws, T, xcc, k0, k1, sk, tid)) break;
    if (tid < 32) __hip_atomic_store(op, pk2(hs[t * DM + col] + xs[t * DM + col], hs[t * DM + col + 1] + xs[t * DM + col + 1]), RLX_AGENT);
    asm volatile("s_waitcnt vmcnt(0)" ::: "memory");
    __syncthreads();
  }
}

__device__ __forceinline__ void red_phase(char* ws, int b, u64 T, unsigned par, int lane) {
  unsigned xcc; asm volatile("s_getreg_b32 %0, hwreg(HW_REG_XCC_ID)" : "=s"(xcc));
  const int g = lane >> 4;
  const unsigned near3 = ((par >> 16) & 1u) * 8192u;
  const unsigned own = (xcc & 3u) * 16384u + near3;
  const unsigned so = (((lane >> 3) & 1) + (lane >> 5) * 2) * 16384 + b * 128 + ((g & 1) + (lane & 1) * 2 + (lane & 4)) * 16 + (lane & 2) * 4 + near3;
  const unsigned io = (unsigned)OFF_G2 + (b >> 1) * 16384 + (b & 1) * 4096 + lane * 16;
  const unsigned one = (lane & 4) ? 0x3f800000u : 0x3f80u;
  asm volatile(
    "s_mov_b64 s[36:37], %0\n"
    "s_mov_b64 s[38:39], %1\n"
    "s_mov_b32 s40, %2\n"
    "v_mov_b32 v32, %3\n"
    "v_mov_b32 v33, %4\n"
    "v_mov_b32 v34, %5\n"
    "v_mov_b32 v35, %6\n"
    "v_mov_b32 v36, %7\n"
    "v_mov_b32 v37, %8\n"
    "v_mov_b32 v39, %9\n"
    "v_add_u32 v38, 0x10000, v37\n"
    "v_mov_b32 v40, 0xffbfffbf\n"
    "v_mov_b32 v41, 0xffbfffbf\n"
    "s_or_b32 s38, s38, 1\n"
    "s_mov_b32 s41, 0\n"
    "s_mov_b32 s47, 0\n"
    "s_add_u32 s64, s36, 0x200200\n"
    "s_addc_u32 s65, s37, 0\n"
    "s_getpc_b64 s[60:61]\n"
    "R_pc%=:\n"
    "s_add_u32 s60, s60, R_go%=-R_pc%=\n"
    "s_addc_u32 s61, s61, 0\n"
    "R_step%=:\n"
    "s_and_b32 s70, s41, 3\n"
    "s_lshl_b32 s71, s70, 16\n"
    "s_add_u32 s71, s71, 0x80000\n"
    "s_add_u32 s48, s36, s71\n"
    "s_addc_u32 s49, s37, 0\n"
    "s_add_u32 s71, s70, 1\n"
    "s_and_b32 s71, s71, 3\n"
    "s_lshl_b32 s71, s71, 17\n"
    "s_add_u32 s52, s36, s71\n"
    "s_addc_u32 s53, s37, 0\n"
    "s_xor_b32 s71, s71, 0x40000\n"
    "s_add_u32 s54, s36, s71\n"
    "s_addc_u32 s55, s37, 0\n"
    "s_lshr_b32 s70, s41, 2\n"
    "s_lshr_b32 s70, s40, s70\n"
    "s_and_b32 s70, s70, 1\n"
    "s_lshl_b32 s70, s70, 13\n"
    "s_lshl_b32 s71, s41, 19\n"
    "s_add_u32 s70, s70, s71\n"
    "s_add_u32 s50, s36, s70\n"
    "s_addc_u32 s51, s37, 0\n"
    "s_mov_b32 s45, 0\n"
    "s_mov_b32 s46, 0\n"
    "s_cmp_eq_u32 s41, 0\n"
    "s_cbranch_scc1 R_look%=\n"
    "s_add_u32 s44, s44, 3000\n"
    "R_lookat%=:\n"
    "R_w1%=:\n"
    "s_memtime s[56:57]\n"
    "s_waitcnt lgkmcnt(0)\n"
    "s_sub_u32 s70, s44, s56\n"
    "s_sub_u32 s71, s70, 800\n"
    "s_cmp_lt_u32 s71, 0x2000\n"
    "s_cbranch_scc1 R_w1%=\n"
    "R_look%=:\n"
    "global_load_dwordx4 v[64:67], v39, s[48:49] sc1\n"
    "s_sleep 3\n"
    "s_memtime s[56:57]\n"
    "s_memrealtime s[70:71]\n"
    "s_nop 3\n"
    "s_memrealtime s[72:73]\n"
    "s_nop 3\n"
    "s_memrealtime s[74:75]\n"
    "s_nop 3\n"
    "s_memrealtime s[76:77]\n"
    "s_nop 3\n"
    "s_memrealtime s[78:79]\n"
    "s_nop 3\n"
    "s_memrealtime s[80:81]\n"
    "s_mov_b32 s59, 0\n"
    "s_waitcnt lgkmcnt(0)\n"
    "s_mul_i32 s58, s70, 24\n"
    "s_cmp_eq_u32 s70, s72\n"
    "s_addc_u32 s59, s59, 0\n"
    "s_cmp_eq_u32 s70, s74\n"
    "s_addc_u32 s59, s59, 0\n"
    "s_cmp_eq_u32 s70, s76\n"
    "s_addc_u32 s59, s59, 0\n"
    "s_cmp_eq_u32 s70, s78\n"
    "s_addc_u32 s59, s59, 0\n"
    "s_cmp_eq_u32 s70, s80\n"
    "s_addc_u32 s59, s59, 0\n"
    "s_mul_i32 s59, s59, 20\n"
    "s_sub_u32 s58, s58, s59\n"
    "s_sub_u32 s58, s58, 190\n"
    "s_waitcnt vmcnt(0)\n"
    "v_subrev_u32 v67, s58, v67\n"
    "v_cmp_ne_u64 s[74:75], s[38:39], v[64:65]\n"
    "v_cmp_ne_u32 vcc, s41, v66\n"
    "v_max_i32_dpp v67, v67, v67 row_ror:1 row_mask:0xf bank_mask:0xf\n"
    "s_nop 1\n"
    "v_max_i32_dpp v67, v67, v67 row_ror:2 row_mask:0xf bank_mask:0xf\n"
    "s_nop 1\n"
    "v_max_i32_dpp v67, v67, v67 row_ror:4 row_mask:0xf bank_mask:0xf\n"
    "s_nop 1\n"
    "v_max_i32_dpp v67, v67, v67 row_ror:8 row_mask:0xf bank_mask:0xf\n"
    "s_nop 0\n"
    "v_readlane_b32 s70, v67, 0\n"
    "v_readlane_b32 s71, v67, 16\n"
    "v_readlane_b32 s72, v67, 32\n"
    "v_readlane_b32 s73, v67, 48\n"
    "s_or_b64 vcc, vcc, s[74:75]\n"
    "s_cbranch_scc1 R_notyet%=\n"
    "s_max_i32 s70, s70, s71\n"
    "s_max_i32 s72, s72, s73\n"
    "s_max_i32 s70, s70, s72\n"
    "s_min_i32 s70, s70, 8000\n"
    "s_add_u32 s44, s56, s70\n"
    "s_cmp_gt_i32 s70, 1210\n"
    "s_cbranch_scc1 R_far%=\n"
    "R_timed%=:\n"
    "R_w2%=:\n"
    "s_memtime s[56:57]\n"
    "s_waitcnt lgkmcnt(0)\n"
    "s_sub_u32 s70, s44, s56\n"
    "s_sub_u32 s71, s70, 200\n"
    "s_cmp_lt_u32 s71, 0x2000\n"
    "s_cbranch_scc1 R_w2%=\n"
    "s_sub_i32 s70, s70, 76\n"
    "s_max_i32 s70, s70, 0\n"
    "s_min_i32 s70, s70, 124\n"
    "s_andn2_b32 s70, s70, 3\n"
    "s_sub_u32 s72, s60, s70\n"
    "s_subb_u32 s73, s61, 0\n"
    "s_setpc_b64 s[72:73]\n"
    ".p2align 6\n"
    "s_nop 0\n"
    "s_nop 0\n"
    "s_nop 0\n"
    "s_nop 0\n"
    "s_nop 0\n"
    "s_nop 0\n"
    "s_nop 0\n"
    "s_nop 0\n"
    "s_nop 0\n"
    "s_nop 0\n"
    "s_nop 0\n"
    "s_nop 0\n"
    "s_nop 0\n"
    "s_nop 0\n"
    "s_nop 0\n"
    "s_nop 0\n"
    "s_nop 0\n"
    "s_nop 0\n"
    "s_nop 0\n"
    "s_nop 0\n"
    "s_nop 0\n"
    "s_nop 0\n"
    "s_nop 0\n"
    "s_nop 0\n"
    "s_nop 0\n"
    "s_nop 0\n"
    "s_nop 0\n"
    "s_nop 0\n"
    "s_nop 0\n"
    "s_nop 0\n"
    "s_nop 0\n"
    "s_nop 0\n"
    "R_go%=:\n"
    "global_load_dwordx4 v[48:51], v36, s[50:51] offset:0 sc1\n"
    "global_load_dwordx4 v[52:55], v36, s[50:51] offset:1024 sc1\n"
    "global_load_dwordx4 v[56:59], v36, s[50:51] offset:2048 sc1\n"
    "global_load_dwordx4 v[60:63], v36, s[50:51] offset:3072 sc1\n"
    "s_waitcnt vmcnt(3)\n"
    "v_mfma_f32_16x16x32_bf16 v[44:47], v[32:35], v[48:51], 0\n"
    "s_waitcnt vmcnt(2)\n"
    "v_mfma_f32_16x16x32_bf16 v[44:47], v[32:35], v[52:55], v[44:47]\n"
    "s_waitcnt vmcnt(1)\n"
    "v_mfma_f32_16x16x32_bf16 v[44:47], v[32:35], v[56:59], v[44:47]\n"
    "s_waitcnt vmcnt(0)\n"
    "v_mfma_f32_16x16x32_bf16 v[44:47], v[32:35], v[60:63], v[44:47]\n"
    "s_nop 7\n"
    "v_add_f32_dpp v44, v44, v44 row_ror:8 row_mask:0xf bank_mask:0xf\n"
    "v_add_f32_dpp v45, v45, v45 row_ror:8 row_mask:0xf bank_mask:0xf\n"
    "v_add_f32_dpp v46, v46, v46 row_ror:8 row_mask:0xf bank_mask:0xf\n"
    "v_add_f32_dpp v47, v47, v47 row_ror:8 row_mask:0xf bank_mask:0xf\n"
    "v_cmpx_o_f32 vcc, v44, v45\n"
    "R_pub%=:\n"
    "v_cvt_pk_bf16_f32 v64, v44, v45\n"
    "v_cvt_pk_bf16_f32 v65, v46, v47\n"
    "global_store_dwordx2 v37, v[64:65], s[52:53] sc1\n"
    "global_store_dwordx2 v38, v[64:65], s[52:53] sc1\n"
    "s_memtime s[58:59]\n"
    "s_cmp_eq_u64 exec, -1\n"
    "s_mov_b64 exec, -1\n"
    "s_cbranch_scc0 R_miss%=\n"
    "s_waitcnt lgkmcnt(0)\n"
    "s_add_u32 s58, s58, 600\n"
    "R_w3%=:\n"
    "s_memtime s[56:57]\n"
    "s_waitcnt lgkmcnt(0)\n"
    "s_sub_u32 s70, s58, s56\n"
    "s_sub_u32 s71, s70, 0\n"
    "s_cmp_lt_u32 s71, 0x2000\n"
    "s_cbranch_scc1 R_w3%=\n"
    "global_store_dwordx2 v37, v[40:41], s[54:55] sc1\n"
    "global_store_dwordx2 v38, v[40:41], s[54:55] sc1\n"
    "s_add_u32 s41, s41, 1\n"
    "s_cmp_lt_u32 s41, 63\n"
    "s_cbranch_scc1 R_step%=\n"
    "s_endpgm\n"
    "R_far%=:\n"
    "s_add_u32 s46, s46, 1\n"
    "s_cmp_lt_u32 s46, 16\n"
    "s_cbranch_scc1 R_lookat%=\n"
    "s_branch R_timed%=\n"
    "R_notyet%=:\n"
    "s_add_u32 s47, s47, 1\n"
    "s_and_b32 s70, s47, 0xff\n"
    "s_cbranch_scc1 R_look%=\n"
    "v_mov_b32 v64, 0\n"
    "global_load_dwordx2 v[66:67], v64, s[64:65] sc1\n"
    "s_sub_u32 s70, s38, 1\n"
    "s_mov_b32 s71, s39\n"
    "s_waitcnt vmcnt(0)\n"
    "v_cmp_eq_u64 vcc, s[70:71], v[66:67]\n"
    "s_nop 1\n"
    "s_cbranch_vccnz R_go%=\n"
    "s_cmp_lt_u32 s47, 0x40000\n"
    "s_cbranch_scc1 R_look%=\n"
    "v_mov_b32 v66, s70\n"
    "v_mov_b32 v67, s71\n"
    "global_store_dwordx2 v64, v[66:67], s[64:65] sc1\n"
    "s_branch R_go%=\n"
    "R_miss%=:\n"
    "s_add_u32 s45, s45, 1\n"
    "s_cmp_eq_u32 s45, 1\n"
    "s_cbranch_scc1 R_go%=\n"
    "s_mov_b64 s[72:73], 0\n"
    "v_cmp_eq_u32 vcc, v40, v48\n"
    "s_nop 0\n"
    "s_or_b64 s[72:73], s[72:73], vcc\n"
    "v_cmp_eq_u32 vcc, v40, v49\n"
    "s_nop 0\n"
    "s_or_b64 s[72:73], s[72:73], vcc\n"
    "v_cmp_eq_u32 vcc, v40, v50\n"
    "s_nop 0\n"
    "s_or_b64 s[72:73], s[72:73], vcc\n"
    "v_cmp_eq_u32 vcc, v40, v51\n"
    "s_nop 0\n"
    "s_or_b64 s[72:73], s[72:73], vcc\n"
    "v_cmp_eq_u32 vcc, v40, v52\n"
    "s_nop 0\n"
    "s_or_b64 s[72:73], s[72:73], vcc\n"
    "v_cmp_eq_u32 vcc, v40, v53\n"
    "s_nop 0\n"
    "s_or_b64 s[72:73], s[72:73], vcc\n"
    "v_cmp_eq_u32 vcc, v40, v54\n"
    "s_nop 0\n"
    "s_or_b64 s[72:73], s[72:73], vcc\n"
    "v_cmp_eq_u32 vcc, v40, v55\n"
    "s_nop 0\n"
    "s_or_b64 s[72:73], s[72:73], vcc\n"
    "v_cmp_eq_u32 vcc, v40, v56\n"
    "s_nop 0\n"
    "s_or_b64 s[72:73], s[72:73], vcc\n"
    "v_cmp_eq_u32 vcc, v40, v57\n"
    "s_nop 0\n"
    "s_or_b64 s[72:73], s[72:73], vcc\n"
    "v_cmp_eq_u32 vcc, v40, v58\n"
    "s_nop 0\n"
    "s_or_b64 s[72:73], s[72:73], vcc\n"
    "v_cmp_eq_u32 vcc, v40, v59\n"
    "s_nop 0\n"
    "s_or_b64 s[72:73], s[72:73], vcc\n"
    "v_cmp_eq_u32 vcc, v40, v60\n"
    "s_nop 0\n"
    "s_or_b64 s[72:73], s[72:73], vcc\n"
    "v_cmp_eq_u32 vcc, v40, v61\n"
    "s_nop 0\n"
    "s_or_b64 s[72:73], s[72:73], vcc\n"
    "v_cmp_eq_u32 vcc, v40, v62\n"
    "s_nop 0\n"
    "s_or_b64 s[72:73], s[72:73], vcc\n"
    "v_cmp_eq_u32 vcc, v40, v63\n"
    "s_nop 0\n"
    "s_or_b64 s[72:73], s[72:73], vcc\n"
    "s_cbranch_scc0 R_pub%=\n"
    "s_add_u32 s47, s47, 1\n"
    "s_and_b32 s70, s47, 0xff\n"
    "s_cbranch_scc1 R_look%=\n"
    "v_mov_b32 v64, 0\n"
    "global_load_dwordx2 v[66:67], v64, s[64:65] sc1\n"
    "s_sub_u32 s70, s38, 1\n"
    "s_mov_b32 s71, s39\n"
    "s_waitcnt vmcnt(0)\n"
    "v_cmp_eq_u64 vcc, s[70:71], v[66:67]\n"
    "s_nop 1\n"
    "s_cbranch_vccnz R_pub%=\n"
    "s_cmp_lt_u32 s47, 0x40000\n"
    "s_cbranch_scc1 R_look%=\n"
    "v_mov_b32 v66, s70\n"
    "v_mov_b32 v67, s71\n"
    "global_store_dwordx2 v64, v[66:67], s[64:65] sc1\n"
    "s_branch R_pub%=\n"
    :: "s"(ws), "s"(T), "s"(__builtin_amdgcn_readfirstlane(par)), "v"((lane & 3) == 0 ? one : 0u), "v"((lane & 3) == 1 ? one : 0u), "v"((lane & 3) == 2 ? one : 0u), "v"((lane & 3) == 3 ? one : 0u),
       "v"(io), "v"(so), "v"(own + lane * 64), "s"(ws + 204 * MBY + b * 2048)
    : "memory", "vcc", "scc", "s36", "s37", "s38", "s39", "s40", "s62", "s63", "s99", "v32", "v33", "v34", "v35", "v36", "v37", "v38", "v39", "v67");
  __builtin_unreachable();
}

__global__ void __launch_bounds__(256, 1) rec_kernel(char* ws, const u16* __restrict__ x, const u16* __restrict__ mem0, const u16* __restrict__ Wq,
                                                     const u16* __restrict__ Wk, const u16* __restrict__ Wv, const u16* __restrict__ Wo,
                                                     const u16* __restrict__ w_r, const u16* __restrict__ wg, const u16* __restrict__ wu,
                                                     const u16* __restrict__ wd, u16* out) {
  __shared__ __attribute__((aligned(1024))) char lds[158720];
  const int tid = threadIdx.x, lane = tid & 63, wv = tid >> 6;
  TL(0);
  const u64 ep0 = *(const __attribute__((address_space(4))) u64*)(ws + OFF_EP);
  int role;
  {
    unsigned xcc; asm volatile("s_getreg_b32 %0, hwreg(HW_REG_XCC_ID)" : "=s"(xcc));
    role = (int)((blockIdx.x & ~7u) | ((xcc + XROT) & 7u));
    if (tid == 0) {
      const u64 T0 = tag_of(ep0);
      while (__hip_atomic_exchange((gu64*)(ws + OFF_CLM + role * 128), T0, RLX_AGENT) == T0) role = (role + 1) & 255;
      *(int*)(lds + 158704) = role;
    }
    __syncthreads();
    role = __builtin_amdgcn_readfirstlane(*(int*)(lds + 158704));
  }
  const int bx = role & 7, by = role >> 3;
  if (bx >= 4 || by >= 16) {
    const int xc = bx & 3;
    if (bx < 4) {
      const int i = (by - 16) * 4 + bx;
      if (i >= 47) {
        if (tid == 0) probe(ws, i - 47, tag_of(ep0));
        __syncthreads();
      }
      const u64 T = ln1_phase(ws, x, i, ep0, lds, tid);
      const int p = lane >> 4, q = lane & 3;
      unsigned lo = SENT, hi = SENT;
      if (wv == 0) {
        u16 el[4];
#pragma unroll
        for (int k = 0; k < 4; ++k) {
          u16 t = mem0[((i & 1) * 4 + p) * DM + (i >> 1) * 16 + k * 4 + q];
          if ((t & 0x7f80) == 0x7f80 && (t & 0x7f)) t |= 0x40;
          el[k] = t;
        }
        lo = el[0] | ((unsigned)el[1] << 16); hi = el[2] | ((unsigned)el[3] << 16);
      }
      const unsigned par = wait_par(ws, T, lane);
      gu64* gp = (gu64*)(ws + OFF_G3 + wv * 131072 + ((lane >> 2) & 3) * 16384 + ((par >> 16) & 1u) * 8192 + i * 128 + ((p & 1) + (q & 1) * 2 + (p & 2) * 2) * 16 + (q & 2) * 4);
      __hip_atomic_store(gp, ((u64)hi << 32) | lo, RLX_AGENT);
      __hip_atomic_store(gp + 8192, ((u64)hi << 32) | lo, RLX_AGENT);
      asm volatile("s_waitcnt vmcnt(0)" ::: "memory");
      __syncthreads();
      if (wv == 0) {
        if (lane == 0) set_flag(ws, OFF_TAB + i * 128, T, 1u);
        red_phase(ws, i, T, par, lane);
      }
    } else if (by < 8) {
      xa_phase(ws, by & 3, (xc * 2 + (by >> 2)) * 4 + wv, tag_of(ep0), lane);
    } else {
      const int jj = by - 8, h = xc * 8 + (jj & 7), m = (jj & 7) * 4 + xc;
      u64 T;
      if (jj < 8) T = qkv_phase<1>(ws, x, Wk, h, ep0, lds, tid);
      else if (jj < 16) T = qkv_phase<2>(ws, x, Wv, h, ep0, lds, tid);
      else T = qkv_phase<0>(ws, x, Wq, h, ep0, lds, tid);
      TL(2);
      op_phase(ws, x, w_r, out, Wo, jj >> 2, xc, jj & 3, T, lds, tid);
      if (jj >= 16) __syncthreads();
      if (jj < 16) {}
      else if (m < 14) mlp1_phase(ws, x, wg, wu, m, T, lds, tid);
      else if (m >= 16) {
        mlp2_phase(ws, x, wd, out, m - 16, T, lds, tid);
        if (m == 31 && tid == 0) __hip_atomic_store((gu64*)(ws + OFF_EP), T >> 6, RLX_AGENT);
      }
    }
    TL(5);
    return;
  }
  const int wave = __builtin_amdgcn_readfirstlane(wv);
  const unsigned ldsb = (unsigned)(size_t)(LAS char*)lds;
  asm volatile(
    "s_mov_b64 s[36:37], %0\n"
    "s_mov_b32 s38, %1\n"
    "s_mov_b32 s39, %2\n"
    "s_mov_b32 s40, %3\n"
    "s_load_dwordx2 s[96:97], %4, 0x0\n"
    "s_mov_b64 s[22:23], %5\n"
    "s_mov_b64 s[24:25], %6\n"
    "s_mov_b64 s[26:27], %7\n"
    "s_mov_b64 s[28:29], %8\n"
    "s_mov_b64 exec, -1\n"
    "s_mov_b32 s90, 0\n"
    "s_getreg_b32 s10, hwreg(HW_REG_XCC_ID)\n"
    "s_and_b32 s41, s38, 3\n"
    "s_lshl_b32 s41, s41, 3\n"
    "s_bfe_u32 s42, s38, 0x30002\n"
    "s_add_u32 s41, s41, s42\n"
    "s_lshr_b32 s42, s38, 5\n"
    "v_mbcnt_lo_u32_b32 v0, -1, 0\n"
    "v_mbcnt_hi_u32_b32 v0, -1, v0\n"
    "v_and_b32 v1, 15, v0\n"
    "v_lshrrev_b32 v2, 4, v0\n"
    "v_lshlrev_b32 v71, 4, v0\n"
    "s_and_b32 s11, s10, 7\n"
    "s_add_u32 s68, s36, 0x200200\n"
    "s_addc_u32 s69, s37, 0\n"
    "s_lshl_b32 s78, s39, 4\n"
    "v_add_u32 v8, s78, v2\n"
    "v_lshlrev_b32 v12, 1, v2\n"
    "v_xor_b32 v12, v12, v1\n"
    "v_and_b32 v9, 1, v2\n"
    "v_lshlrev_b32 v9, 1, v9\n"
    "v_xor_b32 v9, v9, v1\n"
    "s_and_b32 s79, s39, 1\n"
    "s_lshl_b32 s79, s79, 3\n"
    "v_add_u32 v10, 0, v8\n"
    "v_xor_b32 v11, 0, v12\n"
    "v_lshlrev_b32 v11, 4, v11\n"
    "v_lshl_add_u32 v84, v10, 13, v11\n"
    "v_xor_b32 v11, 0, v9\n"
    "v_xor_b32 v11, s79, v11\n"
    "v_lshlrev_b32 v11, 4, v11\n"
    "v_lshl_add_u32 v88, v10, 10, v11\n"
    "v_add_u32 v10, 4, v8\n"
    "v_xor_b32 v11, 8, v12\n"
    "v_lshlrev_b32 v11, 4, v11\n"
    "v_lshl_add_u32 v85, v10, 13, v11\n"
    "v_xor_b32 v11, 4, v9\n"
    "v_xor_b32 v11, s79, v11\n"
    "v_lshlrev_b32 v11, 4, v11\n"
    "v_lshl_add_u32 v89, v10, 10, v11\n"
    "v_add_u32 v10, 8, v8\n"
    "v_xor_b32 v11, 0, v12\n"
    "v_lshlrev_b32 v11, 4, v11\n"
    "v_lshl_add_u32 v86, v10, 13, v11\n"
    "v_xor_b32 v11, 0, v9\n"
    "v_xor_b32 v11, s79, v11\n"
    "v_lshlrev_b32 v11, 4, v11\n"
    "v_lshl_add_u32 v90, v10, 10, v11\n"
    "v_add_u32 v10, 12, v8\n"
    "v_xor_b32 v11, 8, v12\n"
    "v_lshlrev_b32 v11, 4, v11\n"
    "v_lshl_add_u32 v87, v10, 13, v11\n"
    "v_xor_b32 v11, 4, v9\n"
    "v_xor_b32 v11, s79, v11\n"
    "v_lshlrev_b32 v11, 4, v11\n"
    "v_lshl_add_u32 v91, v10, 10, v11\n"
    "s_lshl_b32 s78, s41, 8\n"
    "s_add_u32 s22, s22, s78\n"
    "s_addc_u32 s23, s23, 0\n"
    "s_add_u32 s24, s24, s78\n"
    "s_addc_u32 s25, s25, 0\n"
    "s_add_u32 s26, s26, s78\n"
    "s_addc_u32 s27, s27, 0\n"
    "s_lshl_b32 s78, s41, 17\n"
    "s_lshl_b32 s79, s42, 9\n"
    "s_add_u32 s78, s78, s79\n"
    "s_add_u32 s28, s28, s78\n"
    "s_addc_u32 s29, s29, 0\n"
    "s_lshl_b32 s30, s39, 12\n"
    "s_add_u32 s30, s30, s40\n"
    "s_add_u32 m0, s30, 0\n"
    "s_nop 0\n"
    "global_load_lds_dwordx4 v88, s[28:29]\n"
    "s_add_u32 m0, s30, 1024\n"
    "s_nop 0\n"
    "global_load_lds_dwordx4 v89, s[28:29]\n"
    "s_add_u32 m0, s30, 2048\n"
    "s_nop 0\n"
    "global_load_lds_dwordx4 v90, s[28:29]\n"
    "s_add_u32 m0, s30, 3072\n"
    "s_nop 0\n"
    "global_load_lds_dwordx4 v91, s[28:29]\n"
    "s_add_u32 s28, s28, 256\n"
    "s_addc_u32 s29, s29, 0\n"
    "s_add_u32 m0, s30, 16384\n"
    "s_nop 0\n"
    "global_load_lds_dwordx4 v88, s[28:29]\n"
    "s_add_u32 m0, s30, 17408\n"
    "s_nop 0\n"
    "global_load_lds_dwordx4 v89, s[28:29]\n"
    "s_add_u32 m0, s30, 18432\n"
    "s_nop 0\n"
    "global_load_lds_dwordx4 v90, s[28:29]\n"
    "s_add_u32 m0, s30, 19456\n"
    "s_nop 0\n"
    "global_load_lds_dwordx4 v91, s[28:29]\n"
    "s_add_u32 s28, s28, 65280\n"
    "s_addc_u32 s29, s29, 0\n"
    "s_add_u32 m0, s30, 32768\n"
    "s_nop 0\n"
    "global_load_lds_dwordx4 v88, s[28:29]\n"
    "s_add_u32 m0, s30, 33792\n"
    "s_nop 0\n"
    "global_load_lds_dwordx4 v89, s[28:29]\n"
    "s_add_u32 m0, s30, 34816\n"
    "s_nop 0\n"
    "global_load_lds_dwordx4 v90, s[28:29]\n"
    "s_add_u32 m0, s30, 35840\n"
    "s_nop 0\n"
    "global_load_lds_dwordx4 v91, s[28:29]\n"
    "s_add_u32 s28, s28, 256\n"
    "s_addc_u32 s29, s29, 0\n"
    "s_add_u32 m0, s30, 49152\n"
    "s_nop 0\n"
    "global_load_lds_dwordx4 v88, s[28:29]\n"
    "s_add_u32 m0, s30, 50176\n"
    "s_nop 0\n"
    "global_load_lds_dwordx4 v89, s[28:29]\n"
    "s_add_u32 m0, s30, 51200\n"
    "s_nop 0\n"
    "global_load_lds_dwordx4 v90, s[28:29]\n"
    "s_add_u32 m0, s30, 52224\n"
    "s_nop 0\n"
    "global_load_lds_dwordx4 v91, s[28:29]\n"
    "s_add_u32 s28, s28, 65280\n"
    "s_addc_u32 s29, s29, 0\n"
    "s_add_u32 m0, s30, 65536\n"
    "s_nop 0\n"
    "global_load_lds_dwordx4 v84, s[24:25]\n"
    "s_add_u32 m0, s30, 66560\n"
    "s_nop 0\n"
    "global_load_lds_dwordx4 v85, s[24:25]\n"
    "s_add_u32 m0, s30, 67584\n"
    "s_nop 0\n"
    "global_load_lds_dwordx4 v86, s[24:25]\n"
    "s_add_u32 m0, s30, 68608\n"
    "s_nop 0\n"
    "global_load_lds_dwordx4 v87, s[24:25]\n"
    "s_add_u32 s24, s24, 0x80000\n"
    "s_addc_u32 s25, s25, 0\n"
    "s_add_u32 m0, s30, 81920\n"
    "s_nop 0\n"
    "global_load_lds_dwordx4 v84, s[24:25]\n"
    "s_add_u32 m0, s30, 82944\n"
    "s_nop 0\n"
    "global_load_lds_dwordx4 v85, s[24:25]\n"
    "s_add_u32 m0, s30, 83968\n"
    "s_nop 0\n"
    "global_load_lds_dwordx4 v86, s[24:25]\n"
    "s_add_u32 m0, s30, 84992\n"
    "s_nop 0\n"
    "global_load_lds_dwordx4 v87, s[24:25]\n"
    "s_add_u32 s24, s24, 0x80000\n"
    "s_addc_u32 s25, s25, 0\n"
    "s_add_u32 m0, s30, 98304\n"
    "s_nop 0\n"
    "global_load_lds_dwordx4 v84, s[24:25]\n"
    "s_add_u32 m0, s30, 99328\n"
    "s_nop 0\n"
    "global_load_lds_dwordx4 v85, s[24:25]\n"
    "s_add_u32 m0, s30, 100352\n"
    "s_nop 0\n"
    "global_load_lds_dwordx4 v86, s[24:25]\n"
    "s_add_u32 m0, s30, 101376\n"
    "s_nop 0\n"
    "global_load_lds_dwordx4 v87, s[24:25]\n"
    "s_add_u32 s24, s24, 0x80000\n"
    "s_addc_u32 s25, s25, 0\n"
    "v_lshrrev_b32 v8, 2, v1\n"
    "v_and_b32 v9, 1, v8\n"
    "v_lshrrev_b32 v10, 1, v8\n"
    "v_and_b32 v11, 1, v2\n"
    "v_and_b32 v12, 3, v1\n"
    "v_lshl_add_u32 v14, v11, 1, v10\n"
    "v_lshl_add_u32 v14, v9, 2, v14\n"
    "v_lshrrev_b32 v13, 1, v2\n"
    "v_lshl_add_u32 v13, v13, 4, v14\n"
    "v_lshlrev_b32 v13, 8, v13\n"
    "v_add_u32 v13, s40, v13\n"
    "v_lshlrev_b32 v14, 5, v14\n"
    "s_lshl_b32 s78, s39, 6\n"
    "v_lshl_add_u32 v15, v12, 3, s78\n"
    "v_xor_b32 v15, v15, v14\n"
    "v_add_u32 v92, v15, v13\n"
    "v_xor_b32 v15, 32, v15\n"
    "v_add_u32 v93, v15, v13\n"
    "s_lshr_b32 s78, s39, 1\n"
    "s_lshl_b32 s78, s78, 7\n"
    "s_and_b32 s79, s39, 1\n"
    "s_lshl_b32 s79, s79, 4\n"
    "s_add_u32 s78, s78, s79\n"
    "v_and_b32 v15, 1, v12\n"
    "v_lshl_add_u32 v15, v15, 5, s78\n"
    "v_lshrrev_b32 v16, 1, v12\n"
    "v_lshl_add_u32 v15, v16, 3, v15\n"
    "v_xor_b32 v15, v15, v14\n"
    "v_add_u32 v96, v15, v13\n"
    "v_xor_b32 v15, 64, v15\n"
    "v_add_u32 v97, v15, v13\n"
    "v_add_u32 v94, 0x10000, v92\n"
    "v_add_u32 v95, 0x10000, v93\n"
    "v_add_u32 v98, 0x10000, v96\n"
    "v_add_u32 v99, 0x10000, v97\n"
    "v_lshl_add_u32 v13, v2, 2, v9\n"
    "v_lshl_add_u32 v13, v10, 4, v13\n"
    "v_lshl_add_u32 v14, v11, 1, v9\n"
    "v_lshl_add_u32 v14, v10, 2, v14\n"
    "v_xor_b32 v14, v14, v12\n"
    "v_lshlrev_b32 v14, 1, v14\n"
    "s_and_b32 s78, s39, 1\n"
    "s_lshl_b32 s78, s78, 3\n"
    "v_xor_b32 v14, s78, v14\n"
    "v_lshlrev_b32 v14, 4, v14\n"
    "v_lshl_add_u32 v14, v13, 8, v14\n"
    "s_lshr_b32 s78, s39, 1\n"
    "s_lshl_b32 s78, s78, 14\n"
    "s_add_u32 s78, s78, s40\n"
    "v_add_u32 v104, s78, v14\n"
    "s_mul_i32 s78, s39, 34816\n"
    "s_add_u32 s78, s78, 18688\n"
    "v_add_u32 v116, s78, v71\n"
    "s_mov_b32 s70, 0xcccccccc\n"
    "s_mov_b32 s71, 0xcccccccc\n"
    "s_mov_b32 s50, 0xffbfffbf\n"
    "s_mov_b32 s72, 0x33333333\n"
    "s_mov_b32 s73, 0x0\n"
    "s_mov_b32 s74, 0x0\n"
    "s_mov_b32 s75, 0x33333333\n"
    "v_mov_b32 v9, 0x4444\n"
    "v_mov_b32 v10, 0xeeee\n"
    "v_cndmask_b32 v67, v9, v10, s[70:71]\n"
    "s_mov_b32 s76, 0x3e0293ee\n"
    "s_mov_b32 s77, 0x3e0293ee\n"
    "v_mov_b32 v80, 0x3f803f80\n"
    "v_mov_b32 v81, 0x3f803f80\n"
    "v_mov_b32 v82, 0x3f803f80\n"
    "v_mov_b32 v83, 0x3f803f80\n"
    "v_lshrrev_b32 v8, 2, v1\n"
    "v_lshlrev_b32 v8, 3, v8\n"
    "v_lshl_add_u32 v8, v2, 8, v8\n"
    "v_and_b32 v9, 1, v2\n"
    "v_lshlrev_b32 v10, 1, v9\n"
    "v_and_b32 v11, 3, v1\n"
    "v_xor_b32 v10, v10, v11\n"
    "v_lshl_add_u32 v13, v10, 5, v8\n"
    "v_lshl_add_u32 v112, v9, 7, v13\n"
    "v_xor_b32 v10, 1, v10\n"
    "v_xor_b32 v9, 1, v9\n"
    "v_lshl_add_u32 v13, v10, 5, v8\n"
    "v_lshl_add_u32 v113, v9, 7, v13\n"
    "v_and_b32 v8, 13, v1\n"
    "v_lshlrev_b32 v8, 4, v8\n"
    "v_lshrrev_b32 v9, 1, v2\n"
    "v_lshl_add_u32 v8, v9, 8, v8\n"
    "v_and_b32 v9, 1, v2\n"
    "v_lshl_add_u32 v8, v9, 3, v8\n"
    "v_add_u32 v9, 1056, v8\n"
    "v_cndmask_b32 v75, v8, v9, s[70:71]\n"
    "v_lshrrev_b32 v8, 2, v1\n"
    "v_lshrrev_b32 v9, 3, v1\n"
    "v_xor_b32 v9, v9, v8\n"
    "v_lshlrev_b32 v9, 7, v9\n"
    "v_and_b32 v10, 3, v1\n"
    "v_lshl_add_u32 v9, v10, 3, v9\n"
    "v_xor_b32 v10, v8, v2\n"
    "v_lshl_add_u32 v114, v10, 5, v9\n"
    "v_and_b32 v10, 1, v2\n"
    "v_lshrrev_b32 v11, 1, v2\n"
    "v_lshl_add_u32 v10, v10, 1, v11\n"
    "v_xor_b32 v10, v8, v10\n"
    "v_lshl_add_u32 v115, v10, 5, v9\n"
    "v_lshrrev_b32 v79, 1, v71\n"
    "s_add_u32 s62, s36, 0x63e0000\n"
    "s_addc_u32 s63, s37, 0\n"
    "s_lshl_b32 s78, s42, 23\n"
    "s_add_u32 s62, s62, s78\n"
    "s_addc_u32 s63, s63, 0\n"
    "s_lshl_b32 s78, s41, 12\n"
    "s_add_u32 s62, s62, s78\n"
    "s_addc_u32 s63, s63, 0\n"
    "s_lshl_b32 s78, s41, 13\n"
    "s_lshl_b32 s79, s39, 10\n"
    "s_add_u32 s78, s78, s79\n"
    "s_add_u32 s58, s36, 0x1500800\n"
    "s_addc_u32 s59, s37, 0\n"
    "s_add_u32 s58, s58, s78\n"
    "s_addc_u32 s59, s59, 0\n"
    "s_add_u32 s78, s78, s79\n"
    "s_add_u32 s60, s36, 0x2500000\n"
    "s_addc_u32 s61, s37, 0\n"
    "s_add_u32 s60, s60, s78\n"
    "s_addc_u32 s61, s61, 0\n"
    "s_add_u32 s12, s36, 0x80000\n"
    "s_addc_u32 s13, s37, 0\n"
    "s_add_u32 s66, s36, 0x202000\n"
    "s_addc_u32 s67, s37, 0\n"
    "s_lshl_b32 s78, s41, 7\n"
    "s_add_u32 s66, s66, s78\n"
    "s_addc_u32 s67, s67, 0\n"
    "s_add_u32 s44, s36, 0x7400000\n"
    "s_addc_u32 s45, s37, 0\n"
    "s_waitcnt lgkmcnt(0)\n"
    "s_add_u32 s96, s96, 1\n"
    "s_addc_u32 s97, s97, 0\n"
    "s_lshl_b64 s[96:97], s[96:97], 6\n"
    "s_waitcnt vmcnt(24) lgkmcnt(0)\n"
    "s_barrier\n"
    "s_add_u32 m0, s30, 114688\n"
    "s_nop 0\n"
    "global_load_lds_dwordx4 v84, s[24:25]\n"
    "s_add_u32 m0, s30, 115712\n"
    "s_nop 0\n"
    "global_load_lds_dwordx4 v85, s[24:25]\n"
    "s_add_u32 m0, s30, 116736\n"
    "s_nop 0\n"
    "global_load_lds_dwordx4 v86, s[24:25]\n"
    "s_add_u32 m0, s30, 117760\n"
    "s_nop 0\n"
    "global_load_lds_dwordx4 v87, s[24:25]\n"
    "s_add_u32 s24, s24, 0x80000\n"
    "s_addc_u32 s25, s25, 0\n"
    "s_waitcnt vmcnt(24) lgkmcnt(0)\n"
    "s_barrier\n"
    "ds_read_b64_tr_b16 v[0:1], v104 offset:0\n"
    "ds_read_b64_tr_b16 v[2:3], v104 offset:512\n"
    "ds_read_b64_tr_b16 v[4:5], v104 offset:8192\n"
    "ds_read_b64_tr_b16 v[6:7], v104 offset:8704\n"
    "ds_read_b64_tr_b16 v[16:17], v104 offset:8\n"
    "ds_read_b64_tr_b16 v[18:19], v104 offset:520\n"
    "ds_read_b64_tr_b16 v[20:21], v104 offset:8200\n"
    "ds_read_b64_tr_b16 v[22:23], v104 offset:8712\n"
    "s_waitcnt lgkmcnt(4)\n"
    "ds_read_b64_tr_b16 v[32:33], v104 offset:16\n"
    "ds_read_b64_tr_b16 v[34:35], v104 offset:528\n"
    "ds_read_b64_tr_b16 v[36:37], v104 offset:8208\n"
    "ds_read_b64_tr_b16 v[38:39], v104 offset:8720\n"
    "ds_read_b64_tr_b16 v[48:49], v104 offset:24\n"
    "ds_read_b64_tr_b16 v[50:51], v104 offset:536\n"
    "ds_read_b64_tr_b16 v[52:53], v104 offset:8216\n"
    "ds_read_b64_tr_b16 v[54:55], v104 offset:8728\n"
    "s_waitcnt vmcnt(20) lgkmcnt(0)\n"
    "s_barrier\n"
    "s_add_u32 m0, s30, 0\n"
    "s_nop 0\n"
    "global_load_lds_dwordx4 v84, s[24:25]\n"
    "s_add_u32 m0, s30, 1024\n"
    "s_nop 0\n"
    "global_load_lds_dwordx4 v85, s[24:25]\n"
    "s_add_u32 m0, s30, 2048\n"
    "s_nop 0\n"
    "global_load_lds_dwordx4 v86, s[24:25]\n"
    "s_add_u32 m0, s30, 3072\n"
    "s_nop 0\n"
    "global_load_lds_dwordx4 v87, s[24:25]\n"
    "s_add_u32 s24, s24, 0x80000\n"
    "s_addc_u32 s25, s25, 0\n"
    "s_add_u32 m0, s30, 16384\n"
    "s_nop 0\n"
    "global_load_lds_dwordx4 v84, s[24:25]\n"
    "s_add_u32 m0, s30, 17408\n"
    "s_nop 0\n"
    "global_load_lds_dwordx4 v85, s[24:25]\n"
    "s_add_u32 m0, s30, 18432\n"
    "s_nop 0\n"
    "global_load_lds_dwordx4 v86, s[24:25]\n"
    "s_add_u32 m0, s30, 19456\n"
    "s_nop 0\n"
    "global_load_lds_dwordx4 v87, s[24:25]\n"
    "s_add_u32 s24, s24, 0x80000\n"
    "s_addc_u32 s25, s25, 0\n"
    "s_waitcnt vmcnt(24) lgkmcnt(0)\n"
    "s_barrier\n"
    "ds_read_b64_tr_b16 v[8:9], v104 offset:32768\n"
    "ds_read_b64_tr_b16 v[10:11], v104 offset:33280\n"
    "ds_read_b64_tr_b16 v[12:13], v104 offset:40960\n"
    "ds_read_b64_tr_b16 v[14:15], v104 offset:41472\n"
    "ds_read_b64_tr_b16 v[24:25], v104 offset:32776\n"
    "ds_read_b64_tr_b16 v[26:27], v104 offset:33288\n"
    "ds_read_b64_tr_b16 v[28:29], v104 offset:40968\n"
    "ds_read_b64_tr_b16 v[30:31], v104 offset:41480\n"
    "s_waitcnt lgkmcnt(4)\n"
    "ds_read_b64_tr_b16 v[40:41], v104 offset:32784\n"
    "ds_read_b64_tr_b16 v[42:43], v104 offset:33296\n"
    "ds_read_b64_tr_b16 v[44:45], v104 offset:40976\n"
    "ds_read_b64_tr_b16 v[46:47], v104 offset:41488\n"
    "ds_read_b64_tr_b16 v[56:57], v104 offset:32792\n"
    "ds_read_b64_tr_b16 v[58:59], v104 offset:33304\n"
    "ds_read_b64_tr_b16 v[60:61], v104 offset:40984\n"
    "ds_read_b64_tr_b16 v[62:63], v104 offset:41496\n"
    "s_waitcnt vmcnt(20) lgkmcnt(0)\n"
    "s_barrier\n"
    "s_add_u32 m0, s30, 32768\n"
    "s_nop 0\n"
    "global_load_lds_dwordx4 v84, s[24:25]\n"
    "s_add_u32 m0, s30, 33792\n"
    "s_nop 0\n"
    "global_load_lds_dwordx4 v85, s[24:25]\n"
    "s_add_u32 m0, s30, 34816\n"
    "s_nop 0\n"
    "global_load_lds_dwordx4 v86, s[24:25]\n"
    "s_add_u32 m0, s30, 35840\n"
    "s_nop 0\n"
    "global_load_lds_dwordx4 v87, s[24:25]\n"
    "s_add_u32 s24, s24, 0x80000\n"
    "s_addc_u32 s25, s25, 0\n"
    "s_add_u32 m0, s30, 49152\n"
    "s_nop 0\n"
    "global_load_lds_dwordx4 v84, s[24:25]\n"
    "s_add_u32 m0, s30, 50176\n"
    "s_nop 0\n"
    "global_load_lds_dwordx4 v85, s[24:25]\n"
    "s_add_u32 m0, s30, 51200\n"
    "s_nop 0\n"
    "global_load_lds_dwordx4 v86, s[24:25]\n"
    "s_add_u32 m0, s30, 52224\n"
    "s_nop 0\n"
    "global_load_lds_dwordx4 v87, s[24:25]\n"
    "s_add_u32 s24, s24, 0x80000\n"
    "s_addc_u32 s25, s25, 0\n"
    "ds_read_b64_tr_b16 a[0:1], v98 offset:0\n"
    "ds_read_b64_tr_b16 a[2:3], v98 offset:2048\n"
    "ds_read_b64_tr_b16 a[4:5], v98 offset:8192\n"
    "ds_read_b64_tr_b16 a[6:7], v98 offset:10240\n"
    "ds_read_b64_tr_b16 a[64:65], v99 offset:0\n"
    "ds_read_b64_tr_b16 a[66:67], v99 offset:2048\n"
    "ds_read_b64_tr_b16 a[68:69], v99 offset:8192\n"
    "ds_read_b64_tr_b16 a[70:71], v99 offset:10240\n"
    "s_waitcnt vmcnt(24) lgkmcnt(0)\n"
    "s_barrier\n"
    "s_add_u32 m0, s30, 65536\n"
    "s_nop 0\n"
    "global_load_lds_dwordx4 v84, s[26:27]\n"
    "s_add_u32 m0, s30, 66560\n"
    "s_nop 0\n"
    "global_load_lds_dwordx4 v85, s[26:27]\n"
    "s_add_u32 m0, s30, 67584\n"
    "s_nop 0\n"
    "global_load_lds_dwordx4 v86, s[26:27]\n"
    "s_add_u32 m0, s30, 68608\n"
    "s_nop 0\n"
    "global_load_lds_dwordx4 v87, s[26:27]\n"
    "s_add_u32 s26, s26, 0x80000\n"
    "s_addc_u32 s27, s27, 0\n"
    "ds_read_b64_tr_b16 a[8:9], v98 offset:16384\n"
    "ds_read_b64_tr_b16 a[10:11], v98 offset:18432\n"
    "ds_read_b64_tr_b16 a[12:13], v98 offset:24576\n"
    "ds_read_b64_tr_b16 a[14:15], v98 offset:26624\n"
    "ds_read_b64_tr_b16 a[72:73], v99 offset:16384\n"
    "ds_read_b64_tr_b16 a[74:75], v99 offset:18432\n"
    "ds_read_b64_tr_b16 a[76:77], v99 offset:24576\n"
    "ds_read_b64_tr_b16 a[78:79], v99 offset:26624\n"
    "s_waitcnt vmcnt(24) lgkmcnt(0)\n"
    "s_barrier\n"
    "s_add_u32 m0, s30, 81920\n"
    "s_nop 0\n"
    "global_load_lds_dwordx4 v84, s[26:27]\n"
    "s_add_u32 m0, s30, 82944\n"
    "s_nop 0\n"
    "global_load_lds_dwordx4 v85, s[26:27]\n"
    "s_add_u32 m0, s30, 83968\n"
    "s_nop 0\n"
    "global_load_lds_dwordx4 v86, s[26:27]\n"
    "s_add_u32 m0, s30, 84992\n"
    "s_nop 0\n"
    "global_load_lds_dwordx4 v87, s[26:27]\n"
    "s_add_u32 s26, s26, 0x80000\n"
    "s_addc_u32 s27, s27, 0\n"
    "ds_read_b64_tr_b16 a[16:17], v98 offset:32768\n"
    "ds_read_b64_tr_b16 a[18:19], v98 offset:34816\n"
    "ds_read_b64_tr_b16 a[20:21], v98 offset:40960\n"
    "ds_read_b64_tr_b16 a[22:23], v98 offset:43008\n"
    "ds_read_b64_tr_b16 a[80:81], v99 offset:32768\n"
    "ds_read_b64_tr_b16 a[82:83], v99 offset:34816\n"
    "ds_read_b64_tr_b16 a[84:85], v99 offset:40960\n"
    "ds_read_b64_tr_b16 a[86:87], v99 offset:43008\n"
    "s_waitcnt vmcnt(24) lgkmcnt(0)\n"
    "s_barrier\n"
    "s_add_u32 m0, s30, 98304\n"
    "s_nop 0\n"
    "global_load_lds_dwordx4 v84, s[26:27]\n"
    "s_add_u32 m0, s30, 99328\n"
    "s_nop 0\n"
    "global_load_lds_dwordx4 v85, s[26:27]\n"
    "s_add_u32 m0, s30, 100352\n"
    "s_nop 0\n"
    "global_load_lds_dwordx4 v86, s[26:27]\n"
    "s_add_u32 m0, s30, 101376\n"
    "s_nop 0\n"
    "global_load_lds_dwordx4 v87, s[26:27]\n"
    "s_add_u32 s26, s26, 0x80000\n"
    "s_addc_u32 s27, s27, 0\n"
    "ds_read_b64_tr_b16 a[24:25], v98 offset:49152\n"
    "ds_read_b64_tr_b16 a[26:27], v98 offset:51200\n"
    "ds_read_b64_tr_b16 a[28:29], v98 offset:57344\n"
    "ds_read_b64_tr_b16 a[30:31], v98 offset:59392\n"
    "ds_read_b64_tr_b16 a[88:89], v99 offset:49152\n"
    "ds_read_b64_tr_b16 a[90:91], v99 offset:51200\n"
    "ds_read_b64_tr_b16 a[92:93], v99 offset:57344\n"
    "ds_read_b64_tr_b16 a[94:95], v99 offset:59392\n"
    "s_waitcnt vmcnt(24) lgkmcnt(0)\n"
    "s_barrier\n"
    "s_add_u32 m0, s30, 114688\n"
    "s_nop 0\n"
    "global_load_lds_dwordx4 v84, s[26:27]\n"
    "s_add_u32 m0, s30, 115712\n"
    "s_nop 0\n"
    "global_load_lds_dwordx4 v85, s[26:27]\n"
    "s_add_u32 m0, s30, 116736\n"
    "s_nop 0\n"
    "global_load_lds_dwordx4 v86, s[26:27]\n"
    "s_add_u32 m0, s30, 117760\n"
    "s_nop 0\n"
    "global_load_lds_dwordx4 v87, s[26:27]\n"
    "s_add_u32 s26, s26, 0x80000\n"
    "s_addc_u32 s27, s27, 0\n"
    "ds_read_b64_tr_b16 a[32:33], v96 offset:0\n"
    "ds_read_b64_tr_b16 a[34:35], v96 offset:2048\n"
    "ds_read_b64_tr_b16 a[36:37], v96 offset:8192\n"
    "ds_read_b64_tr_b16 a[38:39], v96 offset:10240\n"
    "ds_read_b64_tr_b16 a[96:97], v97 offset:0\n"
    "ds_read_b64_tr_b16 a[98:99], v97 offset:2048\n"
    "ds_read_b64_tr_b16 a[100:101], v97 offset:8192\n"
    "ds_read_b64_tr_b16 a[102:103], v97 offset:10240\n"
    "s_waitcnt vmcnt(24) lgkmcnt(0)\n"
    "s_barrier\n"
    "s_add_u32 m0, s30, 0\n"
    "s_nop 0\n"
    "global_load_lds_dwordx4 v84, s[26:27]\n"
    "s_add_u32 m0, s30, 1024\n"
    "s_nop 0\n"
    "global_load_lds_dwordx4 v85, s[26:27]\n"
    "s_add_u32 m0, s30, 2048\n"
    "s_nop 0\n"
    "global_load_lds_dwordx4 v86, s[26:27]\n"
    "s_add_u32 m0, s30, 3072\n"
    "s_nop 0\n"
    "global_load_lds_dwordx4 v87, s[26:27]\n"
    "s_add_u32 s26, s26, 0x80000\n"
    "s_addc_u32 s27, s27, 0\n"
    "ds_read_b64_tr_b16 a[40:41], v96 offset:16384\n"
    "ds_read_b64_tr_b16 a[42:43], v96 offset:18432\n"
    "ds_read_b64_tr_b16 a[44:45], v96 offset:24576\n"
    "ds_read_b64_tr_b16 a[46:47], v96 offset:26624\n"
    "ds_read_b64_tr_b16 a[104:105], v97 offset:16384\n"
    "ds_read_b64_tr_b16 a[106:107], v97 offset:18432\n"
    "ds_read_b64_tr_b16 a[108:109], v97 offset:24576\n"
    "ds_read_b64_tr_b16 a[110:111], v97 offset:26624\n"
    "s_waitcnt vmcnt(24) lgkmcnt(0)\n"
    "s_barrier\n"
    "s_add_u32 m0, s30, 16384\n"
    "s_nop 0\n"
    "global_load_lds_dwordx4 v84, s[26:27]\n"
    "s_add_u32 m0, s30, 17408\n"
    "s_nop 0\n"
    "global_load_lds_dwordx4 v85, s[26:27]\n"
    "s_add_u32 m0, s30, 18432\n"
    "s_nop 0\n"
    "global_load_lds_dwordx4 v86, s[26:27]\n"
    "s_add_u32 m0, s30, 19456\n"
    "s_nop 0\n"
    "global_load_lds_dwordx4 v87, s[26:27]\n"
    "s_add_u32 s26, s26, 0x80000\n"
    "s_addc_u32 s27, s27, 0\n"
    "ds_read_b64_tr_b16 a[48:49], v96 offset:32768\n"
    "ds_read_b64_tr_b16 a[50:51], v96 offset:34816\n"
    "ds_read_b64_tr_b16 a[52:53], v96 offset:40960\n"
    "ds_read_b64_tr_b16 a[54:55], v96 offset:43008\n"
    "ds_read_b64_tr_b16 a[112:113], v97 offset:32768\n"
    "ds_read_b64_tr_b16 a[114:115], v97 offset:34816\n"
    "ds_read_b64_tr_b16 a[116:117], v97 offset:40960\n"
    "ds_read_b64_tr_b16 a[118:119], v97 offset:43008\n"
    "s_waitcnt vmcnt(24) lgkmcnt(0)\n"
    "s_barrier\n"
    "s_add_u32 m0, s30, 32768\n"
    "s_nop 0\n"
    "global_load_lds_dwordx4 v84, s[26:27]\n"
    "s_add_u32 m0, s30, 33792\n"
    "s_nop 0\n"
    "global_load_lds_dwordx4 v85, s[26:27]\n"
    "s_add_u32 m0, s30, 34816\n"
    "s_nop 0\n"
    "global_load_lds_dwordx4 v86, s[26:27]\n"
    "s_add_u32 m0, s30, 35840\n"
    "s_nop 0\n"
    "global_load_lds_dwordx4 v87, s[26:27]\n"
    "s_add_u32 s26, s26, 0x80000\n"
    "s_addc_u32 s27, s27, 0\n"
    "ds_read_b64_tr_b16 a[56:57], v96 offset:49152\n"
    "ds_read_b64_tr_b16 a[58:59], v96 offset:51200\n"
    "ds_read_b64_tr_b16 a[60:61], v96 offset:57344\n"
    "ds_read_b64_tr_b16 a[62:63], v96 offset:59392\n"
    "ds_read_b64_tr_b16 a[120:121], v97 offset:49152\n"
    "ds_read_b64_tr_b16 a[122:123], v97 offset:51200\n"
    "ds_read_b64_tr_b16 a[124:125], v97 offset:57344\n"
    "ds_read_b64_tr_b16 a[126:127], v97 offset:59392\n"
    "s_waitcnt vmcnt(24) lgkmcnt(0)\n"
    "s_barrier\n"
    "s_add_u32 m0, s30, 49152\n"
    "s_nop 0\n"
    "global_load_lds_dwordx4 v84, s[26:27]\n"
    "s_add_u32 m0, s30, 50176\n"
    "s_nop 0\n"
    "global_load_lds_dwordx4 v85, s[26:27]\n"
    "s_add_u32 m0, s30, 51200\n"
    "s_nop 0\n"
    "global_load_lds_dwordx4 v86, s[26:27]\n"
    "s_add_u32 m0, s30, 52224\n"
    "s_nop 0\n"
    "global_load_lds_dwordx4 v87, s[26:27]\n"
    "s_add_u32 s26, s26, 0x80000\n"
    "s_addc_u32 s27, s27, 0\n"
    "ds_read_b64_tr_b16 a[128:129], v94 offset:0\n"
    "ds_read_b64_tr_b16 a[130:131], v94 offset:2048\n"
    "ds_read_b64_tr_b16 a[132:133], v94 offset:8192\n"
    "ds_read_b64_tr_b16 a[134:135], v94 offset:10240\n"
    "ds_read_b64_tr_b16 a[192:193], v95 offset:0\n"
    "ds_read_b64_tr_b16 a[194:195], v95 offset:2048\n"
    "ds_read_b64_tr_b16 a[196:197], v95 offset:8192\n"
    "ds_read_b64_tr_b16 a[198:199], v95 offset:10240\n"
    "s_waitcnt vmcnt(24) lgkmcnt(0)\n"
    "s_barrier\n"
    "s_add_u32 m0, s30, 65536\n"
    "s_nop 0\n"
    "global_load_lds_dwordx4 v84, s[22:23]\n"
    "s_add_u32 m0, s30, 66560\n"
    "s_nop 0\n"
    "global_load_lds_dwordx4 v85, s[22:23]\n"
    "s_add_u32 m0, s30, 67584\n"
    "s_nop 0\n"
    "global_load_lds_dwordx4 v86, s[22:23]\n"
    "s_add_u32 m0, s30, 68608\n"
    "s_nop 0\n"
    "global_load_lds_dwordx4 v87, s[22:23]\n"
    "s_add_u32 s22, s22, 0x80000\n"
    "s_addc_u32 s23, s23, 0\n"
    "ds_read_b64_tr_b16 a[136:137], v94 offset:16384\n"
    "ds_read_b64_tr_b16 a[138:139], v94 offset:18432\n"
    "ds_read_b64_tr_b16 a[140:141], v94 offset:24576\n"
    "ds_read_b64_tr_b16 a[142:143], v94 offset:26624\n"
    "ds_read_b64_tr_b16 a[200:201], v95 offset:16384\n"
    "ds_read_b64_tr_b16 a[202:203], v95 offset:18432\n"
    "ds_read_b64_tr_b16 a[204:205], v95 offset:24576\n"
    "ds_read_b64_tr_b16 a[206:207], v95 offset:26624\n"
    "s_waitcnt vmcnt(24) lgkmcnt(0)\n"
    "s_barrier\n"
    "s_add_u32 m0, s30, 81920\n"
    "s_nop 0\n"
    "global_load_lds_dwordx4 v84, s[22:23]\n"
    "s_add_u32 m0, s30, 82944\n"
    "s_nop 0\n"
    "global_load_lds_dwordx4 v85, s[22:23]\n"
    "s_add_u32 m0, s30, 83968\n"
    "s_nop 0\n"
    "global_load_lds_dwordx4 v86, s[22:23]\n"
    "s_add_u32 m0, s30, 84992\n"
    "s_nop 0\n"
    "global_load_lds_dwordx4 v87, s[22:23]\n"
    "s_add_u32 s22, s22, 0x80000\n"
    "s_addc_u32 s23, s23, 0\n"
    "ds_read_b64_tr_b16 a[144:145], v94 offset:32768\n"
    "ds_read_b64_tr_b16 a[146:147], v94 offset:34816\n"
    "ds_read_b64_tr_b16 a[148:149], v94 offset:40960\n"
    "ds_read_b64_tr_b16 a[150:151], v94 offset:43008\n"
    "ds_read_b64_tr_b16 a[208:209], v95 offset:32768\n"
    "ds_read_b64_tr_b16 a[210:211], v95 offset:34816\n"
    "ds_read_b64_tr_b16 a[212:213], v95 offset:40960\n"
    "ds_read_b64_tr_b16 a[214:215], v95 offset:43008\n"
    "s_waitcnt vmcnt(24) lgkmcnt(0)\n"
    "s_barrier\n"
    "s_add_u32 m0, s30, 98304\n"
    "s_nop 0\n"
    "global_load_lds_dwordx4 v84, s[22:23]\n"
    "s_add_u32 m0, s30, 99328\n"
    "s_nop 0\n"
    "global_load_lds_dwordx4 v85, s[22:23]\n"
    "s_add_u32 m0, s30, 100352\n"
    "s_nop 0\n"
    "global_load_lds_dwordx4 v86, s[22:23]\n"
    "s_add_u32 m0, s30, 101376\n"
    "s_nop 0\n"
    "global_load_lds_dwordx4 v87, s[22:23]\n"
    "s_add_u32 s22, s22, 0x80000\n"
    "s_addc_u32 s23, s23, 0\n"
    "ds_read_b64_tr_b16 a[152:153], v94 offset:49152\n"
    "ds_read_b64_tr_b16 a[154:155], v94 offset:51200\n"
    "ds_read_b64_tr_b16 a[156:157], v94 offset:57344\n"
    "ds_read_b64_tr_b16 a[158:159], v94 offset:59392\n"
    "ds_read_b64_tr_b16 a[216:217], v95 offset:49152\n"
    "ds_read_b64_tr_b16 a[218:219], v95 offset:51200\n"
    "ds_read_b64_tr_b16 a[220:221], v95 offset:57344\n"
    "ds_read_b64_tr_b16 a[222:223], v95 offset:59392\n"
    "s_waitcnt vmcnt(24) lgkmcnt(0)\n"
    "s_barrier\n"
    "s_add_u32 m0, s30, 114688\n"
    "s_nop 0\n"
    "global_load_lds_dwordx4 v84, s[22:23]\n"
    "s_add_u32 m0, s30, 115712\n"
    "s_nop 0\n"
    "global_load_lds_dwordx4 v85, s[22:23]\n"
    "s_add_u32 m0, s30, 116736\n"
    "s_nop 0\n"
    "global_load_lds_dwordx4 v86, s[22:23]\n"
    "s_add_u32 m0, s30, 117760\n"
    "s_nop 0\n"
    "global_load_lds_dwordx4 v87, s[22:23]\n"
    "s_add_u32 s22, s22, 0x80000\n"
    "s_addc_u32 s23, s23, 0\n"
    "ds_read_b64_tr_b16 a[160:161], v92 offset:0\n"
    "ds_read_b64_tr_b16 a[162:163], v92 offset:2048\n"
    "ds_read_b64_tr_b16 a[164:165], v92 offset:8192\n"
    "ds_read_b64_tr_b16 a[166:167], v92 offset:10240\n"
    "ds_read_b64_tr_b16 a[224:225], v93 offset:0\n"
    "ds_read_b64_tr_b16 a[226:227], v93 offset:2048\n"
    "ds_read_b64_tr_b16 a[228:229], v93 offset:8192\n"
    "ds_read_b64_tr_b16 a[230:231], v93 offset:10240\n"
    "s_waitcnt vmcnt(24) lgkmcnt(0)\n"
    "s_barrier\n"
    "s_add_u32 m0, s30, 0\n"
    "s_nop 0\n"
    "global_load_lds_dwordx4 v84, s[22:23]\n"
    "s_add_u32 m0, s30, 1024\n"
    "s_nop 0\n"
    "global_load_lds_dwordx4 v85, s[22:23]\n"
    "s_add_u32 m0, s30, 2048\n"
    "s_nop 0\n"
    "global_load_lds_dwordx4 v86, s[22:23]\n"
    "s_add_u32 m0, s30, 3072\n"
    "s_nop 0\n"
    "global_load_lds_dwordx4 v87, s[22:23]\n"
    "s_add_u32 s22, s22, 0x80000\n"
    "s_addc_u32 s23, s23, 0\n"
    "ds_read_b64_tr_b16 a[168:169], v92 offset:16384\n"
    "ds_read_b64_tr_b16 a[170:171], v92 offset:18432\n"
    "ds_read_b64_tr_b16 a[172:173], v92 offset:24576\n"
    "ds_read_b64_tr_b16 a[174:175], v92 offset:26624\n"
    "ds_read_b64_tr_b16 a[232:233], v93 offset:16384\n"
    "ds_read_b64_tr_b16 a[234:235], v93 offset:18432\n"
    "ds_read_b64_tr_b16 a[236:237], v93 offset:24576\n"
    "ds_read_b64_tr_b16 a[238:239], v93 offset:26624\n"
    "s_waitcnt vmcnt(24) lgkmcnt(0)\n"
    "s_barrier\n"
    "s_add_u32 m0, s30, 16384\n"
    "s_nop 0\n"
    "global_load_lds_dwordx4 v84, s[22:23]\n"
    "s_add_u32 m0, s30, 17408\n"
    "s_nop 0\n"
    "global_load_lds_dwordx4 v85, s[22:23]\n"
    "s_add_u32 m0, s30, 18432\n"
    "s_nop 0\n"
    "global_load_lds_dwordx4 v86, s[22:23]\n"
    "s_add_u32 m0, s30, 19456\n"
    "s_nop 0\n"
    "global_load_lds_dwordx4 v87, s[22:23]\n"
    "s_add_u32 s22, s22, 0x80000\n"
    "s_addc_u32 s23, s23, 0\n"
    "ds_read_b64_tr_b16 a[176:177], v92 offset:32768\n"
    "ds_read_b64_tr_b16 a[178:179], v92 offset:34816\n"
    "ds_read_b64_tr_b16 a[180:181], v92 offset:40960\n"
    "ds_read_b64_tr_b16 a[182:183], v92 offset:43008\n"
    "ds_read_b64_tr_b16 a[240:241], v93 offset:32768\n"
    "ds_read_b64_tr_b16 a[242:243], v93 offset:34816\n"
    "ds_read_b64_tr_b16 a[244:245], v93 offset:40960\n"
    "ds_read_b64_tr_b16 a[246:247], v93 offset:43008\n"
    "s_waitcnt vmcnt(24) lgkmcnt(0)\n"
    "s_barrier\n"
    "s_add_u32 m0, s30, 32768\n"
    "s_nop 0\n"
    "global_load_lds_dwordx4 v84, s[22:23]\n"
    "s_add_u32 m0, s30, 33792\n"
    "s_nop 0\n"
    "global_load_lds_dwordx4 v85, s[22:23]\n"
    "s_add_u32 m0, s30, 34816\n"
    "s_nop 0\n"
    "global_load_lds_dwordx4 v86, s[22:23]\n"
    "s_add_u32 m0, s30, 35840\n"
    "s_nop 0\n"
    "global_load_lds_dwordx4 v87, s[22:23]\n"
    "s_add_u32 s22, s22, 0x80000\n"
    "s_addc_u32 s23, s23, 0\n"
    "ds_read_b64_tr_b16 a[184:185], v92 offset:49152\n"
    "ds_read_b64_tr_b16 a[186:187], v92 offset:51200\n"
    "ds_read_b64_tr_b16 a[188:189], v92 offset:57344\n"
    "ds_read_b64_tr_b16 a[190:191], v92 offset:59392\n"
    "ds_read_b64_tr_b16 a[248:249], v93 offset:49152\n"
    "ds_read_b64_tr_b16 a[250:251], v93 offset:51200\n"
    "ds_read_b64_tr_b16 a[252:253], v93 offset:57344\n"
    "ds_read_b64_tr_b16 a[254:255], v93 offset:59392\n"
    "s_waitcnt vmcnt(24) lgkmcnt(0)\n"
    "s_barrier\n"
    "s_add_u32 m0, s30, 49152\n"
    "s_nop 0\n"
    "global_load_lds_dwordx4 v84, s[22:23]\n"
    "s_add_u32 m0, s30, 50176\n"
    "s_nop 0\n"
    "global_load_lds_dwordx4 v85, s[22:23]\n"
    "s_add_u32 m0, s30, 51200\n"
    "s_nop 0\n"
    "global_load_lds_dwordx4 v86, s[22:23]\n"
    "s_add_u32 m0, s30, 52224\n"
    "s_nop 0\n"
    "global_load_lds_dwordx4 v87, s[22:23]\n"
    "s_add_u32 s22, s22, 0x80000\n"
    "s_addc_u32 s23, s23, 0\n"
    "ds_read_b64_tr_b16 v[128:129], v98 offset:0\n"
    "ds_read_b64_tr_b16 v[130:131], v98 offset:2048\n"
    "ds_read_b64_tr_b16 v[132:133], v98 offset:8192\n"
    "ds_read_b64_tr_b16 v[134:135], v98 offset:10240\n"
    "ds_read_b64_tr_b16 v[136:137], v99 offset:0\n"
    "ds_read_b64_tr_b16 v[138:139], v99 offset:2048\n"
    "ds_read_b64_tr_b16 v[140:141], v99 offset:8192\n"
    "ds_read_b64_tr_b16 v[142:143], v99 offset:10240\n"
    "s_waitcnt vmcnt(24) lgkmcnt(0)\n"
    "s_barrier\n"
    "ds_read_b64_tr_b16 v[144:145], v98 offset:16384\n"
    "ds_read_b64_tr_b16 v[146:147], v98 offset:18432\n"
    "ds_read_b64_tr_b16 v[148:149], v98 offset:24576\n"
    "ds_read_b64_tr_b16 v[150:151], v98 offset:26624\n"
    "ds_read_b64_tr_b16 v[152:153], v99 offset:16384\n"
    "ds_read_b64_tr_b16 v[154:155], v99 offset:18432\n"
    "ds_read_b64_tr_b16 v[156:157], v99 offset:24576\n"
    "ds_read_b64_tr_b16 v[158:159], v99 offset:26624\n"
    "s_waitcnt vmcnt(20) lgkmcnt(0)\n"
    "s_barrier\n"
    "ds_read_b64_tr_b16 v[160:161], v98 offset:32768\n"
    "ds_read_b64_tr_b16 v[162:163], v98 offset:34816\n"
    "ds_read_b64_tr_b16 v[164:165], v98 offset:40960\n"
    "ds_read_b64_tr_b16 v[166:167], v98 offset:43008\n"
    "ds_read_b64_tr_b16 v[168:169], v99 offset:32768\n"
    "ds_read_b64_tr_b16 v[170:171], v99 offset:34816\n"
    "ds_read_b64_tr_b16 v[172:173], v99 offset:40960\n"
    "ds_read_b64_tr_b16 v[174:175], v99 offset:43008\n"
    "s_waitcnt vmcnt(16) lgkmcnt(0)\n"
    "s_barrier\n"
    "ds_read_b64_tr_b16 v[176:177], v98 offset:49152\n"
    "ds_read_b64_tr_b16 v[178:179], v98 offset:51200\n"
    "ds_read_b64_tr_b16 v[180:181], v98 offset:57344\n"
    "ds_read_b64_tr_b16 v[182:183], v98 offset:59392\n"
    "ds_read_b64_tr_b16 v[184:185], v99 offset:49152\n"
    "ds_read_b64_tr_b16 v[186:187], v99 offset:51200\n"
    "ds_read_b64_tr_b16 v[188:189], v99 offset:57344\n"
    "ds_read_b64_tr_b16 v[190:191], v99 offset:59392\n"
    "s_waitcnt vmcnt(12) lgkmcnt(0)\n"
    "s_barrier\n"
    "ds_read_b64_tr_b16 v[192:193], v96 offset:0\n"
    "ds_read_b64_tr_b16 v[194:195], v96 offset:2048\n"
    "ds_read_b64_tr_b16 v[196:197], v96 offset:8192\n"
    "ds_read_b64_tr_b16 v[198:199], v96 offset:10240\n"
    "ds_read_b64_tr_b16 v[200:201], v97 offset:0\n"
    "ds_read_b64_tr_b16 v[202:203], v97 offset:2048\n"
    "ds_read_b64_tr_b16 v[204:205], v97 offset:8192\n"
    "ds_read_b64_tr_b16 v[206:207], v97 offset:10240\n"
    "s_waitcnt vmcnt(8) lgkmcnt(0)\n"
    "s_barrier\n"
    "v_lshlrev_b32 v100, 3, v71\n"
    "v_min_u32 v101, 0x800, v100\n"
    "s_add_u32 s82, s36, 0x20c000\n"
    "s_addc_u32 s83, s37, 0\n"
    "s_add_u32 s84, s36, 0x312000\n"
    "s_addc_u32 s85, s37, 0\n"
    "v_and_b32 v126, 16, v71\n"
    "v_lshlrev_b32 v126, 8, v126\n"
    "global_load_dwordx2 v[84:85], v100, s[82:83] sc1\n"
    "global_load_dwordx2 v[86:87], v101, s[84:85] sc1\n"
    "global_load_dwordx2 v[124:125], v126, s[66:67] sc1\n"
    "ds_read_b64_tr_b16 v[208:209], v96 offset:16384\n"
    "ds_read_b64_tr_b16 v[210:211], v96 offset:18432\n"
    "ds_read_b64_tr_b16 v[212:213], v96 offset:24576\n"
    "ds_read_b64_tr_b16 v[214:215], v96 offset:26624\n"
    "ds_read_b64_tr_b16 v[216:217], v97 offset:16384\n"
    "ds_read_b64_tr_b16 v[218:219], v97 offset:18432\n"
    "ds_read_b64_tr_b16 v[220:221], v97 offset:24576\n"
    "ds_read_b64_tr_b16 v[222:223], v97 offset:26624\n"
    "s_waitcnt vmcnt(7) lgkmcnt(0)\n"
    "s_barrier\n"
    "ds_read_b64_tr_b16 v[224:225], v96 offset:32768\n"
    "ds_read_b64_tr_b16 v[226:227], v96 offset:34816\n"
    "ds_read_b64_tr_b16 v[228:229], v96 offset:40960\n"
    "ds_read_b64_tr_b16 v[230:231], v96 offset:43008\n"
    "ds_read_b64_tr_b16 v[232:233], v97 offset:32768\n"
    "ds_read_b64_tr_b16 v[234:235], v97 offset:34816\n"
    "ds_read_b64_tr_b16 v[236:237], v97 offset:40960\n"
    "ds_read_b64_tr_b16 v[238:239], v97 offset:43008\n"
    "s_waitcnt vmcnt(3) lgkmcnt(0)\n"
    "s_barrier\n"
    "ds_read_b64_tr_b16 v[240:241], v96 offset:49152\n"
    "ds_read_b64_tr_b16 v[242:243], v96 offset:51200\n"
    "ds_read_b64_tr_b16 v[244:245], v96 offset:57344\n"
    "ds_read_b64_tr_b16 v[246:247], v96 offset:59392\n"
    "ds_read_b64_tr_b16 v[248:249], v97 offset:49152\n"
    "ds_read_b64_tr_b16 v[250:251], v97 offset:51200\n"
    "ds_read_b64_tr_b16 v[252:253], v97 offset:57344\n"
    "ds_read_b64_tr_b16 v[254:255], v97 offset:59392\n"
    "s_waitcnt vmcnt(0) lgkmcnt(0)\n"
    "s_barrier\n"
    "v_xor_b32 v124, s96, v124\n"
    "v_xor_b32 v125, s97, v125\n"
    "v_cmp_gt_u64 vcc, 64, v[124:125]\n"
    "s_nop 1\n"
    "v_cndmask_b32 v124, 0, v124, vcc\n"
    "s_nop 0\n"
    "v_readlane_b32 s80, v124, 0\n"
    "v_readlane_b32 s81, v124, 1\n"
    "ds_write_b128 v116, v[60:63] offset:15360\n"
    "ds_write_b128 v116, v[56:59] offset:14336\n"
    "ds_write_b128 v116, v[52:55] offset:13312\n"
    "ds_write_b128 v116, v[48:51] offset:12288\n"
    "ds_write_b128 v116, v[44:47] offset:11264\n"
    "ds_write_b128 v116, v[40:43] offset:10240\n"
    "ds_write_b128 v116, v[36:39] offset:9216\n"
    "ds_write_b128 v116, v[32:35] offset:8192\n"
    "s_waitcnt lgkmcnt(4)\n"
    "ds_write_b128 v116, v[28:31] offset:7168\n"
    "ds_write_b128 v116, v[24:27] offset:6144\n"
    "ds_write_b128 v116, v[20:23] offset:5120\n"
    "ds_write_b128 v116, v[16:19] offset:4096\n"
    "ds_write_b128 v116, v[12:15] offset:3072\n"
    "ds_write_b128 v116, v[8:11] offset:2048\n"
    "ds_write_b128 v116, v[4:7] offset:1024\n"
    "ds_write_b128 v116, v[0:3] offset:0\n"
    "s_waitcnt lgkmcnt(4)\n"
    "s_min_u32 s92, s80, s81\n"
    "s_mov_b32 s43, 0\n"
    "s_mov_b32 s19, 0\n"
    "s_cmp_eq_u32 s92, 0\n"
    "s_cbranch_scc1 L_nokv%=\n"
    "global_load_dwordx4 v[56:59], v71, s[58:59] offset:-2048 sc1\n"
    "global_load_dwordx4 v[60:63], v71, s[58:59] offset:2048 sc1\n"
    "global_load_dwordx2 v[64:65], v71, s[60:61] offset:0 sc1\n"
    "global_load_dwordx2 v[68:69], v71, s[60:61] offset:8 sc1\n"
    "global_load_dwordx2 v[72:73], v71, s[60:61] offset:1024 sc1\n"
    "global_load_dwordx2 v[76:77], v71, s[60:61] offset:1032 sc1\n"
    "s_mov_b32 s19, 1\n"
    "L_nokv%=:\n"
    "s_cmp_lg_u32 s39, 0\n"
    "s_cbranch_scc1 L_sv1%=\n"
    "ds_write_b128 v116, a[0:3] offset:16384\n"
    "ds_write_b128 v116, a[4:7] offset:17408\n"
    "ds_write_b128 v116, a[64:67] offset:18432\n"
    "ds_write_b128 v116, a[68:71] offset:19456\n"
    "ds_write_b128 v116, a[8:11] offset:20480\n"
    "ds_write_b128 v116, a[12:15] offset:21504\n"
    "ds_write_b128 v116, a[72:75] offset:22528\n"
    "ds_write_b128 v116, a[76:79] offset:23552\n"
    "s_waitcnt lgkmcnt(4)\n"
    "ds_write_b128 v116, a[16:19] offset:24576\n"
    "ds_write_b128 v116, a[20:23] offset:25600\n"
    "ds_write_b128 v116, a[80:83] offset:26624\n"
    "ds_write_b128 v116, a[84:87] offset:27648\n"
    "ds_write_b128 v116, a[32:35] offset:28672\n"
    "ds_write_b128 v116, a[36:39] offset:29696\n"
    "ds_write_b128 v116, a[96:99] offset:30720\n"
    "ds_write_b128 v116, a[100:103] offset:31744\n"
    "s_waitcnt lgkmcnt(4)\n"
    "ds_write_b128 v116, v[128:131] offset:32768\n"
    "ds_write_b128 v116, v[132:135] offset:33792\n"
    "s_branch L_sv4%=\n"
    "L_sv1%=:\n"
    "s_cmp_lg_u32 s39, 1\n"
    "s_cbranch_scc1 L_sv2%=\n"
    "ds_write_b128 v116, a[16:19] offset:16384\n"
    "ds_write_b128 v116, a[20:23] offset:17408\n"
    "ds_write_b128 v116, a[80:83] offset:18432\n"
    "ds_write_b128 v116, a[84:87] offset:19456\n"
    "ds_write_b128 v116, a[24:27] offset:20480\n"
    "ds_write_b128 v116, a[28:31] offset:21504\n"
    "ds_write_b128 v116, a[88:91] offset:22528\n"
    "ds_write_b128 v116, a[92:95] offset:23552\n"
    "s_waitcnt lgkmcnt(4)\n"
    "ds_write_b128 v116, a[0:3] offset:24576\n"
    "ds_write_b128 v116, a[4:7] offset:25600\n"
    "ds_write_b128 v116, a[64:67] offset:26624\n"
    "ds_write_b128 v116, a[68:71] offset:27648\n"
    "ds_write_b128 v116, a[48:51] offset:28672\n"
    "ds_write_b128 v116, a[52:55] offset:29696\n"
    "ds_write_b128 v116, a[112:115] offset:30720\n"
    "ds_write_b128 v116, a[116:119] offset:31744\n"
    "s_waitcnt lgkmcnt(4)\n"
    "ds_write_b128 v116, v[160:163] offset:32768\n"
    "ds_write_b128 v116, v[164:167] offset:33792\n"
    "s_branch L_sv4%=\n"
    "L_sv2%=:\n"
    "s_cmp_lg_u32 s39, 2\n"
    "s_cbranch_scc1 L_sv3%=\n"
    "ds_write_b128 v116, a[32:35] offset:16384\n"
    "ds_write_b128 v116, a[36:39] offset:17408\n"
    "ds_write_b128 v116, a[96:99] offset:18432\n"
    "ds_write_b128 v116, a[100:103] offset:19456\n"
    "ds_write_b128 v116, a[40:43] offset:20480\n"
    "ds_write_b128 v116, a[44:47] offset:21504\n"
    "ds_write_b128 v116, a[104:107] offset:22528\n"
    "ds_write_b128 v116, a[108:111] offset:23552\n"
    "s_waitcnt lgkmcnt(4)\n"
    "ds_write_b128 v116, a[48:51] offset:24576\n"
    "ds_write_b128 v116, a[52:55] offset:25600\n"
    "ds_write_b128 v116, a[112:115] offset:26624\n"
    "ds_write_b128 v116, a[116:119] offset:27648\n"
    "ds_write_b128 v116, a[0:3] offset:28672\n"
    "ds_write_b128 v116, a[4:7] offset:29696\n"
    "ds_write_b128 v116, a[64:67] offset:30720\n"
    "ds_write_b128 v116, a[68:71] offset:31744\n"
    "s_waitcnt lgkmcnt(4)\n"
    "ds_write_b128 v116, v[192:195] offset:32768\n"
    "ds_write_b128 v116, v[196:199] offset:33792\n"
    "s_branch L_sv4%=\n"
    "L_sv3%=:\n"
    "ds_write_b128 v116, a[48:51] offset:16384\n"
    "ds_write_b128 v116, a[52:55] offset:17408\n"
    "ds_write_b128 v116, a[112:115] offset:18432\n"
    "ds_write_b128 v116, a[116:119] offset:19456\n"
    "ds_write_b128 v116, a[56:59] offset:20480\n"
    "ds_write_b128 v116, a[60:63] offset:21504\n"
    "ds_write_b128 v116, a[120:123] offset:22528\n"
    "ds_write_b128 v116, a[124:127] offset:23552\n"
    "s_waitcnt lgkmcnt(4)\n"
    "ds_write_b128 v116, a[32:35] offset:24576\n"
    "ds_write_b128 v116, a[36:39] offset:25600\n"
    "ds_write_b128 v116, a[96:99] offset:26624\n"
    "ds_write_b128 v116, a[100:103] offset:27648\n"
    "ds_write_b128 v116, a[16:19] offset:28672\n"
    "ds_write_b128 v116, a[20:23] offset:29696\n"
    "ds_write_b128 v116, a[80:83] offset:30720\n"
    "ds_write_b128 v116, a[84:87] offset:31744\n"
    "s_waitcnt lgkmcnt(4)\n"
    "ds_write_b128 v116, v[224:227] offset:32768\n"
    "ds_write_b128 v116, v[228:231] offset:33792\n"
    "L_sv4%=:\n"
    "v_lshrrev_b32 v0, 4, v71\n"
    "v_and_b32 v1, 15, v0\n"
    "v_lshrrev_b32 v2, 4, v0\n"
    "s_branch L_tabc%=\n"
    "L_tab%=:\n"
    "global_load_dwordx2 v[84:85], v100, s[82:83] sc1\n"
    "global_load_dwordx2 v[86:87], v101, s[84:85] sc1\n"
    "s_waitcnt vmcnt(0)\n"
    "L_tabc%=:\n"
    "v_xor_b32 v84, s96, v84\n"
    "v_xor_b32 v85, s97, v85\n"
    "v_cmp_gt_u64 vcc, 64, v[84:85]\n"
    "s_nop 1\n"
    "v_cndmask_b32 v84, 0, v84, vcc\n"
    "v_xor_b32 v86, s96, v86\n"
    "v_xor_b32 v87, s97, v87\n"
    "v_cmp_gt_u64 vcc, 64, v[86:87]\n"
    "s_nop 1\n"
    "v_cndmask_b32 v86, 0, v86, vcc\n"
    "v_min_u32 v9, v84, v86\n"
    "v_cmp_eq_u32 vcc, 0, v9\n"
    "s_nop 0\n"
    "s_cbranch_vccz L_tabok%=\n"
    "s_sleep 2\n"
    "s_add_u32 s90, s90, 1\n"
    "s_and_b32 s78, s90, 0xff\n"
    "s_cmp_lg_u32 s78, 0\n"
    "s_cbranch_scc1 L_tab%=\n"
    "v_mov_b32 v126, 0\n"
    "global_load_dwordx2 v[126:127], v126, s[68:69] sc1\n"
    "s_waitcnt vmcnt(0)\n"
    "v_cmp_eq_u64 vcc, s[96:97], v[126:127]\n"
    "s_nop 1\n"
    "s_cbranch_vccnz L_bail_tab%=\n"
    "s_cmp_lt_u32 s90, 0x40000\n"
    "s_cbranch_scc1 L_tab%=\n"
    "v_mov_b32 v126, s96\n"
    "v_mov_b32 v127, s97\n"
    "s_mov_b64 exec, 1\n"
    "global_store_dwordx2 v71, v[126:127], s[68:69] sc1\n"
    "s_mov_b64 exec, -1\n"
    "L_bail_tab%=:\n"
    "L_tabok%=:\n"
    "v_cmp_eq_u32 vcc, 2, v86\n"
    "s_nop 0\n"
    "s_mov_b32 s93, vcc_lo\n"
    "s_lshl_b32 s78, s42, 4\n"
    "s_lshl_b32 s79, s39, 2\n"
    "s_add_u32 s78, s78, s79\n"
    "v_lshrrev_b32 v8, 2, v1\n"
    "v_add_u32 v8, s78, v8\n"
    "v_lshlrev_b32 v10, 14, v8\n"
    "v_xor_b32 v8, s41, v8\n"
    "v_lshl_add_u32 v10, v8, 7, v10\n"
    "v_lshrrev_b32 v8, 1, v2\n"
    "v_lshl_add_u32 v10, v8, 12, v10\n"
    "v_and_b32 v8, 1, v2\n"
    "v_lshl_add_u32 v10, v8, 6, v10\n"
    "v_and_b32 v8, 3, v1\n"
    "v_lshl_add_u32 v10, v8, 4, v10\n"
    "s_and_b32 s78, s93, 1\n"
    "s_lshl_b32 s78, s78, 13\n"
    "v_add_u32 v117, s78, v10\n"
    "s_lshl_b32 s78, s42, 5\n"
    "s_add_u32 s78, s78, s41\n"
    "s_lshl_b32 s78, s78, 7\n"
    "s_add_u32 s64, s36, 0x220000\n"
    "s_addc_u32 s65, s37, 0\n"
    "s_add_u32 s64, s64, s78\n"
    "s_addc_u32 s65, s65, 0\n"
    "s_lshr_b32 s81, s93, 16\n"
    "s_and_b32 s81, s81, 1\n"
    "s_lshl_b32 s81, s81, 13\n"
    "s_and_b32 s79, s11, 3\n"
    "s_lshl_b32 s78, s42, 2\n"
    "s_add_u32 s79, s79, s78\n"
    "s_lshl_b32 s79, s79, 14\n"
    "s_add_u32 s78, s81, s79\n"
    "s_add_u32 s54, s36, 0x0\n"
    "s_addc_u32 s55, s37, 0\n"
    "s_add_u32 s54, s54, s78\n"
    "s_addc_u32 s55, s55, 0\n"
    "s_lshl_b32 s80, s39, 11\n"
    "s_add_u32 s54, s54, s80\n"
    "s_addc_u32 s55, s55, 0\n"
    "s_mov_b64 s[48:49], s[54:55]\n"
    "s_lshl_b32 s79, s38, 6\n"
    "s_add_u32 s79, s79, s81\n"
    "s_add_u32 s56, s12, s79\n"
    "s_addc_u32 s57, s13, 0\n"
    "s_and_b32 s78, s78, 0xffff\n"
    "s_add_u32 s12, s12, s78\n"
    "s_addc_u32 s13, s13, 0\n"
    "v_lshlrev_b32 v118, 10, v71\n"
    "v_lshlrev_b32 v119, 2, v71\n"
    "s_mov_b32 s43, 0\n"
    "v_mov_b64 v[104:105], 0\n"
    "v_mov_b64 v[106:107], 0\n"
    "v_mov_b64 v[108:109], 0\n"
    "v_mov_b64 v[110:111], 0\n"
    "v_mov_b64 v[32:33], 0\n"
    "v_mov_b64 v[34:35], 0\n"
    "v_mov_b64 v[36:37], 0\n"
    "v_mov_b64 v[38:39], 0\n"
    "v_mov_b64 v[40:41], 0\n"
    "v_mov_b64 v[42:43], 0\n"
    "v_mov_b64 v[44:45], 0\n"
    "v_mov_b64 v[46:47], 0\n"
    "v_mov_b64 v[48:49], 0\n"
    "v_mov_b64 v[50:51], 0\n"
    "v_mov_b64 v[52:53], 0\n"
    "v_mov_b64 v[54:55], 0\n"
    "s_cmp_lg_u32 s19, 0\n"
    "s_cbranch_scc1 L_kvdone%=\n"
    "s_add_u32 s79, s43, 1\n"
    "s_min_u32 s79, s79, 63\n"
    "s_lshr_b32 s79, s79, 1\n"
    "s_cmp_lt_u32 s79, s92\n"
    "s_cbranch_scc1 L_cntokp%=\n"
    "L_cntp%=:\n"
    "v_and_b32 v126, 16, v71\n"
    "v_lshlrev_b32 v126, 8, v126\n"
    "global_load_dwordx2 v[126:127], v126, s[66:67] sc1\n"
    "s_waitcnt vmcnt(0)\n"
    "v_xor_b32 v126, s96, v126\n"
    "v_xor_b32 v127, s97, v127\n"
    "v_cmp_gt_u64 vcc, 64, v[126:127]\n"
    "s_nop 1\n"
    "v_cndmask_b32 v126, 0, v126, vcc\n"
    "s_nop 0\n"
    "v_readlane_b32 s80, v126, 0\n"
    "v_readlane_b32 s81, v126, 1\n"
    "s_nop 1\n"
    "s_min_u32 s92, s80, s81\n"
    "s_cmp_lt_u32 s79, s92\n"
    "s_cbranch_scc1 L_cntokp%=\n"
    "s_sleep 2\n"
    "s_add_u32 s90, s90, 1\n"
    "s_and_b32 s78, s90, 0xff\n"
    "s_cmp_lg_u32 s78, 0\n"
    "s_cbranch_scc1 L_cntp%=\n"
    "v_mov_b32 v126, 0\n"
    "global_load_dwordx2 v[126:127], v126, s[68:69] sc1\n"
    "s_waitcnt vmcnt(0)\n"
    "v_cmp_eq_u64 vcc, s[96:97], v[126:127]\n"
    "s_nop 1\n"
    "s_cbranch_vccnz L_bail_cntp%=\n"
    "s_cmp_lt_u32 s90, 0x40000\n"
    "s_cbranch_scc1 L_cntp%=\n"
    "v_mov_b32 v126, s96\n"
    "v_mov_b32 v127, s97\n"
    "s_mov_b64 exec, 1\n"
    "global_store_dwordx2 v71, v[126:127], s[68:69] sc1\n"
    "s_mov_b64 exec, -1\n"
    "L_bail_cntp%=:\n"
    "L_cntokp%=:\n"
    "global_load_dwordx4 v[56:59], v71, s[58:59] offset:-2048 sc1\n"
    "global_load_dwordx4 v[60:63], v71, s[58:59] offset:2048 sc1\n"
    "global_load_dwordx2 v[64:65], v71, s[60:61] offset:0 sc1\n"
    "global_load_dwordx2 v[68:69], v71, s[60:61] offset:8 sc1\n"
    "global_load_dwordx2 v[72:73], v71, s[60:61] offset:1024 sc1\n"
    "global_load_dwordx2 v[76:77], v71, s[60:61] offset:1032 sc1\n"
    "L_kvdone%=:\n"
    "s_add_u32 s58, s58, 0x40000\n"
    "s_addc_u32 s59, s59, 0\n"
    "s_add_u32 s60, s60, 0x40000\n"
    "s_addc_u32 s61, s61, 0\n"
    "s_waitcnt lgkmcnt(0)\n"
    "s_memtime s[6:7]\n"
    "s_memrealtime s[78:79]\n"
    "s_nop 3\n"
    "s_memrealtime s[80:81]\n"
    "s_nop 3\n"
    "s_memrealtime s[82:83]\n"
    "s_nop 3\n"
    "s_memrealtime s[84:85]\n"
    "s_nop 3\n"
    "s_memrealtime s[86:87]\n"
    "s_nop 3\n"
    "s_memrealtime s[88:89]\n"
    "s_mov_b32 s10, 0\n"
    "s_waitcnt lgkmcnt(0)\n"
    "s_mul_i32 s91, s78, 24\n"
    "s_cmp_eq_u32 s78, s80\n"
    "s_addc_u32 s10, s10, 0\n"
    "s_cmp_eq_u32 s78, s82\n"
    "s_addc_u32 s10, s10, 0\n"
    "s_cmp_eq_u32 s78, s84\n"
    "s_addc_u32 s10, s10, 0\n"
    "s_cmp_eq_u32 s78, s86\n"
    "s_addc_u32 s10, s10, 0\n"
    "s_cmp_eq_u32 s78, s88\n"
    "s_addc_u32 s10, s10, 0\n"
    "s_mul_i32 s10, s10, 20\n"
    "s_sub_u32 s91, s91, s6\n"
    "s_sub_u32 s91, s91, s10\n"
    "s_mov_b32 s94, 0\n"
    "s_mov_b32 s51, 1900\n"
    "s_mov_b32 s16, s6\n"
    "s_add_u32 s46, s91, 2400\n"
    "s_cmp_lt_u32 s43, 29\n"
    "s_cselect_b32 s79, 100, 0\n"
    "s_cselect_b32 s95, 20, 0\n"
    "s_add_u32 s95, s95, 1080\n"
    "s_add_u32 s46, s46, s79\n"
    "s_mov_b32 s47, s91\n"
    "s_mov_b32 s18, s16\n"
    "s_getpc_b64 s[4:5]\n"
    "L_pc%=:\n"
    "s_add_u32 s4, s4, L_sled%=-L_pc%=\n"
    "s_addc_u32 s5, s5, 0\n"
    "v_or_b32 v84, 1, s96\n"
    "v_mov_b32 v85, s97\n"
    "v_mov_b32 v86, s43\n"
    "s_cmp_eq_u32 s39, 1\n"
    "s_cbranch_scc1 L_poll1%=\n"
    "s_cmp_eq_u32 s39, 2\n"
    "s_cbranch_scc1 L_poll2%=\n"
    "s_cmp_eq_u32 s39, 3\n"
    "s_cbranch_scc1 L_poll3%=\n"
    "s_branch L_poll0%=\n"
    ".p2align 6\n"
    "L_poll0%=:\n"
    "global_load_dwordx4 v[0:3], v71, s[54:55] offset:0 sc1\n"
    "global_load_dwordx4 v[4:7], v71, s[54:55] offset:1024 sc1\n"
    "s_waitcnt vmcnt(0)\n"
    "s_branch L_top0%=\n"
    ".p2align 6\n"
    "L_top0%=:\n"
    "s_waitcnt vmcnt(3)\n"
    "v_smfmac_f32_16x16x64_bf16 v[40:43], v[0:3], a[0:7], v67\n"
    "ds_write_b128 v71, v[0:3] offset:0\n"
    "v_smfmac_f32_16x16x64_bf16 v[44:47], v[0:3], a[64:71], v67\n"
    "v_smfmac_f32_16x16x64_bf16 v[32:35], v[0:3], v[128:135], v67\n"
    "v_smfmac_f32_16x16x64_bf16 v[36:39], v[0:3], v[136:143], v67\n"
    "s_waitcnt vmcnt(2)\n"
    "v_smfmac_f32_16x16x64_bf16 v[40:43], v[4:7], a[8:15], v67\n"
    "ds_write_b128 v71, v[4:7] offset:1024\n"
    "s_waitcnt lgkmcnt(1)\n"
    "s_barrier\n"
    "ds_read_b128 v[8:11], v71 offset:2048\n"
    "v_smfmac_f32_16x16x64_bf16 v[44:47], v[4:7], a[72:79], v67\n"
    "ds_read_b128 v[16:19], v71 offset:4096\n"
    "v_smfmac_f32_16x16x64_bf16 v[32:35], v[4:7], v[144:151], v67\n"
    "ds_read_b128 v[24:27], v71 offset:6144\n"
    "v_smfmac_f32_16x16x64_bf16 v[36:39], v[4:7], v[152:159], v67\n"
    "s_waitcnt lgkmcnt(3)\n"
    "s_barrier\n"
    "v_smfmac_f32_16x16x64_bf16 v[48:51], v[0:3], a[128:135], v67\n"
    "s_waitcnt lgkmcnt(2)\n"
    "ds_read_b128 v[12:15], v71 offset:3072\n"
    "v_smfmac_f32_16x16x64_bf16 v[32:35], v[8:11], v[160:167], v67\n"
    "ds_read_b128 v[20:23], v71 offset:5120\n"
    "v_smfmac_f32_16x16x64_bf16 v[36:39], v[8:11], v[168:175], v67\n"
    "s_waitcnt lgkmcnt(2)\n"
    "ds_read_b128 v[28:31], v71 offset:7168\n"
    "v_smfmac_f32_16x16x64_bf16 v[32:35], v[16:19], v[192:199], v67\n"
    "ds_read_b128 a[0:3], v116 offset:0\n"
    "v_smfmac_f32_16x16x64_bf16 v[36:39], v[16:19], v[200:207], v67\n"
    "ds_read_b128 a[4:7], v116 offset:1024\n"
    "v_smfmac_f32_16x16x64_bf16 v[32:35], v[24:27], v[224:231], v67\n"
    "ds_read_b128 a[64:67], v116 offset:4096\n"
    "v_smfmac_f32_16x16x64_bf16 v[36:39], v[24:27], v[232:239], v67\n"
    "s_waitcnt lgkmcnt(5)\n"
    "ds_read_b128 a[68:71], v116 offset:5120\n"
    "v_smfmac_f32_16x16x64_bf16 v[32:35], v[12:15], v[176:183], v67\n"
    "ds_read_b128 a[8:11], v116 offset:2048\n"
    "v_smfmac_f32_16x16x64_bf16 v[36:39], v[12:15], v[184:191], v67\n"
    "s_waitcnt lgkmcnt(5)\n"
    "ds_read_b128 a[12:15], v116 offset:3072\n"
    "v_smfmac_f32_16x16x64_bf16 v[32:35], v[20:23], v[208:215], v67\n"
    "ds_read_b128 a[72:75], v116 offset:6144\n"
    "v_smfmac_f32_16x16x64_bf16 v[36:39], v[20:23], v[216:223], v67\n"
    "ds_read_b128 a[76:79], v116 offset:7168\n"
    "v_smfmac_f32_16x16x64_bf16 v[32:35], v[28:31], v[240:247], v67\n"
    "v_smfmac_f32_16x16x64_bf16 v[36:39], v[28:31], v[248:255], v67\n"
    "v_smfmac_f32_16x16x64_bf16 v[40:43], v[8:11], a[16:23], v67\n"
    "v_add_f32 v32, v32, v34\n"
    "v_add_f32 v33, v33, v35\n"
    "v_smfmac_f32_16x16x64_bf16 v[44:47], v[8:11], a[80:87], v67\n"
    "ds_read_b128 a[16:19], v116 offset:8192\n"
    "v_cvt_pk_bf16_f32 v126, v32, v33\n"
    "v_add_f32 v36, v36, v38\n"
    "v_add_f32 v37, v37, v39\n"
    "v_cvt_pk_bf16_f32 v127, v36, v37\n"
    "v_smfmac_f32_16x16x64_bf16 v[40:43], v[16:19], a[32:39], v67\n"
    "ds_write_b64 v114, v[126:127] offset:8192\n"
    "v_smfmac_f32_16x16x64_bf16 v[44:47], v[16:19], a[96:103], v67\n"
    "ds_read_b128 a[20:23], v116 offset:9216\n"
    "v_cmp_u_f32 vcc, v32, v33\n"
    "v_smfmac_f32_16x16x64_bf16 v[40:43], v[24:27], a[48:55], v67\n"
    "ds_read_b128 a[80:83], v116 offset:12288\n"
    "v_smfmac_f32_16x16x64_bf16 v[44:47], v[24:27], a[112:119], v67\n"
    "s_cbranch_vccnz L_slow0%=\n"
    "L_go0%=:\n"
    "v_smfmac_f32_16x16x64_bf16 v[40:43], v[12:15], a[24:31], v67\n"
    "s_waitcnt lgkmcnt(2)\n"
    "s_barrier\n"
    "ds_read_b64_tr_b16 v[32:33], v112 offset:8192\n"
    "v_smfmac_f32_16x16x64_bf16 v[44:47], v[12:15], a[88:95], v67\n"
    "ds_read_b64_tr_b16 v[34:35], v113 offset:8192\n"
    "ds_read_b64_tr_b16 v[36:37], v112 offset:9216\n"
    "v_smfmac_f32_16x16x64_bf16 v[40:43], v[20:23], a[40:47], v67\n"
    "ds_read_b64_tr_b16 v[38:39], v113 offset:9216\n"
    "v_smfmac_f32_16x16x64_bf16 v[44:47], v[20:23], a[104:111], v67\n"
    "global_load_dwordx4 v[132:135], v71, s[60:61] offset:1024 sc1\n"
    "v_smfmac_f32_16x16x64_bf16 v[40:43], v[28:31], a[56:63], v67\n"
    "ds_read_b128 a[84:87], v116 offset:13312\n"
    "v_smfmac_f32_16x16x64_bf16 v[44:47], v[28:31], a[120:127], v67\n"
    "s_waitcnt lgkmcnt(1)\n"
    "ds_read_b128 a[32:35], v116 offset:10240\n"
    "v_mfma_f32_16x16x32_bf16 v[88:91], v[56:59], v[32:35], 0\n"
    "v_add_f32 v40, v40, v42\n"
    "v_add_f32 v41, v41, v43\n"
    "v_mfma_f32_16x16x32_bf16 v[88:91], v[60:63], v[36:39], v[88:91]\n"
    "v_cvt_pk_bf16_f32 v126, v40, v41\n"
    "v_add_f32 v44, v44, v46\n"
    "v_add_f32 v45, v45, v47\n"
    "v_cvt_pk_bf16_f32 v127, v44, v45\n"
    "v_smfmac_f32_16x16x64_bf16 v[52:55], v[0:3], a[192:199], v67\n"
    "ds_write_b64 v115, v[126:127] offset:10240\n"
    "v_add_f32_dpp v88, v90, v88 quad_perm:[2,3,2,3] row_mask:0xf bank_mask:0xf\n"
    "v_add_f32_dpp v89, v91, v89 quad_perm:[2,3,2,3] row_mask:0xf bank_mask:0xf\n"
    "v_smfmac_f32_16x16x64_bf16 v[48:51], v[4:7], a[136:143], v67\n"
    "ds_write_b64 v75, v[88:89] offset:14336\n"
    "v_smfmac_f32_16x16x64_bf16 v[52:55], v[4:7], a[200:207], v67\n"
    "global_load_dwordx4 v[56:59], v71, s[58:59] offset:-2048 sc1\n"
    "v_smfmac_f32_16x16x64_bf16 v[48:51], v[8:11], a[144:151], v67\n"
    "global_load_dwordx4 v[60:63], v71, s[58:59] offset:2048 sc1\n"
    "v_smfmac_f32_16x16x64_bf16 v[52:55], v[8:11], a[208:215], v67\n"
    "global_load_dwordx4 v[128:131], v71, s[60:61] offset:0 sc1\n"
    "v_smfmac_f32_16x16x64_bf16 v[48:51], v[16:19], a[160:167], v67\n"
    "s_waitcnt lgkmcnt(0)\n"
    "s_barrier\n"
    "ds_read_b64_tr_b16 v[40:41], v112 offset:10240\n"
    "v_smfmac_f32_16x16x64_bf16 v[52:55], v[16:19], a[224:231], v67\n"
    "ds_read_b64_tr_b16 v[42:43], v113 offset:10240\n"
    "ds_read_b64_tr_b16 v[44:45], v112 offset:11264\n"
    "v_smfmac_f32_16x16x64_bf16 v[48:51], v[24:27], a[176:183], v67\n"
    "ds_read_b64_tr_b16 v[46:47], v113 offset:11264\n"
    "v_smfmac_f32_16x16x64_bf16 v[52:55], v[24:27], a[240:247], v67\n"
    "ds_read_b128 v[88:91], v71 offset:14336\n"
    "s_cmp_eq_u32 s43, 63\n"
    "v_smfmac_f32_16x16x64_bf16 v[48:51], v[12:15], a[152:159], v67\n"
    "s_waitcnt lgkmcnt(3)\n"
    "ds_read_b128 a[36:39], v116 offset:11264\n"
    "v_mfma_f32_16x16x32_bf16 v[96:99], v[40:43], v[32:35], 0\n"
    "s_waitcnt lgkmcnt(2)\n"
    "ds_read_b128 a[96:99], v116 offset:14336\n"
    "v_mfma_f32_16x16x32_bf16 v[96:99], v[44:47], v[36:39], v[96:99]\n"
    "s_cbranch_scc1 L_last0%=\n"
    "v_smfmac_f32_16x16x64_bf16 v[52:55], v[12:15], a[216:223], v67\n"
    "s_waitcnt lgkmcnt(2)\n"
    "ds_read_b128 a[100:103], v116 offset:15360\n"
    "v_max3_f32 v122, v88, v89, v90\n"
    "v_max_f32 v122, v122, v91\n"
    "v_add_f32_dpp v96, v98, v96 quad_perm:[2,3,2,3] row_mask:0xf bank_mask:0xf\n"
    "v_add_f32_dpp v97, v99, v97 quad_perm:[2,3,2,3] row_mask:0xf bank_mask:0xf\n"
    "v_max_f32_dpp v123, v122, v122 quad_perm:[2,3,0,1] row_mask:0xf bank_mask:0xf\n"
    "v_max3_f32 v122, v123, v96, v97\n"
    "v_smfmac_f32_16x16x64_bf16 v[48:51], v[20:23], a[168:175], v67\n"
    "v_max_f32_dpp v121, v122, v123 quad_perm:[0,1,0,1] row_mask:0xf bank_mask:0xf\n"
    "v_max_f32_dpp v123, v122, v123 quad_perm:[0,1,0,1] row_mask:0xf bank_mask:0xf\n"
    "v_smfmac_f32_16x16x64_bf16 v[52:55], v[20:23], a[232:239], v67\n"
    "v_permlane16_swap_b32 v123, v121\n"
    "v_max_f32 v122, v123, v121\n"
    "v_max_f32 v123, v123, v121\n"
    "v_smfmac_f32_16x16x64_bf16 v[48:51], v[28:31], a[184:191], v67\n"
    "v_smfmac_f32_16x16x64_bf16 v[52:55], v[28:31], a[248:255], v67\n"
    "v_permlane32_swap_b32 v122, v123\n"
    "v_max_f32 v122, v122, v123\n"
    "v_mul_f32 v122, s76, v122\n"
    "v_pk_fma_f32 v[96:97], v[96:97], s[76:77], v[122:123] op_sel_hi:[1,1,0] neg_lo:[0,0,1] neg_hi:[0,0,1]\n"
    "v_pk_fma_f32 v[88:89], v[88:89], s[76:77], v[122:123] op_sel_hi:[1,1,0] neg_lo:[0,0,1] neg_hi:[0,0,1]\n"
    "v_pk_fma_f32 v[90:91], v[90:91], s[76:77], v[122:123] op_sel_hi:[1,1,0] neg_lo:[0,0,1] neg_hi:[0,0,1]\n"
    "v_exp_f32 v96, v96\n"
    "v_exp_f32 v97, v97\n"
    "v_exp_f32 v88, v88\n"
    "v_exp_f32 v89, v89\n"
    "v_exp_f32 v90, v90\n"
    "v_exp_f32 v91, v91\n"
    "v_pk_add_f32 v[48:49], v[48:49], v[50:51]\n"
    "v_pk_add_f32 v[52:53], v[52:53], v[54:55]\n"
    "v_cvt_pk_bf16_f32 v126, v96, v97\n"
    "v_cvt_pk_bf16_f32 v100, v88, v89\n"
    "v_cvt_pk_bf16_f32 v102, v90, v91\n"
    "v_cndmask_b32 v101, 0, v126, s[72:73]\n"
    "v_cndmask_b32 v103, 0, v126, s[74:75]\n"
    "v_cvt_pk_bf16_f32 v66, v48, v49\n"
    "v_cvt_pk_bf16_f32 v70, v48, v49\n"
    "v_cvt_pk_bf16_f32 v74, v52, v53\n"
    "v_cvt_pk_bf16_f32 v78, v52, v53\n"
    "v_smfmac_f32_16x16x64_bf16 v[104:107], v[100:103], v[64:71], v67\n"
    "v_smfmac_f32_16x16x64_bf16 v[108:111], v[100:103], v[72:79], v67\n"
    "v_mfma_f32_16x16x32_bf16 v[84:87], v[100:103], v[80:83], 0\n"
    "v_add_f32 v104, v104, v106\n"
    "v_add_f32 v105, v105, v107\n"
    "v_cvt_pk_bf16_f32 v104, v104, v105\n"
    "v_add_f32 v108, v108, v110\n"
    "v_add_f32 v109, v109, v111\n"
    "v_cvt_pk_bf16_f32 v105, v108, v109\n"
    "ds_write_b64 v114, v[104:105] offset:12288\n"
    "v_pk_add_f32 v[124:125], v[84:85], v[86:87]\n"
    "v_rcp_f32 v124, v124\n"
    "v_rcp_f32 v125, v125\n"
    "v_mov_b64 v[0:1], 0\n"
    "v_mov_b64 v[2:3], 0\n"
    "v_mov_b64 v[4:5], 0\n"
    "v_mov_b64 v[6:7], 0\n"
    "v_mov_b64 v[8:9], 0\n"
    "v_mov_b64 v[10:11], 0\n"
    "v_mov_b64 v[12:13], 0\n"
    "v_mov_b64 v[14:15], 0\n"
    "v_mfma_f32_16x16x32_bf16 v[96:99], v[100:103], v[80:83], 0\n"
    "s_waitcnt lgkmcnt(0)\n"
    "s_barrier\n"
    "ds_read_b64_tr_b16 v[32:33], v112 offset:12288\n"
    "v_mfma_f32_16x16x32_bf16 v[96:99], v[100:103], v[80:83], 0\n"
    "ds_read_b64_tr_b16 v[34:35], v113 offset:12288\n"
    "ds_read_b64_tr_b16 v[36:37], v112 offset:13312\n"
    "v_mfma_f32_16x16x32_bf16 v[96:99], v[100:103], v[80:83], 0\n"
    "ds_read_b64_tr_b16 v[38:39], v113 offset:13312\n"
    "v_mfma_f32_16x16x32_bf16 v[96:99], v[100:103], v[80:83], 0\n"
    "s_waitcnt lgkmcnt(2)\n"
    "v_smfmac_f32_16x16x64_bf16 v[0:3], v[32:35], a[0:7], v67\n"
    "v_smfmac_f32_16x16x64_bf16 v[4:7], v[32:35], a[64:71], v67\n"
    "s_waitcnt lgkmcnt(0)\n"
    "v_smfmac_f32_16x16x64_bf16 v[0:3], v[36:39], a[8:15], v67\n"
    "v_smfmac_f32_16x16x64_bf16 v[4:7], v[36:39], a[72:79], v67\n"
    "v_smfmac_f32_16x16x64_bf16 v[8:11], v[32:35], a[16:23], v67\n"
    "v_add_f32 v0, v0, v2\n"
    "v_smfmac_f32_16x16x64_bf16 v[12:15], v[32:35], a[80:87], v67\n"
    "v_add_f32 v1, v1, v3\n"
    "v_smfmac_f32_16x16x64_bf16 v[8:11], v[36:39], a[32:39], v67\n"
    "v_add_f32 v4, v4, v6\n"
    "v_smfmac_f32_16x16x64_bf16 v[12:15], v[36:39], a[96:103], v67\n"
    "v_add_f32 v5, v5, v7\n"
    "v_mul_f32 v0, v0, v124\n"
    "v_mul_f32 v1, v1, v125\n"
    "v_cvt_pk_bf16_f32 v88, v0, v1\n"
    "v_pk_mul_f32 v[4:5], v[4:5], v[124:125]\n"
    "v_pk_add_f32 v[8:9], v[8:9], v[10:11]\n"
    "v_pk_add_f32 v[12:13], v[12:13], v[14:15]\n"
    "v_cvt_pk_bf16_f32 v89, v4, v5\n"
    "v_pk_mul_f32 v[8:9], v[8:9], v[124:125]\n"
    "v_pk_mul_f32 v[12:13], v[12:13], v[124:125]\n"
    "v_cvt_pk_bf16_f32 v90, v8, v9\n"
    "v_cvt_pk_bf16_f32 v91, v12, v13\n"
    "global_store_dwordx4 v117, v[88:91], s[44:45] sc1\n"
    "s_add_u32 s43, s43, 1\n"
    "s_sleep 3\n"
    "ds_read_b128 a[0:3], v116 offset:16384\n"
    "ds_read_b128 a[4:7], v116 offset:17408\n"
    "ds_read_b128 a[64:67], v116 offset:18432\n"
    "ds_read_b128 a[68:71], v116 offset:19456\n"
    "ds_read_b128 a[8:11], v116 offset:20480\n"
    "ds_read_b128 a[12:15], v116 offset:21504\n"
    "ds_read_b128 a[72:75], v116 offset:22528\n"
    "ds_read_b128 a[76:79], v116 offset:23552\n"
    "ds_read_b128 a[16:19], v116 offset:24576\n"
    "ds_read_b128 a[20:23], v116 offset:25600\n"
    "ds_read_b128 a[80:83], v116 offset:26624\n"
    "ds_read_b128 a[84:87], v116 offset:27648\n"
    "ds_read_b128 a[32:35], v116 offset:28672\n"
    "s_waitcnt lgkmcnt(8)\n"
    "ds_read_b128 a[36:39], v116 offset:29696\n"
    "ds_read_b128 a[96:99], v116 offset:30720\n"
    "ds_read_b128 a[100:103], v116 offset:31744\n"
    "s_and_b32 s78, s43, 3\n"
    "s_lshl_b32 s78, s78, 17\n"
    "s_add_u32 s54, s48, s78\n"
    "s_addc_u32 s55, s49, 0\n"
    "s_add_u32 s62, s62, 0x20000\n"
    "s_addc_u32 s63, s63, 0\n"
    "v_mov_b64 v[32:33], 0\n"
    "v_mov_b64 v[34:35], 0\n"
    "v_mov_b64 v[36:37], 0\n"
    "v_mov_b64 v[38:39], 0\n"
    "v_mov_b64 v[48:49], 0\n"
    "v_mov_b64 v[50:51], 0\n"
    "v_mov_b64 v[52:53], 0\n"
    "v_mov_b64 v[54:55], 0\n"
    "v_mov_b64 v[104:105], 0\n"
    "v_mov_b64 v[106:107], 0\n"
    "v_mov_b64 v[108:109], 0\n"
    "v_mov_b64 v[110:111], 0\n"
    "s_sub_u32 s78, s43, 1\n"
    "s_lshr_b32 s78, s78, 2\n"
    "s_lshr_b32 s79, s43, 2\n"
    "s_lshr_b32 s78, s93, s78\n"
    "s_lshr_b32 s79, s93, s79\n"
    "s_xor_b32 s78, s78, s79\n"
    "s_and_b32 s78, s78, 1\n"
    "s_lshl_b32 s78, s78, 13\n"
    "v_xor_b32 v117, s78, v117\n"
    "s_add_u32 s44, s44, 0x80000\n"
    "s_addc_u32 s45, s45, 0\n"
    "s_add_u32 s58, s58, 0x40000\n"
    "s_addc_u32 s59, s59, 0\n"
    "s_add_u32 s60, s60, 0x40000\n"
    "s_addc_u32 s61, s61, 0\n"
    "v_or_b32 v84, 1, s96\n"
    "v_mov_b32 v85, s97\n"
    "v_mov_b32 v86, s43\n"
    "s_waitcnt vmcnt(1)\n"
    "v_mov_b64 v[64:65], v[128:129]\n"
    "v_mov_b64 v[68:69], v[130:131]\n"
    "v_mov_b64 v[72:73], v[132:133]\n"
    "v_mov_b64 v[76:77], v[134:135]\n"
    "ds_read_b128 v[128:131], v116 offset:32768\n"
    "ds_read_b128 v[132:135], v116 offset:33792\n"
    "s_waitcnt lgkmcnt(0)\n"
    "s_barrier\n"
    "global_load_dwordx4 v[0:3], v71, s[54:55] offset:0 sc1\n"
    "s_nop 3\n"
    "global_load_dwordx4 v[4:7], v71, s[54:55] offset:1024 sc1\n"
    "s_nop 15\n"
    "s_cmp_lg_u32 s42, 0\n"
    "s_cbranch_scc1 L_vmb0%=\n"
    "s_mov_b32 exec_hi, 0\n"
    "global_store_dwordx4 v71, v[40:43], s[62:63] offset:0 sc1\n"
    "s_mov_b64 exec, -1\n"
    "s_branch L_kvb0%=\n"
    "L_vmb0%=:\n"
    "v_mov_b32 v126, v66\n"
    "v_mov_b32 v127, v74\n"
    "global_store_dwordx2 v79, v[126:127], s[62:63] offset:0 sc1\n"
    "L_kvb0%=:\n"
    "s_mov_b64 exec, 1\n"
    "global_store_dwordx2 v71, v[84:85], s[64:65] sc1\n"
    "s_mov_b64 exec, -1\n"
    "s_add_u32 s64, s64, 8192\n"
    "s_addc_u32 s65, s65, 0\n"
    "s_nop 1\n"
    "v_mov_b64 v[40:41], 0\n"
    "v_mov_b64 v[42:43], 0\n"
    "v_mov_b64 v[44:45], 0\n"
    "v_mov_b64 v[46:47], 0\n"
    "s_branch L_top0%=\n"
    "L_slow0%=:\n"
    "v_cmp_eq_u32 s[78:79], s50, v0\n"
    "v_cmp_eq_u32 s[80:81], s50, v1\n"
    "v_cmp_eq_u32 s[82:83], s50, v2\n"
    "v_cmp_eq_u32 s[84:85], s50, v3\n"
    "v_cmp_eq_u32 s[86:87], s50, v4\n"
    "v_cmp_eq_u32 s[88:89], s50, v5\n"
    "v_cmp_eq_u32 s[52:53], s50, v6\n"
    "v_cmp_eq_u32 vcc, s50, v7\n"
    "global_load_dwordx4 v[0:3], v71, s[54:55] offset:0 sc1\n"
    "global_load_dwordx4 v[4:7], v71, s[54:55] offset:1024 sc1\n"
    "s_waitcnt lgkmcnt(0)\n"
    "s_barrier\n"
    "s_or_b64 vcc, vcc, s[78:79]\n"
    "s_or_b64 vcc, vcc, s[80:81]\n"
    "s_or_b64 vcc, vcc, s[82:83]\n"
    "s_or_b64 vcc, vcc, s[84:85]\n"
    "s_or_b64 vcc, vcc, s[86:87]\n"
    "s_or_b64 vcc, vcc, s[88:89]\n"
    "s_or_b64 vcc, vcc, s[52:53]\n"
    "s_cbranch_scc1 L_redo0%=\n"
    "v_cmp_eq_u32 s[78:79], s50, v8\n"
    "v_cmp_eq_u32 s[80:81], s50, v9\n"
    "v_cmp_eq_u32 s[82:83], s50, v10\n"
    "v_cmp_eq_u32 s[84:85], s50, v11\n"
    "v_cmp_eq_u32 s[86:87], s50, v16\n"
    "v_cmp_eq_u32 s[88:89], s50, v17\n"
    "v_cmp_eq_u32 s[52:53], s50, v18\n"
    "v_cmp_eq_u32 vcc, s50, v19\n"
    "s_nop 0\n"
    "s_or_b64 vcc, vcc, s[78:79]\n"
    "s_or_b64 vcc, vcc, s[80:81]\n"
    "s_or_b64 vcc, vcc, s[82:83]\n"
    "s_or_b64 vcc, vcc, s[84:85]\n"
    "s_or_b64 vcc, vcc, s[86:87]\n"
    "s_or_b64 vcc, vcc, s[88:89]\n"
    "s_or_b64 vcc, vcc, s[52:53]\n"
    "s_cbranch_scc1 L_redo0%=\n"
    "v_cmp_eq_u32 s[78:79], s50, v24\n"
    "v_cmp_eq_u32 s[80:81], s50, v25\n"
    "v_cmp_eq_u32 s[82:83], s50, v26\n"
    "v_cmp_eq_u32 s[84:85], s50, v27\n"
    "v_cmp_eq_u32 s[86:87], s50, v12\n"
    "v_cmp_eq_u32 s[88:89], s50, v13\n"
    "v_cmp_eq_u32 s[52:53], s50, v14\n"
    "v_cmp_eq_u32 vcc, s50, v15\n"
    "s_nop 0\n"
    "s_or_b64 vcc, vcc, s[78:79]\n"
    "s_or_b64 vcc, vcc, s[80:81]\n"
    "s_or_b64 vcc, vcc, s[82:83]\n"
    "s_or_b64 vcc, vcc, s[84:85]\n"
    "s_or_b64 vcc, vcc, s[86:87]\n"
    "s_or_b64 vcc, vcc, s[88:89]\n"
    "s_or_b64 vcc, vcc, s[52:53]\n"
    "s_cbranch_scc1 L_redo0%=\n"
    "v_cmp_eq_u32 s[78:79], s50, v20\n"
    "v_cmp_eq_u32 s[80:81], s50, v21\n"
    "v_cmp_eq_u32 s[82:83], s50, v22\n"
    "v_cmp_eq_u32 s[84:85], s50, v23\n"
    "v_cmp_eq_u32 s[86:87], s50, v28\n"
    "v_cmp_eq_u32 s[88:89], s50, v29\n"
    "v_cmp_eq_u32 s[52:53], s50, v30\n"
    "v_cmp_eq_u32 vcc, s50, v31\n"
    "s_nop 0\n"
    "s_or_b64 vcc, vcc, s[78:79]\n"
    "s_or_b64 vcc, vcc, s[80:81]\n"
    "s_or_b64 vcc, vcc, s[82:83]\n"
    "s_or_b64 vcc, vcc, s[84:85]\n"
    "s_or_b64 vcc, vcc, s[86:87]\n"
    "s_or_b64 vcc, vcc, s[88:89]\n"
    "s_or_b64 vcc, vcc, s[52:53]\n"
    "s_cbranch_scc1 L_redo0%=\n"
    "s_waitcnt vmcnt(0)\n"
    "s_branch L_go0%=\n"
    "L_redo0%=:\n"
    "ds_read_b128 a[0:3], v116 offset:16384\n"
    "ds_read_b128 a[4:7], v116 offset:17408\n"
    "ds_read_b128 a[64:67], v116 offset:18432\n"
    "ds_read_b128 a[68:71], v116 offset:19456\n"
    "ds_read_b128 a[8:11], v116 offset:20480\n"
    "ds_read_b128 a[12:15], v116 offset:21504\n"
    "ds_read_b128 a[72:75], v116 offset:22528\n"
    "ds_read_b128 a[76:79], v116 offset:23552\n"
    "ds_read_b128 a[16:19], v116 offset:24576\n"
    "ds_read_b128 a[20:23], v116 offset:25600\n"
    "ds_read_b128 a[80:83], v116 offset:26624\n"
    "ds_read_b128 a[84:87], v116 offset:27648\n"
    "ds_read_b128 a[32:35], v116 offset:28672\n"
    "ds_read_b128 a[36:39], v116 offset:29696\n"
    "s_waitcnt lgkmcnt(8)\n"
    "ds_read_b128 a[96:99], v116 offset:30720\n"
    "ds_read_b128 a[100:103], v116 offset:31744\n"
    "v_mov_b64 v[32:33], 0\n"
    "v_mov_b64 v[34:35], 0\n"
    "v_mov_b64 v[36:37], 0\n"
    "v_mov_b64 v[38:39], 0\n"
    "v_mov_b64 v[40:41], 0\n"
    "v_mov_b64 v[42:43], 0\n"
    "v_mov_b64 v[44:45], 0\n"
    "v_mov_b64 v[46:47], 0\n"
    "v_mov_b64 v[48:49], 0\n"
    "v_mov_b64 v[50:51], 0\n"
    "v_mov_b64 v[52:53], 0\n"
    "v_mov_b64 v[54:55], 0\n"
    "s_add_u32 s90, s90, 1\n"
    "s_and_b32 s78, s90, 0xff\n"
    "s_cmp_lg_u32 s78, 0\n"
    "s_cbranch_scc1 L_re0%=\n"
    "v_mov_b32 v126, 0\n"
    "global_load_dwordx2 v[126:127], v126, s[68:69] sc1\n"
    "s_waitcnt vmcnt(0)\n"
    "v_cmp_eq_u64 vcc, s[96:97], v[126:127]\n"
    "s_nop 1\n"
    "s_cbranch_vccnz L_bail_g0%=\n"
    "s_cmp_lt_u32 s90, 0x40000\n"
    "s_cbranch_scc1 L_re0%=\n"
    "v_mov_b32 v126, s96\n"
    "v_mov_b32 v127, s97\n"
    "s_mov_b64 exec, 1\n"
    "global_store_dwordx2 v71, v[126:127], s[68:69] sc1\n"
    "s_mov_b64 exec, -1\n"
    "L_bail_g0%=:\n"
    "s_branch L_go0%=\n"
    "L_re0%=:\n"
    "s_waitcnt vmcnt(0) lgkmcnt(0)\n"
    "s_branch L_top0%=\n"
    "L_last0%=:\n"
    "v_smfmac_f32_16x16x64_bf16 v[52:55], v[12:15], a[216:223], v67\n"
    "v_smfmac_f32_16x16x64_bf16 v[48:51], v[20:23], a[168:175], v67\n"
    "v_smfmac_f32_16x16x64_bf16 v[52:55], v[20:23], a[232:239], v67\n"
    "v_smfmac_f32_16x16x64_bf16 v[48:51], v[28:31], a[184:191], v67\n"
    "v_smfmac_f32_16x16x64_bf16 v[52:55], v[28:31], a[248:255], v67\n"
    "s_nop 3\n"
    "v_pk_add_f32 v[48:49], v[48:49], v[50:51]\n"
    "s_nop 2\n"
    "v_pk_add_f32 v[52:53], v[52:53], v[54:55]\n"
    "v_cvt_pk_bf16_f32 v66, v48, v49\n"
    "v_cvt_pk_bf16_f32 v74, v52, v53\n"
    "s_add_u32 s62, s62, 0x20000\n"
    "s_addc_u32 s63, s63, 0\n"
    "s_cmp_lg_u32 s42, 0\n"
    "s_cbranch_scc1 L_vmc0%=\n"
    "s_mov_b32 exec_hi, 0\n"
    "global_store_dwordx4 v71, v[40:43], s[62:63] offset:0 sc1\n"
    "s_mov_b64 exec, -1\n"
    "s_branch L_kvc0%=\n"
    "L_vmc0%=:\n"
    "v_mov_b32 v126, v66\n"
    "v_mov_b32 v127, v74\n"
    "global_store_dwordx2 v79, v[126:127], s[62:63] offset:0 sc1\n"
    "L_kvc0%=:\n"
    "s_waitcnt vmcnt(0) lgkmcnt(0)\n"
    "s_barrier\n"
    "v_or_b32 v86, 1, s96\n"
    "v_mov_b32 v87, s97\n"
    "s_mov_b64 exec, 1\n"
    "global_store_dwordx2 v71, v[86:87], s[64:65] sc1\n"
    "s_add_u32 s64, s64, 8192\n"
    "s_addc_u32 s65, s65, 0\n"
    "global_store_dwordx2 v71, v[86:87], s[64:65] sc1\n"
    "s_waitcnt vmcnt(0)\n"
    "s_mov_b64 exec, -1\n"
    "s_endpgm\n"
    ".p2align 6\n"
    "L_poll1%=:\n"
    "global_load_dwordx4 v[8:11], v71, s[54:55] offset:0 sc1\n"
    "global_load_dwordx4 v[12:15], v71, s[54:55] offset:1024 sc1\n"
    "s_memtime s[16:17]\n"
    "s_waitcnt lgkmcnt(0)\n"
    "s_add_u32 s14, s16, s46\n"
    "s_mov_b64 exec, 15\n"
    "v_mov_b32 v87, s14\n"
    "global_store_dwordx4 v118, v[84:87], s[56:57] sc1\n"
    "s_mov_b64 exec, -1\n"
    "s_waitcnt vmcnt(0)\n"
    "s_branch L_top1%=\n"
    ".p2align 6\n"
    "L_top1%=:\n"
    "s_waitcnt vmcnt(3)\n"
    "v_smfmac_f32_16x16x64_bf16 v[40:43], v[8:11], a[16:23], v67\n"
    "ds_write_b128 v71, v[8:11] offset:2048\n"
    "v_smfmac_f32_16x16x64_bf16 v[44:47], v[8:11], a[80:87], v67\n"
    "v_smfmac_f32_16x16x64_bf16 v[32:35], v[8:11], v[160:167], v67\n"
    "v_smfmac_f32_16x16x64_bf16 v[36:39], v[8:11], v[168:175], v67\n"
    "s_waitcnt vmcnt(2)\n"
    "v_smfmac_f32_16x16x64_bf16 v[40:43], v[12:15], a[24:31], v67\n"
    "ds_write_b128 v71, v[12:15] offset:3072\n"
    "s_waitcnt lgkmcnt(1)\n"
    "s_barrier\n"
    "ds_read_b128 v[0:3], v71 offset:0\n"
    "v_smfmac_f32_16x16x64_bf16 v[44:47], v[12:15], a[88:95], v67\n"
    "ds_read_b128 v[24:27], v71 offset:6144\n"
    "v_smfmac_f32_16x16x64_bf16 v[32:35], v[12:15], v[176:183], v67\n"
    "ds_read_b128 v[16:19], v71 offset:4096\n"
    "v_smfmac_f32_16x16x64_bf16 v[36:39], v[12:15], v[184:191], v67\n"
    "s_waitcnt lgkmcnt(3)\n"
    "s_barrier\n"
    "v_smfmac_f32_16x16x64_bf16 v[48:51], v[8:11], a[144:151], v67\n"
    "s_waitcnt lgkmcnt(2)\n"
    "ds_read_b128 v[4:7], v71 offset:1024\n"
    "v_smfmac_f32_16x16x64_bf16 v[32:35], v[0:3], v[128:135], v67\n"
    "ds_read_b128 v[28:31], v71 offset:7168\n"
    "v_smfmac_f32_16x16x64_bf16 v[36:39], v[0:3], v[136:143], v67\n"
    "s_waitcnt lgkmcnt(2)\n"
    "ds_read_b128 v[20:23], v71 offset:5120\n"
    "v_smfmac_f32_16x16x64_bf16 v[32:35], v[24:27], v[224:231], v67\n"
    "ds_read_b128 a[16:19], v116 offset:0\n"
    "v_smfmac_f32_16x16x64_bf16 v[36:39], v[24:27], v[232:239], v67\n"
    "ds_read_b128 a[20:23], v116 offset:1024\n"
    "v_smfmac_f32_16x16x64_bf16 v[32:35], v[16:19], v[192:199], v67\n"
    "ds_read_b128 a[80:83], v116 offset:4096\n"
    "v_smfmac_f32_16x16x64_bf16 v[36:39], v[16:19], v[200:207], v67\n"
    "s_waitcnt lgkmcnt(5)\n"
    "ds_read_b128 a[84:87], v116 offset:5120\n"
    "v_smfmac_f32_16x16x64_bf16 v[32:35], v[4:7], v[144:151], v67\n"
    "ds_read_b128 a[24:27], v116 offset:2048\n"
    "v_smfmac_f32_16x16x64_bf16 v[36:39], v[4:7], v[152:159], v67\n"
    "s_waitcnt lgkmcnt(5)\n"
    "ds_read_b128 a[28:31], v116 offset:3072\n"
    "v_smfmac_f32_16x16x64_bf16 v[32:35], v[28:31], v[240:247], v67\n"
    "ds_read_b128 a[88:91], v116 offset:6144\n"
    "v_smfmac_f32_16x16x64_bf16 v[36:39], v[28:31], v[248:255], v67\n"
    "ds_read_b128 a[92:95], v116 offset:7168\n"
    "v_smfmac_f32_16x16x64_bf16 v[32:35], v[20:23], v[208:215], v67\n"
    "v_smfmac_f32_16x16x64_bf16 v[36:39], v[20:23], v[216:223], v67\n"
    "v_smfmac_f32_16x16x64_bf16 v[40:43], v[0:3], a[0:7], v67\n"
    "v_add_f32 v32, v32, v34\n"
    "v_add_f32 v33, v33, v35\n"
    "v_smfmac_f32_16x16x64_bf16 v[44:47], v[0:3], a[64:71], v67\n"
    "ds_read_b128 a[0:3], v116 offset:8192\n"
    "v_cvt_pk_bf16_f32 v126, v32, v33\n"
    "v_add_f32 v36, v36, v38\n"
    "v_add_f32 v37, v37, v39\n"
    "v_cvt_pk_bf16_f32 v127, v36, v37\n"
    "v_smfmac_f32_16x16x64_bf16 v[40:43], v[24:27], a[48:55], v67\n"
    "ds_write_b64 v114, v[126:127] offset:8704\n"
    "v_smfmac_f32_16x16x64_bf16 v[44:47], v[24:27], a[112:119], v67\n"
    "ds_read_b128 a[4:7], v116 offset:9216\n"
    "v_cmp_u_f32 vcc, v32, v33\n"
    "v_smfmac_f32_16x16x64_bf16 v[40:43], v[16:19], a[32:39], v67\n"
    "ds_read_b128 a[64:67], v116 offset:12288\n"
    "v_smfmac_f32_16x16x64_bf16 v[44:47], v[16:19], a[96:103], v67\n"
    "s_cbranch_vccnz L_slow1%=\n"
    "L_go1%=:\n"
    "v_smfmac_f32_16x16x64_bf16 v[40:43], v[4:7], a[8:15], v67\n"
    "s_waitcnt lgkmcnt(2)\n"
    "s_barrier\n"
    "ds_read_b64_tr_b16 v[32:33], v112 offset:8192\n"
    "v_smfmac_f32_16x16x64_bf16 v[44:47], v[4:7], a[72:79], v67\n"
    "ds_read_b64_tr_b16 v[34:35], v113 offset:8192\n"
    "ds_read_b64_tr_b16 v[36:37], v112 offset:9216\n"
    "v_smfmac_f32_16x16x64_bf16 v[40:43], v[28:31], a[56:63], v67\n"
    "ds_read_b64_tr_b16 v[38:39], v113 offset:9216\n"
    "v_smfmac_f32_16x16x64_bf16 v[44:47], v[28:31], a[120:127], v67\n"
    "global_load_dwordx4 v[164:167], v71, s[60:61] offset:1024 sc1\n"
    "v_smfmac_f32_16x16x64_bf16 v[40:43], v[20:23], a[40:47], v67\n"
    "ds_read_b128 a[68:71], v116 offset:13312\n"
    "v_smfmac_f32_16x16x64_bf16 v[44:47], v[20:23], a[104:111], v67\n"
    "s_waitcnt lgkmcnt(1)\n"
    "ds_read_b128 a[48:51], v116 offset:10240\n"
    "v_mfma_f32_16x16x32_bf16 v[88:91], v[56:59], v[32:35], 0\n"
    "v_add_f32 v40, v40, v42\n"
    "v_add_f32 v41, v41, v43\n"
    "v_mfma_f32_16x16x32_bf16 v[88:91], v[60:63], v[36:39], v[88:91]\n"
    "v_cvt_pk_bf16_f32 v126, v40, v41\n"
    "v_add_f32 v44, v44, v46\n"
    "v_add_f32 v45, v45, v47\n"
    "v_cvt_pk_bf16_f32 v127, v44, v45\n"
    "v_smfmac_f32_16x16x64_bf16 v[52:55], v[8:11], a[208:215], v67\n"
    "ds_write_b64 v115, v[126:127] offset:10752\n"
    "v_add_f32_dpp v88, v90, v88 quad_perm:[2,3,2,3] row_mask:0xf bank_mask:0xf\n"
    "v_add_f32_dpp v89, v91, v89 quad_perm:[2,3,2,3] row_mask:0xf bank_mask:0xf\n"
    "v_smfmac_f32_16x16x64_bf16 v[48:51], v[12:15], a[152:159], v67\n"
    "ds_write_b64 v75, v[88:89] offset:14368\n"
    "v_smfmac_f32_16x16x64_bf16 v[52:55], v[12:15], a[216:223], v67\n"
    "global_load_dwordx4 v[56:59], v71, s[58:59] offset:-2048 sc1\n"
    "v_smfmac_f32_16x16x64_bf16 v[48:51], v[0:3], a[128:135], v67\n"
    "global_load_dwordx4 v[60:63], v71, s[58:59] offset:2048 sc1\n"
    "v_smfmac_f32_16x16x64_bf16 v[52:55], v[0:3], a[192:199], v67\n"
    "global_load_dwordx4 v[160:163], v71, s[60:61] offset:0 sc1\n"
    "v_smfmac_f32_16x16x64_bf16 v[48:51], v[24:27], a[176:183], v67\n"
    "s_waitcnt lgkmcnt(0)\n"
    "s_barrier\n"
    "ds_read_b64_tr_b16 v[40:41], v112 offset:10240\n"
    "v_smfmac_f32_16x16x64_bf16 v[52:55], v[24:27], a[240:247], v67\n"
    "ds_read_b64_tr_b16 v[42:43], v113 offset:10240\n"
    "ds_read_b64_tr_b16 v[44:45], v112 offset:11264\n"
    "v_smfmac_f32_16x16x64_bf16 v[48:51], v[16:19], a[160:167], v67\n"
    "ds_read_b64_tr_b16 v[46:47], v113 offset:11264\n"
    "v_smfmac_f32_16x16x64_bf16 v[52:55], v[16:19], a[224:231], v67\n"
    "ds_read_b128 v[88:91], v71 offset:14336\n"
    "s_cmp_eq_u32 s43, 63\n"
    "v_smfmac_f32_16x16x64_bf16 v[48:51], v[4:7], a[136:143], v67\n"
    "s_waitcnt lgkmcnt(3)\n"
    "ds_read_b128 a[52:55], v116 offset:11264\n"
    "v_mfma_f32_16x16x32_bf16 v[96:99], v[40:43], v[32:35], 0\n"
    "s_waitcnt lgkmcnt(2)\n"
    "ds_read_b128 a[112:115], v116 offset:14336\n"
    "v_mfma_f32_16x16x32_bf16 v[96:99], v[44:47], v[36:39], v[96:99]\n"
    "s_cbranch_scc1 L_last1%=\n"
    "v_smfmac_f32_16x16x64_bf16 v[52:55], v[4:7], a[200:207], v67\n"
    "s_waitcnt lgkmcnt(2)\n"
    "ds_read_b128 a[116:119], v116 offset:15360\n"
    "v_max3_f32 v122, v88, v89, v90\n"
    "v_max_f32 v122, v122, v91\n"
    "v_add_f32_dpp v96, v98, v96 quad_perm:[2,3,2,3] row_mask:0xf bank_mask:0xf\n"
    "v_add_f32_dpp v97, v99, v97 quad_perm:[2,3,2,3] row_mask:0xf bank_mask:0xf\n"
    "v_max_f32_dpp v123, v122, v122 quad_perm:[2,3,0,1] row_mask:0xf bank_mask:0xf\n"
    "v_max3_f32 v122, v123, v96, v97\n"
    "v_smfmac_f32_16x16x64_bf16 v[48:51], v[28:31], a[184:191], v67\n"
    "v_max_f32_dpp v121, v122, v123 quad_perm:[0,1,0,1] row_mask:0xf bank_mask:0xf\n"
    "v_max_f32_dpp v123, v122, v123 quad_perm:[0,1,0,1] row_mask:0xf bank_mask:0xf\n"
    "v_smfmac_f32_16x16x64_bf16 v[52:55], v[28:31], a[248:255], v67\n"
    "v_permlane16_swap_b32 v123, v121\n"
    "v_max_f32 v122, v123, v121\n"
    "v_max_f32 v123, v123, v121\n"
    "v_smfmac_f32_16x16x64_bf16 v[48:51], v[20:23], a[168:175], v67\n"
    "v_smfmac_f32_16x16x64_bf16 v[52:55], v[20:23], a[232:239], v67\n"
    "v_permlane32_swap_b32 v122, v123\n"
    "v_max_f32 v122, v122, v123\n"
    "v_mul_f32 v122, s76, v122\n"
    "v_pk_fma_f32 v[96:97], v[96:97], s[76:77], v[122:123] op_sel_hi:[1,1,0] neg_lo:[0,0,1] neg_hi:[0,0,1]\n"
    "v_pk_fma_f32 v[88:89], v[88:89], s[76:77], v[122:123] op_sel_hi:[1,1,0] neg_lo:[0,0,1] neg_hi:[0,0,1]\n"
    "v_pk_fma_f32 v[90:91], v[90:91], s[76:77], v[122:123] op_sel_hi:[1,1,0] neg_lo:[0,0,1] neg_hi:[0,0,1]\n"
    "v_exp_f32 v96, v96\n"
    "v_exp_f32 v97, v97\n"
    "v_exp_f32 v88, v88\n"
    "v_exp_f32 v89, v89\n"
    "v_exp_f32 v90, v90\n"
    "v_exp_f32 v91, v91\n"
    "v_pk_add_f32 v[48:49], v[48:49], v[50:51]\n"
    "v_pk_add_f32 v[52:53], v[52:53], v[54:55]\n"
    "v_cvt_pk_bf16_f32 v126, v96, v97\n"
    "v_cvt_pk_bf16_f32 v100, v88, v89\n"
    "v_cvt_pk_bf16_f32 v102, v90, v91\n"
    "v_cndmask_b32 v101, 0, v126, s[72:73]\n"
    "v_cndmask_b32 v103, 0, v126, s[74:75]\n"
    "v_cvt_pk_bf16_f32 v66, v48, v49\n"
    "v_cvt_pk_bf16_f32 v70, v48, v49\n"
    "v_cvt_pk_bf16_f32 v74, v52, v53\n"
    "v_cvt_pk_bf16_f32 v78, v52, v53\n"
    "v_smfmac_f32_16x16x64_bf16 v[104:107], v[100:103], v[64:71], v67\n"
    "v_smfmac_f32_16x16x64_bf16 v[108:111], v[100:103], v[72:79], v67\n"
    "v_mfma_f32_16x16x32_bf16 v[84:87], v[100:103], v[80:83], 0\n"
    "v_add_f32 v104, v104, v106\n"
    "v_add_f32 v105, v105, v107\n"
    "v_cvt_pk_bf16_f32 v104, v104, v105\n"
    "v_add_f32 v108, v108, v110\n"
    "v_add_f32 v109, v109, v111\n"
    "v_cvt_pk_bf16_f32 v105, v108, v109\n"
    "ds_write_b64 v114, v[104:105] offset:12800\n"
    "global_load_dword v120, v119, s[12:13] offset:12 sc1\n"
    "v_pk_add_f32 v[124:125], v[84:85], v[86:87]\n"
    "v_rcp_f32 v124, v124\n"
    "v_rcp_f32 v125, v125\n"
    "v_mov_b64 v[0:1], 0\n"
    "v_mov_b64 v[2:3], 0\n"
    "v_mov_b64 v[4:5], 0\n"
    "v_mov_b64 v[6:7], 0\n"
    "v_mov_b64 v[8:9], 0\n"
    "v_mov_b64 v[10:11], 0\n"
    "v_mov_b64 v[12:13], 0\n"
    "v_mov_b64 v[14:15], 0\n"
    "v_mfma_f32_16x16x32_bf16 v[96:99], v[100:103], v[80:83], 0\n"
    "s_waitcnt lgkmcnt(0)\n"
    "s_barrier\n"
    "ds_read_b64_tr_b16 v[32:33], v112 offset:12288\n"
    "v_mfma_f32_16x16x32_bf16 v[96:99], v[100:103], v[80:83], 0\n"
    "ds_read_b64_tr_b16 v[34:35], v113 offset:12288\n"
    "ds_read_b64_tr_b16 v[36:37], v112 offset:13312\n"
    "v_mfma_f32_16x16x32_bf16 v[96:99], v[100:103], v[80:83], 0\n"
    "ds_read_b64_tr_b16 v[38:39], v113 offset:13312\n"
    "v_mfma_f32_16x16x32_bf16 v[96:99], v[100:103], v[80:83], 0\n"
    "s_waitcnt lgkmcnt(2)\n"
    "v_smfmac_f32_16x16x64_bf16 v[0:3], v[32:35], a[16:23], v67\n"
    "v_smfmac_f32_16x16x64_bf16 v[4:7], v[32:35], a[80:87], v67\n"
    "s_waitcnt lgkmcnt(0)\n"
    "v_smfmac_f32_16x16x64_bf16 v[0:3], v[36:39], a[24:31], v67\n"
    "v_smfmac_f32_16x16x64_bf16 v[4:7], v[36:39], a[88:95], v67\n"
    "v_smfmac_f32_16x16x64_bf16 v[8:11], v[32:35], a[0:7], v67\n"
    "v_add_f32 v0, v0, v2\n"
    "v_smfmac_f32_16x16x64_bf16 v[12:15], v[32:35], a[64:71], v67\n"
    "v_add_f32 v1, v1, v3\n"
    "v_smfmac_f32_16x16x64_bf16 v[8:11], v[36:39], a[48:55], v67\n"
    "v_add_f32 v4, v4, v6\n"
    "v_smfmac_f32_16x16x64_bf16 v[12:15], v[36:39], a[112:119], v67\n"
    "v_add_f32 v5, v5, v7\n"
    "v_mul_f32 v0, v0, v124\n"
    "v_mul_f32 v1, v1, v125\n"
    "v_cvt_pk_bf16_f32 v88, v0, v1\n"
    "v_pk_mul_f32 v[4:5], v[4:5], v[124:125]\n"
    "v_pk_add_f32 v[8:9], v[8:9], v[10:11]\n"
    "v_pk_add_f32 v[12:13], v[12:13], v[14:15]\n"
    "v_cvt_pk_bf16_f32 v89, v4, v5\n"
    "v_pk_mul_f32 v[8:9], v[8:9], v[124:125]\n"
    "v_pk_mul_f32 v[12:13], v[12:13], v[124:125]\n"
    "v_cvt_pk_bf16_f32 v90, v8, v9\n"
    "v_cvt_pk_bf16_f32 v91, v12, v13\n"
    "global_store_dwordx4 v117, v[88:91], s[44:45] sc1\n"
    "s_memtime s[8:9]\n"
    "s_add_u32 s43, s43, 1\n"
    "ds_read_b128 a[16:19], v116 offset:16384\n"
    "ds_read_b128 a[20:23], v116 offset:17408\n"
    "ds_read_b128 a[80:83], v116 offset:18432\n"
    "ds_read_b128 a[84:87], v116 offset:19456\n"
    "ds_read_b128 a[24:27], v116 offset:20480\n"
    "ds_read_b128 a[28:31], v116 offset:21504\n"
    "ds_read_b128 a[88:91], v116 offset:22528\n"
    "ds_read_b128 a[92:95], v116 offset:23552\n"
    "ds_read_b128 a[0:3], v116 offset:24576\n"
    "ds_read_b128 a[4:7], v116 offset:25600\n"
    "ds_read_b128 a[64:67], v116 offset:26624\n"
    "ds_read_b128 a[68:71], v116 offset:27648\n"
    "ds_read_b128 a[48:51], v116 offset:28672\n"
    "s_waitcnt lgkmcnt(8)\n"
    "ds_read_b128 a[52:55], v116 offset:29696\n"
    "ds_read_b128 a[112:115], v116 offset:30720\n"
    "ds_read_b128 a[116:119], v116 offset:31744\n"
    "s_and_b32 s78, s43, 3\n"
    "s_lshl_b32 s78, s78, 17\n"
    "s_add_u32 s54, s48, s78\n"
    "s_addc_u32 s55, s49, 0\n"
    "s_add_u32 s62, s62, 0x20000\n"
    "s_addc_u32 s63, s63, 0\n"
    "v_mov_b64 v[32:33], 0\n"
    "v_mov_b64 v[34:35], 0\n"
    "v_mov_b64 v[36:37], 0\n"
    "v_mov_b64 v[38:39], 0\n"
    "v_mov_b64 v[48:49], 0\n"
    "v_mov_b64 v[50:51], 0\n"
    "v_mov_b64 v[52:53], 0\n"
    "v_mov_b64 v[54:55], 0\n"
    "v_mov_b64 v[104:105], 0\n"
    "v_mov_b64 v[106:107], 0\n"
    "v_mov_b64 v[108:109], 0\n"
    "v_mov_b64 v[110:111], 0\n"
    "s_sub_u32 s78, s43, 1\n"
    "s_lshr_b32 s78, s78, 2\n"
    "s_lshr_b32 s79, s43, 2\n"
    "s_lshr_b32 s78, s93, s78\n"
    "s_lshr_b32 s79, s93, s79\n"
    "s_xor_b32 s78, s78, s79\n"
    "s_and_b32 s78, s78, 1\n"
    "s_lshl_b32 s78, s78, 13\n"
    "v_xor_b32 v117, s78, v117\n"
    "s_add_u32 s44, s44, 0x80000\n"
    "s_addc_u32 s45, s45, 0\n"
    "s_add_u32 s58, s58, 0x40000\n"
    "s_addc_u32 s59, s59, 0\n"
    "s_add_u32 s60, s60, 0x40000\n"
    "s_addc_u32 s61, s61, 0\n"
    "v_or_b32 v84, 1, s96\n"
    "v_mov_b32 v85, s97\n"
    "v_mov_b32 v86, s43\n"
    "s_waitcnt vmcnt(1)\n"
    "v_mov_b64 v[64:65], v[160:161]\n"
    "v_mov_b64 v[68:69], v[162:163]\n"
    "v_mov_b64 v[72:73], v[164:165]\n"
    "v_mov_b64 v[76:77], v[166:167]\n"
    "ds_read_b128 v[160:163], v116 offset:32768\n"
    "ds_read_b128 v[164:167], v116 offset:33792\n"
    "s_sub_u32 s19, s8, s16\n"
    "L_lk%=:\n"
    "v_subrev_u32 v120, s14, v120\n"
    "v_cmp_gt_u32 vcc, 0xe00, v120\n"
    "s_nop 1\n"
    "v_cndmask_b32 v120, 0, v120, vcc\n"
    "s_nop 1\n"
    "v_max_u32_dpp v120, v120, v120 row_ror:1 row_mask:0xf bank_mask:0xf\n"
    "s_nop 1\n"
    "v_max_u32_dpp v120, v120, v120 row_ror:2 row_mask:0xf bank_mask:0xf\n"
    "s_nop 1\n"
    "v_max_u32_dpp v120, v120, v120 row_ror:4 row_mask:0xf bank_mask:0xf\n"
    "s_nop 1\n"
    "v_max_u32_dpp v120, v120, v120 row_ror:8 row_mask:0xf bank_mask:0xf\n"
    "s_memtime s[6:7]\n"
    "v_readlane_b32 s78, v120, 0\n"
    "v_readlane_b32 s79, v120, 16\n"
    "v_readlane_b32 s80, v120, 32\n"
    "v_readlane_b32 s81, v120, 48\n"
    "s_add_u32 s9, s14, s95\n"
    "s_max_u32 s78, s78, s79\n"
    "s_max_u32 s80, s80, s81\n"
    "s_max_u32 s78, s78, s80\n"
    "s_add_u32 s9, s9, s78\n"
    "s_sub_u32 s8, s9, s91\n"
    "s_sub_u32 s8, s8, s94\n"
    "s_waitcnt lgkmcnt(0)\n"
    "s_sub_i32 s78, s8, s6\n"
    "s_cmp_gt_i32 s78, 1300\n"
    "s_cbranch_scc1 L_far%=\n"
    "L_near%=:\n"
    "L_wa%=:\n"
    "s_memtime s[6:7]\n"
    "s_waitcnt lgkmcnt(0)\n"
    "s_sub_i32 s78, s8, s6\n"
    "s_sub_u32 s79, s78, 520\n"
    "s_cmp_lt_u32 s79, 0x1400\n"
    "s_cbranch_scc1 L_wa%=\n"
    "s_memtime s[6:7]\n"
    "s_memrealtime s[78:79]\n"
    "s_nop 3\n"
    "s_memrealtime s[80:81]\n"
    "s_nop 3\n"
    "s_memrealtime s[82:83]\n"
    "s_nop 3\n"
    "s_memrealtime s[84:85]\n"
    "s_nop 3\n"
    "s_memrealtime s[86:87]\n"
    "s_nop 3\n"
    "s_memrealtime s[88:89]\n"
    "s_mov_b32 s10, 0\n"
    "s_waitcnt lgkmcnt(0)\n"
    "s_mul_i32 s91, s78, 24\n"
    "s_cmp_eq_u32 s78, s80\n"
    "s_addc_u32 s10, s10, 0\n"
    "s_cmp_eq_u32 s78, s82\n"
    "s_addc_u32 s10, s10, 0\n"
    "s_cmp_eq_u32 s78, s84\n"
    "s_addc_u32 s10, s10, 0\n"
    "s_cmp_eq_u32 s78, s86\n"
    "s_addc_u32 s10, s10, 0\n"
    "s_cmp_eq_u32 s78, s88\n"
    "s_addc_u32 s10, s10, 0\n"
    "s_mul_i32 s10, s10, 20\n"
    "s_sub_u32 s91, s91, s6\n"
    "s_sub_u32 s91, s91, s10\n"
    "s_sub_u32 s8, s9, s91\n"
    "L_wb%=:\n"
    "s_memtime s[6:7]\n"
    "s_waitcnt lgkmcnt(0)\n"
    "s_sub_i32 s78, s8, s6\n"
    "s_sub_u32 s79, s78, 200\n"
    "s_cmp_lt_u32 s79, 0x1400\n"
    "s_cbranch_scc1 L_wb%=\n"
    "s_sub_i32 s78, s78, 84\n"
    "s_max_i32 s78, s78, 0\n"
    "s_min_i32 s78, s78, 124\n"
    "s_andn2_b32 s78, s78, 3\n"
    "s_sub_u32 s80, s4, s78\n"
    "s_subb_u32 s81, s5, 0\n"
    "s_setpc_b64 s[80:81]\n"
    "L_far%=:\n"
    "L_wc%=:\n"
    "s_memtime s[6:7]\n"
    "s_waitcnt lgkmcnt(0)\n"
    "s_sub_i32 s78, s8, s6\n"
    "s_sub_u32 s79, s78, 1300\n"
    "s_cmp_lt_u32 s79, 0x1400\n"
    "s_cbranch_scc1 L_wc%=\n"
    "global_load_dword v120, v119, s[12:13] offset:12 sc1\n"
    "s_cmp_gt_i32 s78, 1300\n"
    "s_waitcnt vmcnt(0)\n"
    "s_cbranch_scc0 L_lk%=\n"
    "s_branch L_near%=\n"
    ".p2align 6\n"
    "s_nop 0\n"
    "s_nop 0\n"
    "s_nop 0\n"
    "s_nop 0\n"
    "s_nop 0\n"
    "s_nop 0\n"
    "s_nop 0\n"
    "s_nop 0\n"
    "s_nop 0\n"
    "s_nop 0\n"
    "s_nop 0\n"
    "s_nop 0\n"
    "s_nop 0\n"
    "s_nop 0\n"
    "s_nop 0\n"
    "s_nop 0\n"
    "s_nop 0\n"
    "s_nop 0\n"
    "s_nop 0\n"
    "s_nop 0\n"
    "s_nop 0\n"
    "s_nop 0\n"
    "s_nop 0\n"
    "s_nop 0\n"
    "s_nop 0\n"
    "s_nop 0\n"
    "s_nop 0\n"
    "s_nop 0\n"
    "s_nop 0\n"
    "s_nop 0\n"
    "s_nop 0\n"
    "s_nop 0\n"
    "L_sled%=:\n"
    "s_barrier\n"
    "global_load_dwordx4 v[8:11], v71, s[54:55] offset:0 sc1\n"
    "s_nop 3\n"
    "global_load_dwordx4 v[12:15], v71, s[54:55] offset:1024 sc1\n"
    "s_memtime s[16:17]\n"
    "s_nop 15\n"
    "s_cmp_lg_u32 s42, 0\n"
    "s_cbranch_scc1 L_vmb1%=\n"
    "s_mov_b32 exec_lo, 0\n"
    "global_store_dwordx4 v71, v[40:43], s[62:63] offset:0 sc1\n"
    "s_mov_b64 exec, -1\n"
    "s_branch L_kvb1%=\n"
    "L_vmb1%=:\n"
    "v_mov_b32 v126, v66\n"
    "v_mov_b32 v127, v74\n"
    "global_store_dwordx2 v79, v[126:127], s[62:63] offset:512 sc1\n"
    "L_kvb1%=:\n"
    "s_nop 1\n"
    "v_mov_b64 v[40:41], 0\n"
    "v_mov_b64 v[42:43], 0\n"
    "v_mov_b64 v[44:45], 0\n"
    "v_mov_b64 v[46:47], 0\n"
    "s_add_u32 s12, s12, 0x10000\n"
    "s_andn2_b32 s12, s12, 0x40000\n"
    "s_add_u32 s56, s56, 0x10000\n"
    "s_andn2_b32 s56, s56, 0x40000\n"
    "s_waitcnt lgkmcnt(0)\n"
    "s_sub_i32 s78, s91, s47\n"
    "s_sub_u32 s79, s16, s18\n"
    "v_cvt_f32_u32 v127, s79\n"
    "v_cvt_f32_i32 v126, s78\n"
    "v_rcp_f32 v127, v127\n"
    "v_mul_f32 v126, 0x450fc000, v126\n"
    "v_mul_f32 v126, v126, v127\n"
    "v_cvt_i32_f32 v126, v126\n"
    "s_nop 0\n"
    "v_readfirstlane_b32 s78, v126\n"
    "s_min_u32 s19, s19, 2200\n"
    "s_sub_u32 s51, s51, 10\n"
    "s_max_u32 s51, s19, s51\n"
    "s_max_i32 s78, s78, -200\n"
    "s_min_i32 s78, s78, 900\n"
    "s_add_i32 s78, s78, s94\n"
    "s_ashr_i32 s94, s78, 1\n"
    "s_add_u32 s46, s91, s94\n"
    "s_add_u32 s46, s46, s51\n"
    "s_cmp_lt_u32 s43, 29\n"
    "s_cselect_b32 s79, 100, 0\n"
    "s_cselect_b32 s95, 20, 0\n"
    "s_add_u32 s95, s95, 1080\n"
    "s_add_u32 s46, s46, s79\n"
    "s_mov_b32 s47, s91\n"
    "s_mov_b32 s18, s16\n"
    "s_add_u32 s14, s16, s46\n"
    "s_mov_b64 exec, 15\n"
    "v_mov_b32 v87, s14\n"
    "global_store_dwordx4 v118, v[84:87], s[56:57] sc1\n"
    "s_mov_b64 exec, -1\n"
    "s_branch L_top1%=\n"
    "L_slow1%=:\n"
    "v_cmp_eq_u32 s[78:79], s50, v8\n"
    "v_cmp_eq_u32 s[80:81], s50, v9\n"
    "v_cmp_eq_u32 s[82:83], s50, v10\n"
    "v_cmp_eq_u32 s[84:85], s50, v11\n"
    "v_cmp_eq_u32 s[86:87], s50, v12\n"
    "v_cmp_eq_u32 s[88:89], s50, v13\n"
    "v_cmp_eq_u32 s[52:53], s50, v14\n"
    "v_cmp_eq_u32 vcc, s50, v15\n"
    "global_load_dwordx4 v[8:11], v71, s[54:55] offset:0 sc1\n"
    "global_load_dwordx4 v[12:15], v71, s[54:55] offset:1024 sc1\n"
    "s_memtime s[16:17]\n"
    "s_waitcnt lgkmcnt(0)\n"
    "s_barrier\n"
    "s_add_u32 s16, s16, 250\n"
    "s_add_u32 s14, s16, s46\n"
    "s_mov_b64 exec, 15\n"
    "v_mov_b32 v87, s14\n"
    "global_store_dwordx4 v118, v[84:87], s[56:57] sc1\n"
    "s_mov_b64 exec, -1\n"
    "s_or_b64 vcc, vcc, s[78:79]\n"
    "s_or_b64 vcc, vcc, s[80:81]\n"
    "s_or_b64 vcc, vcc, s[82:83]\n"
    "s_or_b64 vcc, vcc, s[84:85]\n"
    "s_or_b64 vcc, vcc, s[86:87]\n"
    "s_or_b64 vcc, vcc, s[88:89]\n"
    "s_or_b64 vcc, vcc, s[52:53]\n"
    "s_cbranch_scc1 L_redo1%=\n"
    "v_cmp_eq_u32 s[78:79], s50, v0\n"
    "v_cmp_eq_u32 s[80:81], s50, v1\n"
    "v_cmp_eq_u32 s[82:83], s50, v2\n"
    "v_cmp_eq_u32 s[84:85], s50, v3\n"
    "v_cmp_eq_u32 s[86:87], s50, v24\n"
    "v_cmp_eq_u32 s[88:89], s50, v25\n"
    "v_cmp_eq_u32 s[52:53], s50, v26\n"
    "v_cmp_eq_u32 vcc, s50, v27\n"
    "s_nop 0\n"
    "s_or_b64 vcc, vcc, s[78:79]\n"
    "s_or_b64 vcc, vcc, s[80:81]\n"
    "s_or_b64 vcc, vcc, s[82:83]\n"
    "s_or_b64 vcc, vcc, s[84:85]\n"
    "s_or_b64 vcc, vcc, s[86:87]\n"
    "s_or_b64 vcc, vcc, s[88:89]\n"
    "s_or_b64 vcc, vcc, s[52:53]\n"
    "s_cbranch_scc1 L_redo1%=\n"
    "v_cmp_eq_u32 s[78:79], s50, v16\n"
    "v_cmp_eq_u32 s[80:81], s50, v17\n"
    "v_cmp_eq_u32 s[82:83], s50, v18\n"
    "v_cmp_eq_u32 s[84:85], s50, v19\n"
    "v_cmp_eq_u32 s[86:87], s50, v4\n"
    "v_cmp_eq_u32 s[88:89], s50, v5\n"
    "v_cmp_eq_u32 s[52:53], s50, v6\n"
    "v_cmp_eq_u32 vcc, s50, v7\n"
    "s_nop 0\n"
    "s_or_b64 vcc, vcc, s[78:79]\n"
    "s_or_b64 vcc, vcc, s[80:81]\n"
    "s_or_b64 vcc, vcc, s[82:83]\n"
    "s_or_b64 vcc, vcc, s[84:85]\n"
    "s_or_b64 vcc, vcc, s[86:87]\n"
    "s_or_b64 vcc, vcc, s[88:89]\n"
    "s_or_b64 vcc, vcc, s[52:53]\n"
    "s_cbranch_scc1 L_redo1%=\n"
    "v_cmp_eq_u32 s[78:79], s50, v28\n"
    "v_cmp_eq_u32 s[80:81], s50, v29\n"
    "v_cmp_eq_u32 s[82:83], s50, v30\n"
    "v_cmp_eq_u32 s[84:85], s50, v31\n"
    "v_cmp_eq_u32 s[86:87], s50, v20\n"
    "v_cmp_eq_u32 s[88:89], s50, v21\n"
    "v_cmp_eq_u32 s[52:53], s50, v22\n"
    "v_cmp_eq_u32 vcc, s50, v23\n"
    "s_nop 0\n"
    "s_or_b64 vcc, vcc, s[78:79]\n"
    "s_or_b64 vcc, vcc, s[80:81]\n"
    "s_or_b64 vcc, vcc, s[82:83]\n"
    "s_or_b64 vcc, vcc, s[84:85]\n"
    "s_or_b64 vcc, vcc, s[86:87]\n"
    "s_or_b64 vcc, vcc, s[88:89]\n"
    "s_or_b64 vcc, vcc, s[52:53]\n"
    "s_cbranch_scc1 L_redo1%=\n"
    "s_waitcnt vmcnt(0)\n"
    "s_branch L_go1%=\n"
    "L_redo1%=:\n"
    "ds_read_b128 a[16:19], v116 offset:16384\n"
    "ds_read_b128 a[20:23], v116 offset:17408\n"
    "ds_read_b128 a[80:83], v116 offset:18432\n"
    "ds_read_b128 a[84:87], v116 offset:19456\n"
    "ds_read_b128 a[24:27], v116 offset:20480\n"
    "ds_read_b128 a[28:31], v116 offset:21504\n"
    "ds_read_b128 a[88:91], v116 offset:22528\n"
    "ds_read_b128 a[92:95], v116 offset:23552\n"
    "ds_read_b128 a[0:3], v116 offset:24576\n"
    "ds_read_b128 a[4:7], v116 offset:25600\n"
    "ds_read_b128 a[64:67], v116 offset:26624\n"
    "ds_read_b128 a[68:71], v116 offset:27648\n"
    "ds_read_b128 a[48:51], v116 offset:28672\n"
    "ds_read_b128 a[52:55], v116 offset:29696\n"
    "s_waitcnt lgkmcnt(8)\n"
    "ds_read_b128 a[112:115], v116 offset:30720\n"
    "ds_read_b128 a[116:119], v116 offset:31744\n"
    "v_mov_b64 v[32:33], 0\n"
    "v_mov_b64 v[34:35], 0\n"
    "v_mov_b64 v[36:37], 0\n"
    "v_mov_b64 v[38:39], 0\n"
    "v_mov_b64 v[40:41], 0\n"
    "v_mov_b64 v[42:43], 0\n"
    "v_mov_b64 v[44:45], 0\n"
    "v_mov_b64 v[46:47], 0\n"
    "v_mov_b64 v[48:49], 0\n"
    "v_mov_b64 v[50:51], 0\n"
    "v_mov_b64 v[52:53], 0\n"
    "v_mov_b64 v[54:55], 0\n"
    "s_add_u32 s90, s90, 1\n"
    "s_and_b32 s78, s90, 0xff\n"
    "s_cmp_lg_u32 s78, 0\n"
    "s_cbranch_scc1 L_re1%=\n"
    "v_mov_b32 v126, 0\n"
    "global_load_dwordx2 v[126:127], v126, s[68:69] sc1\n"
    "s_waitcnt vmcnt(0)\n"
    "v_cmp_eq_u64 vcc, s[96:97], v[126:127]\n"
    "s_nop 1\n"
    "s_cbranch_vccnz L_bail_g1%=\n"
    "s_cmp_lt_u32 s90, 0x40000\n"
    "s_cbranch_scc1 L_re1%=\n"
    "v_mov_b32 v126, s96\n"
    "v_mov_b32 v127, s97\n"
    "s_mov_b64 exec, 1\n"
    "global_store_dwordx2 v71, v[126:127], s[68:69] sc1\n"
    "s_mov_b64 exec, -1\n"
    "L_bail_g1%=:\n"
    "s_branch L_go1%=\n"
    "L_re1%=:\n"
    "s_waitcnt vmcnt(1) lgkmcnt(0)\n"
    "s_branch L_top1%=\n"
    "L_last1%=:\n"
    "v_smfmac_f32_16x16x64_bf16 v[52:55], v[4:7], a[200:207], v67\n"
    "v_smfmac_f32_16x16x64_bf16 v[48:51], v[28:31], a[184:191], v67\n"
    "v_smfmac_f32_16x16x64_bf16 v[52:55], v[28:31], a[248:255], v67\n"
    "v_smfmac_f32_16x16x64_bf16 v[48:51], v[20:23], a[168:175], v67\n"
    "v_smfmac_f32_16x16x64_bf16 v[52:55], v[20:23], a[232:239], v67\n"
    "s_nop 3\n"
    "v_pk_add_f32 v[48:49], v[48:49], v[50:51]\n"
    "s_nop 2\n"
    "v_pk_add_f32 v[52:53], v[52:53], v[54:55]\n"
    "v_cvt_pk_bf16_f32 v66, v48, v49\n"
    "v_cvt_pk_bf16_f32 v74, v52, v53\n"
    "s_add_u32 s62, s62, 0x20000\n"
    "s_addc_u32 s63, s63, 0\n"
    "s_cmp_lg_u32 s42, 0\n"
    "s_cbranch_scc1 L_vmc1%=\n"
    "s_mov_b32 exec_lo, 0\n"
    "global_store_dwordx4 v71, v[40:43], s[62:63] offset:0 sc1\n"
    "s_mov_b64 exec, -1\n"
    "s_branch L_kvc1%=\n"
    "L_vmc1%=:\n"
    "v_mov_b32 v126, v66\n"
    "v_mov_b32 v127, v74\n"
    "global_store_dwordx2 v79, v[126:127], s[62:63] offset:512 sc1\n"
    "L_kvc1%=:\n"
    "s_waitcnt vmcnt(0) lgkmcnt(0)\n"
    "s_barrier\n"
    "s_endpgm\n"
    ".p2align 6\n"
    "L_poll2%=:\n"
    "global_load_dwordx4 v[16:19], v71, s[54:55] offset:0 sc1\n"
    "global_load_dwordx4 v[20:23], v71, s[54:55] offset:1024 sc1\n"
    "s_waitcnt vmcnt(0)\n"
    "s_branch L_top2%=\n"
    ".p2align 6\n"
    "L_top2%=:\n"
    "s_waitcnt vmcnt(2)\n"
    "v_smfmac_f32_16x16x64_bf16 v[40:43], v[16:19], a[32:39], v67\n"
    "ds_write_b128 v71, v[16:19] offset:4096\n"
    "v_smfmac_f32_16x16x64_bf16 v[44:47], v[16:19], a[96:103], v67\n"
    "v_smfmac_f32_16x16x64_bf16 v[32:35], v[16:19], v[192:199], v67\n"
    "v_smfmac_f32_16x16x64_bf16 v[36:39], v[16:19], v[200:207], v67\n"
    "s_waitcnt vmcnt(1)\n"
    "v_smfmac_f32_16x16x64_bf16 v[40:43], v[20:23], a[40:47], v67\n"
    "ds_write_b128 v71, v[20:23] offset:5120\n"
    "s_waitcnt lgkmcnt(1)\n"
    "s_barrier\n"
    "ds_read_b128 v[24:27], v71 offset:6144\n"
    "v_smfmac_f32_16x16x64_bf16 v[44:47], v[20:23], a[104:111], v67\n"
    "ds_read_b128 v[0:3], v71 offset:0\n"
    "v_smfmac_f32_16x16x64_bf16 v[32:35], v[20:23], v[208:215], v67\n"
    "ds_read_b128 v[8:11], v71 offset:2048\n"
    "v_smfmac_f32_16x16x64_bf16 v[36:39], v[20:23], v[216:223], v67\n"
    "s_waitcnt lgkmcnt(3)\n"
    "s_barrier\n"
    "v_smfmac_f32_16x16x64_bf16 v[48:51], v[16:19], a[160:167], v67\n"
    "s_waitcnt lgkmcnt(2)\n"
    "ds_read_b128 v[28:31], v71 offset:7168\n"
    "v_smfmac_f32_16x16x64_bf16 v[32:35], v[24:27], v[224:231], v67\n"
    "ds_read_b128 v[4:7], v71 offset:1024\n"
    "v_smfmac_f32_16x16x64_bf16 v[36:39], v[24:27], v[232:239], v67\n"
    "s_waitcnt lgkmcnt(2)\n"
    "ds_read_b128 v[12:15], v71 offset:3072\n"
    "v_smfmac_f32_16x16x64_bf16 v[32:35], v[0:3], v[128:135], v67\n"
    "ds_read_b128 a[32:35], v116 offset:0\n"
    "v_smfmac_f32_16x16x64_bf16 v[36:39], v[0:3], v[136:143], v67\n"
    "ds_read_b128 a[36:39], v116 offset:1024\n"
    "v_smfmac_f32_16x16x64_bf16 v[32:35], v[8:11], v[160:167], v67\n"
    "ds_read_b128 a[96:99], v116 offset:4096\n"
    "v_smfmac_f32_16x16x64_bf16 v[36:39], v[8:11], v[168:175], v67\n"
    "s_waitcnt lgkmcnt(5)\n"
    "ds_read_b128 a[100:103], v116 offset:5120\n"
    "v_smfmac_f32_16x16x64_bf16 v[32:35], v[28:31], v[240:247], v67\n"
    "ds_read_b128 a[40:43], v116 offset:2048\n"
    "v_smfmac_f32_16x16x64_bf16 v[36:39], v[28:31], v[248:255], v67\n"
    "s_waitcnt lgkmcnt(5)\n"
    "ds_read_b128 a[44:47], v116 offset:3072\n"
    "v_smfmac_f32_16x16x64_bf16 v[32:35], v[4:7], v[144:151], v67\n"
    "ds_read_b128 a[104:107], v116 offset:6144\n"
    "v_smfmac_f32_16x16x64_bf16 v[36:39], v[4:7], v[152:159], v67\n"
    "ds_read_b128 a[108:111], v116 offset:7168\n"
    "v_smfmac_f32_16x16x64_bf16 v[32:35], v[12:15], v[176:183], v67\n"
    "v_smfmac_f32_16x16x64_bf16 v[36:39], v[12:15], v[184:191], v67\n"
    "v_smfmac_f32_16x16x64_bf16 v[40:43], v[24:27], a[48:55], v67\n"
    "v_add_f32 v32, v32, v34\n"
    "v_add_f32 v33, v33, v35\n"
    "v_smfmac_f32_16x16x64_bf16 v[44:47], v[24:27], a[112:119], v67\n"
    "ds_read_b128 a[48:51], v116 offset:8192\n"
    "v_cvt_pk_bf16_f32 v126, v32, v33\n"
    "v_add_f32 v36, v36, v38\n"
    "v_add_f32 v37, v37, v39\n"
    "v_cvt_pk_bf16_f32 v127, v36, v37\n"
    "v_smfmac_f32_16x16x64_bf16 v[40:43], v[0:3], a[0:7], v67\n"
    "ds_write_b64 v114, v[126:127] offset:9216\n"
    "v_smfmac_f32_16x16x64_bf16 v[44:47], v[0:3], a[64:71], v67\n"
    "ds_read_b128 a[52:55], v116 offset:9216\n"
    "v_cmp_u_f32 vcc, v32, v33\n"
    "v_smfmac_f32_16x16x64_bf16 v[40:43], v[8:11], a[16:23], v67\n"
    "ds_read_b128 a[112:115], v116 offset:12288\n"
    "v_smfmac_f32_16x16x64_bf16 v[44:47], v[8:11], a[80:87], v67\n"
    "s_cbranch_vccnz L_slow2%=\n"
    "L_go2%=:\n"
    "v_smfmac_f32_16x16x64_bf16 v[40:43], v[28:31], a[56:63], v67\n"
    "s_waitcnt lgkmcnt(2)\n"
    "s_barrier\n"
    "ds_read_b64_tr_b16 v[32:33], v112 offset:8192\n"
    "v_smfmac_f32_16x16x64_bf16 v[44:47], v[28:31], a[120:127], v67\n"
    "ds_read_b64_tr_b16 v[34:35], v113 offset:8192\n"
    "ds_read_b64_tr_b16 v[36:37], v112 offset:9216\n"
    "v_smfmac_f32_16x16x64_bf16 v[40:43], v[4:7], a[8:15], v67\n"
    "ds_read_b64_tr_b16 v[38:39], v113 offset:9216\n"
    "v_smfmac_f32_16x16x64_bf16 v[44:47], v[4:7], a[72:79], v67\n"
    "global_load_dwordx4 v[196:199], v71, s[60:61] offset:1024 sc1\n"
    "v_smfmac_f32_16x16x64_bf16 v[40:43], v[12:15], a[24:31], v67\n"
    "ds_read_b128 a[116:119], v116 offset:13312\n"
    "v_smfmac_f32_16x16x64_bf16 v[44:47], v[12:15], a[88:95], v67\n"
    "s_waitcnt lgkmcnt(1)\n"
    "ds_read_b128 a[0:3], v116 offset:10240\n"
    "v_mfma_f32_16x16x32_bf16 v[88:91], v[56:59], v[32:35], 0\n"
    "v_add_f32 v40, v40, v42\n"
    "v_add_f32 v41, v41, v43\n"
    "v_mfma_f32_16x16x32_bf16 v[88:91], v[60:63], v[36:39], v[88:91]\n"
    "v_cvt_pk_bf16_f32 v126, v40, v41\n"
    "v_add_f32 v44, v44, v46\n"
    "v_add_f32 v45, v45, v47\n"
    "v_cvt_pk_bf16_f32 v127, v44, v45\n"
    "v_smfmac_f32_16x16x64_bf16 v[52:55], v[16:19], a[224:231], v67\n"
    "ds_write_b64 v115, v[126:127] offset:11264\n"
    "v_add_f32_dpp v88, v90, v88 quad_perm:[2,3,2,3] row_mask:0xf bank_mask:0xf\n"
    "v_add_f32_dpp v89, v91, v89 quad_perm:[2,3,2,3] row_mask:0xf bank_mask:0xf\n"
    "v_smfmac_f32_16x16x64_bf16 v[48:51], v[20:23], a[168:175], v67\n"
    "ds_write_b64 v75, v[88:89] offset:14848\n"
    "v_smfmac_f32_16x16x64_bf16 v[52:55], v[20:23], a[232:239], v67\n"
    "global_load_dwordx4 v[56:59], v71, s[58:59] offset:-2048 sc1\n"
    "v_smfmac_f32_16x16x64_bf16 v[48:51], v[24:27], a[176:183], v67\n"
    "global_load_dwordx4 v[60:63], v71, s[58:59] offset:2048 sc1\n"
    "v_smfmac_f32_16x16x64_bf16 v[52:55], v[24:27], a[240:247], v67\n"
    "global_load_dwordx4 v[192:195], v71, s[60:61] offset:0 sc1\n"
    "v_smfmac_f32_16x16x64_bf16 v[48:51], v[0:3], a[128:135], v67\n"
    "s_waitcnt lgkmcnt(0)\n"
    "s_barrier\n"
    "ds_read_b64_tr_b16 v[40:41], v112 offset:10240\n"
    "v_smfmac_f32_16x16x64_bf16 v[52:55], v[0:3], a[192:199], v67\n"
    "ds_read_b64_tr_b16 v[42:43], v113 offset:10240\n"
    "ds_read_b64_tr_b16 v[44:45], v112 offset:11264\n"
    "v_smfmac_f32_16x16x64_bf16 v[48:51], v[8:11], a[144:151], v67\n"
    "ds_read_b64_tr_b16 v[46:47], v113 offset:11264\n"
    "v_smfmac_f32_16x16x64_bf16 v[52:55], v[8:11], a[208:215], v67\n"
    "ds_read_b128 v[88:91], v71 offset:14336\n"
    "s_cmp_eq_u32 s43, 63\n"
    "v_smfmac_f32_16x16x64_bf16 v[48:51], v[28:31], a[184:191], v67\n"
    "s_waitcnt lgkmcnt(3)\n"
    "ds_read_b128 a[4:7], v116 offset:11264\n"
    "v_mfma_f32_16x16x32_bf16 v[96:99], v[40:43], v[32:35], 0\n"
    "s_waitcnt lgkmcnt(2)\n"
    "ds_read_b128 a[64:67], v116 offset:14336\n"
    "v_mfma_f32_16x16x32_bf16 v[96:99], v[44:47], v[36:39], v[96:99]\n"
    "s_cbranch_scc1 L_last2%=\n"
    "v_smfmac_f32_16x16x64_bf16 v[52:55], v[28:31], a[248:255], v67\n"
    "s_waitcnt lgkmcnt(2)\n"
    "ds_read_b128 a[68:71], v116 offset:15360\n"
    "v_max3_f32 v122, v88, v89, v90\n"
    "v_max_f32 v122, v122, v91\n"
    "v_add_f32_dpp v96, v98, v96 quad_perm:[2,3,2,3] row_mask:0xf bank_mask:0xf\n"
    "v_add_f32_dpp v97, v99, v97 quad_perm:[2,3,2,3] row_mask:0xf bank_mask:0xf\n"
    "v_max_f32_dpp v123, v122, v122 quad_perm:[2,3,0,1] row_mask:0xf bank_mask:0xf\n"
    "v_max3_f32 v122, v123, v96, v97\n"
    "v_smfmac_f32_16x16x64_bf16 v[48:51], v[4:7], a[136:143], v67\n"
    "v_max_f32_dpp v121, v122, v123 quad_perm:[0,1,0,1] row_mask:0xf bank_mask:0xf\n"
    "v_max_f32_dpp v123, v122, v123 quad_perm:[0,1,0,1] row_mask:0xf bank_mask:0xf\n"
    "v_smfmac_f32_16x16x64_bf16 v[52:55], v[4:7], a[200:207], v67\n"
    "v_permlane16_swap_b32 v123, v121\n"
    "v_max_f32 v122, v123, v121\n"
    "v_max_f32 v123, v123, v121\n"
    "v_smfmac_f32_16x16x64_bf16 v[48:51], v[12:15], a[152:159], v67\n"
    "v_smfmac_f32_16x16x64_bf16 v[52:55], v[12:15], a[216:223], v67\n"
    "v_permlane32_swap_b32 v122, v123\n"
    "v_max_f32 v122, v122, v123\n"
    "v_mul_f32 v122, s76, v122\n"
    "v_pk_fma_f32 v[96:97], v[96:97], s[76:77], v[122:123] op_sel_hi:[1,1,0] neg_lo:[0,0,1] neg_hi:[0,0,1]\n"
    "v_pk_fma_f32 v[88:89], v[88:89], s[76:77], v[122:123] op_sel_hi:[1,1,0] neg_lo:[0,0,1] neg_hi:[0,0,1]\n"
    "v_pk_fma_f32 v[90:91], v[90:91], s[76:77], v[122:123] op_sel_hi:[1,1,0] neg_lo:[0,0,1] neg_hi:[0,0,1]\n"
    "v_exp_f32 v96, v96\n"
    "v_exp_f32 v97, v97\n"
    "v_exp_f32 v88, v88\n"
    "v_exp_f32 v89, v89\n"
    "v_exp_f32 v90, v90\n"
    "v_exp_f32 v91, v91\n"
    "v_pk_add_f32 v[48:49], v[48:49], v[50:51]\n"
    "v_pk_add_f32 v[52:53], v[52:53], v[54:55]\n"
    "v_cvt_pk_bf16_f32 v126, v96, v97\n"
    "v_cvt_pk_bf16_f32 v100, v88, v89\n"
    "v_cvt_pk_bf16_f32 v102, v90, v91\n"
    "v_cndmask_b32 v101, 0, v126, s[72:73]\n"
    "v_cndmask_b32 v103, 0, v126, s[74:75]\n"
    "v_cvt_pk_bf16_f32 v66, v48, v49\n"
    "v_cvt_pk_bf16_f32 v70, v48, v49\n"
    "v_cvt_pk_bf16_f32 v74, v52, v53\n"
    "v_cvt_pk_bf16_f32 v78, v52, v53\n"
    "v_smfmac_f32_16x16x64_bf16 v[104:107], v[100:103], v[64:71], v67\n"
    "v_smfmac_f32_16x16x64_bf16 v[108:111], v[100:103], v[72:79], v67\n"
    "v_mfma_f32_16x16x32_bf16 v[84:87], v[100:103], v[80:83], 0\n"
    "v_add_f32 v104, v104, v106\n"
    "v_add_f32 v105, v105, v107\n"
    "v_cvt_pk_bf16_f32 v104, v104, v105\n"
    "v_add_f32 v108, v108, v110\n"
    "v_add_f32 v109, v109, v111\n"
    "v_cvt_pk_bf16_f32 v105, v108, v109\n"
    "ds_write_b64 v114, v[104:105] offset:13312\n"
    "v_pk_add_f32 v[124:125], v[84:85], v[86:87]\n"
    "v_rcp_f32 v124, v124\n"
    "v_rcp_f32 v125, v125\n"
    "v_mov_b64 v[0:1], 0\n"
    "v_mov_b64 v[2:3], 0\n"
    "v_mov_b64 v[4:5], 0\n"
    "v_mov_b64 v[6:7], 0\n"
    "v_mov_b64 v[8:9], 0\n"
    "v_mov_b64 v[10:11], 0\n"
    "v_mov_b64 v[12:13], 0\n"
    "v_mov_b64 v[14:15], 0\n"
    "v_mfma_f32_16x16x32_bf16 v[96:99], v[100:103], v[80:83], 0\n"
    "s_waitcnt lgkmcnt(0)\n"
    "s_barrier\n"
    "ds_read_b64_tr_b16 v[32:33], v112 offset:12288\n"
    "v_mfma_f32_16x16x32_bf16 v[96:99], v[100:103], v[80:83], 0\n"
    "ds_read_b64_tr_b16 v[34:35], v113 offset:12288\n"
    "ds_read_b64_tr_b16 v[36:37], v112 offset:13312\n"
    "v_mfma_f32_16x16x32_bf16 v[96:99], v[100:103], v[80:83], 0\n"
    "ds_read_b64_tr_b16 v[38:39], v113 offset:13312\n"
    "v_mfma_f32_16x16x32_bf16 v[96:99], v[100:103], v[80:83], 0\n"
    "s_waitcnt lgkmcnt(2)\n"
    "v_smfmac_f32_16x16x64_bf16 v[0:3], v[32:35], a[32:39], v67\n"
    "v_smfmac_f32_16x16x64_bf16 v[4:7], v[32:35], a[96:103], v67\n"
    "s_waitcnt lgkmcnt(0)\n"
    "v_smfmac_f32_16x16x64_bf16 v[0:3], v[36:39], a[40:47], v67\n"
    "v_smfmac_f32_16x16x64_bf16 v[4:7], v[36:39], a[104:111], v67\n"
    "v_smfmac_f32_16x16x64_bf16 v[8:11], v[32:35], a[48:55], v67\n"
    "v_add_f32 v0, v0, v2\n"
    "v_smfmac_f32_16x16x64_bf16 v[12:15], v[32:35], a[112:119], v67\n"
    "v_add_f32 v1, v1, v3\n"
    "v_smfmac_f32_16x16x64_bf16 v[8:11], v[36:39], a[0:7], v67\n"
    "v_add_f32 v4, v4, v6\n"
    "v_smfmac_f32_16x16x64_bf16 v[12:15], v[36:39], a[64:71], v67\n"
    "v_add_f32 v5, v5, v7\n"
    "v_mul_f32 v0, v0, v124\n"
    "v_mul_f32 v1, v1, v125\n"
    "v_cvt_pk_bf16_f32 v88, v0, v1\n"
    "v_pk_mul_f32 v[4:5], v[4:5], v[124:125]\n"
    "v_pk_add_f32 v[8:9], v[8:9], v[10:11]\n"
    "v_pk_add_f32 v[12:13], v[12:13], v[14:15]\n"
    "v_cvt_pk_bf16_f32 v89, v4, v5\n"
    "v_pk_mul_f32 v[8:9], v[8:9], v[124:125]\n"
    "v_pk_mul_f32 v[12:13], v[12:13], v[124:125]\n"
    "v_cvt_pk_bf16_f32 v90, v8, v9\n"
    "v_cvt_pk_bf16_f32 v91, v12, v13\n"
    "global_store_dwordx4 v117, v[88:91], s[44:45] sc1\n"
    "s_add_u32 s43, s43, 1\n"
    "s_sleep 3\n"
    "s_add_u32 s79, s43, 1\n"
    "s_min_u32 s79, s79, 63\n"
    "s_lshr_b32 s79, s79, 1\n"
    "s_cmp_lt_u32 s79, s92\n"
    "s_cbranch_scc1 L_cntokl%=\n"
    "L_cntl%=:\n"
    "v_and_b32 v126, 16, v71\n"
    "v_lshlrev_b32 v126, 8, v126\n"
    "global_load_dwordx2 v[126:127], v126, s[66:67] sc1\n"
    "s_waitcnt vmcnt(0)\n"
    "v_xor_b32 v126, s96, v126\n"
    "v_xor_b32 v127, s97, v127\n"
    "v_cmp_gt_u64 vcc, 64, v[126:127]\n"
    "s_nop 1\n"
    "v_cndmask_b32 v126, 0, v126, vcc\n"
    "s_nop 0\n"
    "v_readlane_b32 s80, v126, 0\n"
    "v_readlane_b32 s81, v126, 1\n"
    "s_nop 1\n"
    "s_min_u32 s92, s80, s81\n"
    "s_cmp_lt_u32 s79, s92\n"
    "s_cbranch_scc1 L_cntokl%=\n"
    "s_sleep 2\n"
    "s_add_u32 s90, s90, 1\n"
    "s_and_b32 s78, s90, 0xff\n"
    "s_cmp_lg_u32 s78, 0\n"
    "s_cbranch_scc1 L_cntl%=\n"
    "v_mov_b32 v126, 0\n"
    "global_load_dwordx2 v[126:127], v126, s[68:69] sc1\n"
    "s_waitcnt vmcnt(0)\n"
    "v_cmp_eq_u64 vcc, s[96:97], v[126:127]\n"
    "s_nop 1\n"
    "s_cbranch_vccnz L_bail_cntl%=\n"
    "s_cmp_lt_u32 s90, 0x40000\n"
    "s_cbranch_scc1 L_cntl%=\n"
    "v_mov_b32 v126, s96\n"
    "v_mov_b32 v127, s97\n"
    "s_mov_b64 exec, 1\n"
    "global_store_dwordx2 v71, v[126:127], s[68:69] sc1\n"
    "s_mov_b64 exec, -1\n"
    "L_bail_cntl%=:\n"
    "L_cntokl%=:\n"
    "ds_read_b128 a[32:35], v116 offset:16384\n"
    "ds_read_b128 a[36:39], v116 offset:17408\n"
    "ds_read_b128 a[96:99], v116 offset:18432\n"
    "ds_read_b128 a[100:103], v116 offset:19456\n"
    "ds_read_b128 a[40:43], v116 offset:20480\n"
    "ds_read_b128 a[44:47], v116 offset:21504\n"
    "ds_read_b128 a[104:107], v116 offset:22528\n"
    "ds_read_b128 a[108:111], v116 offset:23552\n"
    "ds_read_b128 a[48:51], v116 offset:24576\n"
    "ds_read_b128 a[52:55], v116 offset:25600\n"
    "ds_read_b128 a[112:115], v116 offset:26624\n"
    "ds_read_b128 a[116:119], v116 offset:27648\n"
    "ds_read_b128 a[0:3], v116 offset:28672\n"
    "s_waitcnt lgkmcnt(8)\n"
    "ds_read_b128 a[4:7], v116 offset:29696\n"
    "ds_read_b128 a[64:67], v116 offset:30720\n"
    "ds_read_b128 a[68:71], v116 offset:31744\n"
    "s_and_b32 s78, s43, 3\n"
    "s_lshl_b32 s78, s78, 17\n"
    "s_add_u32 s54, s48, s78\n"
    "s_addc_u32 s55, s49, 0\n"
    "s_add_u32 s62, s62, 0x20000\n"
    "s_addc_u32 s63, s63, 0\n"
    "v_mov_b64 v[32:33], 0\n"
    "v_mov_b64 v[34:35], 0\n"
    "v_mov_b64 v[36:37], 0\n"
    "v_mov_b64 v[38:39], 0\n"
    "v_mov_b64 v[48:49], 0\n"
    "v_mov_b64 v[50:51], 0\n"
    "v_mov_b64 v[52:53], 0\n"
    "v_mov_b64 v[54:55], 0\n"
    "v_mov_b64 v[104:105], 0\n"
    "v_mov_b64 v[106:107], 0\n"
    "v_mov_b64 v[108:109], 0\n"
    "v_mov_b64 v[110:111], 0\n"
    "s_sub_u32 s78, s43, 1\n"
    "s_lshr_b32 s78, s78, 2\n"
    "s_lshr_b32 s79, s43, 2\n"
    "s_lshr_b32 s78, s93, s78\n"
    "s_lshr_b32 s79, s93, s79\n"
    "s_xor_b32 s78, s78, s79\n"
    "s_and_b32 s78, s78, 1\n"
    "s_lshl_b32 s78, s78, 13\n"
    "v_xor_b32 v117, s78, v117\n"
    "s_add_u32 s44, s44, 0x80000\n"
    "s_addc_u32 s45, s45, 0\n"
    "s_add_u32 s58, s58, 0x40000\n"
    "s_addc_u32 s59, s59, 0\n"
    "s_add_u32 s60, s60, 0x40000\n"
    "s_addc_u32 s61, s61, 0\n"
    "s_waitcnt vmcnt(1)\n"
    "v_mov_b64 v[64:65], v[192:193]\n"
    "v_mov_b64 v[68:69], v[194:195]\n"
    "v_mov_b64 v[72:73], v[196:197]\n"
    "v_mov_b64 v[76:77], v[198:199]\n"
    "ds_read_b128 v[192:195], v116 offset:32768\n"
    "ds_read_b128 v[196:199], v116 offset:33792\n"
    "s_waitcnt lgkmcnt(0)\n"
    "s_barrier\n"
    "global_load_dwordx4 v[16:19], v71, s[54:55] offset:0 sc1\n"
    "s_nop 3\n"
    "global_load_dwordx4 v[20:23], v71, s[54:55] offset:1024 sc1\n"
    "s_nop 15\n"
    "s_cmp_lg_u32 s42, 0\n"
    "s_cbranch_scc1 L_vmb2%=\n"
    "s_mov_b32 exec_hi, 0\n"
    "global_store_dwordx4 v71, v[44:47], s[62:63] offset:1024 sc1\n"
    "s_mov_b64 exec, -1\n"
    "s_branch L_kvb2%=\n"
    "L_vmb2%=:\n"
    "v_mov_b32 v126, v66\n"
    "v_mov_b32 v127, v74\n"
    "global_store_dwordx2 v79, v[126:127], s[62:63] offset:1024 sc1\n"
    "L_kvb2%=:\n"
    "s_nop 1\n"
    "v_mov_b64 v[40:41], 0\n"
    "v_mov_b64 v[42:43], 0\n"
    "v_mov_b64 v[44:45], 0\n"
    "v_mov_b64 v[46:47], 0\n"
    "s_branch L_top2%=\n"
    "L_slow2%=:\n"
    "v_cmp_eq_u32 s[78:79], s50, v16\n"
    "v_cmp_eq_u32 s[80:81], s50, v17\n"
    "v_cmp_eq_u32 s[82:83], s50, v18\n"
    "v_cmp_eq_u32 s[84:85], s50, v19\n"
    "v_cmp_eq_u32 s[86:87], s50, v20\n"
    "v_cmp_eq_u32 s[88:89], s50, v21\n"
    "v_cmp_eq_u32 s[52:53], s50, v22\n"
    "v_cmp_eq_u32 vcc, s50, v23\n"
    "global_load_dwordx4 v[16:19], v71, s[54:55] offset:0 sc1\n"
    "global_load_dwordx4 v[20:23], v71, s[54:55] offset:1024 sc1\n"
    "s_waitcnt lgkmcnt(0)\n"
    "s_barrier\n"
    "s_or_b64 vcc, vcc, s[78:79]\n"
    "s_or_b64 vcc, vcc, s[80:81]\n"
    "s_or_b64 vcc, vcc, s[82:83]\n"
    "s_or_b64 vcc, vcc, s[84:85]\n"
    "s_or_b64 vcc, vcc, s[86:87]\n"
    "s_or_b64 vcc, vcc, s[88:89]\n"
    "s_or_b64 vcc, vcc, s[52:53]\n"
    "s_cbranch_scc1 L_redo2%=\n"
    "v_cmp_eq_u32 s[78:79], s50, v24\n"
    "v_cmp_eq_u32 s[80:81], s50, v25\n"
    "v_cmp_eq_u32 s[82:83], s50, v26\n"
    "v_cmp_eq_u32 s[84:85], s50, v27\n"
    "v_cmp_eq_u32 s[86:87], s50, v0\n"
    "v_cmp_eq_u32 s[88:89], s50, v1\n"
    "v_cmp_eq_u32 s[52:53], s50, v2\n"
    "v_cmp_eq_u32 vcc, s50, v3\n"
    "s_nop 0\n"
    "s_or_b64 vcc, vcc, s[78:79]\n"
    "s_or_b64 vcc, vcc, s[80:81]\n"
    "s_or_b64 vcc, vcc, s[82:83]\n"
    "s_or_b64 vcc, vcc, s[84:85]\n"
    "s_or_b64 vcc, vcc, s[86:87]\n"
    "s_or_b64 vcc, vcc, s[88:89]\n"
    "s_or_b64 vcc, vcc, s[52:53]\n"
    "s_cbranch_scc1 L_redo2%=\n"
    "v_cmp_eq_u32 s[78:79], s50, v8\n"
    "v_cmp_eq_u32 s[80:81], s50, v9\n"
    "v_cmp_eq_u32 s[82:83], s50, v10\n"
    "v_cmp_eq_u32 s[84:85], s50, v11\n"
    "v_cmp_eq_u32 s[86:87], s50, v28\n"
    "v_cmp_eq_u32 s[88:89], s50, v29\n"
    "v_cmp_eq_u32 s[52:53], s50, v30\n"
    "v_cmp_eq_u32 vcc, s50, v31\n"
    "s_nop 0\n"
    "s_or_b64 vcc, vcc, s[78:79]\n"
    "s_or_b64 vcc, vcc, s[80:81]\n"
    "s_or_b64 vcc, vcc, s[82:83]\n"
    "s_or_b64 vcc, vcc, s[84:85]\n"
    "s_or_b64 vcc, vcc, s[86:87]\n"
    "s_or_b64 vcc, vcc, s[88:89]\n"
    "s_or_b64 vcc, vcc, s[52:53]\n"
    "s_cbranch_scc1 L_redo2%=\n"
    "v_cmp_eq_u32 s[78:79], s50, v4\n"
    "v_cmp_eq_u32 s[80:81], s50, v5\n"
    "v_cmp_eq_u32 s[82:83], s50, v6\n"
    "v_cmp_eq_u32 s[84:85], s50, v7\n"
    "v_cmp_eq_u32 s[86:87], s50, v12\n"
    "v_cmp_eq_u32 s[88:89], s50, v13\n"
    "v_cmp_eq_u32 s[52:53], s50, v14\n"
    "v_cmp_eq_u32 vcc, s50, v15\n"
    "s_nop 0\n"
    "s_or_b64 vcc, vcc, s[78:79]\n"
    "s_or_b64 vcc, vcc, s[80:81]\n"
    "s_or_b64 vcc, vcc, s[82:83]\n"
    "s_or_b64 vcc, vcc, s[84:85]\n"
    "s_or_b64 vcc, vcc, s[86:87]\n"
    "s_or_b64 vcc, vcc, s[88:89]\n"
    "s_or_b64 vcc, vcc, s[52:53]\n"
    "s_cbranch_scc1 L_redo2%=\n"
    "s_waitcnt vmcnt(0)\n"
    "s_branch L_go2%=\n"
    "L_redo2%=:\n"
    "ds_read_b128 a[32:35], v116 offset:16384\n"
    "ds_read_b128 a[36:39], v116 offset:17408\n"
    "ds_read_b128 a[96:99], v116 offset:18432\n"
    "ds_read_b128 a[100:103], v116 offset:19456\n"
    "ds_read_b128 a[40:43], v116 offset:20480\n"
    "ds_read_b128 a[44:47], v116 offset:21504\n"
    "ds_read_b128 a[104:107], v116 offset:22528\n"
    "ds_read_b128 a[108:111], v116 offset:23552\n"
    "ds_read_b128 a[48:51], v116 offset:24576\n"
    "ds_read_b128 a[52:55], v116 offset:25600\n"
    "ds_read_b128 a[112:115], v116 offset:26624\n"
    "ds_read_b128 a[116:119], v116 offset:27648\n"
    "ds_read_b128 a[0:3], v116 offset:28672\n"
    "ds_read_b128 a[4:7], v116 offset:29696\n"
    "s_waitcnt lgkmcnt(8)\n"
    "ds_read_b128 a[64:67], v116 offset:30720\n"
    "ds_read_b128 a[68:71], v116 offset:31744\n"
    "v_mov_b64 v[32:33], 0\n"
    "v_mov_b64 v[34:35], 0\n"
    "v_mov_b64 v[36:37], 0\n"
    "v_mov_b64 v[38:39], 0\n"
    "v_mov_b64 v[40:41], 0\n"
    "v_mov_b64 v[42:43], 0\n"
    "v_mov_b64 v[44:45], 0\n"
    "v_mov_b64 v[46:47], 0\n"
    "v_mov_b64 v[48:49], 0\n"
    "v_mov_b64 v[50:51], 0\n"
    "v_mov_b64 v[52:53], 0\n"
    "v_mov_b64 v[54:55], 0\n"
    "s_add_u32 s90, s90, 1\n"
    "s_and_b32 s78, s90, 0xff\n"
    "s_cmp_lg_u32 s78, 0\n"
    "s_cbranch_scc1 L_re2%=\n"
    "v_mov_b32 v126, 0\n"
    "global_load_dwordx2 v[126:127], v126, s[68:69] sc1\n"
    "s_waitcnt vmcnt(0)\n"
    "v_cmp_eq_u64 vcc, s[96:97], v[126:127]\n"
    "s_nop 1\n"
    "s_cbranch_vccnz L_bail_g2%=\n"
    "s_cmp_lt_u32 s90, 0x40000\n"
    "s_cbranch_scc1 L_re2%=\n"
    "v_mov_b32 v126, s96\n"
    "v_mov_b32 v127, s97\n"
    "s_mov_b64 exec, 1\n"
    "global_store_dwordx2 v71, v[126:127], s[68:69] sc1\n"
    "s_mov_b64 exec, -1\n"
    "L_bail_g2%=:\n"
    "s_branch L_go2%=\n"
    "L_re2%=:\n"
    "s_waitcnt vmcnt(0) lgkmcnt(0)\n"
    "s_branch L_top2%=\n"
    "L_last2%=:\n"
    "v_smfmac_f32_16x16x64_bf16 v[52:55], v[28:31], a[248:255], v67\n"
    "v_smfmac_f32_16x16x64_bf16 v[48:51], v[4:7], a[136:143], v67\n"
    "v_smfmac_f32_16x16x64_bf16 v[52:55], v[4:7], a[200:207], v67\n"
    "v_smfmac_f32_16x16x64_bf16 v[48:51], v[12:15], a[152:159], v67\n"
    "v_smfmac_f32_16x16x64_bf16 v[52:55], v[12:15], a[216:223], v67\n"
    "s_nop 3\n"
    "v_pk_add_f32 v[48:49], v[48:49], v[50:51]\n"
    "s_nop 2\n"
    "v_pk_add_f32 v[52:53], v[52:53], v[54:55]\n"
    "v_cvt_pk_bf16_f32 v66, v48, v49\n"
    "v_cvt_pk_bf16_f32 v74, v52, v53\n"
    "s_add_u32 s62, s62, 0x20000\n"
    "s_addc_u32 s63, s63, 0\n"
    "s_cmp_lg_u32 s42, 0\n"
    "s_cbranch_scc1 L_vmc2%=\n"
    "s_mov_b32 exec_hi, 0\n"
    "global_store_dwordx4 v71, v[44:47], s[62:63] offset:1024 sc1\n"
    "s_mov_b64 exec, -1\n"
    "s_branch L_kvc2%=\n"
    "L_vmc2%=:\n"
    "v_mov_b32 v126, v66\n"
    "v_mov_b32 v127, v74\n"
    "global_store_dwordx2 v79, v[126:127], s[62:63] offset:1024 sc1\n"
    "L_kvc2%=:\n"
    "s_waitcnt vmcnt(0) lgkmcnt(0)\n"
    "s_barrier\n"
    "s_endpgm\n"
    ".p2align 6\n"
    "L_poll3%=:\n"
    "global_load_dwordx4 v[24:27], v71, s[54:55] offset:0 sc1\n"
    "global_load_dwordx4 v[28:31], v71, s[54:55] offset:1024 sc1\n"
    "s_waitcnt vmcnt(0)\n"
    "s_branch L_top3%=\n"
    ".p2align 6\n"
    "L_top3%=:\n"
    "s_waitcnt vmcnt(2)\n"
    "v_smfmac_f32_16x16x64_bf16 v[40:43], v[24:27], a[48:55], v67\n"
    "ds_write_b128 v71, v[24:27] offset:6144\n"
    "v_smfmac_f32_16x16x64_bf16 v[44:47], v[24:27], a[112:119], v67\n"
    "v_smfmac_f32_16x16x64_bf16 v[32:35], v[24:27], v[224:231], v67\n"
    "v_smfmac_f32_16x16x64_bf16 v[36:39], v[24:27], v[232:239], v67\n"
    "s_waitcnt vmcnt(1)\n"
    "v_smfmac_f32_16x16x64_bf16 v[40:43], v[28:31], a[56:63], v67\n"
    "ds_write_b128 v71, v[28:31] offset:7168\n"
    "s_waitcnt lgkmcnt(1)\n"
    "s_barrier\n"
    "ds_read_b128 v[16:19], v71 offset:4096\n"
    "v_smfmac_f32_16x16x64_bf16 v[44:47], v[28:31], a[120:127], v67\n"
    "ds_read_b128 v[8:11], v71 offset:2048\n"
    "v_smfmac_f32_16x16x64_bf16 v[32:35], v[28:31], v[240:247], v67\n"
    "ds_read_b128 v[0:3], v71 offset:0\n"
    "v_smfmac_f32_16x16x64_bf16 v[36:39], v[28:31], v[248:255], v67\n"
    "s_waitcnt lgkmcnt(3)\n"
    "s_barrier\n"
    "v_smfmac_f32_16x16x64_bf16 v[48:51], v[24:27], a[176:183], v67\n"
    "s_waitcnt lgkmcnt(2)\n"
    "ds_read_b128 v[20:23], v71 offset:5120\n"
    "v_smfmac_f32_16x16x64_bf16 v[32:35], v[16:19], v[192:199], v67\n"
    "ds_read_b128 v[12:15], v71 offset:3072\n"
    "v_smfmac_f32_16x16x64_bf16 v[36:39], v[16:19], v[200:207], v67\n"
    "s_waitcnt lgkmcnt(2)\n"
    "ds_read_b128 v[4:7], v71 offset:1024\n"
    "v_smfmac_f32_16x16x64_bf16 v[32:35], v[8:11], v[160:167], v67\n"
    "ds_read_b128 a[48:51], v116 offset:0\n"
    "v_smfmac_f32_16x16x64_bf16 v[36:39], v[8:11], v[168:175], v67\n"
    "ds_read_b128 a[52:55], v116 offset:1024\n"
    "v_smfmac_f32_16x16x64_bf16 v[32:35], v[0:3], v[128:135], v67\n"
    "ds_read_b128 a[112:115], v116 offset:4096\n"
    "v_smfmac_f32_16x16x64_bf16 v[36:39], v[0:3], v[136:143], v67\n"
    "s_waitcnt lgkmcnt(5)\n"
    "ds_read_b128 a[116:119], v116 offset:5120\n"
    "v_smfmac_f32_16x16x64_bf16 v[32:35], v[20:23], v[208:215], v67\n"
    "ds_read_b128 a[56:59], v116 offset:2048\n"
    "v_smfmac_f32_16x16x64_bf16 v[36:39], v[20:23], v[216:223], v67\n"
    "s_waitcnt lgkmcnt(5)\n"
    "ds_read_b128 a[60:63], v116 offset:3072\n"
    "v_smfmac_f32_16x16x64_bf16 v[32:35], v[12:15], v[176:183], v67\n"
    "ds_read_b128 a[120:123], v116 offset:6144\n"
    "v_smfmac_f32_16x16x64_bf16 v[36:39], v[12:15], v[184:191], v67\n"
    "ds_read_b128 a[124:127], v116 offset:7168\n"
    "v_smfmac_f32_16x16x64_bf16 v[32:35], v[4:7], v[144:151], v67\n"
    "v_smfmac_f32_16x16x64_bf16 v[36:39], v[4:7], v[152:159], v67\n"
    "v_smfmac_f32_16x16x64_bf16 v[40:43], v[16:19], a[32:39], v67\n"
    "v_add_f32 v32, v32, v34\n"
    "v_add_f32 v33, v33, v35\n"
    "v_smfmac_f32_16x16x64_bf16 v[44:47], v[16:19], a[96:103], v67\n"
    "ds_read_b128 a[32:35], v116 offset:8192\n"
    "v_cvt_pk_bf16_f32 v126, v32, v33\n"
    "v_add_f32 v36, v36, v38\n"
    "v_add_f32 v37, v37, v39\n"
    "v_cvt_pk_bf16_f32 v127, v36, v37\n"
    "v_smfmac_f32_16x16x64_bf16 v[40:43], v[8:11], a[16:23], v67\n"
    "ds_write_b64 v114, v[126:127] offset:9728\n"
    "v_smfmac_f32_16x16x64_bf16 v[44:47], v[8:11], a[80:87], v67\n"
    "ds_read_b128 a[36:39], v116 offset:9216\n"
    "v_cmp_u_f32 vcc, v32, v33\n"
    "v_smfmac_f32_16x16x64_bf16 v[40:43], v[0:3], a[0:7], v67\n"
    "ds_read_b128 a[96:99], v116 offset:12288\n"
    "v_smfmac_f32_16x16x64_bf16 v[44:47], v[0:3], a[64:71], v67\n"
    "s_cbranch_vccnz L_slow3%=\n"
    "L_go3%=:\n"
    "v_smfmac_f32_16x16x64_bf16 v[40:43], v[20:23], a[40:47], v67\n"
    "s_waitcnt lgkmcnt(2)\n"
    "s_barrier\n"
    "ds_read_b64_tr_b16 v[32:33], v112 offset:8192\n"
    "v_smfmac_f32_16x16x64_bf16 v[44:47], v[20:23], a[104:111], v67\n"
    "ds_read_b64_tr_b16 v[34:35], v113 offset:8192\n"
    "ds_read_b64_tr_b16 v[36:37], v112 offset:9216\n"
    "v_smfmac_f32_16x16x64_bf16 v[40:43], v[12:15], a[24:31], v67\n"
    "ds_read_b64_tr_b16 v[38:39], v113 offset:9216\n"
    "v_smfmac_f32_16x16x64_bf16 v[44:47], v[12:15], a[88:95], v67\n"
    "global_load_dwordx4 v[228:231], v71, s[60:61] offset:1024 sc1\n"
    "v_smfmac_f32_16x16x64_bf16 v[40:43], v[4:7], a[8:15], v67\n"
    "ds_read_b128 a[100:103], v116 offset:13312\n"
    "v_smfmac_f32_16x16x64_bf16 v[44:47], v[4:7], a[72:79], v67\n"
    "s_waitcnt lgkmcnt(1)\n"
    "ds_read_b128 a[16:19], v116 offset:10240\n"
    "v_mfma_f32_16x16x32_bf16 v[88:91], v[56:59], v[32:35], 0\n"
    "v_add_f32 v40, v40, v42\n"
    "v_add_f32 v41, v41, v43\n"
    "v_mfma_f32_16x16x32_bf16 v[88:91], v[60:63], v[36:39], v[88:91]\n"
    "v_cvt_pk_bf16_f32 v126, v40, v41\n"
    "v_add_f32 v44, v44, v46\n"
    "v_add_f32 v45, v45, v47\n"
    "v_cvt_pk_bf16_f32 v127, v44, v45\n"
    "v_smfmac_f32_16x16x64_bf16 v[52:55], v[24:27], a[240:247], v67\n"
    "ds_write_b64 v115, v[126:127] offset:11776\n"
    "v_add_f32_dpp v88, v90, v88 quad_perm:[2,3,2,3] row_mask:0xf bank_mask:0xf\n"
    "v_add_f32_dpp v89, v91, v89 quad_perm:[2,3,2,3] row_mask:0xf bank_mask:0xf\n"
    "v_smfmac_f32_16x16x64_bf16 v[48:51], v[28:31], a[184:191], v67\n"
    "ds_write_b64 v75, v[88:89] offset:14880\n"
    "v_smfmac_f32_16x16x64_bf16 v[52:55], v[28:31], a[248:255], v67\n"
    "global_load_dwordx4 v[56:59], v71, s[58:59] offset:-2048 sc1\n"
    "v_smfmac_f32_16x16x64_bf16 v[48:51], v[16:19], a[160:167], v67\n"
    "global_load_dwordx4 v[60:63], v71, s[58:59] offset:2048 sc1\n"
    "v_smfmac_f32_16x16x64_bf16 v[52:55], v[16:19], a[224:231], v67\n"
    "global_load_dwordx4 v[224:227], v71, s[60:61] offset:0 sc1\n"
    "v_smfmac_f32_16x16x64_bf16 v[48:51], v[8:11], a[144:151], v67\n"
    "s_waitcnt lgkmcnt(0)\n"
    "s_barrier\n"
    "ds_read_b64_tr_b16 v[40:41], v112 offset:10240\n"
    "v_smfmac_f32_16x16x64_bf16 v[52:55], v[8:11], a[208:215], v67\n"
    "ds_read_b64_tr_b16 v[42:43], v113 offset:10240\n"
    "ds_read_b64_tr_b16 v[44:45], v112 offset:11264\n"
    "v_smfmac_f32_16x16x64_bf16 v[48:51], v[0:3], a[128:135], v67\n"
    "ds_read_b64_tr_b16 v[46:47], v113 offset:11264\n"
    "v_smfmac_f32_16x16x64_bf16 v[52:55], v[0:3], a[192:199], v67\n"
    "ds_read_b128 v[88:91], v71 offset:14336\n"
    "s_cmp_eq_u32 s43, 63\n"
    "v_smfmac_f32_16x16x64_bf16 v[48:51], v[20:23], a[168:175], v67\n"
    "s_waitcnt lgkmcnt(3)\n"
    "ds_read_b128 a[20:23], v116 offset:11264\n"
    "v_mfma_f32_16x16x32_bf16 v[96:99], v[40:43], v[32:35], 0\n"
    "s_waitcnt lgkmcnt(2)\n"
    "ds_read_b128 a[80:83], v116 offset:14336\n"
    "v_mfma_f32_16x16x32_bf16 v[96:99], v[44:47], v[36:39], v[96:99]\n"
    "s_cbranch_scc1 L_last3%=\n"
    "v_smfmac_f32_16x16x64_bf16 v[52:55], v[20:23], a[232:239], v67\n"
    "s_waitcnt lgkmcnt(2)\n"
    "ds_read_b128 a[84:87], v116 offset:15360\n"
    "v_max3_f32 v122, v88, v89, v90\n"
    "v_max_f32 v122, v122, v91\n"
    "v_add_f32_dpp v96, v98, v96 quad_perm:[2,3,2,3] row_mask:0xf bank_mask:0xf\n"
    "v_add_f32_dpp v97, v99, v97 quad_perm:[2,3,2,3] row_mask:0xf bank_mask:0xf\n"
    "v_max_f32_dpp v123, v122, v122 quad_perm:[2,3,0,1] row_mask:0xf bank_mask:0xf\n"
    "v_max3_f32 v122, v123, v96, v97\n"
    "v_smfmac_f32_16x16x64_bf16 v[48:51], v[12:15], a[152:159], v67\n"
    "v_max_f32_dpp v121, v122, v123 quad_perm:[0,1,0,1] row_mask:0xf bank_mask:0xf\n"
    "v_max_f32_dpp v123, v122, v123 quad_perm:[0,1,0,1] row_mask:0xf bank_mask:0xf\n"
    "v_smfmac_f32_16x16x64_bf16 v[52:55], v[12:15], a[216:223], v67\n"
    "v_permlane16_swap_b32 v123, v121\n"
    "v_max_f32 v122, v123, v121\n"
    "v_max_f32 v123, v123, v121\n"
    "v_smfmac_f32_16x16x64_bf16 v[48:51], v[4:7], a[136:143], v67\n"
    "v_smfmac_f32_16x16x64_bf16 v[52:55], v[4:7], a[200:207], v67\n"
    "v_permlane32_swap_b32 v122, v123\n"
    "v_max_f32 v122, v122, v123\n"
    "v_mul_f32 v122, s76, v122\n"
    "v_pk_fma_f32 v[96:97], v[96:97], s[76:77], v[122:123] op_sel_hi:[1,1,0] neg_lo:[0,0,1] neg_hi:[0,0,1]\n"
    "v_pk_fma_f32 v[88:89], v[88:89], s[76:77], v[122:123] op_sel_hi:[1,1,0] neg_lo:[0,0,1] neg_hi:[0,0,1]\n"
    "v_pk_fma_f32 v[90:91], v[90:91], s[76:77], v[122:123] op_sel_hi:[1,1,0] neg_lo:[0,0,1] neg_hi:[0,0,1]\n"
    "v_exp_f32 v96, v96\n"
    "v_exp_f32 v97, v97\n"
    "v_exp_f32 v88, v88\n"
    "v_exp_f32 v89, v89\n"
    "v_exp_f32 v90, v90\n"
    "v_exp_f32 v91, v91\n"
    "v_pk_add_f32 v[48:49], v[48:49], v[50:51]\n"
    "v_pk_add_f32 v[52:53], v[52:53], v[54:55]\n"
    "v_cvt_pk_bf16_f32 v126, v96, v97\n"
    "v_cvt_pk_bf16_f32 v100, v88, v89\n"
    "v_cvt_pk_bf16_f32 v102, v90, v91\n"
    "v_cndmask_b32 v101, 0, v126, s[72:73]\n"
    "v_cndmask_b32 v103, 0, v126, s[74:75]\n"
    "v_cvt_pk_bf16_f32 v66, v48, v49\n"
    "v_cvt_pk_bf16_f32 v70, v48, v49\n"
    "v_cvt_pk_bf16_f32 v74, v52, v53\n"
    "v_cvt_pk_bf16_f32 v78, v52, v53\n"
    "v_smfmac_f32_16x16x64_bf16 v[104:107], v[100:103], v[64:71], v67\n"
    "v_smfmac_f32_16x16x64_bf16 v[108:111], v[100:103], v[72:79], v67\n"
    "v_mfma_f32_16x16x32_bf16 v[84:87], v[100:103], v[80:83], 0\n"
    "v_add_f32 v104, v104, v106\n"
    "v_add_f32 v105, v105, v107\n"
    "v_cvt_pk_bf16_f32 v104, v104, v105\n"
    "v_add_f32 v108, v108, v110\n"
    "v_add_f32 v109, v109, v111\n"
    "v_cvt_pk_bf16_f32 v105, v108, v109\n"
    "ds_write_b64 v114, v[104:105] offset:13824\n"
    "v_pk_add_f32 v[124:125], v[84:85], v[86:87]\n"
    "v_rcp_f32 v124, v124\n"
    "v_rcp_f32 v125, v125\n"
    "v_mov_b64 v[0:1], 0\n"
    "v_mov_b64 v[2:3], 0\n"
    "v_mov_b64 v[4:5], 0\n"
    "v_mov_b64 v[6:7], 0\n"
    "v_mov_b64 v[8:9], 0\n"
    "v_mov_b64 v[10:11], 0\n"
    "v_mov_b64 v[12:13], 0\n"
    "v_mov_b64 v[14:15], 0\n"
    "v_mfma_f32_16x16x32_bf16 v[96:99], v[100:103], v[80:83], 0\n"
    "s_waitcnt lgkmcnt(0)\n"
    "s_barrier\n"
    "ds_read_b64_tr_b16 v[32:33], v112 offset:12288\n"
    "v_mfma_f32_16x16x32_bf16 v[96:99], v[100:103], v[80:83], 0\n"
    "ds_read_b64_tr_b16 v[34:35], v113 offset:12288\n"
    "ds_read_b64_tr_b16 v[36:37], v112 offset:13312\n"
    "v_mfma_f32_16x16x32_bf16 v[96:99], v[100:103], v[80:83], 0\n"
    "ds_read_b64_tr_b16 v[38:39], v113 offset:13312\n"
    "v_mfma_f32_16x16x32_bf16 v[96:99], v[100:103], v[80:83], 0\n"
    "s_waitcnt lgkmcnt(2)\n"
    "v_smfmac_f32_16x16x64_bf16 v[0:3], v[32:35], a[48:55], v67\n"
    "v_smfmac_f32_16x16x64_bf16 v[4:7], v[32:35], a[112:119], v67\n"
    "s_waitcnt lgkmcnt(0)\n"
    "v_smfmac_f32_16x16x64_bf16 v[0:3], v[36:39], a[56:63], v67\n"
    "v_smfmac_f32_16x16x64_bf16 v[4:7], v[36:39], a[120:127], v67\n"
    "v_smfmac_f32_16x16x64_bf16 v[8:11], v[32:35], a[32:39], v67\n"
    "v_add_f32 v0, v0, v2\n"
    "v_smfmac_f32_16x16x64_bf16 v[12:15], v[32:35], a[96:103], v67\n"
    "v_add_f32 v1, v1, v3\n"
    "v_smfmac_f32_16x16x64_bf16 v[8:11], v[36:39], a[16:23], v67\n"
    "v_add_f32 v4, v4, v6\n"
    "v_smfmac_f32_16x16x64_bf16 v[12:15], v[36:39], a[80:87], v67\n"
    "v_add_f32 v5, v5, v7\n"
    "v_mul_f32 v0, v0, v124\n"
    "v_mul_f32 v1, v1, v125\n"
    "v_cvt_pk_bf16_f32 v88, v0, v1\n"
    "v_pk_mul_f32 v[4:5], v[4:5], v[124:125]\n"
    "v_pk_add_f32 v[8:9], v[8:9], v[10:11]\n"
    "v_pk_add_f32 v[12:13], v[12:13], v[14:15]\n"
    "v_cvt_pk_bf16_f32 v89, v4, v5\n"
    "v_pk_mul_f32 v[8:9], v[8:9], v[124:125]\n"
    "v_pk_mul_f32 v[12:13], v[12:13], v[124:125]\n"
    "v_cvt_pk_bf16_f32 v90, v8, v9\n"
    "v_cvt_pk_bf16_f32 v91, v12, v13\n"
    "global_store_dwordx4 v117, v[88:91], s[44:45] sc1\n"
    "s_add_u32 s43, s43, 1\n"
    "s_sleep 3\n"
    "ds_read_b128 a[48:51], v116 offset:16384\n"
    "ds_read_b128 a[52:55], v116 offset:17408\n"
    "ds_read_b128 a[112:115], v116 offset:18432\n"
    "ds_read_b128 a[116:119], v116 offset:19456\n"
    "ds_read_b128 a[56:59], v116 offset:20480\n"
    "ds_read_b128 a[60:63], v116 offset:21504\n"
    "ds_read_b128 a[120:123], v116 offset:22528\n"
    "ds_read_b128 a[124:127], v116 offset:23552\n"
    "ds_read_b128 a[32:35], v116 offset:24576\n"
    "ds_read_b128 a[36:39], v116 offset:25600\n"
    "ds_read_b128 a[96:99], v116 offset:26624\n"
    "ds_read_b128 a[100:103], v116 offset:27648\n"
    "ds_read_b128 a[16:19], v116 offset:28672\n"
    "s_waitcnt lgkmcnt(8)\n"
    "ds_read_b128 a[20:23], v116 offset:29696\n"
    "ds_read_b128 a[80:83], v116 offset:30720\n"
    "ds_read_b128 a[84:87], v116 offset:31744\n"
    "s_and_b32 s78, s43, 3\n"
    "s_lshl_b32 s78, s78, 17\n"
    "s_add_u32 s54, s48, s78\n"
    "s_addc_u32 s55, s49, 0\n"
    "s_add_u32 s62, s62, 0x20000\n"
    "s_addc_u32 s63, s63, 0\n"
    "v_mov_b64 v[32:33], 0\n"
    "v_mov_b64 v[34:35], 0\n"
    "v_mov_b64 v[36:37], 0\n"
    "v_mov_b64 v[38:39], 0\n"
    "v_mov_b64 v[48:49], 0\n"
    "v_mov_b64 v[50:51], 0\n"
    "v_mov_b64 v[52:53], 0\n"
    "v_mov_b64 v[54:55], 0\n"
    "v_mov_b64 v[104:105], 0\n"
    "v_mov_b64 v[106:107], 0\n"
    "v_mov_b64 v[108:109], 0\n"
    "v_mov_b64 v[110:111], 0\n"
    "s_sub_u32 s78, s43, 1\n"
    "s_lshr_b32 s78, s78, 2\n"
    "s_lshr_b32 s79, s43, 2\n"
    "s_lshr_b32 s78, s93, s78\n"
    "s_lshr_b32 s79, s93, s79\n"
    "s_xor_b32 s78, s78, s79\n"
    "s_and_b32 s78, s78, 1\n"
    "s_lshl_b32 s78, s78, 13\n"
    "v_xor_b32 v117, s78, v117\n"
    "s_add_u32 s44, s44, 0x80000\n"
    "s_addc_u32 s45, s45, 0\n"
    "s_add_u32 s58, s58, 0x40000\n"
    "s_addc_u32 s59, s59, 0\n"
    "s_add_u32 s60, s60, 0x40000\n"
    "s_addc_u32 s61, s61, 0\n"
    "s_waitcnt vmcnt(1)\n"
    "v_mov_b64 v[64:65], v[224:225]\n"
    "v_mov_b64 v[68:69], v[226:227]\n"
    "v_mov_b64 v[72:73], v[228:229]\n"
    "v_mov_b64 v[76:77], v[230:231]\n"
    "ds_read_b128 v[224:227], v116 offset:32768\n"
    "ds_read_b128 v[228:231], v116 offset:33792\n"
    "s_waitcnt lgkmcnt(0)\n"
    "s_barrier\n"
    "global_load_dwordx4 v[24:27], v71, s[54:55] offset:0 sc1\n"
    "s_nop 3\n"
    "global_load_dwordx4 v[28:31], v71, s[54:55] offset:1024 sc1\n"
    "s_nop 15\n"
    "s_cmp_lg_u32 s42, 0\n"
    "s_cbranch_scc1 L_vmb3%=\n"
    "s_mov_b32 exec_lo, 0\n"
    "global_store_dwordx4 v71, v[44:47], s[62:63] offset:1024 sc1\n"
    "s_mov_b64 exec, -1\n"
    "s_branch L_kvb3%=\n"
    "L_vmb3%=:\n"
    "v_mov_b32 v126, v66\n"
    "v_mov_b32 v127, v74\n"
    "global_store_dwordx2 v79, v[126:127], s[62:63] offset:1536 sc1\n"
    "L_kvb3%=:\n"
    "s_nop 1\n"
    "v_mov_b64 v[40:41], 0\n"
    "v_mov_b64 v[42:43], 0\n"
    "v_mov_b64 v[44:45], 0\n"
    "v_mov_b64 v[46:47], 0\n"
    "s_branch L_top3%=\n"
    "L_slow3%=:\n"
    "v_cmp_eq_u32 s[78:79], s50, v24\n"
    "v_cmp_eq_u32 s[80:81], s50, v25\n"
    "v_cmp_eq_u32 s[82:83], s50, v26\n"
    "v_cmp_eq_u32 s[84:85], s50, v27\n"
    "v_cmp_eq_u32 s[86:87], s50, v28\n"
    "v_cmp_eq_u32 s[88:89], s50, v29\n"
    "v_cmp_eq_u32 s[52:53], s50, v30\n"
    "v_cmp_eq_u32 vcc, s50, v31\n"
    "global_load_dwordx4 v[24:27], v71, s[54:55] offset:0 sc1\n"
    "global_load_dwordx4 v[28:31], v71, s[54:55] offset:1024 sc1\n"
    "s_waitcnt lgkmcnt(0)\n"
    "s_barrier\n"
    "s_or_b64 vcc, vcc, s[78:79]\n"
    "s_or_b64 vcc, vcc, s[80:81]\n"
    "s_or_b64 vcc, vcc, s[82:83]\n"
    "s_or_b64 vcc, vcc, s[84:85]\n"
    "s_or_b64 vcc, vcc, s[86:87]\n"
    "s_or_b64 vcc, vcc, s[88:89]\n"
    "s_or_b64 vcc, vcc, s[52:53]\n"
    "s_cbranch_scc1 L_redo3%=\n"
    "v_cmp_eq_u32 s[78:79], s50, v16\n"
    "v_cmp_eq_u32 s[80:81], s50, v17\n"
    "v_cmp_eq_u32 s[82:83], s50, v18\n"
    "v_cmp_eq_u32 s[84:85], s50, v19\n"
    "v_cmp_eq_u32 s[86:87], s50, v8\n"
    "v_cmp_eq_u32 s[88:89], s50, v9\n"
    "v_cmp_eq_u32 s[52:53], s50, v10\n"
    "v_cmp_eq_u32 vcc, s50, v11\n"
    "s_nop 0\n"
    "s_or_b64 vcc, vcc, s[78:79]\n"
    "s_or_b64 vcc, vcc, s[80:81]\n"
    "s_or_b64 vcc, vcc, s[82:83]\n"
    "s_or_b64 vcc, vcc, s[84:85]\n"
    "s_or_b64 vcc, vcc, s[86:87]\n"
    "s_or_b64 vcc, vcc, s[88:89]\n"
    "s_or_b64 vcc, vcc, s[52:53]\n"
    "s_cbranch_scc1 L_redo3%=\n"
    "v_cmp_eq_u32 s[78:79], s50, v0\n"
    "v_cmp_eq_u32 s[80:81], s50, v1\n"
    "v_cmp_eq_u32 s[82:83], s50, v2\n"
    "v_cmp_eq_u32 s[84:85], s50, v3\n"
    "v_cmp_eq_u32 s[86:87], s50, v20\n"
    "v_cmp_eq_u32 s[88:89], s50, v21\n"
    "v_cmp_eq_u32 s[52:53], s50, v22\n"
    "v_cmp_eq_u32 vcc, s50, v23\n"
    "s_nop 0\n"
    "s_or_b64 vcc, vcc, s[78:79]\n"
    "s_or_b64 vcc, vcc, s[80:81]\n"
    "s_or_b64 vcc, vcc, s[82:83]\n"
    "s_or_b64 vcc, vcc, s[84:85]\n"
    "s_or_b64 vcc, vcc, s[86:87]\n"
    "s_or_b64 vcc, vcc, s[88:89]\n"
    "s_or_b64 vcc, vcc, s[52:53]\n"
    "s_cbranch_scc1 L_redo3%=\n"
    "v_cmp_eq_u32 s[78:79], s50, v12\n"
    "v_cmp_eq_u32 s[80:81], s50, v13\n"
    "v_cmp_eq_u32 s[82:83], s50, v14\n"
    "v_cmp_eq_u32 s[84:85], s50, v15\n"
    "v_cmp_eq_u32 s[86:87], s50, v4\n"
    "v_cmp_eq_u32 s[88:89], s50, v5\n"
    "v_cmp_eq_u32 s[52:53], s50, v6\n"
    "v_cmp_eq_u32 vcc, s50, v7\n"
    "s_nop 0\n"
    "s_or_b64 vcc, vcc, s[78:79]\n"
    "s_or_b64 vcc, vcc, s[80:81]\n"
    "s_or_b64 vcc, vcc, s[82:83]\n"
    "s_or_b64 vcc, vcc, s[84:85]\n"
    "s_or_b64 vcc, vcc, s[86:87]\n"
    "s_or_b64 vcc, vcc, s[88:89]\n"
    "s_or_b64 vcc, vcc, s[52:53]\n"
    "s_cbranch_scc1 L_redo3%=\n"
    "s_waitcnt vmcnt(0)\n"
    "s_branch L_go3%=\n"
    "L_redo3%=:\n"
    "ds_read_b128 a[48:51], v116 offset:16384\n"
    "ds_read_b128 a[52:55], v116 offset:17408\n"
    "ds_read_b128 a[112:115], v116 offset:18432\n"
    "ds_read_b128 a[116:119], v116 offset:19456\n"
    "ds_read_b128 a[56:59], v116 offset:20480\n"
    "ds_read_b128 a[60:63], v116 offset:21504\n"
    "ds_read_b128 a[120:123], v116 offset:22528\n"
    "ds_read_b128 a[124:127], v116 offset:23552\n"
    "ds_read_b128 a[32:35], v116 offset:24576\n"
    "ds_read_b128 a[36:39], v116 offset:25600\n"
    "ds_read_b128 a[96:99], v116 offset:26624\n"
    "ds_read_b128 a[100:103], v116 offset:27648\n"
    "ds_read_b128 a[16:19], v116 offset:28672\n"
    "ds_read_b128 a[20:23], v116 offset:29696\n"
    "s_waitcnt lgkmcnt(8)\n"
    "ds_read_b128 a[80:83], v116 offset:30720\n"
    "ds_read_b128 a[84:87], v116 offset:31744\n"
    "v_mov_b64 v[32:33], 0\n"
    "v_mov_b64 v[34:35], 0\n"
    "v_mov_b64 v[36:37], 0\n"
    "v_mov_b64 v[38:39], 0\n"
    "v_mov_b64 v[40:41], 0\n"
    "v_mov_b64 v[42:43], 0\n"
    "v_mov_b64 v[44:45], 0\n"
    "v_mov_b64 v[46:47], 0\n"
    "v_mov_b64 v[48:49], 0\n"
    "v_mov_b64 v[50:51], 0\n"
    "v_mov_b64 v[52:53], 0\n"
    "v_mov_b64 v[54:55], 0\n"
    "s_add_u32 s90, s90, 1\n"
    "s_and_b32 s78, s90, 0xff\n"
    "s_cmp_lg_u32 s78, 0\n"
    "s_cbranch_scc1 L_re3%=\n"
    "v_mov_b32 v126, 0\n"
    "global_load_dwordx2 v[126:127], v126, s[68:69] sc1\n"
    "s_waitcnt vmcnt(0)\n"
    "v_cmp_eq_u64 vcc, s[96:97], v[126:127]\n"
    "s_nop 1\n"
    "s_cbranch_vccnz L_bail_g3%=\n"
    "s_cmp_lt_u32 s90, 0x40000\n"
    "s_cbranch_scc1 L_re3%=\n"
    "v_mov_b32 v126, s96\n"
    "v_mov_b32 v127, s97\n"
    "s_mov_b64 exec, 1\n"
    "global_store_dwordx2 v71, v[126:127], s[68:69] sc1\n"
    "s_mov_b64 exec, -1\n"
    "L_bail_g3%=:\n"
    "s_branch L_go3%=\n"
    "L_re3%=:\n"
    "s_waitcnt vmcnt(0) lgkmcnt(0)\n"
    "s_branch L_top3%=\n"
    "L_last3%=:\n"
    "v_smfmac_f32_16x16x64_bf16 v[52:55], v[20:23], a[232:239], v67\n"
    "v_smfmac_f32_16x16x64_bf16 v[48:51], v[12:15], a[152:159], v67\n"
    "v_smfmac_f32_16x16x64_bf16 v[52:55], v[12:15], a[216:223], v67\n"
    "v_smfmac_f32_16x16x64_bf16 v[48:51], v[4:7], a[136:143], v67\n"
    "v_smfmac_f32_16x16x64_bf16 v[52:55], v[4:7], a[200:207], v67\n"
    "s_nop 3\n"
    "v_pk_add_f32 v[48:49], v[48:49], v[50:51]\n"
    "s_nop 2\n"
    "v_pk_add_f32 v[52:53], v[52:53], v[54:55]\n"
    "v_cvt_pk_bf16_f32 v66, v48, v49\n"
    "v_cvt_pk_bf16_f32 v74, v52, v53\n"
    "s_add_u32 s62, s62, 0x20000\n"
    "s_addc_u32 s63, s63, 0\n"
    "s_cmp_lg_u32 s42, 0\n"
    "s_cbranch_scc1 L_vmc3%=\n"
    "s_mov_b32 exec_lo, 0\n"
    "global_store_dwordx4 v71, v[44:47], s[62:63] offset:1024 sc1\n"
    "s_mov_b64 exec, -1\n"
    "s_branch L_kvc3%=\n"
    "L_vmc3%=:\n"
    "v_mov_b32 v126, v66\n"
    "v_mov_b32 v127, v74\n"
    "global_store_dwordx2 v79, v[126:127], s[62:63] offset:1536 sc1\n"
    "L_kvc3%=:\n"
    "s_waitcnt vmcnt(0) lgkmcnt(0)\n"
    "s_barrier\n"
    "s_endpgm\n"
    :: "s"(ws), "s"(__builtin_amdgcn_readfirstlane(by * 4 + bx)), "s"(wave), "s"(ldsb), "s"(ws + OFF_EP), "s"(Wq), "s"(Wk), "s"(Wv), "s"(Wo)
    : "memory", "vcc", "v255", "a255", "s36", "s37", "s38", "s39", "s40", "s96", "s97", "s99", "s22", "s23", "s24", "s25", "s26", "s27", "s28", "s29", "scc");
  __builtin_unreachable();
}

extern "C" void kernel_launch(void* const* d_in, const int* in_sizes, int n_in, void* d_out, int out_size, void* d_ws, size_t ws_size,
                              hipStream_t stream) {
  const u16* x = (const u16*)d_in[0];
  const u16* mem0 = (const u16*)d_in[1];
  const u16* Wq = (const u16*)d_in[2];
  const u16* Wk = (const u16*)d_in[3];
  const u16* Wv = (const u16*)d_in[4];
  const u16* Wo = (const u16*)d_in[5];
  const u16* w_r = (const u16*)d_in[8];
  const u16* w_gate = (const u16*)d_in[9];
  const u16* w_up = (const u16*)d_in[10];
  const u16* w_down = (const u16*)d_in[11];
  u16* out = (u16*)d_out;
  char* ws = (char*)(((size_t)d_ws + 2 * MBY - 1) & ~(size_t)(2 * MBY - 1));
  rec_kernel<<<256, 256, 0, stream>>>(ws, x, mem0, Wq, Wk, Wv, Wo, w_r, w_gate, w_up, w_down, out);
}
```

```cpp
#include <hip/hip_runtime.h>
#include <hip/hip_bf16.h>

typedef unsigned short u16;
typedef unsigned long long u64;
typedef __attribute__((ext_vector_type(8))) short bf16x8;
typedef __attribute__((ext_vector_type(4))) float f32x4;
typedef __attribute__((ext_vector_type(4))) unsigned u32x4;
#define GAS __attribute__((address_space(1)))
typedef GAS u64 gu64;
typedef GAS unsigned gu32;
#define RLX_AGENT __ATOMIC_RELAXED, __HIP_MEMORY_SCOPE_AGENT

constexpr int S_ = 2048, DM = 512, NH = 32, DH = 128, MM = 8, SEG = 32, NSEG = 64;
constexpr int HD = NH * DH;
constexpr int QKVN = 3 * HD;
constexpr int FF = 1792;

__device__ __forceinline__ float bf2f(u16 v) { return __builtin_bit_cast(float, (unsigned)v << 16); }
__device__ __forceinline__ float rbf(float f) { return (float)(__bf16)f; }

template <int CTRL> __device__ __forceinline__ float dpp_add(float v) {
  return v + __builtin_bit_cast(float, __builtin_amdgcn_update_dpp(0, __builtin_bit_cast(int, v), CTRL, 0xf, 0xf, false));
}
__device__ __forceinline__ float wave_sum(float v) {
  v = dpp_add<0x121>(v); v = dpp_add<0x122>(v); v = dpp_add<0x124>(v); v = dpp_add<0x128>(v);
  auto r = __builtin_amdgcn_permlane16_swap(__builtin_bit_cast(unsigned, v), __builtin_bit_cast(unsigned, v), false, false);
  const unsigned r0 = r[0], r1 = r[1];
  v = __builtin_bit_cast(float, r0) + __builtin_bit_cast(float, r1);
  auto q = __builtin_amdgcn_permlane32_swap(__builtin_bit_cast(unsigned, v), __builtin_bit_cast(unsigned, v), false, false);
  const unsigned q0 = q[0], q1 = q[1];
  return __builtin_bit_cast(float, q0) + __builtin_bit_cast(float, q1);
}
typedef __bf16 bf16x2_t __attribute__((ext_vector_type(2)));
typedef float f32x2_t __attribute__((ext_vector_type(2)));
__device__ __forceinline__ unsigned pk2(float a, float b) {
  f32x2_t v; v.x = a; v.y = b;
  return __builtin_bit_cast(unsigned, __builtin_convertvector(v, bf16x2_t));
}
__device__ __forceinline__ void rbf2(float& a, float& b) {
  const unsigned p = pk2(a, b);
  a = __builtin_bit_cast(float, p << 16); b = __builtin_bit_cast(float, p & 0xffff0000u);
}
__device__ __forceinline__ void ln_row(const float (&x)[8], float (&y)[8]) {
  float s = ((x[0] + x[1]) + (x[2] + x[3])) + ((x[4] + x[5]) + (x[6] + x[7]));
  s = wave_sum(s);
  const float mean = s * (1.0f / DM);
  float q = 0.f;
#pragma unroll
  for (int i = 0; i < 8; ++i) { float d = x[i] - mean; q += d * d; }
  q = wave_sum(q);
  float var = q * (1.0f / DM), mb = mean;
  rbf2(var, mb);
  const float sd = rbf(__builtin_amdgcn_sqrtf(rbf(var + 1e-5f)));
  const float inv = __builtin_amdgcn_rcpf(sd);
#pragma unroll
  for (int i = 0; i < 8; i += 2) {
    float a = x[i] - mb, b = x[i + 1] - mb;
    rbf2(a, b);
    a *= inv; b *= inv;
    rbf2(a, b);
    y[i] = a; y[i + 1] = b;
  }
}

constexpr size_t MBY = 1u << 20;
constexpr size_t OFF_G3 = 0, OFF_SYNC = 2 * MBY, OFF_G2 = 116 * MBY;
constexpr size_t OFF_EP = OFF_SYNC, OFF_ERR = OFF_SYNC + 512, OFF_RDY = OFF_SYNC + 0x1000, OFF_ACT = OFF_SYNC + 0x140000, OFF_RC2 = OFF_SYNC + 0x128000;
constexpr size_t OFF_CLM = OFF_SYNC + 0x10000, OFF_TAB = OFF_SYNC + 0xC000, OFF_XFL = OFF_SYNC + 0xE000;
constexpr size_t OFF_PIL = OFF_G3 + 0x80000, OFF_FLAGS = OFF_SYNC + 0x22000, OFF_OFL = OFF_SYNC + 0xA2000, OFF_HFL = OFF_SYNC + 0xE2000;
constexpr size_t OFF_RC = OFF_SYNC + 0x102000, OFF_PAR = OFF_SYNC + 0x112000;
constexpr unsigned SENT = 0xFFBFFFBFu;
constexpr size_t OFF_KF = 21 * MBY, OFF_VF = 37 * MBY, OFF_KM = 100 * MBY, OFF_VM = 108 * MBY;
constexpr size_t OFF_QF = 61 * MBY, OFF_XN = 77 * MBY, OFF_O = 79 * MBY, OFF_HP = 148 * MBY;

__device__ __forceinline__ unsigned tl_idx() { unsigned xcc; asm volatile("s_getreg_b32 %0, hwreg(HW_REG_XCC_ID)" : "=s"(xcc)); return (blockIdx.x & ~7u) | (xcc & 7u); }
#define GS(i) do {} while (0)
#define TS2(i) do {} while (0)
#define TSD() do {} while (0)
#define TL(i) do {} while (0)
#define TLP() do {} while (0)
#define TLX(i) do {} while (0)
__device__ __forceinline__ u64 now_clk() { u64 t; asm volatile("s_memtime %0\n\ts_waitcnt lgkmcnt(0)" : "=s"(t) :: "memory"); return t; }

constexpr int NREC = 64;

__device__ __forceinline__ bool spin_bail(unsigned& spins, char* ws, u64 T) {
  ++spins;
  if ((spins & 255u) == 0u) {
    gu64* err = (gu64*)(ws + OFF_ERR);
    if (__hip_atomic_load(err, RLX_AGENT) == T) return true;
    if (spins > (1u << 20)) { __hip_atomic_store(err, T, RLX_AGENT); return true; }
  }
  return false;
}
__device__ __forceinline__ void set_flag(char* ws, size_t off, u64 T, unsigned v) { __hip_atomic_store((gu64*)(ws + off), T + v, RLX_AGENT); }
__device__ __forceinline__ bf16x8 mk8(unsigned a, unsigned b, unsigned c, unsigned d) {
  u32x4 v; v.x = a; v.y = b; v.z = c; v.w = d; return __builtin_bit_cast(bf16x8, v);
}
__device__ __forceinline__ void store16_sc1(const void* base, unsigned off, u32x4 v) {
  asm volatile("global_store_dwordx4 %0, %1, %2 sc1\n\ts_nop 1" :: "v"(off), "v"(v), "s"(base) : "memory");
}

#define NSPEC (59 * 32)
#define ASL 24
#define QSL 1
#define ADL 0
#define ATN 0
#define QDL 90
#define NSIX 42
#define XROT 0
#define NPROBE 8
#define PSL 4
#define HSL 10
#define LAS __attribute__((address_space(3)))
__device__ __forceinline__ void unpack8(const u32x4 v, float (&f)[8]) {
  f[0] = bf2f(v.x & 0xffff); f[1] = bf2f(v.x >> 16); f[2] = bf2f(v.y & 0xffff); f[3] = bf2f(v.y >> 16);
  f[4] = bf2f(v.z & 0xffff); f[5] = bf2f(v.z >> 16); f[6] = bf2f(v.w & 0xffff); f[7] = bf2f(v.w >> 16);
}
template <int SL>
__device__ __forceinline__ void wait_flag(char* ws, unsigned off, u64 T, unsigned target) {
  gu64* p = (gu64*)(ws + off);
  unsigned spins = 0;
  while (!__all(__hip_atomic_load(p, RLX_AGENT) - T - target < 64u - target)) {
    __builtin_amdgcn_s_sleep(SL);
    if (spin_bail(spins, ws, T)) break;
  }
  __builtin_amdgcn_fence(__ATOMIC_ACQUIRE, "wavefront");
}
typedef __attribute__((ext_vector_type(4))) short s16x4;
__device__ __forceinline__ bf16x8 tr_frag(const char* p0, const char* p1) {
  const s16x4 a = __builtin_amdgcn_ds_read_tr16_b64_v4i16((LAS s16x4*)p0), b = __builtin_amdgcn_ds_read_tr16_b64_v4i16((LAS s16x4*)p1);
  return __builtin_shufflevector(a, b, 0, 1, 2, 3, 4, 5, 6, 7);
}

__device__ __forceinline__ float sum_g(float v) {
  auto r = __builtin_amdgcn_permlane16_swap(__builtin_bit_cast(unsigned, v), __builtin_bit_cast(unsigned, v), false, false);
  const unsigned r0 = r[0], r1 = r[1];
  v = __builtin_bit_cast(float, r0) + __builtin_bit_cast(float, r1);
  auto q = __builtin_amdgcn_permlane32_swap(__builtin_bit_cast(unsigned, v), __builtin_bit_cast(unsigned, v), false, false);
  const unsigned q0 = q[0], q1 = q[1];
  return __builtin_bit_cast(float, q0) + __builtin_bit_cast(float, q1);
}
__device__ __forceinline__ float max_g(float v) {
  auto r = __builtin_amdgcn_permlane16_swap(__builtin_bit_cast(unsigned, v), __builtin_bit_cast(unsigned, v), false, false);
  const unsigned r0 = r[0], r1 = r[1];
  v = fmaxf(__builtin_bit_cast(float, r0), __builtin_bit_cast(float, r1));
  auto q = __builtin_amdgcn_permlane32_swap(__builtin_bit_cast(unsigned, v), __builtin_bit_cast(unsigned, v), false, false);
  const unsigned q0 = q[0], q1 = q[1];
  return fmaxf(__builtin_bit_cast(float, q0), __builtin_bit_cast(float, q1));
}
__device__ __forceinline__ void ln_tile(u32x4 (&xr)[8]) {
  auto up = [](unsigned u) { f32x2_t v; v.x = __builtin_bit_cast(float, u << 16); v.y = __builtin_bit_cast(float, u & 0xffff0000u); return v; };
  f32x2_t f[8][4], sb[4] = {{0.f, 0.f}, {0.f, 0.f}, {0.f, 0.f}, {0.f, 0.f}};
#pragma unroll
  for (int k = 0; k < 8; ++k)
#pragma unroll
    for (int e = 0; e < 4; ++e) { const unsigned u = xr[k][e]; f[k][e] = up(u); sb[e] += f[k][e]; }
  const f32x2_t sa = (sb[0] + sb[1]) + (sb[2] + sb[3]);
  const float mean = sum_g(dpp_add<0x128>(sa.x + sa.y)) * (1.0f / DM);
  f32x2_t qb[4] = {{0.f, 0.f}, {0.f, 0.f}, {0.f, 0.f}, {0.f, 0.f}};
#pragma unroll
  for (int k = 0; k < 8; ++k)
#pragma unroll
    for (int e = 0; e < 4; ++e) { const f32x2_t d = f[k][e] - mean; qb[e] += d * d; }
  const f32x2_t qa = (qb[0] + qb[1]) + (qb[2] + qb[3]);
  float var = sum_g(dpp_add<0x128>(qa.x + qa.y)) * (1.0f / DM), mb = mean;
  rbf2(var, mb);
  const float inv = __builtin_amdgcn_rcpf(rbf(__builtin_amdgcn_sqrtf(rbf(var + 1e-5f))));
#pragma unroll
  for (int k = 0; k < 8; ++k)
#pragma unroll
    for (int e = 0; e < 4; ++e) {
      const f32x2_t d = f[k][e] - mb;
      const f32x2_t y = up(pk2(d.x, d.y)) * inv;
      xr[k][e] = pk2(y.x, y.y);
    }
}
__device__ __forceinline__ unsigned probe(char* ws, int q) {
      unsigned best[2] = {~0u, ~0u};
#pragma unroll 1
      for (int i = 0; i < NPROBE; ++i) {
        const char* p = ws + (q < 16 ? OFF_G2 + q * 2 * MBY + MBY : OFF_G3 + 0xC0000) + (i & 1) * 8192;
        unsigned z = 0;
        const u64 t0 = now_clk();
        asm volatile("global_atomic_or %0, %0, %0, %1 sc0 sc1\n\ts_waitcnt vmcnt(0)" : "+v"(z) : "s"(p) : "memory");
        const unsigned d = (unsigned)(now_clk() - t0);
        if (d < best[i & 1]) best[i & 1] = d;
      }
      return best[1] < best[0] ? 2u : 1u;
}
__device__ __forceinline__ u64 uni(u64 v) { return ((u64)(unsigned)__builtin_amdgcn_readfirstlane((unsigned)(v >> 32)) << 32) | (u64)(unsigned)__builtin_amdgcn_readfirstlane((unsigned)v); }
__device__ __forceinline__ u64 tag_of(u64 ep) { return (ep + 1) << 6; }
__device__ __forceinline__ void ln1_phase(char* ws, int q, u64 T, char* lds, int tid) {
  const int lane = tid & 63, wv = tid >> 6;
  u32x4 xr[8];
#pragma unroll
  for (int k = 0; k < 8; ++k) xr[k] = *(const u32x4*)(lds + (wv * 8 + (lane & 7)) * 1040 + (lane >> 3) * 128 + k * 16);
  ln_tile(xr);
#pragma unroll
  for (int k = 0; k < 8; ++k) store16_sc1(ws + OFF_XN, (unsigned)((q * 2 + (wv >> 1)) * 16384 + ((lane >> 3) * 8 + k) * 256 + ((wv & 1) * 8 + (lane & 7)) * 16), xr[k]);
  if (wv < 2) store16_sc1(ws, (unsigned)((wv == 0 ? OFF_RC : OFF_RC2) + q * 1024 + lane * 16), (u32x4){0u, 0u, 0u, 0u});
  store16_sc1(ws + OFF_ACT, (unsigned)(q * 4096 + tid * 16), (u32x4){SENT, SENT, SENT, SENT});
  asm volatile("s_waitcnt vmcnt(0)" ::: "memory");
  __syncthreads();
  if (tid == 0) set_flag(ws, OFF_XFL + q * 128, T, 1);
  TL(6);
}

__device__ __forceinline__ void load_h(const __amdgpu_buffer_rsrc_t rs, const u32x4 xv, int row, int lane, float (&f)[8]) {
  f32x4 p[4][2];
#pragma unroll
  for (int k = 0; k < 4; ++k)
#pragma unroll
    for (int u = 0; u < 2; ++u)
      p[k][u] = __builtin_bit_cast(f32x4, __builtin_amdgcn_raw_buffer_load_b128(rs, (int)OFF_HP + (((row >> 5) * 4 + k) * 4 + (lane >> 4)) * 16384 + (row & 31) * 512 + (lane & 15) * 32 + u * 16, 0, 16));
  float xf[8];
  unpack8(xv, xf);
#pragma unroll
  for (int i = 0; i < 8; ++i) f[i] = rbf(xf[i] + rbf((p[0][i >> 2][i & 3] + p[1][i >> 2][i & 3]) + (p[2][i >> 2][i & 3] + p[3][i >> 2][i & 3])));
}

__device__ __forceinline__ unsigned wait_par(char* ws, u64 T, int lane) {
  unsigned spins = 0; u64 p;
  for (;;) {
    p = __hip_atomic_load((gu64*)(ws + OFF_PAR + min(lane, 16) * 128), RLX_AGENT) - T;
    if (__all(p - 1 < 2)) break;
    __builtin_amdgcn_s_sleep(8);
    if (spin_bail(spins, ws, T)) break;
  }
  return (unsigned)__ballot(p == 2);
}
#define GCLOB "memory", "vcc", "scc", "s40", "s41", "s42", "s43", "s44", "s45", "s46", "s47", "s48", "s49", "s50", "s51", "s52", "s53", "s54", "s55", "s56", "s57", "s58", "s59", "s60", "s61", "s62", "s63", "s64", "s65", "s66", "s67", "s68", "s69", "s70", "s71", "v109", "v110", "v111", "v112", "v113", "v114", "v115", "v116", "v117", "v118", "v119", "v120", "v121", "v122", "v123", "v124", "v125", "v126", "v127", "v128", "v129", "v130", "v131", "v132", "v133", "v134", "v135", "v136", "v137", "v138", "v139", "v140", "v141", "v142", "v143", "v144", "v145", "v146", "v147", "v148", "v149", "v150", "v151", "v152", "v153", "v154", "v155", "v156", "v157", "v158", "v159", "v160", "v161", "v162", "v163", "v164", "v165", "v166", "v167", "v168", "v169", "v170", "v171", "v172", "v173", "v174", "v175", "v176", "v177", "v178", "v179", "v180", "v181", "v182", "v183", "v184", "v185", "v186", "v187", "v188", "v189", "v190", "v191", "v192", "v193", "v194", "v195", "v196", "v197", "v198", "v199", "v200", "v201", "v202", "v203", "v204", "v205", "v206", "v207", "v208", "v209", "v210", "v211", "v212", "v213", "v214", "v215", "v216", "v217", "v218", "v219", "v220", "v221", "v222", "v223", "v224", "v225", "v226", "v227", "v228", "v229", "v230", "v231", "v232", "v233", "v234", "v235", "v236", "v237", "v238", "v239", "v240", "v241", "v242", "v243", "v244", "v245", "v246", "v247", "v248", "v249", "v250", "v251", "v252", "v253", "v254", "v255", "a0", "a1", "a2", "a3", "a4", "a5", "a6", "a7", "a8", "a9", "a10", "a11", "a12", "a13", "a14", "a15", "a16", "a17", "a18", "a19", "a20", "a21", "a22", "a23", "a24", "a25", "a26", "a27", "a28", "a29", "a30", "a31", "a32", "a33", "a34", "a35", "a36", "a37", "a38", "a39", "a40", "a41", "a42", "a43", "a44", "a45", "a46", "a47", "a48", "a49", "a50", "a51", "a52", "a53", "a54", "a55", "a56", "a57", "a58", "a59", "a60", "a61", "a62", "a63", "a64", "a65", "a66", "a67", "a68", "a69", "a70", "a71", "a72", "a73", "a74", "a75", "a76", "a77", "a78", "a79", "a80", "a81", "a82", "a83", "a84", "a85", "a86", "a87", "a88", "a89", "a90", "a91", "a92", "a93", "a94", "a95", "a96", "a97", "a98", "a99", "a100", "a101", "a102", "a103", "a104", "a105", "a106", "a107", "a108", "a109", "a110", "a111", "a112", "a113", "a114", "a115", "a116", "a117", "a118", "a119", "a120", "a121", "a122", "a123", "a124", "a125", "a126", "a127", "a128", "a129", "a130", "a131", "a132", "a133", "a134", "a135", "a136", "a137", "a138", "a139", "a140", "a141", "a142", "a143", "a144", "a145", "a146", "a147", "a148", "a149", "a150", "a151", "a152", "a153", "a154", "a155", "a156", "a157", "a158", "a159", "a160", "a161", "a162", "a163", "a164", "a165", "a166", "a167", "a168", "a169", "a170", "a171", "a172", "a173", "a174", "a175", "a176", "a177", "a178", "a179", "a180", "a181", "a182", "a183", "a184", "a185", "a186", "a187", "a188", "a189", "a190", "a191", "a192", "a193", "a194", "a195", "a196", "a197", "a198", "a199", "a200", "a201", "a202", "a203", "a204", "a205", "a206", "a207", "a208", "a209", "a210", "a211", "a212", "a213", "a214", "a215", "a216", "a217", "a218", "a219", "a220", "a221", "a222", "a223", "a224", "a225", "a226", "a227", "a228", "a229", "a230", "a231", "a232", "a233", "a234", "a235", "a236", "a237", "a238", "a239", "a240", "a241", "a242", "a243", "a244", "a245", "a246", "a247", "a248", "a249", "a250", "a251", "a252", "a253", "a254", "a255"
template <int MAT>
__device__ __forceinline__ u64 qkv_phase(char* ws, int h, u64 ep0, int tid) {
  const int lane = tid & 63, wv = __builtin_amdgcn_readfirstlane(tid >> 6), l15 = lane & 15, g = lane >> 4;
  const int wr = wv >> 1, wc = wv & 1;
  TLX(512 + (MAT * 32 + h) * 16 + 1);
  const u64 T = tag_of(ep0);
  TLX(512 + (MAT * 32 + h) * 16 + 2);
  const int qq = l15 >> 2, f = qq | ((g & 1) << 2);
  const unsigned rp = (g * 8 + qq) * 256 + (l15 & 3) * 8 + ((wc * 4) ^ f) * 32;
  const unsigned pz = (tid >> 4) * 16384 + ((tid >> 3) & 1) * 4096 + (h ^ (tid >> 4)) * 128 + (tid & 7) * 16;
  char* op = ws + (MAT == 0 ? OFF_QF : (MAT == 1 ? OFF_KF : OFF_VF)) + (wr * 32 + h) * 8192 + wc * 4096;
  char* rdy = ws + OFF_RDY + (MAT * 32 + h) * 128;
  asm volatile("s_waitcnt vmcnt(0)\n\ts_barrier" ::: "memory");
#define GIN "s"(ws), "s"(uni(T)), "s"(op), "s"(rdy), "s"(wv), "v"(lane * 16), "v"(rp), "v"(pz), "v"((pz + 16 * 16384) ^ (16 * 128))
  if constexpr (MAT == 0) asm volatile(
    "s_mov_b64 s[40:41], %0\n"
    "s_mov_b64 s[42:43], %1\n"
    "s_mov_b64 s[44:45], %2\n"
    "s_mov_b64 s[54:55], %3\n"
    "s_mov_b32 s56, %4\n"
    "v_mov_b32 v121, %5\n"
    "v_mov_b32 v119, %7\n"
    "v_mov_b32 v120, %8\n"
    "s_add_u32 s58, s40, 0x20e000\n"
    "s_addc_u32 s59, s41, 0\n"
    "v_lshlrev_b32 v225, 3, v121\n"
    "v_and_b32 v224, 0x380, v225\n"
    "global_load_dwordx2 v[226:227], v224, s[58:59] sc1\n"
    "v_xor_b32 v112, 0, %6\n"
    "v_xor_b32 v113, 32, %6\n"
    "v_xor_b32 v114, 64, %6\n"
    "v_xor_b32 v115, 96, %6\n"
    "ds_read_b64_tr_b16 a[0:1], v112 offset:0\n"
    "ds_read_b64_tr_b16 a[2:3], v112 offset:1024\n"
    "ds_read_b64_tr_b16 a[4:5], v112 offset:8192\n"
    "ds_read_b64_tr_b16 a[6:7], v112 offset:9216\n"
    "ds_read_b64_tr_b16 a[8:9], v112 offset:16384\n"
    "ds_read_b64_tr_b16 a[10:11], v112 offset:17408\n"
    "ds_read_b64_tr_b16 a[12:13], v112 offset:24576\n"
    "ds_read_b64_tr_b16 a[14:15], v112 offset:25600\n"
    "s_waitcnt lgkmcnt(6)\n"
    "ds_read_b64_tr_b16 a[16:17], v112 offset:32768\n"
    "ds_read_b64_tr_b16 a[18:19], v112 offset:33792\n"
    "ds_read_b64_tr_b16 a[20:21], v112 offset:40960\n"
    "ds_read_b64_tr_b16 a[22:23], v112 offset:41984\n"
    "ds_read_b64_tr_b16 a[24:25], v112 offset:49152\n"
    "ds_read_b64_tr_b16 a[26:27], v112 offset:50176\n"
    "ds_read_b64_tr_b16 a[28:29], v112 offset:57344\n"
    "ds_read_b64_tr_b16 a[30:31], v112 offset:58368\n"
    "s_waitcnt lgkmcnt(6)\n"
    "ds_read_b64_tr_b16 a[64:65], v113 offset:0\n"
    "ds_read_b64_tr_b16 a[66:67], v113 offset:1024\n"
    "ds_read_b64_tr_b16 a[68:69], v113 offset:8192\n"
    "ds_read_b64_tr_b16 a[70:71], v113 offset:9216\n"
    "ds_read_b64_tr_b16 a[72:73], v113 offset:16384\n"
    "ds_read_b64_tr_b16 a[74:75], v113 offset:17408\n"
    "ds_read_b64_tr_b16 a[76:77], v113 offset:24576\n"
    "ds_read_b64_tr_b16 a[78:79], v113 offset:25600\n"
    "s_waitcnt lgkmcnt(6)\n"
    "ds_read_b64_tr_b16 a[80:81], v113 offset:32768\n"
    "ds_read_b64_tr_b16 a[82:83], v113 offset:33792\n"
    "ds_read_b64_tr_b16 a[84:85], v113 offset:40960\n"
    "ds_read_b64_tr_b16 a[86:87], v113 offset:41984\n"
    "ds_read_b64_tr_b16 a[88:89], v113 offset:49152\n"
    "ds_read_b64_tr_b16 a[90:91], v113 offset:50176\n"
    "ds_read_b64_tr_b16 a[92:93], v113 offset:57344\n"
    "ds_read_b64_tr_b16 a[94:95], v113 offset:58368\n"
    "s_waitcnt lgkmcnt(6)\n"
    "ds_read_b64_tr_b16 a[128:129], v114 offset:0\n"
    "ds_read_b64_tr_b16 a[130:131], v114 offset:1024\n"
    "ds_read_b64_tr_b16 a[132:133], v114 offset:8192\n"
    "ds_read_b64_tr_b16 a[134:135], v114 offset:9216\n"
    "ds_read_b64_tr_b16 a[136:137], v114 offset:16384\n"
    "ds_read_b64_tr_b16 a[138:139], v114 offset:17408\n"
    "ds_read_b64_tr_b16 a[140:141], v114 offset:24576\n"
    "ds_read_b64_tr_b16 a[142:143], v114 offset:25600\n"
    "s_waitcnt lgkmcnt(6)\n"
    "ds_read_b64_tr_b16 a[144:145], v114 offset:32768\n"
    "ds_read_b64_tr_b16 a[146:147], v114 offset:33792\n"
    "ds_read_b64_tr_b16 a[148:149], v114 offset:40960\n"
    "ds_read_b64_tr_b16 a[150:151], v114 offset:41984\n"
    "ds_read_b64_tr_b16 a[152:153], v114 offset:49152\n"
    "ds_read_b64_tr_b16 a[154:155], v114 offset:50176\n"
    "ds_read_b64_tr_b16 a[156:157], v114 offset:57344\n"
    "ds_read_b64_tr_b16 a[158:159], v114 offset:58368\n"
    "s_waitcnt lgkmcnt(6)\n"
    "ds_read_b64_tr_b16 a[192:193], v115 offset:0\n"
    "ds_read_b64_tr_b16 a[194:195], v115 offset:1024\n"
    "ds_read_b64_tr_b16 a[196:197], v115 offset:8192\n"
    "ds_read_b64_tr_b16 a[198:199], v115 offset:9216\n"
    "ds_read_b64_tr_b16 a[200:201], v115 offset:16384\n"
    "ds_read_b64_tr_b16 a[202:203], v115 offset:17408\n"
    "ds_read_b64_tr_b16 a[204:205], v115 offset:24576\n"
    "ds_read_b64_tr_b16 a[206:207], v115 offset:25600\n"
    "s_waitcnt lgkmcnt(6)\n"
    "ds_read_b64_tr_b16 a[208:209], v115 offset:32768\n"
    "ds_read_b64_tr_b16 a[210:211], v115 offset:33792\n"
    "ds_read_b64_tr_b16 a[212:213], v115 offset:40960\n"
    "ds_read_b64_tr_b16 a[214:215], v115 offset:41984\n"
    "ds_read_b64_tr_b16 a[216:217], v115 offset:49152\n"
    "ds_read_b64_tr_b16 a[218:219], v115 offset:50176\n"
    "ds_read_b64_tr_b16 a[220:221], v115 offset:57344\n"
    "ds_read_b64_tr_b16 a[222:223], v115 offset:58368\n"
    "s_waitcnt lgkmcnt(6)\n"
    "v_xor_b32 v112, 0, %6\n"
    "v_add_u32 v112, 0x10000, v112\n"
    "v_xor_b32 v113, 32, %6\n"
    "v_add_u32 v113, 0x10000, v113\n"
    "v_xor_b32 v114, 64, %6\n"
    "v_add_u32 v114, 0x10000, v114\n"
    "v_xor_b32 v115, 96, %6\n"
    "v_add_u32 v115, 0x10000, v115\n"
    "ds_read_b64_tr_b16 a[32:33], v112 offset:0\n"
    "ds_read_b64_tr_b16 a[34:35], v112 offset:1024\n"
    "ds_read_b64_tr_b16 a[36:37], v112 offset:8192\n"
    "ds_read_b64_tr_b16 a[38:39], v112 offset:9216\n"
    "ds_read_b64_tr_b16 a[40:41], v112 offset:16384\n"
    "ds_read_b64_tr_b16 a[42:43], v112 offset:17408\n"
    "ds_read_b64_tr_b16 a[44:45], v112 offset:24576\n"
    "ds_read_b64_tr_b16 a[46:47], v112 offset:25600\n"
    "s_waitcnt lgkmcnt(6)\n"
    "ds_read_b64_tr_b16 a[48:49], v112 offset:32768\n"
    "ds_read_b64_tr_b16 a[50:51], v112 offset:33792\n"
    "ds_read_b64_tr_b16 a[52:53], v112 offset:40960\n"
    "ds_read_b64_tr_b16 a[54:55], v112 offset:41984\n"
    "ds_read_b64_tr_b16 a[56:57], v112 offset:49152\n"
    "ds_read_b64_tr_b16 a[58:59], v112 offset:50176\n"
    "ds_read_b64_tr_b16 a[60:61], v112 offset:57344\n"
    "ds_read_b64_tr_b16 a[62:63], v112 offset:58368\n"
    "s_waitcnt lgkmcnt(6)\n"
    "ds_read_b64_tr_b16 a[96:97], v113 offset:0\n"
    "ds_read_b64_tr_b16 a[98:99], v113 offset:1024\n"
    "ds_read_b64_tr_b16 a[100:101], v113 offset:8192\n"
    "ds_read_b64_tr_b16 a[102:103], v113 offset:9216\n"
    "ds_read_b64_tr_b16 a[104:105], v113 offset:16384\n"
    "ds_read_b64_tr_b16 a[106:107], v113 offset:17408\n"
    "ds_read_b64_tr_b16 a[108:109], v113 offset:24576\n"
    "ds_read_b64_tr_b16 a[110:111], v113 offset:25600\n"
    "s_waitcnt lgkmcnt(6)\n"
    "ds_read_b64_tr_b16 a[112:113], v113 offset:32768\n"
    "ds_read_b64_tr_b16 a[114:115], v113 offset:33792\n"
    "ds_read_b64_tr_b16 a[116:117], v113 offset:40960\n"
    "ds_read_b64_tr_b16 a[118:119], v113 offset:41984\n"
    "ds_read_b64_tr_b16 a[120:121], v113 offset:49152\n"
    "ds_read_b64_tr_b16 a[122:123], v113 offset:50176\n"
    "ds_read_b64_tr_b16 a[124:125], v113 offset:57344\n"
    "ds_read_b64_tr_b16 a[126:127], v113 offset:58368\n"
    "s_waitcnt lgkmcnt(6)\n"
    "ds_read_b64_tr_b16 a[160:161], v114 offset:0\n"
    "ds_read_b64_tr_b16 a[162:163], v114 offset:1024\n"
    "ds_read_b64_tr_b16 a[164:165], v114 offset:8192\n"
    "ds_read_b64_tr_b16 a[166:167], v114 offset:9216\n"
    "ds_read_b64_tr_b16 a[168:169], v114 offset:16384\n"
    "ds_read_b64_tr_b16 a[170:171], v114 offset:17408\n"
    "ds_read_b64_tr_b16 a[172:173], v114 offset:24576\n"
    "ds_read_b64_tr_b16 a[174:175], v114 offset:25600\n"
    "s_waitcnt lgkmcnt(6)\n"
    "ds_read_b64_tr_b16 a[176:177], v114 offset:32768\n"
    "ds_read_b64_tr_b16 a[178:179], v114 offset:33792\n"
    "ds_read_b64_tr_b16 a[180:181], v114 offset:40960\n"
    "ds_read_b64_tr_b16 a[182:183], v114 offset:41984\n"
    "ds_read_b64_tr_b16 a[184:185], v114 offset:49152\n"
    "ds_read_b64_tr_b16 a[186:187], v114 offset:50176\n"
    "ds_read_b64_tr_b16 a[188:189], v114 offset:57344\n"
    "ds_read_b64_tr_b16 a[190:191], v114 offset:58368\n"
    "s_waitcnt lgkmcnt(6)\n"
    "ds_read_b64_tr_b16 a[224:225], v115 offset:0\n"
    "ds_read_b64_tr_b16 a[226:227], v115 offset:1024\n"
    "ds_read_b64_tr_b16 a[228:229], v115 offset:8192\n"
    "ds_read_b64_tr_b16 a[230:231], v115 offset:9216\n"
    "ds_read_b64_tr_b16 a[232:233], v115 offset:16384\n"
    "ds_read_b64_tr_b16 a[234:235], v115 offset:17408\n"
    "ds_read_b64_tr_b16 a[236:237], v115 offset:24576\n"
    "ds_read_b64_tr_b16 a[238:239], v115 offset:25600\n"
    "s_waitcnt lgkmcnt(6)\n"
    "ds_read_b64_tr_b16 a[240:241], v115 offset:32768\n"
    "ds_read_b64_tr_b16 a[242:243], v115 offset:33792\n"
    "ds_read_b64_tr_b16 a[244:245], v115 offset:40960\n"
    "ds_read_b64_tr_b16 a[246:247], v115 offset:41984\n"
    "ds_read_b64_tr_b16 a[248:249], v115 offset:49152\n"
    "ds_read_b64_tr_b16 a[250:251], v115 offset:50176\n"
    "ds_read_b64_tr_b16 a[252:253], v115 offset:57344\n"
    "ds_read_b64_tr_b16 a[254:255], v115 offset:58368\n"
    "s_waitcnt lgkmcnt(6)\n"
    "s_lshr_b32 s58, s56, 1\n"
    "s_lshl_b32 s58, s58, 15\n"
    "v_add_u32 v122, s58, v121\n"
    "v_add_u32 v123, 0x10000, v122\n"
    "s_lshl_b32 s50, s56, 14\n"
    "v_mov_b32 v124, 0xffbfffbf\n"
    "v_mov_b32 v125, 0xffbfffbf\n"
    "v_mov_b32 v126, 0xffbfffbf\n"
    "v_mov_b32 v127, 0xffbfffbf\n"
    "v_mov_b32 v118, 0\n"
    "v_mov_b32 v117, s43\n"
    "s_add_u32 s58, s50, 0x4d00000\n"
    "s_add_u32 s48, s40, s58\n"
    "s_addc_u32 s49, s41, 0\n"
    "s_mov_b32 s57, 0\n"
    "s_mov_b32 s51, 0\n"
    "s_waitcnt lgkmcnt(0)\n"
    "s_barrier\n"
    "s_add_u32 s58, s40, 0x20e000\n"
    "s_addc_u32 s59, s41, 0\n"
    "s_add_u32 s60, s40, 0x200200\n"
    "s_addc_u32 s61, s41, 0\n"
    "s_or_b32 s62, s42, 1\n"
    "s_mov_b32 s63, s43\n"
    "s_branch G_xc0%=\n"
    "G_xw0%=:\n"
    "global_load_dwordx2 v[226:227], v224, s[58:59] sc1\n"
    "G_xc0%=:\n"
    "s_waitcnt vmcnt(0)\n"
    "v_cmp_ne_u64 vcc, s[62:63], v[226:227]\n"
    "s_nop 0\n"
    "s_cbranch_vccz G_xok0%=\n"
    "s_sleep 4\n"
    "s_add_u32 s57, s57, 1\n"
    "s_and_b32 s68, s57, 0xff\n"
    "s_cbranch_scc1 G_xw0%=\n"
    "global_load_dwordx2 v[226:227], v118, s[60:61] sc1\n"
    "s_waitcnt vmcnt(0)\n"
    "v_cmp_eq_u64 vcc, s[42:43], v[226:227]\n"
    "s_nop 0\n"
    "s_cbranch_vccnz G_xok0%=\n"
    "s_cmp_lt_u32 s57, 0x40000\n"
    "s_cbranch_scc1 G_xw0%=\n"
    "v_mov_b32 v116, s42\n"
    "global_store_dwordx2 v118, v[116:117], s[60:61] sc1\n"
    "G_xok0%=:\n"
    "s_mov_b32 vcc_lo, 0xcccccccc\n"
    "s_mov_b32 vcc_hi, 0xcccccccc\n"
    "s_add_u32 m0, s50, 0\n"
    "s_add_u32 s64, s48, 0\n"
    "s_addc_u32 s65, s49, 0\n"
    "global_load_lds_dwordx4 v121, s[64:65] offset:0 sc1\n"
    "global_load_lds_dwordx4 v121, s[64:65] offset:1024 sc1\n"
    "global_load_lds_dwordx4 v121, s[64:65] offset:2048 sc1\n"
    "global_load_lds_dwordx4 v121, s[64:65] offset:3072 sc1\n"
    "s_add_u32 m0, s50, 4096\n"
    "s_add_u32 s64, s48, 4096\n"
    "s_addc_u32 s65, s49, 0\n"
    "global_load_lds_dwordx4 v121, s[64:65] offset:0 sc1\n"
    "global_load_lds_dwordx4 v121, s[64:65] offset:1024 sc1\n"
    "global_load_lds_dwordx4 v121, s[64:65] offset:2048 sc1\n"
    "global_load_lds_dwordx4 v121, s[64:65] offset:3072 sc1\n"
    "s_add_u32 m0, s50, 8192\n"
    "s_add_u32 s64, s48, 8192\n"
    "s_addc_u32 s65, s49, 0\n"
    "global_load_lds_dwordx4 v121, s[64:65] offset:0 sc1\n"
    "global_load_lds_dwordx4 v121, s[64:65] offset:1024 sc1\n"
    "global_load_lds_dwordx4 v121, s[64:65] offset:2048 sc1\n"
    "global_load_lds_dwordx4 v121, s[64:65] offset:3072 sc1\n"
    "s_add_u32 m0, s50, 12288\n"
    "s_add_u32 s64, s48, 12288\n"
    "s_addc_u32 s65, s49, 0\n"
    "s_add_u32 s48, s48, 0x10000\n"
    "s_addc_u32 s49, s49, 0\n"
    "global_load_lds_dwordx4 v121, s[64:65] offset:0 sc1\n"
    "global_load_lds_dwordx4 v121, s[64:65] offset:1024 sc1\n"
    "global_load_lds_dwordx4 v121, s[64:65] offset:2048 sc1\n"
    "global_load_lds_dwordx4 v121, s[64:65] offset:3072 sc1\n"
    "s_add_u32 m0, s50, 65536\n"
    "s_add_u32 s64, s48, 0\n"
    "s_addc_u32 s65, s49, 0\n"
    "global_load_lds_dwordx4 v121, s[64:65] offset:0 sc1\n"
    "global_load_lds_dwordx4 v121, s[64:65] offset:1024 sc1\n"
    "global_load_lds_dwordx4 v121, s[64:65] offset:2048 sc1\n"
    "global_load_lds_dwordx4 v121, s[64:65] offset:3072 sc1\n"
    "s_add_u32 m0, s50, 69632\n"
    "s_add_u32 s64, s48, 4096\n"
    "s_addc_u32 s65, s49, 0\n"
    "global_load_lds_dwordx4 v121, s[64:65] offset:0 sc1\n"
    "global_load_lds_dwordx4 v121, s[64:65] offset:1024 sc1\n"
    "global_load_lds_dwordx4 v121, s[64:65] offset:2048 sc1\n"
    "global_load_lds_dwordx4 v121, s[64:65] offset:3072 sc1\n"
    "s_add_u32 m0, s50, 73728\n"
    "s_add_u32 s64, s48, 8192\n"
    "s_addc_u32 s65, s49, 0\n"
    "global_load_lds_dwordx4 v121, s[64:65] offset:0 sc1\n"
    "global_load_lds_dwordx4 v121, s[64:65] offset:1024 sc1\n"
    "global_load_lds_dwordx4 v121, s[64:65] offset:2048 sc1\n"
    "s_mov_b64 s[66:67], s[44:45]\n"
    "s_add_u32 s46, s40, 0xaa00000\n"
    "s_addc_u32 s47, s41, 0\n"
    "s_add_u32 s44, s44, 0x6f00000\n"
    "s_addc_u32 s45, s45, 0\n"
    "s_waitcnt vmcnt(19) lgkmcnt(0)\n"
    "s_barrier\n"
    "ds_read_b128 v[192:195], v122 offset:0\n"
    "ds_read_b128 v[196:199], v122 offset:16384\n"
    "ds_read_b128 v[200:203], v122 offset:1024\n"
    "ds_read_b128 v[204:207], v122 offset:17408\n"
    "ds_read_b128 v[208:211], v122 offset:2048\n"
    "ds_read_b128 v[212:215], v122 offset:18432\n"
    "s_waitcnt lgkmcnt(4)\n"
    "s_branch G_even%=\n"
    ".p2align 6\n"
    "G_loop%=:\n"
    "v_mfma_f32_16x16x32_bf16 v[160:163], a[0:3], v[192:195], 0\n"
    "ds_read_b128 v[216:219], v123 offset:3072\n"
    "v_mfma_f32_16x16x32_bf16 v[164:167], a[64:67], v[192:195], 0\n"
    "ds_read_b128 v[220:223], v123 offset:19456\n"
    "v_mfma_f32_16x16x32_bf16 v[168:171], a[128:131], v[192:195], 0\n"
    "v_mfma_f32_16x16x32_bf16 v[172:175], a[192:195], v[192:195], 0\n"
    "v_cvt_pk_bf16_f32 v224, v128, v129\n"
    "v_cvt_pk_bf16_f32 v225, v130, v131\n"
    "v_mfma_f32_16x16x32_bf16 v[176:179], a[0:3], v[196:199], 0\n"
    "global_load_lds_dwordx4 v121, s[64:65] offset:3072 sc1\n"
    "v_mfma_f32_16x16x32_bf16 v[180:183], a[64:67], v[196:199], 0\n"
    "v_cvt_pk_bf16_f32 v226, v132, v133\n"
    "v_cvt_pk_bf16_f32 v227, v134, v135\n"
    "v_mfma_f32_16x16x32_bf16 v[184:187], a[128:131], v[196:199], 0\n"
    "v_cvt_pk_bf16_f32 v228, v144, v145\n"
    "v_cvt_pk_bf16_f32 v229, v146, v147\n"
    "v_mfma_f32_16x16x32_bf16 v[188:191], a[192:195], v[196:199], 0\n"
    "v_cvt_pk_bf16_f32 v230, v148, v149\n"
    "v_cvt_pk_bf16_f32 v231, v150, v151\n"
    "s_waitcnt lgkmcnt(4)\n"
    "v_mfma_f32_16x16x32_bf16 v[160:163], a[4:7], v[200:203], v[160:163]\n"
    "ds_read_b128 v[192:195], v123 offset:4096\n"
    "v_mfma_f32_16x16x32_bf16 v[164:167], a[68:71], v[200:203], v[164:167]\n"
    "ds_read_b128 v[196:199], v123 offset:20480\n"
    "v_mfma_f32_16x16x32_bf16 v[168:171], a[132:135], v[200:203], v[168:171]\n"
    "s_add_u32 m0, s50, 12288\n"
    "s_add_u32 s64, s48, 12288\n"
    "s_addc_u32 s65, s49, 0\n"
    "v_cvt_pk_bf16_f32 v232, v136, v137\n"
    "v_cvt_pk_bf16_f32 v233, v138, v139\n"
    "v_mfma_f32_16x16x32_bf16 v[172:175], a[196:199], v[200:203], v[172:175]\n"
    "s_add_u32 s48, s48, 0x10000\n"
    "s_addc_u32 s49, s49, 0\n"
    "v_cvt_pk_bf16_f32 v234, v140, v141\n"
    "v_cvt_pk_bf16_f32 v235, v142, v143\n"
    "v_mfma_f32_16x16x32_bf16 v[176:179], a[4:7], v[204:207], v[176:179]\n"
    "global_load_lds_dwordx4 v121, s[64:65] offset:0 sc1\n"
    "v_mfma_f32_16x16x32_bf16 v[180:183], a[68:71], v[204:207], v[180:183]\n"
    "v_cvt_pk_bf16_f32 v236, v152, v153\n"
    "v_cvt_pk_bf16_f32 v237, v154, v155\n"
    "v_mfma_f32_16x16x32_bf16 v[184:187], a[132:135], v[204:207], v[184:187]\n"
    "global_store_dwordx4 v121, v[224:227], s[44:45] offset:0 sc1\n"
    "v_mfma_f32_16x16x32_bf16 v[188:191], a[196:199], v[204:207], v[188:191]\n"
    "v_cvt_pk_bf16_f32 v238, v156, v157\n"
    "v_cvt_pk_bf16_f32 v239, v158, v159\n"
    "s_waitcnt lgkmcnt(4)\n"
    "v_mfma_f32_16x16x32_bf16 v[160:163], a[8:11], v[208:211], v[160:163]\n"
    "ds_read_b128 v[200:203], v123 offset:5120\n"
    "v_mfma_f32_16x16x32_bf16 v[164:167], a[72:75], v[208:211], v[164:167]\n"
    "ds_read_b128 v[204:207], v123 offset:21504\n"
    "v_mfma_f32_16x16x32_bf16 v[168:171], a[136:139], v[208:211], v[168:171]\n"
    "v_mfma_f32_16x16x32_bf16 v[172:175], a[200:203], v[208:211], v[172:175]\n"
    "v_mfma_f32_16x16x32_bf16 v[176:179], a[8:11], v[212:215], v[176:179]\n"
    "global_load_lds_dwordx4 v121, s[64:65] offset:1024 sc1\n"
    "v_mfma_f32_16x16x32_bf16 v[180:183], a[72:75], v[212:215], v[180:183]\n"
    "v_mfma_f32_16x16x32_bf16 v[184:187], a[136:139], v[212:215], v[184:187]\n"
    "global_store_dwordx4 v121, v[228:231], s[44:45] offset:1024 sc1\n"
    "v_mfma_f32_16x16x32_bf16 v[188:191], a[200:203], v[212:215], v[188:191]\n"
    "s_waitcnt lgkmcnt(4)\n"
    "v_mfma_f32_16x16x32_bf16 v[160:163], a[12:15], v[216:219], v[160:163]\n"
    "ds_read_b128 v[208:211], v123 offset:6144\n"
    "v_mfma_f32_16x16x32_bf16 v[164:167], a[76:79], v[216:219], v[164:167]\n"
    "ds_read_b128 v[212:215], v123 offset:22528\n"
    "v_mfma_f32_16x16x32_bf16 v[168:171], a[140:143], v[216:219], v[168:171]\n"
    "v_mfma_f32_16x16x32_bf16 v[172:175], a[204:207], v[216:219], v[172:175]\n"
    "v_mfma_f32_16x16x32_bf16 v[176:179], a[12:15], v[220:223], v[176:179]\n"
    "global_load_lds_dwordx4 v121, s[64:65] offset:2048 sc1\n"
    "v_mfma_f32_16x16x32_bf16 v[180:183], a[76:79], v[220:223], v[180:183]\n"
    "v_mfma_f32_16x16x32_bf16 v[184:187], a[140:143], v[220:223], v[184:187]\n"
    "global_store_dwordx4 v121, v[232:235], s[44:45] offset:2048 sc1\n"
    "v_mfma_f32_16x16x32_bf16 v[188:191], a[204:207], v[220:223], v[188:191]\n"
    "s_waitcnt lgkmcnt(4)\n"
    "v_mfma_f32_16x16x32_bf16 v[160:163], a[16:19], v[192:195], v[160:163]\n"
    "ds_read_b128 v[216:219], v123 offset:7168\n"
    "v_mfma_f32_16x16x32_bf16 v[164:167], a[80:83], v[192:195], v[164:167]\n"
    "ds_read_b128 v[220:223], v123 offset:23552\n"
    "v_mfma_f32_16x16x32_bf16 v[168:171], a[144:147], v[192:195], v[168:171]\n"
    "v_mfma_f32_16x16x32_bf16 v[172:175], a[208:211], v[192:195], v[172:175]\n"
    "v_mfma_f32_16x16x32_bf16 v[176:179], a[16:19], v[196:199], v[176:179]\n"
    "global_load_lds_dwordx4 v121, s[64:65] offset:3072 sc1\n"
    "v_mfma_f32_16x16x32_bf16 v[180:183], a[80:83], v[196:199], v[180:183]\n"
    "v_mfma_f32_16x16x32_bf16 v[184:187], a[144:147], v[196:199], v[184:187]\n"
    "global_store_dwordx4 v121, v[236:239], s[44:45] offset:3072 sc1\n"
    "v_mfma_f32_16x16x32_bf16 v[188:191], a[208:211], v[196:199], v[188:191]\n"
    "s_waitcnt lgkmcnt(4)\n"
    "v_mfma_f32_16x16x32_bf16 v[160:163], a[20:23], v[200:203], v[160:163]\n"
    "s_waitcnt vmcnt(12) lgkmcnt(0)\n"
    "s_barrier\n"
    "v_mfma_f32_16x16x32_bf16 v[164:167], a[84:87], v[200:203], v[164:167]\n"
    "ds_read_b128 v[192:195], v123 offset:8192\n"
    "v_mfma_f32_16x16x32_bf16 v[168:171], a[148:151], v[200:203], v[168:171]\n"
    "ds_read_b128 v[196:199], v123 offset:24576\n"
    "s_add_u32 m0, s50, 65536\n"
    "s_add_u32 s64, s48, 0\n"
    "s_addc_u32 s65, s49, 0\n"
    "v_mfma_f32_16x16x32_bf16 v[172:175], a[212:215], v[200:203], v[172:175]\n"
    "v_mfma_f32_16x16x32_bf16 v[176:179], a[20:23], v[204:207], v[176:179]\n"
    "global_load_lds_dwordx4 v121, s[64:65] offset:0 sc1\n"
    "v_mfma_f32_16x16x32_bf16 v[180:183], a[84:87], v[204:207], v[180:183]\n"
    "v_mfma_f32_16x16x32_bf16 v[184:187], a[148:151], v[204:207], v[184:187]\n"
    "v_mfma_f32_16x16x32_bf16 v[188:191], a[212:215], v[204:207], v[188:191]\n"
    "s_waitcnt lgkmcnt(4)\n"
    "v_mfma_f32_16x16x32_bf16 v[160:163], a[24:27], v[208:211], v[160:163]\n"
    "ds_read_b128 v[200:203], v123 offset:9216\n"
    "v_mfma_f32_16x16x32_bf16 v[164:167], a[88:91], v[208:211], v[164:167]\n"
    "ds_read_b128 v[204:207], v123 offset:25600\n"
    "v_mfma_f32_16x16x32_bf16 v[168:171], a[152:155], v[208:211], v[168:171]\n"
    "v_mfma_f32_16x16x32_bf16 v[172:175], a[216:219], v[208:211], v[172:175]\n"
    "v_mfma_f32_16x16x32_bf16 v[176:179], a[24:27], v[212:215], v[176:179]\n"
    "global_load_lds_dwordx4 v121, s[64:65] offset:1024 sc1\n"
    "v_mfma_f32_16x16x32_bf16 v[180:183], a[88:91], v[212:215], v[180:183]\n"
    "v_mfma_f32_16x16x32_bf16 v[184:187], a[152:155], v[212:215], v[184:187]\n"
    "v_mfma_f32_16x16x32_bf16 v[188:191], a[216:219], v[212:215], v[188:191]\n"
    "s_waitcnt lgkmcnt(4)\n"
    "v_mfma_f32_16x16x32_bf16 v[160:163], a[28:31], v[216:219], v[160:163]\n"
    "ds_read_b128 v[208:211], v123 offset:10240\n"
    "v_mfma_f32_16x16x32_bf16 v[164:167], a[92:95], v[216:219], v[164:167]\n"
    "ds_read_b128 v[212:215], v123 offset:26624\n"
    "v_mfma_f32_16x16x32_bf16 v[168:171], a[156:159], v[216:219], v[168:171]\n"
    "v_mfma_f32_16x16x32_bf16 v[172:175], a[220:223], v[216:219], v[172:175]\n"
    "v_mfma_f32_16x16x32_bf16 v[176:179], a[28:31], v[220:223], v[176:179]\n"
    "global_load_lds_dwordx4 v121, s[64:65] offset:2048 sc1\n"
    "v_mfma_f32_16x16x32_bf16 v[180:183], a[92:95], v[220:223], v[180:183]\n"
    "s_mov_b64 s[44:45], s[66:67]\n"
    "s_add_u32 s66, s66, 0x80000\n"
    "s_addc_u32 s67, s67, 0\n"
    "v_mfma_f32_16x16x32_bf16 v[184:187], a[156:159], v[220:223], v[184:187]\n"
    "v_mfma_f32_16x16x32_bf16 v[188:191], a[220:223], v[220:223], v[188:191]\n"
    "s_waitcnt lgkmcnt(4)\n"
    "v_mfma_f32_16x16x32_bf16 v[160:163], a[32:35], v[192:195], v[160:163]\n"
    "ds_read_b128 v[216:219], v123 offset:11264\n"
    "v_mfma_f32_16x16x32_bf16 v[164:167], a[96:99], v[192:195], v[164:167]\n"
    "ds_read_b128 v[220:223], v123 offset:27648\n"
    "v_mfma_f32_16x16x32_bf16 v[168:171], a[160:163], v[192:195], v[168:171]\n"
    "v_mfma_f32_16x16x32_bf16 v[172:175], a[224:227], v[192:195], v[172:175]\n"
    "v_mfma_f32_16x16x32_bf16 v[176:179], a[32:35], v[196:199], v[176:179]\n"
    "global_load_lds_dwordx4 v121, s[64:65] offset:3072 sc1\n"
    "v_mfma_f32_16x16x32_bf16 v[180:183], a[96:99], v[196:199], v[180:183]\n"
    "v_mfma_f32_16x16x32_bf16 v[184:187], a[160:163], v[196:199], v[184:187]\n"
    "v_mfma_f32_16x16x32_bf16 v[188:191], a[224:227], v[196:199], v[188:191]\n"
    "s_waitcnt lgkmcnt(4)\n"
    "v_mfma_f32_16x16x32_bf16 v[160:163], a[36:39], v[200:203], v[160:163]\n"
    "ds_read_b128 v[192:195], v123 offset:12288\n"
    "v_mfma_f32_16x16x32_bf16 v[164:167], a[100:103], v[200:203], v[164:167]\n"
    "ds_read_b128 v[196:199], v123 offset:28672\n"
    "v_mfma_f32_16x16x32_bf16 v[168:171], a[164:167], v[200:203], v[168:171]\n"
    "s_add_u32 m0, s50, 69632\n"
    "s_add_u32 s64, s48, 4096\n"
    "s_addc_u32 s65, s49, 0\n"
    "v_mfma_f32_16x16x32_bf16 v[172:175], a[228:231], v[200:203], v[172:175]\n"
    "v_mfma_f32_16x16x32_bf16 v[176:179], a[36:39], v[204:207], v[176:179]\n"
    "global_load_lds_dwordx4 v121, s[64:65] offset:0 sc1\n"
    "v_mfma_f32_16x16x32_bf16 v[180:183], a[100:103], v[204:207], v[180:183]\n"
    "v_mfma_f32_16x16x32_bf16 v[184:187], a[164:167], v[204:207], v[184:187]\n"
    "v_mfma_f32_16x16x32_bf16 v[188:191], a[228:231], v[204:207], v[188:191]\n"
    "s_waitcnt lgkmcnt(4)\n"
    "v_mfma_f32_16x16x32_bf16 v[160:163], a[40:43], v[208:211], v[160:163]\n"
    "ds_read_b128 v[200:203], v123 offset:13312\n"
    "v_mfma_f32_16x16x32_bf16 v[164:167], a[104:107], v[208:211], v[164:167]\n"
    "ds_read_b128 v[204:207], v123 offset:29696\n"
    "v_mfma_f32_16x16x32_bf16 v[168:171], a[168:171], v[208:211], v[168:171]\n"
    "v_mfma_f32_16x16x32_bf16 v[172:175], a[232:235], v[208:211], v[172:175]\n"
    "v_mfma_f32_16x16x32_bf16 v[176:179], a[40:43], v[212:215], v[176:179]\n"
    "global_load_lds_dwordx4 v121, s[64:65] offset:1024 sc1\n"
    "v_mfma_f32_16x16x32_bf16 v[180:183], a[104:107], v[212:215], v[180:183]\n"
    "v_mfma_f32_16x16x32_bf16 v[184:187], a[168:171], v[212:215], v[184:187]\n"
    "v_mfma_f32_16x16x32_bf16 v[188:191], a[232:235], v[212:215], v[188:191]\n"
    "s_waitcnt lgkmcnt(4)\n"
    "v_mfma_f32_16x16x32_bf16 v[160:163], a[44:47], v[216:219], v[160:163]\n"
    "ds_read_b128 v[208:211], v123 offset:14336\n"
    "v_mfma_f32_16x16x32_bf16 v[164:167], a[108:111], v[216:219], v[164:167]\n"
    "ds_read_b128 v[212:215], v123 offset:30720\n"
    "v_mfma_f32_16x16x32_bf16 v[168:171], a[172:175], v[216:219], v[168:171]\n"
    "v_mfma_f32_16x16x32_bf16 v[172:175], a[236:239], v[216:219], v[172:175]\n"
    "v_mfma_f32_16x16x32_bf16 v[176:179], a[44:47], v[220:223], v[176:179]\n"
    "global_load_lds_dwordx4 v121, s[64:65] offset:2048 sc1\n"
    "v_mfma_f32_16x16x32_bf16 v[180:183], a[108:111], v[220:223], v[180:183]\n"
    "v_mfma_f32_16x16x32_bf16 v[184:187], a[172:175], v[220:223], v[184:187]\n"
    "v_mfma_f32_16x16x32_bf16 v[188:191], a[236:239], v[220:223], v[188:191]\n"
    "s_waitcnt lgkmcnt(4)\n"
    "v_mfma_f32_16x16x32_bf16 v[160:163], a[48:51], v[192:195], v[160:163]\n"
    "ds_read_b128 v[216:219], v123 offset:15360\n"
    "v_mfma_f32_16x16x32_bf16 v[164:167], a[112:115], v[192:195], v[164:167]\n"
    "ds_read_b128 v[220:223], v123 offset:31744\n"
    "v_mfma_f32_16x16x32_bf16 v[168:171], a[176:179], v[192:195], v[168:171]\n"
    "v_mfma_f32_16x16x32_bf16 v[172:175], a[240:243], v[192:195], v[172:175]\n"
    "v_mfma_f32_16x16x32_bf16 v[176:179], a[48:51], v[196:199], v[176:179]\n"
    "global_load_lds_dwordx4 v121, s[64:65] offset:3072 sc1\n"
    "v_mfma_f32_16x16x32_bf16 v[180:183], a[112:115], v[196:199], v[180:183]\n"
    "v_mfma_f32_16x16x32_bf16 v[184:187], a[176:179], v[196:199], v[184:187]\n"
    "v_mfma_f32_16x16x32_bf16 v[188:191], a[240:243], v[196:199], v[188:191]\n"
    "s_waitcnt lgkmcnt(4)\n"
    "v_mfma_f32_16x16x32_bf16 v[160:163], a[52:55], v[200:203], v[160:163]\n"
    "s_waitcnt vmcnt(8) lgkmcnt(0)\n"
    "s_barrier\n"
    "v_mfma_f32_16x16x32_bf16 v[164:167], a[116:119], v[200:203], v[164:167]\n"
    "ds_read_b128 v[192:195], v122 offset:0\n"
    "v_mfma_f32_16x16x32_bf16 v[168:171], a[180:183], v[200:203], v[168:171]\n"
    "ds_read_b128 v[196:199], v122 offset:16384\n"
    "s_add_u32 m0, s50, 73728\n"
    "s_add_u32 s64, s48, 8192\n"
    "s_addc_u32 s65, s49, 0\n"
    "v_mfma_f32_16x16x32_bf16 v[172:175], a[244:247], v[200:203], v[172:175]\n"
    "v_mfma_f32_16x16x32_bf16 v[176:179], a[52:55], v[204:207], v[176:179]\n"
    "global_load_lds_dwordx4 v121, s[64:65] offset:0 sc1\n"
    "v_mfma_f32_16x16x32_bf16 v[180:183], a[116:119], v[204:207], v[180:183]\n"
    "s_cmp_lg_u32 s56, 0\n"
    "s_cbranch_scc1 G_f1%=\n"
    "s_or_b32 s58, s42, s51\n"
    "s_mov_b64 exec, 1\n"
    "v_mov_b32 v116, s58\n"
    "global_store_dwordx2 v118, v[116:117], s[54:55] sc1\n"
    "s_mov_b64 exec, -1\n"
    "G_f1%=:\n"
    "v_mfma_f32_16x16x32_bf16 v[184:187], a[180:183], v[204:207], v[184:187]\n"
    "v_mfma_f32_16x16x32_bf16 v[188:191], a[244:247], v[204:207], v[188:191]\n"
    "s_waitcnt lgkmcnt(4)\n"
    "v_mfma_f32_16x16x32_bf16 v[160:163], a[56:59], v[208:211], v[160:163]\n"
    "ds_read_b128 v[200:203], v122 offset:1024\n"
    "v_mfma_f32_16x16x32_bf16 v[164:167], a[120:123], v[208:211], v[164:167]\n"
    "ds_read_b128 v[204:207], v122 offset:17408\n"
    "v_mfma_f32_16x16x32_bf16 v[168:171], a[184:187], v[208:211], v[168:171]\n"
    "v_mfma_f32_16x16x32_bf16 v[172:175], a[248:251], v[208:211], v[172:175]\n"
    "v_mfma_f32_16x16x32_bf16 v[176:179], a[56:59], v[212:215], v[176:179]\n"
    "global_load_lds_dwordx4 v121, s[64:65] offset:1024 sc1\n"
    "v_mfma_f32_16x16x32_bf16 v[180:183], a[120:123], v[212:215], v[180:183]\n"
    "v_mfma_f32_16x16x32_bf16 v[184:187], a[184:187], v[212:215], v[184:187]\n"
    "v_mfma_f32_16x16x32_bf16 v[188:191], a[248:251], v[212:215], v[188:191]\n"
    "s_add_u32 s51, s51, 1\n"
    "s_waitcnt lgkmcnt(4)\n"
    "v_mfma_f32_16x16x32_bf16 v[160:163], a[60:63], v[216:219], v[160:163]\n"
    "ds_read_b128 v[208:211], v122 offset:2048\n"
    "v_mfma_f32_16x16x32_bf16 v[164:167], a[124:127], v[216:219], v[164:167]\n"
    "ds_read_b128 v[212:215], v122 offset:18432\n"
    "v_mfma_f32_16x16x32_bf16 v[168:171], a[188:191], v[216:219], v[168:171]\n"
    "v_mfma_f32_16x16x32_bf16 v[172:175], a[252:255], v[216:219], v[172:175]\n"
    "v_mfma_f32_16x16x32_bf16 v[176:179], a[60:63], v[220:223], v[176:179]\n"
    "global_load_lds_dwordx4 v121, s[64:65] offset:2048 sc1\n"
    "v_mfma_f32_16x16x32_bf16 v[180:183], a[124:127], v[220:223], v[180:183]\n"
    "v_mfma_f32_16x16x32_bf16 v[184:187], a[188:191], v[220:223], v[184:187]\n"
    "v_mfma_f32_16x16x32_bf16 v[188:191], a[252:255], v[220:223], v[188:191]\n"
    "s_add_u32 s58, s51, -3\n"
    "s_cmp_gt_u32 s58, 28\n"
    "s_waitcnt lgkmcnt(4)\n"
    "s_cbranch_scc1 G_t1%=\n"
    "G_even%=:\n"
    "v_mfma_f32_16x16x32_bf16 v[128:131], a[0:3], v[192:195], 0\n"
    "ds_read_b128 v[216:219], v122 offset:3072\n"
    "v_mfma_f32_16x16x32_bf16 v[132:135], a[64:67], v[192:195], 0\n"
    "ds_read_b128 v[220:223], v122 offset:19456\n"
    "v_mfma_f32_16x16x32_bf16 v[136:139], a[128:131], v[192:195], 0\n"
    "v_mfma_f32_16x16x32_bf16 v[140:143], a[192:195], v[192:195], 0\n"
    "v_cvt_pk_bf16_f32 v224, v160, v161\n"
    "v_cvt_pk_bf16_f32 v225, v162, v163\n"
    "v_mfma_f32_16x16x32_bf16 v[144:147], a[0:3], v[196:199], 0\n"
    "global_load_lds_dwordx4 v121, s[64:65] offset:3072 sc1\n"
    "v_mfma_f32_16x16x32_bf16 v[148:151], a[64:67], v[196:199], 0\n"
    "v_cvt_pk_bf16_f32 v226, v164, v165\n"
    "v_cvt_pk_bf16_f32 v227, v166, v167\n"
    "v_mfma_f32_16x16x32_bf16 v[152:155], a[128:131], v[196:199], 0\n"
    "v_cvt_pk_bf16_f32 v228, v176, v177\n"
    "v_cvt_pk_bf16_f32 v229, v178, v179\n"
    "v_mfma_f32_16x16x32_bf16 v[156:159], a[192:195], v[196:199], 0\n"
    "v_cvt_pk_bf16_f32 v230, v180, v181\n"
    "v_cvt_pk_bf16_f32 v231, v182, v183\n"
    "s_waitcnt lgkmcnt(4)\n"
    "v_mfma_f32_16x16x32_bf16 v[128:131], a[4:7], v[200:203], v[128:131]\n"
    "ds_read_b128 v[192:195], v122 offset:4096\n"
    "v_mfma_f32_16x16x32_bf16 v[132:135], a[68:71], v[200:203], v[132:135]\n"
    "ds_read_b128 v[196:199], v122 offset:20480\n"
    "v_mfma_f32_16x16x32_bf16 v[136:139], a[132:135], v[200:203], v[136:139]\n"
    "s_add_u32 m0, s50, 77824\n"
    "s_add_u32 s64, s48, 12288\n"
    "s_addc_u32 s65, s49, 0\n"
    "v_cvt_pk_bf16_f32 v232, v168, v169\n"
    "v_cvt_pk_bf16_f32 v233, v170, v171\n"
    "v_mfma_f32_16x16x32_bf16 v[140:143], a[196:199], v[200:203], v[140:143]\n"
    "s_add_u32 s48, s48, 0x10000\n"
    "s_addc_u32 s49, s49, 0\n"
    "v_cvt_pk_bf16_f32 v234, v172, v173\n"
    "v_cvt_pk_bf16_f32 v235, v174, v175\n"
    "v_mfma_f32_16x16x32_bf16 v[144:147], a[4:7], v[204:207], v[144:147]\n"
    "global_load_lds_dwordx4 v121, s[64:65] offset:0 sc1\n"
    "v_mfma_f32_16x16x32_bf16 v[148:151], a[68:71], v[204:207], v[148:151]\n"
    "v_cvt_pk_bf16_f32 v236, v184, v185\n"
    "v_cvt_pk_bf16_f32 v237, v186, v187\n"
    "v_mfma_f32_16x16x32_bf16 v[152:155], a[132:135], v[204:207], v[152:155]\n"
    "global_store_dwordx4 v121, v[224:227], s[44:45] offset:0 sc1\n"
    "v_mfma_f32_16x16x32_bf16 v[156:159], a[196:199], v[204:207], v[156:159]\n"
    "v_cvt_pk_bf16_f32 v238, v188, v189\n"
    "v_cvt_pk_bf16_f32 v239, v190, v191\n"
    "s_waitcnt lgkmcnt(4)\n"
    "v_mfma_f32_16x16x32_bf16 v[128:131], a[8:11], v[208:211], v[128:131]\n"
    "ds_read_b128 v[200:203], v122 offset:5120\n"
    "v_mfma_f32_16x16x32_bf16 v[132:135], a[72:75], v[208:211], v[132:135]\n"
    "ds_read_b128 v[204:207], v122 offset:21504\n"
    "v_mfma_f32_16x16x32_bf16 v[136:139], a[136:139], v[208:211], v[136:139]\n"
    "v_mfma_f32_16x16x32_bf16 v[140:143], a[200:203], v[208:211], v[140:143]\n"
    "v_mfma_f32_16x16x32_bf16 v[144:147], a[8:11], v[212:215], v[144:147]\n"
    "global_load_lds_dwordx4 v121, s[64:65] offset:1024 sc1\n"
    "v_mfma_f32_16x16x32_bf16 v[148:151], a[72:75], v[212:215], v[148:151]\n"
    "v_mfma_f32_16x16x32_bf16 v[152:155], a[136:139], v[212:215], v[152:155]\n"
    "global_store_dwordx4 v121, v[228:231], s[44:45] offset:1024 sc1\n"
    "v_mfma_f32_16x16x32_bf16 v[156:159], a[200:203], v[212:215], v[156:159]\n"
    "s_waitcnt lgkmcnt(4)\n"
    "v_mfma_f32_16x16x32_bf16 v[128:131], a[12:15], v[216:219], v[128:131]\n"
    "ds_read_b128 v[208:211], v122 offset:6144\n"
    "v_mfma_f32_16x16x32_bf16 v[132:135], a[76:79], v[216:219], v[132:135]\n"
    "ds_read_b128 v[212:215], v122 offset:22528\n"
    "v_mfma_f32_16x16x32_bf16 v[136:139], a[140:143], v[216:219], v[136:139]\n"
    "v_mfma_f32_16x16x32_bf16 v[140:143], a[204:207], v[216:219], v[140:143]\n"
    "v_mfma_f32_16x16x32_bf16 v[144:147], a[12:15], v[220:223], v[144:147]\n"
    "global_load_lds_dwordx4 v121, s[64:65] offset:2048 sc1\n"
    "v_mfma_f32_16x16x32_bf16 v[148:151], a[76:79], v[220:223], v[148:151]\n"
    "v_mfma_f32_16x16x32_bf16 v[152:155], a[140:143], v[220:223], v[152:155]\n"
    "global_store_dwordx4 v121, v[232:235], s[44:45] offset:2048 sc1\n"
    "v_mfma_f32_16x16x32_bf16 v[156:159], a[204:207], v[220:223], v[156:159]\n"
    "s_waitcnt lgkmcnt(4)\n"
    "v_mfma_f32_16x16x32_bf16 v[128:131], a[16:19], v[192:195], v[128:131]\n"
    "ds_read_b128 v[216:219], v122 offset:7168\n"
    "v_mfma_f32_16x16x32_bf16 v[132:135], a[80:83], v[192:195], v[132:135]\n"
    "ds_read_b128 v[220:223], v122 offset:23552\n"
    "v_mfma_f32_16x16x32_bf16 v[136:139], a[144:147], v[192:195], v[136:139]\n"
    "v_mfma_f32_16x16x32_bf16 v[140:143], a[208:211], v[192:195], v[140:143]\n"
    "v_mfma_f32_16x16x32_bf16 v[144:147], a[16:19], v[196:199], v[144:147]\n"
    "global_load_lds_dwordx4 v121, s[64:65] offset:3072 sc1\n"
    "v_mfma_f32_16x16x32_bf16 v[148:151], a[80:83], v[196:199], v[148:151]\n"
    "v_mfma_f32_16x16x32_bf16 v[152:155], a[144:147], v[196:199], v[152:155]\n"
    "global_store_dwordx4 v121, v[236:239], s[44:45] offset:3072 sc1\n"
    "v_mfma_f32_16x16x32_bf16 v[156:159], a[208:211], v[196:199], v[156:159]\n"
    "s_waitcnt lgkmcnt(4)\n"
    "v_mfma_f32_16x16x32_bf16 v[128:131], a[20:23], v[200:203], v[128:131]\n"
    "s_waitcnt vmcnt(12) lgkmcnt(0)\n"
    "s_barrier\n"
    "v_mfma_f32_16x16x32_bf16 v[132:135], a[84:87], v[200:203], v[132:135]\n"
    "ds_read_b128 v[192:195], v122 offset:8192\n"
    "v_mfma_f32_16x16x32_bf16 v[136:139], a[148:151], v[200:203], v[136:139]\n"
    "ds_read_b128 v[196:199], v122 offset:24576\n"
    "s_add_u32 m0, s50, 0\n"
    "s_add_u32 s64, s48, 0\n"
    "s_addc_u32 s65, s49, 0\n"
    "v_mfma_f32_16x16x32_bf16 v[140:143], a[212:215], v[200:203], v[140:143]\n"
    "v_mfma_f32_16x16x32_bf16 v[144:147], a[20:23], v[204:207], v[144:147]\n"
    "global_load_lds_dwordx4 v121, s[64:65] offset:0 sc1\n"
    "v_mfma_f32_16x16x32_bf16 v[148:151], a[84:87], v[204:207], v[148:151]\n"
    "v_mfma_f32_16x16x32_bf16 v[152:155], a[148:151], v[204:207], v[152:155]\n"
    "v_mfma_f32_16x16x32_bf16 v[156:159], a[212:215], v[204:207], v[156:159]\n"
    "s_waitcnt lgkmcnt(4)\n"
    "v_mfma_f32_16x16x32_bf16 v[128:131], a[24:27], v[208:211], v[128:131]\n"
    "ds_read_b128 v[200:203], v122 offset:9216\n"
    "v_mfma_f32_16x16x32_bf16 v[132:135], a[88:91], v[208:211], v[132:135]\n"
    "ds_read_b128 v[204:207], v122 offset:25600\n"
    "v_mfma_f32_16x16x32_bf16 v[136:139], a[152:155], v[208:211], v[136:139]\n"
    "v_mfma_f32_16x16x32_bf16 v[140:143], a[216:219], v[208:211], v[140:143]\n"
    "v_mfma_f32_16x16x32_bf16 v[144:147], a[24:27], v[212:215], v[144:147]\n"
    "global_load_lds_dwordx4 v121, s[64:65] offset:1024 sc1\n"
    "v_mfma_f32_16x16x32_bf16 v[148:151], a[88:91], v[212:215], v[148:151]\n"
    "v_mfma_f32_16x16x32_bf16 v[152:155], a[152:155], v[212:215], v[152:155]\n"
    "v_mfma_f32_16x16x32_bf16 v[156:159], a[216:219], v[212:215], v[156:159]\n"
    "s_waitcnt lgkmcnt(4)\n"
    "v_mfma_f32_16x16x32_bf16 v[128:131], a[28:31], v[216:219], v[128:131]\n"
    "ds_read_b128 v[208:211], v122 offset:10240\n"
    "v_mfma_f32_16x16x32_bf16 v[132:135], a[92:95], v[216:219], v[132:135]\n"
    "ds_read_b128 v[212:215], v122 offset:26624\n"
    "v_mfma_f32_16x16x32_bf16 v[136:139], a[156:159], v[216:219], v[136:139]\n"
    "v_mfma_f32_16x16x32_bf16 v[140:143], a[220:223], v[216:219], v[140:143]\n"
    "v_mfma_f32_16x16x32_bf16 v[144:147], a[28:31], v[220:223], v[144:147]\n"
    "global_load_lds_dwordx4 v121, s[64:65] offset:2048 sc1\n"
    "v_mfma_f32_16x16x32_bf16 v[148:151], a[92:95], v[220:223], v[148:151]\n"
    "s_mov_b64 s[44:45], s[66:67]\n"
    "s_add_u32 s66, s66, 0x80000\n"
    "s_addc_u32 s67, s67, 0\n"
    "v_mfma_f32_16x16x32_bf16 v[152:155], a[156:159], v[220:223], v[152:155]\n"
    "v_mfma_f32_16x16x32_bf16 v[156:159], a[220:223], v[220:223], v[156:159]\n"
    "s_waitcnt lgkmcnt(4)\n"
    "v_mfma_f32_16x16x32_bf16 v[128:131], a[32:35], v[192:195], v[128:131]\n"
    "ds_read_b128 v[216:219], v122 offset:11264\n"
    "v_mfma_f32_16x16x32_bf16 v[132:135], a[96:99], v[192:195], v[132:135]\n"
    "ds_read_b128 v[220:223], v122 offset:27648\n"
    "v_mfma_f32_16x16x32_bf16 v[136:139], a[160:163], v[192:195], v[136:139]\n"
    "v_mfma_f32_16x16x32_bf16 v[140:143], a[224:227], v[192:195], v[140:143]\n"
    "v_mfma_f32_16x16x32_bf16 v[144:147], a[32:35], v[196:199], v[144:147]\n"
    "global_load_lds_dwordx4 v121, s[64:65] offset:3072 sc1\n"
    "v_mfma_f32_16x16x32_bf16 v[148:151], a[96:99], v[196:199], v[148:151]\n"
    "v_mfma_f32_16x16x32_bf16 v[152:155], a[160:163], v[196:199], v[152:155]\n"
    "v_mfma_f32_16x16x32_bf16 v[156:159], a[224:227], v[196:199], v[156:159]\n"
    "s_waitcnt lgkmcnt(4)\n"
    "v_mfma_f32_16x16x32_bf16 v[128:131], a[36:39], v[200:203], v[128:131]\n"
    "ds_read_b128 v[192:195], v122 offset:12288\n"
    "v_mfma_f32_16x16x32_bf16 v[132:135], a[100:103], v[200:203], v[132:135]\n"
    "ds_read_b128 v[196:199], v122 offset:28672\n"
    "v_mfma_f32_16x16x32_bf16 v[136:139], a[164:167], v[200:203], v[136:139]\n"
    "s_add_u32 m0, s50, 4096\n"
    "s_add_u32 s64, s48, 4096\n"
    "s_addc_u32 s65, s49, 0\n"
    "v_mfma_f32_16x16x32_bf16 v[140:143], a[228:231], v[200:203], v[140:143]\n"
    "v_mfma_f32_16x16x32_bf16 v[144:147], a[36:39], v[204:207], v[144:147]\n"
    "global_load_lds_dwordx4 v121, s[64:65] offset:0 sc1\n"
    "v_mfma_f32_16x16x32_bf16 v[148:151], a[100:103], v[204:207], v[148:151]\n"
    "v_mfma_f32_16x16x32_bf16 v[152:155], a[164:167], v[204:207], v[152:155]\n"
    "v_mfma_f32_16x16x32_bf16 v[156:159], a[228:231], v[204:207], v[156:159]\n"
    "s_waitcnt lgkmcnt(4)\n"
    "v_mfma_f32_16x16x32_bf16 v[128:131], a[40:43], v[208:211], v[128:131]\n"
    "ds_read_b128 v[200:203], v122 offset:13312\n"
    "v_mfma_f32_16x16x32_bf16 v[132:135], a[104:107], v[208:211], v[132:135]\n"
    "ds_read_b128 v[204:207], v122 offset:29696\n"
    "v_mfma_f32_16x16x32_bf16 v[136:139], a[168:171], v[208:211], v[136:139]\n"
    "v_mfma_f32_16x16x32_bf16 v[140:143], a[232:235], v[208:211], v[140:143]\n"
    "v_mfma_f32_16x16x32_bf16 v[144:147], a[40:43], v[212:215], v[144:147]\n"
    "global_load_lds_dwordx4 v121, s[64:65] offset:1024 sc1\n"
    "v_mfma_f32_16x16x32_bf16 v[148:151], a[104:107], v[212:215], v[148:151]\n"
    "v_mfma_f32_16x16x32_bf16 v[152:155], a[168:171], v[212:215], v[152:155]\n"
    "v_mfma_f32_16x16x32_bf16 v[156:159], a[232:235], v[212:215], v[156:159]\n"
    "s_waitcnt lgkmcnt(4)\n"
    "v_mfma_f32_16x16x32_bf16 v[128:131], a[44:47], v[216:219], v[128:131]\n"
    "ds_read_b128 v[208:211], v122 offset:14336\n"
    "v_mfma_f32_16x16x32_bf16 v[132:135], a[108:111], v[216:219], v[132:135]\n"
    "ds_read_b128 v[212:215], v122 offset:30720\n"
    "v_mfma_f32_16x16x32_bf16 v[136:139], a[172:175], v[216:219], v[136:139]\n"
    "v_mfma_f32_16x16x32_bf16 v[140:143], a[236:239], v[216:219], v[140:143]\n"
    "v_mfma_f32_16x16x32_bf16 v[144:147], a[44:47], v[220:223], v[144:147]\n"
    "global_load_lds_dwordx4 v121, s[64:65] offset:2048 sc1\n"
    "v_mfma_f32_16x16x32_bf16 v[148:151], a[108:111], v[220:223], v[148:151]\n"
    "v_mfma_f32_16x16x32_bf16 v[152:155], a[172:175], v[220:223], v[152:155]\n"
    "v_mfma_f32_16x16x32_bf16 v[156:159], a[236:239], v[220:223], v[156:159]\n"
    "s_waitcnt lgkmcnt(4)\n"
    "v_mfma_f32_16x16x32_bf16 v[128:131], a[48:51], v[192:195], v[128:131]\n"
    "ds_read_b128 v[216:219], v122 offset:15360\n"
    "v_mfma_f32_16x16x32_bf16 v[132:135], a[112:115], v[192:195], v[132:135]\n"
    "ds_read_b128 v[220:223], v122 offset:31744\n"
    "v_mfma_f32_16x16x32_bf16 v[136:139], a[176:179], v[192:195], v[136:139]\n"
    "v_mfma_f32_16x16x32_bf16 v[140:143], a[240:243], v[192:195], v[140:143]\n"
    "v_mfma_f32_16x16x32_bf16 v[144:147], a[48:51], v[196:199], v[144:147]\n"
    "global_load_lds_dwordx4 v121, s[64:65] offset:3072 sc1\n"
    "v_mfma_f32_16x16x32_bf16 v[148:151], a[112:115], v[196:199], v[148:151]\n"
    "v_mfma_f32_16x16x32_bf16 v[152:155], a[176:179], v[196:199], v[152:155]\n"
    "v_mfma_f32_16x16x32_bf16 v[156:159], a[240:243], v[196:199], v[156:159]\n"
    "s_waitcnt lgkmcnt(4)\n"
    "v_mfma_f32_16x16x32_bf16 v[128:131], a[52:55], v[200:203], v[128:131]\n"
    "s_waitcnt vmcnt(8) lgkmcnt(0)\n"
    "s_barrier\n"
    "v_mfma_f32_16x16x32_bf16 v[132:135], a[116:119], v[200:203], v[132:135]\n"
    "ds_read_b128 v[192:195], v123 offset:0\n"
    "v_mfma_f32_16x16x32_bf16 v[136:139], a[180:183], v[200:203], v[136:139]\n"
    "ds_read_b128 v[196:199], v123 offset:16384\n"
    "s_add_u32 m0, s50, 8192\n"
    "s_add_u32 s64, s48, 8192\n"
    "s_addc_u32 s65, s49, 0\n"
    "v_mfma_f32_16x16x32_bf16 v[140:143], a[244:247], v[200:203], v[140:143]\n"
    "v_mfma_f32_16x16x32_bf16 v[144:147], a[52:55], v[204:207], v[144:147]\n"
    "global_load_lds_dwordx4 v121, s[64:65] offset:0 sc1\n"
    "v_mfma_f32_16x16x32_bf16 v[148:151], a[116:119], v[204:207], v[148:151]\n"
    "s_cmp_lg_u32 s56, 0\n"
    "s_cbranch_scc1 G_f2%=\n"
    "s_or_b32 s58, s42, s51\n"
    "s_mov_b64 exec, 1\n"
    "v_mov_b32 v116, s58\n"
    "global_store_dwordx2 v118, v[116:117], s[54:55] sc1\n"
    "s_mov_b64 exec, -1\n"
    "G_f2%=:\n"
    "v_mfma_f32_16x16x32_bf16 v[152:155], a[180:183], v[204:207], v[152:155]\n"
    "v_mfma_f32_16x16x32_bf16 v[156:159], a[244:247], v[204:207], v[156:159]\n"
    "s_waitcnt lgkmcnt(4)\n"
    "v_mfma_f32_16x16x32_bf16 v[128:131], a[56:59], v[208:211], v[128:131]\n"
    "ds_read_b128 v[200:203], v123 offset:1024\n"
    "v_mfma_f32_16x16x32_bf16 v[132:135], a[120:123], v[208:211], v[132:135]\n"
    "ds_read_b128 v[204:207], v123 offset:17408\n"
    "v_mfma_f32_16x16x32_bf16 v[136:139], a[184:187], v[208:211], v[136:139]\n"
    "v_mfma_f32_16x16x32_bf16 v[140:143], a[248:251], v[208:211], v[140:143]\n"
    "v_mfma_f32_16x16x32_bf16 v[144:147], a[56:59], v[212:215], v[144:147]\n"
    "global_load_lds_dwordx4 v121, s[64:65] offset:1024 sc1\n"
    "v_mfma_f32_16x16x32_bf16 v[148:151], a[120:123], v[212:215], v[148:151]\n"
    "v_mfma_f32_16x16x32_bf16 v[152:155], a[184:187], v[212:215], v[152:155]\n"
    "v_mfma_f32_16x16x32_bf16 v[156:159], a[248:251], v[212:215], v[156:159]\n"
    "s_add_u32 s51, s51, 1\n"
    "s_waitcnt lgkmcnt(4)\n"
    "v_mfma_f32_16x16x32_bf16 v[128:131], a[60:63], v[216:219], v[128:131]\n"
    "ds_read_b128 v[208:211], v123 offset:2048\n"
    "v_mfma_f32_16x16x32_bf16 v[132:135], a[124:127], v[216:219], v[132:135]\n"
    "ds_read_b128 v[212:215], v123 offset:18432\n"
    "v_mfma_f32_16x16x32_bf16 v[136:139], a[188:191], v[216:219], v[136:139]\n"
    "v_mfma_f32_16x16x32_bf16 v[140:143], a[252:255], v[216:219], v[140:143]\n"
    "v_mfma_f32_16x16x32_bf16 v[144:147], a[60:63], v[220:223], v[144:147]\n"
    "global_load_lds_dwordx4 v121, s[64:65] offset:2048 sc1\n"
    "v_mfma_f32_16x16x32_bf16 v[148:151], a[124:127], v[220:223], v[148:151]\n"
    "v_mfma_f32_16x16x32_bf16 v[152:155], a[188:191], v[220:223], v[152:155]\n"
    "v_mfma_f32_16x16x32_bf16 v[156:159], a[252:255], v[220:223], v[156:159]\n"
    "s_add_u32 s58, s51, -3\n"
    "s_cmp_gt_u32 s58, 28\n"
    "s_waitcnt lgkmcnt(4)\n"
    "s_cbranch_scc0 G_loop%=\n"
    "s_nop 7\n"
    "v_cvt_pk_bf16_f32 v224, v128, v129\n"
    "v_cvt_pk_bf16_f32 v225, v130, v131\n"
    "v_cvt_pk_bf16_f32 v226, v132, v133\n"
    "v_cvt_pk_bf16_f32 v227, v134, v135\n"
    "v_cvt_pk_bf16_f32 v228, v144, v145\n"
    "v_cvt_pk_bf16_f32 v229, v146, v147\n"
    "v_cvt_pk_bf16_f32 v230, v148, v149\n"
    "v_cvt_pk_bf16_f32 v231, v150, v151\n"
    "v_cvt_pk_bf16_f32 v232, v136, v137\n"
    "v_cvt_pk_bf16_f32 v233, v138, v139\n"
    "v_cvt_pk_bf16_f32 v234, v140, v141\n"
    "v_cvt_pk_bf16_f32 v235, v142, v143\n"
    "v_cvt_pk_bf16_f32 v236, v152, v153\n"
    "v_cvt_pk_bf16_f32 v237, v154, v155\n"
    "v_cvt_pk_bf16_f32 v238, v156, v157\n"
    "v_cvt_pk_bf16_f32 v239, v158, v159\n"
    "global_store_dwordx4 v121, v[224:227], s[44:45] offset:0 sc1\n"
    "global_store_dwordx4 v121, v[228:231], s[44:45] offset:1024 sc1\n"
    "global_store_dwordx4 v121, v[232:235], s[44:45] offset:2048 sc1\n"
    "global_store_dwordx4 v121, v[236:239], s[44:45] offset:3072 sc1\n"
    "s_add_u32 s46, s40, 0xaa00000\n"
    "s_addc_u32 s47, s41, 0\n"
    "s_waitcnt vmcnt(0)\n"
    "s_barrier\n"
    "s_cmp_lg_u32 s56, 0\n"
    "s_cbranch_scc1 G_f3%=\n"
    "s_or_b32 s58, s42, s51\n"
    "s_mov_b64 exec, 1\n"
    "v_mov_b32 v116, s58\n"
    "global_store_dwordx2 v118, v[116:117], s[54:55] sc1\n"
    "s_mov_b64 exec, -1\n"
    "G_f3%=:\n"
    "s_branch G_loop%=\n"
    "G_t1%=:\n"
    "s_nop 7\n"
    "v_cvt_pk_bf16_f32 v224, v160, v161\n"
    "v_cvt_pk_bf16_f32 v225, v162, v163\n"
    "v_cvt_pk_bf16_f32 v226, v164, v165\n"
    "v_cvt_pk_bf16_f32 v227, v166, v167\n"
    "v_cvt_pk_bf16_f32 v228, v176, v177\n"
    "v_cvt_pk_bf16_f32 v229, v178, v179\n"
    "v_cvt_pk_bf16_f32 v230, v180, v181\n"
    "v_cvt_pk_bf16_f32 v231, v182, v183\n"
    "v_cvt_pk_bf16_f32 v232, v168, v169\n"
    "v_cvt_pk_bf16_f32 v233, v170, v171\n"
    "v_cvt_pk_bf16_f32 v234, v172, v173\n"
    "v_cvt_pk_bf16_f32 v235, v174, v175\n"
    "v_cvt_pk_bf16_f32 v236, v184, v185\n"
    "v_cvt_pk_bf16_f32 v237, v186, v187\n"
    "v_cvt_pk_bf16_f32 v238, v188, v189\n"
    "v_cvt_pk_bf16_f32 v239, v190, v191\n"
    "global_store_dwordx4 v121, v[224:227], s[44:45] offset:0 sc1\n"
    "global_store_dwordx4 v121, v[228:231], s[44:45] offset:1024 sc1\n"
    "global_store_dwordx4 v121, v[232:235], s[44:45] offset:2048 sc1\n"
    "global_store_dwordx4 v121, v[236:239], s[44:45] offset:3072 sc1\n"
    "s_add_u32 s46, s40, 0xaa00000\n"
    "s_addc_u32 s47, s41, 0\n"
    "s_waitcnt vmcnt(0)\n"
    "s_barrier\n"
    "s_cmp_lg_u32 s56, 0\n"
    "s_cbranch_scc1 G_f4%=\n"
    "s_or_b32 s58, s42, s51\n"
    "s_mov_b64 exec, 1\n"
    "v_mov_b32 v116, s58\n"
    "global_store_dwordx2 v118, v[116:117], s[54:55] sc1\n"
    "s_mov_b64 exec, -1\n"
    "G_f4%=:\n"
    "s_cmp_eq_u32 s51, 32\n"
    "s_cbranch_scc1 G_end%=\n"
    "s_add_u32 s58, s40, 0x20e000\n"
    "s_addc_u32 s59, s41, 0\n"
    "s_add_u32 s60, s40, 0x200200\n"
    "s_addc_u32 s61, s41, 0\n"
    "s_or_b32 s62, s42, 1\n"
    "s_mov_b32 s63, s43\n"
    "v_lshlrev_b32 v224, 3, v121\n"
    "G_xw1%=:\n"
    "global_load_dwordx2 v[226:227], v224, s[58:59] sc1\n"
    "G_xc1%=:\n"
    "s_waitcnt vmcnt(0)\n"
    "v_cmp_ne_u64 vcc, s[62:63], v[226:227]\n"
    "s_nop 0\n"
    "s_cbranch_vccz G_xok1%=\n"
    "s_sleep 4\n"
    "s_add_u32 s57, s57, 1\n"
    "s_and_b32 s68, s57, 0xff\n"
    "s_cbranch_scc1 G_xw1%=\n"
    "global_load_dwordx2 v[226:227], v118, s[60:61] sc1\n"
    "s_waitcnt vmcnt(0)\n"
    "v_cmp_eq_u64 vcc, s[42:43], v[226:227]\n"
    "s_nop 0\n"
    "s_cbranch_vccnz G_xok1%=\n"
    "s_cmp_lt_u32 s57, 0x40000\n"
    "s_cbranch_scc1 G_xw1%=\n"
    "v_mov_b32 v116, s42\n"
    "global_store_dwordx2 v118, v[116:117], s[60:61] sc1\n"
    "G_xok1%=:\n"
    "s_mov_b32 vcc_lo, 0xcccccccc\n"
    "s_mov_b32 vcc_hi, 0xcccccccc\n"
    "s_branch G_even%=\n"
    "G_end%=:\n"
    "s_waitcnt lgkmcnt(0)\n"
    :: GIN : GCLOB);
  else if constexpr (MAT == 1) asm volatile(
    "s_mov_b64 s[40:41], %0\n"
    "s_mov_b64 s[42:43], %1\n"
    "s_mov_b64 s[44:45], %2\n"
    "s_mov_b64 s[54:55], %3\n"
    "s_mov_b32 s56, %4\n"
    "v_mov_b32 v121, %5\n"
    "v_mov_b32 v119, %7\n"
    "v_mov_b32 v120, %8\n"
    "s_add_u32 s58, s40, 0x20e000\n"
    "s_addc_u32 s59, s41, 0\n"
    "v_lshlrev_b32 v225, 3, v121\n"
    "v_and_b32 v224, 0x380, v225\n"
    "global_load_dwordx2 v[226:227], v224, s[58:59] sc1\n"
    "v_xor_b32 v112, 0, %6\n"
    "v_xor_b32 v113, 32, %6\n"
    "v_xor_b32 v114, 64, %6\n"
    "v_xor_b32 v115, 96, %6\n"
    "ds_read_b64_tr_b16 a[0:1], v112 offset:0\n"
    "ds_read_b64_tr_b16 a[2:3], v112 offset:1024\n"
    "ds_read_b64_tr_b16 a[4:5], v112 offset:8192\n"
    "ds_read_b64_tr_b16 a[6:7], v112 offset:9216\n"
    "ds_read_b64_tr_b16 a[8:9], v112 offset:16384\n"
    "ds_read_b64_tr_b16 a[10:11], v112 offset:17408\n"
    "ds_read_b64_tr_b16 a[12:13], v112 offset:24576\n"
    "ds_read_b64_tr_b16 a[14:15], v112 offset:25600\n"
    "s_waitcnt lgkmcnt(6)\n"
    "ds_read_b64_tr_b16 a[16:17], v112 offset:32768\n"
    "ds_read_b64_tr_b16 a[18:19], v112 offset:33792\n"
    "ds_read_b64_tr_b16 a[20:21], v112 offset:40960\n"
    "ds_read_b64_tr_b16 a[22:23], v112 offset:41984\n"
    "ds_read_b64_tr_b16 a[24:25], v112 offset:49152\n"
    "ds_read_b64_tr_b16 a[26:27], v112 offset:50176\n"
    "ds_read_b64_tr_b16 a[28:29], v112 offset:57344\n"
    "ds_read_b64_tr_b16 a[30:31], v112 offset:58368\n"
    "s_waitcnt lgkmcnt(6)\n"
    "ds_read_b64_tr_b16 a[64:65], v113 offset:0\n"
    "ds_read_b64_tr_b16 a[66:67], v113 offset:1024\n"
    "ds_read_b64_tr_b16 a[68:69], v113 offset:8192\n"
    "ds_read_b64_tr_b16 a[70:71], v113 offset:9216\n"
    "ds_read_b64_tr_b16 a[72:73], v113 offset:16384\n"
    "ds_read_b64_tr_b16 a[74:75], v113 offset:17408\n"
    "ds_read_b64_tr_b16 a[76:77], v113 offset:24576\n"
    "ds_read_b64_tr_b16 a[78:79], v113 offset:25600\n"
    "s_waitcnt lgkmcnt(6)\n"
    "ds_read_b64_tr_b16 a[80:81], v113 offset:32768\n"
    "ds_read_b64_tr_b16 a[82:83], v113 offset:33792\n"
    "ds_read_b64_tr_b16 a[84:85], v113 offset:40960\n"
    "ds_read_b64_tr_b16 a[86:87], v113 offset:41984\n"
    "ds_read_b64_tr_b16 a[88:89], v113 offset:49152\n"
    "ds_read_b64_tr_b16 a[90:91], v113 offset:50176\n"
    "ds_read_b64_tr_b16 a[92:93], v113 offset:57344\n"
    "ds_read_b64_tr_b16 a[94:95], v113 offset:58368\n"
    "s_waitcnt lgkmcnt(6)\n"
    "ds_read_b64_tr_b16 a[128:129], v114 offset:0\n"
    "ds_read_b64_tr_b16 a[130:131], v114 offset:1024\n"
    "ds_read_b64_tr_b16 a[132:133], v114 offset:8192\n"
    "ds_read_b64_tr_b16 a[134:135], v114 offset:9216\n"
    "ds_read_b64_tr_b16 a[136:137], v114 offset:16384\n"
    "ds_read_b64_tr_b16 a[138:139], v114 offset:17408\n"
    "ds_read_b64_tr_b16 a[140:141], v114 offset:24576\n"
    "ds_read_b64_tr_b16 a[142:143], v114 offset:25600\n"
    "s_waitcnt lgkmcnt(6)\n"
    "ds_read_b64_tr_b16 a[144:145], v114 offset:32768\n"
    "ds_read_b64_tr_b16 a[146:147], v114 offset:33792\n"
    "ds_read_b64_tr_b16 a[148:149], v114 offset:40960\n"
    "ds_read_b64_tr_b16 a[150:151], v114 offset:41984\n"
    "ds_read_b64_tr_b16 a[152:153], v114 offset:49152\n"
    "ds_read_b64_tr_b16 a[154:155], v114 offset:50176\n"
    "ds_read_b64_tr_b16 a[156:157], v114 offset:57344\n"
    "ds_read_b64_tr_b16 a[158:159], v114 offset:58368\n"
    "s_waitcnt lgkmcnt(6)\n"
    "ds_read_b64_tr_b16 a[192:193], v115 offset:0\n"
    "ds_read_b64_tr_b16 a[194:195], v115 offset:1024\n"
    "ds_read_b64_tr_b16 a[196:197], v115 offset:8192\n"
    "ds_read_b64_tr_b16 a[198:199], v115 offset:9216\n"
    "ds_read_b64_tr_b16 a[200:201], v115 offset:16384\n"
    "ds_read_b64_tr_b16 a[202:203], v115 offset:17408\n"
    "ds_read_b64_tr_b16 a[204:205], v115 offset:24576\n"
    "ds_read_b64_tr_b16 a[206:207], v115 offset:25600\n"
    "s_waitcnt lgkmcnt(6)\n"
    "ds_read_b64_tr_b16 a[208:209], v115 offset:32768\n"
    "ds_read_b64_tr_b16 a[210:211], v115 offset:33792\n"
    "ds_read_b64_tr_b16 a[212:213], v115 offset:40960\n"
    "ds_read_b64_tr_b16 a[214:215], v115 offset:41984\n"
    "ds_read_b64_tr_b16 a[216:217], v115 offset:49152\n"
    "ds_read_b64_tr_b16 a[218:219], v115 offset:50176\n"
    "ds_read_b64_tr_b16 a[220:221], v115 offset:57344\n"
    "ds_read_b64_tr_b16 a[222:223], v115 offset:58368\n"
    "s_waitcnt lgkmcnt(6)\n"
    "v_xor_b32 v112, 0, %6\n"
    "v_add_u32 v112, 0x10000, v112\n"
    "v_xor_b32 v113, 32, %6\n"
    "v_add_u32 v113, 0x10000, v113\n"
    "v_xor_b32 v114, 64, %6\n"
    "v_add_u32 v114, 0x10000, v114\n"
    "v_xor_b32 v115, 96, %6\n"
    "v_add_u32 v115, 0x10000, v115\n"
    "ds_read_b64_tr_b16 a[32:33], v112 offset:0\n"
    "ds_read_b64_tr_b16 a[34:35], v112 offset:1024\n"
    "ds_read_b64_tr_b16 a[36:37], v112 offset:8192\n"
    "ds_read_b64_tr_b16 a[38:39], v112 offset:9216\n"
    "ds_read_b64_tr_b16 a[40:41], v112 offset:16384\n"
    "ds_read_b64_tr_b16 a[42:43], v112 offset:17408\n"
    "ds_read_b64_tr_b16 a[44:45], v112 offset:24576\n"
    "ds_read_b64_tr_b16 a[46:47], v112 offset:25600\n"
    "s_waitcnt lgkmcnt(6)\n"
    "ds_read_b64_tr_b16 a[48:49], v112 offset:32768\n"
    "ds_read_b64_tr_b16 a[50:51], v112 offset:33792\n"
    "ds_read_b64_tr_b16 a[52:53], v112 offset:40960\n"
    "ds_read_b64_tr_b16 a[54:55], v112 offset:41984\n"
    "ds_read_b64_tr_b16 a[56:57], v112 offset:49152\n"
    "ds_read_b64_tr_b16 a[58:59], v112 offset:50176\n"
    "ds_read_b64_tr_b16 a[60:61], v112 offset:57344\n"
    "ds_read_b64_tr_b16 a[62:63], v112 offset:58368\n"
    "s_waitcnt lgkmcnt(6)\n"
    "ds_read_b64_tr_b16 a[96:97], v113 offset:0\n"
    "ds_read_b64_tr_b16 a[98:99], v113 offset:1024\n"
    "ds_read_b64_tr_b16 a[100:101], v113 offset:8192\n"
    "ds_read_b64_tr_b16 a[102:103], v113 offset:9216\n"
    "ds_read_b64_tr_b16 a[104:105], v113 offset:16384\n"
    "ds_read_b64_tr_b16 a[106:107], v113 offset:17408\n"
    "ds_read_b64_tr_b16 a[108:109], v113 offset:24576\n"
    "ds_read_b64_tr_b16 a[110:111], v113 offset:25600\n"
    "s_waitcnt lgkmcnt(6)\n"
    "ds_read_b64_tr_b16 a[112:113], v113 offset:32768\n"
    "ds_read_b64_tr_b16 a[114:115], v113 offset:33792\n"
    "ds_read_b64_tr_b16 a[116:117], v113 offset:40960\n"
    "ds_read_b64_tr_b16 a[118:119], v113 offset:41984\n"
    "ds_read_b64_tr_b16 a[120:121], v113 offset:49152\n"
    "ds_read_b64_tr_b16 a[122:123], v113 offset:50176\n"
    "ds_read_b64_tr_b16 a[124:125], v113 offset:57344\n"
    "ds_read_b64_tr_b16 a[126:127], v113 offset:58368\n"
    "s_waitcnt lgkmcnt(6)\n"
    "ds_read_b64_tr_b16 a[160:161], v114 offset:0\n"
    "ds_read_b64_tr_b16 a[162:163], v114 offset:1024\n"
    "ds_read_b64_tr_b16 a[164:165], v114 offset:8192\n"
    "ds_read_b64_tr_b16 a[166:167], v114 offset:9216\n"
    "ds_read_b64_tr_b16 a[168:169], v114 offset:16384\n"
    "ds_read_b64_tr_b16 a[170:171], v114 offset:17408\n"
    "ds_read_b64_tr_b16 a[172:173], v114 offset:24576\n"
    "ds_read_b64_tr_b16 a[174:175], v114 offset:25600\n"
    "s_waitcnt lgkmcnt(6)\n"
    "ds_read_b64_tr_b16 a[176:177], v114 offset:32768\n"
    "ds_read_b64_tr_b16 a[178:179], v114 offset:33792\n"
    "ds_read_b64_tr_b16 a[180:181], v114 offset:40960\n"
    "ds_read_b64_tr_b16 a[182:183], v114 offset:41984\n"
    "ds_read_b64_tr_b16 a[184:185], v114 offset:49152\n"
    "ds_read_b64_tr_b16 a[186:187], v114 offset:50176\n"
    "ds_read_b64_tr_b16 a[188:189], v114 offset:57344\n"
    "ds_read_b64_tr_b16 a[190:191], v114 offset:58368\n"
    "s_waitcnt lgkmcnt(6)\n"
    "ds_read_b64_tr_b16 a[224:225], v115 offset:0\n"
    "ds_read_b64_tr_b16 a[226:227], v115 offset:1024\n"
    "ds_read_b64_tr_b16 a[228:229], v115 offset:8192\n"
    "ds_read_b64_tr_b16 a[230:231], v115 offset:9216\n"
    "ds_read_b64_tr_b16 a[232:233], v115 offset:16384\n"
    "ds_read_b64_tr_b16 a[234:235], v115 offset:17408\n"
    "ds_read_b64_tr_b16 a[236:237], v115 offset:24576\n"
    "ds_read_b64_tr_b16 a[238:239], v115 offset:25600\n"
    "s_waitcnt lgkmcnt(6)\n"
    "ds_read_b64_tr_b16 a[240:241], v115 offset:32768\n"
    "ds_read_b64_tr_b16 a[242:243], v115 offset:33792\n"
    "ds_read_b64_tr_b16 a[244:245], v115 offset:40960\n"
    "ds_read_b64_tr_b16 a[246:247], v115 offset:41984\n"
    "ds_read_b64_tr_b16 a[248:249], v115 offset:49152\n"
    "ds_read_b64_tr_b16 a[250:251], v115 offset:50176\n"
    "ds_read_b64_tr_b16 a[252:253], v115 offset:57344\n"
    "ds_read_b64_tr_b16 a[254:255], v115 offset:58368\n"
    "s_waitcnt lgkmcnt(6)\n"
    "s_lshr_b32 s58, s56, 1\n"
    "s_lshl_b32 s58, s58, 15\n"
    "v_add_u32 v122, s58, v121\n"
    "v_add_u32 v123, 0x10000, v122\n"
    "s_lshl_b32 s50, s56, 14\n"
    "v_mov_b32 v124, 0xffbfffbf\n"
    "v_mov_b32 v125, 0xffbfffbf\n"
    "v_mov_b32 v126, 0xffbfffbf\n"
    "v_mov_b32 v127, 0xffbfffbf\n"
    "v_mov_b32 v118, 0\n"
    "v_mov_b32 v117, s43\n"
    "s_add_u32 s58, s50, 0x4d00000\n"
    "s_add_u32 s48, s40, s58\n"
    "s_addc_u32 s49, s41, 0\n"
    "s_mov_b32 s57, 0\n"
    "s_mov_b32 s51, 0\n"
    "s_waitcnt lgkmcnt(0)\n"
    "s_barrier\n"
    "s_add_u32 s58, s40, 0x20e000\n"
    "s_addc_u32 s59, s41, 0\n"
    "s_add_u32 s60, s40, 0x200200\n"
    "s_addc_u32 s61, s41, 0\n"
    "s_or_b32 s62, s42, 1\n"
    "s_mov_b32 s63, s43\n"
    "s_branch G_xc0%=\n"
    "G_xw0%=:\n"
    "global_load_dwordx2 v[226:227], v224, s[58:59] sc1\n"
    "G_xc0%=:\n"
    "s_waitcnt vmcnt(0)\n"
    "v_cmp_ne_u64 vcc, s[62:63], v[226:227]\n"
    "s_nop 0\n"
    "s_cbranch_vccz G_xok0%=\n"
    "s_sleep 4\n"
    "s_add_u32 s57, s57, 1\n"
    "s_and_b32 s68, s57, 0xff\n"
    "s_cbranch_scc1 G_xw0%=\n"
    "global_load_dwordx2 v[226:227], v118, s[60:61] sc1\n"
    "s_waitcnt vmcnt(0)\n"
    "v_cmp_eq_u64 vcc, s[42:43], v[226:227]\n"
    "s_nop 0\n"
    "s_cbranch_vccnz G_xok0%=\n"
    "s_cmp_lt_u32 s57, 0x40000\n"
    "s_cbranch_scc1 G_xw0%=\n"
    "v_mov_b32 v116, s42\n"
    "global_store_dwordx2 v118, v[116:117], s[60:61] sc1\n"
    "G_xok0%=:\n"
    "s_mov_b32 vcc_lo, 0xcccccccc\n"
    "s_mov_b32 vcc_hi, 0xcccccccc\n"
    "s_add_u32 m0, s50, 0\n"
    "s_add_u32 s64, s48, 0\n"
    "s_addc_u32 s65, s49, 0\n"
    "global_load_lds_dwordx4 v121, s[64:65] offset:0 sc1\n"
    "global_load_lds_dwordx4 v121, s[64:65] offset:1024 sc1\n"
    "global_load_lds_dwordx4 v121, s[64:65] offset:2048 sc1\n"
    "global_load_lds_dwordx4 v121, s[64:65] offset:3072 sc1\n"
    "s_add_u32 m0, s50, 4096\n"
    "s_add_u32 s64, s48, 4096\n"
    "s_addc_u32 s65, s49, 0\n"
    "global_load_lds_dwordx4 v121, s[64:65] offset:0 sc1\n"
    "global_load_lds_dwordx4 v121, s[64:65] offset:1024 sc1\n"
    "global_load_lds_dwordx4 v121, s[64:65] offset:2048 sc1\n"
    "global_load_lds_dwordx4 v121, s[64:65] offset:3072 sc1\n"
    "s_add_u32 m0, s50, 8192\n"
    "s_add_u32 s64, s48, 8192\n"
    "s_addc_u32 s65, s49, 0\n"
    "global_load_lds_dwordx4 v121, s[64:65] offset:0 sc1\n"
    "global_load_lds_dwordx4 v121, s[64:65] offset:1024 sc1\n"
    "global_load_lds_dwordx4 v121, s[64:65] offset:2048 sc1\n"
    "global_load_lds_dwordx4 v121, s[64:65] offset:3072 sc1\n"
    "s_add_u32 m0, s50, 12288\n"
    "s_add_u32 s64, s48, 12288\n"
    "s_addc_u32 s65, s49, 0\n"
    "s_add_u32 s48, s48, 0x10000\n"
    "s_addc_u32 s49, s49, 0\n"
    "global_load_lds_dwordx4 v121, s[64:65] offset:0 sc1\n"
    "global_load_lds_dwordx4 v121, s[64:65] offset:1024 sc1\n"
    "global_load_lds_dwordx4 v121, s[64:65] offset:2048 sc1\n"
    "global_load_lds_dwordx4 v121, s[64:65] offset:3072 sc1\n"
    "s_add_u32 m0, s50, 65536\n"
    "s_add_u32 s64, s48, 0\n"
    "s_addc_u32 s65, s49, 0\n"
    "global_load_lds_dwordx4 v121, s[64:65] offset:0 sc1\n"
    "global_load_lds_dwordx4 v121, s[64:65] offset:1024 sc1\n"
    "global_load_lds_dwordx4 v121, s[64:65] offset:2048 sc1\n"
    "global_load_lds_dwordx4 v121, s[64:65] offset:3072 sc1\n"
    "s_add_u32 m0, s50, 69632\n"
    "s_add_u32 s64, s48, 4096\n"
    "s_addc_u32 s65, s49, 0\n"
    "global_load_lds_dwordx4 v121, s[64:65] offset:0 sc1\n"
    "global_load_lds_dwordx4 v121, s[64:65] offset:1024 sc1\n"
    "global_load_lds_dwordx4 v121, s[64:65] offset:2048 sc1\n"
    "global_load_lds_dwordx4 v121, s[64:65] offset:3072 sc1\n"
    "s_add_u32 m0, s50, 73728\n"
    "s_add_u32 s64, s48, 8192\n"
    "s_addc_u32 s65, s49, 0\n"
    "global_load_lds_dwordx4 v121, s[64:65] offset:0 sc1\n"
    "global_load_lds_dwordx4 v121, s[64:65] offset:1024 sc1\n"
    "global_load_lds_dwordx4 v121, s[64:65] offset:2048 sc1\n"
    "s_mov_b64 s[66:67], s[44:45]\n"
    "s_add_u32 s46, s40, 0xab00000\n"
    "s_addc_u32 s47, s41, 0\n"
    "s_add_u32 s44, s44, 0x9800000\n"
    "s_addc_u32 s45, s45, 0\n"
    "s_waitcnt vmcnt(19) lgkmcnt(0)\n"
    "s_barrier\n"
    "v_lshlrev_b32 v112, 3, v121\n"
    "v_min_u32 v112, 0x800, v112\n"
    "s_add_u32 s58, s40, 0x312000\n"
    "s_addc_u32 s59, s41, 0\n"
    "global_load_dwordx2 v[114:115], v112, s[58:59] sc1\n"
    "ds_read_b128 v[192:195], v122 offset:0\n"
    "ds_read_b128 v[196:199], v122 offset:16384\n"
    "ds_read_b128 v[200:203], v122 offset:1024\n"
    "ds_read_b128 v[204:207], v122 offset:17408\n"
    "ds_read_b128 v[208:211], v122 offset:2048\n"
    "ds_read_b128 v[212:215], v122 offset:18432\n"
    "s_waitcnt lgkmcnt(4)\n"
    "s_branch G_even%=\n"
    ".p2align 6\n"
    "G_loop%=:\n"
    "v_mfma_f32_16x16x32_bf16 v[160:163], a[0:3], v[192:195], 0\n"
    "ds_read_b128 v[216:219], v123 offset:3072\n"
    "v_mfma_f32_16x16x32_bf16 v[164:167], a[64:67], v[192:195], 0\n"
    "ds_read_b128 v[220:223], v123 offset:19456\n"
    "v_mfma_f32_16x16x32_bf16 v[168:171], a[128:131], v[192:195], 0\n"
    "v_mfma_f32_16x16x32_bf16 v[172:175], a[192:195], v[192:195], 0\n"
    "v_cvt_pk_bf16_f32 v224, v128, v129\n"
    "v_cvt_pk_bf16_f32 v225, v130, v131\n"
    "v_mfma_f32_16x16x32_bf16 v[176:179], a[0:3], v[196:199], 0\n"
    "global_load_lds_dwordx4 v121, s[64:65] offset:3072 sc1\n"
    "v_mfma_f32_16x16x32_bf16 v[180:183], a[64:67], v[196:199], 0\n"
    "v_cvt_pk_bf16_f32 v226, v132, v133\n"
    "v_cvt_pk_bf16_f32 v227, v134, v135\n"
    "v_mfma_f32_16x16x32_bf16 v[184:187], a[128:131], v[196:199], 0\n"
    "global_store_dwordx4 v119, v[124:127], s[46:47] sc1\n"
    "v_mfma_f32_16x16x32_bf16 v[188:191], a[192:195], v[196:199], 0\n"
    "v_cvt_pk_bf16_f32 v228, v144, v145\n"
    "v_cvt_pk_bf16_f32 v229, v146, v147\n"
    "s_waitcnt lgkmcnt(4)\n"
    "v_mfma_f32_16x16x32_bf16 v[160:163], a[4:7], v[200:203], v[160:163]\n"
    "ds_read_b128 v[192:195], v123 offset:4096\n"
    "v_mfma_f32_16x16x32_bf16 v[164:167], a[68:71], v[200:203], v[164:167]\n"
    "ds_read_b128 v[196:199], v123 offset:20480\n"
    "v_mfma_f32_16x16x32_bf16 v[168:171], a[132:135], v[200:203], v[168:171]\n"
    "s_add_u32 m0, s50, 12288\n"
    "s_add_u32 s64, s48, 12288\n"
    "s_addc_u32 s65, s49, 0\n"
    "v_cvt_pk_bf16_f32 v230, v148, v149\n"
    "v_cvt_pk_bf16_f32 v231, v150, v151\n"
    "v_mfma_f32_16x16x32_bf16 v[172:175], a[196:199], v[200:203], v[172:175]\n"
    "s_add_u32 s48, s48, 0x10000\n"
    "s_addc_u32 s49, s49, 0\n"
    "v_cvt_pk_bf16_f32 v232, v136, v137\n"
    "v_cvt_pk_bf16_f32 v233, v138, v139\n"
    "v_mfma_f32_16x16x32_bf16 v[176:179], a[4:7], v[204:207], v[176:179]\n"
    "global_load_lds_dwordx4 v121, s[64:65] offset:0 sc1\n"
    "v_mfma_f32_16x16x32_bf16 v[180:183], a[68:71], v[204:207], v[180:183]\n"
    "v_cvt_pk_bf16_f32 v234, v140, v141\n"
    "v_cvt_pk_bf16_f32 v235, v142, v143\n"
    "v_mfma_f32_16x16x32_bf16 v[184:187], a[132:135], v[204:207], v[184:187]\n"
    "global_store_dwordx4 v120, v[124:127], s[46:47] sc1\n"
    "v_mfma_f32_16x16x32_bf16 v[188:191], a[196:199], v[204:207], v[188:191]\n"
    "v_cvt_pk_bf16_f32 v236, v152, v153\n"
    "v_cvt_pk_bf16_f32 v237, v154, v155\n"
    "s_waitcnt lgkmcnt(4)\n"
    "v_mfma_f32_16x16x32_bf16 v[160:163], a[8:11], v[208:211], v[160:163]\n"
    "ds_read_b128 v[200:203], v123 offset:5120\n"
    "v_mfma_f32_16x16x32_bf16 v[164:167], a[72:75], v[208:211], v[164:167]\n"
    "ds_read_b128 v[204:207], v123 offset:21504\n"
    "v_mfma_f32_16x16x32_bf16 v[168:171], a[136:139], v[208:211], v[168:171]\n"
    "v_cvt_pk_bf16_f32 v238, v156, v157\n"
    "v_cvt_pk_bf16_f32 v239, v158, v159\n"
    "v_mfma_f32_16x16x32_bf16 v[172:175], a[200:203], v[208:211], v[172:175]\n"
    "s_not_b64 vcc, vcc\n"
    "v_cndmask_b32_dpp v240, v232, v224, vcc quad_perm:[0,1,0,1] row_mask:0xf bank_mask:0xf\n"
    "v_cndmask_b32_dpp v241, v233, v225, vcc quad_perm:[0,1,0,1] row_mask:0xf bank_mask:0xf\n"
    "v_mfma_f32_16x16x32_bf16 v[176:179], a[8:11], v[212:215], v[176:179]\n"
    "global_load_lds_dwordx4 v121, s[64:65] offset:1024 sc1\n"
    "v_mfma_f32_16x16x32_bf16 v[180:183], a[72:75], v[212:215], v[180:183]\n"
    "v_cndmask_b32_dpp v242, v234, v226, vcc quad_perm:[0,1,0,1] row_mask:0xf bank_mask:0xf\n"
    "v_cndmask_b32_dpp v243, v235, v227, vcc quad_perm:[0,1,0,1] row_mask:0xf bank_mask:0xf\n"
    "v_mfma_f32_16x16x32_bf16 v[184:187], a[136:139], v[212:215], v[184:187]\n"
    "global_store_dwordx4 v121, v[240:243], s[44:45] offset:0 sc1\n"
    "v_mfma_f32_16x16x32_bf16 v[188:191], a[200:203], v[212:215], v[188:191]\n"
    "v_cndmask_b32_dpp v248, v236, v228, vcc quad_perm:[0,1,0,1] row_mask:0xf bank_mask:0xf\n"
    "v_cndmask_b32_dpp v249, v237, v229, vcc quad_perm:[0,1,0,1] row_mask:0xf bank_mask:0xf\n"
    "s_waitcnt lgkmcnt(4)\n"
    "v_mfma_f32_16x16x32_bf16 v[160:163], a[12:15], v[216:219], v[160:163]\n"
    "ds_read_b128 v[208:211], v123 offset:6144\n"
    "v_mfma_f32_16x16x32_bf16 v[164:167], a[76:79], v[216:219], v[164:167]\n"
    "ds_read_b128 v[212:215], v123 offset:22528\n"
    "v_mfma_f32_16x16x32_bf16 v[168:171], a[140:143], v[216:219], v[168:171]\n"
    "v_cndmask_b32_dpp v250, v238, v230, vcc quad_perm:[0,1,0,1] row_mask:0xf bank_mask:0xf\n"
    "v_cndmask_b32_dpp v251, v239, v231, vcc quad_perm:[0,1,0,1] row_mask:0xf bank_mask:0xf\n"
    "v_mfma_f32_16x16x32_bf16 v[172:175], a[204:207], v[216:219], v[172:175]\n"
    "s_not_b64 vcc, vcc\n"
    "v_cndmask_b32_dpp v244, v224, v232, vcc quad_perm:[2,3,2,3] row_mask:0xf bank_mask:0xf\n"
    "v_cndmask_b32_dpp v245, v225, v233, vcc quad_perm:[2,3,2,3] row_mask:0xf bank_mask:0xf\n"
    "v_mfma_f32_16x16x32_bf16 v[176:179], a[12:15], v[220:223], v[176:179]\n"
    "global_load_lds_dwordx4 v121, s[64:65] offset:2048 sc1\n"
    "v_mfma_f32_16x16x32_bf16 v[180:183], a[76:79], v[220:223], v[180:183]\n"
    "v_cndmask_b32_dpp v246, v226, v234, vcc quad_perm:[2,3,2,3] row_mask:0xf bank_mask:0xf\n"
    "v_cndmask_b32_dpp v247, v227, v235, vcc quad_perm:[2,3,2,3] row_mask:0xf bank_mask:0xf\n"
    "v_mfma_f32_16x16x32_bf16 v[184:187], a[140:143], v[220:223], v[184:187]\n"
    "global_store_dwordx4 v121, v[248:251], s[44:45] offset:2048 sc1\n"
    "v_mfma_f32_16x16x32_bf16 v[188:191], a[204:207], v[220:223], v[188:191]\n"
    "v_cndmask_b32_dpp v252, v228, v236, vcc quad_perm:[2,3,2,3] row_mask:0xf bank_mask:0xf\n"
    "v_cndmask_b32_dpp v253, v229, v237, vcc quad_perm:[2,3,2,3] row_mask:0xf bank_mask:0xf\n"
    "s_waitcnt lgkmcnt(4)\n"
    "v_mfma_f32_16x16x32_bf16 v[160:163], a[16:19], v[192:195], v[160:163]\n"
    "ds_read_b128 v[216:219], v123 offset:7168\n"
    "v_mfma_f32_16x16x32_bf16 v[164:167], a[80:83], v[192:195], v[164:167]\n"
    "ds_read_b128 v[220:223], v123 offset:23552\n"
    "v_mfma_f32_16x16x32_bf16 v[168:171], a[144:147], v[192:195], v[168:171]\n"
    "global_store_dwordx4 v121, v[244:247], s[44:45] offset:1024 sc1\n"
    "v_cndmask_b32_dpp v254, v230, v238, vcc quad_perm:[2,3,2,3] row_mask:0xf bank_mask:0xf\n"
    "v_cndmask_b32_dpp v255, v231, v239, vcc quad_perm:[2,3,2,3] row_mask:0xf bank_mask:0xf\n"
    "v_mfma_f32_16x16x32_bf16 v[172:175], a[208:211], v[192:195], v[172:175]\n"
    "v_mfma_f32_16x16x32_bf16 v[176:179], a[16:19], v[196:199], v[176:179]\n"
    "global_load_lds_dwordx4 v121, s[64:65] offset:3072 sc1\n"
    "v_mfma_f32_16x16x32_bf16 v[180:183], a[80:83], v[196:199], v[180:183]\n"
    "v_mfma_f32_16x16x32_bf16 v[184:187], a[144:147], v[196:199], v[184:187]\n"
    "global_store_dwordx4 v121, v[252:255], s[44:45] offset:3072 sc1\n"
    "v_mfma_f32_16x16x32_bf16 v[188:191], a[208:211], v[196:199], v[188:191]\n"
    "s_waitcnt lgkmcnt(4)\n"
    "v_mfma_f32_16x16x32_bf16 v[160:163], a[20:23], v[200:203], v[160:163]\n"
    "s_waitcnt vmcnt(14) lgkmcnt(0)\n"
    "s_barrier\n"
    "v_mfma_f32_16x16x32_bf16 v[164:167], a[84:87], v[200:203], v[164:167]\n"
    "ds_read_b128 v[192:195], v123 offset:8192\n"
    "v_mfma_f32_16x16x32_bf16 v[168:171], a[148:151], v[200:203], v[168:171]\n"
    "ds_read_b128 v[196:199], v123 offset:24576\n"
    "s_add_u32 m0, s50, 65536\n"
    "s_add_u32 s64, s48, 0\n"
    "s_addc_u32 s65, s49, 0\n"
    "v_mfma_f32_16x16x32_bf16 v[172:175], a[212:215], v[200:203], v[172:175]\n"
    "v_mfma_f32_16x16x32_bf16 v[176:179], a[20:23], v[204:207], v[176:179]\n"
    "global_load_lds_dwordx4 v121, s[64:65] offset:0 sc1\n"
    "v_mfma_f32_16x16x32_bf16 v[180:183], a[84:87], v[204:207], v[180:183]\n"
    "v_mfma_f32_16x16x32_bf16 v[184:187], a[148:151], v[204:207], v[184:187]\n"
    "v_mfma_f32_16x16x32_bf16 v[188:191], a[212:215], v[204:207], v[188:191]\n"
    "s_waitcnt lgkmcnt(4)\n"
    "v_mfma_f32_16x16x32_bf16 v[160:163], a[24:27], v[208:211], v[160:163]\n"
    "ds_read_b128 v[200:203], v123 offset:9216\n"
    "v_mfma_f32_16x16x32_bf16 v[164:167], a[88:91], v[208:211], v[164:167]\n"
    "ds_read_b128 v[204:207], v123 offset:25600\n"
    "v_mfma_f32_16x16x32_bf16 v[168:171], a[152:155], v[208:211], v[168:171]\n"
    "v_mfma_f32_16x16x32_bf16 v[172:175], a[216:219], v[208:211], v[172:175]\n"
    "v_mfma_f32_16x16x32_bf16 v[176:179], a[24:27], v[212:215], v[176:179]\n"
    "global_load_lds_dwordx4 v121, s[64:65] offset:1024 sc1\n"
    "v_mfma_f32_16x16x32_bf16 v[180:183], a[88:91], v[212:215], v[180:183]\n"
    "s_lshr_b32 s58, s51, 1\n"
    "s_lshr_b32 s58, s52, s58\n"
    "s_and_b32 s58, s58, 1\n"
    "v_mfma_f32_16x16x32_bf16 v[184:187], a[152:155], v[212:215], v[184:187]\n"
    "s_lshl_b32 s58, s58, 13\n"
    "s_lshl_b32 s59, s51, 20\n"
    "s_add_u32 s58, s58, s59\n"
    "v_mfma_f32_16x16x32_bf16 v[188:191], a[216:219], v[212:215], v[188:191]\n"
    "s_add_u32 s58, s58, 0x7400000\n"
    "s_add_u32 s46, s40, s58\n"
    "s_addc_u32 s47, s41, 0\n"
    "s_waitcnt lgkmcnt(4)\n"
    "v_mfma_f32_16x16x32_bf16 v[160:163], a[28:31], v[216:219], v[160:163]\n"
    "ds_read_b128 v[208:211], v123 offset:10240\n"
    "v_mfma_f32_16x16x32_bf16 v[164:167], a[92:95], v[216:219], v[164:167]\n"
    "ds_read_b128 v[212:215], v123 offset:26624\n"
    "v_mfma_f32_16x16x32_bf16 v[168:171], a[156:159], v[216:219], v[168:171]\n"
    "v_mfma_f32_16x16x32_bf16 v[172:175], a[220:223], v[216:219], v[172:175]\n"
    "v_mfma_f32_16x16x32_bf16 v[176:179], a[28:31], v[220:223], v[176:179]\n"
    "global_load_lds_dwordx4 v121, s[64:65] offset:2048 sc1\n"
    "v_mfma_f32_16x16x32_bf16 v[180:183], a[92:95], v[220:223], v[180:183]\n"
    "s_mov_b64 s[44:45], s[66:67]\n"
    "s_add_u32 s66, s66, 0x80000\n"
    "s_addc_u32 s67, s67, 0\n"
    "v_mfma_f32_16x16x32_bf16 v[184:187], a[156:159], v[220:223], v[184:187]\n"
    "v_mfma_f32_16x16x32_bf16 v[188:191], a[220:223], v[220:223], v[188:191]\n"
    "s_waitcnt lgkmcnt(4)\n"
    "v_mfma_f32_16x16x32_bf16 v[160:163], a[32:35], v[192:195], v[160:163]\n"
    "ds_read_b128 v[216:219], v123 offset:11264\n"
    "v_mfma_f32_16x16x32_bf16 v[164:167], a[96:99], v[192:195], v[164:167]\n"
    "ds_read_b128 v[220:223], v123 offset:27648\n"
    "v_mfma_f32_16x16x32_bf16 v[168:171], a[160:163], v[192:195], v[168:171]\n"
    "v_mfma_f32_16x16x32_bf16 v[172:175], a[224:227], v[192:195], v[172:175]\n"
    "v_mfma_f32_16x16x32_bf16 v[176:179], a[32:35], v[196:199], v[176:179]\n"
    "global_load_lds_dwordx4 v121, s[64:65] offset:3072 sc1\n"
    "v_mfma_f32_16x16x32_bf16 v[180:183], a[96:99], v[196:199], v[180:183]\n"
    "v_mfma_f32_16x16x32_bf16 v[184:187], a[160:163], v[196:199], v[184:187]\n"
    "v_mfma_f32_16x16x32_bf16 v[188:191], a[224:227], v[196:199], v[188:191]\n"
    "s_waitcnt lgkmcnt(4)\n"
    "v_mfma_f32_16x16x32_bf16 v[160:163], a[36:39], v[200:203], v[160:163]\n"
    "ds_read_b128 v[192:195], v123 offset:12288\n"
    "v_mfma_f32_16x16x32_bf16 v[164:167], a[100:103], v[200:203], v[164:167]\n"
    "ds_read_b128 v[196:199], v123 offset:28672\n"
    "v_mfma_f32_16x16x32_bf16 v[168:171], a[164:167], v[200:203], v[168:171]\n"
    "s_add_u32 m0, s50, 69632\n"
    "s_add_u32 s64, s48, 4096\n"
    "s_addc_u32 s65, s49, 0\n"
    "v_mfma_f32_16x16x32_bf16 v[172:175], a[228:231], v[200:203], v[172:175]\n"
    "v_mfma_f32_16x16x32_bf16 v[176:179], a[36:39], v[204:207], v[176:179]\n"
    "global_load_lds_dwordx4 v121, s[64:65] offset:0 sc1\n"
    "v_mfma_f32_16x16x32_bf16 v[180:183], a[100:103], v[204:207], v[180:183]\n"
    "v_mfma_f32_16x16x32_bf16 v[184:187], a[164:167], v[204:207], v[184:187]\n"
    "v_mfma_f32_16x16x32_bf16 v[188:191], a[228:231], v[204:207], v[188:191]\n"
    "s_waitcnt lgkmcnt(4)\n"
    "v_mfma_f32_16x16x32_bf16 v[160:163], a[40:43], v[208:211], v[160:163]\n"
    "ds_read_b128 v[200:203], v123 offset:13312\n"
    "v_mfma_f32_16x16x32_bf16 v[164:167], a[104:107], v[208:211], v[164:167]\n"
    "ds_read_b128 v[204:207], v123 offset:29696\n"
    "v_mfma_f32_16x16x32_bf16 v[168:171], a[168:171], v[208:211], v[168:171]\n"
    "v_mfma_f32_16x16x32_bf16 v[172:175], a[232:235], v[208:211], v[172:175]\n"
    "v_mfma_f32_16x16x32_bf16 v[176:179], a[40:43], v[212:215], v[176:179]\n"
    "global_load_lds_dwordx4 v121, s[64:65] offset:1024 sc1\n"
    "v_mfma_f32_16x16x32_bf16 v[180:183], a[104:107], v[212:215], v[180:183]\n"
    "v_mfma_f32_16x16x32_bf16 v[184:187], a[168:171], v[212:215], v[184:187]\n"
    "v_mfma_f32_16x16x32_bf16 v[188:191], a[232:235], v[212:215], v[188:191]\n"
    "s_waitcnt lgkmcnt(4)\n"
    "v_mfma_f32_16x16x32_bf16 v[160:163], a[44:47], v[216:219], v[160:163]\n"
    "ds_read_b128 v[208:211], v123 offset:14336\n"
    "v_mfma_f32_16x16x32_bf16 v[164:167], a[108:111], v[216:219], v[164:167]\n"
    "ds_read_b128 v[212:215], v123 offset:30720\n"
    "v_mfma_f32_16x16x32_bf16 v[168:171], a[172:175], v[216:219], v[168:171]\n"
    "v_mfma_f32_16x16x32_bf16 v[172:175], a[236:239], v[216:219], v[172:175]\n"
    "v_mfma_f32_16x16x32_bf16 v[176:179], a[44:47], v[220:223], v[176:179]\n"
    "global_load_lds_dwordx4 v121, s[64:65] offset:2048 sc1\n"
    "v_mfma_f32_16x16x32_bf16 v[180:183], a[108:111], v[220:223], v[180:183]\n"
    "v_mfma_f32_16x16x32_bf16 v[184:187], a[172:175], v[220:223], v[184:187]\n"
    "v_mfma_f32_16x16x32_bf16 v[188:191], a[236:239], v[220:223], v[188:191]\n"
    "s_waitcnt lgkmcnt(4)\n"
    "v_mfma_f32_16x16x32_bf16 v[160:163], a[48:51], v[192:195], v[160:163]\n"
    "ds_read_b128 v[216:219], v123 offset:15360\n"
    "v_mfma_f32_16x16x32_bf16 v[164:167], a[112:115], v[192:195], v[164:167]\n"
    "ds_read_b128 v[220:223], v123 offset:31744\n"
    "v_mfma_f32_16x16x32_bf16 v[168:171], a[176:179], v[192:195], v[168:171]\n"
    "v_mfma_f32_16x16x32_bf16 v[172:175], a[240:243], v[192:195], v[172:175]\n"
    "v_mfma_f32_16x16x32_bf16 v[176:179], a[48:51], v[196:199], v[176:179]\n"
    "global_load_lds_dwordx4 v121, s[64:65] offset:3072 sc1\n"
    "v_mfma_f32_16x16x32_bf16 v[180:183], a[112:115], v[196:199], v[180:183]\n"
    "v_mfma_f32_16x16x32_bf16 v[184:187], a[176:179], v[196:199], v[184:187]\n"
    "v_mfma_f32_16x16x32_bf16 v[188:191], a[240:243], v[196:199], v[188:191]\n"
    "s_waitcnt lgkmcnt(4)\n"
    "v_mfma_f32_16x16x32_bf16 v[160:163], a[52:55], v[200:203], v[160:163]\n"
    "s_waitcnt vmcnt(8) lgkmcnt(0)\n"
    "s_barrier\n"
    "v_mfma_f32_16x16x32_bf16 v[164:167], a[116:119], v[200:203], v[164:167]\n"
    "ds_read_b128 v[192:195], v122 offset:0\n"
    "v_mfma_f32_16x16x32_bf16 v[168:171], a[180:183], v[200:203], v[168:171]\n"
    "ds_read_b128 v[196:199], v122 offset:16384\n"
    "s_add_u32 m0, s50, 73728\n"
    "s_add_u32 s64, s48, 8192\n"
    "s_addc_u32 s65, s49, 0\n"
    "v_mfma_f32_16x16x32_bf16 v[172:175], a[244:247], v[200:203], v[172:175]\n"
    "v_mfma_f32_16x16x32_bf16 v[176:179], a[52:55], v[204:207], v[176:179]\n"
    "global_load_lds_dwordx4 v121, s[64:65] offset:0 sc1\n"
    "v_mfma_f32_16x16x32_bf16 v[180:183], a[116:119], v[204:207], v[180:183]\n"
    "s_cmp_lg_u32 s56, 0\n"
    "s_cbranch_scc1 G_f1%=\n"
    "s_or_b32 s58, s42, s51\n"
    "s_mov_b64 exec, 1\n"
    "v_mov_b32 v116, s58\n"
    "global_store_dwordx2 v118, v[116:117], s[54:55] sc1\n"
    "s_mov_b64 exec, -1\n"
    "G_f1%=:\n"
    "v_mfma_f32_16x16x32_bf16 v[184:187], a[180:183], v[204:207], v[184:187]\n"
    "v_mfma_f32_16x16x32_bf16 v[188:191], a[244:247], v[204:207], v[188:191]\n"
    "s_waitcnt lgkmcnt(4)\n"
    "v_mfma_f32_16x16x32_bf16 v[160:163], a[56:59], v[208:211], v[160:163]\n"
    "ds_read_b128 v[200:203], v122 offset:1024\n"
    "v_mfma_f32_16x16x32_bf16 v[164:167], a[120:123], v[208:211], v[164:167]\n"
    "ds_read_b128 v[204:207], v122 offset:17408\n"
    "v_mfma_f32_16x16x32_bf16 v[168:171], a[184:187], v[208:211], v[168:171]\n"
    "v_mfma_f32_16x16x32_bf16 v[172:175], a[248:251], v[208:211], v[172:175]\n"
    "v_mfma_f32_16x16x32_bf16 v[176:179], a[56:59], v[212:215], v[176:179]\n"
    "global_load_lds_dwordx4 v121, s[64:65] offset:1024 sc1\n"
    "v_mfma_f32_16x16x32_bf16 v[180:183], a[120:123], v[212:215], v[180:183]\n"
    "v_mfma_f32_16x16x32_bf16 v[184:187], a[184:187], v[212:215], v[184:187]\n"
    "v_mfma_f32_16x16x32_bf16 v[188:191], a[248:251], v[212:215], v[188:191]\n"
    "s_add_u32 s51, s51, 1\n"
    "s_waitcnt lgkmcnt(4)\n"
    "v_mfma_f32_16x16x32_bf16 v[160:163], a[60:63], v[216:219], v[160:163]\n"
    "ds_read_b128 v[208:211], v122 offset:2048\n"
    "v_mfma_f32_16x16x32_bf16 v[164:167], a[124:127], v[216:219], v[164:167]\n"
    "ds_read_b128 v[212:215], v122 offset:18432\n"
    "v_mfma_f32_16x16x32_bf16 v[168:171], a[188:191], v[216:219], v[168:171]\n"
    "v_mfma_f32_16x16x32_bf16 v[172:175], a[252:255], v[216:219], v[172:175]\n"
    "v_mfma_f32_16x16x32_bf16 v[176:179], a[60:63], v[220:223], v[176:179]\n"
    "global_load_lds_dwordx4 v121, s[64:65] offset:2048 sc1\n"
    "v_mfma_f32_16x16x32_bf16 v[180:183], a[124:127], v[220:223], v[180:183]\n"
    "v_mfma_f32_16x16x32_bf16 v[184:187], a[188:191], v[220:223], v[184:187]\n"
    "v_mfma_f32_16x16x32_bf16 v[188:191], a[252:255], v[220:223], v[188:191]\n"
    "s_add_u32 s58, s51, -3\n"
    "s_cmp_gt_u32 s58, 28\n"
    "s_waitcnt lgkmcnt(4)\n"
    "s_cbranch_scc1 G_t1%=\n"
    "G_even%=:\n"
    "v_mfma_f32_16x16x32_bf16 v[128:131], a[0:3], v[192:195], 0\n"
    "ds_read_b128 v[216:219], v122 offset:3072\n"
    "v_mfma_f32_16x16x32_bf16 v[132:135], a[64:67], v[192:195], 0\n"
    "ds_read_b128 v[220:223], v122 offset:19456\n"
    "v_mfma_f32_16x16x32_bf16 v[136:139], a[128:131], v[192:195], 0\n"
    "v_mfma_f32_16x16x32_bf16 v[140:143], a[192:195], v[192:195], 0\n"
    "v_cvt_pk_bf16_f32 v224, v160, v161\n"
    "v_cvt_pk_bf16_f32 v225, v162, v163\n"
    "v_mfma_f32_16x16x32_bf16 v[144:147], a[0:3], v[196:199], 0\n"
    "global_load_lds_dwordx4 v121, s[64:65] offset:3072 sc1\n"
    "v_mfma_f32_16x16x32_bf16 v[148:151], a[64:67], v[196:199], 0\n"
    "v_cvt_pk_bf16_f32 v226, v164, v165\n"
    "v_cvt_pk_bf16_f32 v227, v166, v167\n"
    "v_mfma_f32_16x16x32_bf16 v[152:155], a[128:131], v[196:199], 0\n"
    "global_store_dwordx4 v119, v[124:127], s[46:47] sc1\n"
    "v_mfma_f32_16x16x32_bf16 v[156:159], a[192:195], v[196:199], 0\n"
    "v_cvt_pk_bf16_f32 v228, v176, v177\n"
    "v_cvt_pk_bf16_f32 v229, v178, v179\n"
    "s_waitcnt lgkmcnt(4)\n"
    "v_mfma_f32_16x16x32_bf16 v[128:131], a[4:7], v[200:203], v[128:131]\n"
    "ds_read_b128 v[192:195], v122 offset:4096\n"
    "v_mfma_f32_16x16x32_bf16 v[132:135], a[68:71], v[200:203], v[132:135]\n"
    "ds_read_b128 v[196:199], v122 offset:20480\n"
    "v_mfma_f32_16x16x32_bf16 v[136:139], a[132:135], v[200:203], v[136:139]\n"
    "s_add_u32 m0, s50, 77824\n"
    "s_add_u32 s64, s48, 12288\n"
    "s_addc_u32 s65, s49, 0\n"
    "v_cvt_pk_bf16_f32 v230, v180, v181\n"
    "v_cvt_pk_bf16_f32 v231, v182, v183\n"
    "v_mfma_f32_16x16x32_bf16 v[140:143], a[196:199], v[200:203], v[140:143]\n"
    "s_add_u32 s48, s48, 0x10000\n"
    "s_addc_u32 s49, s49, 0\n"
    "v_cvt_pk_bf16_f32 v232, v168, v169\n"
    "v_cvt_pk_bf16_f32 v233, v170, v171\n"
    "v_mfma_f32_16x16x32_bf16 v[144:147], a[4:7], v[204:207], v[144:147]\n"
    "global_load_lds_dwordx4 v121, s[64:65] offset:0 sc1\n"
    "v_mfma_f32_16x16x32_bf16 v[148:151], a[68:71], v[204:207], v[148:151]\n"
    "v_cvt_pk_bf16_f32 v234, v172, v173\n"
    "v_cvt_pk_bf16_f32 v235, v174, v175\n"
    "v_mfma_f32_16x16x32_bf16 v[152:155], a[132:135], v[204:207], v[152:155]\n"
    "global_store_dwordx4 v120, v[124:127], s[46:47] sc1\n"
    "v_mfma_f32_16x16x32_bf16 v[156:159], a[196:199], v[204:207], v[156:159]\n"
    "v_cvt_pk_bf16_f32 v236, v184, v185\n"
    "v_cvt_pk_bf16_f32 v237, v186, v187\n"
    "s_waitcnt lgkmcnt(4)\n"
    "v_mfma_f32_16x16x32_bf16 v[128:131], a[8:11], v[208:211], v[128:131]\n"
    "ds_read_b128 v[200:203], v122 offset:5120\n"
    "v_mfma_f32_16x16x32_bf16 v[132:135], a[72:75], v[208:211], v[132:135]\n"
    "ds_read_b128 v[204:207], v122 offset:21504\n"
    "v_mfma_f32_16x16x32_bf16 v[136:139], a[136:139], v[208:211], v[136:139]\n"
    "v_cvt_pk_bf16_f32 v238, v188, v189\n"
    "v_cvt_pk_bf16_f32 v239, v190, v191\n"
    "v_mfma_f32_16x16x32_bf16 v[140:143], a[200:203], v[208:211], v[140:143]\n"
    "s_not_b64 vcc, vcc\n"
    "v_cndmask_b32_dpp v240, v232, v224, vcc quad_perm:[0,1,0,1] row_mask:0xf bank_mask:0xf\n"
    "v_cndmask_b32_dpp v241, v233, v225, vcc quad_perm:[0,1,0,1] row_mask:0xf bank_mask:0xf\n"
    "v_mfma_f32_16x16x32_bf16 v[144:147], a[8:11], v[212:215], v[144:147]\n"
    "global_load_lds_dwordx4 v121, s[64:65] offset:1024 sc1\n"
    "v_mfma_f32_16x16x32_bf16 v[148:151], a[72:75], v[212:215], v[148:151]\n"
    "v_cndmask_b32_dpp v242, v234, v226, vcc quad_perm:[0,1,0,1] row_mask:0xf bank_mask:0xf\n"
    "v_cndmask_b32_dpp v243, v235, v227, vcc quad_perm:[0,1,0,1] row_mask:0xf bank_mask:0xf\n"
    "v_mfma_f32_16x16x32_bf16 v[152:155], a[136:139], v[212:215], v[152:155]\n"
    "global_store_dwordx4 v121, v[240:243], s[44:45] offset:0 sc1\n"
    "v_mfma_f32_16x16x32_bf16 v[156:159], a[200:203], v[212:215], v[156:159]\n"
    "v_cndmask_b32_dpp v248, v236, v228, vcc quad_perm:[0,1,0,1] row_mask:0xf bank_mask:0xf\n"
    "v_cndmask_b32_dpp v249, v237, v229, vcc quad_perm:[0,1,0,1] row_mask:0xf bank_mask:0xf\n"
    "s_waitcnt lgkmcnt(4)\n"
    "v_mfma_f32_16x16x32_bf16 v[128:131], a[12:15], v[216:219], v[128:131]\n"
    "ds_read_b128 v[208:211], v122 offset:6144\n"
    "v_mfma_f32_16x16x32_bf16 v[132:135], a[76:79], v[216:219], v[132:135]\n"
    "ds_read_b128 v[212:215], v122 offset:22528\n"
    "v_mfma_f32_16x16x32_bf16 v[136:139], a[140:143], v[216:219], v[136:139]\n"
    "v_cndmask_b32_dpp v250, v238, v230, vcc quad_perm:[0,1,0,1] row_mask:0xf bank_mask:0xf\n"
    "v_cndmask_b32_dpp v251, v239, v231, vcc quad_perm:[0,1,0,1] row_mask:0xf bank_mask:0xf\n"
    "v_mfma_f32_16x16x32_bf16 v[140:143], a[204:207], v[216:219], v[140:143]\n"
    "s_not_b64 vcc, vcc\n"
    "v_cndmask_b32_dpp v244, v224, v232, vcc quad_perm:[2,3,2,3] row_mask:0xf bank_mask:0xf\n"
    "v_cndmask_b32_dpp v245, v225, v233, vcc quad_perm:[2,3,2,3] row_mask:0xf bank_mask:0xf\n"
    "v_mfma_f32_16x16x32_bf16 v[144:147], a[12:15], v[220:223], v[144:147]\n"
    "global_load_lds_dwordx4 v121, s[64:65] offset:2048 sc1\n"
    "v_mfma_f32_16x16x32_bf16 v[148:151], a[76:79], v[220:223], v[148:151]\n"
    "v_cndmask_b32_dpp v246, v226, v234, vcc quad_perm:[2,3,2,3] row_mask:0xf bank_mask:0xf\n"
    "v_cndmask_b32_dpp v247, v227, v235, vcc quad_perm:[2,3,2,3] row_mask:0xf bank_mask:0xf\n"
    "v_mfma_f32_16x16x32_bf16 v[152:155], a[140:143], v[220:223], v[152:155]\n"
    "global_store_dwordx4 v121, v[248:251], s[44:45] offset:2048 sc1\n"
    "v_mfma_f32_16x16x32_bf16 v[156:159], a[204:207], v[220:223], v[156:159]\n"
    "v_cndmask_b32_dpp v252, v228, v236, vcc quad_perm:[2,3,2,3] row_mask:0xf bank_mask:0xf\n"
    "v_cndmask_b32_dpp v253, v229, v237, vcc quad_perm:[2,3,2,3] row_mask:0xf bank_mask:0xf\n"
    "s_waitcnt lgkmcnt(4)\n"
    "v_mfma_f32_16x16x32_bf16 v[128:131], a[16:19], v[192:195], v[128:131]\n"
    "ds_read_b128 v[216:219], v122 offset:7168\n"
    "v_mfma_f32_16x16x32_bf16 v[132:135], a[80:83], v[192:195], v[132:135]\n"
    "ds_read_b128 v[220:223], v122 offset:23552\n"
    "v_mfma_f32_16x16x32_bf16 v[136:139], a[144:147], v[192:195], v[136:139]\n"
    "global_store_dwordx4 v121, v[244:247], s[44:45] offset:1024 sc1\n"
    "v_cndmask_b32_dpp v254, v230, v238, vcc quad_perm:[2,3,2,3] row_mask:0xf bank_mask:0xf\n"
    "v_cndmask_b32_dpp v255, v231, v239, vcc quad_perm:[2,3,2,3] row_mask:0xf bank_mask:0xf\n"
    "v_mfma_f32_16x16x32_bf16 v[140:143], a[208:211], v[192:195], v[140:143]\n"
    "v_mfma_f32_16x16x32_bf16 v[144:147], a[16:19], v[196:199], v[144:147]\n"
    "global_load_lds_dwordx4 v121, s[64:65] offset:3072 sc1\n"
    "v_mfma_f32_16x16x32_bf16 v[148:151], a[80:83], v[196:199], v[148:151]\n"
    "v_mfma_f32_16x16x32_bf16 v[152:155], a[144:147], v[196:199], v[152:155]\n"
    "global_store_dwordx4 v121, v[252:255], s[44:45] offset:3072 sc1\n"
    "v_mfma_f32_16x16x32_bf16 v[156:159], a[208:211], v[196:199], v[156:159]\n"
    "s_waitcnt lgkmcnt(4)\n"
    "v_mfma_f32_16x16x32_bf16 v[128:131], a[20:23], v[200:203], v[128:131]\n"
    "s_waitcnt vmcnt(14) lgkmcnt(0)\n"
    "s_barrier\n"
    "v_mfma_f32_16x16x32_bf16 v[132:135], a[84:87], v[200:203], v[132:135]\n"
    "ds_read_b128 v[192:195], v122 offset:8192\n"
    "v_mfma_f32_16x16x32_bf16 v[136:139], a[148:151], v[200:203], v[136:139]\n"
    "ds_read_b128 v[196:199], v122 offset:24576\n"
    "s_add_u32 m0, s50, 0\n"
    "s_add_u32 s64, s48, 0\n"
    "s_addc_u32 s65, s49, 0\n"
    "v_mfma_f32_16x16x32_bf16 v[140:143], a[212:215], v[200:203], v[140:143]\n"
    "v_mfma_f32_16x16x32_bf16 v[144:147], a[20:23], v[204:207], v[144:147]\n"
    "global_load_lds_dwordx4 v121, s[64:65] offset:0 sc1\n"
    "v_mfma_f32_16x16x32_bf16 v[148:151], a[84:87], v[204:207], v[148:151]\n"
    "v_mfma_f32_16x16x32_bf16 v[152:155], a[148:151], v[204:207], v[152:155]\n"
    "v_mfma_f32_16x16x32_bf16 v[156:159], a[212:215], v[204:207], v[156:159]\n"
    "s_waitcnt lgkmcnt(4)\n"
    "v_mfma_f32_16x16x32_bf16 v[128:131], a[24:27], v[208:211], v[128:131]\n"
    "ds_read_b128 v[200:203], v122 offset:9216\n"
    "v_mfma_f32_16x16x32_bf16 v[132:135], a[88:91], v[208:211], v[132:135]\n"
    "ds_read_b128 v[204:207], v122 offset:25600\n"
    "v_mfma_f32_16x16x32_bf16 v[136:139], a[152:155], v[208:211], v[136:139]\n"
    "v_mfma_f32_16x16x32_bf16 v[140:143], a[216:219], v[208:211], v[140:143]\n"
    "v_mfma_f32_16x16x32_bf16 v[144:147], a[24:27], v[212:215], v[144:147]\n"
    "global_load_lds_dwordx4 v121, s[64:65] offset:1024 sc1\n"
    "v_mfma_f32_16x16x32_bf16 v[148:151], a[88:91], v[212:215], v[148:151]\n"
    "s_lshr_b32 s58, s51, 1\n"
    "s_lshr_b32 s58, s52, s58\n"
    "s_and_b32 s58, s58, 1\n"
    "v_mfma_f32_16x16x32_bf16 v[152:155], a[152:155], v[212:215], v[152:155]\n"
    "s_lshl_b32 s58, s58, 13\n"
    "s_lshl_b32 s59, s51, 20\n"
    "s_add_u32 s58, s58, s59\n"
    "v_mfma_f32_16x16x32_bf16 v[156:159], a[216:219], v[212:215], v[156:159]\n"
    "s_add_u32 s58, s58, 0x7400000\n"
    "s_add_u32 s46, s40, s58\n"
    "s_addc_u32 s47, s41, 0\n"
    "s_waitcnt lgkmcnt(4)\n"
    "v_mfma_f32_16x16x32_bf16 v[128:131], a[28:31], v[216:219], v[128:131]\n"
    "ds_read_b128 v[208:211], v122 offset:10240\n"
    "v_mfma_f32_16x16x32_bf16 v[132:135], a[92:95], v[216:219], v[132:135]\n"
    "ds_read_b128 v[212:215], v122 offset:26624\n"
    "v_mfma_f32_16x16x32_bf16 v[136:139], a[156:159], v[216:219], v[136:139]\n"
    "v_mfma_f32_16x16x32_bf16 v[140:143], a[220:223], v[216:219], v[140:143]\n"
    "v_mfma_f32_16x16x32_bf16 v[144:147], a[28:31], v[220:223], v[144:147]\n"
    "global_load_lds_dwordx4 v121, s[64:65] offset:2048 sc1\n"
    "v_mfma_f32_16x16x32_bf16 v[148:151], a[92:95], v[220:223], v[148:151]\n"
    "s_mov_b64 s[44:45], s[66:67]\n"
    "s_add_u32 s66, s66, 0x80000\n"
    "s_addc_u32 s67, s67, 0\n"
    "v_mfma_f32_16x16x32_bf16 v[152:155], a[156:159], v[220:223], v[152:155]\n"
    "v_mfma_f32_16x16x32_bf16 v[156:159], a[220:223], v[220:223], v[156:159]\n"
    "s_waitcnt lgkmcnt(4)\n"
    "v_mfma_f32_16x16x32_bf16 v[128:131], a[32:35], v[192:195], v[128:131]\n"
    "ds_read_b128 v[216:219], v122 offset:11264\n"
    "v_mfma_f32_16x16x32_bf16 v[132:135], a[96:99], v[192:195], v[132:135]\n"
    "ds_read_b128 v[220:223], v122 offset:27648\n"
    "v_mfma_f32_16x16x32_bf16 v[136:139], a[160:163], v[192:195], v[136:139]\n"
    "v_mfma_f32_16x16x32_bf16 v[140:143], a[224:227], v[192:195], v[140:143]\n"
    "v_mfma_f32_16x16x32_bf16 v[144:147], a[32:35], v[196:199], v[144:147]\n"
    "global_load_lds_dwordx4 v121, s[64:65] offset:3072 sc1\n"
    "v_mfma_f32_16x16x32_bf16 v[148:151], a[96:99], v[196:199], v[148:151]\n"
    "v_mfma_f32_16x16x32_bf16 v[152:155], a[160:163], v[196:199], v[152:155]\n"
    "v_mfma_f32_16x16x32_bf16 v[156:159], a[224:227], v[196:199], v[156:159]\n"
    "s_waitcnt lgkmcnt(4)\n"
    "v_mfma_f32_16x16x32_bf16 v[128:131], a[36:39], v[200:203], v[128:131]\n"
    "ds_read_b128 v[192:195], v122 offset:12288\n"
    "v_mfma_f32_16x16x32_bf16 v[132:135], a[100:103], v[200:203], v[132:135]\n"
    "ds_read_b128 v[196:199], v122 offset:28672\n"
    "v_mfma_f32_16x16x32_bf16 v[136:139], a[164:167], v[200:203], v[136:139]\n"
    "s_add_u32 m0, s50, 4096\n"
    "s_add_u32 s64, s48, 4096\n"
    "s_addc_u32 s65, s49, 0\n"
    "v_mfma_f32_16x16x32_bf16 v[140:143], a[228:231], v[200:203], v[140:143]\n"
    "v_mfma_f32_16x16x32_bf16 v[144:147], a[36:39], v[204:207], v[144:147]\n"
    "global_load_lds_dwordx4 v121, s[64:65] offset:0 sc1\n"
    "v_mfma_f32_16x16x32_bf16 v[148:151], a[100:103], v[204:207], v[148:151]\n"
    "v_mfma_f32_16x16x32_bf16 v[152:155], a[164:167], v[204:207], v[152:155]\n"
    "v_mfma_f32_16x16x32_bf16 v[156:159], a[228:231], v[204:207], v[156:159]\n"
    "s_waitcnt lgkmcnt(4)\n"
    "v_mfma_f32_16x16x32_bf16 v[128:131], a[40:43], v[208:211], v[128:131]\n"
    "ds_read_b128 v[200:203], v122 offset:13312\n"
    "v_mfma_f32_16x16x32_bf16 v[132:135], a[104:107], v[208:211], v[132:135]\n"
    "ds_read_b128 v[204:207], v122 offset:29696\n"
    "v_mfma_f32_16x16x32_bf16 v[136:139], a[168:171], v[208:211], v[136:139]\n"
    "v_mfma_f32_16x16x32_bf16 v[140:143], a[232:235], v[208:211], v[140:143]\n"
    "v_mfma_f32_16x16x32_bf16 v[144:147], a[40:43], v[212:215], v[144:147]\n"
    "global_load_lds_dwordx4 v121, s[64:65] offset:1024 sc1\n"
    "v_mfma_f32_16x16x32_bf16 v[148:151], a[104:107], v[212:215], v[148:151]\n"
    "v_mfma_f32_16x16x32_bf16 v[152:155], a[168:171], v[212:215], v[152:155]\n"
    "v_mfma_f32_16x16x32_bf16 v[156:159], a[232:235], v[212:215], v[156:159]\n"
    "s_waitcnt lgkmcnt(4)\n"
    "v_mfma_f32_16x16x32_bf16 v[128:131], a[44:47], v[216:219], v[128:131]\n"
    "ds_read_b128 v[208:211], v122 offset:14336\n"
    "v_mfma_f32_16x16x32_bf16 v[132:135], a[108:111], v[216:219], v[132:135]\n"
    "ds_read_b128 v[212:215], v122 offset:30720\n"
    "v_mfma_f32_16x16x32_bf16 v[136:139], a[172:175], v[216:219], v[136:139]\n"
    "v_mfma_f32_16x16x32_bf16 v[140:143], a[236:239], v[216:219], v[140:143]\n"
    "v_mfma_f32_16x16x32_bf16 v[144:147], a[44:47], v[220:223], v[144:147]\n"
    "global_load_lds_dwordx4 v121, s[64:65] offset:2048 sc1\n"
    "v_mfma_f32_16x16x32_bf16 v[148:151], a[108:111], v[220:223], v[148:151]\n"
    "v_mfma_f32_16x16x32_bf16 v[152:155], a[172:175], v[220:223], v[152:155]\n"
    "v_mfma_f32_16x16x32_bf16 v[156:159], a[236:239], v[220:223], v[156:159]\n"
    "s_waitcnt lgkmcnt(4)\n"
    "v_mfma_f32_16x16x32_bf16 v[128:131], a[48:51], v[192:195], v[128:131]\n"
    "ds_read_b128 v[216:219], v122 offset:15360\n"
    "v_mfma_f32_16x16x32_bf16 v[132:135], a[112:115], v[192:195], v[132:135]\n"
    "ds_read_b128 v[220:223], v122 offset:31744\n"
    "v_mfma_f32_16x16x32_bf16 v[136:139], a[176:179], v[192:195], v[136:139]\n"
    "v_mfma_f32_16x16x32_bf16 v[140:143], a[240:243], v[192:195], v[140:143]\n"
    "v_mfma_f32_16x16x32_bf16 v[144:147], a[48:51], v[196:199], v[144:147]\n"
    "global_load_lds_dwordx4 v121, s[64:65] offset:3072 sc1\n"
    "v_mfma_f32_16x16x32_bf16 v[148:151], a[112:115], v[196:199], v[148:151]\n"
    "v_mfma_f32_16x16x32_bf16 v[152:155], a[176:179], v[196:199], v[152:155]\n"
    "v_mfma_f32_16x16x32_bf16 v[156:159], a[240:243], v[196:199], v[156:159]\n"
    "s_waitcnt lgkmcnt(4)\n"
    "v_mfma_f32_16x16x32_bf16 v[128:131], a[52:55], v[200:203], v[128:131]\n"
    "s_waitcnt vmcnt(8) lgkmcnt(0)\n"
    "s_barrier\n"
    "v_mfma_f32_16x16x32_bf16 v[132:135], a[116:119], v[200:203], v[132:135]\n"
    "ds_read_b128 v[192:195], v123 offset:0\n"
    "v_mfma_f32_16x16x32_bf16 v[136:139], a[180:183], v[200:203], v[136:139]\n"
    "ds_read_b128 v[196:199], v123 offset:16384\n"
    "s_add_u32 m0, s50, 8192\n"
    "s_add_u32 s64, s48, 8192\n"
    "s_addc_u32 s65, s49, 0\n"
    "v_mfma_f32_16x16x32_bf16 v[140:143], a[244:247], v[200:203], v[140:143]\n"
    "v_mfma_f32_16x16x32_bf16 v[144:147], a[52:55], v[204:207], v[144:147]\n"
    "global_load_lds_dwordx4 v121, s[64:65] offset:0 sc1\n"
    "v_mfma_f32_16x16x32_bf16 v[148:151], a[116:119], v[204:207], v[148:151]\n"
    "s_cmp_lg_u32 s56, 0\n"
    "s_cbranch_scc1 G_f2%=\n"
    "s_or_b32 s58, s42, s51\n"
    "s_mov_b64 exec, 1\n"
    "v_mov_b32 v116, s58\n"
    "global_store_dwordx2 v118, v[116:117], s[54:55] sc1\n"
    "s_mov_b64 exec, -1\n"
    "G_f2%=:\n"
    "v_mfma_f32_16x16x32_bf16 v[152:155], a[180:183], v[204:207], v[152:155]\n"
    "v_mfma_f32_16x16x32_bf16 v[156:159], a[244:247], v[204:207], v[156:159]\n"
    "s_waitcnt lgkmcnt(4)\n"
    "v_mfma_f32_16x16x32_bf16 v[128:131], a[56:59], v[208:211], v[128:131]\n"
    "ds_read_b128 v[200:203], v123 offset:1024\n"
    "v_mfma_f32_16x16x32_bf16 v[132:135], a[120:123], v[208:211], v[132:135]\n"
    "ds_read_b128 v[204:207], v123 offset:17408\n"
    "v_mfma_f32_16x16x32_bf16 v[136:139], a[184:187], v[208:211], v[136:139]\n"
    "v_mfma_f32_16x16x32_bf16 v[140:143], a[248:251], v[208:211], v[140:143]\n"
    "v_mfma_f32_16x16x32_bf16 v[144:147], a[56:59], v[212:215], v[144:147]\n"
    "global_load_lds_dwordx4 v121, s[64:65] offset:1024 sc1\n"
    "v_mfma_f32_16x16x32_bf16 v[148:151], a[120:123], v[212:215], v[148:151]\n"
    "v_mfma_f32_16x16x32_bf16 v[152:155], a[184:187], v[212:215], v[152:155]\n"
    "v_mfma_f32_16x16x32_bf16 v[156:159], a[248:251], v[212:215], v[156:159]\n"
    "s_add_u32 s51, s51, 1\n"
    "s_waitcnt lgkmcnt(4)\n"
    "v_mfma_f32_16x16x32_bf16 v[128:131], a[60:63], v[216:219], v[128:131]\n"
    "ds_read_b128 v[208:211], v123 offset:2048\n"
    "v_mfma_f32_16x16x32_bf16 v[132:135], a[124:127], v[216:219], v[132:135]\n"
    "ds_read_b128 v[212:215], v123 offset:18432\n"
    "v_mfma_f32_16x16x32_bf16 v[136:139], a[188:191], v[216:219], v[136:139]\n"
    "v_mfma_f32_16x16x32_bf16 v[140:143], a[252:255], v[216:219], v[140:143]\n"
    "v_mfma_f32_16x16x32_bf16 v[144:147], a[60:63], v[220:223], v[144:147]\n"
    "global_load_lds_dwordx4 v121, s[64:65] offset:2048 sc1\n"
    "v_mfma_f32_16x16x32_bf16 v[148:151], a[124:127], v[220:223], v[148:151]\n"
    "v_mfma_f32_16x16x32_bf16 v[152:155], a[188:191], v[220:223], v[152:155]\n"
    "v_mfma_f32_16x16x32_bf16 v[156:159], a[252:255], v[220:223], v[156:159]\n"
    "s_add_u32 s58, s51, -3\n"
    "s_cmp_gt_u32 s58, 28\n"
    "s_waitcnt lgkmcnt(4)\n"
    "s_cbranch_scc0 G_loop%=\n"
    "s_branch G_pc%=\n"
    "G_pw%=:\n"
    "s_add_u32 s58, s40, 0x312000\n"
    "s_addc_u32 s59, s41, 0\n"
    "global_load_dwordx2 v[114:115], v112, s[58:59] sc1\n"
    "s_waitcnt vmcnt(0)\n"
    "G_pc%=:\n"
    "v_xor_b32 v114, s42, v114\n"
    "v_xor_b32 v115, s43, v115\n"
    "v_cmp_eq_u64 s[52:53], 2, v[114:115]\n"
    "v_cmp_eq_u64 s[58:59], 1, v[114:115]\n"
    "s_nop 0\n"
    "s_or_b64 s[58:59], s[58:59], s[52:53]\n"
    "s_cmp_eq_u64 s[58:59], -1\n"
    "s_cbranch_scc1 G_pok%=\n"
    "s_sleep 4\n"
    "s_add_u32 s57, s57, 1\n"
    "s_cmp_lt_u32 s57, 0x40000\n"
    "s_cbranch_scc1 G_pw%=\n"
    "G_pok%=:\n"
    "s_and_b32 s58, s52, 1\n"
    "s_lshl_b32 s58, s58, 13\n"
    "s_add_u32 s58, s58, 0x7400000\n"
    "s_add_u32 s46, s40, s58\n"
    "s_addc_u32 s47, s41, 0\n"
    "global_store_dwordx4 v119, v[124:127], s[46:47] sc1\n"
    "global_store_dwordx4 v120, v[124:127], s[46:47] sc1\n"
    "s_nop 7\n"
    "v_cvt_pk_bf16_f32 v224, v128, v129\n"
    "v_cvt_pk_bf16_f32 v225, v130, v131\n"
    "v_cvt_pk_bf16_f32 v226, v132, v133\n"
    "v_cvt_pk_bf16_f32 v227, v134, v135\n"
    "v_cvt_pk_bf16_f32 v228, v144, v145\n"
    "v_cvt_pk_bf16_f32 v229, v146, v147\n"
    "v_cvt_pk_bf16_f32 v230, v148, v149\n"
    "v_cvt_pk_bf16_f32 v231, v150, v151\n"
    "v_cvt_pk_bf16_f32 v232, v136, v137\n"
    "v_cvt_pk_bf16_f32 v233, v138, v139\n"
    "v_cvt_pk_bf16_f32 v234, v140, v141\n"
    "v_cvt_pk_bf16_f32 v235, v142, v143\n"
    "v_cvt_pk_bf16_f32 v236, v152, v153\n"
    "v_cvt_pk_bf16_f32 v237, v154, v155\n"
    "v_cvt_pk_bf16_f32 v238, v156, v157\n"
    "v_cvt_pk_bf16_f32 v239, v158, v159\n"
    "s_not_b64 vcc, vcc\n"
    "v_cndmask_b32_dpp v240, v232, v224, vcc quad_perm:[0,1,0,1] row_mask:0xf bank_mask:0xf\n"
    "v_cndmask_b32_dpp v241, v233, v225, vcc quad_perm:[0,1,0,1] row_mask:0xf bank_mask:0xf\n"
    "v_cndmask_b32_dpp v242, v234, v226, vcc quad_perm:[0,1,0,1] row_mask:0xf bank_mask:0xf\n"
    "v_cndmask_b32_dpp v243, v235, v227, vcc quad_perm:[0,1,0,1] row_mask:0xf bank_mask:0xf\n"
    "v_cndmask_b32_dpp v248, v236, v228, vcc quad_perm:[0,1,0,1] row_mask:0xf bank_mask:0xf\n"
    "v_cndmask_b32_dpp v249, v237, v229, vcc quad_perm:[0,1,0,1] row_mask:0xf bank_mask:0xf\n"
    "v_cndmask_b32_dpp v250, v238, v230, vcc quad_perm:[0,1,0,1] row_mask:0xf bank_mask:0xf\n"
    "v_cndmask_b32_dpp v251, v239, v231, vcc quad_perm:[0,1,0,1] row_mask:0xf bank_mask:0xf\n"
    "s_not_b64 vcc, vcc\n"
    "v_cndmask_b32_dpp v244, v224, v232, vcc quad_perm:[2,3,2,3] row_mask:0xf bank_mask:0xf\n"
    "v_cndmask_b32_dpp v245, v225, v233, vcc quad_perm:[2,3,2,3] row_mask:0xf bank_mask:0xf\n"
    "v_cndmask_b32_dpp v246, v226, v234, vcc quad_perm:[2,3,2,3] row_mask:0xf bank_mask:0xf\n"
    "v_cndmask_b32_dpp v247, v227, v235, vcc quad_perm:[2,3,2,3] row_mask:0xf bank_mask:0xf\n"
    "v_cndmask_b32_dpp v252, v228, v236, vcc quad_perm:[2,3,2,3] row_mask:0xf bank_mask:0xf\n"
    "v_cndmask_b32_dpp v253, v229, v237, vcc quad_perm:[2,3,2,3] row_mask:0xf bank_mask:0xf\n"
    "v_cndmask_b32_dpp v254, v230, v238, vcc quad_perm:[2,3,2,3] row_mask:0xf bank_mask:0xf\n"
    "v_cndmask_b32_dpp v255, v231, v239, vcc quad_perm:[2,3,2,3] row_mask:0xf bank_mask:0xf\n"
    "global_store_dwordx4 v121, v[240:243], s[44:45] offset:0 sc1\n"
    "global_store_dwordx4 v121, v[248:251], s[44:45] offset:2048 sc1\n"
    "global_store_dwordx4 v121, v[244:247], s[44:45] offset:1024 sc1\n"
    "global_store_dwordx4 v121, v[252:255], s[44:45] offset:3072 sc1\n"
    "s_add_u32 s46, s40, 0xab00000\n"
    "s_addc_u32 s47, s41, 0\n"
    "s_waitcnt vmcnt(0)\n"
    "s_barrier\n"
    "s_cmp_lg_u32 s56, 0\n"
    "s_cbranch_scc1 G_f3%=\n"
    "s_or_b32 s58, s42, s51\n"
    "s_mov_b64 exec, 1\n"
    "v_mov_b32 v116, s58\n"
    "global_store_dwordx2 v118, v[116:117], s[54:55] sc1\n"
    "s_mov_b64 exec, -1\n"
    "G_f3%=:\n"
    "s_branch G_loop%=\n"
    "G_t1%=:\n"
    "global_store_dwordx4 v119, v[124:127], s[46:47] sc1\n"
    "global_store_dwordx4 v120, v[124:127], s[46:47] sc1\n"
    "s_nop 7\n"
    "v_cvt_pk_bf16_f32 v224, v160, v161\n"
    "v_cvt_pk_bf16_f32 v225, v162, v163\n"
    "v_cvt_pk_bf16_f32 v226, v164, v165\n"
    "v_cvt_pk_bf16_f32 v227, v166, v167\n"
    "v_cvt_pk_bf16_f32 v228, v176, v177\n"
    "v_cvt_pk_bf16_f32 v229, v178, v179\n"
    "v_cvt_pk_bf16_f32 v230, v180, v181\n"
    "v_cvt_pk_bf16_f32 v231, v182, v183\n"
    "v_cvt_pk_bf16_f32 v232, v168, v169\n"
    "v_cvt_pk_bf16_f32 v233, v170, v171\n"
    "v_cvt_pk_bf16_f32 v234, v172, v173\n"
    "v_cvt_pk_bf16_f32 v235, v174, v175\n"
    "v_cvt_pk_bf16_f32 v236, v184, v185\n"
    "v_cvt_pk_bf16_f32 v237, v186, v187\n"
    "v_cvt_pk_bf16_f32 v238, v188, v189\n"
    "v_cvt_pk_bf16_f32 v239, v190, v191\n"
    "s_not_b64 vcc, vcc\n"
    "v_cndmask_b32_dpp v240, v232, v224, vcc quad_perm:[0,1,0,1] row_mask:0xf bank_mask:0xf\n"
    "v_cndmask_b32_dpp v241, v233, v225, vcc quad_perm:[0,1,0,1] row_mask:0xf bank_mask:0xf\n"
    "v_cndmask_b32_dpp v242, v234, v226, vcc quad_perm:[0,1,0,1] row_mask:0xf bank_mask:0xf\n"
    "v_cndmask_b32_dpp v243, v235, v227, vcc quad_perm:[0,1,0,1] row_mask:0xf bank_mask:0xf\n"
    "v_cndmask_b32_dpp v248, v236, v228, vcc quad_perm:[0,1,0,1] row_mask:0xf bank_mask:0xf\n"
    "v_cndmask_b32_dpp v249, v237, v229, vcc quad_perm:[0,1,0,1] row_mask:0xf bank_mask:0xf\n"
    "v_cndmask_b32_dpp v250, v238, v230, vcc quad_perm:[0,1,0,1] row_mask:0xf bank_mask:0xf\n"
    "v_cndmask_b32_dpp v251, v239, v231, vcc quad_perm:[0,1,0,1] row_mask:0xf bank_mask:0xf\n"
    "s_not_b64 vcc, vcc\n"
    "v_cndmask_b32_dpp v244, v224, v232, vcc quad_perm:[2,3,2,3] row_mask:0xf bank_mask:0xf\n"
    "v_cndmask_b32_dpp v245, v225, v233, vcc quad_perm:[2,3,2,3] row_mask:0xf bank_mask:0xf\n"
    "v_cndmask_b32_dpp v246, v226, v234, vcc quad_perm:[2,3,2,3] row_mask:0xf bank_mask:0xf\n"
    "v_cndmask_b32_dpp v247, v227, v235, vcc quad_perm:[2,3,2,3] row_mask:0xf bank_mask:0xf\n"
    "v_cndmask_b32_dpp v252, v228, v236, vcc quad_perm:[2,3,2,3] row_mask:0xf bank_mask:0xf\n"
    "v_cndmask_b32_dpp v253, v229, v237, vcc quad_perm:[2,3,2,3] row_mask:0xf bank_mask:0xf\n"
    "v_cndmask_b32_dpp v254, v230, v238, vcc quad_perm:[2,3,2,3] row_mask:0xf bank_mask:0xf\n"
    "v_cndmask_b32_dpp v255, v231, v239, vcc quad_perm:[2,3,2,3] row_mask:0xf bank_mask:0xf\n"
    "global_store_dwordx4 v121, v[240:243], s[44:45] offset:0 sc1\n"
    "global_store_dwordx4 v121, v[248:251], s[44:45] offset:2048 sc1\n"
    "global_store_dwordx4 v121, v[244:247], s[44:45] offset:1024 sc1\n"
    "global_store_dwordx4 v121, v[252:255], s[44:45] offset:3072 sc1\n"
    "s_add_u32 s46, s40, 0xab00000\n"
    "s_addc_u32 s47, s41, 0\n"
    "s_waitcnt vmcnt(0)\n"
    "s_barrier\n"
    "s_cmp_lg_u32 s56, 0\n"
    "s_cbranch_scc1 G_f4%=\n"
    "s_or_b32 s58, s42, s51\n"
    "s_mov_b64 exec, 1\n"
    "v_mov_b32 v116, s58\n"
    "global_store_dwordx2 v118, v[116:117], s[54:55] sc1\n"
    "s_mov_b64 exec, -1\n"
    "G_f4%=:\n"
    "s_cmp_eq_u32 s51, 32\n"
    "s_cbranch_scc1 G_end%=\n"
    "s_add_u32 s58, s40, 0x20e000\n"
    "s_addc_u32 s59, s41, 0\n"
    "s_add_u32 s60, s40, 0x200200\n"
    "s_addc_u32 s61, s41, 0\n"
    "s_or_b32 s62, s42, 1\n"
    "s_mov_b32 s63, s43\n"
    "v_lshlrev_b32 v224, 3, v121\n"
    "G_xw1%=:\n"
    "global_load_dwordx2 v[226:227], v224, s[58:59] sc1\n"
    "G_xc1%=:\n"
    "s_waitcnt vmcnt(0)\n"
    "v_cmp_ne_u64 vcc, s[62:63], v[226:227]\n"
    "s_nop 0\n"
    "s_cbranch_vccz G_xok1%=\n"
    "s_sleep 4\n"
    "s_add_u32 s57, s57, 1\n"
    "s_and_b32 s68, s57, 0xff\n"
    "s_cbranch_scc1 G_xw1%=\n"
    "global_load_dwordx2 v[226:227], v118, s[60:61] sc1\n"
    "s_waitcnt vmcnt(0)\n"
    "v_cmp_eq_u64 vcc, s[42:43], v[226:227]\n"
    "s_nop 0\n"
    "s_cbranch_vccnz G_xok1%=\n"
    "s_cmp_lt_u32 s57, 0x40000\n"
    "s_cbranch_scc1 G_xw1%=\n"
    "v_mov_b32 v116, s42\n"
    "global_store_dwordx2 v118, v[116:117], s[60:61] sc1\n"
    "G_xok1%=:\n"
    "s_mov_b32 vcc_lo, 0xcccccccc\n"
    "s_mov_b32 vcc_hi, 0xcccccccc\n"
    "s_branch G_even%=\n"
    "G_end%=:\n"
    "s_waitcnt lgkmcnt(0)\n"
    :: GIN : GCLOB);
  else asm volatile(
    "s_mov_b64 s[40:41], %0\n"
    "s_mov_b64 s[42:43], %1\n"
    "s_mov_b64 s[44:45], %2\n"
    "s_mov_b64 s[54:55], %3\n"
    "s_mov_b32 s56, %4\n"
    "v_mov_b32 v121, %5\n"
    "v_mov_b32 v119, %7\n"
    "v_mov_b32 v120, %8\n"
    "s_add_u32 s58, s40, 0x20e000\n"
    "s_addc_u32 s59, s41, 0\n"
    "v_lshlrev_b32 v225, 3, v121\n"
    "v_and_b32 v224, 0x380, v225\n"
    "global_load_dwordx2 v[226:227], v224, s[58:59] sc1\n"
    "v_xor_b32 v112, 0, %6\n"
    "v_xor_b32 v113, 32, %6\n"
    "v_xor_b32 v114, 64, %6\n"
    "v_xor_b32 v115, 96, %6\n"
    "ds_read_b64_tr_b16 a[0:1], v112 offset:0\n"
    "ds_read_b64_tr_b16 a[2:3], v112 offset:1024\n"
    "ds_read_b64_tr_b16 a[4:5], v112 offset:8192\n"
    "ds_read_b64_tr_b16 a[6:7], v112 offset:9216\n"
    "ds_read_b64_tr_b16 a[8:9], v112 offset:16384\n"
    "ds_read_b64_tr_b16 a[10:11], v112 offset:17408\n"
    "ds_read_b64_tr_b16 a[12:13], v112 offset:24576\n"
    "ds_read_b64_tr_b16 a[14:15], v112 offset:25600\n"
    "s_waitcnt lgkmcnt(6)\n"
    "ds_read_b64_tr_b16 a[16:17], v112 offset:32768\n"
    "ds_read_b64_tr_b16 a[18:19], v112 offset:33792\n"
    "ds_read_b64_tr_b16 a[20:21], v112 offset:40960\n"
    "ds_read_b64_tr_b16 a[22:23], v112 offset:41984\n"
    "ds_read_b64_tr_b16 a[24:25], v112 offset:49152\n"
    "ds_read_b64_tr_b16 a[26:27], v112 offset:50176\n"
    "ds_read_b64_tr_b16 a[28:29], v112 offset:57344\n"
    "ds_read_b64_tr_b16 a[30:31], v112 offset:58368\n"
    "s_waitcnt lgkmcnt(6)\n"
    "ds_read_b64_tr_b16 a[64:65], v113 offset:0\n"
    "ds_read_b64_tr_b16 a[66:67], v113 offset:1024\n"
    "ds_read_b64_tr_b16 a[68:69], v113 offset:8192\n"
    "ds_read_b64_tr_b16 a[70:71], v113 offset:9216\n"
    "ds_read_b64_tr_b16 a[72:73], v113 offset:16384\n"
    "ds_read_b64_tr_b16 a[74:75], v113 offset:17408\n"
    "ds_read_b64_tr_b16 a[76:77], v113 offset:24576\n"
    "ds_read_b64_tr_b16 a[78:79], v113 offset:25600\n"
    "s_waitcnt lgkmcnt(6)\n"
    "ds_read_b64_tr_b16 a[80:81], v113 offset:32768\n"
    "ds_read_b64_tr_b16 a[82:83], v113 offset:33792\n"
    "ds_read_b64_tr_b16 a[84:85], v113 offset:40960\n"
    "ds_read_b64_tr_b16 a[86:87], v113 offset:41984\n"
    "ds_read_b64_tr_b16 a[88:89], v113 offset:49152\n"
    "ds_read_b64_tr_b16 a[90:91], v113 offset:50176\n"
    "ds_read_b64_tr_b16 a[92:93], v113 offset:57344\n"
    "ds_read_b64_tr_b16 a[94:95], v113 offset:58368\n"
    "s_waitcnt lgkmcnt(6)\n"
    "ds_read_b64_tr_b16 a[128:129], v114 offset:0\n"
    "ds_read_b64_tr_b16 a[130:131], v114 offset:1024\n"
    "ds_read_b64_tr_b16 a[132:133], v114 offset:8192\n"
    "ds_read_b64_tr_b16 a[134:135], v114 offset:9216\n"
    "ds_read_b64_tr_b16 a[136:137], v114 offset:16384\n"
    "ds_read_b64_tr_b16 a[138:139], v114 offset:17408\n"
    "ds_read_b64_tr_b16 a[140:141], v114 offset:24576\n"
    "ds_read_b64_tr_b16 a[142:143], v114 offset:25600\n"
    "s_waitcnt lgkmcnt(6)\n"
    "ds_read_b64_tr_b16 a[144:145], v114 offset:32768\n"
    "ds_read_b64_tr_b16 a[146:147], v114 offset:33792\n"
    "ds_read_b64_tr_b16 a[148:149], v114 offset:40960\n"
    "ds_read_b64_tr_b16 a[150:151], v114 offset:41984\n"
    "ds_read_b64_tr_b16 a[152:153], v114 offset:49152\n"
    "ds_read_b64_tr_b16 a[154:155], v114 offset:50176\n"
    "ds_read_b64_tr_b16 a[156:157], v114 offset:57344\n"
    "ds_read_b64_tr_b16 a[158:159], v114 offset:58368\n"
    "s_waitcnt lgkmcnt(6)\n"
    "ds_read_b64_tr_b16 a[192:193], v115 offset:0\n"
    "ds_read_b64_tr_b16 a[194:195], v115 offset:1024\n"
    "ds_read_b64_tr_b16 a[196:197], v115 offset:8192\n"
    "ds_read_b64_tr_b16 a[198:199], v115 offset:9216\n"
    "ds_read_b64_tr_b16 a[200:201], v115 offset:16384\n"
    "ds_read_b64_tr_b16 a[202:203], v115 offset:17408\n"
    "ds_read_b64_tr_b16 a[204:205], v115 offset:24576\n"
    "ds_read_b64_tr_b16 a[206:207], v115 offset:25600\n"
    "s_waitcnt lgkmcnt(6)\n"
    "ds_read_b64_tr_b16 a[208:209], v115 offset:32768\n"
    "ds_read_b64_tr_b16 a[210:211], v115 offset:33792\n"
    "ds_read_b64_tr_b16 a[212:213], v115 offset:40960\n"
    "ds_read_b64_tr_b16 a[214:215], v115 offset:41984\n"
    "ds_read_b64_tr_b16 a[216:217], v115 offset:49152\n"
    "ds_read_b64_tr_b16 a[218:219], v115 offset:50176\n"
    "ds_read_b64_tr_b16 a[220:221], v115 offset:57344\n"
    "ds_read_b64_tr_b16 a[222:223], v115 offset:58368\n"
    "s_waitcnt lgkmcnt(6)\n"
    "v_xor_b32 v112, 0, %6\n"
    "v_add_u32 v112, 0x10000, v112\n"
    "v_xor_b32 v113, 32, %6\n"
    "v_add_u32 v113, 0x10000, v113\n"
    "v_xor_b32 v114, 64, %6\n"
    "v_add_u32 v114, 0x10000, v114\n"
    "v_xor_b32 v115, 96, %6\n"
    "v_add_u32 v115, 0x10000, v115\n"
    "ds_read_b64_tr_b16 a[32:33], v112 offset:0\n"
    "ds_read_b64_tr_b16 a[34:35], v112 offset:1024\n"
    "ds_read_b64_tr_b16 a[36:37], v112 offset:8192\n"
    "ds_read_b64_tr_b16 a[38:39], v112 offset:9216\n"
    "ds_read_b64_tr_b16 a[40:41], v112 offset:16384\n"
    "ds_read_b64_tr_b16 a[42:43], v112 offset:17408\n"
    "ds_read_b64_tr_b16 a[44:45], v112 offset:24576\n"
    "ds_read_b64_tr_b16 a[46:47], v112 offset:25600\n"
    "s_waitcnt lgkmcnt(6)\n"
    "ds_read_b64_tr_b16 a[48:49], v112 offset:32768\n"
    "ds_read_b64_tr_b16 a[50:51], v112 offset:33792\n"
    "ds_read_b64_tr_b16 a[52:53], v112 offset:40960\n"
    "ds_read_b64_tr_b16 a[54:55], v112 offset:41984\n"
    "ds_read_b64_tr_b16 a[56:57], v112 offset:49152\n"
    "ds_read_b64_tr_b16 a[58:59], v112 offset:50176\n"
    "ds_read_b64_tr_b16 a[60:61], v112 offset:57344\n"
    "ds_read_b64_tr_b16 a[62:63], v112 offset:58368\n"
    "s_waitcnt lgkmcnt(6)\n"
    "ds_read_b64_tr_b16 a[96:97], v113 offset:0\n"
    "ds_read_b64_tr_b16 a[98:99], v113 offset:1024\n"
    "ds_read_b64_tr_b16 a[100:101], v113 offset:8192\n"
    "ds_read_b64_tr_b16 a[102:103], v113 offset:9216\n"
    "ds_read_b64_tr_b16 a[104:105], v113 offset:16384\n"
    "ds_read_b64_tr_b16 a[106:107], v113 offset:17408\n"
    "ds_read_b64_tr_b16 a[108:109], v113 offset:24576\n"
    "ds_read_b64_tr_b16 a[110:111], v113 offset:25600\n"
    "s_waitcnt lgkmcnt(6)\n"
    "ds_read_b64_tr_b16 a[112:113], v113 offset:32768\n"
    "ds_read_b64_tr_b16 a[114:115], v113 offset:33792\n"
    "ds_read_b64_tr_b16 a[116:117], v113 offset:40960\n"
    "ds_read_b64_tr_b16 a[118:119], v113 offset:41984\n"
    "ds_read_b64_tr_b16 a[120:121], v113 offset:49152\n"
    "ds_read_b64_tr_b16 a[122:123], v113 offset:50176\n"
    "ds_read_b64_tr_b16 a[124:125], v113 offset:57344\n"
    "ds_read_b64_tr_b16 a[126:127], v113 offset:58368\n"
    "s_waitcnt lgkmcnt(6)\n"
    "ds_read_b64_tr_b16 a[160:161], v114 offset:0\n"
    "ds_read_b64_tr_b16 a[162:163], v114 offset:1024\n"
    "ds_read_b64_tr_b16 a[164:165], v114 offset:8192\n"
    "ds_read_b64_tr_b16 a[166:167], v114 offset:9216\n"
    "ds_read_b64_tr_b16 a[168:169], v114 offset:16384\n"
    "ds_read_b64_tr_b16 a[170:171], v114 offset:17408\n"
    "ds_read_b64_tr_b16 a[172:173], v114 offset:24576\n"
    "ds_read_b64_tr_b16 a[174:175], v114 offset:25600\n"
    "s_waitcnt lgkmcnt(6)\n"
    "ds_read_b64_tr_b16 a[176:177], v114 offset:32768\n"
    "ds_read_b64_tr_b16 a[178:179], v114 offset:33792\n"
    "ds_read_b64_tr_b16 a[180:181], v114 offset:40960\n"
    "ds_read_b64_tr_b16 a[182:183], v114 offset:41984\n"
    "ds_read_b64_tr_b16 a[184:185], v114 offset:49152\n"
    "ds_read_b64_tr_b16 a[186:187], v114 offset:50176\n"
    "ds_read_b64_tr_b16 a[188:189], v114 offset:57344\n"
    "ds_read_b64_tr_b16 a[190:191], v114 offset:58368\n"
    "s_waitcnt lgkmcnt(6)\n"
    "ds_read_b64_tr_b16 a[224:225], v115 offset:0\n"
    "ds_read_b64_tr_b16 a[226:227], v115 offset:1024\n"
    "ds_read_b64_tr_b16 a[228:229], v115 offset:8192\n"
    "ds_read_b64_tr_b16 a[230:231], v115 offset:9216\n"
    "ds_read_b64_tr_b16 a[232:233], v115 offset:16384\n"
    "ds_read_b64_tr_b16 a[234:235], v115 offset:17408\n"
    "ds_read_b64_tr_b16 a[236:237], v115 offset:24576\n"
    "ds_read_b64_tr_b16 a[238:239], v115 offset:25600\n"
    "s_waitcnt lgkmcnt(6)\n"
    "ds_read_b64_tr_b16 a[240:241], v115 offset:32768\n"
    "ds_read_b64_tr_b16 a[242:243], v115 offset:33792\n"
    "ds_read_b64_tr_b16 a[244:245], v115 offset:40960\n"
    "ds_read_b64_tr_b16 a[246:247], v115 offset:41984\n"
    "ds_read_b64_tr_b16 a[248:249], v115 offset:49152\n"
    "ds_read_b64_tr_b16 a[250:251], v115 offset:50176\n"
    "ds_read_b64_tr_b16 a[252:253], v115 offset:57344\n"
    "ds_read_b64_tr_b16 a[254:255], v115 offset:58368\n"
    "s_waitcnt lgkmcnt(6)\n"
    "s_lshr_b32 s58, s56, 1\n"
    "s_lshl_b32 s58, s58, 15\n"
    "v_add_u32 v122, s58, v121\n"
    "v_add_u32 v123, 0x10000, v122\n"
    "s_lshl_b32 s50, s56, 14\n"
    "v_mov_b32 v124, 0xffbfffbf\n"
    "v_mov_b32 v125, 0xffbfffbf\n"
    "v_mov_b32 v126, 0xffbfffbf\n"
    "v_mov_b32 v127, 0xffbfffbf\n"
    "v_mov_b32 v118, 0\n"
    "v_mov_b32 v117, s43\n"
    "s_add_u32 s58, s50, 0x4d00000\n"
    "s_add_u32 s48, s40, s58\n"
    "s_addc_u32 s49, s41, 0\n"
    "s_mov_b32 s57, 0\n"
    "s_mov_b32 s51, 0\n"
    "s_waitcnt lgkmcnt(0)\n"
    "s_barrier\n"
    "s_add_u32 s58, s40, 0x20e000\n"
    "s_addc_u32 s59, s41, 0\n"
    "s_add_u32 s60, s40, 0x200200\n"
    "s_addc_u32 s61, s41, 0\n"
    "s_or_b32 s62, s42, 1\n"
    "s_mov_b32 s63, s43\n"
    "s_branch G_xc0%=\n"
    "G_xw0%=:\n"
    "global_load_dwordx2 v[226:227], v224, s[58:59] sc1\n"
    "G_xc0%=:\n"
    "s_waitcnt vmcnt(0)\n"
    "v_cmp_ne_u64 vcc, s[62:63], v[226:227]\n"
    "s_nop 0\n"
    "s_cbranch_vccz G_xok0%=\n"
    "s_sleep 4\n"
    "s_add_u32 s57, s57, 1\n"
    "s_and_b32 s68, s57, 0xff\n"
    "s_cbranch_scc1 G_xw0%=\n"
    "global_load_dwordx2 v[226:227], v118, s[60:61] sc1\n"
    "s_waitcnt vmcnt(0)\n"
    "v_cmp_eq_u64 vcc, s[42:43], v[226:227]\n"
    "s_nop 0\n"
    "s_cbranch_vccnz G_xok0%=\n"
    "s_cmp_lt_u32 s57, 0x40000\n"
    "s_cbranch_scc1 G_xw0%=\n"
    "v_mov_b32 v116, s42\n"
    "global_store_dwordx2 v118, v[116:117], s[60:61] sc1\n"
    "G_xok0%=:\n"
    "s_mov_b32 vcc_lo, 0xcccccccc\n"
    "s_mov_b32 vcc_hi, 0xcccccccc\n"
    "s_add_u32 m0, s50, 0\n"
    "s_add_u32 s64, s48, 0\n"
    "s_addc_u32 s65, s49, 0\n"
    "global_load_lds_dwordx4 v121, s[64:65] offset:0 sc1\n"
    "global_load_lds_dwordx4 v121, s[64:65] offset:1024 sc1\n"
    "global_load_lds_dwordx4 v121, s[64:65] offset:2048 sc1\n"
    "global_load_lds_dwordx4 v121, s[64:65] offset:3072 sc1\n"
    "s_add_u32 m0, s50, 4096\n"
    "s_add_u32 s64, s48, 4096\n"
    "s_addc_u32 s65, s49, 0\n"
    "global_load_lds_dwordx4 v121, s[64:65] offset:0 sc1\n"
    "global_load_lds_dwordx4 v121, s[64:65] offset:1024 sc1\n"
    "global_load_lds_dwordx4 v121, s[64:65] offset:2048 sc1\n"
    "global_load_lds_dwordx4 v121, s[64:65] offset:3072 sc1\n"
    "s_add_u32 m0, s50, 8192\n"
    "s_add_u32 s64, s48, 8192\n"
    "s_addc_u32 s65, s49, 0\n"
    "global_load_lds_dwordx4 v121, s[64:65] offset:0 sc1\n"
    "global_load_lds_dwordx4 v121, s[64:65] offset:1024 sc1\n"
    "global_load_lds_dwordx4 v121, s[64:65] offset:2048 sc1\n"
    "global_load_lds_dwordx4 v121, s[64:65] offset:3072 sc1\n"
    "s_add_u32 m0, s50, 12288\n"
    "s_add_u32 s64, s48, 12288\n"
    "s_addc_u32 s65, s49, 0\n"
    "s_add_u32 s48, s48, 0x10000\n"
    "s_addc_u32 s49, s49, 0\n"
    "global_load_lds_dwordx4 v121, s[64:65] offset:0 sc1\n"
    "global_load_lds_dwordx4 v121, s[64:65] offset:1024 sc1\n"
    "global_load_lds_dwordx4 v121, s[64:65] offset:2048 sc1\n"
    "global_load_lds_dwordx4 v121, s[64:65] offset:3072 sc1\n"
    "s_add_u32 m0, s50, 65536\n"
    "s_add_u32 s64, s48, 0\n"
    "s_addc_u32 s65, s49, 0\n"
    "global_load_lds_dwordx4 v121, s[64:65] offset:0 sc1\n"
    "global_load_lds_dwordx4 v121, s[64:65] offset:1024 sc1\n"
    "global_load_lds_dwordx4 v121, s[64:65] offset:2048 sc1\n"
    "global_load_lds_dwordx4 v121, s[64:65] offset:3072 sc1\n"
    "s_add_u32 m0, s50, 69632\n"
    "s_add_u32 s64, s48, 4096\n"
    "s_addc_u32 s65, s49, 0\n"
    "global_load_lds_dwordx4 v121, s[64:65] offset:0 sc1\n"
    "global_load_lds_dwordx4 v121, s[64:65] offset:1024 sc1\n"
    "global_load_lds_dwordx4 v121, s[64:65] offset:2048 sc1\n"
    "global_load_lds_dwordx4 v121, s[64:65] offset:3072 sc1\n"
    "s_add_u32 m0, s50, 73728\n"
    "s_add_u32 s64, s48, 8192\n"
    "s_addc_u32 s65, s49, 0\n"
    "global_load_lds_dwordx4 v121, s[64:65] offset:0 sc1\n"
    "global_load_lds_dwordx4 v121, s[64:65] offset:1024 sc1\n"
    "global_load_lds_dwordx4 v121, s[64:65] offset:2048 sc1\n"
    "s_mov_b64 s[66:67], s[44:45]\n"
    "s_add_u32 s46, s40, 0xaa00000\n"
    "s_addc_u32 s47, s41, 0\n"
    "s_add_u32 s44, s44, 0x8900000\n"
    "s_addc_u32 s45, s45, 0\n"
    "s_waitcnt vmcnt(19) lgkmcnt(0)\n"
    "s_barrier\n"
    "v_lshlrev_b32 v112, 3, v121\n"
    "v_min_u32 v112, 0x800, v112\n"
    "s_add_u32 s58, s40, 0x312000\n"
    "s_addc_u32 s59, s41, 0\n"
    "global_load_dwordx2 v[114:115], v112, s[58:59] sc1\n"
    "ds_read_b128 v[192:195], v122 offset:0\n"
    "ds_read_b128 v[196:199], v122 offset:16384\n"
    "ds_read_b128 v[200:203], v122 offset:1024\n"
    "ds_read_b128 v[204:207], v122 offset:17408\n"
    "ds_read_b128 v[208:211], v122 offset:2048\n"
    "ds_read_b128 v[212:215], v122 offset:18432\n"
    "s_waitcnt lgkmcnt(4)\n"
    "s_branch G_even%=\n"
    ".p2align 6\n"
    "G_loop%=:\n"
    "v_mfma_f32_16x16x32_bf16 v[160:163], v[192:195], a[0:3], 0\n"
    "ds_read_b128 v[216:219], v123 offset:3072\n"
    "v_mfma_f32_16x16x32_bf16 v[164:167], v[192:195], a[64:67], 0\n"
    "ds_read_b128 v[220:223], v123 offset:19456\n"
    "v_mfma_f32_16x16x32_bf16 v[168:171], v[192:195], a[128:131], 0\n"
    "v_mfma_f32_16x16x32_bf16 v[172:175], v[192:195], a[192:195], 0\n"
    "v_cvt_pk_bf16_f32 v224, v128, v129\n"
    "v_cvt_pk_bf16_f32 v225, v130, v131\n"
    "v_mfma_f32_16x16x32_bf16 v[176:179], v[196:199], a[0:3], 0\n"
    "global_load_lds_dwordx4 v121, s[64:65] offset:3072 sc1\n"
    "v_mfma_f32_16x16x32_bf16 v[180:183], v[196:199], a[64:67], 0\n"
    "v_cvt_pk_bf16_f32 v226, v144, v145\n"
    "v_cvt_pk_bf16_f32 v227, v146, v147\n"
    "v_mfma_f32_16x16x32_bf16 v[184:187], v[196:199], a[128:131], 0\n"
    "global_store_dwordx4 v119, v[124:127], s[46:47] sc1\n"
    "v_mfma_f32_16x16x32_bf16 v[188:191], v[196:199], a[192:195], 0\n"
    "v_cvt_pk_bf16_f32 v228, v132, v133\n"
    "v_cvt_pk_bf16_f32 v229, v134, v135\n"
    "s_waitcnt lgkmcnt(4)\n"
    "v_mfma_f32_16x16x32_bf16 v[160:163], v[200:203], a[4:7], v[160:163]\n"
    "ds_read_b128 v[192:195], v123 offset:4096\n"
    "v_mfma_f32_16x16x32_bf16 v[164:167], v[200:203], a[68:71], v[164:167]\n"
    "ds_read_b128 v[196:199], v123 offset:20480\n"
    "v_mfma_f32_16x16x32_bf16 v[168:171], v[200:203], a[132:135], v[168:171]\n"
    "s_add_u32 m0, s50, 12288\n"
    "s_add_u32 s64, s48, 12288\n"
    "s_addc_u32 s65, s49, 0\n"
    "v_cvt_pk_bf16_f32 v230, v148, v149\n"
    "v_cvt_pk_bf16_f32 v231, v150, v151\n"
    "v_mfma_f32_16x16x32_bf16 v[172:175], v[200:203], a[196:199], v[172:175]\n"
    "s_add_u32 s48, s48, 0x10000\n"
    "s_addc_u32 s49, s49, 0\n"
    "v_cvt_pk_bf16_f32 v232, v136, v137\n"
    "v_cvt_pk_bf16_f32 v233, v138, v139\n"
    "v_mfma_f32_16x16x32_bf16 v[176:179], v[204:207], a[4:7], v[176:179]\n"
    "global_load_lds_dwordx4 v121, s[64:65] offset:0 sc1\n"
    "v_mfma_f32_16x16x32_bf16 v[180:183], v[204:207], a[68:71], v[180:183]\n"
    "v_cvt_pk_bf16_f32 v234, v152, v153\n"
    "v_cvt_pk_bf16_f32 v235, v154, v155\n"
    "v_mfma_f32_16x16x32_bf16 v[184:187], v[204:207], a[132:135], v[184:187]\n"
    "global_store_dwordx4 v120, v[124:127], s[46:47] sc1\n"
    "v_mfma_f32_16x16x32_bf16 v[188:191], v[204:207], a[196:199], v[188:191]\n"
    "v_cvt_pk_bf16_f32 v236, v140, v141\n"
    "v_cvt_pk_bf16_f32 v237, v142, v143\n"
    "s_waitcnt lgkmcnt(4)\n"
    "v_mfma_f32_16x16x32_bf16 v[160:163], v[208:211], a[8:11], v[160:163]\n"
    "ds_read_b128 v[200:203], v123 offset:5120\n"
    "v_mfma_f32_16x16x32_bf16 v[164:167], v[208:211], a[72:75], v[164:167]\n"
    "ds_read_b128 v[204:207], v123 offset:21504\n"
    "v_mfma_f32_16x16x32_bf16 v[168:171], v[208:211], a[136:139], v[168:171]\n"
    "v_cvt_pk_bf16_f32 v238, v156, v157\n"
    "v_cvt_pk_bf16_f32 v239, v158, v159\n"
    "v_mfma_f32_16x16x32_bf16 v[172:175], v[208:211], a[200:203], v[172:175]\n"
    "v_mfma_f32_16x16x32_bf16 v[176:179], v[212:215], a[8:11], v[176:179]\n"
    "global_load_lds_dwordx4 v121, s[64:65] offset:1024 sc1\n"
    "v_mfma_f32_16x16x32_bf16 v[180:183], v[212:215], a[72:75], v[180:183]\n"
    "v_mfma_f32_16x16x32_bf16 v[184:187], v[212:215], a[136:139], v[184:187]\n"
    "global_store_dwordx4 v121, v[224:227], s[44:45] offset:0 sc1\n"
    "v_mfma_f32_16x16x32_bf16 v[188:191], v[212:215], a[200:203], v[188:191]\n"
    "s_waitcnt lgkmcnt(4)\n"
    "v_mfma_f32_16x16x32_bf16 v[160:163], v[216:219], a[12:15], v[160:163]\n"
    "ds_read_b128 v[208:211], v123 offset:6144\n"
    "v_mfma_f32_16x16x32_bf16 v[164:167], v[216:219], a[76:79], v[164:167]\n"
    "ds_read_b128 v[212:215], v123 offset:22528\n"
    "v_mfma_f32_16x16x32_bf16 v[168:171], v[216:219], a[140:143], v[168:171]\n"
    "v_mfma_f32_16x16x32_bf16 v[172:175], v[216:219], a[204:207], v[172:175]\n"
    "v_mfma_f32_16x16x32_bf16 v[176:179], v[220:223], a[12:15], v[176:179]\n"
    "global_load_lds_dwordx4 v121, s[64:65] offset:2048 sc1\n"
    "v_mfma_f32_16x16x32_bf16 v[180:183], v[220:223], a[76:79], v[180:183]\n"
    "v_mfma_f32_16x16x32_bf16 v[184:187], v[220:223], a[140:143], v[184:187]\n"
    "global_store_dwordx4 v121, v[228:231], s[44:45] offset:1024 sc1\n"
    "v_mfma_f32_16x16x32_bf16 v[188:191], v[220:223], a[204:207], v[188:191]\n"
    "s_waitcnt lgkmcnt(4)\n"
    "v_mfma_f32_16x16x32_bf16 v[160:163], v[192:195], a[16:19], v[160:163]\n"
    "ds_read_b128 v[216:219], v123 offset:7168\n"
    "v_mfma_f32_16x16x32_bf16 v[164:167], v[192:195], a[80:83], v[164:167]\n"
    "ds_read_b128 v[220:223], v123 offset:23552\n"
    "v_mfma_f32_16x16x32_bf16 v[168:171], v[192:195], a[144:147], v[168:171]\n"
    "global_store_dwordx4 v121, v[232:235], s[44:45] offset:2048 sc1\n"
    "v_mfma_f32_16x16x32_bf16 v[172:175], v[192:195], a[208:211], v[172:175]\n"
    "v_mfma_f32_16x16x32_bf16 v[176:179], v[196:199], a[16:19], v[176:179]\n"
    "global_load_lds_dwordx4 v121, s[64:65] offset:3072 sc1\n"
    "v_mfma_f32_16x16x32_bf16 v[180:183], v[196:199], a[80:83], v[180:183]\n"
    "v_mfma_f32_16x16x32_bf16 v[184:187], v[196:199], a[144:147], v[184:187]\n"
    "global_store_dwordx4 v121, v[236:239], s[44:45] offset:3072 sc1\n"
    "v_mfma_f32_16x16x32_bf16 v[188:191], v[196:199], a[208:211], v[188:191]\n"
    "s_waitcnt lgkmcnt(4)\n"
    "v_mfma_f32_16x16x32_bf16 v[160:163], v[200:203], a[20:23], v[160:163]\n"
    "s_waitcnt vmcnt(14) lgkmcnt(0)\n"
    "s_barrier\n"
    "v_mfma_f32_16x16x32_bf16 v[164:167], v[200:203], a[84:87], v[164:167]\n"
    "ds_read_b128 v[192:195], v123 offset:8192\n"
    "v_mfma_f32_16x16x32_bf16 v[168:171], v[200:203], a[148:151], v[168:171]\n"
    "ds_read_b128 v[196:199], v123 offset:24576\n"
    "s_add_u32 m0, s50, 65536\n"
    "s_add_u32 s64, s48, 0\n"
    "s_addc_u32 s65, s49, 0\n"
    "v_mfma_f32_16x16x32_bf16 v[172:175], v[200:203], a[212:215], v[172:175]\n"
    "v_mfma_f32_16x16x32_bf16 v[176:179], v[204:207], a[20:23], v[176:179]\n"
    "global_load_lds_dwordx4 v121, s[64:65] offset:0 sc1\n"
    "v_mfma_f32_16x16x32_bf16 v[180:183], v[204:207], a[84:87], v[180:183]\n"
    "v_mfma_f32_16x16x32_bf16 v[184:187], v[204:207], a[148:151], v[184:187]\n"
    "v_mfma_f32_16x16x32_bf16 v[188:191], v[204:207], a[212:215], v[188:191]\n"
    "s_waitcnt lgkmcnt(4)\n"
    "v_mfma_f32_16x16x32_bf16 v[160:163], v[208:211], a[24:27], v[160:163]\n"
    "ds_read_b128 v[200:203], v123 offset:9216\n"
    "v_mfma_f32_16x16x32_bf16 v[164:167], v[208:211], a[88:91], v[164:167]\n"
    "ds_read_b128 v[204:207], v123 offset:25600\n"
    "v_mfma_f32_16x16x32_bf16 v[168:171], v[208:211], a[152:155], v[168:171]\n"
    "v_mfma_f32_16x16x32_bf16 v[172:175], v[208:211], a[216:219], v[172:175]\n"
    "v_mfma_f32_16x16x32_bf16 v[176:179], v[212:215], a[24:27], v[176:179]\n"
    "global_load_lds_dwordx4 v121, s[64:65] offset:1024 sc1\n"
    "v_mfma_f32_16x16x32_bf16 v[180:183], v[212:215], a[88:91], v[180:183]\n"
    "s_lshr_b32 s58, s51, 1\n"
    "s_lshr_b32 s58, s52, s58\n"
    "s_and_b32 s58, s58, 1\n"
    "v_mfma_f32_16x16x32_bf16 v[184:187], v[212:215], a[152:155], v[184:187]\n"
    "s_lshl_b32 s58, s58, 13\n"
    "s_lshl_b32 s59, s51, 20\n"
    "s_add_u32 s58, s58, s59\n"
    "v_mfma_f32_16x16x32_bf16 v[188:191], v[212:215], a[216:219], v[188:191]\n"
    "s_add_u32 s58, s58, 0x7480000\n"
    "s_add_u32 s46, s40, s58\n"
    "s_addc_u32 s47, s41, 0\n"
    "s_waitcnt lgkmcnt(4)\n"
    "v_mfma_f32_16x16x32_bf16 v[160:163], v[216:219], a[28:31], v[160:163]\n"
    "ds_read_b128 v[208:211], v123 offset:10240\n"
    "v_mfma_f32_16x16x32_bf16 v[164:167], v[216:219], a[92:95], v[164:167]\n"
    "ds_read_b128 v[212:215], v123 offset:26624\n"
    "v_mfma_f32_16x16x32_bf16 v[168:171], v[216:219], a[156:159], v[168:171]\n"
    "v_mfma_f32_16x16x32_bf16 v[172:175], v[216:219], a[220:223], v[172:175]\n"
    "v_mfma_f32_16x16x32_bf16 v[176:179], v[220:223], a[28:31], v[176:179]\n"
    "global_load_lds_dwordx4 v121, s[64:65] offset:2048 sc1\n"
    "v_mfma_f32_16x16x32_bf16 v[180:183], v[220:223], a[92:95], v[180:183]\n"
    "s_mov_b64 s[44:45], s[66:67]\n"
    "s_add_u32 s66, s66, 0x80000\n"
    "s_addc_u32 s67, s67, 0\n"
    "v_mfma_f32_16x16x32_bf16 v[184:187], v[220:223], a[156:159], v[184:187]\n"
    "v_mfma_f32_16x16x32_bf16 v[188:191], v[220:223], a[220:223], v[188:191]\n"
    "s_waitcnt lgkmcnt(4)\n"
    "v_mfma_f32_16x16x32_bf16 v[160:163], v[192:195], a[32:35], v[160:163]\n"
    "ds_read_b128 v[216:219], v123 offset:11264\n"
    "v_mfma_f32_16x16x32_bf16 v[164:167], v[192:195], a[96:99], v[164:167]\n"
    "ds_read_b128 v[220:223], v123 offset:27648\n"
    "v_mfma_f32_16x16x32_bf16 v[168:171], v[192:195], a[160:163], v[168:171]\n"
    "v_mfma_f32_16x16x32_bf16 v[172:175], v[192:195], a[224:227], v[172:175]\n"
    "v_mfma_f32_16x16x32_bf16 v[176:179], v[196:199], a[32:35], v[176:179]\n"
    "global_load_lds_dwordx4 v121, s[64:65] offset:3072 sc1\n"
    "v_mfma_f32_16x16x32_bf16 v[180:183], v[196:199], a[96:99], v[180:183]\n"
    "v_mfma_f32_16x16x32_bf16 v[184:187], v[196:199], a[160:163], v[184:187]\n"
    "v_mfma_f32_16x16x32_bf16 v[188:191], v[196:199], a[224:227], v[188:191]\n"
    "s_waitcnt lgkmcnt(4)\n"
    "v_mfma_f32_16x16x32_bf16 v[160:163], v[200:203], a[36:39], v[160:163]\n"
    "ds_read_b128 v[192:195], v123 offset:12288\n"
    "v_mfma_f32_16x16x32_bf16 v[164:167], v[200:203], a[100:103], v[164:167]\n"
    "ds_read_b128 v[196:199], v123 offset:28672\n"
    "v_mfma_f32_16x16x32_bf16 v[168:171], v[200:203], a[164:167], v[168:171]\n"
    "s_add_u32 m0, s50, 69632\n"
    "s_add_u32 s64, s48, 4096\n"
    "s_addc_u32 s65, s49, 0\n"
    "v_mfma_f32_16x16x32_bf16 v[172:175], v[200:203], a[228:231], v[172:175]\n"
    "v_mfma_f32_16x16x32_bf16 v[176:179], v[204:207], a[36:39], v[176:179]\n"
    "global_load_lds_dwordx4 v121, s[64:65] offset:0 sc1\n"
    "v_mfma_f32_16x16x32_bf16 v[180:183], v[204:207], a[100:103], v[180:183]\n"
    "v_mfma_f32_16x16x32_bf16 v[184:187], v[204:207], a[164:167], v[184:187]\n"
    "v_mfma_f32_16x16x32_bf16 v[188:191], v[204:207], a[228:231], v[188:191]\n"
    "s_waitcnt lgkmcnt(4)\n"
    "v_mfma_f32_16x16x32_bf16 v[160:163], v[208:211], a[40:43], v[160:163]\n"
    "ds_read_b128 v[200:203], v123 offset:13312\n"
    "v_mfma_f32_16x16x32_bf16 v[164:167], v[208:211], a[104:107], v[164:167]\n"
    "ds_read_b128 v[204:207], v123 offset:29696\n"
    "v_mfma_f32_16x16x32_bf16 v[168:171], v[208:211], a[168:171], v[168:171]\n"
    "v_mfma_f32_16x16x32_bf16 v[172:175], v[208:211], a[232:235], v[172:175]\n"
    "v_mfma_f32_16x16x32_bf16 v[176:179], v[212:215], a[40:43], v[176:179]\n"
    "global_load_lds_dwordx4 v121, s[64:65] offset:1024 sc1\n"
    "v_mfma_f32_16x16x32_bf16 v[180:183], v[212:215], a[104:107], v[180:183]\n"
    "v_mfma_f32_16x16x32_bf16 v[184:187], v[212:215], a[168:171], v[184:187]\n"
    "v_mfma_f32_16x16x32_bf16 v[188:191], v[212:215], a[232:235], v[188:191]\n"
    "s_waitcnt lgkmcnt(4)\n"
    "v_mfma_f32_16x16x32_bf16 v[160:163], v[216:219], a[44:47], v[160:163]\n"
    "ds_read_b128 v[208:211], v123 offset:14336\n"
    "v_mfma_f32_16x16x32_bf16 v[164:167], v[216:219], a[108:111], v[164:167]\n"
    "ds_read_b128 v[212:215], v123 offset:30720\n"
    "v_mfma_f32_16x16x32_bf16 v[168:171], v[216:219], a[172:175], v[168:171]\n"
    "v_mfma_f32_16x16x32_bf16 v[172:175], v[216:219], a[236:239], v[172:175]\n"
    "v_mfma_f32_16x16x32_bf16 v[176:179], v[220:223], a[44:47], v[176:179]\n"
    "global_load_lds_dwordx4 v121, s[64:65] offset:2048 sc1\n"
    "v_mfma_f32_16x16x32_bf16 v[180:183], v[220:223], a[108:111], v[180:183]\n"
    "v_mfma_f32_16x16x32_bf16 v[184:187], v[220:223], a[172:175], v[184:187]\n"
    "v_mfma_f32_16x16x32_bf16 v[188:191], v[220:223], a[236:239], v[188:191]\n"
    "s_waitcnt lgkmcnt(4)\n"
    "v_mfma_f32_16x16x32_bf16 v[160:163], v[192:195], a[48:51], v[160:163]\n"
    "ds_read_b128 v[216:219], v123 offset:15360\n"
    "v_mfma_f32_16x16x32_bf16 v[164:167], v[192:195], a[112:115], v[164:167]\n"
    "ds_read_b128 v[220:223], v123 offset:31744\n"
    "v_mfma_f32_16x16x32_bf16 v[168:171], v[192:195], a[176:179], v[168:171]\n"
    "v_mfma_f32_16x16x32_bf16 v[172:175], v[192:195], a[240:243], v[172:175]\n"
    "v_mfma_f32_16x16x32_bf16 v[176:179], v[196:199], a[48:51], v[176:179]\n"
    "global_load_lds_dwordx4 v121, s[64:65] offset:3072 sc1\n"
    "v_mfma_f32_16x16x32_bf16 v[180:183], v[196:199], a[112:115], v[180:183]\n"
    "v_mfma_f32_16x16x32_bf16 v[184:187], v[196:199], a[176:179], v[184:187]\n"
    "v_mfma_f32_16x16x32_bf16 v[188:191], v[196:199], a[240:243], v[188:191]\n"
    "s_waitcnt lgkmcnt(4)\n"
    "v_mfma_f32_16x16x32_bf16 v[160:163], v[200:203], a[52:55], v[160:163]\n"
    "s_waitcnt vmcnt(8) lgkmcnt(0)\n"
    "s_barrier\n"
    "v_mfma_f32_16x16x32_bf16 v[164:167], v[200:203], a[116:119], v[164:167]\n"
    "ds_read_b128 v[192:195], v122 offset:0\n"
    "v_mfma_f32_16x16x32_bf16 v[168:171], v[200:203], a[180:183], v[168:171]\n"
    "ds_read_b128 v[196:199], v122 offset:16384\n"
    "s_add_u32 m0, s50, 73728\n"
    "s_add_u32 s64, s48, 8192\n"
    "s_addc_u32 s65, s49, 0\n"
    "v_mfma_f32_16x16x32_bf16 v[172:175], v[200:203], a[244:247], v[172:175]\n"
    "v_mfma_f32_16x16x32_bf16 v[176:179], v[204:207], a[52:55], v[176:179]\n"
    "global_load_lds_dwordx4 v121, s[64:65] offset:0 sc1\n"
    "v_mfma_f32_16x16x32_bf16 v[180:183], v[204:207], a[116:119], v[180:183]\n"
    "s_cmp_lg_u32 s56, 0\n"
    "s_cbranch_scc1 G_f1%=\n"
    "s_or_b32 s58, s42, s51\n"
    "s_mov_b64 exec, 1\n"
    "v_mov_b32 v116, s58\n"
    "global_store_dwordx2 v118, v[116:117], s[54:55] sc1\n"
    "s_mov_b64 exec, -1\n"
    "G_f1%=:\n"
    "v_mfma_f32_16x16x32_bf16 v[184:187], v[204:207], a[180:183], v[184:187]\n"
    "v_mfma_f32_16x16x32_bf16 v[188:191], v[204:207], a[244:247], v[188:191]\n"
    "s_waitcnt lgkmcnt(4)\n"
    "v_mfma_f32_16x16x32_bf16 v[160:163], v[208:211], a[56:59], v[160:163]\n"
    "ds_read_b128 v[200:203], v122 offset:1024\n"
    "v_mfma_f32_16x16x32_bf16 v[164:167], v[208:211], a[120:123], v[164:167]\n"
    "ds_read_b128 v[204:207], v122 offset:17408\n"
    "v_mfma_f32_16x16x32_bf16 v[168:171], v[208:211], a[184:187], v[168:171]\n"
    "v_mfma_f32_16x16x32_bf16 v[172:175], v[208:211], a[248:251], v[172:175]\n"
    "v_mfma_f32_16x16x32_bf16 v[176:179], v[212:215], a[56:59], v[176:179]\n"
    "global_load_lds_dwordx4 v121, s[64:65] offset:1024 sc1\n"
    "v_mfma_f32_16x16x32_bf16 v[180:183], v[212:215], a[120:123], v[180:183]\n"
    "v_mfma_f32_16x16x32_bf16 v[184:187], v[212:215], a[184:187], v[184:187]\n"
    "v_mfma_f32_16x16x32_bf16 v[188:191], v[212:215], a[248:251], v[188:191]\n"
    "s_add_u32 s51, s51, 1\n"
    "s_waitcnt lgkmcnt(4)\n"
    "v_mfma_f32_16x16x32_bf16 v[160:163], v[216:219], a[60:63], v[160:163]\n"
    "ds_read_b128 v[208:211], v122 offset:2048\n"
    "v_mfma_f32_16x16x32_bf16 v[164:167], v[216:219], a[124:127], v[164:167]\n"
    "ds_read_b128 v[212:215], v122 offset:18432\n"
    "v_mfma_f32_16x16x32_bf16 v[168:171], v[216:219], a[188:191], v[168:171]\n"
    "v_mfma_f32_16x16x32_bf16 v[172:175], v[216:219], a[252:255], v[172:175]\n"
    "v_mfma_f32_16x16x32_bf16 v[176:179], v[220:223], a[60:63], v[176:179]\n"
    "global_load_lds_dwordx4 v121, s[64:65] offset:2048 sc1\n"
    "v_mfma_f32_16x16x32_bf16 v[180:183], v[220:223], a[124:127], v[180:183]\n"
    "v_mfma_f32_16x16x32_bf16 v[184:187], v[220:223], a[188:191], v[184:187]\n"
    "v_mfma_f32_16x16x32_bf16 v[188:191], v[220:223], a[252:255], v[188:191]\n"
    "s_add_u32 s58, s51, -3\n"
    "s_cmp_gt_u32 s58, 28\n"
    "s_waitcnt lgkmcnt(4)\n"
    "s_cbranch_scc1 G_t1%=\n"
    "G_even%=:\n"
    "v_mfma_f32_16x16x32_bf16 v[128:131], v[192:195], a[0:3], 0\n"
    "ds_read_b128 v[216:219], v122 offset:3072\n"
    "v_mfma_f32_16x16x32_bf16 v[132:135], v[192:195], a[64:67], 0\n"
    "ds_read_b128 v[220:223], v122 offset:19456\n"
    "v_mfma_f32_16x16x32_bf16 v[136:139], v[192:195], a[128:131], 0\n"
    "v_mfma_f32_16x16x32_bf16 v[140:143], v[192:195], a[192:195], 0\n"
    "v_cvt_pk_bf16_f32 v224, v160, v161\n"
    "v_cvt_pk_bf16_f32 v225, v162, v163\n"
    "v_mfma_f32_16x16x32_bf16 v[144:147], v[196:199], a[0:3], 0\n"
    "global_load_lds_dwordx4 v121, s[64:65] offset:3072 sc1\n"
    "v_mfma_f32_16x16x32_bf16 v[148:151], v[196:199], a[64:67], 0\n"
    "v_cvt_pk_bf16_f32 v226, v176, v177\n"
    "v_cvt_pk_bf16_f32 v227, v178, v179\n"
    "v_mfma_f32_16x16x32_bf16 v[152:155], v[196:199], a[128:131], 0\n"
    "global_store_dwordx4 v119, v[124:127], s[46:47] sc1\n"
    "v_mfma_f32_16x16x32_bf16 v[156:159], v[196:199], a[192:195], 0\n"
    "v_cvt_pk_bf16_f32 v228, v164, v165\n"
    "v_cvt_pk_bf16_f32 v229, v166, v167\n"
    "s_waitcnt lgkmcnt(4)\n"
    "v_mfma_f32_16x16x32_bf16 v[128:131], v[200:203], a[4:7], v[128:131]\n"
    "ds_read_b128 v[192:195], v122 offset:4096\n"
    "v_mfma_f32_16x16x32_bf16 v[132:135], v[200:203], a[68:71], v[132:135]\n"
    "ds_read_b128 v[196:199], v122 offset:20480\n"
    "v_mfma_f32_16x16x32_bf16 v[136:139], v[200:203], a[132:135], v[136:139]\n"
    "s_add_u32 m0, s50, 77824\n"
    "s_add_u32 s64, s48, 12288\n"
    "s_addc_u32 s65, s49, 0\n"
    "v_cvt_pk_bf16_f32 v230, v180, v181\n"
    "v_cvt_pk_bf16_f32 v231, v182, v183\n"
    "v_mfma_f32_16x16x32_bf16 v[140:143], v[200:203], a[196:199], v[140:143]\n"
    "s_add_u32 s48, s48, 0x10000\n"
    "s_addc_u32 s49, s49, 0\n"
    "v_cvt_pk_bf16_f32 v232, v168, v169\n"
    "v_cvt_pk_bf16_f32 v233, v170, v171\n"
    "v_mfma_f32_16x16x32_bf16 v[144:147], v[204:207], a[4:7], v[144:147]\n"
    "global_load_lds_dwordx4 v121, s[64:65] offset:0 sc1\n"
    "v_mfma_f32_16x16x32_bf16 v[148:151], v[204:207], a[68:71], v[148:151]\n"
    "v_cvt_pk_bf16_f32 v234, v184, v185\n"
    "v_cvt_pk_bf16_f32 v235, v186, v187\n"
    "v_mfma_f32_16x16x32_bf16 v[152:155], v[204:207], a[132:135], v[152:155]\n"
    "global_store_dwordx4 v120, v[124:127], s[46:47] sc1\n"
    "v_mfma_f32_16x16x32_bf16 v[156:159], v[204:207], a[196:199], v[156:159]\n"
    "v_cvt_pk_bf16_f32 v236, v172, v173\n"
    "v_cvt_pk_bf16_f32 v237, v174, v175\n"
    "s_waitcnt lgkmcnt(4)\n"
    "v_mfma_f32_16x16x32_bf16 v[128:131], v[208:211], a[8:11], v[128:131]\n"
    "ds_read_b128 v[200:203], v122 offset:5120\n"
    "v_mfma_f32_16x16x32_bf16 v[132:135], v[208:211], a[72:75], v[132:135]\n"
    "ds_read_b128 v[204:207], v122 offset:21504\n"
    "v_mfma_f32_16x16x32_bf16 v[136:139], v[208:211], a[136:139], v[136:139]\n"
    "v_cvt_pk_bf16_f32 v238, v188, v189\n"
    "v_cvt_pk_bf16_f32 v239, v190, v191\n"
    "v_mfma_f32_16x16x32_bf16 v[140:143], v[208:211], a[200:203], v[140:143]\n"
    "v_mfma_f32_16x16x32_bf16 v[144:147], v[212:215], a[8:11], v[144:147]\n"
    "global_load_lds_dwordx4 v121, s[64:65] offset:1024 sc1\n"
    "v_mfma_f32_16x16x32_bf16 v[148:151], v[212:215], a[72:75], v[148:151]\n"
    "v_mfma_f32_16x16x32_bf16 v[152:155], v[212:215], a[136:139], v[152:155]\n"
    "global_store_dwordx4 v121, v[224:227], s[44:45] offset:0 sc1\n"
    "v_mfma_f32_16x16x32_bf16 v[156:159], v[212:215], a[200:203], v[156:159]\n"
    "s_waitcnt lgkmcnt(4)\n"
    "v_mfma_f32_16x16x32_bf16 v[128:131], v[216:219], a[12:15], v[128:131]\n"
    "ds_read_b128 v[208:211], v122 offset:6144\n"
    "v_mfma_f32_16x16x32_bf16 v[132:135], v[216:219], a[76:79], v[132:135]\n"
    "ds_read_b128 v[212:215], v122 offset:22528\n"
    "v_mfma_f32_16x16x32_bf16 v[136:139], v[216:219], a[140:143], v[136:139]\n"
    "v_mfma_f32_16x16x32_bf16 v[140:143], v[216:219], a[204:207], v[140:143]\n"
    "v_mfma_f32_16x16x32_bf16 v[144:147], v[220:223], a[12:15], v[144:147]\n"
    "global_load_lds_dwordx4 v121, s[64:65] offset:2048 sc1\n"
    "v_mfma_f32_16x16x32_bf16 v[148:151], v[220:223], a[76:79], v[148:151]\n"
    "v_mfma_f32_16x16x32_bf16 v[152:155], v[220:223], a[140:143], v[152:155]\n"
    "global_store_dwordx4 v121, v[228:231], s[44:45] offset:1024 sc1\n"
    "v_mfma_f32_16x16x32_bf16 v[156:159], v[220:223], a[204:207], v[156:159]\n"
    "s_waitcnt lgkmcnt(4)\n"
    "v_mfma_f32_16x16x32_bf16 v[128:131], v[192:195], a[16:19], v[128:131]\n"
    "ds_read_b128 v[216:219], v122 offset:7168\n"
    "v_mfma_f32_16x16x32_bf16 v[132:135], v[192:195], a[80:83], v[132:135]\n"
    "ds_read_b128 v[220:223], v122 offset:23552\n"
    "v_mfma_f32_16x16x32_bf16 v[136:139], v[192:195], a[144:147], v[136:139]\n"
    "global_store_dwordx4 v121, v[232:235], s[44:45] offset:2048 sc1\n"
    "v_mfma_f32_16x16x32_bf16 v[140:143], v[192:195], a[208:211], v[140:143]\n"
    "v_mfma_f32_16x16x32_bf16 v[144:147], v[196:199], a[16:19], v[144:147]\n"
    "global_load_lds_dwordx4 v121, s[64:65] offset:3072 sc1\n"
    "v_mfma_f32_16x16x32_bf16 v[148:151], v[196:199], a[80:83], v[148:151]\n"
    "v_mfma_f32_16x16x32_bf16 v[152:155], v[196:199], a[144:147], v[152:155]\n"
    "global_store_dwordx4 v121, v[236:239], s[44:45] offset:3072 sc1\n"
    "v_mfma_f32_16x16x32_bf16 v[156:159], v[196:199], a[208:211], v[156:159]\n"
    "s_waitcnt lgkmcnt(4)\n"
    "v_mfma_f32_16x16x32_bf16 v[128:131], v[200:203], a[20:23], v[128:131]\n"
    "s_waitcnt vmcnt(14) lgkmcnt(0)\n"
    "s_barrier\n"
    "v_mfma_f32_16x16x32_bf16 v[132:135], v[200:203], a[84:87], v[132:135]\n"
    "ds_read_b128 v[192:195], v122 offset:8192\n"
    "v_mfma_f32_16x16x32_bf16 v[136:139], v[200:203], a[148:151], v[136:139]\n"
    "ds_read_b128 v[196:199], v122 offset:24576\n"
    "s_add_u32 m0, s50, 0\n"
    "s_add_u32 s64, s48, 0\n"
    "s_addc_u32 s65, s49, 0\n"
    "v_mfma_f32_16x16x32_bf16 v[140:143], v[200:203], a[212:215], v[140:143]\n"
    "v_mfma_f32_16x16x32_bf16 v[144:147], v[204:207], a[20:23], v[144:147]\n"
    "global_load_lds_dwordx4 v121, s[64:65] offset:0 sc1\n"
    "v_mfma_f32_16x16x32_bf16 v[148:151], v[204:207], a[84:87], v[148:151]\n"
    "v_mfma_f32_16x16x32_bf16 v[152:155], v[204:207], a[148:151], v[152:155]\n"
    "v_mfma_f32_16x16x32_bf16 v[156:159], v[204:207], a[212:215], v[156:159]\n"
    "s_waitcnt lgkmcnt(4)\n"
    "v_mfma_f32_16x16x32_bf16 v[128:131], v[208:211], a[24:27], v[128:131]\n"
    "ds_read_b128 v[200:203], v122 offset:9216\n"
    "v_mfma_f32_16x16x32_bf16 v[132:135], v[208:211], a[88:91], v[132:135]\n"
    "ds_read_b128 v[204:207], v122 offset:25600\n"
    "v_mfma_f32_16x16x32_bf16 v[136:139], v[208:211], a[152:155], v[136:139]\n"
    "v_mfma_f32_16x16x32_bf16 v[140:143], v[208:211], a[216:219], v[140:143]\n"
    "v_mfma_f32_16x16x32_bf16 v[144:147], v[212:215], a[24:27], v[144:147]\n"
    "global_load_lds_dwordx4 v121, s[64:65] offset:1024 sc1\n"
    "v_mfma_f32_16x16x32_bf16 v[148:151], v[212:215], a[88:91], v[148:151]\n"
    "s_lshr_b32 s58, s51, 1\n"
    "s_lshr_b32 s58, s52, s58\n"
    "s_and_b32 s58, s58, 1\n"
    "v_mfma_f32_16x16x32_bf16 v[152:155], v[212:215], a[152:155], v[152:155]\n"
    "s_lshl_b32 s58, s58, 13\n"
    "s_lshl_b32 s59, s51, 20\n"
    "s_add_u32 s58, s58, s59\n"
    "v_mfma_f32_16x16x32_bf16 v[156:159], v[212:215], a[216:219], v[156:159]\n"
    "s_add_u32 s58, s58, 0x7480000\n"
    "s_add_u32 s46, s40, s58\n"
    "s_addc_u32 s47, s41, 0\n"
    "s_waitcnt lgkmcnt(4)\n"
    "v_mfma_f32_16x16x32_bf16 v[128:131], v[216:219], a[28:31], v[128:131]\n"
    "ds_read_b128 v[208:211], v122 offset:10240\n"
    "v_mfma_f32_16x16x32_bf16 v[132:135], v[216:219], a[92:95], v[132:135]\n"
    "ds_read_b128 v[212:215], v122 offset:26624\n"
    "v_mfma_f32_16x16x32_bf16 v[136:139], v[216:219], a[156:159], v[136:139]\n"
    "v_mfma_f32_16x16x32_bf16 v[140:143], v[216:219], a[220:223], v[140:143]\n"
    "v_mfma_f32_16x16x32_bf16 v[144:147], v[220:223], a[28:31], v[144:147]\n"
    "global_load_lds_dwordx4 v121, s[64:65] offset:2048 sc1\n"
    "v_mfma_f32_16x16x32_bf16 v[148:151], v[220:223], a[92:95], v[148:151]\n"
    "s_mov_b64 s[44:45], s[66:67]\n"
    "s_add_u32 s66, s66, 0x80000\n"
    "s_addc_u32 s67, s67, 0\n"
    "v_mfma_f32_16x16x32_bf16 v[152:155], v[220:223], a[156:159], v[152:155]\n"
    "v_mfma_f32_16x16x32_bf16 v[156:159], v[220:223], a[220:223], v[156:159]\n"
    "s_waitcnt lgkmcnt(4)\n"
    "v_mfma_f32_16x16x32_bf16 v[128:131], v[192:195], a[32:35], v[128:131]\n"
    "ds_read_b128 v[216:219], v122 offset:11264\n"
    "v_mfma_f32_16x16x32_bf16 v[132:135], v[192:195], a[96:99], v[132:135]\n"
    "ds_read_b128 v[220:223], v122 offset:27648\n"
    "v_mfma_f32_16x16x32_bf16 v[136:139], v[192:195], a[160:163], v[136:139]\n"
    "v_mfma_f32_16x16x32_bf16 v[140:143], v[192:195], a[224:227], v[140:143]\n"
    "v_mfma_f32_16x16x32_bf16 v[144:147], v[196:199], a[32:35], v[144:147]\n"
    "global_load_lds_dwordx4 v121, s[64:65] offset:3072 sc1\n"
    "v_mfma_f32_16x16x32_bf16 v[148:151], v[196:199], a[96:99], v[148:151]\n"
    "v_mfma_f32_16x16x32_bf16 v[152:155], v[196:199], a[160:163], v[152:155]\n"
    "v_mfma_f32_16x16x32_bf16 v[156:159], v[196:199], a[224:227], v[156:159]\n"
    "s_waitcnt lgkmcnt(4)\n"
    "v_mfma_f32_16x16x32_bf16 v[128:131], v[200:203], a[36:39], v[128:131]\n"
    "ds_read_b128 v[192:195], v122 offset:12288\n"
    "v_mfma_f32_16x16x32_bf16 v[132:135], v[200:203], a[100:103], v[132:135]\n"
    "ds_read_b128 v[196:199], v122 offset:28672\n"
    "v_mfma_f32_16x16x32_bf16 v[136:139], v[200:203], a[164:167], v[136:139]\n"
    "s_add_u32 m0, s50, 4096\n"
    "s_add_u32 s64, s48, 4096\n"
    "s_addc_u32 s65, s49, 0\n"
    "v_mfma_f32_16x16x32_bf16 v[140:143], v[200:203], a[228:231], v[140:143]\n"
    "v_mfma_f32_16x16x32_bf16 v[144:147], v[204:207], a[36:39], v[144:147]\n"
    "global_load_lds_dwordx4 v121, s[64:65] offset:0 sc1\n"
    "v_mfma_f32_16x16x32_bf16 v[148:151], v[204:207], a[100:103], v[148:151]\n"
    "v_mfma_f32_16x16x32_bf16 v[152:155], v[204:207], a[164:167], v[152:155]\n"
    "v_mfma_f32_16x16x32_bf16 v[156:159], v[204:207], a[228:231], v[156:159]\n"
    "s_waitcnt lgkmcnt(4)\n"
    "v_mfma_f32_16x16x32_bf16 v[128:131], v[208:211], a[40:43], v[128:131]\n"
    "ds_read_b128 v[200:203], v122 offset:13312\n"
    "v_mfma_f32_16x16x32_bf16 v[132:135], v[208:211], a[104:107], v[132:135]\n"
    "ds_read_b128 v[204:207], v122 offset:29696\n"
    "v_mfma_f32_16x16x32_bf16 v[136:139], v[208:211], a[168:171], v[136:139]\n"
    "v_mfma_f32_16x16x32_bf16 v[140:143], v[208:211], a[232:235], v[140:143]\n"
    "v_mfma_f32_16x16x32_bf16 v[144:147], v[212:215], a[40:43], v[144:147]\n"
    "global_load_lds_dwordx4 v121, s[64:65] offset:1024 sc1\n"
    "v_mfma_f32_16x16x32_bf16 v[148:151], v[212:215], a[104:107], v[148:151]\n"
    "v_mfma_f32_16x16x32_bf16 v[152:155], v[212:215], a[168:171], v[152:155]\n"
    "v_mfma_f32_16x16x32_bf16 v[156:159], v[212:215], a[232:235], v[156:159]\n"
    "s_waitcnt lgkmcnt(4)\n"
    "v_mfma_f32_16x16x32_bf16 v[128:131], v[216:219], a[44:47], v[128:131]\n"
    "ds_read_b128 v[208:211], v122 offset:14336\n"
    "v_mfma_f32_16x16x32_bf16 v[132:135], v[216:219], a[108:111], v[132:135]\n"
    "ds_read_b128 v[212:215], v122 offset:30720\n"
    "v_mfma_f32_16x16x32_bf16 v[136:139], v[216:219], a[172:175], v[136:139]\n"
    "v_mfma_f32_16x16x32_bf16 v[140:143], v[216:219], a[236:239], v[140:143]\n"
    "v_mfma_f32_16x16x32_bf16 v[144:147], v[220:223], a[44:47], v[144:147]\n"
    "global_load_lds_dwordx4 v121, s[64:65] offset:2048 sc1\n"
    "v_mfma_f32_16x16x32_bf16 v[148:151], v[220:223], a[108:111], v[148:151]\n"
    "v_mfma_f32_16x16x32_bf16 v[152:155], v[220:223], a[172:175], v[152:155]\n"
    "v_mfma_f32_16x16x32_bf16 v[156:159], v[220:223], a[236:239], v[156:159]\n"
    "s_waitcnt lgkmcnt(4)\n"
    "v_mfma_f32_16x16x32_bf16 v[128:131], v[192:195], a[48:51], v[128:131]\n"
    "ds_read_b128 v[216:219], v122 offset:15360\n"
    "v_mfma_f32_16x16x32_bf16 v[132:135], v[192:195], a[112:115], v[132:135]\n"
    "ds_read_b128 v[220:223], v122 offset:31744\n"
    "v_mfma_f32_16x16x32_bf16 v[136:139], v[192:195], a[176:179], v[136:139]\n"
    "v_mfma_f32_16x16x32_bf16 v[140:143], v[192:195], a[240:243], v[140:143]\n"
    "v_mfma_f32_16x16x32_bf16 v[144:147], v[196:199], a[48:51], v[144:147]\n"
    "global_load_lds_dwordx4 v121, s[64:65] offset:3072 sc1\n"
    "v_mfma_f32_16x16x32_bf16 v[148:151], v[196:199], a[112:115], v[148:151]\n"
    "v_mfma_f32_16x16x32_bf16 v[152:155], v[196:199], a[176:179], v[152:155]\n"
    "v_mfma_f32_16x16x32_bf16 v[156:159], v[196:199], a[240:243], v[156:159]\n"
    "s_waitcnt lgkmcnt(4)\n"
    "v_mfma_f32_16x16x32_bf16 v[128:131], v[200:203], a[52:55], v[128:131]\n"
    "s_waitcnt vmcnt(8) lgkmcnt(0)\n"
    "s_barrier\n"
    "v_mfma_f32_16x16x32_bf16 v[132:135], v[200:203], a[116:119], v[132:135]\n"
    "ds_read_b128 v[192:195], v123 offset:0\n"
    "v_mfma_f32_16x16x32_bf16 v[136:139], v[200:203], a[180:183], v[136:139]\n"
    "ds_read_b128 v[196:199], v123 offset:16384\n"
    "s_add_u32 m0, s50, 8192\n"
    "s_add_u32 s64, s48, 8192\n"
    "s_addc_u32 s65, s49, 0\n"
    "v_mfma_f32_16x16x32_bf16 v[140:143], v[200:203], a[244:247], v[140:143]\n"
    "v_mfma_f32_16x16x32_bf16 v[144:147], v[204:207], a[52:55], v[144:147]\n"
    "global_load_lds_dwordx4 v121, s[64:65] offset:0 sc1\n"
    "v_mfma_f32_16x16x32_bf16 v[148:151], v[204:207], a[116:119], v[148:151]\n"
    "s_cmp_lg_u32 s56, 0\n"
    "s_cbranch_scc1 G_f2%=\n"
    "s_or_b32 s58, s42, s51\n"
    "s_mov_b64 exec, 1\n"
    "v_mov_b32 v116, s58\n"
    "global_store_dwordx2 v118, v[116:117], s[54:55] sc1\n"
    "s_mov_b64 exec, -1\n"
    "G_f2%=:\n"
    "v_mfma_f32_16x16x32_bf16 v[152:155], v[204:207], a[180:183], v[152:155]\n"
    "v_mfma_f32_16x16x32_bf16 v[156:159], v[204:207], a[244:247], v[156:159]\n"
    "s_waitcnt lgkmcnt(4)\n"
    "v_mfma_f32_16x16x32_bf16 v[128:131], v[208:211], a[56:59], v[128:131]\n"
    "ds_read_b128 v[200:203], v123 offset:1024\n"
    "v_mfma_f32_16x16x32_bf16 v[132:135], v[208:211], a[120:123], v[132:135]\n"
    "ds_read_b128 v[204:207], v123 offset:17408\n"
    "v_mfma_f32_16x16x32_bf16 v[136:139], v[208:211], a[184:187], v[136:139]\n"
    "v_mfma_f32_16x16x32_bf16 v[140:143], v[208:211], a[248:251], v[140:143]\n"
    "v_mfma_f32_16x16x32_bf16 v[144:147], v[212:215], a[56:59], v[144:147]\n"
    "global_load_lds_dwordx4 v121, s[64:65] offset:1024 sc1\n"
    "v_mfma_f32_16x16x32_bf16 v[148:151], v[212:215], a[120:123], v[148:151]\n"
    "v_mfma_f32_16x16x32_bf16 v[152:155], v[212:215], a[184:187], v[152:155]\n"
    "v_mfma_f32_16x16x32_bf16 v[156:159], v[212:215], a[248:251], v[156:159]\n"
    "s_add_u32 s51, s51, 1\n"
    "s_waitcnt lgkmcnt(4)\n"
    "v_mfma_f32_16x16x32_bf16 v[128:131], v[216:219], a[60:63], v[128:131]\n"
    "ds_read_b128 v[208:211], v123 offset:2048\n"
    "v_mfma_f32_16x16x32_bf16 v[132:135], v[216:219], a[124:127], v[132:135]\n"
    "ds_read_b128 v[212:215], v123 offset:18432\n"
    "v_mfma_f32_16x16x32_bf16 v[136:139], v[216:219], a[188:191], v[136:139]\n"
    "v_mfma_f32_16x16x32_bf16 v[140:143], v[216:219], a[252:255], v[140:143]\n"
    "v_mfma_f32_16x16x32_bf16 v[144:147], v[220:223], a[60:63], v[144:147]\n"
    "global_load_lds_dwordx4 v121, s[64:65] offset:2048 sc1\n"
    "v_mfma_f32_16x16x32_bf16 v[148:151], v[220:223], a[124:127], v[148:151]\n"
    "v_mfma_f32_16x16x32_bf16 v[152:155], v[220:223], a[188:191], v[152:155]\n"
    "v_mfma_f32_16x16x32_bf16 v[156:159], v[220:223], a[252:255], v[156:159]\n"
    "s_add_u32 s58, s51, -3\n"
    "s_cmp_gt_u32 s58, 28\n"
    "s_waitcnt lgkmcnt(4)\n"
    "s_cbranch_scc0 G_loop%=\n"
    "s_branch G_pc%=\n"
    "G_pw%=:\n"
    "s_add_u32 s58, s40, 0x312000\n"
    "s_addc_u32 s59, s41, 0\n"
    "global_load_dwordx2 v[114:115], v112, s[58:59] sc1\n"
    "s_waitcnt vmcnt(0)\n"
    "G_pc%=:\n"
    "v_xor_b32 v114, s42, v114\n"
    "v_xor_b32 v115, s43, v115\n"
    "v_cmp_eq_u64 s[52:53], 2, v[114:115]\n"
    "v_cmp_eq_u64 s[58:59], 1, v[114:115]\n"
    "s_nop 0\n"
    "s_or_b64 s[58:59], s[58:59], s[52:53]\n"
    "s_cmp_eq_u64 s[58:59], -1\n"
    "s_cbranch_scc1 G_pok%=\n"
    "s_sleep 4\n"
    "s_add_u32 s57, s57, 1\n"
    "s_cmp_lt_u32 s57, 0x40000\n"
    "s_cbranch_scc1 G_pw%=\n"
    "G_pok%=:\n"
    "s_and_b32 s58, s52, 1\n"
    "s_lshl_b32 s58, s58, 13\n"
    "s_add_u32 s58, s58, 0x7480000\n"
    "s_add_u32 s46, s40, s58\n"
    "s_addc_u32 s47, s41, 0\n"
    "global_store_dwordx4 v119, v[124:127], s[46:47] sc1\n"
    "global_store_dwordx4 v120, v[124:127], s[46:47] sc1\n"
    "s_nop 7\n"
    "v_cvt_pk_bf16_f32 v224, v128, v129\n"
    "v_cvt_pk_bf16_f32 v225, v130, v131\n"
    "v_cvt_pk_bf16_f32 v226, v144, v145\n"
    "v_cvt_pk_bf16_f32 v227, v146, v147\n"
    "v_cvt_pk_bf16_f32 v228, v132, v133\n"
    "v_cvt_pk_bf16_f32 v229, v134, v135\n"
    "v_cvt_pk_bf16_f32 v230, v148, v149\n"
    "v_cvt_pk_bf16_f32 v231, v150, v151\n"
    "v_cvt_pk_bf16_f32 v232, v136, v137\n"
    "v_cvt_pk_bf16_f32 v233, v138, v139\n"
    "v_cvt_pk_bf16_f32 v234, v152, v153\n"
    "v_cvt_pk_bf16_f32 v235, v154, v155\n"
    "v_cvt_pk_bf16_f32 v236, v140, v141\n"
    "v_cvt_pk_bf16_f32 v237, v142, v143\n"
    "v_cvt_pk_bf16_f32 v238, v156, v157\n"
    "v_cvt_pk_bf16_f32 v239, v158, v159\n"
    "global_store_dwordx4 v121, v[224:227], s[44:45] offset:0 sc1\n"
    "global_store_dwordx4 v121, v[228:231], s[44:45] offset:1024 sc1\n"
    "global_store_dwordx4 v121, v[232:235], s[44:45] offset:2048 sc1\n"
    "global_store_dwordx4 v121, v[236:239], s[44:45] offset:3072 sc1\n"
    "s_add_u32 s46, s40, 0xaa00000\n"
    "s_addc_u32 s47, s41, 0\n"
    "s_waitcnt vmcnt(0)\n"
    "s_barrier\n"
    "s_cmp_lg_u32 s56, 0\n"
    "s_cbranch_scc1 G_f3%=\n"
    "s_or_b32 s58, s42, s51\n"
    "s_mov_b64 exec, 1\n"
    "v_mov_b32 v116, s58\n"
    "global_store_dwordx2 v118, v[116:117], s[54:55] sc1\n"
    "s_mov_b64 exec, -1\n"
    "G_f3%=:\n"
    "s_branch G_loop%=\n"
    "G_t1%=:\n"
    "global_store_dwordx4 v119, v[124:127], s[46:47] sc1\n"
    "global_store_dwordx4 v120, v[124:127], s[46:47] sc1\n"
    "s_nop 7\n"
    "v_cvt_pk_bf16_f32 v224, v160, v161\n"
    "v_cvt_pk_bf16_f32 v225, v162, v163\n"
    "v_cvt_pk_bf16_f32 v226, v176, v177\n"
    "v_cvt_pk_bf16_f32 v227, v178, v179\n"
    "v_cvt_pk_bf16_f32 v228, v164, v165\n"
    "v_cvt_pk_bf16_f32 v229, v166, v167\n"
    "v_cvt_pk_bf16_f32 v230, v180, v181\n"
    "v_cvt_pk_bf16_f32 v231, v182, v183\n"
    "v_cvt_pk_bf16_f32 v232, v168, v169\n"
    "v_cvt_pk_bf16_f32 v233, v170, v171\n"
    "v_cvt_pk_bf16_f32 v234, v184, v185\n"
    "v_cvt_pk_bf16_f32 v235, v186, v187\n"
    "v_cvt_pk_bf16_f32 v236, v172, v173\n"
    "v_cvt_pk_bf16_f32 v237, v174, v175\n"
    "v_cvt_pk_bf16_f32 v238, v188, v189\n"
    "v_cvt_pk_bf16_f32 v239, v190, v191\n"
    "global_store_dwordx4 v121, v[224:227], s[44:45] offset:0 sc1\n"
    "global_store_dwordx4 v121, v[228:231], s[44:45] offset:1024 sc1\n"
    "global_store_dwordx4 v121, v[232:235], s[44:45] offset:2048 sc1\n"
    "global_store_dwordx4 v121, v[236:239], s[44:45] offset:3072 sc1\n"
    "s_add_u32 s46, s40, 0xaa00000\n"
    "s_addc_u32 s47, s41, 0\n"
    "s_waitcnt vmcnt(0)\n"
    "s_barrier\n"
    "s_cmp_lg_u32 s56, 0\n"
    "s_cbranch_scc1 G_f4%=\n"
    "s_or_b32 s58, s42, s51\n"
    "s_mov_b64 exec, 1\n"
    "v_mov_b32 v116, s58\n"
    "global_store_dwordx2 v118, v[116:117], s[54:55] sc1\n"
    "s_mov_b64 exec, -1\n"
    "G_f4%=:\n"
    "s_cmp_eq_u32 s51, 32\n"
    "s_cbranch_scc1 G_end%=\n"
    "s_add_u32 s58, s40, 0x20e000\n"
    "s_addc_u32 s59, s41, 0\n"
    "s_add_u32 s60, s40, 0x200200\n"
    "s_addc_u32 s61, s41, 0\n"
    "s_or_b32 s62, s42, 1\n"
    "s_mov_b32 s63, s43\n"
    "v_lshlrev_b32 v224, 3, v121\n"
    "G_xw1%=:\n"
    "global_load_dwordx2 v[226:227], v224, s[58:59] sc1\n"
    "G_xc1%=:\n"
    "s_waitcnt vmcnt(0)\n"
    "v_cmp_ne_u64 vcc, s[62:63], v[226:227]\n"
    "s_nop 0\n"
    "s_cbranch_vccz G_xok1%=\n"
    "s_sleep 4\n"
    "s_add_u32 s57, s57, 1\n"
    "s_and_b32 s68, s57, 0xff\n"
    "s_cbranch_scc1 G_xw1%=\n"
    "global_load_dwordx2 v[226:227], v118, s[60:61] sc1\n"
    "s_waitcnt vmcnt(0)\n"
    "v_cmp_eq_u64 vcc, s[42:43], v[226:227]\n"
    "s_nop 0\n"
    "s_cbranch_vccnz G_xok1%=\n"
    "s_cmp_lt_u32 s57, 0x40000\n"
    "s_cbranch_scc1 G_xw1%=\n"
    "v_mov_b32 v116, s42\n"
    "global_store_dwordx2 v118, v[116:117], s[60:61] sc1\n"
    "G_xok1%=:\n"
    "s_mov_b32 vcc_lo, 0xcccccccc\n"
    "s_mov_b32 vcc_hi, 0xcccccccc\n"
    "s_branch G_even%=\n"
    "G_end%=:\n"
    "s_waitcnt lgkmcnt(0)\n"
    :: GIN : GCLOB);
  return T;
}

__device__ __forceinline__ void xa_phase(char* ws, int grp, int h, u64 T, int lane) {
  const int l15 = lane & 15, g = lane >> 4;
  const float scale = 0.08838834764831845f;
  const __amdgpu_buffer_rsrc_t rs = __builtin_amdgcn_make_buffer_rsrc(ws, 0, (int)(164 * MBY), 0x00020000);
  auto ld16 = [&](int off) { return __builtin_bit_cast(bf16x8, __builtin_amdgcn_raw_buffer_load_b128(rs, off, 0, 16)); };
#pragma unroll 1
  for (int s = grp; s < NSEG; s += 4) {
    wait_flag<8>(ws, (unsigned)OFF_RDY + ((lane < 3 ? lane : 0) * 32 + h) * 128, T, (unsigned)(s >> 1) + 1u);
    const int qp = (int)OFF_QF + (s * 32 + h) * 8192 + lane * 16;
    const int kp = (int)OFF_KF + (s * 32 + h) * 8192 + ((l15 >> 1) & 1) * 1024 + (g * 16 + (l15 & 13)) * 16;
    const int vp = (int)OFF_VF + (s * 32 + h) * 8192 + lane * 16;
    const int km = (int)OFF_KM + (s * 32 + h) * 4096;
    const int vm = (int)OFF_VM + (s * 32 + h) * 4096;
    bf16x8 qf[4][2], vf[8];
    f32x4 sc[2][3];
#pragma unroll
    for (int nt = 0; nt < 2; ++nt)
#pragma unroll
      for (int t = 0; t < 3; ++t) sc[nt][t] = (f32x4){0.f, 0.f, 0.f, 0.f};
#pragma unroll
    for (int ks = 0; ks < 4; ++ks) {
      bf16x8 kf[2];
#pragma unroll
      for (int nt = 0; nt < 2; ++nt) qf[ks][nt] = ld16(qp + ks * 2048 + nt * 1024);
#pragma unroll
      for (int t = 0; t < 2; ++t) kf[t] = ld16(kp + (ks >> 1) * 4096 + (ks & 1) * 32 + t * 2048);
#pragma unroll
      for (int nt = 0; nt < 2; ++nt)
#pragma unroll
        for (int t = 0; t < 2; ++t) sc[nt][t] = __builtin_amdgcn_mfma_f32_16x16x32_bf16(kf[t], qf[ks][nt], sc[nt][t], 0, 0, 0);
    }
#pragma unroll
    for (int e = 0; e < 8; ++e) vf[e] = ld16(vp + e * 1024);
    float mt[2], lt[2];
    f32x4 acc[2][8];
#pragma unroll
    for (int nt = 0; nt < 2; ++nt) {
      const int qi = nt * 16 + l15;
      float v[2][4], m = -3.0e38f;
#pragma unroll
      for (int t = 0; t < 2; ++t)
#pragma unroll
        for (int r = 0; r < 4; ++r) {
          v[t][r] = (t * 16 + g * 4 + r <= qi) ? rbf(rbf(sc[nt][t][r]) * scale) : -3.0e38f;
          m = fmaxf(m, v[t][r]);
        }
      m = max_g(m);
#pragma unroll
      for (int t = 0; t < 2; ++t)
#pragma unroll
        for (int r = 0; r < 4; r += 2) {
          v[t][r] = (t * 16 + g * 4 + r <= qi) ? __expf(v[t][r] - m) : 0.f;
          v[t][r + 1] = (t * 16 + g * 4 + r + 1 <= qi) ? __expf(v[t][r + 1] - m) : 0.f;
          rbf2(v[t][r], v[t][r + 1]);
        }
      mt[nt] = m;
      lt[nt] = sum_g(((v[0][0] + v[0][1]) + (v[0][2] + v[0][3])) + ((v[1][0] + v[1][1]) + (v[1][2] + v[1][3])));
      const bf16x8 pf = mk8(pk2(v[0][0], v[0][1]), pk2(v[0][2], v[0][3]), pk2(v[1][0], v[1][1]), pk2(v[1][2], v[1][3]));
#pragma unroll
      for (int e = 0; e < 8; ++e) acc[nt][e] = __builtin_amdgcn_mfma_f32_16x16x32_bf16(vf[e], pf, (f32x4){0.f, 0.f, 0.f, 0.f}, 0, 0, 0);
    }
#pragma unroll
    for (int e = 0; e < 8; ++e) asm volatile("" : "+v"(acc[0][e]), "+v"(acc[1][e]));
    asm volatile("" : "+v"(mt[0]), "+v"(mt[1]), "+v"(lt[0]), "+v"(lt[1]));
    if (h == 5) TLX(s * 8);
    wait_flag<PSL>(ws, (unsigned)OFF_FLAGS + (s * 64 + h + (lane & 1) * 32) * 128, T, 1u);
    if (s == 63) TL(4);
    if (h == 5) TLX(s * 8 + 1);
    bf16x8 k2[4];
    unsigned mv[4][2];
#pragma unroll
    for (int ks = 0; ks < 4; ++ks) k2[ks] = ld16(km + (ks >> 1) * 1024 + (g * 16 + ((l15 >> 3) | ((l15 >> 1) & 2)) * 4 + (ks & 1) * 2 + (l15 & 1)) * 16);
#pragma unroll
    for (int w = 0; w < 4; ++w) { const auto t = __builtin_amdgcn_raw_buffer_load_b64(rs, vm + w * 512 + lane * 8, 0, 16); mv[w][0] = t[0]; mv[w][1] = t[1]; }
#pragma unroll
    for (int ks = 0; ks < 4; ++ks)
#pragma unroll
      for (int nt = 0; nt < 2; ++nt) sc[nt][2] = __builtin_amdgcn_mfma_f32_16x16x32_bf16(k2[ks], qf[ks][nt], sc[nt][2], 0, 0, 0);
#pragma unroll
    for (int nt = 0; nt < 2; ++nt) {
      float a = sc[nt][2][0], b = sc[nt][2][1];
      rbf2(a, b); a *= scale; b *= scale; rbf2(a, b);
      const float m = fmaxf(mt[nt], max_g(fmaxf(a, b)));
      const float al = __expf(mt[nt] - m);
      a = __expf(a - m); b = __expf(b - m);
      const float inv = __builtin_amdgcn_rcpf(al * lt[nt] + sum_g(a + b));
      const float sa = al * inv;
      const bf16x8 pf1 = mk8(pk2(a * inv, b * inv), 0u, 0u, 0u);
#pragma unroll
      for (int e = 0; e < 8; ++e) acc[nt][e] = __builtin_amdgcn_mfma_f32_16x16x32_bf16(mk8(mv[e >> 1][e & 1], 0u, 0u, 0u), pf1, acc[nt][e] * sa, 0, 0, 0);
#pragma unroll
      for (int w = 0; w < 4; ++w) {
        const f32x4 a0 = acc[nt][2 * w], a1 = acc[nt][2 * w + 1];
        u32x4 o; o.x = pk2(a0[0], a0[1]); o.y = pk2(a1[0], a1[1]); o.z = pk2(a0[2], a0[3]); o.w = pk2(a1[2], a1[3]);
        store16_sc1(ws + OFF_O, (unsigned)(((s * 32 + h) * 8 + nt * 4 + w) * 1024 + lane * 16), o);
      }
    }
    asm volatile("s_waitcnt vmcnt(0)" ::: "memory");
    if (lane == 0) set_flag(ws, OFF_OFL + (s * 32 + h) * 128, T, 1);
    if (h == 5) TLX(s * 8 + 2);
  }
}

__device__ __forceinline__ void op_phase(char* ws, const u16* __restrict__ x, const u16* __restrict__ w_r, u16* out, const u16* __restrict__ Wo, int grp, int ks4, int cs, u64 T, char* lds, int tid) {
  const int lane = tid & 63, wv = __builtin_amdgcn_readfirstlane(tid >> 6), l15 = lane & 15, g = lane >> 4;
  const __amdgpu_buffer_rsrc_t rs = __builtin_amdgcn_make_buffer_rsrc(ws, 0, (int)(164 * MBY), 0x00020000);
  const int h0 = ks4 * 8 + wv * 2;
  bf16x8 wo[2][32];
#pragma unroll
  for (int hh = 0; hh < 2; ++hh) {
#pragma unroll
    for (int i = 0; i < 32; ++i) {
      const int r = i * 4 + g, f = (r & 1) | ((r >> 1) & 2) | ((r >> 2) & 4);
      __builtin_amdgcn_global_load_lds((const GAS void*)(Wo + (size_t)((h0 + hh) * DH + r) * DM + cs * 128 + ((l15 ^ (f << 1)) * 8)),
                                       (LAS void*)(lds + wv * 32768 + i * 1024), 16, 0, 0);
    }
    asm volatile("s_waitcnt vmcnt(0)" ::: "memory");
    const int qq = l15 >> 2, f = (qq & 1) | ((g & 1) << 1) | ((qq >> 1) << 2);
    const char* rp = lds + wv * 32768 + (g * 4 + (qq & 1) + (qq >> 1) * 16) * 256 + (l15 & 3) * 8;
#pragma unroll
    for (int j = 0; j < 8; ++j)
#pragma unroll
      for (int ks = 0; ks < 4; ++ks) {
        const char* p = rp + ks * 8192 + ((j ^ f) * 32);
        wo[hh][j * 4 + ks] = tr_frag(p, p + 512);
      }
#pragma unroll
    for (int f2 = 0; f2 < 32; ++f2) asm volatile("" : "+a"(wo[hh][f2]));
    asm volatile("s_waitcnt lgkmcnt(0)" ::: "memory");
  }
  __syncthreads();
  constexpr int RS = 528, RW = 32 * RS;
  auto ln2 = [&](int s, bool fresh) {
      const int row = s * 32 + (ks4 * 4 + cs) * 2 + (wv & 1);
      const u32x4 xv = *(const u32x4*)(x + (size_t)row * DM + lane * 8);
      float wr[8];
      unpack8(*(const u32x4*)(w_r + lane * 8), wr);
      if (fresh) __builtin_amdgcn_s_sleep(HSL);
      wait_flag<PSL>(ws, (unsigned)OFF_HFL + s * 2048 + l15 * 128, T, 1u);
      if (s == 63) TL(7);
      float f[8], y[8];
      load_h(rs, xv, row, lane, f);
      ln_row(f, y);
      float r = 0.f;
#pragma unroll
      for (int i = 0; i < 8; ++i) r += y[i] * wr[i];
      r = wave_sum(r);
      u32x4 z;
      z.x = pk2(f[0] + y[0], f[1] + y[1]); z.y = pk2(f[2] + y[2], f[3] + y[3]); z.z = pk2(f[4] + y[4], f[5] + y[5]); z.w = pk2(f[6] + y[6], f[7] + y[7]);
      const unsigned rw = (pk2(r, r) & 0xffff0000u) | 1u;
      if (lane < 8 && row >= NSPEC) __hip_atomic_store((gu32*)(ws + OFF_RC2 + (lane * 64 + ((row - NSPEC) >> 2)) * 128 + (row & 3) * 4), rw, RLX_AGENT);
      store16_sc1(out, (unsigned)(row * 1024 + lane * 16), z);
      asm volatile("s_waitcnt vmcnt(0)" ::: "memory");
      if (lane == 0) __hip_atomic_store((gu32*)(ws + OFF_RC + (row >> 2) * 128 + (row & 3) * 4), rw, RLX_AGENT);
      if ((row & 31) == 0) TLX(s * 8 + 6);
  };
  int pend = -1, turn = 0;
  bool have = false;
  bf16x8 of[16];
  auto load_o = [&](int s) {
    const int ob = (int)OFF_O + (s * 32 + h0) * 8192 + lane * 16;
#pragma unroll
    for (int f = 0; f < 16; ++f) of[f] = __builtin_bit_cast(bf16x8, __builtin_amdgcn_raw_buffer_load_b128(rs, ob + f * 1024, 0, 16));
  };
#pragma unroll 1
  for (int s = grp, sn; s < NSEG; s = sn) {
    sn = s + 6 < NSIX ? s + 6 : (s < NSIX ? (grp < 4 ? NSIX + grp : NSEG) : s + 4);
    f32x4 acc[2][8];
#pragma unroll
    for (int i = 0; i < 2; ++i)
#pragma unroll
      for (int j = 0; j < 8; ++j) { acc[i][j] = (f32x4){0.f, 0.f, 0.f, 0.f}; asm volatile("" : "+v"(acc[i][j])); }
    if (!have) {
      if (pend >= 0) { ln2(pend, true); pend = -1; }
      wait_flag<PSL>(ws, (unsigned)OFF_OFL + (s * 32 + h0 + (lane & 1)) * 128, T, 1u);
      if (s == 63) TL(1);
      load_o(s);
    }
    if (ks4 == 0 && cs == 0 && tid == 0) TLX(s * 8 + 3);
    const u64 nf = __hip_atomic_load((gu64*)(ws + OFF_OFL + (min(sn, NSEG - 1) * 32 + h0 + (lane & 1)) * 128), RLX_AGENT);
    __builtin_amdgcn_sched_barrier(0);
#pragma unroll
    for (int f = 0; f < 16; ++f)
#pragma unroll
      for (int j = 0; j < 8; ++j)
        asm volatile("v_mfma_f32_16x16x32_bf16 %0, %1, %2, %0" : "+v"(acc[(f >> 2) & 1][j]) : "a"(wo[f >> 3][j * 4 + (f & 3)]), "v"(of[f]));
    __builtin_amdgcn_sched_barrier(0);
    have = sn < NSEG && __all(nf == T + 1);
    if (have) load_o(sn);
    asm volatile("s_nop 7\n\ts_nop 3" : "+v"(acc[1][4]), "+v"(acc[1][5]), "+v"(acc[1][6]), "+v"(acc[1][7]), "+v"(acc[1][0]), "+v"(acc[1][1]), "+v"(acc[1][2]), "+v"(acc[1][3]));
    __builtin_amdgcn_sched_barrier(0);
    if (s == 63) TL(3);
    if (ks4 == 0 && cs == 0 && tid == 0) TLX(s * 8 + 4);
#pragma unroll
    for (int tt = 0; tt < 2; ++tt)
#pragma unroll
      for (int j = 0; j < 8; ++j) *(f32x4*)(lds + wv * RW + (tt * 16 + l15) * RS + j * 64 + g * 16) = acc[tt][j];
    asm volatile("s_waitcnt lgkmcnt(0)" ::: "memory");
    __builtin_amdgcn_s_barrier();
    asm volatile("" ::: "memory");
#pragma unroll
    for (int i = 0; i < 4; ++i) {
      const int row = i * 8 + (tid >> 5);
      const char* p = lds + row * RS + (tid & 31) * 16;
      const f32x4 a = (*(const f32x4*)p + *(const f32x4*)(p + RW)) + (*(const f32x4*)(p + 2 * RW) + *(const f32x4*)(p + 3 * RW));
      store16_sc1(ws + OFF_HP, (unsigned)(((s * 4 + ks4) * 4 + cs) * 16384 + i * 4096 + tid * 16), __builtin_bit_cast(u32x4, a));
    }
    asm volatile("s_waitcnt vmcnt(0)" ::: "memory");
    __syncthreads();
    if (tid == 0) set_flag(ws, OFF_HFL + (s * 16 + ks4 * 4 + cs) * 128, T, 1);
    if (s == 63) TL(6);
    if (ks4 == 0 && cs == 0 && tid == 0) TLX(s * 8 + 5);
    if (turn == 0 && pend >= 0) { ln2(pend, false); pend = -1; }
    if ((wv >> 1) == turn) pend = s;
    turn ^= 1;
  }
  if (pend >= 0) ln2(pend, true);
}

__device__ __forceinline__ unsigned rkey(unsigned w, int idx) { return ((w ^ (((int)w >> 31) | 0x80000000u)) & 0xffff0000u) | (unsigned)(0xffff - idx); }
__device__ __forceinline__ int key_row(unsigned k) { return (0xffff - (int)(k & 0xffffu)) & 2047; }
__device__ __forceinline__ float key_val(unsigned k) { k &= 0xffff0000u; return __builtin_bit_cast(float, (k & 0x80000000u) ? k ^ 0x80000000u : (~k) & 0xffff0000u); }
__device__ __forceinline__ unsigned wave_umax(unsigned v) {
  v = max(v, (unsigned)__builtin_amdgcn_update_dpp(0, (int)v, 0x121, 0xf, 0xf, false)); v = max(v, (unsigned)__builtin_amdgcn_update_dpp(0, (int)v, 0x122, 0xf, 0xf, false));
  v = max(v, (unsigned)__builtin_amdgcn_update_dpp(0, (int)v, 0x124, 0xf, 0xf, false)); v = max(v, (unsigned)__builtin_amdgcn_update_dpp(0, (int)v, 0x128, 0xf, 0xf, false));
  return max(max((unsigned)__builtin_amdgcn_readlane((int)v, 0), (unsigned)__builtin_amdgcn_readlane((int)v, 16)),
             max((unsigned)__builtin_amdgcn_readlane((int)v, 32), (unsigned)__builtin_amdgcn_readlane((int)v, 48)));
}
__device__ __forceinline__ void push2(unsigned& a, unsigned& b, unsigned k) { b = max(b, min(a, k)); a = max(a, k); }
__device__ __forceinline__ void wave_top2(unsigned& a, unsigned& b) {
  const unsigned m = wave_umax(a);
  b = wave_umax(a == m ? b : a); a = m;
}
__device__ __forceinline__ void top2(char* ws, u64 T, int nrows, int tid, unsigned& k0, unsigned& k1, unsigned* sk) {
  const __amdgpu_buffer_rsrc_t rs = __builtin_amdgcn_make_buffer_rsrc(ws, 0, (int)(164 * MBY), 0x00020000);
  const int lane = tid & 63, wv = tid >> 6;
  const bool use0 = tid * 4 < nrows, use1 = (256 + tid) * 4 < nrows;
  u32x4 c[2];
  unsigned spins = 0;
  if (wv == 0) { wait_flag<12>(ws, (unsigned)OFF_HFL + ((nrows >> 5) - 1) * 2048, T, 1u); __builtin_amdgcn_s_sleep(48); }
  __syncthreads();
  for (;;) {
    c[0] = __builtin_bit_cast(u32x4, __builtin_amdgcn_raw_buffer_load_b128(rs, (int)OFF_RC + (use0 ? tid : 0) * 128, 0, 16));
    c[1] = __builtin_bit_cast(u32x4, __builtin_amdgcn_raw_buffer_load_b128(rs, (int)OFF_RC + (use1 ? 256 + tid : 0) * 128, 0, 16));
    if (__all(c[0].x & c[0].y & c[0].z & c[0].w & c[1].x & c[1].y & c[1].z & c[1].w & 1u)) break;
    __builtin_amdgcn_s_sleep(8);
    if (spin_bail(spins, ws, T)) break;
    asm volatile("" ::: "memory");
  }
  unsigned a = 0, b = 0;
#pragma unroll
  for (int k = 0; k < 2; ++k)
#pragma unroll
    for (int e = 0; e < 4; ++e) { const unsigned w = c[k][e]; push2(a, b, (k == 0 ? use0 : use1) ? rkey(w, (k * 256 + tid) * 4 + e) : 0u); }
  wave_top2(a, b);
  if (lane == 0) { sk[wv * 2] = a; sk[wv * 2 + 1] = b; }
  __syncthreads();
  a = 0; b = 0;
#pragma unroll
  for (int i = 0; i < 8; ++i) push2(a, b, sk[i]);
  k0 = a; k1 = b;
}
__device__ __forceinline__ bool spec_holds(char* ws, u64 T, unsigned xcc, unsigned& k0, unsigned& k1, unsigned* sk, int tid) {
  static_assert(S_ - NSPEC <= 256, "one line per lane");
  if (tid < 64) {
    const __amdgpu_buffer_rsrc_t rs = __builtin_amdgcn_make_buffer_rsrc(ws, 0, (int)(164 * MBY), 0x00020000);
    const bool use = tid < (S_ - NSPEC) / 4;
    u32x4 c;
    unsigned spins = 0;
    for (;;) {
      c = __builtin_bit_cast(u32x4, __builtin_amdgcn_raw_buffer_load_b128(rs, (int)OFF_RC2 + (xcc * 64 + (use ? tid : 0)) * 128, 0, 16));
      if (__all(c.x & c.y & c.z & c.w & 1u)) break;
      __builtin_amdgcn_s_sleep(2);
      if (spin_bail(spins, ws, T)) break;
      asm volatile("" ::: "memory");
    }
    unsigned a = 0, b = 0;
#pragma unroll
    for (int e = 0; e < 4; ++e) { const unsigned w = c[e]; push2(a, b, use ? rkey(w, NSPEC + tid * 4 + e) : 0u); }
    wave_top2(a, b);
    push2(a, b, k0); push2(a, b, k1);
    if (tid == 0) { sk[8] = a; sk[9] = b; }
  }
  __syncthreads();
  const bool same = sk[8] == k0 && sk[9] == k1;
  k0 = sk[8]; k1 = sk[9];
  return same;
}
__device__ __forceinline__ void sel_rows(char* ws, const u16* __restrict__ x, u64 T, int tid, int i0, int i1, float* hs, float* ys, char* yb) {
  const __amdgpu_buffer_rsrc_t rs = __builtin_amdgcn_make_buffer_rsrc(ws, 0, (int)(164 * MBY), 0x00020000);
  const int lane = tid & 63, wv = tid >> 6;
  if (wv < 2) {
    const int row = wv ? i1 : i0;
    for (unsigned spins = 0; __hip_atomic_load((gu32*)(ws + OFF_RC + (row >> 2) * 128 + (row & 3) * 4), RLX_AGENT) == 0u;)
      if (spin_bail(spins, ws, T)) break;
    float f[8], y[8];
    load_h(rs, *(const u32x4*)(x + (size_t)row * DM + lane * 8), row, lane, f);
    ln_row(f, y);
#pragma unroll
    for (int i = 0; i < 8; ++i) { hs[wv * DM + lane * 8 + i] = f[i]; ys[wv * DM + lane * 8 + i] = y[i]; }
    u32x4 o; o.x = pk2(y[0], y[1]); o.y = pk2(y[2], y[3]); o.z = pk2(y[4], y[5]); o.w = pk2(y[6], y[7]);
    *(u32x4*)(yb + wv * 1040 + lane * 16) = o;
  }
  __syncthreads();
}
template <int NF>
__device__ __forceinline__ void load_frags(bf16x8 (&w)[NF], const u16* __restrict__ src, int pitch, char* st, int lane) {
  const int l15 = lane & 15, g = lane >> 4, qq = l15 >> 2;
#pragma unroll
  for (int b = 0; b < NF / 2; b += 16) {
    constexpr int NK = 16;
#pragma unroll
    for (int i = 0; i < NK * 2; ++i) {
      if (b * 2 + i < NF) {
        const int r = i * 16 + (lane >> 2);
        __builtin_amdgcn_global_load_lds((const GAS void*)(src + (size_t)(b * 32 + r) * pitch + (((lane & 3) ^ ((r >> 2) & 2)) * 8)), (LAS void*)(st + i * 1024), 16, 0, 0);
      }
    }
    asm volatile("s_waitcnt vmcnt(0)" ::: "memory");
#pragma unroll
    for (int ks = 0; ks < NK; ++ks)
#pragma unroll
      for (int j = 0; j < 2; ++j)
        if ((b + ks) * 2 < NF) {
          const char* p = st + (ks * 32 + g * 8 + qq) * 64 + ((j ^ (g & 1)) * 32) + (l15 & 3) * 8;
          w[(b + ks) * 2 + j] = tr_frag(p, p + 256);
        }
#pragma unroll
    for (int f = 0; f < NK * 2; ++f) if (b * 2 + f < NF) asm volatile("" : "+a"(w[b * 2 + f]));
    asm volatile("s_waitcnt lgkmcnt(0)" ::: "memory");
  }
}

__device__ __forceinline__ void mlp1_phase(char* ws, const u16* __restrict__ x, const u16* __restrict__ wg, const u16* __restrict__ wu, int m, u64 T, char* lds, int tid) {
  unsigned* sk = (unsigned*)lds; float* hs = (float*)(lds + 128); float* xs = hs + 2 * DM; char* yb = (char*)(xs + 2 * DM);
  const int lane = tid & 63, wv = __builtin_amdgcn_readfirstlane(tid >> 6), l15 = lane & 15, g = lane >> 4;
  bf16x8 a[32], b[32];
  load_frags<32>(a, wg + m * 128 + wv * 32, FF, lds + 16384 + wv * 32768, lane);
  load_frags<32>(b, wu + m * 128 + wv * 32, FF, lds + 16384 + wv * 32768, lane);
  unsigned xcc, k0, k1;
  asm volatile("s_getreg_b32 %0, hwreg(HW_REG_XCC_ID)" : "=s"(xcc));
  xcc &= 7u;
#pragma unroll 1
  for (int round = 1; round < 3; ++round) {
    if (round < 2) top2(ws, T, NSPEC, tid, k0, k1, sk);
    const int i0 = key_row(k0), i1 = key_row(k1);
    TL(1);
    sel_rows(ws, x, T, tid, i0, i1, hs, xs, yb);
    TL(3);
    bf16x8 xf[16];
#pragma unroll
    for (int ks = 0; ks < 16; ++ks) xf[ks] = *(const bf16x8*)(yb + (l15 & 1) * 1040 + ks * 64 + g * 16);
    f32x4 acc[4];
#pragma unroll
    for (int j = 0; j < 4; ++j) { acc[j] = (f32x4){0.f, 0.f, 0.f, 0.f}; asm volatile("" : "+v"(acc[j])); }
    asm volatile("s_nop 1" ::: "memory");
#pragma unroll
    for (int ks = 0; ks < 16; ++ks)
#pragma unroll
      for (int j = 0; j < 2; ++j) {
        asm volatile("v_mfma_f32_16x16x32_bf16 %0, %1, %2, %0" : "+v"(acc[j]) : "v"(xf[ks]), "a"(a[ks * 2 + j]));
        asm volatile("v_mfma_f32_16x16x32_bf16 %0, %1, %2, %0" : "+v"(acc[2 + j]) : "v"(xf[ks]), "a"(b[ks * 2 + j]));
      }
    asm volatile("s_nop 7\n\ts_nop 3" : "+v"(acc[0]), "+v"(acc[1]), "+v"(acc[2]), "+v"(acc[3]));
    {
      const int t = g >> 1, j = g & 1;
      const float g0 = j ? acc[1][0] : acc[0][0], g1 = j ? acc[1][1] : acc[0][1], u0 = j ? acc[3][0] : acc[2][0], u1 = j ? acc[3][1] : acc[2][1];
      const float gt = rbf(t ? g1 : g0), u = rbf(t ? u1 : u0);
      const float sg = rbf(gt / (1.0f + __expf(-gt)));
      const float av = sg * u;
      u16* ab = (u16*)(lds + 12288) + wv * 64;
      ab[lane] = (u16)pk2(av, av);
      __builtin_amdgcn_fence(__ATOMIC_RELEASE, "wavefront"); __builtin_amdgcn_wave_barrier(); __builtin_amdgcn_fence(__ATOMIC_ACQUIRE, "wavefront");
      store16_sc1(ws + OFF_ACT, (unsigned)(round * 65536 + (lane >> 3) * 8192 + (m * 4 + wv) * 128 + (lane & 7) * 16), *(const u32x4*)(ab + (lane & 7) * 8));
    }
    TL(6);
    if (round == 1 && spec_holds(ws, T, xcc, k0, k1, sk, tid)) break;
  }
}

__device__ __forceinline__ void mlp2_phase(char* ws, const u16* __restrict__ x, const u16* __restrict__ wd, u16* out, int m, u64 T, char* lds, int tid) {
  unsigned* sk = (unsigned*)lds; float* hs = (float*)(lds + 128); float* xs = hs + 2 * DM; char* yb = (char*)(xs + 2 * DM);
  float* part = (float*)(lds + 12288);
  const __amdgpu_buffer_rsrc_t rs = __builtin_amdgcn_make_buffer_rsrc(ws, 0, (int)(164 * MBY), 0x00020000);
  const int lane = tid & 63, wv = __builtin_amdgcn_readfirstlane(tid >> 6), l15 = lane & 15, g = lane >> 4;
  bf16x8 a[28];
  load_frags<28>(a, wd + (size_t)wv * 448 * DM + m * 32, DM, lds + 16384 + wv * 32768, lane);
  unsigned xcc, k0, k1;
  asm volatile("s_getreg_b32 %0, hwreg(HW_REG_XCC_ID)" : "=s"(xcc));
  xcc &= 7u;
#pragma unroll 1
  for (int round = 1; round < 3; ++round) {
    if (round < 2) top2(ws, T, NSPEC, tid, k0, k1, sk);
    const int i0 = key_row(k0), i1 = key_row(k1);
    TL(1);
    sel_rows(ws, x, T, tid, i0, i1, hs, xs, yb);
    TL(3);
    u32x4 xf[14];
    unsigned spins = 0;
    __builtin_amdgcn_s_sleep(ASL);
    for (;;) {
      bool ok = true;
#pragma unroll
      for (int ks = 0; ks < 14; ++ks) {
        xf[ks] = __builtin_bit_cast(u32x4, __builtin_amdgcn_raw_buffer_load_b128(rs, (int)OFF_ACT + round * 65536 + xcc * 8192 + (wv * 14 + ks) * 128 + (l15 & 1) * 64 + g * 16, 0, 16));
        ok = ok && xf[ks].x != SENT && xf[ks].y != SENT && xf[ks].z != SENT && xf[ks].w != SENT;
      }
      if (__all(ok)) break;
      __builtin_amdgcn_s_sleep(2);
      if (spin_bail(spins, ws, T)) break;
      asm volatile("" ::: "memory");
    }
    TL(7);
    f32x4 acc[2];
#pragma unroll
    for (int j = 0; j < 2; ++j) { acc[j] = (f32x4){0.f, 0.f, 0.f, 0.f}; asm volatile("" : "+v"(acc[j])); }
    asm volatile("s_nop 1" ::: "memory");
#pragma unroll
    for (int ks = 0; ks < 14; ++ks)
#pragma unroll
      for (int j = 0; j < 2; ++j) asm volatile("v_mfma_f32_16x16x32_bf16 %0, %1, %2, %0" : "+v"(acc[j]) : "v"(xf[ks]), "a"(a[ks * 2 + j]));
    asm volatile("s_nop 7\n\ts_nop 3" : "+v"(acc[0]), "+v"(acc[1]));
    {
      const float e0 = (g & 1) ? acc[1][0] : acc[0][0], e1 = (g & 1) ? acc[1][1] : acc[0][1];
      part[tid] = (g >> 1) ? e1 : e0;
    }
    __syncthreads();
    const int t = (tid >> 4) & 1, cc = (tid & 15) * 2, idx = t ? i1 : i0, col = m * 32 + cc;
    const float r = key_val(t ? k1 : k0);
    const float sg = rbf(1.0f / (1.0f + __expf(-r)));
    float o[2];
#pragma unroll
    for (int e = 0; e < 2; ++e) {
      const int q = t * 32 + cc + e;
      const float y = (part[q] + part[64 + q]) + (part[128 + q] + part[192 + q]);
      const float upd = rbf(rbf(y) * sg);
      o[e] = hs[t * DM + col + e] + rbf(xs[t * DM + col + e] + upd);
    }
    TL(4);
    gu32* op = (gu32*)(out + (size_t)idx * DM + col);
    if (tid < 32) __hip_atomic_store(op, pk2(o[0], o[1]), RLX_AGENT);
    if (round == 2 || spec_holds(ws, T, xcc, k0, k1, sk, tid)) break;
    if (tid < 32) __hip_atomic_store(op, pk2(hs[t * DM + col] + xs[t * DM + col], hs[t * DM + col + 1] + xs[t * DM + col + 1]), RLX_AGENT);
    asm volatile("s_waitcnt vmcnt(0)" ::: "memory");
    __syncthreads();
  }
}

__device__ __forceinline__ void red_phase(char* ws, int b, u64 T, unsigned par, int lane) {
  unsigned xcc; asm volatile("s_getreg_b32 %0, hwreg(HW_REG_XCC_ID)" : "=s"(xcc));
  const int g = lane >> 4;
  const unsigned near3 = ((par >> 16) & 1u) * 8192u;
  const unsigned own = (xcc & 3u) * 16384u + near3;
  const unsigned so = (((lane >> 3) & 1) + (lane >> 5) * 2) * 16384 + b * 128 + ((g & 1) + (lane & 1) * 2 + (lane & 4)) * 16 + (lane & 2) * 4 + near3;
  const unsigned io = (unsigned)OFF_G2 + (b >> 1) * 16384 + (b & 1) * 4096 + lane * 16;
  const unsigned one = (lane & 4) ? 0x3f800000u : 0x3f80u;
  asm volatile(
    "s_mov_b64 s[36:37], %0\n"
    "s_mov_b64 s[38:39], %1\n"
    "s_mov_b32 s40, %2\n"
    "v_mov_b32 v32, %3\n"
    "v_mov_b32 v33, %4\n"
    "v_mov_b32 v34, %5\n"
    "v_mov_b32 v35, %6\n"
    "v_mov_b32 v36, %7\n"
    "v_mov_b32 v37, %8\n"
    "v_mov_b32 v39, %9\n"
    "v_add_u32 v38, 0x10000, v37\n"
    "v_mov_b32 v40, 0xffbfffbf\n"
    "v_mov_b32 v41, 0xffbfffbf\n"
    "s_or_b32 s38, s38, 1\n"
    "s_mov_b32 s41, 0\n"
    "s_mov_b32 s47, 0\n"
    "s_add_u32 s64, s36, 0x200200\n"
    "s_addc_u32 s65, s37, 0\n"
    "s_getpc_b64 s[60:61]\n"
    "R_pc%=:\n"
    "s_add_u32 s60, s60, R_go%=-R_pc%=\n"
    "s_addc_u32 s61, s61, 0\n"
    "R_step%=:\n"
    "s_and_b32 s70, s41, 3\n"
    "s_lshl_b32 s71, s70, 16\n"
    "s_add_u32 s71, s71, 0x80000\n"
    "s_add_u32 s48, s36, s71\n"
    "s_addc_u32 s49, s37, 0\n"
    "s_add_u32 s71, s70, 1\n"
    "s_and_b32 s71, s71, 3\n"
    "s_lshl_b32 s71, s71, 17\n"
    "s_add_u32 s52, s36, s71\n"
    "s_addc_u32 s53, s37, 0\n"
    "s_xor_b32 s71, s71, 0x40000\n"
    "s_add_u32 s54, s36, s71\n"
    "s_addc_u32 s55, s37, 0\n"
    "s_lshr_b32 s70, s41, 2\n"
    "s_lshr_b32 s70, s40, s70\n"
    "s_and_b32 s70, s70, 1\n"
    "s_lshl_b32 s70, s70, 13\n"
    "s_lshl_b32 s71, s41, 19\n"
    "s_add_u32 s70, s70, s71\n"
    "s_add_u32 s50, s36, s70\n"
    "s_addc_u32 s51, s37, 0\n"
    "s_mov_b32 s45, 0\n"
    "s_mov_b32 s46, 0\n"
    "s_cmp_eq_u32 s41, 0\n"
    "s_cbranch_scc1 R_look%=\n"
    "s_add_u32 s44, s44, 3030\n"
    "R_lookat%=:\n"
    "R_w1%=:\n"
    "s_memtime s[56:57]\n"
    "s_waitcnt lgkmcnt(0)\n"
    "s_sub_u32 s70, s44, s56\n"
    "s_sub_u32 s71, s70, 800\n"
    "s_cmp_lt_u32 s71, 0x2000\n"
    "s_cbranch_scc1 R_w1%=\n"
    "R_look%=:\n"
    "global_load_dwordx4 v[64:67], v39, s[48:49] sc1\n"
    "s_sleep 3\n"
    "s_memtime s[56:57]\n"
    "s_memrealtime s[70:71]\n"
    "s_nop 3\n"
    "s_memrealtime s[72:73]\n"
    "s_nop 3\n"
    "s_memrealtime s[74:75]\n"
    "s_nop 3\n"
    "s_memrealtime s[76:77]\n"
    "s_nop 3\n"
    "s_memrealtime s[78:79]\n"
    "s_nop 3\n"
    "s_memrealtime s[80:81]\n"
    "s_mov_b32 s59, 0\n"
    "s_waitcnt lgkmcnt(0)\n"
    "s_mul_i32 s58, s70, 24\n"
    "s_cmp_eq_u32 s70, s72\n"
    "s_addc_u32 s59, s59, 0\n"
    "s_cmp_eq_u32 s70, s74\n"
    "s_addc_u32 s59, s59, 0\n"
    "s_cmp_eq_u32 s70, s76\n"
    "s_addc_u32 s59, s59, 0\n"
    "s_cmp_eq_u32 s70, s78\n"
    "s_addc_u32 s59, s59, 0\n"
    "s_cmp_eq_u32 s70, s80\n"
    "s_addc_u32 s59, s59, 0\n"
    "s_mul_i32 s59, s59, 20\n"
    "s_sub_u32 s58, s58, s59\n"
    "s_sub_u32 s58, s58, 300\n"
    "s_waitcnt vmcnt(0)\n"
    "v_subrev_u32 v67, s58, v67\n"
    "v_cmp_ne_u64 s[74:75], s[38:39], v[64:65]\n"
    "v_cmp_ne_u32 vcc, s41, v66\n"
    "v_max_i32_dpp v67, v67, v67 row_ror:1 row_mask:0xf bank_mask:0xf\n"
    "s_nop 1\n"
    "v_max_i32_dpp v67, v67, v67 row_ror:2 row_mask:0xf bank_mask:0xf\n"
    "s_nop 1\n"
    "v_max_i32_dpp v67, v67, v67 row_ror:4 row_mask:0xf bank_mask:0xf\n"
    "s_nop 1\n"
    "v_max_i32_dpp v67, v67, v67 row_ror:8 row_mask:0xf bank_mask:0xf\n"
    "s_nop 0\n"
    "v_readlane_b32 s70, v67, 0\n"
    "v_readlane_b32 s71, v67, 16\n"
    "v_readlane_b32 s72, v67, 32\n"
    "v_readlane_b32 s73, v67, 48\n"
    "s_or_b64 vcc, vcc, s[74:75]\n"
    "s_cbranch_scc1 R_notyet%=\n"
    "s_max_i32 s70, s70, s71\n"
    "s_max_i32 s72, s72, s73\n"
    "s_max_i32 s70, s70, s72\n"
    "s_min_i32 s70, s70, 8000\n"
    "s_add_u32 s44, s56, s70\n"
    "s_cmp_gt_i32 s70, 1210\n"
    "s_cbranch_scc1 R_far%=\n"
    "R_timed%=:\n"
    "R_w2%=:\n"
    "s_memtime s[56:57]\n"
    "s_waitcnt lgkmcnt(0)\n"
    "s_sub_u32 s70, s44, s56\n"
    "s_sub_u32 s71, s70, 200\n"
    "s_cmp_lt_u32 s71, 0x2000\n"
    "s_cbranch_scc1 R_w2%=\n"
    "s_sub_i32 s70, s70, 76\n"
    "s_max_i32 s70, s70, 0\n"
    "s_min_i32 s70, s70, 124\n"
    "s_andn2_b32 s70, s70, 3\n"
    "s_sub_u32 s72, s60, s70\n"
    "s_subb_u32 s73, s61, 0\n"
    "s_setpc_b64 s[72:73]\n"
    ".p2align 6\n"
    "s_nop 0\n"
    "s_nop 0\n"
    "s_nop 0\n"
    "s_nop 0\n"
    "s_nop 0\n"
    "s_nop 0\n"
    "s_nop 0\n"
    "s_nop 0\n"
    "s_nop 0\n"
    "s_nop 0\n"
    "s_nop 0\n"
    "s_nop 0\n"
    "s_nop 0\n"
    "s_nop 0\n"
    "s_nop 0\n"
    "s_nop 0\n"
    "s_nop 0\n"
    "s_nop 0\n"
    "s_nop 0\n"
    "s_nop 0\n"
    "s_nop 0\n"
    "s_nop 0\n"
    "s_nop 0\n"
    "s_nop 0\n"
    "s_nop 0\n"
    "s_nop 0\n"
    "s_nop 0\n"
    "s_nop 0\n"
    "s_nop 0\n"
    "s_nop 0\n"
    "s_nop 0\n"
    "s_nop 0\n"
    "R_go%=:\n"
    "global_load_dwordx4 v[48:51], v36, s[50:51] offset:0 sc1\n"
    "global_load_dwordx4 v[52:55], v36, s[50:51] offset:1024 sc1\n"
    "global_load_dwordx4 v[56:59], v36, s[50:51] offset:2048 sc1\n"
    "global_load_dwordx4 v[60:63], v36, s[50:51] offset:3072 sc1\n"
    "s_waitcnt vmcnt(3)\n"
    "v_mfma_f32_16x16x32_bf16 v[44:47], v[32:35], v[48:51], 0\n"
    "s_waitcnt vmcnt(2)\n"
    "v_mfma_f32_16x16x32_bf16 v[44:47], v[32:35], v[52:55], v[44:47]\n"
    "s_waitcnt vmcnt(1)\n"
    "v_mfma_f32_16x16x32_bf16 v[44:47], v[32:35], v[56:59], v[44:47]\n"
    "s_waitcnt vmcnt(0)\n"
    "v_mfma_f32_16x16x32_bf16 v[44:47], v[32:35], v[60:63], v[44:47]\n"
    "s_nop 7\n"
    "v_add_f32_dpp v44, v44, v44 row_ror:8 row_mask:0xf bank_mask:0xf\n"
    "v_add_f32_dpp v45, v45, v45 row_ror:8 row_mask:0xf bank_mask:0xf\n"
    "v_add_f32_dpp v46, v46, v46 row_ror:8 row_mask:0xf bank_mask:0xf\n"
    "v_add_f32_dpp v47, v47, v47 row_ror:8 row_mask:0xf bank_mask:0xf\n"
    "v_cmpx_o_f32 vcc, v44, v45\n"
    "R_pub%=:\n"
    "v_cvt_pk_bf16_f32 v64, v44, v45\n"
    "v_cvt_pk_bf16_f32 v65, v46, v47\n"
    "global_store_dwordx2 v37, v[64:65], s[52:53] sc1\n"
    "global_store_dwordx2 v38, v[64:65], s[52:53] sc1\n"
    "s_memtime s[58:59]\n"
    "s_cmp_eq_u64 exec, -1\n"
    "s_mov_b64 exec, -1\n"
    "s_cbranch_scc0 R_miss%=\n"
    "s_waitcnt lgkmcnt(0)\n"
    "s_add_u32 s58, s58, 600\n"
    "R_w3%=:\n"
    "s_memtime s[56:57]\n"
    "s_waitcnt lgkmcnt(0)\n"
    "s_sub_u32 s70, s58, s56\n"
    "s_sub_u32 s71, s70, 0\n"
    "s_cmp_lt_u32 s71, 0x2000\n"
    "s_cbranch_scc1 R_w3%=\n"
    "global_store_dwordx2 v37, v[40:41], s[54:55] sc1\n"
    "global_store_dwordx2 v38, v[40:41], s[54:55] sc1\n"
    "s_add_u32 s41, s41, 1\n"
    "s_cmp_lt_u32 s41, 63\n"
    "s_cbranch_scc1 R_step%=\n"
    "s_endpgm\n"
    "R_far%=:\n"
    "s_add_u32 s46, s46, 1\n"
    "s_cmp_lt_u32 s46, 16\n"
    "s_cbranch_scc1 R_lookat%=\n"
    "s_branch R_timed%=\n"
    "R_notyet%=:\n"
    "s_add_u32 s47, s47, 1\n"
    "s_and_b32 s70, s47, 0xff\n"
    "s_cbranch_scc1 R_look%=\n"
    "v_mov_b32 v64, 0\n"
    "global_load_dwordx2 v[66:67], v64, s[64:65] sc1\n"
    "s_sub_u32 s70, s38, 1\n"
    "s_mov_b32 s71, s39\n"
    "s_waitcnt vmcnt(0)\n"
    "v_cmp_eq_u64 vcc, s[70:71], v[66:67]\n"
    "s_nop 1\n"
    "s_cbranch_vccnz R_go%=\n"
    "s_cmp_lt_u32 s47, 0x40000\n"
    "s_cbranch_scc1 R_look%=\n"
    "v_mov_b32 v66, s70\n"
    "v_mov_b32 v67, s71\n"
    "global_store_dwordx2 v64, v[66:67], s[64:65] sc1\n"
    "s_branch R_go%=\n"
    "R_miss%=:\n"
    "s_add_u32 s45, s45, 1\n"
    "s_cmp_eq_u32 s45, 1\n"
    "s_cbranch_scc1 R_go%=\n"
    "s_mov_b64 s[72:73], 0\n"
    "v_cmp_eq_u32 vcc, v40, v48\n"
    "s_nop 0\n"
    "s_or_b64 s[72:73], s[72:73], vcc\n"
    "v_cmp_eq_u32 vcc, v40, v49\n"
    "s_nop 0\n"
    "s_or_b64 s[72:73], s[72:73], vcc\n"
    "v_cmp_eq_u32 vcc, v40, v50\n"
    "s_nop 0\n"
    "s_or_b64 s[72:73], s[72:73], vcc\n"
    "v_cmp_eq_u32 vcc, v40, v51\n"
    "s_nop 0\n"
    "s_or_b64 s[72:73], s[72:73], vcc\n"
    "v_cmp_eq_u32 vcc, v40, v52\n"
    "s_nop 0\n"
    "s_or_b64 s[72:73], s[72:73], vcc\n"
    "v_cmp_eq_u32 vcc, v40, v53\n"
    "s_nop 0\n"
    "s_or_b64 s[72:73], s[72:73], vcc\n"
    "v_cmp_eq_u32 vcc, v40, v54\n"
    "s_nop 0\n"
    "s_or_b64 s[72:73], s[72:73], vcc\n"
    "v_cmp_eq_u32 vcc, v40, v55\n"
    "s_nop 0\n"
    "s_or_b64 s[72:73], s[72:73], vcc\n"
    "v_cmp_eq_u32 vcc, v40, v56\n"
    "s_nop 0\n"
    "s_or_b64 s[72:73], s[72:73], vcc\n"
    "v_cmp_eq_u32 vcc, v40, v57\n"
    "s_nop 0\n"
    "s_or_b64 s[72:73], s[72:73], vcc\n"
    "v_cmp_eq_u32 vcc, v40, v58\n"
    "s_nop 0\n"
    "s_or_b64 s[72:73], s[72:73], vcc\n"
    "v_cmp_eq_u32 vcc, v40, v59\n"
    "s_nop 0\n"
    "s_or_b64 s[72:73], s[72:73], vcc\n"
    "v_cmp_eq_u32 vcc, v40, v60\n"
    "s_nop 0\n"
    "s_or_b64 s[72:73], s[72:73], vcc\n"
    "v_cmp_eq_u32 vcc, v40, v61\n"
    "s_nop 0\n"
    "s_or_b64 s[72:73], s[72:73], vcc\n"
    "v_cmp_eq_u32 vcc, v40, v62\n"
    "s_nop 0\n"
    "s_or_b64 s[72:73], s[72:73], vcc\n"
    "v_cmp_eq_u32 vcc, v40, v63\n"
    "s_nop 0\n"
    "s_or_b64 s[72:73], s[72:73], vcc\n"
    "s_cbranch_scc0 R_pub%=\n"
    "s_add_u32 s47, s47, 1\n"
    "s_and_b32 s70, s47, 0xff\n"
    "s_cbranch_scc1 R_look%=\n"
    "v_mov_b32 v64, 0\n"
    "global_load_dwordx2 v[66:67], v64, s[64:65] sc1\n"
    "s_sub_u32 s70, s38, 1\n"
    "s_mov_b32 s71, s39\n"
    "s_waitcnt vmcnt(0)\n"
    "v_cmp_eq_u64 vcc, s[70:71], v[66:67]\n"
    "s_nop 1\n"
    "s_cbranch_vccnz R_pub%=\n"
    "s_cmp_lt_u32 s47, 0x40000\n"
    "s_cbranch_scc1 R_look%=\n"
    "v_mov_b32 v66, s70\n"
    "v_mov_b32 v67, s71\n"
    "global_store_dwordx2 v64, v[66:67], s[64:65] sc1\n"
    "s_branch R_pub%=\n"
    "mk_code_end:\n"
    :: "s"(ws), "s"(uni(T)), "s"(__builtin_amdgcn_readfirstlane(par)), "v"((lane & 3) == 0 ? one : 0u), "v"((lane & 3) == 1 ? one : 0u), "v"((lane & 3) == 2 ? one : 0u), "v"((lane & 3) == 3 ? one : 0u),
       "v"(io), "v"(so), "v"(own + lane * 64), "s"(ws + 204 * MBY + b * 2048)
    : "memory", "vcc", "scc", "s36", "s37", "s38", "s39", "s40", "s62", "s63", "s99", "v32", "v33", "v34", "v35", "v36", "v37", "v38", "v39", "v67");
  __builtin_unreachable();
}

__global__ void __launch_bounds__(256, 1) rec_kernel(char* ws, const u16* __restrict__ x, const u16* __restrict__ mem0, const u16* __restrict__ Wq,
                                                     const u16* __restrict__ Wk, const u16* __restrict__ Wv, const u16* __restrict__ Wo,
                                                     const u16* __restrict__ w_r, const u16* __restrict__ wg, const u16* __restrict__ wu,
                                                     const u16* __restrict__ wd, u16* out) {
  __shared__ __attribute__((aligned(1024))) char lds[158720];
  const int tid = threadIdx.x, lane = tid & 63, wv = tid >> 6;
#define PADOP
  TL(0);
  u64 ep0;
  int role;
  {
    unsigned xcc; asm volatile("s_getreg_b32 %0, hwreg(HW_REG_XCC_ID)" : "=s"(xcc));
    role = (int)((blockIdx.x & ~7u) | ((xcc + XROT) & 7u));
  }
  if ((role >> 5) == ((role & 4) ? 0 : 6)) {
    unsigned tmp;
    asm volatile("s_getpc_b64 s[40:41]\n"
                 "mk_pc:\n\t"
                 "s_mov_b32 m0, 0x24000\n\t"
                 "v_min_u32 %0, mk_code_end-mk_pc-4, %1\n\t"
                 "global_load_lds_dword %0, s[40:41]" : "=&v"(tmp) : "v"((((blockIdx.x >> 3) & 3) * 256 + tid) * 128) : "s40", "s41", "memory");
  }
  const int wave = __builtin_amdgcn_readfirstlane(wv);
  unsigned near = 0;
#pragma unroll 1
  for (int pass = 0;; ++pass) {
    role = __builtin_amdgcn_readfirstlane(role);
    const int bx = role & 7, by = role >> 3;
    const bool red = bx < 4 && by >= 16, att = bx >= 4 && by < 8;
    const char* src = red ? (const char*)(x + (size_t)((by - 16) * 4 + bx) * 32 * DM) : att ? (const char*)(Wo + (size_t)((bx & 3) * 8 + by) * DH * DM)
                    : (const char*)((bx < 4 ? (by < 8 ? Wk : Wq) : (by < 16 ? Wk : (by < 24 ? Wv : Wq))) + ((bx & 3) * 8 + (by & 7)) * DH);
    const unsigned sstep = red || att ? 16384u : 64u * HD * 2u, lstep = red ? 1040u : 1024u;
    unsigned vo[4];
#pragma unroll
    for (int i = 0; i < 4; ++i) {
      const int r = wave * 16 + i * 4 + (lane >> 4), f = (r & 3) | ((bx < 4 ? r : r >> 1) & 4);
      vo[i] = red || att ? (wave * 4 + i) * 1024 + lane * 16 : r * (HD * 2) + (((lane & 15) ^ (f << 1)) * 16);
    }
    if (att) __builtin_amdgcn_s_sleep(ADL);
    if (bx >= 4 && by >= 24) __builtin_amdgcn_s_sleep(QDL);
    unsigned olo, ohi;
#define FD4 "global_load_lds_dwordx4 %3, s[42:43]\n\ts_add_u32 m0, m0, %10\n\ts_nop 0\n\tglobal_load_lds_dwordx4 %4, s[42:43]\n\ts_add_u32 m0, m0, %10\n\ts_nop 0\n\t" \
            "global_load_lds_dwordx4 %5, s[42:43]\n\ts_add_u32 m0, m0, %10\n\ts_nop 0\n\tglobal_load_lds_dwordx4 %6, s[42:43]\n\t" \
            "s_add_u32 m0, m0, s44\n\ts_add_u32 s42, s42, %9\n\ts_addc_u32 s43, s43, 0\n\t"
    asm volatile("s_load_dwordx2 %0, %11, 0x0\n\ts_mov_b64 s[42:43], %7\n\ts_mov_b32 m0, %8\n\ts_mul_i32 s44, %10, 13\n\ts_mov_b32 s45, %13\n\t"
                 FD4 FD4
                 "s_waitcnt lgkmcnt(0)\n\ts_mov_b64 s[46:47], %0\n\ts_add_u32 s46, s46, 1\n\ts_addc_u32 s47, s47, 0\n\ts_lshl_b64 s[46:47], s[46:47], 6\n\t"
                 "v_mov_b32 v40, s46\n\tv_mov_b32 v41, s47\n\tv_mov_b32 v42, 0\n\t"
                 "s_cmp_eq_u32 %14, 0\n\ts_cbranch_scc0 2f\n\t"
                 "s_mov_b64 exec, 1\n\tglobal_atomic_swap_x2 v[40:41], v42, v[40:41], %12 sc0\n\ts_mov_b64 exec, -1\n"
                 "2:\n\ts_cmp_eq_u32 s45, 0\n\ts_cbranch_scc0 1f\n\ts_waitcnt vmcnt(0)\n\ts_branch 3f\n"
                 "1:\n\t" FD4 "s_sub_u32 s45, s45, 1\n\ts_cmp_lg_u32 s45, 0\n\ts_cbranch_scc1 1b\n\ts_waitcnt vmcnt(24)\n"
                 "3:\n\tv_mov_b32 %1, v40\n\tv_mov_b32 %2, v41"
                 : "=&s"(ep0), "=v"(olo), "=v"(ohi)
                 : "v"(vo[0]), "v"(vo[1]), "v"(vo[2]), "v"(vo[3]), "s"(uni((u64)src)), "s"(wave * 4 * lstep), "s"(sstep), "s"(lstep), "s"(ws + OFF_EP), "s"(ws + OFF_CLM + role * 128),
                   "s"(red ? 0 : (att ? ATN : 6)), "s"(wave + pass)
                 : "memory", "scc", "s42", "s43", "s44", "s45", "s46", "s47", "v40", "v41", "v42");
    ep0 = uni(ep0);
    u64 old = ((u64)ohi << 32) | olo;
    if (red && (by - 16) * 4 + bx >= 47 && tid == 192) near = probe(ws, (by - 16) * 4 + bx - 47);
    int got = role;
    if (tid == 0 && pass == 0) {
      const u64 T0 = tag_of(ep0);
      while (old == T0) { got = (got + 1) & 255; old = __hip_atomic_exchange((gu64*)(ws + OFF_CLM + got * 128), T0, RLX_AGENT); }
      *(int*)(lds + 158704) = got;
    }
    asm volatile("s_waitcnt lgkmcnt(0)\n\ts_barrier" ::: "memory");
    got = __builtin_amdgcn_readfirstlane(*(int*)(lds + 158704));
    if (got == role) break;
    asm volatile("s_waitcnt vmcnt(0)\n\ts_barrier" ::: "memory");
    role = got;
  }
  const int bx = role & 7, by = role >> 3;
  if (bx >= 4 || by >= 16) {
    const int xc = bx & 3;
    if (bx < 4) {
      const int i = (by - 16) * 4 + bx;
      if (i >= 47 && tid == 192) set_flag(ws, OFF_PAR + (i - 47) * 128, tag_of(ep0), near);
      const u64 T = tag_of(ep0);
      if (i < 47) ln1_phase(ws, i, T, lds, tid);
      const int p = lane >> 4, q = lane & 3;
      unsigned lo = SENT, hi = SENT;
      if (wv == 0) {
        u16 el[4];
#pragma unroll
        for (int k = 0; k < 4; ++k) {
          u16 t = mem0[((i & 1) * 4 + p) * DM + (i >> 1) * 16 + k * 4 + q];
          if ((t & 0x7f80) == 0x7f80 && (t & 0x7f)) t |= 0x40;
          el[k] = t;
        }
        lo = el[0] | ((unsigned)el[1] << 16); hi = el[2] | ((unsigned)el[3] << 16);
      }
      const unsigned par = wait_par(ws, T, lane);
      gu64* gp = (gu64*)(ws + OFF_G3 + wv * 131072 + ((lane >> 2) & 3) * 16384 + ((par >> 16) & 1u) * 8192 + i * 128 + ((p & 1) + (q & 1) * 2 + (p & 2) * 2) * 16 + (q & 2) * 4);
      __hip_atomic_store(gp, ((u64)hi << 32) | lo, RLX_AGENT);
      __hip_atomic_store(gp + 8192, ((u64)hi << 32) | lo, RLX_AGENT);
      asm volatile("s_waitcnt vmcnt(0)" ::: "memory");
      __syncthreads();
      if (tid == 0) set_flag(ws, OFF_TAB + i * 128, T, 1u);
      if (i >= 47) ln1_phase(ws, i, T, lds, tid);
      if (wv == 0) red_phase(ws, i, T, par, lane);
    } else if (by < 8) {
      xa_phase(ws, by & 3, (xc * 2 + (by >> 2)) * 4 + wv, tag_of(ep0), lane);
    } else {
      const int jj = by - 8, h = xc * 8 + (jj & 7), m = (jj & 7) * 4 + xc;
      u64 T;
      if (jj < 8) T = qkv_phase<1>(ws, h, ep0, tid);
      else if (jj < 16) T = qkv_phase<2>(ws, h, ep0, tid);
      else T = qkv_phase<0>(ws, h, ep0, tid);
      TL(2);
      op_phase(ws, x, w_r, out, Wo, jj >> 2, xc, jj & 3, T, lds, tid);
      if (jj >= 16) __syncthreads();
      if (jj < 16) {}
      else if (m < 14) mlp1_phase(ws, x, wg, wu, m, T, lds, tid);
      else if (m >= 16) {
        mlp2_phase(ws, x, wd, out, m - 16, T, lds, tid);
        if (m == 31 && tid == 0) __hip_atomic_store((gu64*)(ws + OFF_EP), T >> 6, RLX_AGENT);
      }
    }
    TL(5);
    return;
  }
  const unsigned ldsb = (unsigned)(size_t)(LAS char*)lds;
  asm volatile(
    "s_mov_b64 s[36:37], %0\n"
    "s_mov_b32 s38, %1\n"
    "s_mov_b32 s39, %2\n"
    "s_mov_b32 s40, %3\n"
    "s_load_dwordx2 s[96:97], %4, 0x0\n"
    "s_mov_b64 s[22:23], %5\n"
    "s_mov_b64 s[24:25], %6\n"
    "s_mov_b64 s[26:27], %7\n"
    "s_mov_b64 s[28:29], %8\n"
    "s_mov_b64 exec, -1\n"
    "s_mov_b32 s90, 0\n"
    "s_getreg_b32 s10, hwreg(HW_REG_XCC_ID)\n"
    "s_and_b32 s41, s38, 3\n"
    "s_lshl_b32 s41, s41, 3\n"
    "s_bfe_u32 s42, s38, 0x30002\n"
    "s_add_u32 s41, s41, s42\n"
    "s_lshr_b32 s42, s38, 5\n"
    "v_mbcnt_lo_u32_b32 v0, -1, 0\n"
    "v_mbcnt_hi_u32_b32 v0, -1, v0\n"
    "v_and_b32 v1, 15, v0\n"
    "v_lshrrev_b32 v2, 4, v0\n"
    "v_lshlrev_b32 v71, 4, v0\n"
    "s_and_b32 s11, s10, 7\n"
    "s_add_u32 s68, s36, 0x200200\n"
    "s_addc_u32 s69, s37, 0\n"
    "s_lshl_b32 s78, s39, 4\n"
    "v_add_u32 v8, s78, v2\n"
    "v_lshlrev_b32 v12, 1, v2\n"
    "v_xor_b32 v12, v12, v1\n"
    "v_and_b32 v9, 1, v2\n"
    "v_lshlrev_b32 v9, 1, v9\n"
    "v_xor_b32 v9, v9, v1\n"
    "s_and_b32 s79, s39, 1\n"
    "s_lshl_b32 s79, s79, 3\n"
    "v_add_u32 v10, 0, v8\n"
    "v_xor_b32 v11, 0, v12\n"
    "v_lshlrev_b32 v11, 4, v11\n"
    "v_lshl_add_u32 v84, v10, 13, v11\n"
    "v_xor_b32 v11, 0, v9\n"
    "v_xor_b32 v11, s79, v11\n"
    "v_lshlrev_b32 v11, 4, v11\n"
    "v_lshl_add_u32 v88, v10, 10, v11\n"
    "v_add_u32 v10, 4, v8\n"
    "v_xor_b32 v11, 8, v12\n"
    "v_lshlrev_b32 v11, 4, v11\n"
    "v_lshl_add_u32 v85, v10, 13, v11\n"
    "v_xor_b32 v11, 4, v9\n"
    "v_xor_b32 v11, s79, v11\n"
    "v_lshlrev_b32 v11, 4, v11\n"
    "v_lshl_add_u32 v89, v10, 10, v11\n"
    "v_add_u32 v10, 8, v8\n"
    "v_xor_b32 v11, 0, v12\n"
    "v_lshlrev_b32 v11, 4, v11\n"
    "v_lshl_add_u32 v86, v10, 13, v11\n"
    "v_xor_b32 v11, 0, v9\n"
    "v_xor_b32 v11, s79, v11\n"
    "v_lshlrev_b32 v11, 4, v11\n"
    "v_lshl_add_u32 v90, v10, 10, v11\n"
    "v_add_u32 v10, 12, v8\n"
    "v_xor_b32 v11, 8, v12\n"
    "v_lshlrev_b32 v11, 4, v11\n"
    "v_lshl_add_u32 v87, v10, 13, v11\n"
    "v_xor_b32 v11, 4, v9\n"
    "v_xor_b32 v11, s79, v11\n"
    "v_lshlrev_b32 v11, 4, v11\n"
    "v_lshl_add_u32 v91, v10, 10, v11\n"
    "s_lshl_b32 s78, s41, 8\n"
    "s_add_u32 s22, s22, s78\n"
    "s_addc_u32 s23, s23, 0\n"
    "s_add_u32 s24, s24, s78\n"
    "s_addc_u32 s25, s25, 0\n"
    "s_add_u32 s26, s26, s78\n"
    "s_addc_u32 s27, s27, 0\n"
    "s_lshl_b32 s78, s41, 17\n"
    "s_lshl_b32 s79, s42, 9\n"
    "s_add_u32 s78, s78, s79\n"
    "s_add_u32 s28, s28, s78\n"
    "s_addc_u32 s29, s29, 0\n"
    "s_lshl_b32 s30, s39, 12\n"
    "s_add_u32 s30, s30, s40\n"
    "v_lshrrev_b32 v8, 2, v1\n"
    "v_and_b32 v9, 1, v8\n"
    "v_lshrrev_b32 v10, 1, v8\n"
    "v_and_b32 v11, 1, v2\n"
    "v_and_b32 v12, 3, v1\n"
    "v_lshl_add_u32 v14, v11, 1, v10\n"
    "v_lshl_add_u32 v14, v9, 2, v14\n"
    "v_lshrrev_b32 v13, 1, v2\n"
    "v_lshl_add_u32 v13, v13, 4, v14\n"
    "v_lshlrev_b32 v13, 8, v13\n"
    "v_add_u32 v13, s40, v13\n"
    "v_lshlrev_b32 v14, 5, v14\n"
    "s_lshl_b32 s78, s39, 6\n"
    "v_lshl_add_u32 v15, v12, 3, s78\n"
    "v_xor_b32 v15, v15, v14\n"
    "v_add_u32 v92, v15, v13\n"
    "v_xor_b32 v15, 32, v15\n"
    "v_add_u32 v93, v15, v13\n"
    "s_lshr_b32 s78, s39, 1\n"
    "s_lshl_b32 s78, s78, 7\n"
    "s_and_b32 s79, s39, 1\n"
    "s_lshl_b32 s79, s79, 4\n"
    "s_add_u32 s78, s78, s79\n"
    "v_and_b32 v15, 1, v12\n"
    "v_lshl_add_u32 v15, v15, 5, s78\n"
    "v_lshrrev_b32 v16, 1, v12\n"
    "v_lshl_add_u32 v15, v16, 3, v15\n"
    "v_xor_b32 v15, v15, v14\n"
    "v_add_u32 v96, v15, v13\n"
    "v_xor_b32 v15, 64, v15\n"
    "v_add_u32 v97, v15, v13\n"
    "v_add_u32 v94, 0x10000, v92\n"
    "v_add_u32 v95, 0x10000, v93\n"
    "v_add_u32 v98, 0x10000, v96\n"
    "v_add_u32 v99, 0x10000, v97\n"
    "v_lshl_add_u32 v13, v2, 2, v9\n"
    "v_lshl_add_u32 v13, v10, 4, v13\n"
    "v_lshl_add_u32 v14, v11, 1, v9\n"
    "v_lshl_add_u32 v14, v10, 2, v14\n"
    "v_xor_b32 v14, v14, v12\n"
    "v_lshlrev_b32 v14, 1, v14\n"
    "s_and_b32 s78, s39, 1\n"
    "s_lshl_b32 s78, s78, 3\n"
    "v_xor_b32 v14, s78, v14\n"
    "v_lshlrev_b32 v14, 4, v14\n"
    "v_lshl_add_u32 v14, v13, 8, v14\n"
    "s_lshr_b32 s78, s39, 1\n"
    "s_lshl_b32 s78, s78, 14\n"
    "s_add_u32 s78, s78, 0x10000\n"
    "v_add_u32 v104, s78, v14\n"
    "s_mul_i32 s78, s39, 34816\n"
    "s_add_u32 s78, s78, 18688\n"
    "v_add_u32 v116, s78, v71\n"
    "s_cmp_lg_u32 s42, 0\n"
    "s_cbranch_scc1 L_rb0%=\n"
    "ds_read_b64_tr_b16 a[0:1], v96 offset:0\n"
    "ds_read_b64_tr_b16 a[2:3], v96 offset:2048\n"
    "ds_read_b64_tr_b16 a[4:5], v96 offset:8192\n"
    "ds_read_b64_tr_b16 a[6:7], v96 offset:10240\n"
    "ds_read_b64_tr_b16 a[64:65], v97 offset:0\n"
    "ds_read_b64_tr_b16 a[66:67], v97 offset:2048\n"
    "ds_read_b64_tr_b16 a[68:69], v97 offset:8192\n"
    "ds_read_b64_tr_b16 a[70:71], v97 offset:10240\n"
    "s_waitcnt vmcnt(24) lgkmcnt(0)\n"
    "s_barrier\n"
    "s_add_u32 s32, s26, 0x0\n"
    "s_addc_u32 s33, s27, 0\n"
    "s_add_u32 m0, s30, 0\n"
    "s_nop 0\n"
    "global_load_lds_dwordx4 v84, s[32:33]\n"
    "s_add_u32 m0, s30, 1024\n"
    "s_nop 0\n"
    "global_load_lds_dwordx4 v85, s[32:33]\n"
    "s_add_u32 m0, s30, 2048\n"
    "s_nop 0\n"
    "global_load_lds_dwordx4 v86, s[32:33]\n"
    "s_add_u32 m0, s30, 3072\n"
    "s_nop 0\n"
    "global_load_lds_dwordx4 v87, s[32:33]\n"
    "ds_read_b64_tr_b16 a[8:9], v96 offset:16384\n"
    "ds_read_b64_tr_b16 a[10:11], v96 offset:18432\n"
    "ds_read_b64_tr_b16 a[12:13], v96 offset:24576\n"
    "ds_read_b64_tr_b16 a[14:15], v96 offset:26624\n"
    "ds_read_b64_tr_b16 a[72:73], v97 offset:16384\n"
    "ds_read_b64_tr_b16 a[74:75], v97 offset:18432\n"
    "ds_read_b64_tr_b16 a[76:77], v97 offset:24576\n"
    "ds_read_b64_tr_b16 a[78:79], v97 offset:26624\n"
    "s_waitcnt vmcnt(24) lgkmcnt(0)\n"
    "s_barrier\n"
    "s_add_u32 s32, s26, 0x80000\n"
    "s_addc_u32 s33, s27, 0\n"
    "s_add_u32 m0, s30, 16384\n"
    "s_nop 0\n"
    "global_load_lds_dwordx4 v84, s[32:33]\n"
    "s_add_u32 m0, s30, 17408\n"
    "s_nop 0\n"
    "global_load_lds_dwordx4 v85, s[32:33]\n"
    "s_add_u32 m0, s30, 18432\n"
    "s_nop 0\n"
    "global_load_lds_dwordx4 v86, s[32:33]\n"
    "s_add_u32 m0, s30, 19456\n"
    "s_nop 0\n"
    "global_load_lds_dwordx4 v87, s[32:33]\n"
    "ds_read_b64_tr_b16 a[16:17], v96 offset:32768\n"
    "ds_read_b64_tr_b16 a[18:19], v96 offset:34816\n"
    "ds_read_b64_tr_b16 a[20:21], v96 offset:40960\n"
    "ds_read_b64_tr_b16 a[22:23], v96 offset:43008\n"
    "ds_read_b64_tr_b16 a[80:81], v97 offset:32768\n"
    "ds_read_b64_tr_b16 a[82:83], v97 offset:34816\n"
    "ds_read_b64_tr_b16 a[84:85], v97 offset:40960\n"
    "ds_read_b64_tr_b16 a[86:87], v97 offset:43008\n"
    "s_waitcnt vmcnt(24) lgkmcnt(0)\n"
    "s_barrier\n"
    "s_add_u32 s32, s26, 0x100000\n"
    "s_addc_u32 s33, s27, 0\n"
    "s_add_u32 m0, s30, 32768\n"
    "s_nop 0\n"
    "global_load_lds_dwordx4 v84, s[32:33]\n"
    "s_add_u32 m0, s30, 33792\n"
    "s_nop 0\n"
    "global_load_lds_dwordx4 v85, s[32:33]\n"
    "s_add_u32 m0, s30, 34816\n"
    "s_nop 0\n"
    "global_load_lds_dwordx4 v86, s[32:33]\n"
    "s_add_u32 m0, s30, 35840\n"
    "s_nop 0\n"
    "global_load_lds_dwordx4 v87, s[32:33]\n"
    "ds_read_b64_tr_b16 a[24:25], v96 offset:49152\n"
    "ds_read_b64_tr_b16 a[26:27], v96 offset:51200\n"
    "ds_read_b64_tr_b16 a[28:29], v96 offset:57344\n"
    "ds_read_b64_tr_b16 a[30:31], v96 offset:59392\n"
    "ds_read_b64_tr_b16 a[88:89], v97 offset:49152\n"
    "ds_read_b64_tr_b16 a[90:91], v97 offset:51200\n"
    "ds_read_b64_tr_b16 a[92:93], v97 offset:57344\n"
    "ds_read_b64_tr_b16 a[94:95], v97 offset:59392\n"
    "s_waitcnt vmcnt(24) lgkmcnt(0)\n"
    "s_barrier\n"
    "s_add_u32 s32, s26, 0x180000\n"
    "s_addc_u32 s33, s27, 0\n"
    "s_add_u32 m0, s30, 49152\n"
    "s_nop 0\n"
    "global_load_lds_dwordx4 v84, s[32:33]\n"
    "s_add_u32 m0, s30, 50176\n"
    "s_nop 0\n"
    "global_load_lds_dwordx4 v85, s[32:33]\n"
    "s_add_u32 m0, s30, 51200\n"
    "s_nop 0\n"
    "global_load_lds_dwordx4 v86, s[32:33]\n"
    "s_add_u32 m0, s30, 52224\n"
    "s_nop 0\n"
    "global_load_lds_dwordx4 v87, s[32:33]\n"
    "ds_read_b64_tr_b16 a[32:33], v98 offset:0\n"
    "ds_read_b64_tr_b16 a[34:35], v98 offset:2048\n"
    "ds_read_b64_tr_b16 a[36:37], v98 offset:8192\n"
    "ds_read_b64_tr_b16 a[38:39], v98 offset:10240\n"
    "ds_read_b64_tr_b16 a[96:97], v99 offset:0\n"
    "ds_read_b64_tr_b16 a[98:99], v99 offset:2048\n"
    "ds_read_b64_tr_b16 a[100:101], v99 offset:8192\n"
    "ds_read_b64_tr_b16 a[102:103], v99 offset:10240\n"
    "s_waitcnt vmcnt(24) lgkmcnt(0)\n"
    "s_barrier\n"
    "s_add_u32 s32, s28, 0x0\n"
    "s_addc_u32 s33, s29, 0\n"
    "s_add_u32 m0, s30, 65536\n"
    "s_nop 0\n"
    "global_load_lds_dwordx4 v88, s[32:33]\n"
    "s_add_u32 m0, s30, 66560\n"
    "s_nop 0\n"
    "global_load_lds_dwordx4 v89, s[32:33]\n"
    "s_add_u32 m0, s30, 67584\n"
    "s_nop 0\n"
    "global_load_lds_dwordx4 v90, s[32:33]\n"
    "s_add_u32 m0, s30, 68608\n"
    "s_nop 0\n"
    "global_load_lds_dwordx4 v91, s[32:33]\n"
    "ds_read_b64_tr_b16 a[40:41], v98 offset:16384\n"
    "ds_read_b64_tr_b16 a[42:43], v98 offset:18432\n"
    "ds_read_b64_tr_b16 a[44:45], v98 offset:24576\n"
    "ds_read_b64_tr_b16 a[46:47], v98 offset:26624\n"
    "ds_read_b64_tr_b16 a[104:105], v99 offset:16384\n"
    "ds_read_b64_tr_b16 a[106:107], v99 offset:18432\n"
    "ds_read_b64_tr_b16 a[108:109], v99 offset:24576\n"
    "ds_read_b64_tr_b16 a[110:111], v99 offset:26624\n"
    "s_waitcnt vmcnt(24) lgkmcnt(0)\n"
    "s_barrier\n"
    "s_add_u32 s32, s28, 0x100\n"
    "s_addc_u32 s33, s29, 0\n"
    "s_add_u32 m0, s30, 81920\n"
    "s_nop 0\n"
    "global_load_lds_dwordx4 v88, s[32:33]\n"
    "s_add_u32 m0, s30, 82944\n"
    "s_nop 0\n"
    "global_load_lds_dwordx4 v89, s[32:33]\n"
    "s_add_u32 m0, s30, 83968\n"
    "s_nop 0\n"
    "global_load_lds_dwordx4 v90, s[32:33]\n"
    "s_add_u32 m0, s30, 84992\n"
    "s_nop 0\n"
    "global_load_lds_dwordx4 v91, s[32:33]\n"
    "ds_read_b64_tr_b16 a[48:49], v98 offset:32768\n"
    "ds_read_b64_tr_b16 a[50:51], v98 offset:34816\n"
    "ds_read_b64_tr_b16 a[52:53], v98 offset:40960\n"
    "ds_read_b64_tr_b16 a[54:55], v98 offset:43008\n"
    "ds_read_b64_tr_b16 a[112:113], v99 offset:32768\n"
    "ds_read_b64_tr_b16 a[114:115], v99 offset:34816\n"
    "ds_read_b64_tr_b16 a[116:117], v99 offset:40960\n"
    "ds_read_b64_tr_b16 a[118:119], v99 offset:43008\n"
    "s_waitcnt vmcnt(24) lgkmcnt(0)\n"
    "s_barrier\n"
    "s_add_u32 s32, s28, 0x10000\n"
    "s_addc_u32 s33, s29, 0\n"
    "s_add_u32 m0, s30, 98304\n"
    "s_nop 0\n"
    "global_load_lds_dwordx4 v88, s[32:33]\n"
    "s_add_u32 m0, s30, 99328\n"
    "s_nop 0\n"
    "global_load_lds_dwordx4 v89, s[32:33]\n"
    "s_add_u32 m0, s30, 100352\n"
    "s_nop 0\n"
    "global_load_lds_dwordx4 v90, s[32:33]\n"
    "s_add_u32 m0, s30, 101376\n"
    "s_nop 0\n"
    "global_load_lds_dwordx4 v91, s[32:33]\n"
    "ds_read_b64_tr_b16 a[56:57], v98 offset:49152\n"
    "ds_read_b64_tr_b16 a[58:59], v98 offset:51200\n"
    "ds_read_b64_tr_b16 a[60:61], v98 offset:57344\n"
    "ds_read_b64_tr_b16 a[62:63], v98 offset:59392\n"
    "ds_read_b64_tr_b16 a[120:121], v99 offset:49152\n"
    "ds_read_b64_tr_b16 a[122:123], v99 offset:51200\n"
    "ds_read_b64_tr_b16 a[124:125], v99 offset:57344\n"
    "ds_read_b64_tr_b16 a[126:127], v99 offset:59392\n"
    "s_branch L_rc0%=\n"
    "L_rb0%=:\n"
    "ds_read_b64_tr_b16 v[128:129], v96 offset:0\n"
    "ds_read_b64_tr_b16 v[130:131], v96 offset:2048\n"
    "ds_read_b64_tr_b16 v[132:133], v96 offset:8192\n"
    "ds_read_b64_tr_b16 v[134:135], v96 offset:10240\n"
    "ds_read_b64_tr_b16 v[136:137], v97 offset:0\n"
    "ds_read_b64_tr_b16 v[138:139], v97 offset:2048\n"
    "ds_read_b64_tr_b16 v[140:141], v97 offset:8192\n"
    "ds_read_b64_tr_b16 v[142:143], v97 offset:10240\n"
    "s_waitcnt vmcnt(24) lgkmcnt(0)\n"
    "s_barrier\n"
    "s_add_u32 s32, s26, 0x200000\n"
    "s_addc_u32 s33, s27, 0\n"
    "s_add_u32 m0, s30, 0\n"
    "s_nop 0\n"
    "global_load_lds_dwordx4 v84, s[32:33]\n"
    "s_add_u32 m0, s30, 1024\n"
    "s_nop 0\n"
    "global_load_lds_dwordx4 v85, s[32:33]\n"
    "s_add_u32 m0, s30, 2048\n"
    "s_nop 0\n"
    "global_load_lds_dwordx4 v86, s[32:33]\n"
    "s_add_u32 m0, s30, 3072\n"
    "s_nop 0\n"
    "global_load_lds_dwordx4 v87, s[32:33]\n"
    "ds_read_b64_tr_b16 v[144:145], v96 offset:16384\n"
    "ds_read_b64_tr_b16 v[146:147], v96 offset:18432\n"
    "ds_read_b64_tr_b16 v[148:149], v96 offset:24576\n"
    "ds_read_b64_tr_b16 v[150:151], v96 offset:26624\n"
    "ds_read_b64_tr_b16 v[152:153], v97 offset:16384\n"
    "ds_read_b64_tr_b16 v[154:155], v97 offset:18432\n"
    "ds_read_b64_tr_b16 v[156:157], v97 offset:24576\n"
    "ds_read_b64_tr_b16 v[158:159], v97 offset:26624\n"
    "s_waitcnt vmcnt(24) lgkmcnt(0)\n"
    "s_barrier\n"
    "s_add_u32 s32, s26, 0x280000\n"
    "s_addc_u32 s33, s27, 0\n"
    "s_add_u32 m0, s30, 16384\n"
    "s_nop 0\n"
    "global_load_lds_dwordx4 v84, s[32:33]\n"
    "s_add_u32 m0, s30, 17408\n"
    "s_nop 0\n"
    "global_load_lds_dwordx4 v85, s[32:33]\n"
    "s_add_u32 m0, s30, 18432\n"
    "s_nop 0\n"
    "global_load_lds_dwordx4 v86, s[32:33]\n"
    "s_add_u32 m0, s30, 19456\n"
    "s_nop 0\n"
    "global_load_lds_dwordx4 v87, s[32:33]\n"
    "ds_read_b64_tr_b16 v[160:161], v96 offset:32768\n"
    "ds_read_b64_tr_b16 v[162:163], v96 offset:34816\n"
    "ds_read_b64_tr_b16 v[164:165], v96 offset:40960\n"
    "ds_read_b64_tr_b16 v[166:167], v96 offset:43008\n"
    "ds_read_b64_tr_b16 v[168:169], v97 offset:32768\n"
    "ds_read_b64_tr_b16 v[170:171], v97 offset:34816\n"
    "ds_read_b64_tr_b16 v[172:173], v97 offset:40960\n"
    "ds_read_b64_tr_b16 v[174:175], v97 offset:43008\n"
    "s_waitcnt vmcnt(24) lgkmcnt(0)\n"
    "s_barrier\n"
    "s_add_u32 s32, s26, 0x300000\n"
    "s_addc_u32 s33, s27, 0\n"
    "s_add_u32 m0, s30, 32768\n"
    "s_nop 0\n"
    "global_load_lds_dwordx4 v84, s[32:33]\n"
    "s_add_u32 m0, s30, 33792\n"
    "s_nop 0\n"
    "global_load_lds_dwordx4 v85, s[32:33]\n"
    "s_add_u32 m0, s30, 34816\n"
    "s_nop 0\n"
    "global_load_lds_dwordx4 v86, s[32:33]\n"
    "s_add_u32 m0, s30, 35840\n"
    "s_nop 0\n"
    "global_load_lds_dwordx4 v87, s[32:33]\n"
    "ds_read_b64_tr_b16 v[176:177], v96 offset:49152\n"
    "ds_read_b64_tr_b16 v[178:179], v96 offset:51200\n"
    "ds_read_b64_tr_b16 v[180:181], v96 offset:57344\n"
    "ds_read_b64_tr_b16 v[182:183], v96 offset:59392\n"
    "ds_read_b64_tr_b16 v[184:185], v97 offset:49152\n"
    "ds_read_b64_tr_b16 v[186:187], v97 offset:51200\n"
    "ds_read_b64_tr_b16 v[188:189], v97 offset:57344\n"
    "ds_read_b64_tr_b16 v[190:191], v97 offset:59392\n"
    "s_waitcnt vmcnt(24) lgkmcnt(0)\n"
    "s_barrier\n"
    "s_add_u32 s32, s26, 0x380000\n"
    "s_addc_u32 s33, s27, 0\n"
    "s_add_u32 m0, s30, 49152\n"
    "s_nop 0\n"
    "global_load_lds_dwordx4 v84, s[32:33]\n"
    "s_add_u32 m0, s30, 50176\n"
    "s_nop 0\n"
    "global_load_lds_dwordx4 v85, s[32:33]\n"
    "s_add_u32 m0, s30, 51200\n"
    "s_nop 0\n"
    "global_load_lds_dwordx4 v86, s[32:33]\n"
    "s_add_u32 m0, s30, 52224\n"
    "s_nop 0\n"
    "global_load_lds_dwordx4 v87, s[32:33]\n"
    "ds_read_b64_tr_b16 v[192:193], v98 offset:0\n"
    "ds_read_b64_tr_b16 v[194:195], v98 offset:2048\n"
    "ds_read_b64_tr_b16 v[196:197], v98 offset:8192\n"
    "ds_read_b64_tr_b16 v[198:199], v98 offset:10240\n"
    "ds_read_b64_tr_b16 v[200:201], v99 offset:0\n"
    "ds_read_b64_tr_b16 v[202:203], v99 offset:2048\n"
    "ds_read_b64_tr_b16 v[204:205], v99 offset:8192\n"
    "ds_read_b64_tr_b16 v[206:207], v99 offset:10240\n"
    "s_waitcnt vmcnt(24) lgkmcnt(0)\n"
    "s_barrier\n"
    "s_add_u32 s32, s28, 0x0\n"
    "s_addc_u32 s33, s29, 0\n"
    "s_add_u32 m0, s30, 65536\n"
    "s_nop 0\n"
    "global_load_lds_dwordx4 v88, s[32:33]\n"
    "s_add_u32 m0, s30, 66560\n"
    "s_nop 0\n"
    "global_load_lds_dwordx4 v89, s[32:33]\n"
    "s_add_u32 m0, s30, 67584\n"
    "s_nop 0\n"
    "global_load_lds_dwordx4 v90, s[32:33]\n"
    "s_add_u32 m0, s30, 68608\n"
    "s_nop 0\n"
    "global_load_lds_dwordx4 v91, s[32:33]\n"
    "ds_read_b64_tr_b16 v[208:209], v98 offset:16384\n"
    "ds_read_b64_tr_b16 v[210:211], v98 offset:18432\n"
    "ds_read_b64_tr_b16 v[212:213], v98 offset:24576\n"
    "ds_read_b64_tr_b16 v[214:215], v98 offset:26624\n"
    "ds_read_b64_tr_b16 v[216:217], v99 offset:16384\n"
    "ds_read_b64_tr_b16 v[218:219], v99 offset:18432\n"
    "ds_read_b64_tr_b16 v[220:221], v99 offset:24576\n"
    "ds_read_b64_tr_b16 v[222:223], v99 offset:26624\n"
    "s_waitcnt vmcnt(24) lgkmcnt(0)\n"
    "s_barrier\n"
    "s_add_u32 s32, s28, 0x100\n"
    "s_addc_u32 s33, s29, 0\n"
    "s_add_u32 m0, s30, 81920\n"
    "s_nop 0\n"
    "global_load_lds_dwordx4 v88, s[32:33]\n"
    "s_add_u32 m0, s30, 82944\n"
    "s_nop 0\n"
    "global_load_lds_dwordx4 v89, s[32:33]\n"
    "s_add_u32 m0, s30, 83968\n"
    "s_nop 0\n"
    "global_load_lds_dwordx4 v90, s[32:33]\n"
    "s_add_u32 m0, s30, 84992\n"
    "s_nop 0\n"
    "global_load_lds_dwordx4 v91, s[32:33]\n"
    "ds_read_b64_tr_b16 v[224:225], v98 offset:32768\n"
    "ds_read_b64_tr_b16 v[226:227], v98 offset:34816\n"
    "ds_read_b64_tr_b16 v[228:229], v98 offset:40960\n"
    "ds_read_b64_tr_b16 v[230:231], v98 offset:43008\n"
    "ds_read_b64_tr_b16 v[232:233], v99 offset:32768\n"
    "ds_read_b64_tr_b16 v[234:235], v99 offset:34816\n"
    "ds_read_b64_tr_b16 v[236:237], v99 offset:40960\n"
    "ds_read_b64_tr_b16 v[238:239], v99 offset:43008\n"
    "s_waitcnt vmcnt(24) lgkmcnt(0)\n"
    "s_barrier\n"
    "s_add_u32 s32, s28, 0x10000\n"
    "s_addc_u32 s33, s29, 0\n"
    "s_add_u32 m0, s30, 98304\n"
    "s_nop 0\n"
    "global_load_lds_dwordx4 v88, s[32:33]\n"
    "s_add_u32 m0, s30, 99328\n"
    "s_nop 0\n"
    "global_load_lds_dwordx4 v89, s[32:33]\n"
    "s_add_u32 m0, s30, 100352\n"
    "s_nop 0\n"
    "global_load_lds_dwordx4 v90, s[32:33]\n"
    "s_add_u32 m0, s30, 101376\n"
    "s_nop 0\n"
    "global_load_lds_dwordx4 v91, s[32:33]\n"
    "ds_read_b64_tr_b16 v[240:241], v98 offset:49152\n"
    "ds_read_b64_tr_b16 v[242:243], v98 offset:51200\n"
    "ds_read_b64_tr_b16 v[244:245], v98 offset:57344\n"
    "ds_read_b64_tr_b16 v[246:247], v98 offset:59392\n"
    "ds_read_b64_tr_b16 v[248:249], v99 offset:49152\n"
    "ds_read_b64_tr_b16 v[250:251], v99 offset:51200\n"
    "ds_read_b64_tr_b16 v[252:253], v99 offset:57344\n"
    "ds_read_b64_tr_b16 v[254:255], v99 offset:59392\n"
    "L_rc0%=:\n"
    "s_mov_b32 s70, 0xcccccccc\n"
    "s_mov_b32 s71, 0xcccccccc\n"
    "s_mov_b32 s50, 0xffbfffbf\n"
    "s_mov_b32 s72, 0x33333333\n"
    "s_mov_b32 s73, 0x0\n"
    "s_mov_b32 s74, 0x0\n"
    "s_mov_b32 s75, 0x33333333\n"
    "v_mov_b32 v9, 0x4444\n"
    "v_mov_b32 v10, 0xeeee\n"
    "v_cndmask_b32 v67, v9, v10, s[70:71]\n"
    "s_mov_b32 s76, 0x3e0293ee\n"
    "s_mov_b32 s77, 0x3e0293ee\n"
    "v_mov_b32 v80, 0x3f803f80\n"
    "v_mov_b32 v81, 0x3f803f80\n"
    "v_mov_b32 v82, 0x3f803f80\n"
    "v_mov_b32 v83, 0x3f803f80\n"
    "v_lshrrev_b32 v8, 2, v1\n"
    "v_lshlrev_b32 v8, 3, v8\n"
    "v_lshl_add_u32 v8, v2, 8, v8\n"
    "v_and_b32 v9, 1, v2\n"
    "v_lshlrev_b32 v10, 1, v9\n"
    "v_and_b32 v11, 3, v1\n"
    "v_xor_b32 v10, v10, v11\n"
    "v_lshl_add_u32 v13, v10, 5, v8\n"
    "v_lshl_add_u32 v112, v9, 7, v13\n"
    "v_xor_b32 v10, 1, v10\n"
    "v_xor_b32 v9, 1, v9\n"
    "v_lshl_add_u32 v13, v10, 5, v8\n"
    "v_lshl_add_u32 v113, v9, 7, v13\n"
    "v_and_b32 v8, 13, v1\n"
    "v_lshlrev_b32 v8, 4, v8\n"
    "v_lshrrev_b32 v9, 1, v2\n"
    "v_lshl_add_u32 v8, v9, 8, v8\n"
    "v_and_b32 v9, 1, v2\n"
    "v_lshl_add_u32 v8, v9, 3, v8\n"
    "v_add_u32 v9, 1056, v8\n"
    "v_cndmask_b32 v75, v8, v9, s[70:71]\n"
    "v_lshrrev_b32 v8, 2, v1\n"
    "v_lshrrev_b32 v9, 3, v1\n"
    "v_xor_b32 v9, v9, v8\n"
    "v_lshlrev_b32 v9, 7, v9\n"
    "v_and_b32 v10, 3, v1\n"
    "v_lshl_add_u32 v9, v10, 3, v9\n"
    "v_xor_b32 v10, v8, v2\n"
    "v_lshl_add_u32 v114, v10, 5, v9\n"
    "v_and_b32 v10, 1, v2\n"
    "v_lshrrev_b32 v11, 1, v2\n"
    "v_lshl_add_u32 v10, v10, 1, v11\n"
    "v_xor_b32 v10, v8, v10\n"
    "v_lshl_add_u32 v115, v10, 5, v9\n"
    "v_lshrrev_b32 v79, 1, v71\n"
    "s_add_u32 s62, s36, 0x63e0000\n"
    "s_addc_u32 s63, s37, 0\n"
    "s_lshl_b32 s78, s42, 23\n"
    "s_add_u32 s62, s62, s78\n"
    "s_addc_u32 s63, s63, 0\n"
    "s_lshl_b32 s78, s41, 12\n"
    "s_add_u32 s62, s62, s78\n"
    "s_addc_u32 s63, s63, 0\n"
    "s_lshl_b32 s78, s41, 13\n"
    "s_lshl_b32 s79, s39, 10\n"
    "s_add_u32 s78, s78, s79\n"
    "s_add_u32 s58, s36, 0x1500800\n"
    "s_addc_u32 s59, s37, 0\n"
    "s_add_u32 s58, s58, s78\n"
    "s_addc_u32 s59, s59, 0\n"
    "s_add_u32 s78, s78, s79\n"
    "s_add_u32 s60, s36, 0x2500000\n"
    "s_addc_u32 s61, s37, 0\n"
    "s_add_u32 s60, s60, s78\n"
    "s_addc_u32 s61, s61, 0\n"
    "s_add_u32 s12, s36, 0x80000\n"
    "s_addc_u32 s13, s37, 0\n"
    "s_add_u32 s66, s36, 0x202000\n"
    "s_addc_u32 s67, s37, 0\n"
    "s_lshl_b32 s78, s41, 7\n"
    "s_add_u32 s66, s66, s78\n"
    "s_addc_u32 s67, s67, 0\n"
    "s_add_u32 s44, s36, 0x7400000\n"
    "s_addc_u32 s45, s37, 0\n"
    "s_waitcnt lgkmcnt(0)\n"
    "s_add_u32 s96, s96, 1\n"
    "s_addc_u32 s97, s97, 0\n"
    "s_lshl_b64 s[96:97], s[96:97], 6\n"
    "s_cmp_lg_u32 s42, 0\n"
    "s_cbranch_scc1 L_rb1%=\n"
    "s_waitcnt vmcnt(24) lgkmcnt(0)\n"
    "s_barrier\n"
    "s_add_u32 s32, s28, 0x10100\n"
    "s_addc_u32 s33, s29, 0\n"
    "s_add_u32 m0, s30, 114688\n"
    "s_nop 0\n"
    "global_load_lds_dwordx4 v88, s[32:33]\n"
    "s_add_u32 m0, s30, 115712\n"
    "s_nop 0\n"
    "global_load_lds_dwordx4 v89, s[32:33]\n"
    "s_add_u32 m0, s30, 116736\n"
    "s_nop 0\n"
    "global_load_lds_dwordx4 v90, s[32:33]\n"
    "s_add_u32 m0, s30, 117760\n"
    "s_nop 0\n"
    "global_load_lds_dwordx4 v91, s[32:33]\n"
    "ds_read_b64_tr_b16 a[128:129], v92 offset:0\n"
    "ds_read_b64_tr_b16 a[130:131], v92 offset:2048\n"
    "ds_read_b64_tr_b16 a[132:133], v92 offset:8192\n"
    "ds_read_b64_tr_b16 a[134:135], v92 offset:10240\n"
    "ds_read_b64_tr_b16 a[192:193], v93 offset:0\n"
    "ds_read_b64_tr_b16 a[194:195], v93 offset:2048\n"
    "ds_read_b64_tr_b16 a[196:197], v93 offset:8192\n"
    "ds_read_b64_tr_b16 a[198:199], v93 offset:10240\n"
    "s_waitcnt vmcnt(24) lgkmcnt(0)\n"
    "s_barrier\n"
    "s_add_u32 s32, s26, 0x200000\n"
    "s_addc_u32 s33, s27, 0\n"
    "s_add_u32 m0, s30, 0\n"
    "s_nop 0\n"
    "global_load_lds_dwordx4 v84, s[32:33]\n"
    "s_add_u32 m0, s30, 1024\n"
    "s_nop 0\n"
    "global_load_lds_dwordx4 v85, s[32:33]\n"
    "s_add_u32 m0, s30, 2048\n"
    "s_nop 0\n"
    "global_load_lds_dwordx4 v86, s[32:33]\n"
    "s_add_u32 m0, s30, 3072\n"
    "s_nop 0\n"
    "global_load_lds_dwordx4 v87, s[32:33]\n"
    "ds_read_b64_tr_b16 a[136:137], v92 offset:16384\n"
    "ds_read_b64_tr_b16 a[138:139], v92 offset:18432\n"
    "ds_read_b64_tr_b16 a[140:141], v92 offset:24576\n"
    "ds_read_b64_tr_b16 a[142:143], v92 offset:26624\n"
    "ds_read_b64_tr_b16 a[200:201], v93 offset:16384\n"
    "ds_read_b64_tr_b16 a[202:203], v93 offset:18432\n"
    "ds_read_b64_tr_b16 a[204:205], v93 offset:24576\n"
    "ds_read_b64_tr_b16 a[206:207], v93 offset:26624\n"
    "s_waitcnt vmcnt(24) lgkmcnt(0)\n"
    "s_barrier\n"
    "s_add_u32 s32, s26, 0x280000\n"
    "s_addc_u32 s33, s27, 0\n"
    "s_add_u32 m0, s30, 16384\n"
    "s_nop 0\n"
    "global_load_lds_dwordx4 v84, s[32:33]\n"
    "s_add_u32 m0, s30, 17408\n"
    "s_nop 0\n"
    "global_load_lds_dwordx4 v85, s[32:33]\n"
    "s_add_u32 m0, s30, 18432\n"
    "s_nop 0\n"
    "global_load_lds_dwordx4 v86, s[32:33]\n"
    "s_add_u32 m0, s30, 19456\n"
    "s_nop 0\n"
    "global_load_lds_dwordx4 v87, s[32:33]\n"
    "ds_read_b64_tr_b16 a[144:145], v92 offset:32768\n"
    "ds_read_b64_tr_b16 a[146:147], v92 offset:34816\n"
    "ds_read_b64_tr_b16 a[148:149], v92 offset:40960\n"
    "ds_read_b64_tr_b16 a[150:151], v92 offset:43008\n"
    "ds_read_b64_tr_b16 a[208:209], v93 offset:32768\n"
    "ds_read_b64_tr_b16 a[210:211], v93 offset:34816\n"
    "ds_read_b64_tr_b16 a[212:213], v93 offset:40960\n"
    "ds_read_b64_tr_b16 a[214:215], v93 offset:43008\n"
    "s_waitcnt vmcnt(24) lgkmcnt(0)\n"
    "s_barrier\n"
    "s_add_u32 s32, s26, 0x300000\n"
    "s_addc_u32 s33, s27, 0\n"
    "s_add_u32 m0, s30, 32768\n"
    "s_nop 0\n"
    "global_load_lds_dwordx4 v84, s[32:33]\n"
    "s_add_u32 m0, s30, 33792\n"
    "s_nop 0\n"
    "global_load_lds_dwordx4 v85, s[32:33]\n"
    "s_add_u32 m0, s30, 34816\n"
    "s_nop 0\n"
    "global_load_lds_dwordx4 v86, s[32:33]\n"
    "s_add_u32 m0, s30, 35840\n"
    "s_nop 0\n"
    "global_load_lds_dwordx4 v87, s[32:33]\n"
    "ds_read_b64_tr_b16 a[152:153], v92 offset:49152\n"
    "ds_read_b64_tr_b16 a[154:155], v92 offset:51200\n"
    "ds_read_b64_tr_b16 a[156:157], v92 offset:57344\n"
    "ds_read_b64_tr_b16 a[158:159], v92 offset:59392\n"
    "ds_read_b64_tr_b16 a[216:217], v93 offset:49152\n"
    "ds_read_b64_tr_b16 a[218:219], v93 offset:51200\n"
    "ds_read_b64_tr_b16 a[220:221], v93 offset:57344\n"
    "ds_read_b64_tr_b16 a[222:223], v93 offset:59392\n"
    "s_waitcnt vmcnt(24) lgkmcnt(0)\n"
    "s_barrier\n"
    "s_add_u32 s32, s26, 0x380000\n"
    "s_addc_u32 s33, s27, 0\n"
    "s_add_u32 m0, s30, 49152\n"
    "s_nop 0\n"
    "global_load_lds_dwordx4 v84, s[32:33]\n"
    "s_add_u32 m0, s30, 50176\n"
    "s_nop 0\n"
    "global_load_lds_dwordx4 v85, s[32:33]\n"
    "s_add_u32 m0, s30, 51200\n"
    "s_nop 0\n"
    "global_load_lds_dwordx4 v86, s[32:33]\n"
    "s_add_u32 m0, s30, 52224\n"
    "s_nop 0\n"
    "global_load_lds_dwordx4 v87, s[32:33]\n"
    "s_waitcnt vmcnt(24) lgkmcnt(0)\n"
    "s_barrier\n"
    "ds_read_b64_tr_b16 v[0:1], v104 offset:0\n"
    "ds_read_b64_tr_b16 v[2:3], v104 offset:512\n"
    "ds_read_b64_tr_b16 v[4:5], v104 offset:8192\n"
    "ds_read_b64_tr_b16 v[6:7], v104 offset:8704\n"
    "ds_read_b64_tr_b16 v[16:17], v104 offset:8\n"
    "ds_read_b64_tr_b16 v[18:19], v104 offset:520\n"
    "ds_read_b64_tr_b16 v[20:21], v104 offset:8200\n"
    "ds_read_b64_tr_b16 v[22:23], v104 offset:8712\n"
    "s_waitcnt lgkmcnt(4)\n"
    "ds_read_b64_tr_b16 v[32:33], v104 offset:16\n"
    "ds_read_b64_tr_b16 v[34:35], v104 offset:528\n"
    "ds_read_b64_tr_b16 v[36:37], v104 offset:8208\n"
    "ds_read_b64_tr_b16 v[38:39], v104 offset:8720\n"
    "ds_read_b64_tr_b16 v[48:49], v104 offset:24\n"
    "ds_read_b64_tr_b16 v[50:51], v104 offset:536\n"
    "ds_read_b64_tr_b16 v[52:53], v104 offset:8216\n"
    "ds_read_b64_tr_b16 v[54:55], v104 offset:8728\n"
    "s_waitcnt vmcnt(20) lgkmcnt(0)\n"
    "s_barrier\n"
    "s_add_u32 s32, s22, 0x0\n"
    "s_addc_u32 s33, s23, 0\n"
    "s_add_u32 m0, s30, 65536\n"
    "s_nop 0\n"
    "global_load_lds_dwordx4 v84, s[32:33]\n"
    "s_add_u32 m0, s30, 66560\n"
    "s_nop 0\n"
    "global_load_lds_dwordx4 v85, s[32:33]\n"
    "s_add_u32 m0, s30, 67584\n"
    "s_nop 0\n"
    "global_load_lds_dwordx4 v86, s[32:33]\n"
    "s_add_u32 m0, s30, 68608\n"
    "s_nop 0\n"
    "global_load_lds_dwordx4 v87, s[32:33]\n"
    "s_add_u32 s32, s22, 0x80000\n"
    "s_addc_u32 s33, s23, 0\n"
    "s_add_u32 m0, s30, 81920\n"
    "s_nop 0\n"
    "global_load_lds_dwordx4 v84, s[32:33]\n"
    "s_add_u32 m0, s30, 82944\n"
    "s_nop 0\n"
    "global_load_lds_dwordx4 v85, s[32:33]\n"
    "s_add_u32 m0, s30, 83968\n"
    "s_nop 0\n"
    "global_load_lds_dwordx4 v86, s[32:33]\n"
    "s_add_u32 m0, s30, 84992\n"
    "s_nop 0\n"
    "global_load_lds_dwordx4 v87, s[32:33]\n"
    "s_waitcnt vmcnt(24) lgkmcnt(0)\n"
    "s_barrier\n"
    "ds_read_b64_tr_b16 v[8:9], v104 offset:32768\n"
    "ds_read_b64_tr_b16 v[10:11], v104 offset:33280\n"
    "ds_read_b64_tr_b16 v[12:13], v104 offset:40960\n"
    "ds_read_b64_tr_b16 v[14:15], v104 offset:41472\n"
    "ds_read_b64_tr_b16 v[24:25], v104 offset:32776\n"
    "ds_read_b64_tr_b16 v[26:27], v104 offset:33288\n"
    "ds_read_b64_tr_b16 v[28:29], v104 offset:40968\n"
    "ds_read_b64_tr_b16 v[30:31], v104 offset:41480\n"
    "s_waitcnt lgkmcnt(4)\n"
    "ds_read_b64_tr_b16 v[40:41], v104 offset:32784\n"
    "ds_read_b64_tr_b16 v[42:43], v104 offset:33296\n"
    "ds_read_b64_tr_b16 v[44:45], v104 offset:40976\n"
    "ds_read_b64_tr_b16 v[46:47], v104 offset:41488\n"
    "ds_read_b64_tr_b16 v[56:57], v104 offset:32792\n"
    "ds_read_b64_tr_b16 v[58:59], v104 offset:33304\n"
    "ds_read_b64_tr_b16 v[60:61], v104 offset:40984\n"
    "ds_read_b64_tr_b16 v[62:63], v104 offset:41496\n"
    "s_waitcnt vmcnt(20) lgkmcnt(0)\n"
    "s_barrier\n"
    "s_add_u32 s32, s22, 0x100000\n"
    "s_addc_u32 s33, s23, 0\n"
    "s_add_u32 m0, s30, 98304\n"
    "s_nop 0\n"
    "global_load_lds_dwordx4 v84, s[32:33]\n"
    "s_add_u32 m0, s30, 99328\n"
    "s_nop 0\n"
    "global_load_lds_dwordx4 v85, s[32:33]\n"
    "s_add_u32 m0, s30, 100352\n"
    "s_nop 0\n"
    "global_load_lds_dwordx4 v86, s[32:33]\n"
    "s_add_u32 m0, s30, 101376\n"
    "s_nop 0\n"
    "global_load_lds_dwordx4 v87, s[32:33]\n"
    "s_add_u32 s32, s22, 0x180000\n"
    "s_addc_u32 s33, s23, 0\n"
    "s_add_u32 m0, s30, 114688\n"
    "s_nop 0\n"
    "global_load_lds_dwordx4 v84, s[32:33]\n"
    "s_add_u32 m0, s30, 115712\n"
    "s_nop 0\n"
    "global_load_lds_dwordx4 v85, s[32:33]\n"
    "s_add_u32 m0, s30, 116736\n"
    "s_nop 0\n"
    "global_load_lds_dwordx4 v86, s[32:33]\n"
    "s_add_u32 m0, s30, 117760\n"
    "s_nop 0\n"
    "global_load_lds_dwordx4 v87, s[32:33]\n"
    "ds_read_b64_tr_b16 a[160:161], v92 offset:0\n"
    "ds_read_b64_tr_b16 a[162:163], v92 offset:2048\n"
    "ds_read_b64_tr_b16 a[164:165], v92 offset:8192\n"
    "ds_read_b64_tr_b16 a[166:167], v92 offset:10240\n"
    "ds_read_b64_tr_b16 a[224:225], v93 offset:0\n"
    "ds_read_b64_tr_b16 a[226:227], v93 offset:2048\n"
    "ds_read_b64_tr_b16 a[228:229], v93 offset:8192\n"
    "ds_read_b64_tr_b16 a[230:231], v93 offset:10240\n"
    "s_waitcnt vmcnt(24) lgkmcnt(0)\n"
    "s_barrier\n"
    "s_add_u32 s32, s22, 0x200000\n"
    "s_addc_u32 s33, s23, 0\n"
    "s_add_u32 m0, s30, 0\n"
    "s_nop 0\n"
    "global_load_lds_dwordx4 v84, s[32:33]\n"
    "s_add_u32 m0, s30, 1024\n"
    "s_nop 0\n"
    "global_load_lds_dwordx4 v85, s[32:33]\n"
    "s_add_u32 m0, s30, 2048\n"
    "s_nop 0\n"
    "global_load_lds_dwordx4 v86, s[32:33]\n"
    "s_add_u32 m0, s30, 3072\n"
    "s_nop 0\n"
    "global_load_lds_dwordx4 v87, s[32:33]\n"
    "ds_read_b64_tr_b16 a[168:169], v92 offset:16384\n"
    "ds_read_b64_tr_b16 a[170:171], v92 offset:18432\n"
    "ds_read_b64_tr_b16 a[172:173], v92 offset:24576\n"
    "ds_read_b64_tr_b16 a[174:175], v92 offset:26624\n"
    "ds_read_b64_tr_b16 a[232:233], v93 offset:16384\n"
    "ds_read_b64_tr_b16 a[234:235], v93 offset:18432\n"
    "ds_read_b64_tr_b16 a[236:237], v93 offset:24576\n"
    "ds_read_b64_tr_b16 a[238:239], v93 offset:26624\n"
    "s_waitcnt vmcnt(24) lgkmcnt(0)\n"
    "s_barrier\n"
    "s_add_u32 s32, s22, 0x280000\n"
    "s_addc_u32 s33, s23, 0\n"
    "s_add_u32 m0, s30, 16384\n"
    "s_nop 0\n"
    "global_load_lds_dwordx4 v84, s[32:33]\n"
    "s_add_u32 m0, s30, 17408\n"
    "s_nop 0\n"
    "global_load_lds_dwordx4 v85, s[32:33]\n"
    "s_add_u32 m0, s30, 18432\n"
    "s_nop 0\n"
    "global_load_lds_dwordx4 v86, s[32:33]\n"
    "s_add_u32 m0, s30, 19456\n"
    "s_nop 0\n"
    "global_load_lds_dwordx4 v87, s[32:33]\n"
    "ds_read_b64_tr_b16 a[176:177], v92 offset:32768\n"
    "ds_read_b64_tr_b16 a[178:179], v92 offset:34816\n"
    "ds_read_b64_tr_b16 a[180:181], v92 offset:40960\n"
    "ds_read_b64_tr_b16 a[182:183], v92 offset:43008\n"
    "ds_read_b64_tr_b16 a[240:241], v93 offset:32768\n"
    "ds_read_b64_tr_b16 a[242:243], v93 offset:34816\n"
    "ds_read_b64_tr_b16 a[244:245], v93 offset:40960\n"
    "ds_read_b64_tr_b16 a[246:247], v93 offset:43008\n"
    "s_waitcnt vmcnt(24) lgkmcnt(0)\n"
    "s_barrier\n"
    "s_add_u32 s32, s22, 0x300000\n"
    "s_addc_u32 s33, s23, 0\n"
    "s_add_u32 m0, s30, 32768\n"
    "s_nop 0\n"
    "global_load_lds_dwordx4 v84, s[32:33]\n"
    "s_add_u32 m0, s30, 33792\n"
    "s_nop 0\n"
    "global_load_lds_dwordx4 v85, s[32:33]\n"
    "s_add_u32 m0, s30, 34816\n"
    "s_nop 0\n"
    "global_load_lds_dwordx4 v86, s[32:33]\n"
    "s_add_u32 m0, s30, 35840\n"
    "s_nop 0\n"
    "global_load_lds_dwordx4 v87, s[32:33]\n"
    "ds_read_b64_tr_b16 a[184:185], v92 offset:49152\n"
    "ds_read_b64_tr_b16 a[186:187], v92 offset:51200\n"
    "ds_read_b64_tr_b16 a[188:189], v92 offset:57344\n"
    "ds_read_b64_tr_b16 a[190:191], v92 offset:59392\n"
    "ds_read_b64_tr_b16 a[248:249], v93 offset:49152\n"
    "ds_read_b64_tr_b16 a[250:251], v93 offset:51200\n"
    "ds_read_b64_tr_b16 a[252:253], v93 offset:57344\n"
    "ds_read_b64_tr_b16 a[254:255], v93 offset:59392\n"
    "s_waitcnt vmcnt(24) lgkmcnt(0)\n"
    "s_barrier\n"
    "s_add_u32 s32, s22, 0x380000\n"
    "s_addc_u32 s33, s23, 0\n"
    "s_add_u32 m0, s30, 49152\n"
    "s_nop 0\n"
    "global_load_lds_dwordx4 v84, s[32:33]\n"
    "s_add_u32 m0, s30, 50176\n"
    "s_nop 0\n"
    "global_load_lds_dwordx4 v85, s[32:33]\n"
    "s_add_u32 m0, s30, 51200\n"
    "s_nop 0\n"
    "global_load_lds_dwordx4 v86, s[32:33]\n"
    "s_add_u32 m0, s30, 52224\n"
    "s_nop 0\n"
    "global_load_lds_dwordx4 v87, s[32:33]\n"
    "ds_read_b64_tr_b16 v[128:129], v98 offset:0\n"
    "ds_read_b64_tr_b16 v[130:131], v98 offset:2048\n"
    "ds_read_b64_tr_b16 v[132:133], v98 offset:8192\n"
    "ds_read_b64_tr_b16 v[134:135], v98 offset:10240\n"
    "ds_read_b64_tr_b16 v[136:137], v99 offset:0\n"
    "ds_read_b64_tr_b16 v[138:139], v99 offset:2048\n"
    "ds_read_b64_tr_b16 v[140:141], v99 offset:8192\n"
    "ds_read_b64_tr_b16 v[142:143], v99 offset:10240\n"
    "s_waitcnt vmcnt(24) lgkmcnt(0)\n"
    "s_barrier\n"
    "ds_read_b64_tr_b16 v[144:145], v98 offset:16384\n"
    "ds_read_b64_tr_b16 v[146:147], v98 offset:18432\n"
    "ds_read_b64_tr_b16 v[148:149], v98 offset:24576\n"
    "ds_read_b64_tr_b16 v[150:151], v98 offset:26624\n"
    "ds_read_b64_tr_b16 v[152:153], v99 offset:16384\n"
    "ds_read_b64_tr_b16 v[154:155], v99 offset:18432\n"
    "ds_read_b64_tr_b16 v[156:157], v99 offset:24576\n"
    "ds_read_b64_tr_b16 v[158:159], v99 offset:26624\n"
    "s_waitcnt vmcnt(20) lgkmcnt(0)\n"
    "s_barrier\n"
    "ds_read_b64_tr_b16 v[160:161], v98 offset:32768\n"
    "ds_read_b64_tr_b16 v[162:163], v98 offset:34816\n"
    "ds_read_b64_tr_b16 v[164:165], v98 offset:40960\n"
    "ds_read_b64_tr_b16 v[166:167], v98 offset:43008\n"
    "ds_read_b64_tr_b16 v[168:169], v99 offset:32768\n"
    "ds_read_b64_tr_b16 v[170:171], v99 offset:34816\n"
    "ds_read_b64_tr_b16 v[172:173], v99 offset:40960\n"
    "ds_read_b64_tr_b16 v[174:175], v99 offset:43008\n"
    "s_waitcnt vmcnt(16) lgkmcnt(0)\n"
    "s_barrier\n"
    "ds_read_b64_tr_b16 v[176:177], v98 offset:49152\n"
    "ds_read_b64_tr_b16 v[178:179], v98 offset:51200\n"
    "ds_read_b64_tr_b16 v[180:181], v98 offset:57344\n"
    "ds_read_b64_tr_b16 v[182:183], v98 offset:59392\n"
    "ds_read_b64_tr_b16 v[184:185], v99 offset:49152\n"
    "ds_read_b64_tr_b16 v[186:187], v99 offset:51200\n"
    "ds_read_b64_tr_b16 v[188:189], v99 offset:57344\n"
    "ds_read_b64_tr_b16 v[190:191], v99 offset:59392\n"
    "s_waitcnt vmcnt(12) lgkmcnt(0)\n"
    "s_barrier\n"
    "ds_read_b64_tr_b16 v[192:193], v96 offset:0\n"
    "ds_read_b64_tr_b16 v[194:195], v96 offset:2048\n"
    "ds_read_b64_tr_b16 v[196:197], v96 offset:8192\n"
    "ds_read_b64_tr_b16 v[198:199], v96 offset:10240\n"
    "ds_read_b64_tr_b16 v[200:201], v97 offset:0\n"
    "ds_read_b64_tr_b16 v[202:203], v97 offset:2048\n"
    "ds_read_b64_tr_b16 v[204:205], v97 offset:8192\n"
    "ds_read_b64_tr_b16 v[206:207], v97 offset:10240\n"
    "s_waitcnt vmcnt(8) lgkmcnt(0)\n"
    "s_barrier\n"
    "v_lshlrev_b32 v100, 3, v71\n"
    "v_min_u32 v101, 0x800, v100\n"
    "s_add_u32 s82, s36, 0x20c000\n"
    "s_addc_u32 s83, s37, 0\n"
    "s_add_u32 s84, s36, 0x312000\n"
    "s_addc_u32 s85, s37, 0\n"
    "v_and_b32 v126, 16, v71\n"
    "v_lshlrev_b32 v126, 8, v126\n"
    "global_load_dwordx2 v[84:85], v100, s[82:83] sc1\n"
    "global_load_dwordx2 v[86:87], v101, s[84:85] sc1\n"
    "global_load_dwordx2 v[124:125], v126, s[66:67] sc1\n"
    "ds_read_b64_tr_b16 v[208:209], v96 offset:16384\n"
    "ds_read_b64_tr_b16 v[210:211], v96 offset:18432\n"
    "ds_read_b64_tr_b16 v[212:213], v96 offset:24576\n"
    "ds_read_b64_tr_b16 v[214:215], v96 offset:26624\n"
    "ds_read_b64_tr_b16 v[216:217], v97 offset:16384\n"
    "ds_read_b64_tr_b16 v[218:219], v97 offset:18432\n"
    "ds_read_b64_tr_b16 v[220:221], v97 offset:24576\n"
    "ds_read_b64_tr_b16 v[222:223], v97 offset:26624\n"
    "s_waitcnt vmcnt(7) lgkmcnt(0)\n"
    "s_barrier\n"
    "ds_read_b64_tr_b16 v[224:225], v96 offset:32768\n"
    "ds_read_b64_tr_b16 v[226:227], v96 offset:34816\n"
    "ds_read_b64_tr_b16 v[228:229], v96 offset:40960\n"
    "ds_read_b64_tr_b16 v[230:231], v96 offset:43008\n"
    "ds_read_b64_tr_b16 v[232:233], v97 offset:32768\n"
    "ds_read_b64_tr_b16 v[234:235], v97 offset:34816\n"
    "ds_read_b64_tr_b16 v[236:237], v97 offset:40960\n"
    "ds_read_b64_tr_b16 v[238:239], v97 offset:43008\n"
    "s_waitcnt vmcnt(3) lgkmcnt(0)\n"
    "s_barrier\n"
    "ds_read_b64_tr_b16 v[240:241], v96 offset:49152\n"
    "ds_read_b64_tr_b16 v[242:243], v96 offset:51200\n"
    "ds_read_b64_tr_b16 v[244:245], v96 offset:57344\n"
    "ds_read_b64_tr_b16 v[246:247], v96 offset:59392\n"
    "ds_read_b64_tr_b16 v[248:249], v97 offset:49152\n"
    "ds_read_b64_tr_b16 v[250:251], v97 offset:51200\n"
    "ds_read_b64_tr_b16 v[252:253], v97 offset:57344\n"
    "ds_read_b64_tr_b16 v[254:255], v97 offset:59392\n"
    "s_branch L_rc1%=\n"
    "L_rb1%=:\n"
    "s_waitcnt vmcnt(24) lgkmcnt(0)\n"
    "s_barrier\n"
    "s_add_u32 s32, s28, 0x10100\n"
    "s_addc_u32 s33, s29, 0\n"
    "s_add_u32 m0, s30, 114688\n"
    "s_nop 0\n"
    "global_load_lds_dwordx4 v88, s[32:33]\n"
    "s_add_u32 m0, s30, 115712\n"
    "s_nop 0\n"
    "global_load_lds_dwordx4 v89, s[32:33]\n"
    "s_add_u32 m0, s30, 116736\n"
    "s_nop 0\n"
    "global_load_lds_dwordx4 v90, s[32:33]\n"
    "s_add_u32 m0, s30, 117760\n"
    "s_nop 0\n"
    "global_load_lds_dwordx4 v91, s[32:33]\n"
    "ds_read_b64_tr_b16 a[160:161], v92 offset:0\n"
    "ds_read_b64_tr_b16 a[162:163], v92 offset:2048\n"
    "ds_read_b64_tr_b16 a[164:165], v92 offset:8192\n"
    "ds_read_b64_tr_b16 a[166:167], v92 offset:10240\n"
    "ds_read_b64_tr_b16 a[224:225], v93 offset:0\n"
    "ds_read_b64_tr_b16 a[226:227], v93 offset:2048\n"
    "ds_read_b64_tr_b16 a[228:229], v93 offset:8192\n"
    "ds_read_b64_tr_b16 a[230:231], v93 offset:10240\n"
    "s_waitcnt vmcnt(24) lgkmcnt(0)\n"
    "s_barrier\n"
    "s_add_u32 s32, s26, 0x0\n"
    "s_addc_u32 s33, s27, 0\n"
    "s_add_u32 m0, s30, 0\n"
    "s_nop 0\n"
    "global_load_lds_dwordx4 v84, s[32:33]\n"
    "s_add_u32 m0, s30, 1024\n"
    "s_nop 0\n"
    "global_load_lds_dwordx4 v85, s[32:33]\n"
    "s_add_u32 m0, s30, 2048\n"
    "s_nop 0\n"
    "global_load_lds_dwordx4 v86, s[32:33]\n"
    "s_add_u32 m0, s30, 3072\n"
    "s_nop 0\n"
    "global_load_lds_dwordx4 v87, s[32:33]\n"
    "ds_read_b64_tr_b16 a[168:169], v92 offset:16384\n"
    "ds_read_b64_tr_b16 a[170:171], v92 offset:18432\n"
    "ds_read_b64_tr_b16 a[172:173], v92 offset:24576\n"
    "ds_read_b64_tr_b16 a[174:175], v92 offset:26624\n"
    "ds_read_b64_tr_b16 a[232:233], v93 offset:16384\n"
    "ds_read_b64_tr_b16 a[234:235], v93 offset:18432\n"
    "ds_read_b64_tr_b16 a[236:237], v93 offset:24576\n"
    "ds_read_b64_tr_b16 a[238:239], v93 offset:26624\n"
    "s_waitcnt vmcnt(24) lgkmcnt(0)\n"
    "s_barrier\n"
    "s_add_u32 s32, s26, 0x80000\n"
    "s_addc_u32 s33, s27, 0\n"
    "s_add_u32 m0, s30, 16384\n"
    "s_nop 0\n"
    "global_load_lds_dwordx4 v84, s[32:33]\n"
    "s_add_u32 m0, s30, 17408\n"
    "s_nop 0\n"
    "global_load_lds_dwordx4 v85, s[32:33]\n"
    "s_add_u32 m0, s30, 18432\n"
    "s_nop 0\n"
    "global_load_lds_dwordx4 v86, s[32:33]\n"
    "s_add_u32 m0, s30, 19456\n"
    "s_nop 0\n"
    "global_load_lds_dwordx4 v87, s[32:33]\n"
    "ds_read_b64_tr_b16 a[176:177], v92 offset:32768\n"
    "ds_read_b64_tr_b16 a[178:179], v92 offset:34816\n"
    "ds_read_b64_tr_b16 a[180:181], v92 offset:40960\n"
    "ds_read_b64_tr_b16 a[182:183], v92 offset:43008\n"
    "ds_read_b64_tr_b16 a[240:241], v93 offset:32768\n"
    "ds_read_b64_tr_b16 a[242:243], v93 offset:34816\n"
    "ds_read_b64_tr_b16 a[244:245], v93 offset:40960\n"
    "ds_read_b64_tr_b16 a[246:247], v93 offset:43008\n"
    "s_waitcnt vmcnt(24) lgkmcnt(0)\n"
    "s_barrier\n"
    "s_add_u32 s32, s26, 0x100000\n"
    "s_addc_u32 s33, s27, 0\n"
    "s_add_u32 m0, s30, 32768\n"
    "s_nop 0\n"
    "global_load_lds_dwordx4 v84, s[32:33]\n"
    "s_add_u32 m0, s30, 33792\n"
    "s_nop 0\n"
    "global_load_lds_dwordx4 v85, s[32:33]\n"
    "s_add_u32 m0, s30, 34816\n"
    "s_nop 0\n"
    "global_load_lds_dwordx4 v86, s[32:33]\n"
    "s_add_u32 m0, s30, 35840\n"
    "s_nop 0\n"
    "global_load_lds_dwordx4 v87, s[32:33]\n"
    "ds_read_b64_tr_b16 a[184:185], v92 offset:49152\n"
    "ds_read_b64_tr_b16 a[186:187], v92 offset:51200\n"
    "ds_read_b64_tr_b16 a[188:189], v92 offset:57344\n"
    "ds_read_b64_tr_b16 a[190:191], v92 offset:59392\n"
    "ds_read_b64_tr_b16 a[248:249], v93 offset:49152\n"
    "ds_read_b64_tr_b16 a[250:251], v93 offset:51200\n"
    "ds_read_b64_tr_b16 a[252:253], v93 offset:57344\n"
    "ds_read_b64_tr_b16 a[254:255], v93 offset:59392\n"
    "s_waitcnt vmcnt(24) lgkmcnt(0)\n"
    "s_barrier\n"
    "s_add_u32 s32, s26, 0x180000\n"
    "s_addc_u32 s33, s27, 0\n"
    "s_add_u32 m0, s30, 49152\n"
    "s_nop 0\n"
    "global_load_lds_dwordx4 v84, s[32:33]\n"
    "s_add_u32 m0, s30, 50176\n"
    "s_nop 0\n"
    "global_load_lds_dwordx4 v85, s[32:33]\n"
    "s_add_u32 m0, s30, 51200\n"
    "s_nop 0\n"
    "global_load_lds_dwordx4 v86, s[32:33]\n"
    "s_add_u32 m0, s30, 52224\n"
    "s_nop 0\n"
    "global_load_lds_dwordx4 v87, s[32:33]\n"
    "s_waitcnt vmcnt(24) lgkmcnt(0)\n"
    "s_barrier\n"
    "ds_read_b64_tr_b16 v[0:1], v104 offset:0\n"
    "ds_read_b64_tr_b16 v[2:3], v104 offset:512\n"
    "ds_read_b64_tr_b16 v[4:5], v104 offset:8192\n"
    "ds_read_b64_tr_b16 v[6:7], v104 offset:8704\n"
    "ds_read_b64_tr_b16 v[16:17], v104 offset:8\n"
    "ds_read_b64_tr_b16 v[18:19], v104 offset:520\n"
    "ds_read_b64_tr_b16 v[20:21], v104 offset:8200\n"
    "ds_read_b64_tr_b16 v[22:23], v104 offset:8712\n"
    "s_waitcnt lgkmcnt(4)\n"
    "ds_read_b64_tr_b16 v[32:33], v104 offset:16\n"
    "ds_read_b64_tr_b16 v[34:35], v104 offset:528\n"
    "ds_read_b64_tr_b16 v[36:37], v104 offset:8208\n"
    "ds_read_b64_tr_b16 v[38:39], v104 offset:8720\n"
    "ds_read_b64_tr_b16 v[48:49], v104 offset:24\n"
    "ds_read_b64_tr_b16 v[50:51], v104 offset:536\n"
    "ds_read_b64_tr_b16 v[52:53], v104 offset:8216\n"
    "ds_read_b64_tr_b16 v[54:55], v104 offset:8728\n"
    "s_waitcnt vmcnt(20) lgkmcnt(0)\n"
    "s_barrier\n"
    "s_add_u32 s32, s24, 0x0\n"
    "s_addc_u32 s33, s25, 0\n"
    "s_add_u32 m0, s30, 65536\n"
    "s_nop 0\n"
    "global_load_lds_dwordx4 v84, s[32:33]\n"
    "s_add_u32 m0, s30, 66560\n"
    "s_nop 0\n"
    "global_load_lds_dwordx4 v85, s[32:33]\n"
    "s_add_u32 m0, s30, 67584\n"
    "s_nop 0\n"
    "global_load_lds_dwordx4 v86, s[32:33]\n"
    "s_add_u32 m0, s30, 68608\n"
    "s_nop 0\n"
    "global_load_lds_dwordx4 v87, s[32:33]\n"
    "s_add_u32 s32, s24, 0x80000\n"
    "s_addc_u32 s33, s25, 0\n"
    "s_add_u32 m0, s30, 81920\n"
    "s_nop 0\n"
    "global_load_lds_dwordx4 v84, s[32:33]\n"
    "s_add_u32 m0, s30, 82944\n"
    "s_nop 0\n"
    "global_load_lds_dwordx4 v85, s[32:33]\n"
    "s_add_u32 m0, s30, 83968\n"
    "s_nop 0\n"
    "global_load_lds_dwordx4 v86, s[32:33]\n"
    "s_add_u32 m0, s30, 84992\n"
    "s_nop 0\n"
    "global_load_lds_dwordx4 v87, s[32:33]\n"
    "s_waitcnt vmcnt(24) lgkmcnt(0)\n"
    "s_barrier\n"
    "ds_read_b64_tr_b16 v[8:9], v104 offset:32768\n"
    "ds_read_b64_tr_b16 v[10:11], v104 offset:33280\n"
    "ds_read_b64_tr_b16 v[12:13], v104 offset:40960\n"
    "ds_read_b64_tr_b16 v[14:15], v104 offset:41472\n"
    "ds_read_b64_tr_b16 v[24:25], v104 offset:32776\n"
    "ds_read_b64_tr_b16 v[26:27], v104 offset:33288\n"
    "ds_read_b64_tr_b16 v[28:29], v104 offset:40968\n"
    "ds_read_b64_tr_b16 v[30:31], v104 offset:41480\n"
    "s_waitcnt lgkmcnt(4)\n"
    "ds_read_b64_tr_b16 v[40:41], v104 offset:32784\n"
    "ds_read_b64_tr_b16 v[42:43], v104 offset:33296\n"
    "ds_read_b64_tr_b16 v[44:45], v104 offset:40976\n"
    "ds_read_b64_tr_b16 v[46:47], v104 offset:41488\n"
    "ds_read_b64_tr_b16 v[56:57], v104 offset:32792\n"
    "ds_read_b64_tr_b16 v[58:59], v104 offset:33304\n"
    "ds_read_b64_tr_b16 v[60:61], v104 offset:40984\n"
    "ds_read_b64_tr_b16 v[62:63], v104 offset:41496\n"
    "s_waitcnt vmcnt(20) lgkmcnt(0)\n"
    "s_barrier\n"
    "s_add_u32 s32, s24, 0x100000\n"
    "s_addc_u32 s33, s25, 0\n"
    "s_add_u32 m0, s30, 98304\n"
    "s_nop 0\n"
    "global_load_lds_dwordx4 v84, s[32:33]\n"
    "s_add_u32 m0, s30, 99328\n"
    "s_nop 0\n"
    "global_load_lds_dwordx4 v85, s[32:33]\n"
    "s_add_u32 m0, s30, 100352\n"
    "s_nop 0\n"
    "global_load_lds_dwordx4 v86, s[32:33]\n"
    "s_add_u32 m0, s30, 101376\n"
    "s_nop 0\n"
    "global_load_lds_dwordx4 v87, s[32:33]\n"
    "s_add_u32 s32, s24, 0x180000\n"
    "s_addc_u32 s33, s25, 0\n"
    "s_add_u32 m0, s30, 114688\n"
    "s_nop 0\n"
    "global_load_lds_dwordx4 v84, s[32:33]\n"
    "s_add_u32 m0, s30, 115712\n"
    "s_nop 0\n"
    "global_load_lds_dwordx4 v85, s[32:33]\n"
    "s_add_u32 m0, s30, 116736\n"
    "s_nop 0\n"
    "global_load_lds_dwordx4 v86, s[32:33]\n"
    "s_add_u32 m0, s30, 117760\n"
    "s_nop 0\n"
    "global_load_lds_dwordx4 v87, s[32:33]\n"
    "ds_read_b64_tr_b16 a[128:129], v92 offset:0\n"
    "ds_read_b64_tr_b16 a[130:131], v92 offset:2048\n"
    "ds_read_b64_tr_b16 a[132:133], v92 offset:8192\n"
    "ds_read_b64_tr_b16 a[134:135], v92 offset:10240\n"
    "ds_read_b64_tr_b16 a[192:193], v93 offset:0\n"
    "ds_read_b64_tr_b16 a[194:195], v93 offset:2048\n"
    "ds_read_b64_tr_b16 a[196:197], v93 offset:8192\n"
    "ds_read_b64_tr_b16 a[198:199], v93 offset:10240\n"
    "s_waitcnt vmcnt(24) lgkmcnt(0)\n"
    "s_barrier\n"
    "s_add_u32 s32, s24, 0x200000\n"
    "s_addc_u32 s33, s25, 0\n"
    "s_add_u32 m0, s30, 0\n"
    "s_nop 0\n"
    "global_load_lds_dwordx4 v84, s[32:33]\n"
    "s_add_u32 m0, s30, 1024\n"
    "s_nop 0\n"
    "global_load_lds_dwordx4 v85, s[32:33]\n"
    "s_add_u32 m0, s30, 2048\n"
    "s_nop 0\n"
    "global_load_lds_dwordx4 v86, s[32:33]\n"
    "s_add_u32 m0, s30, 3072\n"
    "s_nop 0\n"
    "global_load_lds_dwordx4 v87, s[32:33]\n"
    "ds_read_b64_tr_b16 a[136:137], v92 offset:16384\n"
    "ds_read_b64_tr_b16 a[138:139], v92 offset:18432\n"
    "ds_read_b64_tr_b16 a[140:141], v92 offset:24576\n"
    "ds_read_b64_tr_b16 a[142:143], v92 offset:26624\n"
    "ds_read_b64_tr_b16 a[200:201], v93 offset:16384\n"
    "ds_read_b64_tr_b16 a[202:203], v93 offset:18432\n"
    "ds_read_b64_tr_b16 a[204:205], v93 offset:24576\n"
    "ds_read_b64_tr_b16 a[206:207], v93 offset:26624\n"
    "s_waitcnt vmcnt(24) lgkmcnt(0)\n"
    "s_barrier\n"
    "s_add_u32 s32, s24, 0x280000\n"
    "s_addc_u32 s33, s25, 0\n"
    "s_add_u32 m0, s30, 16384\n"
    "s_nop 0\n"
    "global_load_lds_dwordx4 v84, s[32:33]\n"
    "s_add_u32 m0, s30, 17408\n"
    "s_nop 0\n"
    "global_load_lds_dwordx4 v85, s[32:33]\n"
    "s_add_u32 m0, s30, 18432\n"
    "s_nop 0\n"
    "global_load_lds_dwordx4 v86, s[32:33]\n"
    "s_add_u32 m0, s30, 19456\n"
    "s_nop 0\n"
    "global_load_lds_dwordx4 v87, s[32:33]\n"
    "ds_read_b64_tr_b16 a[144:145], v92 offset:32768\n"
    "ds_read_b64_tr_b16 a[146:147], v92 offset:34816\n"
    "ds_read_b64_tr_b16 a[148:149], v92 offset:40960\n"
    "ds_read_b64_tr_b16 a[150:151], v92 offset:43008\n"
    "ds_read_b64_tr_b16 a[208:209], v93 offset:32768\n"
    "ds_read_b64_tr_b16 a[210:211], v93 offset:34816\n"
    "ds_read_b64_tr_b16 a[212:213], v93 offset:40960\n"
    "ds_read_b64_tr_b16 a[214:215], v93 offset:43008\n"
    "s_waitcnt vmcnt(24) lgkmcnt(0)\n"
    "s_barrier\n"
    "s_add_u32 s32, s24, 0x300000\n"
    "s_addc_u32 s33, s25, 0\n"
    "s_add_u32 m0, s30, 32768\n"
    "s_nop 0\n"
    "global_load_lds_dwordx4 v84, s[32:33]\n"
    "s_add_u32 m0, s30, 33792\n"
    "s_nop 0\n"
    "global_load_lds_dwordx4 v85, s[32:33]\n"
    "s_add_u32 m0, s30, 34816\n"
    "s_nop 0\n"
    "global_load_lds_dwordx4 v86, s[32:33]\n"
    "s_add_u32 m0, s30, 35840\n"
    "s_nop 0\n"
    "global_load_lds_dwordx4 v87, s[32:33]\n"
    "ds_read_b64_tr_b16 a[152:153], v92 offset:49152\n"
    "ds_read_b64_tr_b16 a[154:155], v92 offset:51200\n"
    "ds_read_b64_tr_b16 a[156:157], v92 offset:57344\n"
    "ds_read_b64_tr_b16 a[158:159], v92 offset:59392\n"
    "ds_read_b64_tr_b16 a[216:217], v93 offset:49152\n"
    "ds_read_b64_tr_b16 a[218:219], v93 offset:51200\n"
    "ds_read_b64_tr_b16 a[220:221], v93 offset:57344\n"
    "ds_read_b64_tr_b16 a[222:223], v93 offset:59392\n"
    "s_waitcnt vmcnt(24) lgkmcnt(0)\n"
    "s_barrier\n"
    "s_add_u32 s32, s24, 0x380000\n"
    "s_addc_u32 s33, s25, 0\n"
    "s_add_u32 m0, s30, 49152\n"
    "s_nop 0\n"
    "global_load_lds_dwordx4 v84, s[32:33]\n"
    "s_add_u32 m0, s30, 50176\n"
    "s_nop 0\n"
    "global_load_lds_dwordx4 v85, s[32:33]\n"
    "s_add_u32 m0, s30, 51200\n"
    "s_nop 0\n"
    "global_load_lds_dwordx4 v86, s[32:33]\n"
    "s_add_u32 m0, s30, 52224\n"
    "s_nop 0\n"
    "global_load_lds_dwordx4 v87, s[32:33]\n"
    "ds_read_b64_tr_b16 a[0:1], v98 offset:0\n"
    "ds_read_b64_tr_b16 a[2:3], v98 offset:2048\n"
    "ds_read_b64_tr_b16 a[4:5], v98 offset:8192\n"
    "ds_read_b64_tr_b16 a[6:7], v98 offset:10240\n"
    "ds_read_b64_tr_b16 a[64:65], v99 offset:0\n"
    "ds_read_b64_tr_b16 a[66:67], v99 offset:2048\n"
    "ds_read_b64_tr_b16 a[68:69], v99 offset:8192\n"
    "ds_read_b64_tr_b16 a[70:71], v99 offset:10240\n"
    "s_waitcnt vmcnt(24) lgkmcnt(0)\n"
    "s_barrier\n"
    "ds_read_b64_tr_b16 a[8:9], v98 offset:16384\n"
    "ds_read_b64_tr_b16 a[10:11], v98 offset:18432\n"
    "ds_read_b64_tr_b16 a[12:13], v98 offset:24576\n"
    "ds_read_b64_tr_b16 a[14:15], v98 offset:26624\n"
    "ds_read_b64_tr_b16 a[72:73], v99 offset:16384\n"
    "ds_read_b64_tr_b16 a[74:75], v99 offset:18432\n"
    "ds_read_b64_tr_b16 a[76:77], v99 offset:24576\n"
    "ds_read_b64_tr_b16 a[78:79], v99 offset:26624\n"
    "s_waitcnt vmcnt(20) lgkmcnt(0)\n"
    "s_barrier\n"
    "ds_read_b64_tr_b16 a[16:17], v98 offset:32768\n"
    "ds_read_b64_tr_b16 a[18:19], v98 offset:34816\n"
    "ds_read_b64_tr_b16 a[20:21], v98 offset:40960\n"
    "ds_read_b64_tr_b16 a[22:23], v98 offset:43008\n"
    "ds_read_b64_tr_b16 a[80:81], v99 offset:32768\n"
    "ds_read_b64_tr_b16 a[82:83], v99 offset:34816\n"
    "ds_read_b64_tr_b16 a[84:85], v99 offset:40960\n"
    "ds_read_b64_tr_b16 a[86:87], v99 offset:43008\n"
    "s_waitcnt vmcnt(16) lgkmcnt(0)\n"
    "s_barrier\n"
    "ds_read_b64_tr_b16 a[24:25], v98 offset:49152\n"
    "ds_read_b64_tr_b16 a[26:27], v98 offset:51200\n"
    "ds_read_b64_tr_b16 a[28:29], v98 offset:57344\n"
    "ds_read_b64_tr_b16 a[30:31], v98 offset:59392\n"
    "ds_read_b64_tr_b16 a[88:89], v99 offset:49152\n"
    "ds_read_b64_tr_b16 a[90:91], v99 offset:51200\n"
    "ds_read_b64_tr_b16 a[92:93], v99 offset:57344\n"
    "ds_read_b64_tr_b16 a[94:95], v99 offset:59392\n"
    "s_waitcnt vmcnt(12) lgkmcnt(0)\n"
    "s_barrier\n"
    "ds_read_b64_tr_b16 a[32:33], v96 offset:0\n"
    "ds_read_b64_tr_b16 a[34:35], v96 offset:2048\n"
    "ds_read_b64_tr_b16 a[36:37], v96 offset:8192\n"
    "ds_read_b64_tr_b16 a[38:39], v96 offset:10240\n"
    "ds_read_b64_tr_b16 a[96:97], v97 offset:0\n"
    "ds_read_b64_tr_b16 a[98:99], v97 offset:2048\n"
    "ds_read_b64_tr_b16 a[100:101], v97 offset:8192\n"
    "ds_read_b64_tr_b16 a[102:103], v97 offset:10240\n"
    "s_waitcnt vmcnt(8) lgkmcnt(0)\n"
    "s_barrier\n"
    "v_lshlrev_b32 v100, 3, v71\n"
    "v_min_u32 v101, 0x800, v100\n"
    "s_add_u32 s82, s36, 0x20c000\n"
    "s_addc_u32 s83, s37, 0\n"
    "s_add_u32 s84, s36, 0x312000\n"
    "s_addc_u32 s85, s37, 0\n"
    "v_and_b32 v126, 16, v71\n"
    "v_lshlrev_b32 v126, 8, v126\n"
    "global_load_dwordx2 v[84:85], v100, s[82:83] sc1\n"
    "global_load_dwordx2 v[86:87], v101, s[84:85] sc1\n"
    "global_load_dwordx2 v[124:125], v126, s[66:67] sc1\n"
    "ds_read_b64_tr_b16 a[40:41], v96 offset:16384\n"
    "ds_read_b64_tr_b16 a[42:43], v96 offset:18432\n"
    "ds_read_b64_tr_b16 a[44:45], v96 offset:24576\n"
    "ds_read_b64_tr_b16 a[46:47], v96 offset:26624\n"
    "ds_read_b64_tr_b16 a[104:105], v97 offset:16384\n"
    "ds_read_b64_tr_b16 a[106:107], v97 offset:18432\n"
    "ds_read_b64_tr_b16 a[108:109], v97 offset:24576\n"
    "ds_read_b64_tr_b16 a[110:111], v97 offset:26624\n"
    "s_waitcnt vmcnt(7) lgkmcnt(0)\n"
    "s_barrier\n"
    "ds_read_b64_tr_b16 a[48:49], v96 offset:32768\n"
    "ds_read_b64_tr_b16 a[50:51], v96 offset:34816\n"
    "ds_read_b64_tr_b16 a[52:53], v96 offset:40960\n"
    "ds_read_b64_tr_b16 a[54:55], v96 offset:43008\n"
    "ds_read_b64_tr_b16 a[112:113], v97 offset:32768\n"
    "ds_read_b64_tr_b16 a[114:115], v97 offset:34816\n"
    "ds_read_b64_tr_b16 a[116:117], v97 offset:40960\n"
    "ds_read_b64_tr_b16 a[118:119], v97 offset:43008\n"
    "s_waitcnt vmcnt(3) lgkmcnt(0)\n"
    "s_barrier\n"
    "ds_read_b64_tr_b16 a[56:57], v96 offset:49152\n"
    "ds_read_b64_tr_b16 a[58:59], v96 offset:51200\n"
    "ds_read_b64_tr_b16 a[60:61], v96 offset:57344\n"
    "ds_read_b64_tr_b16 a[62:63], v96 offset:59392\n"
    "ds_read_b64_tr_b16 a[120:121], v97 offset:49152\n"
    "ds_read_b64_tr_b16 a[122:123], v97 offset:51200\n"
    "ds_read_b64_tr_b16 a[124:125], v97 offset:57344\n"
    "ds_read_b64_tr_b16 a[126:127], v97 offset:59392\n"
    "L_rc1%=:\n"
    "s_waitcnt vmcnt(0) lgkmcnt(0)\n"
    "s_barrier\n"
    "v_xor_b32 v124, s96, v124\n"
    "v_xor_b32 v125, s97, v125\n"
    "v_cmp_gt_u64 vcc, 64, v[124:125]\n"
    "s_nop 1\n"
    "v_cndmask_b32 v124, 0, v124, vcc\n"
    "s_nop 0\n"
    "v_readlane_b32 s80, v124, 0\n"
    "v_readlane_b32 s81, v124, 1\n"
    "ds_write_b128 v116, v[60:63] offset:15360\n"
    "ds_write_b128 v116, v[56:59] offset:14336\n"
    "ds_write_b128 v116, v[52:55] offset:13312\n"
    "ds_write_b128 v116, v[48:51] offset:12288\n"
    "ds_write_b128 v116, v[44:47] offset:11264\n"
    "ds_write_b128 v116, v[40:43] offset:10240\n"
    "ds_write_b128 v116, v[36:39] offset:9216\n"
    "ds_write_b128 v116, v[32:35] offset:8192\n"
    "s_waitcnt lgkmcnt(4)\n"
    "ds_write_b128 v116, v[28:31] offset:7168\n"
    "ds_write_b128 v116, v[24:27] offset:6144\n"
    "ds_write_b128 v116, v[20:23] offset:5120\n"
    "ds_write_b128 v116, v[16:19] offset:4096\n"
    "ds_write_b128 v116, v[12:15] offset:3072\n"
    "ds_write_b128 v116, v[8:11] offset:2048\n"
    "ds_write_b128 v116, v[4:7] offset:1024\n"
    "ds_write_b128 v116, v[0:3] offset:0\n"
    "s_waitcnt lgkmcnt(4)\n"
    "s_min_u32 s92, s80, s81\n"
    "s_mov_b32 s43, 0\n"
    "s_mov_b32 s19, 0\n"
    "s_cmp_eq_u32 s92, 0\n"
    "s_cbranch_scc1 L_nokv%=\n"
    "global_load_dwordx4 v[56:59], v71, s[58:59] offset:-2048 sc1\n"
    "global_load_dwordx4 v[60:63], v71, s[58:59] offset:2048 sc1\n"
    "global_load_dwordx2 v[64:65], v71, s[60:61] offset:0 sc1\n"
    "global_load_dwordx2 v[68:69], v71, s[60:61] offset:8 sc1\n"
    "global_load_dwordx2 v[72:73], v71, s[60:61] offset:1024 sc1\n"
    "global_load_dwordx2 v[76:77], v71, s[60:61] offset:1032 sc1\n"
    "s_mov_b32 s19, 1\n"
    "L_nokv%=:\n"
    "s_cmp_lg_u32 s39, 0\n"
    "s_cbranch_scc1 L_sv1%=\n"
    "ds_write_b128 v116, a[0:3] offset:16384\n"
    "ds_write_b128 v116, a[4:7] offset:17408\n"
    "ds_write_b128 v116, a[64:67] offset:18432\n"
    "ds_write_b128 v116, a[68:71] offset:19456\n"
    "ds_write_b128 v116, a[8:11] offset:20480\n"
    "ds_write_b128 v116, a[12:15] offset:21504\n"
    "ds_write_b128 v116, a[72:75] offset:22528\n"
    "ds_write_b128 v116, a[76:79] offset:23552\n"
    "s_waitcnt lgkmcnt(4)\n"
    "ds_write_b128 v116, a[16:19] offset:24576\n"
    "ds_write_b128 v116, a[20:23] offset:25600\n"
    "ds_write_b128 v116, a[80:83] offset:26624\n"
    "ds_write_b128 v116, a[84:87] offset:27648\n"
    "ds_write_b128 v116, a[32:35] offset:28672\n"
    "ds_write_b128 v116, a[36:39] offset:29696\n"
    "ds_write_b128 v116, a[96:99] offset:30720\n"
    "ds_write_b128 v116, a[100:103] offset:31744\n"
    "s_waitcnt lgkmcnt(4)\n"
    "ds_write_b128 v116, v[128:131] offset:32768\n"
    "ds_write_b128 v116, v[132:135] offset:33792\n"
    "s_branch L_sv4%=\n"
    "L_sv1%=:\n"
    "s_cmp_lg_u32 s39, 1\n"
    "s_cbranch_scc1 L_sv2%=\n"
    "ds_write_b128 v116, a[16:19] offset:16384\n"
    "ds_write_b128 v116, a[20:23] offset:17408\n"
    "ds_write_b128 v116, a[80:83] offset:18432\n"
    "ds_write_b128 v116, a[84:87] offset:19456\n"
    "ds_write_b128 v116, a[24:27] offset:20480\n"
    "ds_write_b128 v116, a[28:31] offset:21504\n"
    "ds_write_b128 v116, a[88:91] offset:22528\n"
    "ds_write_b128 v116, a[92:95] offset:23552\n"
    "s_waitcnt lgkmcnt(4)\n"
    "ds_write_b128 v116, a[0:3] offset:24576\n"
    "ds_write_b128 v116, a[4:7] offset:25600\n"
    "ds_write_b128 v116, a[64:67] offset:26624\n"
    "ds_write_b128 v116, a[68:71] offset:27648\n"
    "ds_write_b128 v116, a[48:51] offset:28672\n"
    "ds_write_b128 v116, a[52:55] offset:29696\n"
    "ds_write_b128 v116, a[112:115] offset:30720\n"
    "ds_write_b128 v116, a[116:119] offset:31744\n"
    "s_waitcnt lgkmcnt(4)\n"
    "ds_write_b128 v116, v[160:163] offset:32768\n"
    "ds_write_b128 v116, v[164:167] offset:33792\n"
    "s_branch L_sv4%=\n"
    "L_sv2%=:\n"
    "s_cmp_lg_u32 s39, 2\n"
    "s_cbranch_scc1 L_sv3%=\n"
    "ds_write_b128 v116, a[32:35] offset:16384\n"
    "ds_write_b128 v116, a[36:39] offset:17408\n"
    "ds_write_b128 v116, a[96:99] offset:18432\n"
    "ds_write_b128 v116, a[100:103] offset:19456\n"
    "ds_write_b128 v116, a[40:43] offset:20480\n"
    "ds_write_b128 v116, a[44:47] offset:21504\n"
    "ds_write_b128 v116, a[104:107] offset:22528\n"
    "ds_write_b128 v116, a[108:111] offset:23552\n"
    "s_waitcnt lgkmcnt(4)\n"
    "ds_write_b128 v116, a[48:51] offset:24576\n"
    "ds_write_b128 v116, a[52:55] offset:25600\n"
    "ds_write_b128 v116, a[112:115] offset:26624\n"
    "ds_write_b128 v116, a[116:119] offset:27648\n"
    "ds_write_b128 v116, a[0:3] offset:28672\n"
    "ds_write_b128 v116, a[4:7] offset:29696\n"
    "ds_write_b128 v116, a[64:67] offset:30720\n"
    "ds_write_b128 v116, a[68:71] offset:31744\n"
    "s_waitcnt lgkmcnt(4)\n"
    "ds_write_b128 v116, v[192:195] offset:32768\n"
    "ds_write_b128 v116, v[196:199] offset:33792\n"
    "s_branch L_sv4%=\n"
    "L_sv3%=:\n"
    "ds_write_b128 v116, a[48:51] offset:16384\n"
    "ds_write_b128 v116, a[52:55] offset:17408\n"
    "ds_write_b128 v116, a[112:115] offset:18432\n"
    "ds_write_b128 v116, a[116:119] offset:19456\n"
    "ds_write_b128 v116, a[56:59] offset:20480\n"
    "ds_write_b128 v116, a[60:63] offset:21504\n"
    "ds_write_b128 v116, a[120:123] offset:22528\n"
    "ds_write_b128 v116, a[124:127] offset:23552\n"
    "s_waitcnt lgkmcnt(4)\n"
    "ds_write_b128 v116, a[32:35] offset:24576\n"
    "ds_write_b128 v116, a[36:39] offset:25600\n"
    "ds_write_b128 v116, a[96:99] offset:26624\n"
    "ds_write_b128 v116, a[100:103] offset:27648\n"
    "ds_write_b128 v116, a[16:19] offset:28672\n"
    "ds_write_b128 v116, a[20:23] offset:29696\n"
    "ds_write_b128 v116, a[80:83] offset:30720\n"
    "ds_write_b128 v116, a[84:87] offset:31744\n"
    "s_waitcnt lgkmcnt(4)\n"
    "ds_write_b128 v116, v[224:227] offset:32768\n"
    "ds_write_b128 v116, v[228:231] offset:33792\n"
    "L_sv4%=:\n"
    "v_lshrrev_b32 v0, 4, v71\n"
    "v_and_b32 v1, 15, v0\n"
    "v_lshrrev_b32 v2, 4, v0\n"
    "s_branch L_tabc%=\n"
    "L_tab%=:\n"
    "global_load_dwordx2 v[84:85], v100, s[82:83] sc1\n"
    "global_load_dwordx2 v[86:87], v101, s[84:85] sc1\n"
    "s_waitcnt vmcnt(0)\n"
    "L_tabc%=:\n"
    "v_xor_b32 v84, s96, v84\n"
    "v_xor_b32 v85, s97, v85\n"
    "v_cmp_gt_u64 vcc, 64, v[84:85]\n"
    "s_nop 1\n"
    "v_cndmask_b32 v84, 0, v84, vcc\n"
    "v_xor_b32 v86, s96, v86\n"
    "v_xor_b32 v87, s97, v87\n"
    "v_cmp_gt_u64 vcc, 64, v[86:87]\n"
    "s_nop 1\n"
    "v_cndmask_b32 v86, 0, v86, vcc\n"
    "v_min_u32 v9, v84, v86\n"
    "v_cmp_eq_u32 vcc, 0, v9\n"
    "s_nop 0\n"
    "s_cbranch_vccz L_tabok%=\n"
    "s_sleep 2\n"
    "s_add_u32 s90, s90, 1\n"
    "s_and_b32 s78, s90, 0xff\n"
    "s_cmp_lg_u32 s78, 0\n"
    "s_cbranch_scc1 L_tab%=\n"
    "v_mov_b32 v126, 0\n"
    "global_load_dwordx2 v[126:127], v126, s[68:69] sc1\n"
    "s_waitcnt vmcnt(0)\n"
    "v_cmp_eq_u64 vcc, s[96:97], v[126:127]\n"
    "s_nop 1\n"
    "s_cbranch_vccnz L_bail_tab%=\n"
    "s_cmp_lt_u32 s90, 0x40000\n"
    "s_cbranch_scc1 L_tab%=\n"
    "v_mov_b32 v126, s96\n"
    "v_mov_b32 v127, s97\n"
    "s_mov_b64 exec, 1\n"
    "global_store_dwordx2 v71, v[126:127], s[68:69] sc1\n"
    "s_mov_b64 exec, -1\n"
    "L_bail_tab%=:\n"
    "L_tabok%=:\n"
    "v_cmp_eq_u32 vcc, 2, v86\n"
    "s_nop 0\n"
    "s_mov_b32 s93, vcc_lo\n"
    "s_lshl_b32 s78, s42, 4\n"
    "s_lshl_b32 s79, s39, 2\n"
    "s_add_u32 s78, s78, s79\n"
    "v_lshrrev_b32 v8, 2, v1\n"
    "v_add_u32 v8, s78, v8\n"
    "v_lshlrev_b32 v10, 14, v8\n"
    "v_xor_b32 v8, s41, v8\n"
    "v_lshl_add_u32 v10, v8, 7, v10\n"
    "v_lshrrev_b32 v8, 1, v2\n"
    "v_lshl_add_u32 v10, v8, 12, v10\n"
    "v_and_b32 v8, 1, v2\n"
    "v_lshl_add_u32 v10, v8, 6, v10\n"
    "v_and_b32 v8, 3, v1\n"
    "v_lshl_add_u32 v10, v8, 4, v10\n"
    "s_and_b32 s78, s93, 1\n"
    "s_lshl_b32 s78, s78, 13\n"
    "v_add_u32 v117, s78, v10\n"
    "s_lshl_b32 s78, s42, 5\n"
    "s_add_u32 s78, s78, s41\n"
    "s_lshl_b32 s78, s78, 7\n"
    "s_add_u32 s64, s36, 0x220000\n"
    "s_addc_u32 s65, s37, 0\n"
    "s_add_u32 s64, s64, s78\n"
    "s_addc_u32 s65, s65, 0\n"
    "s_lshr_b32 s81, s93, 16\n"
    "s_and_b32 s81, s81, 1\n"
    "s_lshl_b32 s81, s81, 13\n"
    "s_and_b32 s79, s11, 3\n"
    "s_lshl_b32 s78, s42, 2\n"
    "s_add_u32 s79, s79, s78\n"
    "s_lshl_b32 s79, s79, 14\n"
    "s_add_u32 s78, s81, s79\n"
    "s_add_u32 s54, s36, 0x0\n"
    "s_addc_u32 s55, s37, 0\n"
    "s_add_u32 s54, s54, s78\n"
    "s_addc_u32 s55, s55, 0\n"
    "s_lshl_b32 s80, s39, 11\n"
    "s_add_u32 s54, s54, s80\n"
    "s_addc_u32 s55, s55, 0\n"
    "s_mov_b64 s[48:49], s[54:55]\n"
    "s_lshl_b32 s79, s38, 6\n"
    "s_add_u32 s79, s79, s81\n"
    "s_add_u32 s56, s12, s79\n"
    "s_addc_u32 s57, s13, 0\n"
    "s_and_b32 s78, s78, 0xffff\n"
    "s_add_u32 s12, s12, s78\n"
    "s_addc_u32 s13, s13, 0\n"
    "v_lshlrev_b32 v118, 10, v71\n"
    "v_lshlrev_b32 v119, 2, v71\n"
    "s_mov_b32 s43, 0\n"
    "v_mov_b64 v[104:105], 0\n"
    "v_mov_b64 v[106:107], 0\n"
    "v_mov_b64 v[108:109], 0\n"
    "v_mov_b64 v[110:111], 0\n"
    "v_mov_b64 v[32:33], 0\n"
    "v_mov_b64 v[34:35], 0\n"
    "v_mov_b64 v[36:37], 0\n"
    "v_mov_b64 v[38:39], 0\n"
    "v_mov_b64 v[40:41], 0\n"
    "v_mov_b64 v[42:43], 0\n"
    "v_mov_b64 v[44:45], 0\n"
    "v_mov_b64 v[46:47], 0\n"
    "v_mov_b64 v[48:49], 0\n"
    "v_mov_b64 v[50:51], 0\n"
    "v_mov_b64 v[52:53], 0\n"
    "v_mov_b64 v[54:55], 0\n"
    "s_cmp_lg_u32 s19, 0\n"
    "s_cbranch_scc1 L_kvdone%=\n"
    "s_add_u32 s79, s43, 1\n"
    "s_min_u32 s79, s79, 63\n"
    "s_lshr_b32 s79, s79, 1\n"
    "s_cmp_lt_u32 s79, s92\n"
    "s_cbranch_scc1 L_cntokp%=\n"
    "L_cntp%=:\n"
    "v_and_b32 v126, 16, v71\n"
    "v_lshlrev_b32 v126, 8, v126\n"
    "global_load_dwordx2 v[126:127], v126, s[66:67] sc1\n"
    "s_waitcnt vmcnt(0)\n"
    "v_xor_b32 v126, s96, v126\n"
    "v_xor_b32 v127, s97, v127\n"
    "v_cmp_gt_u64 vcc, 64, v[126:127]\n"
    "s_nop 1\n"
    "v_cndmask_b32 v126, 0, v126, vcc\n"
    "s_nop 0\n"
    "v_readlane_b32 s80, v126, 0\n"
    "v_readlane_b32 s81, v126, 1\n"
    "s_nop 1\n"
    "s_min_u32 s92, s80, s81\n"
    "s_cmp_lt_u32 s79, s92\n"
    "s_cbranch_scc1 L_cntokp%=\n"
    "s_sleep 2\n"
    "s_add_u32 s90, s90, 1\n"
    "s_and_b32 s78, s90, 0xff\n"
    "s_cmp_lg_u32 s78, 0\n"
    "s_cbranch_scc1 L_cntp%=\n"
    "v_mov_b32 v126, 0\n"
    "global_load_dwordx2 v[126:127], v126, s[68:69] sc1\n"
    "s_waitcnt vmcnt(0)\n"
    "v_cmp_eq_u64 vcc, s[96:97], v[126:127]\n"
    "s_nop 1\n"
    "s_cbranch_vccnz L_bail_cntp%=\n"
    "s_cmp_lt_u32 s90, 0x40000\n"
    "s_cbranch_scc1 L_cntp%=\n"
    "v_mov_b32 v126, s96\n"
    "v_mov_b32 v127, s97\n"
    "s_mov_b64 exec, 1\n"
    "global_store_dwordx2 v71, v[126:127], s[68:69] sc1\n"
    "s_mov_b64 exec, -1\n"
    "L_bail_cntp%=:\n"
    "L_cntokp%=:\n"
    "global_load_dwordx4 v[56:59], v71, s[58:59] offset:-2048 sc1\n"
    "global_load_dwordx4 v[60:63], v71, s[58:59] offset:2048 sc1\n"
    "global_load_dwordx2 v[64:65], v71, s[60:61] offset:0 sc1\n"
    "global_load_dwordx2 v[68:69], v71, s[60:61] offset:8 sc1\n"
    "global_load_dwordx2 v[72:73], v71, s[60:61] offset:1024 sc1\n"
    "global_load_dwordx2 v[76:77], v71, s[60:61] offset:1032 sc1\n"
    "L_kvdone%=:\n"
    "s_add_u32 s58, s58, 0x40000\n"
    "s_addc_u32 s59, s59, 0\n"
    "s_add_u32 s60, s60, 0x40000\n"
    "s_addc_u32 s61, s61, 0\n"
    "s_waitcnt lgkmcnt(0)\n"
    "s_memtime s[6:7]\n"
    "s_memrealtime s[78:79]\n"
    "s_nop 3\n"
    "s_memrealtime s[80:81]\n"
    "s_nop 3\n"
    "s_memrealtime s[82:83]\n"
    "s_nop 3\n"
    "s_memrealtime s[84:85]\n"
    "s_nop 3\n"
    "s_memrealtime s[86:87]\n"
    "s_nop 3\n"
    "s_memrealtime s[88:89]\n"
    "s_mov_b32 s10, 0\n"
    "s_waitcnt lgkmcnt(0)\n"
    "s_mul_i32 s91, s78, 24\n"
    "s_cmp_eq_u32 s78, s80\n"
    "s_addc_u32 s10, s10, 0\n"
    "s_cmp_eq_u32 s78, s82\n"
    "s_addc_u32 s10, s10, 0\n"
    "s_cmp_eq_u32 s78, s84\n"
    "s_addc_u32 s10, s10, 0\n"
    "s_cmp_eq_u32 s78, s86\n"
    "s_addc_u32 s10, s10, 0\n"
    "s_cmp_eq_u32 s78, s88\n"
    "s_addc_u32 s10, s10, 0\n"
    "s_mul_i32 s10, s10, 20\n"
    "s_sub_u32 s91, s91, s6\n"
    "s_sub_u32 s91, s91, s10\n"
    "s_mov_b32 s94, 90\n"
    "s_mov_b32 s51, 2060\n"
    "s_mov_b32 s15, 740\n"
    "s_mov_b32 s3, 1140\n"
    "s_mov_b32 s16, s6\n"
    "s_add_u32 s46, s91, 2400\n"
    "s_cmp_lt_u32 s43, 29\n"
    "s_cselect_b32 s79, 170, 0\n"
    "s_cselect_b32 s95, 20, 0\n"
    "s_add_u32 s95, s95, 1220\n"
    "s_add_u32 s46, s46, s79\n"
    "s_mov_b32 s47, s91\n"
    "s_mov_b32 s18, s16\n"
    "s_getpc_b64 s[4:5]\n"
    "L_pc%=:\n"
    "s_add_u32 s4, s4, L_sled%=-L_pc%=\n"
    "s_addc_u32 s5, s5, 0\n"
    "v_or_b32 v84, 1, s96\n"
    "v_mov_b32 v85, s97\n"
    "v_mov_b32 v86, s43\n"
    "s_cmp_eq_u32 s39, 1\n"
    "s_cbranch_scc1 L_poll1%=\n"
    "s_cmp_eq_u32 s39, 2\n"
    "s_cbranch_scc1 L_poll2%=\n"
    "s_cmp_eq_u32 s39, 3\n"
    "s_cbranch_scc1 L_poll3%=\n"
    "s_branch L_poll0%=\n"
    ".p2align 6\n"
    "L_poll0%=:\n"
    "global_load_dwordx4 v[0:3], v71, s[54:55] offset:0 sc1\n"
    "global_load_dwordx4 v[4:7], v71, s[54:55] offset:1024 sc1\n"
    "s_waitcnt vmcnt(0)\n"
    "s_branch L_top0%=\n"
    ".p2align 6\n"
    "L_top0%=:\n"
    "s_waitcnt vmcnt(3)\n"
    "v_smfmac_f32_16x16x64_bf16 v[40:43], v[0:3], a[0:7], v67\n"
    "ds_write_b128 v71, v[0:3] offset:0\n"
    "v_smfmac_f32_16x16x64_bf16 v[44:47], v[0:3], a[64:71], v67\n"
    "v_smfmac_f32_16x16x64_bf16 v[32:35], v[0:3], v[128:135], v67\n"
    "v_smfmac_f32_16x16x64_bf16 v[36:39], v[0:3], v[136:143], v67\n"
    "s_waitcnt vmcnt(2)\n"
    "v_smfmac_f32_16x16x64_bf16 v[40:43], v[4:7], a[8:15], v67\n"
    "ds_write_b128 v71, v[4:7] offset:1024\n"
    "s_waitcnt lgkmcnt(1)\n"
    "s_barrier\n"
    "ds_read_b128 v[8:11], v71 offset:2048\n"
    "v_smfmac_f32_16x16x64_bf16 v[44:47], v[4:7], a[72:79], v67\n"
    "ds_read_b128 v[16:19], v71 offset:4096\n"
    "v_smfmac_f32_16x16x64_bf16 v[32:35], v[4:7], v[144:151], v67\n"
    "ds_read_b128 v[24:27], v71 offset:6144\n"
    "v_smfmac_f32_16x16x64_bf16 v[36:39], v[4:7], v[152:159], v67\n"
    "s_waitcnt lgkmcnt(3)\n"
    "s_barrier\n"
    "v_smfmac_f32_16x16x64_bf16 v[48:51], v[0:3], a[128:135], v67\n"
    "s_waitcnt lgkmcnt(2)\n"
    "ds_read_b128 v[12:15], v71 offset:3072\n"
    "v_smfmac_f32_16x16x64_bf16 v[32:35], v[8:11], v[160:167], v67\n"
    "ds_read_b128 v[20:23], v71 offset:5120\n"
    "v_smfmac_f32_16x16x64_bf16 v[36:39], v[8:11], v[168:175], v67\n"
    "s_waitcnt lgkmcnt(2)\n"
    "ds_read_b128 v[28:31], v71 offset:7168\n"
    "v_smfmac_f32_16x16x64_bf16 v[32:35], v[16:19], v[192:199], v67\n"
    "ds_read_b128 a[0:3], v116 offset:0\n"
    "v_smfmac_f32_16x16x64_bf16 v[36:39], v[16:19], v[200:207], v67\n"
    "ds_read_b128 a[4:7], v116 offset:1024\n"
    "v_smfmac_f32_16x16x64_bf16 v[32:35], v[24:27], v[224:231], v67\n"
    "ds_read_b128 a[64:67], v116 offset:4096\n"
    "v_smfmac_f32_16x16x64_bf16 v[36:39], v[24:27], v[232:239], v67\n"
    "s_waitcnt lgkmcnt(5)\n"
    "ds_read_b128 a[68:71], v116 offset:5120\n"
    "v_smfmac_f32_16x16x64_bf16 v[32:35], v[12:15], v[176:183], v67\n"
    "ds_read_b128 a[8:11], v116 offset:2048\n"
    "v_smfmac_f32_16x16x64_bf16 v[36:39], v[12:15], v[184:191], v67\n"
    "s_waitcnt lgkmcnt(5)\n"
    "ds_read_b128 a[12:15], v116 offset:3072\n"
    "v_smfmac_f32_16x16x64_bf16 v[32:35], v[20:23], v[208:215], v67\n"
    "ds_read_b128 a[72:75], v116 offset:6144\n"
    "v_smfmac_f32_16x16x64_bf16 v[36:39], v[20:23], v[216:223], v67\n"
    "ds_read_b128 a[76:79], v116 offset:7168\n"
    "v_smfmac_f32_16x16x64_bf16 v[32:35], v[28:31], v[240:247], v67\n"
    "v_smfmac_f32_16x16x64_bf16 v[36:39], v[28:31], v[248:255], v67\n"
    "v_smfmac_f32_16x16x64_bf16 v[40:43], v[8:11], a[16:23], v67\n"
    "v_add_f32 v32, v32, v34\n"
    "v_add_f32 v33, v33, v35\n"
    "v_smfmac_f32_16x16x64_bf16 v[44:47], v[8:11], a[80:87], v67\n"
    "ds_read_b128 a[16:19], v116 offset:8192\n"
    "v_cvt_pk_bf16_f32 v126, v32, v33\n"
    "v_add_f32 v36, v36, v38\n"
    "v_add_f32 v37, v37, v39\n"
    "v_cvt_pk_bf16_f32 v127, v36, v37\n"
    "v_smfmac_f32_16x16x64_bf16 v[40:43], v[16:19], a[32:39], v67\n"
    "ds_write_b64 v114, v[126:127] offset:8192\n"
    "v_smfmac_f32_16x16x64_bf16 v[44:47], v[16:19], a[96:103], v67\n"
    "ds_read_b128 a[20:23], v116 offset:9216\n"
    "v_cmp_u_f32 vcc, v32, v33\n"
    "v_smfmac_f32_16x16x64_bf16 v[40:43], v[24:27], a[48:55], v67\n"
    "ds_read_b128 a[80:83], v116 offset:12288\n"
    "v_smfmac_f32_16x16x64_bf16 v[44:47], v[24:27], a[112:119], v67\n"
    "s_cbranch_vccnz L_slow0%=\n"
    "L_go0%=:\n"
    "v_smfmac_f32_16x16x64_bf16 v[40:43], v[12:15], a[24:31], v67\n"
    "s_waitcnt lgkmcnt(2)\n"
    "s_barrier\n"
    "ds_read_b64_tr_b16 v[32:33], v112 offset:8192\n"
    "v_smfmac_f32_16x16x64_bf16 v[44:47], v[12:15], a[88:95], v67\n"
    "ds_read_b64_tr_b16 v[34:35], v113 offset:8192\n"
    "ds_read_b64_tr_b16 v[36:37], v112 offset:9216\n"
    "v_smfmac_f32_16x16x64_bf16 v[40:43], v[20:23], a[40:47], v67\n"
    "ds_read_b64_tr_b16 v[38:39], v113 offset:9216\n"
    "v_smfmac_f32_16x16x64_bf16 v[44:47], v[20:23], a[104:111], v67\n"
    "global_load_dwordx4 v[132:135], v71, s[60:61] offset:1024 sc1\n"
    "v_smfmac_f32_16x16x64_bf16 v[40:43], v[28:31], a[56:63], v67\n"
    "ds_read_b128 a[84:87], v116 offset:13312\n"
    "v_smfmac_f32_16x16x64_bf16 v[44:47], v[28:31], a[120:127], v67\n"
    "s_waitcnt lgkmcnt(1)\n"
    "ds_read_b128 a[32:35], v116 offset:10240\n"
    "v_mfma_f32_16x16x32_bf16 v[88:91], v[56:59], v[32:35], 0\n"
    "v_add_f32 v40, v40, v42\n"
    "v_add_f32 v41, v41, v43\n"
    "v_mfma_f32_16x16x32_bf16 v[88:91], v[60:63], v[36:39], v[88:91]\n"
    "v_cvt_pk_bf16_f32 v126, v40, v41\n"
    "v_add_f32 v44, v44, v46\n"
    "v_add_f32 v45, v45, v47\n"
    "v_cvt_pk_bf16_f32 v127, v44, v45\n"
    "v_smfmac_f32_16x16x64_bf16 v[52:55], v[0:3], a[192:199], v67\n"
    "ds_write_b64 v115, v[126:127] offset:10240\n"
    "v_add_f32_dpp v88, v90, v88 quad_perm:[2,3,2,3] row_mask:0xf bank_mask:0xf\n"
    "v_add_f32_dpp v89, v91, v89 quad_perm:[2,3,2,3] row_mask:0xf bank_mask:0xf\n"
    "v_smfmac_f32_16x16x64_bf16 v[48:51], v[4:7], a[136:143], v67\n"
    "ds_write_b64 v75, v[88:89] offset:14336\n"
    "v_smfmac_f32_16x16x64_bf16 v[52:55], v[4:7], a[200:207], v67\n"
    "global_load_dwordx4 v[56:59], v71, s[58:59] offset:-2048 sc1\n"
    "v_smfmac_f32_16x16x64_bf16 v[48:51], v[8:11], a[144:151], v67\n"
    "global_load_dwordx4 v[60:63], v71, s[58:59] offset:2048 sc1\n"
    "v_smfmac_f32_16x16x64_bf16 v[52:55], v[8:11], a[208:215], v67\n"
    "global_load_dwordx4 v[128:131], v71, s[60:61] offset:0 sc1\n"
    "v_smfmac_f32_16x16x64_bf16 v[48:51], v[16:19], a[160:167], v67\n"
    "s_waitcnt lgkmcnt(0)\n"
    "s_barrier\n"
    "ds_read_b64_tr_b16 v[40:41], v112 offset:10240\n"
    "v_smfmac_f32_16x16x64_bf16 v[52:55], v[16:19], a[224:231], v67\n"
    "ds_read_b64_tr_b16 v[42:43], v113 offset:10240\n"
    "ds_read_b64_tr_b16 v[44:45], v112 offset:11264\n"
    "v_smfmac_f32_16x16x64_bf16 v[48:51], v[24:27], a[176:183], v67\n"
    "ds_read_b64_tr_b16 v[46:47], v113 offset:11264\n"
    "v_smfmac_f32_16x16x64_bf16 v[52:55], v[24:27], a[240:247], v67\n"
    "ds_read_b128 v[88:91], v71 offset:14336\n"
    "s_cmp_eq_u32 s43, 63\n"
    "v_smfmac_f32_16x16x64_bf16 v[48:51], v[12:15], a[152:159], v67\n"
    "s_waitcnt lgkmcnt(3)\n"
    "ds_read_b128 a[36:39], v116 offset:11264\n"
    "v_mfma_f32_16x16x32_bf16 v[96:99], v[40:43], v[32:35], 0\n"
    "s_waitcnt lgkmcnt(2)\n"
    "ds_read_b128 a[96:99], v116 offset:14336\n"
    "v_mfma_f32_16x16x32_bf16 v[96:99], v[44:47], v[36:39], v[96:99]\n"
    "s_cbranch_scc1 L_last0%=\n"
    "v_smfmac_f32_16x16x64_bf16 v[52:55], v[12:15], a[216:223], v67\n"
    "s_waitcnt lgkmcnt(2)\n"
    "ds_read_b128 a[100:103], v116 offset:15360\n"
    "v_max3_f32 v122, v88, v89, v90\n"
    "v_max_f32 v122, v122, v91\n"
    "v_add_f32_dpp v96, v98, v96 quad_perm:[2,3,2,3] row_mask:0xf bank_mask:0xf\n"
    "v_add_f32_dpp v97, v99, v97 quad_perm:[2,3,2,3] row_mask:0xf bank_mask:0xf\n"
    "v_max_f32_dpp v123, v122, v122 quad_perm:[2,3,0,1] row_mask:0xf bank_mask:0xf\n"
    "v_max3_f32 v122, v123, v96, v97\n"
    "v_smfmac_f32_16x16x64_bf16 v[48:51], v[20:23], a[168:175], v67\n"
    "v_max_f32_dpp v121, v122, v123 quad_perm:[0,1,0,1] row_mask:0xf bank_mask:0xf\n"
    "v_max_f32_dpp v123, v122, v123 quad_perm:[0,1,0,1] row_mask:0xf bank_mask:0xf\n"
    "v_smfmac_f32_16x16x64_bf16 v[52:55], v[20:23], a[232:239], v67\n"
    "v_permlane16_swap_b32 v123, v121\n"
    "v_max_f32 v122, v123, v121\n"
    "v_max_f32 v123, v123, v121\n"
    "v_smfmac_f32_16x16x64_bf16 v[48:51], v[28:31], a[184:191], v67\n"
    "v_smfmac_f32_16x16x64_bf16 v[52:55], v[28:31], a[248:255], v67\n"
    "v_permlane32_swap_b32 v122, v123\n"
    "v_max_f32 v122, v122, v123\n"
    "v_mul_f32 v122, s76, v122\n"
    "v_pk_fma_f32 v[96:97], v[96:97], s[76:77], v[122:123] op_sel_hi:[1,1,0] neg_lo:[0,0,1] neg_hi:[0,0,1]\n"
    "v_pk_fma_f32 v[88:89], v[88:89], s[76:77], v[122:123] op_sel_hi:[1,1,0] neg_lo:[0,0,1] neg_hi:[0,0,1]\n"
    "v_pk_fma_f32 v[90:91], v[90:91], s[76:77], v[122:123] op_sel_hi:[1,1,0] neg_lo:[0,0,1] neg_hi:[0,0,1]\n"
    "v_exp_f32 v96, v96\n"
    "v_exp_f32 v97, v97\n"
    "v_exp_f32 v88, v88\n"
    "v_exp_f32 v89, v89\n"
    "v_exp_f32 v90, v90\n"
    "v_exp_f32 v91, v91\n"
    "v_pk_add_f32 v[48:49], v[48:49], v[50:51]\n"
    "v_pk_add_f32 v[52:53], v[52:53], v[54:55]\n"
    "v_cvt_pk_bf16_f32 v126, v96, v97\n"
    "v_cvt_pk_bf16_f32 v100, v88, v89\n"
    "v_cvt_pk_bf16_f32 v102, v90, v91\n"
    "v_cndmask_b32 v101, 0, v126, s[72:73]\n"
    "v_cndmask_b32 v103, 0, v126, s[74:75]\n"
    "v_cvt_pk_bf16_f32 v66, v48, v49\n"
    "v_cvt_pk_bf16_f32 v70, v48, v49\n"
    "v_cvt_pk_bf16_f32 v74, v52, v53\n"
    "v_cvt_pk_bf16_f32 v78, v52, v53\n"
    "v_smfmac_f32_16x16x64_bf16 v[104:107], v[100:103], v[64:71], v67\n"
    "v_smfmac_f32_16x16x64_bf16 v[108:111], v[100:103], v[72:79], v67\n"
    "v_mfma_f32_16x16x32_bf16 v[84:87], v[100:103], v[80:83], 0\n"
    "v_add_f32 v104, v104, v106\n"
    "v_add_f32 v105, v105, v107\n"
    "v_cvt_pk_bf16_f32 v104, v104, v105\n"
    "v_add_f32 v108, v108, v110\n"
    "v_add_f32 v109, v109, v111\n"
    "v_cvt_pk_bf16_f32 v105, v108, v109\n"
    "ds_write_b64 v114, v[104:105] offset:12288\n"
    "v_pk_add_f32 v[124:125], v[84:85], v[86:87]\n"
    "v_rcp_f32 v124, v124\n"
    "v_rcp_f32 v125, v125\n"
    "v_mov_b64 v[0:1], 0\n"
    "v_mov_b64 v[2:3], 0\n"
    "v_mov_b64 v[4:5], 0\n"
    "v_mov_b64 v[6:7], 0\n"
    "v_mov_b64 v[8:9], 0\n"
    "v_mov_b64 v[10:11], 0\n"
    "v_mov_b64 v[12:13], 0\n"
    "v_mov_b64 v[14:15], 0\n"
    "v_mfma_f32_16x16x32_bf16 v[96:99], v[100:103], v[80:83], 0\n"
    "s_waitcnt lgkmcnt(0)\n"
    "s_barrier\n"
    "ds_read_b64_tr_b16 v[32:33], v112 offset:12288\n"
    "v_mfma_f32_16x16x32_bf16 v[96:99], v[100:103], v[80:83], 0\n"
    "ds_read_b64_tr_b16 v[34:35], v113 offset:12288\n"
    "ds_read_b64_tr_b16 v[36:37], v112 offset:13312\n"
    "v_mfma_f32_16x16x32_bf16 v[96:99], v[100:103], v[80:83], 0\n"
    "ds_read_b64_tr_b16 v[38:39], v113 offset:13312\n"
    "v_mfma_f32_16x16x32_bf16 v[96:99], v[100:103], v[80:83], 0\n"
    "s_waitcnt lgkmcnt(2)\n"
    "v_smfmac_f32_16x16x64_bf16 v[0:3], v[32:35], a[0:7], v67\n"
    "v_smfmac_f32_16x16x64_bf16 v[4:7], v[32:35], a[64:71], v67\n"
    "s_waitcnt lgkmcnt(0)\n"
    "v_smfmac_f32_16x16x64_bf16 v[0:3], v[36:39], a[8:15], v67\n"
    "v_smfmac_f32_16x16x64_bf16 v[4:7], v[36:39], a[72:79], v67\n"
    "v_smfmac_f32_16x16x64_bf16 v[8:11], v[32:35], a[16:23], v67\n"
    "v_add_f32 v0, v0, v2\n"
    "v_smfmac_f32_16x16x64_bf16 v[12:15], v[32:35], a[80:87], v67\n"
    "v_add_f32 v1, v1, v3\n"
    "v_smfmac_f32_16x16x64_bf16 v[8:11], v[36:39], a[32:39], v67\n"
    "v_add_f32 v4, v4, v6\n"
    "v_smfmac_f32_16x16x64_bf16 v[12:15], v[36:39], a[96:103], v67\n"
    "v_add_f32 v5, v5, v7\n"
    "v_mul_f32 v0, v0, v124\n"
    "v_mul_f32 v1, v1, v125\n"
    "v_cvt_pk_bf16_f32 v88, v0, v1\n"
    "v_pk_mul_f32 v[4:5], v[4:5], v[124:125]\n"
    "v_pk_add_f32 v[8:9], v[8:9], v[10:11]\n"
    "v_pk_add_f32 v[12:13], v[12:13], v[14:15]\n"
    "v_cvt_pk_bf16_f32 v89, v4, v5\n"
    "v_pk_mul_f32 v[8:9], v[8:9], v[124:125]\n"
    "v_pk_mul_f32 v[12:13], v[12:13], v[124:125]\n"
    "v_cvt_pk_bf16_f32 v90, v8, v9\n"
    "v_cvt_pk_bf16_f32 v91, v12, v13\n"
    "global_store_dwordx4 v117, v[88:91], s[44:45] sc1\n"
    "s_add_u32 s43, s43, 1\n"
    "s_sleep 3\n"
    "ds_read_b128 a[0:3], v116 offset:16384\n"
    "ds_read_b128 a[4:7], v116 offset:17408\n"
    "ds_read_b128 a[64:67], v116 offset:18432\n"
    "ds_read_b128 a[68:71], v116 offset:19456\n"
    "ds_read_b128 a[8:11], v116 offset:20480\n"
    "ds_read_b128 a[12:15], v116 offset:21504\n"
    "ds_read_b128 a[72:75], v116 offset:22528\n"
    "ds_read_b128 a[76:79], v116 offset:23552\n"
    "ds_read_b128 a[16:19], v116 offset:24576\n"
    "ds_read_b128 a[20:23], v116 offset:25600\n"
    "ds_read_b128 a[80:83], v116 offset:26624\n"
    "ds_read_b128 a[84:87], v116 offset:27648\n"
    "ds_read_b128 a[32:35], v116 offset:28672\n"
    "s_waitcnt lgkmcnt(8)\n"
    "ds_read_b128 a[36:39], v116 offset:29696\n"
    "ds_read_b128 a[96:99], v116 offset:30720\n"
    "ds_read_b128 a[100:103], v116 offset:31744\n"
    "s_and_b32 s78, s43, 3\n"
    "s_lshl_b32 s78, s78, 17\n"
    "s_add_u32 s54, s48, s78\n"
    "s_addc_u32 s55, s49, 0\n"
    "s_add_u32 s62, s62, 0x20000\n"
    "s_addc_u32 s63, s63, 0\n"
    "v_mov_b64 v[32:33], 0\n"
    "v_mov_b64 v[34:35], 0\n"
    "v_mov_b64 v[36:37], 0\n"
    "v_mov_b64 v[38:39], 0\n"
    "v_mov_b64 v[48:49], 0\n"
    "v_mov_b64 v[50:51], 0\n"
    "v_mov_b64 v[52:53], 0\n"
    "v_mov_b64 v[54:55], 0\n"
    "v_mov_b64 v[104:105], 0\n"
    "v_mov_b64 v[106:107], 0\n"
    "v_mov_b64 v[108:109], 0\n"
    "v_mov_b64 v[110:111], 0\n"
    "s_sub_u32 s78, s43, 1\n"
    "s_lshr_b32 s78, s78, 2\n"
    "s_lshr_b32 s79, s43, 2\n"
    "s_lshr_b32 s78, s93, s78\n"
    "s_lshr_b32 s79, s93, s79\n"
    "s_xor_b32 s78, s78, s79\n"
    "s_and_b32 s78, s78, 1\n"
    "s_lshl_b32 s78, s78, 13\n"
    "v_xor_b32 v117, s78, v117\n"
    "s_add_u32 s44, s44, 0x80000\n"
    "s_addc_u32 s45, s45, 0\n"
    "s_add_u32 s58, s58, 0x40000\n"
    "s_addc_u32 s59, s59, 0\n"
    "s_add_u32 s60, s60, 0x40000\n"
    "s_addc_u32 s61, s61, 0\n"
    "v_or_b32 v84, 1, s96\n"
    "v_mov_b32 v85, s97\n"
    "v_mov_b32 v86, s43\n"
    "s_waitcnt vmcnt(1)\n"
    "v_mov_b64 v[64:65], v[128:129]\n"
    "v_mov_b64 v[68:69], v[130:131]\n"
    "v_mov_b64 v[72:73], v[132:133]\n"
    "v_mov_b64 v[76:77], v[134:135]\n"
    "ds_read_b128 v[128:131], v116 offset:32768\n"
    "ds_read_b128 v[132:135], v116 offset:33792\n"
    "s_waitcnt lgkmcnt(0)\n"
    "s_barrier\n"
    "global_load_dwordx4 v[0:3], v71, s[54:55] offset:0 sc1\n"
    "s_nop 3\n"
    "global_load_dwordx4 v[4:7], v71, s[54:55] offset:1024 sc1\n"
    "s_nop 15\n"
    "s_cmp_lg_u32 s42, 0\n"
    "s_cbranch_scc1 L_vmb0%=\n"
    "s_mov_b32 exec_hi, 0\n"
    "global_store_dwordx4 v71, v[40:43], s[62:63] offset:0 sc1\n"
    "s_mov_b64 exec, -1\n"
    "s_branch L_kvb0%=\n"
    "L_vmb0%=:\n"
    "v_mov_b32 v126, v66\n"
    "v_mov_b32 v127, v74\n"
    "global_store_dwordx2 v79, v[126:127], s[62:63] offset:0 sc1\n"
    "L_kvb0%=:\n"
    "s_mov_b64 exec, 1\n"
    "global_store_dwordx2 v71, v[84:85], s[64:65] sc1\n"
    "s_mov_b64 exec, -1\n"
    "s_add_u32 s64, s64, 8192\n"
    "s_addc_u32 s65, s65, 0\n"
    "s_nop 1\n"
    "v_mov_b64 v[40:41], 0\n"
    "v_mov_b64 v[42:43], 0\n"
    "v_mov_b64 v[44:45], 0\n"
    "v_mov_b64 v[46:47], 0\n"
    "s_branch L_top0%=\n"
    "L_slow0%=:\n"
    "v_cmp_eq_u32 s[78:79], s50, v0\n"
    "v_cmp_eq_u32 s[80:81], s50, v1\n"
    "v_cmp_eq_u32 s[82:83], s50, v2\n"
    "v_cmp_eq_u32 s[84:85], s50, v3\n"
    "v_cmp_eq_u32 s[86:87], s50, v4\n"
    "v_cmp_eq_u32 s[88:89], s50, v5\n"
    "v_cmp_eq_u32 s[52:53], s50, v6\n"
    "v_cmp_eq_u32 vcc, s50, v7\n"
    "global_load_dwordx4 v[0:3], v71, s[54:55] offset:0 sc1\n"
    "global_load_dwordx4 v[4:7], v71, s[54:55] offset:1024 sc1\n"
    "s_waitcnt lgkmcnt(0)\n"
    "s_barrier\n"
    "s_or_b64 vcc, vcc, s[78:79]\n"
    "s_or_b64 vcc, vcc, s[80:81]\n"
    "s_or_b64 vcc, vcc, s[82:83]\n"
    "s_or_b64 vcc, vcc, s[84:85]\n"
    "s_or_b64 vcc, vcc, s[86:87]\n"
    "s_or_b64 vcc, vcc, s[88:89]\n"
    "s_or_b64 vcc, vcc, s[52:53]\n"
    "s_cbranch_scc1 L_redo0%=\n"
    "v_cmp_eq_u32 s[78:79], s50, v8\n"
    "v_cmp_eq_u32 s[80:81], s50, v9\n"
    "v_cmp_eq_u32 s[82:83], s50, v10\n"
    "v_cmp_eq_u32 s[84:85], s50, v11\n"
    "v_cmp_eq_u32 s[86:87], s50, v16\n"
    "v_cmp_eq_u32 s[88:89], s50, v17\n"
    "v_cmp_eq_u32 s[52:53], s50, v18\n"
    "v_cmp_eq_u32 vcc, s50, v19\n"
    "s_nop 0\n"
    "s_or_b64 vcc, vcc, s[78:79]\n"
    "s_or_b64 vcc, vcc, s[80:81]\n"
    "s_or_b64 vcc, vcc, s[82:83]\n"
    "s_or_b64 vcc, vcc, s[84:85]\n"
    "s_or_b64 vcc, vcc, s[86:87]\n"
    "s_or_b64 vcc, vcc, s[88:89]\n"
    "s_or_b64 vcc, vcc, s[52:53]\n"
    "s_cbranch_scc1 L_redo0%=\n"
    "v_cmp_eq_u32 s[78:79], s50, v24\n"
    "v_cmp_eq_u32 s[80:81], s50, v25\n"
    "v_cmp_eq_u32 s[82:83], s50, v26\n"
    "v_cmp_eq_u32 s[84:85], s50, v27\n"
    "v_cmp_eq_u32 s[86:87], s50, v12\n"
    "v_cmp_eq_u32 s[88:89], s50, v13\n"
    "v_cmp_eq_u32 s[52:53], s50, v14\n"
    "v_cmp_eq_u32 vcc, s50, v15\n"
    "s_nop 0\n"
    "s_or_b64 vcc, vcc, s[78:79]\n"
    "s_or_b64 vcc, vcc, s[80:81]\n"
    "s_or_b64 vcc, vcc, s[82:83]\n"
    "s_or_b64 vcc, vcc, s[84:85]\n"
    "s_or_b64 vcc, vcc, s[86:87]\n"
    "s_or_b64 vcc, vcc, s[88:89]\n"
    "s_or_b64 vcc, vcc, s[52:53]\n"
    "s_cbranch_scc1 L_redo0%=\n"
    "v_cmp_eq_u32 s[78:79], s50, v20\n"
    "v_cmp_eq_u32 s[80:81], s50, v21\n"
    "v_cmp_eq_u32 s[82:83], s50, v22\n"
    "v_cmp_eq_u32 s[84:85], s50, v23\n"
    "v_cmp_eq_u32 s[86:87], s50, v28\n"
    "v_cmp_eq_u32 s[88:89], s50, v29\n"
    "v_cmp_eq_u32 s[52:53], s50, v30\n"
    "v_cmp_eq_u32 vcc, s50, v31\n"
    "s_nop 0\n"
    "s_or_b64 vcc, vcc, s[78:79]\n"
    "s_or_b64 vcc, vcc, s[80:81]\n"
    "s_or_b64 vcc, vcc, s[82:83]\n"
    "s_or_b64 vcc, vcc, s[84:85]\n"
    "s_or_b64 vcc, vcc, s[86:87]\n"
    "s_or_b64 vcc, vcc, s[88:89]\n"
    "s_or_b64 vcc, vcc, s[52:53]\n"
    "s_cbranch_scc1 L_redo0%=\n"
    "s_waitcnt vmcnt(0)\n"
    "s_branch L_go0%=\n"
    "L_redo0%=:\n"
    "ds_read_b128 a[0:3], v116 offset:16384\n"
    "ds_read_b128 a[4:7], v116 offset:17408\n"
    "ds_read_b128 a[64:67], v116 offset:18432\n"
    "ds_read_b128 a[68:71], v116 offset:19456\n"
    "ds_read_b128 a[8:11], v116 offset:20480\n"
    "ds_read_b128 a[12:15], v116 offset:21504\n"
    "ds_read_b128 a[72:75], v116 offset:22528\n"
    "ds_read_b128 a[76:79], v116 offset:23552\n"
    "ds_read_b128 a[16:19], v116 offset:24576\n"
    "ds_read_b128 a[20:23], v116 offset:25600\n"
    "ds_read_b128 a[80:83], v116 offset:26624\n"
    "ds_read_b128 a[84:87], v116 offset:27648\n"
    "ds_read_b128 a[32:35], v116 offset:28672\n"
    "ds_read_b128 a[36:39], v116 offset:29696\n"
    "s_waitcnt lgkmcnt(8)\n"
    "ds_read_b128 a[96:99], v116 offset:30720\n"
    "ds_read_b128 a[100:103], v116 offset:31744\n"
    "v_mov_b64 v[32:33], 0\n"
    "v_mov_b64 v[34:35], 0\n"
    "v_mov_b64 v[36:37], 0\n"
    "v_mov_b64 v[38:39], 0\n"
    "v_mov_b64 v[40:41], 0\n"
    "v_mov_b64 v[42:43], 0\n"
    "v_mov_b64 v[44:45], 0\n"
    "v_mov_b64 v[46:47], 0\n"
    "v_mov_b64 v[48:49], 0\n"
    "v_mov_b64 v[50:51], 0\n"
    "v_mov_b64 v[52:53], 0\n"
    "v_mov_b64 v[54:55], 0\n"
    "s_add_u32 s90, s90, 1\n"
    "s_and_b32 s78, s90, 0xff\n"
    "s_cmp_lg_u32 s78, 0\n"
    "s_cbranch_scc1 L_re0%=\n"
    "v_mov_b32 v126, 0\n"
    "global_load_dwordx2 v[126:127], v126, s[68:69] sc1\n"
    "s_waitcnt vmcnt(0)\n"
    "v_cmp_eq_u64 vcc, s[96:97], v[126:127]\n"
    "s_nop 1\n"
    "s_cbranch_vccnz L_bail_g0%=\n"
    "s_cmp_lt_u32 s90, 0x40000\n"
    "s_cbranch_scc1 L_re0%=\n"
    "v_mov_b32 v126, s96\n"
    "v_mov_b32 v127, s97\n"
    "s_mov_b64 exec, 1\n"
    "global_store_dwordx2 v71, v[126:127], s[68:69] sc1\n"
    "s_mov_b64 exec, -1\n"
    "L_bail_g0%=:\n"
    "s_branch L_go0%=\n"
    "L_re0%=:\n"
    "s_waitcnt vmcnt(0) lgkmcnt(0)\n"
    "s_branch L_top0%=\n"
    "L_last0%=:\n"
    "v_smfmac_f32_16x16x64_bf16 v[52:55], v[12:15], a[216:223], v67\n"
    "v_smfmac_f32_16x16x64_bf16 v[48:51], v[20:23], a[168:175], v67\n"
    "v_smfmac_f32_16x16x64_bf16 v[52:55], v[20:23], a[232:239], v67\n"
    "v_smfmac_f32_16x16x64_bf16 v[48:51], v[28:31], a[184:191], v67\n"
    "v_smfmac_f32_16x16x64_bf16 v[52:55], v[28:31], a[248:255], v67\n"
    "s_nop 3\n"
    "v_pk_add_f32 v[48:49], v[48:49], v[50:51]\n"
    "s_nop 2\n"
    "v_pk_add_f32 v[52:53], v[52:53], v[54:55]\n"
    "v_cvt_pk_bf16_f32 v66, v48, v49\n"
    "v_cvt_pk_bf16_f32 v74, v52, v53\n"
    "s_add_u32 s62, s62, 0x20000\n"
    "s_addc_u32 s63, s63, 0\n"
    "s_cmp_lg_u32 s42, 0\n"
    "s_cbranch_scc1 L_vmc0%=\n"
    "s_mov_b32 exec_hi, 0\n"
    "global_store_dwordx4 v71, v[40:43], s[62:63] offset:0 sc1\n"
    "s_mov_b64 exec, -1\n"
    "s_branch L_kvc0%=\n"
    "L_vmc0%=:\n"
    "v_mov_b32 v126, v66\n"
    "v_mov_b32 v127, v74\n"
    "global_store_dwordx2 v79, v[126:127], s[62:63] offset:0 sc1\n"
    "L_kvc0%=:\n"
    "s_waitcnt vmcnt(0) lgkmcnt(0)\n"
    "s_barrier\n"
    "v_or_b32 v86, 1, s96\n"
    "v_mov_b32 v87, s97\n"
    "s_mov_b64 exec, 1\n"
    "global_store_dwordx2 v71, v[86:87], s[64:65] sc1\n"
    "s_add_u32 s64, s64, 8192\n"
    "s_addc_u32 s65, s65, 0\n"
    "global_store_dwordx2 v71, v[86:87], s[64:65] sc1\n"
    "s_waitcnt vmcnt(0)\n"
    "s_mov_b64 exec, -1\n"
    "s_endpgm\n"
    ".p2align 6\n"
    "L_poll1%=:\n"
    "global_load_dwordx4 v[8:11], v71, s[54:55] offset:0 sc1\n"
    "global_load_dwordx4 v[12:15], v71, s[54:55] offset:1024 sc1\n"
    "s_memtime s[16:17]\n"
    "s_waitcnt lgkmcnt(0)\n"
    "s_add_u32 s14, s16, s46\n"
    "s_mov_b64 exec, 15\n"
    "v_mov_b32 v87, s14\n"
    "global_store_dwordx4 v118, v[84:87], s[56:57] sc1\n"
    "s_mov_b64 exec, -1\n"
    "s_waitcnt vmcnt(0)\n"
    "s_branch L_top1%=\n"
    ".p2align 6\n"
    "L_top1%=:\n"
    "s_waitcnt vmcnt(3)\n"
    "v_smfmac_f32_16x16x64_bf16 v[40:43], v[8:11], a[16:23], v67\n"
    "ds_write_b128 v71, v[8:11] offset:2048\n"
    "v_smfmac_f32_16x16x64_bf16 v[44:47], v[8:11], a[80:87], v67\n"
    "v_smfmac_f32_16x16x64_bf16 v[32:35], v[8:11], v[160:167], v67\n"
    "v_smfmac_f32_16x16x64_bf16 v[36:39], v[8:11], v[168:175], v67\n"
    "s_waitcnt vmcnt(2)\n"
    "v_smfmac_f32_16x16x64_bf16 v[40:43], v[12:15], a[24:31], v67\n"
    "ds_write_b128 v71, v[12:15] offset:3072\n"
    "s_waitcnt lgkmcnt(1)\n"
    "s_barrier\n"
    "ds_read_b128 v[0:3], v71 offset:0\n"
    "v_smfmac_f32_16x16x64_bf16 v[44:47], v[12:15], a[88:95], v67\n"
    "ds_read_b128 v[24:27], v71 offset:6144\n"
    "v_smfmac_f32_16x16x64_bf16 v[32:35], v[12:15], v[176:183], v67\n"
    "ds_read_b128 v[16:19], v71 offset:4096\n"
    "v_smfmac_f32_16x16x64_bf16 v[36:39], v[12:15], v[184:191], v67\n"
    "s_waitcnt lgkmcnt(3)\n"
    "s_barrier\n"
    "v_smfmac_f32_16x16x64_bf16 v[48:51], v[8:11], a[144:151], v67\n"
    "s_waitcnt lgkmcnt(2)\n"
    "ds_read_b128 v[4:7], v71 offset:1024\n"
    "v_smfmac_f32_16x16x64_bf16 v[32:35], v[0:3], v[128:135], v67\n"
    "ds_read_b128 v[28:31], v71 offset:7168\n"
    "v_smfmac_f32_16x16x64_bf16 v[36:39], v[0:3], v[136:143], v67\n"
    "s_waitcnt lgkmcnt(2)\n"
    "ds_read_b128 v[20:23], v71 offset:5120\n"
    "v_smfmac_f32_16x16x64_bf16 v[32:35], v[24:27], v[224:231], v67\n"
    "s_memtime s[6:7]\n"
    "ds_read_b128 a[16:19], v116 offset:0\n"
    "v_smfmac_f32_16x16x64_bf16 v[36:39], v[24:27], v[232:239], v67\n"
    "ds_read_b128 a[20:23], v116 offset:1024\n"
    "v_smfmac_f32_16x16x64_bf16 v[32:35], v[16:19], v[192:199], v67\n"
    "ds_read_b128 a[80:83], v116 offset:4096\n"
    "v_smfmac_f32_16x16x64_bf16 v[36:39], v[16:19], v[200:207], v67\n"
    "s_waitcnt lgkmcnt(5)\n"
    "ds_read_b128 a[84:87], v116 offset:5120\n"
    "v_smfmac_f32_16x16x64_bf16 v[32:35], v[4:7], v[144:151], v67\n"
    "s_sub_i32 s2, s6, s16\n"
    "ds_read_b128 a[24:27], v116 offset:2048\n"
    "v_smfmac_f32_16x16x64_bf16 v[36:39], v[4:7], v[152:159], v67\n"
    "s_waitcnt lgkmcnt(5)\n"
    "ds_read_b128 a[28:31], v116 offset:3072\n"
    "v_smfmac_f32_16x16x64_bf16 v[32:35], v[28:31], v[240:247], v67\n"
    "s_cmp_gt_i32 s2, s3\n"
    "ds_read_b128 a[88:91], v116 offset:6144\n"
    "v_smfmac_f32_16x16x64_bf16 v[36:39], v[28:31], v[248:255], v67\n"
    "ds_read_b128 a[92:95], v116 offset:7168\n"
    "v_smfmac_f32_16x16x64_bf16 v[32:35], v[20:23], v[208:215], v67\n"
    "s_cbranch_scc1 L_fix%=\n"
    "L_fixed%=:\n"
    "v_smfmac_f32_16x16x64_bf16 v[36:39], v[20:23], v[216:223], v67\n"
    "v_smfmac_f32_16x16x64_bf16 v[40:43], v[0:3], a[0:7], v67\n"
    "v_add_f32 v32, v32, v34\n"
    "v_add_f32 v33, v33, v35\n"
    "v_smfmac_f32_16x16x64_bf16 v[44:47], v[0:3], a[64:71], v67\n"
    "ds_read_b128 a[0:3], v116 offset:8192\n"
    "v_cvt_pk_bf16_f32 v126, v32, v33\n"
    "v_add_f32 v36, v36, v38\n"
    "v_add_f32 v37, v37, v39\n"
    "v_cvt_pk_bf16_f32 v127, v36, v37\n"
    "v_smfmac_f32_16x16x64_bf16 v[40:43], v[24:27], a[48:55], v67\n"
    "ds_write_b64 v114, v[126:127] offset:8704\n"
    "v_smfmac_f32_16x16x64_bf16 v[44:47], v[24:27], a[112:119], v67\n"
    "ds_read_b128 a[4:7], v116 offset:9216\n"
    "v_cmp_u_f32 vcc, v32, v33\n"
    "v_smfmac_f32_16x16x64_bf16 v[40:43], v[16:19], a[32:39], v67\n"
    "ds_read_b128 a[64:67], v116 offset:12288\n"
    "v_smfmac_f32_16x16x64_bf16 v[44:47], v[16:19], a[96:103], v67\n"
    "s_cbranch_vccnz L_slow1%=\n"
    "L_go1%=:\n"
    "v_smfmac_f32_16x16x64_bf16 v[40:43], v[4:7], a[8:15], v67\n"
    "s_waitcnt lgkmcnt(2)\n"
    "s_barrier\n"
    "ds_read_b64_tr_b16 v[32:33], v112 offset:8192\n"
    "v_smfmac_f32_16x16x64_bf16 v[44:47], v[4:7], a[72:79], v67\n"
    "ds_read_b64_tr_b16 v[34:35], v113 offset:8192\n"
    "ds_read_b64_tr_b16 v[36:37], v112 offset:9216\n"
    "v_smfmac_f32_16x16x64_bf16 v[40:43], v[28:31], a[56:63], v67\n"
    "ds_read_b64_tr_b16 v[38:39], v113 offset:9216\n"
    "v_smfmac_f32_16x16x64_bf16 v[44:47], v[28:31], a[120:127], v67\n"
    "global_load_dwordx4 v[164:167], v71, s[60:61] offset:1024 sc1\n"
    "v_smfmac_f32_16x16x64_bf16 v[40:43], v[20:23], a[40:47], v67\n"
    "ds_read_b128 a[68:71], v116 offset:13312\n"
    "v_smfmac_f32_16x16x64_bf16 v[44:47], v[20:23], a[104:111], v67\n"
    "s_waitcnt lgkmcnt(1)\n"
    "ds_read_b128 a[48:51], v116 offset:10240\n"
    "v_mfma_f32_16x16x32_bf16 v[88:91], v[56:59], v[32:35], 0\n"
    "v_add_f32 v40, v40, v42\n"
    "v_add_f32 v41, v41, v43\n"
    "v_mfma_f32_16x16x32_bf16 v[88:91], v[60:63], v[36:39], v[88:91]\n"
    "v_cvt_pk_bf16_f32 v126, v40, v41\n"
    "v_add_f32 v44, v44, v46\n"
    "v_add_f32 v45, v45, v47\n"
    "v_cvt_pk_bf16_f32 v127, v44, v45\n"
    "v_smfmac_f32_16x16x64_bf16 v[52:55], v[8:11], a[208:215], v67\n"
    "ds_write_b64 v115, v[126:127] offset:10752\n"
    "v_add_f32_dpp v88, v90, v88 quad_perm:[2,3,2,3] row_mask:0xf bank_mask:0xf\n"
    "v_add_f32_dpp v89, v91, v89 quad_perm:[2,3,2,3] row_mask:0xf bank_mask:0xf\n"
    "v_smfmac_f32_16x16x64_bf16 v[48:51], v[12:15], a[152:159], v67\n"
    "ds_write_b64 v75, v[88:89] offset:14368\n"
    "v_smfmac_f32_16x16x64_bf16 v[52:55], v[12:15], a[216:223], v67\n"
    "global_load_dwordx4 v[56:59], v71, s[58:59] offset:-2048 sc1\n"
    "v_smfmac_f32_16x16x64_bf16 v[48:51], v[0:3], a[128:135], v67\n"
    "global_load_dwordx4 v[60:63], v71, s[58:59] offset:2048 sc1\n"
    "v_smfmac_f32_16x16x64_bf16 v[52:55], v[0:3], a[192:199], v67\n"
    "global_load_dwordx4 v[160:163], v71, s[60:61] offset:0 sc1\n"
    "v_smfmac_f32_16x16x64_bf16 v[48:51], v[24:27], a[176:183], v67\n"
    "s_waitcnt lgkmcnt(0)\n"
    "s_barrier\n"
    "ds_read_b64_tr_b16 v[40:41], v112 offset:10240\n"
    "v_smfmac_f32_16x16x64_bf16 v[52:55], v[24:27], a[240:247], v67\n"
    "ds_read_b64_tr_b16 v[42:43], v113 offset:10240\n"
    "ds_read_b64_tr_b16 v[44:45], v112 offset:11264\n"
    "v_smfmac_f32_16x16x64_bf16 v[48:51], v[16:19], a[160:167], v67\n"
    "ds_read_b64_tr_b16 v[46:47], v113 offset:11264\n"
    "v_smfmac_f32_16x16x64_bf16 v[52:55], v[16:19], a[224:231], v67\n"
    "ds_read_b128 v[88:91], v71 offset:14336\n"
    "s_cmp_eq_u32 s43, 63\n"
    "v_smfmac_f32_16x16x64_bf16 v[48:51], v[4:7], a[136:143], v67\n"
    "s_waitcnt lgkmcnt(3)\n"
    "ds_read_b128 a[52:55], v116 offset:11264\n"
    "v_mfma_f32_16x16x32_bf16 v[96:99], v[40:43], v[32:35], 0\n"
    "s_waitcnt lgkmcnt(2)\n"
    "ds_read_b128 a[112:115], v116 offset:14336\n"
    "v_mfma_f32_16x16x32_bf16 v[96:99], v[44:47], v[36:39], v[96:99]\n"
    "s_cbranch_scc1 L_last1%=\n"
    "v_smfmac_f32_16x16x64_bf16 v[52:55], v[4:7], a[200:207], v67\n"
    "s_waitcnt lgkmcnt(2)\n"
    "ds_read_b128 a[116:119], v116 offset:15360\n"
    "v_max3_f32 v122, v88, v89, v90\n"
    "v_max_f32 v122, v122, v91\n"
    "v_add_f32_dpp v96, v98, v96 quad_perm:[2,3,2,3] row_mask:0xf bank_mask:0xf\n"
    "v_add_f32_dpp v97, v99, v97 quad_perm:[2,3,2,3] row_mask:0xf bank_mask:0xf\n"
    "v_max_f32_dpp v123, v122, v122 quad_perm:[2,3,0,1] row_mask:0xf bank_mask:0xf\n"
    "v_max3_f32 v122, v123, v96, v97\n"
    "v_smfmac_f32_16x16x64_bf16 v[48:51], v[28:31], a[184:191], v67\n"
    "v_max_f32_dpp v121, v122, v123 quad_perm:[0,1,0,1] row_mask:0xf bank_mask:0xf\n"
    "v_max_f32_dpp v123, v122, v123 quad_perm:[0,1,0,1] row_mask:0xf bank_mask:0xf\n"
    "v_smfmac_f32_16x16x64_bf16 v[52:55], v[28:31], a[248:255], v67\n"
    "v_permlane16_swap_b32 v123, v121\n"
    "v_max_f32 v122, v123, v121\n"
    "v_max_f32 v123, v123, v121\n"
    "v_smfmac_f32_16x16x64_bf16 v[48:51], v[20:23], a[168:175], v67\n"
    "v_smfmac_f32_16x16x64_bf16 v[52:55], v[20:23], a[232:239], v67\n"
    "v_permlane32_swap_b32 v122, v123\n"
    "v_max_f32 v122, v122, v123\n"
    "v_mul_f32 v122, s76, v122\n"
    "v_pk_fma_f32 v[96:97], v[96:97], s[76:77], v[122:123] op_sel_hi:[1,1,0] neg_lo:[0,0,1] neg_hi:[0,0,1]\n"
    "v_pk_fma_f32 v[88:89], v[88:89], s[76:77], v[122:123] op_sel_hi:[1,1,0] neg_lo:[0,0,1] neg_hi:[0,0,1]\n"
    "v_pk_fma_f32 v[90:91], v[90:91], s[76:77], v[122:123] op_sel_hi:[1,1,0] neg_lo:[0,0,1] neg_hi:[0,0,1]\n"
    "v_exp_f32 v96, v96\n"
    "v_exp_f32 v97, v97\n"
    "v_exp_f32 v88, v88\n"
    "v_exp_f32 v89, v89\n"
    "v_exp_f32 v90, v90\n"
    "v_exp_f32 v91, v91\n"
    "v_pk_add_f32 v[48:49], v[48:49], v[50:51]\n"
    "v_pk_add_f32 v[52:53], v[52:53], v[54:55]\n"
    "v_cvt_pk_bf16_f32 v126, v96, v97\n"
    "v_cvt_pk_bf16_f32 v100, v88, v89\n"
    "v_cvt_pk_bf16_f32 v102, v90, v91\n"
    "v_cndmask_b32 v101, 0, v126, s[72:73]\n"
    "v_cndmask_b32 v103, 0, v126, s[74:75]\n"
    "v_cvt_pk_bf16_f32 v66, v48, v49\n"
    "v_cvt_pk_bf16_f32 v70, v48, v49\n"
    "v_cvt_pk_bf16_f32 v74, v52, v53\n"
    "v_cvt_pk_bf16_f32 v78, v52, v53\n"
    "v_smfmac_f32_16x16x64_bf16 v[104:107], v[100:103], v[64:71], v67\n"
    "v_smfmac_f32_16x16x64_bf16 v[108:111], v[100:103], v[72:79], v67\n"
    "v_mfma_f32_16x16x32_bf16 v[84:87], v[100:103], v[80:83], 0\n"
    "v_add_f32 v104, v104, v106\n"
    "v_add_f32 v105, v105, v107\n"
    "v_cvt_pk_bf16_f32 v104, v104, v105\n"
    "v_add_f32 v108, v108, v110\n"
    "v_add_f32 v109, v109, v111\n"
    "v_cvt_pk_bf16_f32 v105, v108, v109\n"
    "ds_write_b64 v114, v[104:105] offset:12800\n"
    "global_load_dword v120, v119, s[12:13] offset:12 sc1\n"
    "v_pk_add_f32 v[124:125], v[84:85], v[86:87]\n"
    "v_rcp_f32 v124, v124\n"
    "v_rcp_f32 v125, v125\n"
    "v_mov_b64 v[0:1], 0\n"
    "v_mov_b64 v[2:3], 0\n"
    "v_mov_b64 v[4:5], 0\n"
    "v_mov_b64 v[6:7], 0\n"
    "v_mov_b64 v[8:9], 0\n"
    "v_mov_b64 v[10:11], 0\n"
    "v_mov_b64 v[12:13], 0\n"
    "v_mov_b64 v[14:15], 0\n"
    "v_mfma_f32_16x16x32_bf16 v[96:99], v[100:103], v[80:83], 0\n"
    "s_waitcnt lgkmcnt(0)\n"
    "s_barrier\n"
    "ds_read_b64_tr_b16 v[32:33], v112 offset:12288\n"
    "v_mfma_f32_16x16x32_bf16 v[96:99], v[100:103], v[80:83], 0\n"
    "ds_read_b64_tr_b16 v[34:35], v113 offset:12288\n"
    "ds_read_b64_tr_b16 v[36:37], v112 offset:13312\n"
    "v_mfma_f32_16x16x32_bf16 v[96:99], v[100:103], v[80:83], 0\n"
    "ds_read_b64_tr_b16 v[38:39], v113 offset:13312\n"
    "v_mfma_f32_16x16x32_bf16 v[96:99], v[100:103], v[80:83], 0\n"
    "s_waitcnt lgkmcnt(2)\n"
    "v_smfmac_f32_16x16x64_bf16 v[0:3], v[32:35], a[16:23], v67\n"
    "v_smfmac_f32_16x16x64_bf16 v[4:7], v[32:35], a[80:87], v67\n"
    "s_waitcnt lgkmcnt(0)\n"
    "v_smfmac_f32_16x16x64_bf16 v[0:3], v[36:39], a[24:31], v67\n"
    "v_smfmac_f32_16x16x64_bf16 v[4:7], v[36:39], a[88:95], v67\n"
    "v_smfmac_f32_16x16x64_bf16 v[8:11], v[32:35], a[0:7], v67\n"
    "v_add_f32 v0, v0, v2\n"
    "v_smfmac_f32_16x16x64_bf16 v[12:15], v[32:35], a[64:71], v67\n"
    "v_add_f32 v1, v1, v3\n"
    "v_smfmac_f32_16x16x64_bf16 v[8:11], v[36:39], a[48:55], v67\n"
    "v_add_f32 v4, v4, v6\n"
    "v_smfmac_f32_16x16x64_bf16 v[12:15], v[36:39], a[112:119], v67\n"
    "v_add_f32 v5, v5, v7\n"
    "v_mul_f32 v0, v0, v124\n"
    "v_mul_f32 v1, v1, v125\n"
    "v_cvt_pk_bf16_f32 v88, v0, v1\n"
    "v_pk_mul_f32 v[4:5], v[4:5], v[124:125]\n"
    "v_pk_add_f32 v[8:9], v[8:9], v[10:11]\n"
    "v_pk_add_f32 v[12:13], v[12:13], v[14:15]\n"
    "v_cvt_pk_bf16_f32 v89, v4, v5\n"
    "v_pk_mul_f32 v[8:9], v[8:9], v[124:125]\n"
    "v_pk_mul_f32 v[12:13], v[12:13], v[124:125]\n"
    "v_cvt_pk_bf16_f32 v90, v8, v9\n"
    "v_cvt_pk_bf16_f32 v91, v12, v13\n"
    "global_store_dwordx4 v117, v[88:91], s[44:45] sc1\n"
    "s_memtime s[8:9]\n"
    "s_add_u32 s43, s43, 1\n"
    "ds_read_b128 a[16:19], v116 offset:16384\n"
    "ds_read_b128 a[20:23], v116 offset:17408\n"
    "ds_read_b128 a[80:83], v116 offset:18432\n"
    "ds_read_b128 a[84:87], v116 offset:19456\n"
    "ds_read_b128 a[24:27], v116 offset:20480\n"
    "ds_read_b128 a[28:31], v116 offset:21504\n"
    "ds_read_b128 a[88:91], v116 offset:22528\n"
    "ds_read_b128 a[92:95], v116 offset:23552\n"
    "ds_read_b128 a[0:3], v116 offset:24576\n"
    "ds_read_b128 a[4:7], v116 offset:25600\n"
    "ds_read_b128 a[64:67], v116 offset:26624\n"
    "ds_read_b128 a[68:71], v116 offset:27648\n"
    "ds_read_b128 a[48:51], v116 offset:28672\n"
    "s_waitcnt lgkmcnt(8)\n"
    "ds_read_b128 a[52:55], v116 offset:29696\n"
    "ds_read_b128 a[112:115], v116 offset:30720\n"
    "ds_read_b128 a[116:119], v116 offset:31744\n"
    "s_and_b32 s78, s43, 3\n"
    "s_lshl_b32 s78, s78, 17\n"
    "s_add_u32 s54, s48, s78\n"
    "s_addc_u32 s55, s49, 0\n"
    "s_add_u32 s62, s62, 0x20000\n"
    "s_addc_u32 s63, s63, 0\n"
    "v_mov_b64 v[32:33], 0\n"
    "v_mov_b64 v[34:35], 0\n"
    "v_mov_b64 v[36:37], 0\n"
    "v_mov_b64 v[38:39], 0\n"
    "v_mov_b64 v[48:49], 0\n"
    "v_mov_b64 v[50:51], 0\n"
    "v_mov_b64 v[52:53], 0\n"
    "v_mov_b64 v[54:55], 0\n"
    "v_mov_b64 v[104:105], 0\n"
    "v_mov_b64 v[106:107], 0\n"
    "v_mov_b64 v[108:109], 0\n"
    "v_mov_b64 v[110:111], 0\n"
    "s_sub_u32 s78, s43, 1\n"
    "s_lshr_b32 s78, s78, 2\n"
    "s_lshr_b32 s79, s43, 2\n"
    "s_lshr_b32 s78, s93, s78\n"
    "s_lshr_b32 s79, s93, s79\n"
    "s_xor_b32 s78, s78, s79\n"
    "s_and_b32 s78, s78, 1\n"
    "s_lshl_b32 s78, s78, 13\n"
    "v_xor_b32 v117, s78, v117\n"
    "s_add_u32 s44, s44, 0x80000\n"
    "s_addc_u32 s45, s45, 0\n"
    "s_add_u32 s58, s58, 0x40000\n"
    "s_addc_u32 s59, s59, 0\n"
    "s_add_u32 s60, s60, 0x40000\n"
    "s_addc_u32 s61, s61, 0\n"
    "v_or_b32 v84, 1, s96\n"
    "v_mov_b32 v85, s97\n"
    "v_mov_b32 v86, s43\n"
    "s_waitcnt vmcnt(1)\n"
    "v_mov_b64 v[64:65], v[160:161]\n"
    "v_mov_b64 v[68:69], v[162:163]\n"
    "v_mov_b64 v[72:73], v[164:165]\n"
    "v_mov_b64 v[76:77], v[166:167]\n"
    "ds_read_b128 v[160:163], v116 offset:32768\n"
    "ds_read_b128 v[164:167], v116 offset:33792\n"
    "s_sub_u32 s19, s8, s16\n"
    "L_lk%=:\n"
    "v_subrev_u32 v120, s14, v120\n"
    "v_cmp_gt_u32 vcc, 0xe00, v120\n"
    "s_nop 1\n"
    "v_cndmask_b32 v120, 0, v120, vcc\n"
    "s_nop 1\n"
    "v_max_u32_dpp v120, v120, v120 row_ror:1 row_mask:0xf bank_mask:0xf\n"
    "s_nop 1\n"
    "v_max_u32_dpp v120, v120, v120 row_ror:2 row_mask:0xf bank_mask:0xf\n"
    "s_nop 1\n"
    "v_max_u32_dpp v120, v120, v120 row_ror:4 row_mask:0xf bank_mask:0xf\n"
    "s_nop 1\n"
    "v_max_u32_dpp v120, v120, v120 row_ror:8 row_mask:0xf bank_mask:0xf\n"
    "s_memtime s[6:7]\n"
    "v_readlane_b32 s78, v120, 0\n"
    "v_readlane_b32 s79, v120, 16\n"
    "v_readlane_b32 s80, v120, 32\n"
    "v_readlane_b32 s81, v120, 48\n"
    "s_add_u32 s9, s14, s95\n"
    "s_max_u32 s78, s78, s79\n"
    "s_max_u32 s80, s80, s81\n"
    "s_max_u32 s78, s78, s80\n"
    "s_add_u32 s9, s9, s78\n"
    "s_min_u32 s3, s78, 100\n"
    "s_add_u32 s3, s3, 30\n"
    "s_sub_u32 s8, s9, s91\n"
    "s_sub_u32 s8, s8, s94\n"
    "s_waitcnt lgkmcnt(0)\n"
    "s_sub_i32 s78, s8, s6\n"
    "s_cmp_gt_i32 s78, 1300\n"
    "s_cbranch_scc1 L_far%=\n"
    "L_near%=:\n"
    "L_wa%=:\n"
    "s_memtime s[6:7]\n"
    "s_waitcnt lgkmcnt(0)\n"
    "s_sub_i32 s78, s8, s6\n"
    "s_sub_u32 s79, s78, 520\n"
    "s_cmp_lt_u32 s79, 0x1400\n"
    "s_cbranch_scc1 L_wa%=\n"
    "s_memtime s[6:7]\n"
    "s_memrealtime s[78:79]\n"
    "s_nop 3\n"
    "s_memrealtime s[80:81]\n"
    "s_nop 3\n"
    "s_memrealtime s[82:83]\n"
    "s_nop 3\n"
    "s_memrealtime s[84:85]\n"
    "s_nop 3\n"
    "s_memrealtime s[86:87]\n"
    "s_nop 3\n"
    "s_memrealtime s[88:89]\n"
    "s_mov_b32 s10, 0\n"
    "s_waitcnt lgkmcnt(0)\n"
    "s_mul_i32 s91, s78, 24\n"
    "s_cmp_eq_u32 s78, s80\n"
    "s_addc_u32 s10, s10, 0\n"
    "s_cmp_eq_u32 s78, s82\n"
    "s_addc_u32 s10, s10, 0\n"
    "s_cmp_eq_u32 s78, s84\n"
    "s_addc_u32 s10, s10, 0\n"
    "s_cmp_eq_u32 s78, s86\n"
    "s_addc_u32 s10, s10, 0\n"
    "s_cmp_eq_u32 s78, s88\n"
    "s_addc_u32 s10, s10, 0\n"
    "s_mul_i32 s10, s10, 20\n"
    "s_sub_u32 s91, s91, s6\n"
    "s_sub_u32 s91, s91, s10\n"
    "s_sub_u32 s8, s9, s91\n"
    "L_wb%=:\n"
    "s_memtime s[6:7]\n"
    "s_waitcnt lgkmcnt(0)\n"
    "s_sub_i32 s78, s8, s6\n"
    "s_sub_u32 s79, s78, 200\n"
    "s_cmp_lt_u32 s79, 0x1400\n"
    "s_cbranch_scc1 L_wb%=\n"
    "s_sub_i32 s78, s78, 84\n"
    "s_max_i32 s78, s78, 0\n"
    "s_min_i32 s78, s78, 124\n"
    "s_andn2_b32 s78, s78, 3\n"
    "s_sub_u32 s80, s4, s78\n"
    "s_subb_u32 s81, s5, 0\n"
    "s_setpc_b64 s[80:81]\n"
    "L_far%=:\n"
    "L_wc%=:\n"
    "s_memtime s[6:7]\n"
    "s_waitcnt lgkmcnt(0)\n"
    "s_sub_i32 s78, s8, s6\n"
    "s_sub_u32 s79, s78, 1300\n"
    "s_cmp_lt_u32 s79, 0x1400\n"
    "s_cbranch_scc1 L_wc%=\n"
    "global_load_dword v120, v119, s[12:13] offset:12 sc1\n"
    "s_cmp_gt_i32 s78, 1300\n"
    "s_waitcnt vmcnt(0)\n"
    "s_cbranch_scc0 L_lk%=\n"
    "s_branch L_near%=\n"
    ".p2align 6\n"
    "s_nop 0\n"
    "s_nop 0\n"
    "s_nop 0\n"
    "s_nop 0\n"
    "s_nop 0\n"
    "s_nop 0\n"
    "s_nop 0\n"
    "s_nop 0\n"
    "s_nop 0\n"
    "s_nop 0\n"
    "s_nop 0\n"
    "s_nop 0\n"
    "s_nop 0\n"
    "s_nop 0\n"
    "s_nop 0\n"
    "s_nop 0\n"
    "s_nop 0\n"
    "s_nop 0\n"
    "s_nop 0\n"
    "s_nop 0\n"
    "s_nop 0\n"
    "s_nop 0\n"
    "s_nop 0\n"
    "s_nop 0\n"
    "s_nop 0\n"
    "s_nop 0\n"
    "s_nop 0\n"
    "s_nop 0\n"
    "s_nop 0\n"
    "s_nop 0\n"
    "s_nop 0\n"
    "s_nop 0\n"
    "L_sled%=:\n"
    "s_barrier\n"
    "global_load_dwordx4 v[8:11], v71, s[54:55] offset:0 sc1\n"
    "s_nop 3\n"
    "global_load_dwordx4 v[12:15], v71, s[54:55] offset:1024 sc1\n"
    "s_memtime s[16:17]\n"
    "s_nop 15\n"
    "s_cmp_lg_u32 s42, 0\n"
    "s_cbranch_scc1 L_vmb1%=\n"
    "s_mov_b32 exec_lo, 0\n"
    "global_store_dwordx4 v71, v[40:43], s[62:63] offset:0 sc1\n"
    "s_mov_b64 exec, -1\n"
    "s_branch L_kvb1%=\n"
    "L_vmb1%=:\n"
    "v_mov_b32 v126, v66\n"
    "v_mov_b32 v127, v74\n"
    "global_store_dwordx2 v79, v[126:127], s[62:63] offset:512 sc1\n"
    "L_kvb1%=:\n"
    "s_nop 1\n"
    "v_mov_b64 v[40:41], 0\n"
    "v_mov_b64 v[42:43], 0\n"
    "v_mov_b64 v[44:45], 0\n"
    "v_mov_b64 v[46:47], 0\n"
    "s_add_u32 s12, s12, 0x10000\n"
    "s_andn2_b32 s12, s12, 0x40000\n"
    "s_add_u32 s56, s56, 0x10000\n"
    "s_andn2_b32 s56, s56, 0x40000\n"
    "s_waitcnt lgkmcnt(0)\n"
    "s_sub_i32 s78, s91, s47\n"
    "s_sub_u32 s79, s16, s18\n"
    "v_cvt_f32_u32 v127, s79\n"
    "v_cvt_f32_i32 v126, s78\n"
    "v_rcp_f32 v127, v127\n"
    "v_mul_f32 v126, 0x450fc000, v126\n"
    "v_mul_f32 v126, v126, v127\n"
    "v_cvt_i32_f32 v126, v126\n"
    "s_nop 0\n"
    "v_readfirstlane_b32 s78, v126\n"
    "s_sub_i32 s19, s19, s51\n"
    "s_max_i32 s19, s19, -64\n"
    "s_min_i32 s19, s19, 64\n"
    "s_ashr_i32 s19, s19, 1\n"
    "s_add_i32 s51, s51, s19\n"
    "s_sub_i32 s2, s2, s15\n"
    "s_max_i32 s2, s2, -64\n"
    "s_min_i32 s2, s2, 64\n"
    "s_ashr_i32 s2, s2, 1\n"
    "s_add_i32 s15, s15, s2\n"
    "s_add_u32 s3, s3, s15\n"
    "s_max_i32 s78, s78, -200\n"
    "s_min_i32 s78, s78, 900\n"
    "s_add_i32 s78, s78, s94\n"
    "s_ashr_i32 s94, s78, 1\n"
    "s_add_u32 s46, s91, s94\n"
    "s_add_u32 s46, s46, s51\n"
    "s_cmp_lt_u32 s43, 29\n"
    "s_cselect_b32 s79, 170, 0\n"
    "s_cselect_b32 s95, 20, 0\n"
    "s_add_u32 s95, s95, 1220\n"
    "s_add_u32 s46, s46, s79\n"
    "s_mov_b32 s47, s91\n"
    "s_mov_b32 s18, s16\n"
    "s_add_u32 s14, s16, s46\n"
    "s_mov_b64 exec, 15\n"
    "v_mov_b32 v87, s14\n"
    "global_store_dwordx4 v118, v[84:87], s[56:57] sc1\n"
    "s_mov_b64 exec, -1\n"
    "s_branch L_top1%=\n"
    "L_fix%=:\n"
    "s_sub_u32 s16, s6, s15\n"
    "s_add_u32 s16, s16, 60\n"
    "s_add_u32 s14, s16, s46\n"
    "s_mov_b64 exec, 15\n"
    "v_mov_b32 v87, s14\n"
    "global_store_dwordx4 v118, v[84:87], s[56:57] sc1\n"
    "s_mov_b64 exec, -1\n"
    "s_branch L_fixed%=\n"
    "L_slow1%=:\n"
    "v_cmp_eq_u32 s[78:79], s50, v8\n"
    "v_cmp_eq_u32 s[80:81], s50, v9\n"
    "v_cmp_eq_u32 s[82:83], s50, v10\n"
    "v_cmp_eq_u32 s[84:85], s50, v11\n"
    "v_cmp_eq_u32 s[86:87], s50, v12\n"
    "v_cmp_eq_u32 s[88:89], s50, v13\n"
    "v_cmp_eq_u32 s[52:53], s50, v14\n"
    "v_cmp_eq_u32 vcc, s50, v15\n"
    "global_load_dwordx4 v[8:11], v71, s[54:55] offset:0 sc1\n"
    "global_load_dwordx4 v[12:15], v71, s[54:55] offset:1024 sc1\n"
    "s_memtime s[16:17]\n"
    "s_waitcnt lgkmcnt(0)\n"
    "s_barrier\n"
    "s_add_u32 s16, s16, 400\n"
    "s_add_u32 s14, s16, s46\n"
    "s_mov_b64 exec, 15\n"
    "v_mov_b32 v87, s14\n"
    "global_store_dwordx4 v118, v[84:87], s[56:57] sc1\n"
    "s_mov_b64 exec, -1\n"
    "s_or_b64 vcc, vcc, s[78:79]\n"
    "s_or_b64 vcc, vcc, s[80:81]\n"
    "s_or_b64 vcc, vcc, s[82:83]\n"
    "s_or_b64 vcc, vcc, s[84:85]\n"
    "s_or_b64 vcc, vcc, s[86:87]\n"
    "s_or_b64 vcc, vcc, s[88:89]\n"
    "s_or_b64 vcc, vcc, s[52:53]\n"
    "s_cbranch_scc1 L_redo1%=\n"
    "v_cmp_eq_u32 s[78:79], s50, v0\n"
    "v_cmp_eq_u32 s[80:81], s50, v1\n"
    "v_cmp_eq_u32 s[82:83], s50, v2\n"
    "v_cmp_eq_u32 s[84:85], s50, v3\n"
    "v_cmp_eq_u32 s[86:87], s50, v24\n"
    "v_cmp_eq_u32 s[88:89], s50, v25\n"
    "v_cmp_eq_u32 s[52:53], s50, v26\n"
    "v_cmp_eq_u32 vcc, s50, v27\n"
    "s_nop 0\n"
    "s_or_b64 vcc, vcc, s[78:79]\n"
    "s_or_b64 vcc, vcc, s[80:81]\n"
    "s_or_b64 vcc, vcc, s[82:83]\n"
    "s_or_b64 vcc, vcc, s[84:85]\n"
    "s_or_b64 vcc, vcc, s[86:87]\n"
    "s_or_b64 vcc, vcc, s[88:89]\n"
    "s_or_b64 vcc, vcc, s[52:53]\n"
    "s_cbranch_scc1 L_redo1%=\n"
    "v_cmp_eq_u32 s[78:79], s50, v16\n"
    "v_cmp_eq_u32 s[80:81], s50, v17\n"
    "v_cmp_eq_u32 s[82:83], s50, v18\n"
    "v_cmp_eq_u32 s[84:85], s50, v19\n"
    "v_cmp_eq_u32 s[86:87], s50, v4\n"
    "v_cmp_eq_u32 s[88:89], s50, v5\n"
    "v_cmp_eq_u32 s[52:53], s50, v6\n"
    "v_cmp_eq_u32 vcc, s50, v7\n"
    "s_nop 0\n"
    "s_or_b64 vcc, vcc, s[78:79]\n"
    "s_or_b64 vcc, vcc, s[80:81]\n"
    "s_or_b64 vcc, vcc, s[82:83]\n"
    "s_or_b64 vcc, vcc, s[84:85]\n"
    "s_or_b64 vcc, vcc, s[86:87]\n"
    "s_or_b64 vcc, vcc, s[88:89]\n"
    "s_or_b64 vcc, vcc, s[52:53]\n"
    "s_cbranch_scc1 L_redo1%=\n"
    "v_cmp_eq_u32 s[78:79], s50, v28\n"
    "v_cmp_eq_u32 s[80:81], s50, v29\n"
    "v_cmp_eq_u32 s[82:83], s50, v30\n"
    "v_cmp_eq_u32 s[84:85], s50, v31\n"
    "v_cmp_eq_u32 s[86:87], s50, v20\n"
    "v_cmp_eq_u32 s[88:89], s50, v21\n"
    "v_cmp_eq_u32 s[52:53], s50, v22\n"
    "v_cmp_eq_u32 vcc, s50, v23\n"
    "s_nop 0\n"
    "s_or_b64 vcc, vcc, s[78:79]\n"
    "s_or_b64 vcc, vcc, s[80:81]\n"
    "s_or_b64 vcc, vcc, s[82:83]\n"
    "s_or_b64 vcc, vcc, s[84:85]\n"
    "s_or_b64 vcc, vcc, s[86:87]\n"
    "s_or_b64 vcc, vcc, s[88:89]\n"
    "s_or_b64 vcc, vcc, s[52:53]\n"
    "s_cbranch_scc1 L_redo1%=\n"
    "s_waitcnt vmcnt(0)\n"
    "s_branch L_go1%=\n"
    "L_redo1%=:\n"
    "ds_read_b128 a[16:19], v116 offset:16384\n"
    "ds_read_b128 a[20:23], v116 offset:17408\n"
    "ds_read_b128 a[80:83], v116 offset:18432\n"
    "ds_read_b128 a[84:87], v116 offset:19456\n"
    "ds_read_b128 a[24:27], v116 offset:20480\n"
    "ds_read_b128 a[28:31], v116 offset:21504\n"
    "ds_read_b128 a[88:91], v116 offset:22528\n"
    "ds_read_b128 a[92:95], v116 offset:23552\n"
    "ds_read_b128 a[0:3], v116 offset:24576\n"
    "ds_read_b128 a[4:7], v116 offset:25600\n"
    "ds_read_b128 a[64:67], v116 offset:26624\n"
    "ds_read_b128 a[68:71], v116 offset:27648\n"
    "ds_read_b128 a[48:51], v116 offset:28672\n"
    "ds_read_b128 a[52:55], v116 offset:29696\n"
    "s_waitcnt lgkmcnt(8)\n"
    "ds_read_b128 a[112:115], v116 offset:30720\n"
    "ds_read_b128 a[116:119], v116 offset:31744\n"
    "v_mov_b64 v[32:33], 0\n"
    "v_mov_b64 v[34:35], 0\n"
    "v_mov_b64 v[36:37], 0\n"
    "v_mov_b64 v[38:39], 0\n"
    "v_mov_b64 v[40:41], 0\n"
    "v_mov_b64 v[42:43], 0\n"
    "v_mov_b64 v[44:45], 0\n"
    "v_mov_b64 v[46:47], 0\n"
    "v_mov_b64 v[48:49], 0\n"
    "v_mov_b64 v[50:51], 0\n"
    "v_mov_b64 v[52:53], 0\n"
    "v_mov_b64 v[54:55], 0\n"
    "s_add_u32 s90, s90, 1\n"
    "s_and_b32 s78, s90, 0xff\n"
    "s_cmp_lg_u32 s78, 0\n"
    "s_cbranch_scc1 L_re1%=\n"
    "v_mov_b32 v126, 0\n"
    "global_load_dwordx2 v[126:127], v126, s[68:69] sc1\n"
    "s_waitcnt vmcnt(0)\n"
    "v_cmp_eq_u64 vcc, s[96:97], v[126:127]\n"
    "s_nop 1\n"
    "s_cbranch_vccnz L_bail_g1%=\n"
    "s_cmp_lt_u32 s90, 0x40000\n"
    "s_cbranch_scc1 L_re1%=\n"
    "v_mov_b32 v126, s96\n"
    "v_mov_b32 v127, s97\n"
    "s_mov_b64 exec, 1\n"
    "global_store_dwordx2 v71, v[126:127], s[68:69] sc1\n"
    "s_mov_b64 exec, -1\n"
    "L_bail_g1%=:\n"
    "s_branch L_go1%=\n"
    "L_re1%=:\n"
    "s_waitcnt vmcnt(1) lgkmcnt(0)\n"
    "s_branch L_top1%=\n"
    "L_last1%=:\n"
    "v_smfmac_f32_16x16x64_bf16 v[52:55], v[4:7], a[200:207], v67\n"
    "v_smfmac_f32_16x16x64_bf16 v[48:51], v[28:31], a[184:191], v67\n"
    "v_smfmac_f32_16x16x64_bf16 v[52:55], v[28:31], a[248:255], v67\n"
    "v_smfmac_f32_16x16x64_bf16 v[48:51], v[20:23], a[168:175], v67\n"
    "v_smfmac_f32_16x16x64_bf16 v[52:55], v[20:23], a[232:239], v67\n"
    "s_nop 3\n"
    "v_pk_add_f32 v[48:49], v[48:49], v[50:51]\n"
    "s_nop 2\n"
    "v_pk_add_f32 v[52:53], v[52:53], v[54:55]\n"
    "v_cvt_pk_bf16_f32 v66, v48, v49\n"
    "v_cvt_pk_bf16_f32 v74, v52, v53\n"
    "s_add_u32 s62, s62, 0x20000\n"
    "s_addc_u32 s63, s63, 0\n"
    "s_cmp_lg_u32 s42, 0\n"
    "s_cbranch_scc1 L_vmc1%=\n"
    "s_mov_b32 exec_lo, 0\n"
    "global_store_dwordx4 v71, v[40:43], s[62:63] offset:0 sc1\n"
    "s_mov_b64 exec, -1\n"
    "s_branch L_kvc1%=\n"
    "L_vmc1%=:\n"
    "v_mov_b32 v126, v66\n"
    "v_mov_b32 v127, v74\n"
    "global_store_dwordx2 v79, v[126:127], s[62:63] offset:512 sc1\n"
    "L_kvc1%=:\n"
    "s_waitcnt vmcnt(0) lgkmcnt(0)\n"
    "s_barrier\n"
    "s_endpgm\n"
    ".p2align 6\n"
    "L_poll2%=:\n"
    "global_load_dwordx4 v[16:19], v71, s[54:55] offset:0 sc1\n"
    "global_load_dwordx4 v[20:23], v71, s[54:55] offset:1024 sc1\n"
    "s_waitcnt vmcnt(0)\n"
    "s_branch L_top2%=\n"
    ".p2align 6\n"
    "L_top2%=:\n"
    "s_waitcnt vmcnt(2)\n"
    "v_smfmac_f32_16x16x64_bf16 v[40:43], v[16:19], a[32:39], v67\n"
    "ds_write_b128 v71, v[16:19] offset:4096\n"
    "v_smfmac_f32_16x16x64_bf16 v[44:47], v[16:19], a[96:103], v67\n"
    "v_smfmac_f32_16x16x64_bf16 v[32:35], v[16:19], v[192:199], v67\n"
    "v_smfmac_f32_16x16x64_bf16 v[36:39], v[16:19], v[200:207], v67\n"
    "s_waitcnt vmcnt(1)\n"
    "v_smfmac_f32_16x16x64_bf16 v[40:43], v[20:23], a[40:47], v67\n"
    "ds_write_b128 v71, v[20:23] offset:5120\n"
    "s_waitcnt lgkmcnt(1)\n"
    "s_barrier\n"
    "ds_read_b128 v[24:27], v71 offset:6144\n"
    "v_smfmac_f32_16x16x64_bf16 v[44:47], v[20:23], a[104:111], v67\n"
    "ds_read_b128 v[0:3], v71 offset:0\n"
    "v_smfmac_f32_16x16x64_bf16 v[32:35], v[20:23], v[208:215], v67\n"
    "ds_read_b128 v[8:11], v71 offset:2048\n"
    "v_smfmac_f32_16x16x64_bf16 v[36:39], v[20:23], v[216:223], v67\n"
    "s_waitcnt lgkmcnt(3)\n"
    "s_barrier\n"
    "v_smfmac_f32_16x16x64_bf16 v[48:51], v[16:19], a[160:167], v67\n"
    "s_waitcnt lgkmcnt(2)\n"
    "ds_read_b128 v[28:31], v71 offset:7168\n"
    "v_smfmac_f32_16x16x64_bf16 v[32:35], v[24:27], v[224:231], v67\n"
    "ds_read_b128 v[4:7], v71 offset:1024\n"
    "v_smfmac_f32_16x16x64_bf16 v[36:39], v[24:27], v[232:239], v67\n"
    "s_waitcnt lgkmcnt(2)\n"
    "ds_read_b128 v[12:15], v71 offset:3072\n"
    "v_smfmac_f32_16x16x64_bf16 v[32:35], v[0:3], v[128:135], v67\n"
    "ds_read_b128 a[32:35], v116 offset:0\n"
    "v_smfmac_f32_16x16x64_bf16 v[36:39], v[0:3], v[136:143], v67\n"
    "ds_read_b128 a[36:39], v116 offset:1024\n"
    "v_smfmac_f32_16x16x64_bf16 v[32:35], v[8:11], v[160:167], v67\n"
    "ds_read_b128 a[96:99], v116 offset:4096\n"
    "v_smfmac_f32_16x16x64_bf16 v[36:39], v[8:11], v[168:175], v67\n"
    "s_waitcnt lgkmcnt(5)\n"
    "ds_read_b128 a[100:103], v116 offset:5120\n"
    "v_smfmac_f32_16x16x64_bf16 v[32:35], v[28:31], v[240:247], v67\n"
    "ds_read_b128 a[40:43], v116 offset:2048\n"
    "v_smfmac_f32_16x16x64_bf16 v[36:39], v[28:31], v[248:255], v67\n"
    "s_waitcnt lgkmcnt(5)\n"
    "ds_read_b128 a[44:47], v116 offset:3072\n"
    "v_smfmac_f32_16x16x64_bf16 v[32:35], v[4:7], v[144:151], v67\n"
    "ds_read_b128 a[104:107], v116 offset:6144\n"
    "v_smfmac_f32_16x16x64_bf16 v[36:39], v[4:7], v[152:159], v67\n"
    "ds_read_b128 a[108:111], v116 offset:7168\n"
    "v_smfmac_f32_16x16x64_bf16 v[32:35], v[12:15], v[176:183], v67\n"
    "v_smfmac_f32_16x16x64_bf16 v[36:39], v[12:15], v[184:191], v67\n"
    "v_smfmac_f32_16x16x64_bf16 v[40:43], v[24:27], a[48:55], v67\n"
    "v_add_f32 v32, v32, v34\n"
    "v_add_f32 v33, v33, v35\n"
    "v_smfmac_f32_16x16x64_bf16 v[44:47], v[24:27], a[112:119], v67\n"
    "ds_read_b128 a[48:51], v116 offset:8192\n"
    "v_cvt_pk_bf16_f32 v126, v32, v33\n"
    "v_add_f32 v36, v36, v38\n"
    "v_add_f32 v37, v37, v39\n"
    "v_cvt_pk_bf16_f32 v127, v36, v37\n"
    "v_smfmac_f32_16x16x64_bf16 v[40:43], v[0:3], a[0:7], v67\n"
    "ds_write_b64 v114, v[126:127] offset:9216\n"
    "v_smfmac_f32_16x16x64_bf16 v[44:47], v[0:3], a[64:71], v67\n"
    "ds_read_b128 a[52:55], v116 offset:9216\n"
    "v_cmp_u_f32 vcc, v32, v33\n"
    "v_smfmac_f32_16x16x64_bf16 v[40:43], v[8:11], a[16:23], v67\n"
    "ds_read_b128 a[112:115], v116 offset:12288\n"
    "v_smfmac_f32_16x16x64_bf16 v[44:47], v[8:11], a[80:87], v67\n"
    "s_cbranch_vccnz L_slow2%=\n"
    "L_go2%=:\n"
    "v_smfmac_f32_16x16x64_bf16 v[40:43], v[28:31], a[56:63], v67\n"
    "s_waitcnt lgkmcnt(2)\n"
    "s_barrier\n"
    "ds_read_b64_tr_b16 v[32:33], v112 offset:8192\n"
    "v_smfmac_f32_16x16x64_bf16 v[44:47], v[28:31], a[120:127], v67\n"
    "ds_read_b64_tr_b16 v[34:35], v113 offset:8192\n"
    "ds_read_b64_tr_b16 v[36:37], v112 offset:9216\n"
    "v_smfmac_f32_16x16x64_bf16 v[40:43], v[4:7], a[8:15], v67\n"
    "ds_read_b64_tr_b16 v[38:39], v113 offset:9216\n"
    "v_smfmac_f32_16x16x64_bf16 v[44:47], v[4:7], a[72:79], v67\n"
    "global_load_dwordx4 v[196:199], v71, s[60:61] offset:1024 sc1\n"
    "v_smfmac_f32_16x16x64_bf16 v[40:43], v[12:15], a[24:31], v67\n"
    "ds_read_b128 a[116:119], v116 offset:13312\n"
    "v_smfmac_f32_16x16x64_bf16 v[44:47], v[12:15], a[88:95], v67\n"
    "s_waitcnt lgkmcnt(1)\n"
    "ds_read_b128 a[0:3], v116 offset:10240\n"
    "v_mfma_f32_16x16x32_bf16 v[88:91], v[56:59], v[32:35], 0\n"
    "v_add_f32 v40, v40, v42\n"
    "v_add_f32 v41, v41, v43\n"
    "v_mfma_f32_16x16x32_bf16 v[88:91], v[60:63], v[36:39], v[88:91]\n"
    "v_cvt_pk_bf16_f32 v126, v40, v41\n"
    "v_add_f32 v44, v44, v46\n"
    "v_add_f32 v45, v45, v47\n"
    "v_cvt_pk_bf16_f32 v127, v44, v45\n"
    "v_smfmac_f32_16x16x64_bf16 v[52:55], v[16:19], a[224:231], v67\n"
    "ds_write_b64 v115, v[126:127] offset:11264\n"
    "v_add_f32_dpp v88, v90, v88 quad_perm:[2,3,2,3] row_mask:0xf bank_mask:0xf\n"
    "v_add_f32_dpp v89, v91, v89 quad_perm:[2,3,2,3] row_mask:0xf bank_mask:0xf\n"
    "v_smfmac_f32_16x16x64_bf16 v[48:51], v[20:23], a[168:175], v67\n"
    "ds_write_b64 v75, v[88:89] offset:14848\n"
    "v_smfmac_f32_16x16x64_bf16 v[52:55], v[20:23], a[232:239], v67\n"
    "global_load_dwordx4 v[56:59], v71, s[58:59] offset:-2048 sc1\n"
    "v_smfmac_f32_16x16x64_bf16 v[48:51], v[24:27], a[176:183], v67\n"
    "global_load_dwordx4 v[60:63], v71, s[58:59] offset:2048 sc1\n"
    "v_smfmac_f32_16x16x64_bf16 v[52:55], v[24:27], a[240:247], v67\n"
    "global_load_dwordx4 v[192:195], v71, s[60:61] offset:0 sc1\n"
    "v_smfmac_f32_16x16x64_bf16 v[48:51], v[0:3], a[128:135], v67\n"
    "s_waitcnt lgkmcnt(0)\n"
    "s_barrier\n"
    "ds_read_b64_tr_b16 v[40:41], v112 offset:10240\n"
    "v_smfmac_f32_16x16x64_bf16 v[52:55], v[0:3], a[192:199], v67\n"
    "ds_read_b64_tr_b16 v[42:43], v113 offset:10240\n"
    "ds_read_b64_tr_b16 v[44:45], v112 offset:11264\n"
    "v_smfmac_f32_16x16x64_bf16 v[48:51], v[8:11], a[144:151], v67\n"
    "ds_read_b64_tr_b16 v[46:47], v113 offset:11264\n"
    "v_smfmac_f32_16x16x64_bf16 v[52:55], v[8:11], a[208:215], v67\n"
    "ds_read_b128 v[88:91], v71 offset:14336\n"
    "s_cmp_eq_u32 s43, 63\n"
    "v_smfmac_f32_16x16x64_bf16 v[48:51], v[28:31], a[184:191], v67\n"
    "s_waitcnt lgkmcnt(3)\n"
    "ds_read_b128 a[4:7], v116 offset:11264\n"
    "v_mfma_f32_16x16x32_bf16 v[96:99], v[40:43], v[32:35], 0\n"
    "s_waitcnt lgkmcnt(2)\n"
    "ds_read_b128 a[64:67], v116 offset:14336\n"
    "v_mfma_f32_16x16x32_bf16 v[96:99], v[44:47], v[36:39], v[96:99]\n"
    "s_cbranch_scc1 L_last2%=\n"
    "v_smfmac_f32_16x16x64_bf16 v[52:55], v[28:31], a[248:255], v67\n"
    "s_waitcnt lgkmcnt(2)\n"
    "ds_read_b128 a[68:71], v116 offset:15360\n"
    "v_max3_f32 v122, v88, v89, v90\n"
    "v_max_f32 v122, v122, v91\n"
    "v_add_f32_dpp v96, v98, v96 quad_perm:[2,3,2,3] row_mask:0xf bank_mask:0xf\n"
    "v_add_f32_dpp v97, v99, v97 quad_perm:[2,3,2,3] row_mask:0xf bank_mask:0xf\n"
    "v_max_f32_dpp v123, v122, v122 quad_perm:[2,3,0,1] row_mask:0xf bank_mask:0xf\n"
    "v_max3_f32 v122, v123, v96, v97\n"
    "v_smfmac_f32_16x16x64_bf16 v[48:51], v[4:7], a[136:143], v67\n"
    "v_max_f32_dpp v121, v122, v123 quad_perm:[0,1,0,1] row_mask:0xf bank_mask:0xf\n"
    "v_max_f32_dpp v123, v122, v123 quad_perm:[0,1,0,1] row_mask:0xf bank_mask:0xf\n"
    "v_smfmac_f32_16x16x64_bf16 v[52:55], v[4:7], a[200:207], v67\n"
    "v_permlane16_swap_b32 v123, v121\n"
    "v_max_f32 v122, v123, v121\n"
    "v_max_f32 v123, v123, v121\n"
    "v_smfmac_f32_16x16x64_bf16 v[48:51], v[12:15], a[152:159], v67\n"
    "v_smfmac_f32_16x16x64_bf16 v[52:55], v[12:15], a[216:223], v67\n"
    "v_permlane32_swap_b32 v122, v123\n"
    "v_max_f32 v122, v122, v123\n"
    "v_mul_f32 v122, s76, v122\n"
    "v_pk_fma_f32 v[96:97], v[96:97], s[76:77], v[122:123] op_sel_hi:[1,1,0] neg_lo:[0,0,1] neg_hi:[0,0,1]\n"
    "v_pk_fma_f32 v[88:89], v[88:89], s[76:77], v[122:123] op_sel_hi:[1,1,0] neg_lo:[0,0,1] neg_hi:[0,0,1]\n"
    "v_pk_fma_f32 v[90:91], v[90:91], s[76:77], v[122:123] op_sel_hi:[1,1,0] neg_lo:[0,0,1] neg_hi:[0,0,1]\n"
    "v_exp_f32 v96, v96\n"
    "v_exp_f32 v97, v97\n"
    "v_exp_f32 v88, v88\n"
    "v_exp_f32 v89, v89\n"
    "v_exp_f32 v90, v90\n"
    "v_exp_f32 v91, v91\n"
    "v_pk_add_f32 v[48:49], v[48:49], v[50:51]\n"
    "v_pk_add_f32 v[52:53], v[52:53], v[54:55]\n"
    "v_cvt_pk_bf16_f32 v126, v96, v97\n"
    "v_cvt_pk_bf16_f32 v100, v88, v89\n"
    "v_cvt_pk_bf16_f32 v102, v90, v91\n"
    "v_cndmask_b32 v101, 0, v126, s[72:73]\n"
    "v_cndmask_b32 v103, 0, v126, s[74:75]\n"
    "v_cvt_pk_bf16_f32 v66, v48, v49\n"
    "v_cvt_pk_bf16_f32 v70, v48, v49\n"
    "v_cvt_pk_bf16_f32 v74, v52, v53\n"
    "v_cvt_pk_bf16_f32 v78, v52, v53\n"
    "v_smfmac_f32_16x16x64_bf16 v[104:107], v[100:103], v[64:71], v67\n"
    "v_smfmac_f32_16x16x64_bf16 v[108:111], v[100:103], v[72:79], v67\n"
    "v_mfma_f32_16x16x32_bf16 v[84:87], v[100:103], v[80:83], 0\n"
    "v_add_f32 v104, v104, v106\n"
    "v_add_f32 v105, v105, v107\n"
    "v_cvt_pk_bf16_f32 v104, v104, v105\n"
    "v_add_f32 v108, v108, v110\n"
    "v_add_f32 v109, v109, v111\n"
    "v_cvt_pk_bf16_f32 v105, v108, v109\n"
    "ds_write_b64 v114, v[104:105] offset:13312\n"
    "v_pk_add_f32 v[124:125], v[84:85], v[86:87]\n"
    "v_rcp_f32 v124, v124\n"
    "v_rcp_f32 v125, v125\n"
    "v_mov_b64 v[0:1], 0\n"
    "v_mov_b64 v[2:3], 0\n"
    "v_mov_b64 v[4:5], 0\n"
    "v_mov_b64 v[6:7], 0\n"
    "v_mov_b64 v[8:9], 0\n"
    "v_mov_b64 v[10:11], 0\n"
    "v_mov_b64 v[12:13], 0\n"
    "v_mov_b64 v[14:15], 0\n"
    "v_mfma_f32_16x16x32_bf16 v[96:99], v[100:103], v[80:83], 0\n"
    "s_waitcnt lgkmcnt(0)\n"
    "s_barrier\n"
    "ds_read_b64_tr_b16 v[32:33], v112 offset:12288\n"
    "v_mfma_f32_16x16x32_bf16 v[96:99], v[100:103], v[80:83], 0\n"
    "ds_read_b64_tr_b16 v[34:35], v113 offset:12288\n"
    "ds_read_b64_tr_b16 v[36:37], v112 offset:13312\n"
    "v_mfma_f32_16x16x32_bf16 v[96:99], v[100:103], v[80:83], 0\n"
    "ds_read_b64_tr_b16 v[38:39], v113 offset:13312\n"
    "v_mfma_f32_16x16x32_bf16 v[96:99], v[100:103], v[80:83], 0\n"
    "s_waitcnt lgkmcnt(2)\n"
    "v_smfmac_f32_16x16x64_bf16 v[0:3], v[32:35], a[32:39], v67\n"
    "v_smfmac_f32_16x16x64_bf16 v[4:7], v[32:35], a[96:103], v67\n"
    "s_waitcnt lgkmcnt(0)\n"
    "v_smfmac_f32_16x16x64_bf16 v[0:3], v[36:39], a[40:47], v67\n"
    "v_smfmac_f32_16x16x64_bf16 v[4:7], v[36:39], a[104:111], v67\n"
    "v_smfmac_f32_16x16x64_bf16 v[8:11], v[32:35], a[48:55], v67\n"
    "v_add_f32 v0, v0, v2\n"
    "v_smfmac_f32_16x16x64_bf16 v[12:15], v[32:35], a[112:119], v67\n"
    "v_add_f32 v1, v1, v3\n"
    "v_smfmac_f32_16x16x64_bf16 v[8:11], v[36:39], a[0:7], v67\n"
    "v_add_f32 v4, v4, v6\n"
    "v_smfmac_f32_16x16x64_bf16 v[12:15], v[36:39], a[64:71], v67\n"
    "v_add_f32 v5, v5, v7\n"
    "v_mul_f32 v0, v0, v124\n"
    "v_mul_f32 v1, v1, v125\n"
    "v_cvt_pk_bf16_f32 v88, v0, v1\n"
    "v_pk_mul_f32 v[4:5], v[4:5], v[124:125]\n"
    "v_pk_add_f32 v[8:9], v[8:9], v[10:11]\n"
    "v_pk_add_f32 v[12:13], v[12:13], v[14:15]\n"
    "v_cvt_pk_bf16_f32 v89, v4, v5\n"
    "v_pk_mul_f32 v[8:9], v[8:9], v[124:125]\n"
    "v_pk_mul_f32 v[12:13], v[12:13], v[124:125]\n"
    "v_cvt_pk_bf16_f32 v90, v8, v9\n"
    "v_cvt_pk_bf16_f32 v91, v12, v13\n"
    "global_store_dwordx4 v117, v[88:91], s[44:45] sc1\n"
    "s_add_u32 s43, s43, 1\n"
    "s_sleep 3\n"
    "s_add_u32 s79, s43, 1\n"
    "s_min_u32 s79, s79, 63\n"
    "s_lshr_b32 s79, s79, 1\n"
    "s_cmp_lt_u32 s79, s92\n"
    "s_cbranch_scc1 L_cntokl%=\n"
    "L_cntl%=:\n"
    "v_and_b32 v126, 16, v71\n"
    "v_lshlrev_b32 v126, 8, v126\n"
    "global_load_dwordx2 v[126:127], v126, s[66:67] sc1\n"
    "s_waitcnt vmcnt(0)\n"
    "v_xor_b32 v126, s96, v126\n"
    "v_xor_b32 v127, s97, v127\n"
    "v_cmp_gt_u64 vcc, 64, v[126:127]\n"
    "s_nop 1\n"
    "v_cndmask_b32 v126, 0, v126, vcc\n"
    "s_nop 0\n"
    "v_readlane_b32 s80, v126, 0\n"
    "v_readlane_b32 s81, v126, 1\n"
    "s_nop 1\n"
    "s_min_u32 s92, s80, s81\n"
    "s_cmp_lt_u32 s79, s92\n"
    "s_cbranch_scc1 L_cntokl%=\n"
    "s_sleep 2\n"
    "s_add_u32 s90, s90, 1\n"
    "s_and_b32 s78, s90, 0xff\n"
    "s_cmp_lg_u32 s78, 0\n"
    "s_cbranch_scc1 L_cntl%=\n"
    "v_mov_b32 v126, 0\n"
    "global_load_dwordx2 v[126:127], v126, s[68:69] sc1\n"
    "s_waitcnt vmcnt(0)\n"
    "v_cmp_eq_u64 vcc, s[96:97], v[126:127]\n"
    "s_nop 1\n"
    "s_cbranch_vccnz L_bail_cntl%=\n"
    "s_cmp_lt_u32 s90, 0x40000\n"
    "s_cbranch_scc1 L_cntl%=\n"
    "v_mov_b32 v126, s96\n"
    "v_mov_b32 v127, s97\n"
    "s_mov_b64 exec, 1\n"
    "global_store_dwordx2 v71, v[126:127], s[68:69] sc1\n"
    "s_mov_b64 exec, -1\n"
    "L_bail_cntl%=:\n"
    "L_cntokl%=:\n"
    "ds_read_b128 a[32:35], v116 offset:16384\n"
    "ds_read_b128 a[36:39], v116 offset:17408\n"
    "ds_read_b128 a[96:99], v116 offset:18432\n"
    "ds_read_b128 a[100:103], v116 offset:19456\n"
    "ds_read_b128 a[40:43], v116 offset:20480\n"
    "ds_read_b128 a[44:47], v116 offset:21504\n"
    "ds_read_b128 a[104:107], v116 offset:22528\n"
    "ds_read_b128 a[108:111], v116 offset:23552\n"
    "ds_read_b128 a[48:51], v116 offset:24576\n"
    "ds_read_b128 a[52:55], v116 offset:25600\n"
    "ds_read_b128 a[112:115], v116 offset:26624\n"
    "ds_read_b128 a[116:119], v116 offset:27648\n"
    "ds_read_b128 a[0:3], v116 offset:28672\n"
    "s_waitcnt lgkmcnt(8)\n"
    "ds_read_b128 a[4:7], v116 offset:29696\n"
    "ds_read_b128 a[64:67], v116 offset:30720\n"
    "ds_read_b128 a[68:71], v116 offset:31744\n"
    "s_and_b32 s78, s43, 3\n"
    "s_lshl_b32 s78, s78, 17\n"
    "s_add_u32 s54, s48, s78\n"
    "s_addc_u32 s55, s49, 0\n"
    "s_add_u32 s62, s62, 0x20000\n"
    "s_addc_u32 s63, s63, 0\n"
    "v_mov_b64 v[32:33], 0\n"
    "v_mov_b64 v[34:35], 0\n"
    "v_mov_b64 v[36:37], 0\n"
    "v_mov_b64 v[38:39], 0\n"
    "v_mov_b64 v[48:49], 0\n"
    "v_mov_b64 v[50:51], 0\n"
    "v_mov_b64 v[52:53], 0\n"
    "v_mov_b64 v[54:55], 0\n"
    "v_mov_b64 v[104:105], 0\n"
    "v_mov_b64 v[106:107], 0\n"
    "v_mov_b64 v[108:109], 0\n"
    "v_mov_b64 v[110:111], 0\n"
    "s_sub_u32 s78, s43, 1\n"
    "s_lshr_b32 s78, s78, 2\n"
    "s_lshr_b32 s79, s43, 2\n"
    "s_lshr_b32 s78, s93, s78\n"
    "s_lshr_b32 s79, s93, s79\n"
    "s_xor_b32 s78, s78, s79\n"
    "s_and_b32 s78, s78, 1\n"
    "s_lshl_b32 s78, s78, 13\n"
    "v_xor_b32 v117, s78, v117\n"
    "s_add_u32 s44, s44, 0x80000\n"
    "s_addc_u32 s45, s45, 0\n"
    "s_add_u32 s58, s58, 0x40000\n"
    "s_addc_u32 s59, s59, 0\n"
    "s_add_u32 s60, s60, 0x40000\n"
    "s_addc_u32 s61, s61, 0\n"
    "s_waitcnt vmcnt(1)\n"
    "v_mov_b64 v[64:65], v[192:193]\n"
    "v_mov_b64 v[68:69], v[194:195]\n"
    "v_mov_b64 v[72:73], v[196:197]\n"
    "v_mov_b64 v[76:77], v[198:199]\n"
    "ds_read_b128 v[192:195], v116 offset:32768\n"
    "ds_read_b128 v[196:199], v116 offset:33792\n"
    "s_waitcnt lgkmcnt(0)\n"
    "s_barrier\n"
    "global_load_dwordx4 v[16:19], v71, s[54:55] offset:0 sc1\n"
    "s_nop 3\n"
    "global_load_dwordx4 v[20:23], v71, s[54:55] offset:1024 sc1\n"
    "s_nop 15\n"
    "s_cmp_lg_u32 s42, 0\n"
    "s_cbranch_scc1 L_vmb2%=\n"
    "s_mov_b32 exec_hi, 0\n"
    "global_store_dwordx4 v71, v[44:47], s[62:63] offset:1024 sc1\n"
    "s_mov_b64 exec, -1\n"
    "s_branch L_kvb2%=\n"
    "L_vmb2%=:\n"
    "v_mov_b32 v126, v66\n"
    "v_mov_b32 v127, v74\n"
    "global_store_dwordx2 v79, v[126:127], s[62:63] offset:1024 sc1\n"
    "L_kvb2%=:\n"
    "s_nop 1\n"
    "v_mov_b64 v[40:41], 0\n"
    "v_mov_b64 v[42:43], 0\n"
    "v_mov_b64 v[44:45], 0\n"
    "v_mov_b64 v[46:47], 0\n"
    "s_branch L_top2%=\n"
    "L_slow2%=:\n"
    "v_cmp_eq_u32 s[78:79], s50, v16\n"
    "v_cmp_eq_u32 s[80:81], s50, v17\n"
    "v_cmp_eq_u32 s[82:83], s50, v18\n"
    "v_cmp_eq_u32 s[84:85], s50, v19\n"
    "v_cmp_eq_u32 s[86:87], s50, v20\n"
    "v_cmp_eq_u32 s[88:89], s50, v21\n"
    "v_cmp_eq_u32 s[52:53], s50, v22\n"
    "v_cmp_eq_u32 vcc, s50, v23\n"
    "global_load_dwordx4 v[16:19], v71, s[54:55] offset:0 sc1\n"
    "global_load_dwordx4 v[20:23], v71, s[54:55] offset:1024 sc1\n"
    "s_waitcnt lgkmcnt(0)\n"
    "s_barrier\n"
    "s_or_b64 vcc, vcc, s[78:79]\n"
    "s_or_b64 vcc, vcc, s[80:81]\n"
    "s_or_b64 vcc, vcc, s[82:83]\n"
    "s_or_b64 vcc, vcc, s[84:85]\n"
    "s_or_b64 vcc, vcc, s[86:87]\n"
    "s_or_b64 vcc, vcc, s[88:89]\n"
    "s_or_b64 vcc, vcc, s[52:53]\n"
    "s_cbranch_scc1 L_redo2%=\n"
    "v_cmp_eq_u32 s[78:79], s50, v24\n"
    "v_cmp_eq_u32 s[80:81], s50, v25\n"
    "v_cmp_eq_u32 s[82:83], s50, v26\n"
    "v_cmp_eq_u32 s[84:85], s50, v27\n"
    "v_cmp_eq_u32 s[86:87], s50, v0\n"
    "v_cmp_eq_u32 s[88:89], s50, v1\n"
    "v_cmp_eq_u32 s[52:53], s50, v2\n"
    "v_cmp_eq_u32 vcc, s50, v3\n"
    "s_nop 0\n"
    "s_or_b64 vcc, vcc, s[78:79]\n"
    "s_or_b64 vcc, vcc, s[80:81]\n"
    "s_or_b64 vcc, vcc, s[82:83]\n"
    "s_or_b64 vcc, vcc, s[84:85]\n"
    "s_or_b64 vcc, vcc, s[86:87]\n"
    "s_or_b64 vcc, vcc, s[88:89]\n"
    "s_or_b64 vcc, vcc, s[52:53]\n"
    "s_cbranch_scc1 L_redo2%=\n"
    "v_cmp_eq_u32 s[78:79], s50, v8\n"
    "v_cmp_eq_u32 s[80:81], s50, v9\n"
    "v_cmp_eq_u32 s[82:83], s50, v10\n"
    "v_cmp_eq_u32 s[84:85], s50, v11\n"
    "v_cmp_eq_u32 s[86:87], s50, v28\n"
    "v_cmp_eq_u32 s[88:89], s50, v29\n"
    "v_cmp_eq_u32 s[52:53], s50, v30\n"
    "v_cmp_eq_u32 vcc, s50, v31\n"
    "s_nop 0\n"
    "s_or_b64 vcc, vcc, s[78:79]\n"
    "s_or_b64 vcc, vcc, s[80:81]\n"
    "s_or_b64 vcc, vcc, s[82:83]\n"
    "s_or_b64 vcc, vcc, s[84:85]\n"
    "s_or_b64 vcc, vcc, s[86:87]\n"
    "s_or_b64 vcc, vcc, s[88:89]\n"
    "s_or_b64 vcc, vcc, s[52:53]\n"
    "s_cbranch_scc1 L_redo2%=\n"
    "v_cmp_eq_u32 s[78:79], s50, v4\n"
    "v_cmp_eq_u32 s[80:81], s50, v5\n"
    "v_cmp_eq_u32 s[82:83], s50, v6\n"
    "v_cmp_eq_u32 s[84:85], s50, v7\n"
    "v_cmp_eq_u32 s[86:87], s50, v12\n"
    "v_cmp_eq_u32 s[88:89], s50, v13\n"
    "v_cmp_eq_u32 s[52:53], s50, v14\n"
    "v_cmp_eq_u32 vcc, s50, v15\n"
    "s_nop 0\n"
    "s_or_b64 vcc, vcc, s[78:79]\n"
    "s_or_b64 vcc, vcc, s[80:81]\n"
    "s_or_b64 vcc, vcc, s[82:83]\n"
    "s_or_b64 vcc, vcc, s[84:85]\n"
    "s_or_b64 vcc, vcc, s[86:87]\n"
    "s_or_b64 vcc, vcc, s[88:89]\n"
    "s_or_b64 vcc, vcc, s[52:53]\n"
    "s_cbranch_scc1 L_redo2%=\n"
    "s_waitcnt vmcnt(0)\n"
    "s_branch L_go2%=\n"
    "L_redo2%=:\n"
    "ds_read_b128 a[32:35], v116 offset:16384\n"
    "ds_read_b128 a[36:39], v116 offset:17408\n"
    "ds_read_b128 a[96:99], v116 offset:18432\n"
    "ds_read_b128 a[100:103], v116 offset:19456\n"
    "ds_read_b128 a[40:43], v116 offset:20480\n"
    "ds_read_b128 a[44:47], v116 offset:21504\n"
    "ds_read_b128 a[104:107], v116 offset:22528\n"
    "ds_read_b128 a[108:111], v116 offset:23552\n"
    "ds_read_b128 a[48:51], v116 offset:24576\n"
    "ds_read_b128 a[52:55], v116 offset:25600\n"
    "ds_read_b128 a[112:115], v116 offset:26624\n"
    "ds_read_b128 a[116:119], v116 offset:27648\n"
    "ds_read_b128 a[0:3], v116 offset:28672\n"
    "ds_read_b128 a[4:7], v116 offset:29696\n"
    "s_waitcnt lgkmcnt(8)\n"
    "ds_read_b128 a[64:67], v116 offset:30720\n"
    "ds_read_b128 a[68:71], v116 offset:31744\n"
    "v_mov_b64 v[32:33], 0\n"
    "v_mov_b64 v[34:35], 0\n"
    "v_mov_b64 v[36:37], 0\n"
    "v_mov_b64 v[38:39], 0\n"
    "v_mov_b64 v[40:41], 0\n"
    "v_mov_b64 v[42:43], 0\n"
    "v_mov_b64 v[44:45], 0\n"
    "v_mov_b64 v[46:47], 0\n"
    "v_mov_b64 v[48:49], 0\n"
    "v_mov_b64 v[50:51], 0\n"
    "v_mov_b64 v[52:53], 0\n"
    "v_mov_b64 v[54:55], 0\n"
    "s_add_u32 s90, s90, 1\n"
    "s_and_b32 s78, s90, 0xff\n"
    "s_cmp_lg_u32 s78, 0\n"
    "s_cbranch_scc1 L_re2%=\n"
    "v_mov_b32 v126, 0\n"
    "global_load_dwordx2 v[126:127], v126, s[68:69] sc1\n"
    "s_waitcnt vmcnt(0)\n"
    "v_cmp_eq_u64 vcc, s[96:97], v[126:127]\n"
    "s_nop 1\n"
    "s_cbranch_vccnz L_bail_g2%=\n"
    "s_cmp_lt_u32 s90, 0x40000\n"
    "s_cbranch_scc1 L_re2%=\n"
    "v_mov_b32 v126, s96\n"
    "v_mov_b32 v127, s97\n"
    "s_mov_b64 exec, 1\n"
    "global_store_dwordx2 v71, v[126:127], s[68:69] sc1\n"
    "s_mov_b64 exec, -1\n"
    "L_bail_g2%=:\n"
    "s_branch L_go2%=\n"
    "L_re2%=:\n"
    "s_waitcnt vmcnt(0) lgkmcnt(0)\n"
    "s_branch L_top2%=\n"
    "L_last2%=:\n"
    "v_smfmac_f32_16x16x64_bf16 v[52:55], v[28:31], a[248:255], v67\n"
    "v_smfmac_f32_16x16x64_bf16 v[48:51], v[4:7], a[136:143], v67\n"
    "v_smfmac_f32_16x16x64_bf16 v[52:55], v[4:7], a[200:207], v67\n"
    "v_smfmac_f32_16x16x64_bf16 v[48:51], v[12:15], a[152:159], v67\n"
    "v_smfmac_f32_16x16x64_bf16 v[52:55], v[12:15], a[216:223], v67\n"
    "s_nop 3\n"
    "v_pk_add_f32 v[48:49], v[48:49], v[50:51]\n"
    "s_nop 2\n"
    "v_pk_add_f32 v[52:53], v[52:53], v[54:55]\n"
    "v_cvt_pk_bf16_f32 v66, v48, v49\n"
    "v_cvt_pk_bf16_f32 v74, v52, v53\n"
    "s_add_u32 s62, s62, 0x20000\n"
    "s_addc_u32 s63, s63, 0\n"
    "s_cmp_lg_u32 s42, 0\n"
    "s_cbranch_scc1 L_vmc2%=\n"
    "s_mov_b32 exec_hi, 0\n"
    "global_store_dwordx4 v71, v[44:47], s[62:63] offset:1024 sc1\n"
    "s_mov_b64 exec, -1\n"
    "s_branch L_kvc2%=\n"
    "L_vmc2%=:\n"
    "v_mov_b32 v126, v66\n"
    "v_mov_b32 v127, v74\n"
    "global_store_dwordx2 v79, v[126:127], s[62:63] offset:1024 sc1\n"
    "L_kvc2%=:\n"
    "s_waitcnt vmcnt(0) lgkmcnt(0)\n"
    "s_barrier\n"
    "s_endpgm\n"
    ".p2align 6\n"
    "L_poll3%=:\n"
    "global_load_dwordx4 v[24:27], v71, s[54:55] offset:0 sc1\n"
    "global_load_dwordx4 v[28:31], v71, s[54:55] offset:1024 sc1\n"
    "s_waitcnt vmcnt(0)\n"
    "s_branch L_top3%=\n"
    ".p2align 6\n"
    "L_top3%=:\n"
    "s_waitcnt vmcnt(2)\n"
    "v_smfmac_f32_16x16x64_bf16 v[40:43], v[24:27], a[48:55], v67\n"
    "ds_write_b128 v71, v[24:27] offset:6144\n"
    "v_smfmac_f32_16x16x64_bf16 v[44:47], v[24:27], a[112:119], v67\n"
    "v_smfmac_f32_16x16x64_bf16 v[32:35], v[24:27], v[224:231], v67\n"
    "v_smfmac_f32_16x16x64_bf16 v[36:39], v[24:27], v[232:239], v67\n"
    "s_waitcnt vmcnt(1)\n"
    "v_smfmac_f32_16x16x64_bf16 v[40:43], v[28:31], a[56:63], v67\n"
    "ds_write_b128 v71, v[28:31] offset:7168\n"
    "s_waitcnt lgkmcnt(1)\n"
    "s_barrier\n"
    "ds_read_b128 v[16:19], v71 offset:4096\n"
    "v_smfmac_f32_16x16x64_bf16 v[44:47], v[28:31], a[120:127], v67\n"
    "ds_read_b128 v[8:11], v71 offset:2048\n"
    "v_smfmac_f32_16x16x64_bf16 v[32:35], v[28:31], v[240:247], v67\n"
    "ds_read_b128 v[0:3], v71 offset:0\n"
    "v_smfmac_f32_16x16x64_bf16 v[36:39], v[28:31], v[248:255], v67\n"
    "s_waitcnt lgkmcnt(3)\n"
    "s_barrier\n"
    "v_smfmac_f32_16x16x64_bf16 v[48:51], v[24:27], a[176:183], v67\n"
    "s_waitcnt lgkmcnt(2)\n"
    "ds_read_b128 v[20:23], v71 offset:5120\n"
    "v_smfmac_f32_16x16x64_bf16 v[32:35], v[16:19], v[192:199], v67\n"
    "ds_read_b128 v[12:15], v71 offset:3072\n"
    "v_smfmac_f32_16x16x64_bf16 v[36:39], v[16:19], v[200:207], v67\n"
    "s_waitcnt lgkmcnt(2)\n"
    "ds_read_b128 v[4:7], v71 offset:1024\n"
    "v_smfmac_f32_16x16x64_bf16 v[32:35], v[8:11], v[160:167], v67\n"
    "ds_read_b128 a[48:51], v116 offset:0\n"
    "v_smfmac_f32_16x16x64_bf16 v[36:39], v[8:11], v[168:175], v67\n"
    "ds_read_b128 a[52:55], v116 offset:1024\n"
    "v_smfmac_f32_16x16x64_bf16 v[32:35], v[0:3], v[128:135], v67\n"
    "ds_read_b128 a[112:115], v116 offset:4096\n"
    "v_smfmac_f32_16x16x64_bf16 v[36:39], v[0:3], v[136:143], v67\n"
    "s_waitcnt lgkmcnt(5)\n"
    "ds_read_b128 a[116:119], v116 offset:5120\n"
    "v_smfmac_f32_16x16x64_bf16 v[32:35], v[20:23], v[208:215], v67\n"
    "ds_read_b128 a[56:59], v116 offset:2048\n"
    "v_smfmac_f32_16x16x64_bf16 v[36:39], v[20:23], v[216:223], v67\n"
    "s_waitcnt lgkmcnt(5)\n"
    "ds_read_b128 a[60:63], v116 offset:3072\n"
    "v_smfmac_f32_16x16x64_bf16 v[32:35], v[12:15], v[176:183], v67\n"
    "ds_read_b128 a[120:123], v116 offset:6144\n"
    "v_smfmac_f32_16x16x64_bf16 v[36:39], v[12:15], v[184:191], v67\n"
    "ds_read_b128 a[124:127], v116 offset:7168\n"
    "v_smfmac_f32_16x16x64_bf16 v[32:35], v[4:7], v[144:151], v67\n"
    "v_smfmac_f32_16x16x64_bf16 v[36:39], v[4:7], v[152:159], v67\n"
    "v_smfmac_f32_16x16x64_bf16 v[40:43], v[16:19], a[32:39], v67\n"
    "v_add_f32 v32, v32, v34\n"
    "v_add_f32 v33, v33, v35\n"
    "v_smfmac_f32_16x16x64_bf16 v[44:47], v[16:19], a[96:103], v67\n"
    "ds_read_b128 a[32:35], v116 offset:8192\n"
    "v_cvt_pk_bf16_f32 v126, v32, v33\n"
    "v_add_f32 v36, v36, v38\n"
    "v_add_f32 v37, v37, v39\n"
    "v_cvt_pk_bf16_f32 v127, v36, v37\n"
    "v_smfmac_f32_16x16x64_bf16 v[40:43], v[8:11], a[16:23], v67\n"
    "ds_write_b64 v114, v[126:127] offset:9728\n"
    "v_smfmac_f32_16x16x64_bf16 v[44:47], v[8:11], a[80:87], v67\n"
    "ds_read_b128 a[36:39], v116 offset:9216\n"
    "v_cmp_u_f32 vcc, v32, v33\n"
    "v_smfmac_f32_16x16x64_bf16 v[40:43], v[0:3], a[0:7], v67\n"
    "ds_read_b128 a[96:99], v116 offset:12288\n"
    "v_smfmac_f32_16x16x64_bf16 v[44:47], v[0:3], a[64:71], v67\n"
    "s_cbranch_vccnz L_slow3%=\n"
    "L_go3%=:\n"
    "v_smfmac_f32_16x16x64_bf16 v[40:43], v[20:23], a[40:47], v67\n"
    "s_waitcnt lgkmcnt(2)\n"
    "s_barrier\n"
    "ds_read_b64_tr_b16 v[32:33], v112 offset:8192\n"
    "v_smfmac_f32_16x16x64_bf16 v[44:47], v[20:23], a[104:111], v67\n"
    "ds_read_b64_tr_b16 v[34:35], v113 offset:8192\n"
    "ds_read_b64_tr_b16 v[36:37], v112 offset:9216\n"
    "v_smfmac_f32_16x16x64_bf16 v[40:43], v[12:15], a[24:31], v67\n"
    "ds_read_b64_tr_b16 v[38:39], v113 offset:9216\n"
    "v_smfmac_f32_16x16x64_bf16 v[44:47], v[12:15], a[88:95], v67\n"
    "global_load_dwordx4 v[228:231], v71, s[60:61] offset:1024 sc1\n"
    "v_smfmac_f32_16x16x64_bf16 v[40:43], v[4:7], a[8:15], v67\n"
    "ds_read_b128 a[100:103], v116 offset:13312\n"
    "v_smfmac_f32_16x16x64_bf16 v[44:47], v[4:7], a[72:79], v67\n"
    "s_waitcnt lgkmcnt(1)\n"
    "ds_read_b128 a[16:19], v116 offset:10240\n"
    "v_mfma_f32_16x16x32_bf16 v[88:91], v[56:59], v[32:35], 0\n"
    "v_add_f32 v40, v40, v42\n"
    "v_add_f32 v41, v41, v43\n"
    "v_mfma_f32_16x16x32_bf16 v[88:91], v[60:63], v[36:39], v[88:91]\n"
    "v_cvt_pk_bf16_f32 v126, v40, v41\n"
    "v_add_f32 v44, v44, v46\n"
    "v_add_f32 v45, v45, v47\n"
    "v_cvt_pk_bf16_f32 v127, v44, v45\n"
    "v_smfmac_f32_16x16x64_bf16 v[52:55], v[24:27], a[240:247], v67\n"
    "ds_write_b64 v115, v[126:127] offset:11776\n"
    "v_add_f32_dpp v88, v90, v88 quad_perm:[2,3,2,3] row_mask:0xf bank_mask:0xf\n"
    "v_add_f32_dpp v89, v91, v89 quad_perm:[2,3,2,3] row_mask:0xf bank_mask:0xf\n"
    "v_smfmac_f32_16x16x64_bf16 v[48:51], v[28:31], a[184:191], v67\n"
    "ds_write_b64 v75, v[88:89] offset:14880\n"
    "v_smfmac_f32_16x16x64_bf16 v[52:55], v[28:31], a[248:255], v67\n"
    "global_load_dwordx4 v[56:59], v71, s[58:59] offset:-2048 sc1\n"
    "v_smfmac_f32_16x16x64_bf16 v[48:51], v[16:19], a[160:167], v67\n"
    "global_load_dwordx4 v[60:63], v71, s[58:59] offset:2048 sc1\n"
    "v_smfmac_f32_16x16x64_bf16 v[52:55], v[16:19], a[224:231], v67\n"
    "global_load_dwordx4 v[224:227], v71, s[60:61] offset:0 sc1\n"
    "v_smfmac_f32_16x16x64_bf16 v[48:51], v[8:11], a[144:151], v67\n"
    "s_waitcnt lgkmcnt(0)\n"
    "s_barrier\n"
    "ds_read_b64_tr_b16 v[40:41], v112 offset:10240\n"
    "v_smfmac_f32_16x16x64_bf16 v[52:55], v[8:11], a[208:215], v67\n"
    "ds_read_b64_tr_b16 v[42:43], v113 offset:10240\n"
    "ds_read_b64_tr_b16 v[44:45], v112 offset:11264\n"
    "v_smfmac_f32_16x16x64_bf16 v[48:51], v[0:3], a[128:135], v67\n"
    "ds_read_b64_tr_b16 v[46:47], v113 offset:11264\n"
    "v_smfmac_f32_16x16x64_bf16 v[52:55], v[0:3], a[192:199], v67\n"
    "ds_read_b128 v[88:91], v71 offset:14336\n"
    "s_cmp_eq_u32 s43, 63\n"
    "v_smfmac_f32_16x16x64_bf16 v[48:51], v[20:23], a[168:175], v67\n"
    "s_waitcnt lgkmcnt(3)\n"
    "ds_read_b128 a[20:23], v116 offset:11264\n"
    "v_mfma_f32_16x16x32_bf16 v[96:99], v[40:43], v[32:35], 0\n"
    "s_waitcnt lgkmcnt(2)\n"
    "ds_read_b128 a[80:83], v116 offset:14336\n"
    "v_mfma_f32_16x16x32_bf16 v[96:99], v[44:47], v[36:39], v[96:99]\n"
    "s_cbranch_scc1 L_last3%=\n"
    "v_smfmac_f32_16x16x64_bf16 v[52:55], v[20:23], a[232:239], v67\n"
    "s_waitcnt lgkmcnt(2)\n"
    "ds_read_b128 a[84:87], v116 offset:15360\n"
    "v_max3_f32 v122, v88, v89, v90\n"
    "v_max_f32 v122, v122, v91\n"
    "v_add_f32_dpp v96, v98, v96 quad_perm:[2,3,2,3] row_mask:0xf bank_mask:0xf\n"
    "v_add_f32_dpp v97, v99, v97 quad_perm:[2,3,2,3] row_mask:0xf bank_mask:0xf\n"
    "v_max_f32_dpp v123, v122, v122 quad_perm:[2,3,0,1] row_mask:0xf bank_mask:0xf\n"
    "v_max3_f32 v122, v123, v96, v97\n"
    "v_smfmac_f32_16x16x64_bf16 v[48:51], v[12:15], a[152:159], v67\n"
    "v_max_f32_dpp v121, v122, v123 quad_perm:[0,1,0,1] row_mask:0xf bank_mask:0xf\n"
    "v_max_f32_dpp v123, v122, v123 quad_perm:[0,1,0,1] row_mask:0xf bank_mask:0xf\n"
    "v_smfmac_f32_16x16x64_bf16 v[52:55], v[12:15], a[216:223], v67\n"
    "v_permlane16_swap_b32 v123, v121\n"
    "v_max_f32 v122, v123, v121\n"
    "v_max_f32 v123, v123, v121\n"
    "v_smfmac_f32_16x16x64_bf16 v[48:51], v[4:7], a[136:143], v67\n"
    "v_smfmac_f32_16x16x64_bf16 v[52:55], v[4:7], a[200:207], v67\n"
    "v_permlane32_swap_b32 v122, v123\n"
    "v_max_f32 v122, v122, v123\n"
    "v_mul_f32 v122, s76, v122\n"
    "v_pk_fma_f32 v[96:97], v[96:97], s[76:77], v[122:123] op_sel_hi:[1,1,0] neg_lo:[0,0,1] neg_hi:[0,0,1]\n"
    "v_pk_fma_f32 v[88:89], v[88:89], s[76:77], v[122:123] op_sel_hi:[1,1,0] neg_lo:[0,0,1] neg_hi:[0,0,1]\n"
    "v_pk_fma_f32 v[90:91], v[90:91], s[76:77], v[122:123] op_sel_hi:[1,1,0] neg_lo:[0,0,1] neg_hi:[0,0,1]\n"
    "v_exp_f32 v96, v96\n"
    "v_exp_f32 v97, v97\n"
    "v_exp_f32 v88, v88\n"
    "v_exp_f32 v89, v89\n"
    "v_exp_f32 v90, v90\n"
    "v_exp_f32 v91, v91\n"
    "v_pk_add_f32 v[48:49], v[48:49], v[50:51]\n"
    "v_pk_add_f32 v[52:53], v[52:53], v[54:55]\n"
    "v_cvt_pk_bf16_f32 v126, v96, v97\n"
    "v_cvt_pk_bf16_f32 v100, v88, v89\n"
    "v_cvt_pk_bf16_f32 v102, v90, v91\n"
    "v_cndmask_b32 v101, 0, v126, s[72:73]\n"
    "v_cndmask_b32 v103, 0, v126, s[74:75]\n"
    "v_cvt_pk_bf16_f32 v66, v48, v49\n"
    "v_cvt_pk_bf16_f32 v70, v48, v49\n"
    "v_cvt_pk_bf16_f32 v74, v52, v53\n"
    "v_cvt_pk_bf16_f32 v78, v52, v53\n"
    "v_smfmac_f32_16x16x64_bf16 v[104:107], v[100:103], v[64:71], v67\n"
    "v_smfmac_f32_16x16x64_bf16 v[108:111], v[100:103], v[72:79], v67\n"
    "v_mfma_f32_16x16x32_bf16 v[84:87], v[100:103], v[80:83], 0\n"
    "v_add_f32 v104, v104, v106\n"
    "v_add_f32 v105, v105, v107\n"
    "v_cvt_pk_bf16_f32 v104, v104, v105\n"
    "v_add_f32 v108, v108, v110\n"
    "v_add_f32 v109, v109, v111\n"
    "v_cvt_pk_bf16_f32 v105, v108, v109\n"
    "ds_write_b64 v114, v[104:105] offset:13824\n"
    "v_pk_add_f32 v[124:125], v[84:85], v[86:87]\n"
    "v_rcp_f32 v124, v124\n"
    "v_rcp_f32 v125, v125\n"
    "v_mov_b64 v[0:1], 0\n"
    "v_mov_b64 v[2:3], 0\n"
    "v_mov_b64 v[4:5], 0\n"
    "v_mov_b64 v[6:7], 0\n"
    "v_mov_b64 v[8:9], 0\n"
    "v_mov_b64 v[10:11], 0\n"
    "v_mov_b64 v[12:13], 0\n"
    "v_mov_b64 v[14:15], 0\n"
    "v_mfma_f32_16x16x32_bf16 v[96:99], v[100:103], v[80:83], 0\n"
    "s_waitcnt lgkmcnt(0)\n"
    "s_barrier\n"
    "ds_read_b64_tr_b16 v[32:33], v112 offset:12288\n"
    "v_mfma_f32_16x16x32_bf16 v[96:99], v[100:103], v[80:83], 0\n"
    "ds_read_b64_tr_b16 v[34:35], v113 offset:12288\n"
    "ds_read_b64_tr_b16 v[36:37], v112 offset:13312\n"
    "v_mfma_f32_16x16x32_bf16 v[96:99], v[100:103], v[80:83], 0\n"
    "ds_read_b64_tr_b16 v[38:39], v113 offset:13312\n"
    "v_mfma_f32_16x16x32_bf16 v[96:99], v[100:103], v[80:83], 0\n"
    "s_waitcnt lgkmcnt(2)\n"
    "v_smfmac_f32_16x16x64_bf16 v[0:3], v[32:35], a[48:55], v67\n"
    "v_smfmac_f32_16x16x64_bf16 v[4:7], v[32:35], a[112:119], v67\n"
    "s_waitcnt lgkmcnt(0)\n"
    "v_smfmac_f32_16x16x64_bf16 v[0:3], v[36:39], a[56:63], v67\n"
    "v_smfmac_f32_16x16x64_bf16 v[4:7], v[36:39], a[120:127], v67\n"
    "v_smfmac_f32_16x16x64_bf16 v[8:11], v[32:35], a[32:39], v67\n"
    "v_add_f32 v0, v0, v2\n"
    "v_smfmac_f32_16x16x64_bf16 v[12:15], v[32:35], a[96:103], v67\n"
    "v_add_f32 v1, v1, v3\n"
    "v_smfmac_f32_16x16x64_bf16 v[8:11], v[36:39], a[16:23], v67\n"
    "v_add_f32 v4, v4, v6\n"
    "v_smfmac_f32_16x16x64_bf16 v[12:15], v[36:39], a[80:87], v67\n"
    "v_add_f32 v5, v5, v7\n"
    "v_mul_f32 v0, v0, v124\n"
    "v_mul_f32 v1, v1, v125\n"
    "v_cvt_pk_bf16_f32 v88, v0, v1\n"
    "v_pk_mul_f32 v[4:5], v[4:5], v[124:125]\n"
    "v_pk_add_f32 v[8:9], v[8:9], v[10:11]\n"
    "v_pk_add_f32 v[12:13], v[12:13], v[14:15]\n"
    "v_cvt_pk_bf16_f32 v89, v4, v5\n"
    "v_pk_mul_f32 v[8:9], v[8:9], v[124:125]\n"
    "v_pk_mul_f32 v[12:13], v[12:13], v[124:125]\n"
    "v_cvt_pk_bf16_f32 v90, v8, v9\n"
    "v_cvt_pk_bf16_f32 v91, v12, v13\n"
    "global_store_dwordx4 v117, v[88:91], s[44:45] sc1\n"
    "s_add_u32 s43, s43, 1\n"
    "s_sleep 3\n"
    "ds_read_b128 a[48:51], v116 offset:16384\n"
    "ds_read_b128 a[52:55], v116 offset:17408\n"
    "ds_read_b128 a[112:115], v116 offset:18432\n"
    "ds_read_b128 a[116:119], v116 offset:19456\n"
    "ds_read_b128 a[56:59], v116 offset:20480\n"
    "ds_read_b128 a[60:63], v116 offset:21504\n"
    "ds_read_b128 a[120:123], v116 offset:22528\n"
    "ds_read_b128 a[124:127], v116 offset:23552\n"
    "ds_read_b128 a[32:35], v116 offset:24576\n"
    "ds_read_b128 a[36:39], v116 offset:25600\n"
    "ds_read_b128 a[96:99], v116 offset:26624\n"
    "ds_read_b128 a[100:103], v116 offset:27648\n"
    "ds_read_b128 a[16:19], v116 offset:28672\n"
    "s_waitcnt lgkmcnt(8)\n"
    "ds_read_b128 a[20:23], v116 offset:29696\n"
    "ds_read_b128 a[80:83], v116 offset:30720\n"
    "ds_read_b128 a[84:87], v116 offset:31744\n"
    "s_and_b32 s78, s43, 3\n"
    "s_lshl_b32 s78, s78, 17\n"
    "s_add_u32 s54, s48, s78\n"
    "s_addc_u32 s55, s49, 0\n"
    "s_add_u32 s62, s62, 0x20000\n"
    "s_addc_u32 s63, s63, 0\n"
    "v_mov_b64 v[32:33], 0\n"
    "v_mov_b64 v[34:35], 0\n"
    "v_mov_b64 v[36:37], 0\n"
    "v_mov_b64 v[38:39], 0\n"
    "v_mov_b64 v[48:49], 0\n"
    "v_mov_b64 v[50:51], 0\n"
    "v_mov_b64 v[52:53], 0\n"
    "v_mov_b64 v[54:55], 0\n"
    "v_mov_b64 v[104:105], 0\n"
    "v_mov_b64 v[106:107], 0\n"
    "v_mov_b64 v[108:109], 0\n"
    "v_mov_b64 v[110:111], 0\n"
    "s_sub_u32 s78, s43, 1\n"
    "s_lshr_b32 s78, s78, 2\n"
    "s_lshr_b32 s79, s43, 2\n"
    "s_lshr_b32 s78, s93, s78\n"
    "s_lshr_b32 s79, s93, s79\n"
    "s_xor_b32 s78, s78, s79\n"
    "s_and_b32 s78, s78, 1\n"
    "s_lshl_b32 s78, s78, 13\n"
    "v_xor_b32 v117, s78, v117\n"
    "s_add_u32 s44, s44, 0x80000\n"
    "s_addc_u32 s45, s45, 0\n"
    "s_add_u32 s58, s58, 0x40000\n"
    "s_addc_u32 s59, s59, 0\n"
    "s_add_u32 s60, s60, 0x40000\n"
    "s_addc_u32 s61, s61, 0\n"
    "s_waitcnt vmcnt(1)\n"
    "v_mov_b64 v[64:65], v[224:225]\n"
    "v_mov_b64 v[68:69], v[226:227]\n"
    "v_mov_b64 v[72:73], v[228:229]\n"
    "v_mov_b64 v[76:77], v[230:231]\n"
    "ds_read_b128 v[224:227], v116 offset:32768\n"
    "ds_read_b128 v[228:231], v116 offset:33792\n"
    "s_waitcnt lgkmcnt(0)\n"
    "s_barrier\n"
    "global_load_dwordx4 v[24:27], v71, s[54:55] offset:0 sc1\n"
    "s_nop 3\n"
    "global_load_dwordx4 v[28:31], v71, s[54:55] offset:1024 sc1\n"
    "s_nop 15\n"
    "s_cmp_lg_u32 s42, 0\n"
    "s_cbranch_scc1 L_vmb3%=\n"
    "s_mov_b32 exec_lo, 0\n"
    "global_store_dwordx4 v71, v[44:47], s[62:63] offset:1024 sc1\n"
    "s_mov_b64 exec, -1\n"
    "s_branch L_kvb3%=\n"
    "L_vmb3%=:\n"
    "v_mov_b32 v126, v66\n"
    "v_mov_b32 v127, v74\n"
    "global_store_dwordx2 v79, v[126:127], s[62:63] offset:1536 sc1\n"
    "L_kvb3%=:\n"
    "s_nop 1\n"
    "v_mov_b64 v[40:41], 0\n"
    "v_mov_b64 v[42:43], 0\n"
    "v_mov_b64 v[44:45], 0\n"
    "v_mov_b64 v[46:47], 0\n"
    "s_branch L_top3%=\n"
    "L_slow3%=:\n"
    "v_cmp_eq_u32 s[78:79], s50, v24\n"
    "v_cmp_eq_u32 s[80:81], s50, v25\n"
    "v_cmp_eq_u32 s[82:83], s50, v26\n"
    "v_cmp_eq_u32 s[84:85], s50, v27\n"
    "v_cmp_eq_u32 s[86:87], s50, v28\n"
    "v_cmp_eq_u32 s[88:89], s50, v29\n"
    "v_cmp_eq_u32 s[52:53], s50, v30\n"
    "v_cmp_eq_u32 vcc, s50, v31\n"
    "global_load_dwordx4 v[24:27], v71, s[54:55] offset:0 sc1\n"
    "global_load_dwordx4 v[28:31], v71, s[54:55] offset:1024 sc1\n"
    "s_waitcnt lgkmcnt(0)\n"
    "s_barrier\n"
    "s_or_b64 vcc, vcc, s[78:79]\n"
    "s_or_b64 vcc, vcc, s[80:81]\n"
    "s_or_b64 vcc, vcc, s[82:83]\n"
    "s_or_b64 vcc, vcc, s[84:85]\n"
    "s_or_b64 vcc, vcc, s[86:87]\n"
    "s_or_b64 vcc, vcc, s[88:89]\n"
    "s_or_b64 vcc, vcc, s[52:53]\n"
    "s_cbranch_scc1 L_redo3%=\n"
    "v_cmp_eq_u32 s[78:79], s50, v16\n"
    "v_cmp_eq_u32 s[80:81], s50, v17\n"
    "v_cmp_eq_u32 s[82:83], s50, v18\n"
    "v_cmp_eq_u32 s[84:85], s50, v19\n"
    "v_cmp_eq_u32 s[86:87], s50, v8\n"
    "v_cmp_eq_u32 s[88:89], s50, v9\n"
    "v_cmp_eq_u32 s[52:53], s50, v10\n"
    "v_cmp_eq_u32 vcc, s50, v11\n"
    "s_nop 0\n"
    "s_or_b64 vcc, vcc, s[78:79]\n"
    "s_or_b64 vcc, vcc, s[80:81]\n"
    "s_or_b64 vcc, vcc, s[82:83]\n"
    "s_or_b64 vcc, vcc, s[84:85]\n"
    "s_or_b64 vcc, vcc, s[86:87]\n"
    "s_or_b64 vcc, vcc, s[88:89]\n"
    "s_or_b64 vcc, vcc, s[52:53]\n"
    "s_cbranch_scc1 L_redo3%=\n"
    "v_cmp_eq_u32 s[78:79], s50, v0\n"
    "v_cmp_eq_u32 s[80:81], s50, v1\n"
    "v_cmp_eq_u32 s[82:83], s50, v2\n"
    "v_cmp_eq_u32 s[84:85], s50, v3\n"
    "v_cmp_eq_u32 s[86:87], s50, v20\n"
    "v_cmp_eq_u32 s[88:89], s50, v21\n"
    "v_cmp_eq_u32 s[52:53], s50, v22\n"
    "v_cmp_eq_u32 vcc, s50, v23\n"
    "s_nop 0\n"
    "s_or_b64 vcc, vcc, s[78:79]\n"
    "s_or_b64 vcc, vcc, s[80:81]\n"
    "s_or_b64 vcc, vcc, s[82:83]\n"
    "s_or_b64 vcc, vcc, s[84:85]\n"
    "s_or_b64 vcc, vcc, s[86:87]\n"
    "s_or_b64 vcc, vcc, s[88:89]\n"
    "s_or_b64 vcc, vcc, s[52:53]\n"
    "s_cbranch_scc1 L_redo3%=\n"
    "v_cmp_eq_u32 s[78:79], s50, v12\n"
    "v_cmp_eq_u32 s[80:81], s50, v13\n"
    "v_cmp_eq_u32 s[82:83], s50, v14\n"
    "v_cmp_eq_u32 s[84:85], s50, v15\n"
    "v_cmp_eq_u32 s[86:87], s50, v4\n"
    "v_cmp_eq_u32 s[88:89], s50, v5\n"
    "v_cmp_eq_u32 s[52:53], s50, v6\n"
    "v_cmp_eq_u32 vcc, s50, v7\n"
    "s_nop 0\n"
    "s_or_b64 vcc, vcc, s[78:79]\n"
    "s_or_b64 vcc, vcc, s[80:81]\n"
    "s_or_b64 vcc, vcc, s[82:83]\n"
    "s_or_b64 vcc, vcc, s[84:85]\n"
    "s_or_b64 vcc, vcc, s[86:87]\n"
    "s_or_b64 vcc, vcc, s[88:89]\n"
    "s_or_b64 vcc, vcc, s[52:53]\n"
    "s_cbranch_scc1 L_redo3%=\n"
    "s_waitcnt vmcnt(0)\n"
    "s_branch L_go3%=\n"
    "L_redo3%=:\n"
    "ds_read_b128 a[48:51], v116 offset:16384\n"
    "ds_read_b128 a[52:55], v116 offset:17408\n"
    "ds_read_b128 a[112:115], v116 offset:18432\n"
    "ds_read_b128 a[116:119], v116 offset:19456\n"
    "ds_read_b128 a[56:59], v116 offset:20480\n"
    "ds_read_b128 a[60:63], v116 offset:21504\n"
    "ds_read_b128 a[120:123], v116 offset:22528\n"
    "ds_read_b128 a[124:127], v116 offset:23552\n"
    "ds_read_b128 a[32:35], v116 offset:24576\n"
    "ds_read_b128 a[36:39], v116 offset:25600\n"
    "ds_read_b128 a[96:99], v116 offset:26624\n"
    "ds_read_b128 a[100:103], v116 offset:27648\n"
    "ds_read_b128 a[16:19], v116 offset:28672\n"
    "ds_read_b128 a[20:23], v116 offset:29696\n"
    "s_waitcnt lgkmcnt(8)\n"
    "ds_read_b128 a[80:83], v116 offset:30720\n"
    "ds_read_b128 a[84:87], v116 offset:31744\n"
    "v_mov_b64 v[32:33], 0\n"
    "v_mov_b64 v[34:35], 0\n"
    "v_mov_b64 v[36:37], 0\n"
    "v_mov_b64 v[38:39], 0\n"
    "v_mov_b64 v[40:41], 0\n"
    "v_mov_b64 v[42:43], 0\n"
    "v_mov_b64 v[44:45], 0\n"
    "v_mov_b64 v[46:47], 0\n"
    "v_mov_b64 v[48:49], 0\n"
    "v_mov_b64 v[50:51], 0\n"
    "v_mov_b64 v[52:53], 0\n"
    "v_mov_b64 v[54:55], 0\n"
    "s_add_u32 s90, s90, 1\n"
    "s_and_b32 s78, s90, 0xff\n"
    "s_cmp_lg_u32 s78, 0\n"
    "s_cbranch_scc1 L_re3%=\n"
    "v_mov_b32 v126, 0\n"
    "global_load_dwordx2 v[126:127], v126, s[68:69] sc1\n"
    "s_waitcnt vmcnt(0)\n"
    "v_cmp_eq_u64 vcc, s[96:97], v[126:127]\n"
    "s_nop 1\n"
    "s_cbranch_vccnz L_bail_g3%=\n"
    "s_cmp_lt_u32 s90, 0x40000\n"
    "s_cbranch_scc1 L_re3%=\n"
    "v_mov_b32 v126, s96\n"
    "v_mov_b32 v127, s97\n"
    "s_mov_b64 exec, 1\n"
    "global_store_dwordx2 v71, v[126:127], s[68:69] sc1\n"
    "s_mov_b64 exec, -1\n"
    "L_bail_g3%=:\n"
    "s_branch L_go3%=\n"
    "L_re3%=:\n"
    "s_waitcnt vmcnt(0) lgkmcnt(0)\n"
    "s_branch L_top3%=\n"
    "L_last3%=:\n"
    "v_smfmac_f32_16x16x64_bf16 v[52:55], v[20:23], a[232:239], v67\n"
    "v_smfmac_f32_16x16x64_bf16 v[48:51], v[12:15], a[152:159], v67\n"
    "v_smfmac_f32_16x16x64_bf16 v[52:55], v[12:15], a[216:223], v67\n"
    "v_smfmac_f32_16x16x64_bf16 v[48:51], v[4:7], a[136:143], v67\n"
    "v_smfmac_f32_16x16x64_bf16 v[52:55], v[4:7], a[200:207], v67\n"
    "s_nop 3\n"
    "v_pk_add_f32 v[48:49], v[48:49], v[50:51]\n"
    "s_nop 2\n"
    "v_pk_add_f32 v[52:53], v[52:53], v[54:55]\n"
    "v_cvt_pk_bf16_f32 v66, v48, v49\n"
    "v_cvt_pk_bf16_f32 v74, v52, v53\n"
    "s_add_u32 s62, s62, 0x20000\n"
    "s_addc_u32 s63, s63, 0\n"
    "s_cmp_lg_u32 s42, 0\n"
    "s_cbranch_scc1 L_vmc3%=\n"
    "s_mov_b32 exec_lo, 0\n"
    "global_store_dwordx4 v71, v[44:47], s[62:63] offset:1024 sc1\n"
    "s_mov_b64 exec, -1\n"
    "s_branch L_kvc3%=\n"
    "L_vmc3%=:\n"
    "v_mov_b32 v126, v66\n"
    "v_mov_b32 v127, v74\n"
    "global_store_dwordx2 v79, v[126:127], s[62:63] offset:1536 sc1\n"
    "L_kvc3%=:\n"
    "s_waitcnt vmcnt(0) lgkmcnt(0)\n"
    "s_barrier\n"
    "s_endpgm\n"
    :: "s"(ws), "s"(__builtin_amdgcn_readfirstlane(by * 4 + bx)), "s"(wave), "s"(ldsb), "s"(ws + OFF_EP), "s"(Wq), "s"(Wk), "s"(Wv), "s"(Wo) PADOP
    : "memory", "vcc", "v255", "a255", "s34", "s35", "s36", "s37", "s38", "s39", "s40", "s96", "s97", "s99", "s22", "s23", "s24", "s25", "s26", "s27", "s28", "s29", "scc");
  __builtin_unreachable();
}

extern "C" void kernel_launch(void* const* d_in, const int* in_sizes, int n_in, void* d_out, int out_size, void* d_ws, size_t ws_size,
                              hipStream_t stream) {
  const u16* x = (const u16*)d_in[0];
  const u16* mem0 = (const u16*)d_in[1];
  const u16* Wq = (const u16*)d_in[2];
  const u16* Wk = (const u16*)d_in[3];
  const u16* Wv = (const u16*)d_in[4];
  const u16* Wo = (const u16*)d_in[5];
  const u16* w_r = (const u16*)d_in[8];
  const u16* w_gate = (const u16*)d_in[9];
  const u16* w_up = (const u16*)d_in[10];
  const u16* w_down = (const u16*)d_in[11];
  u16* out = (u16*)d_out;
  char* ws = (char*)(((size_t)d_ws + 2 * MBY - 1) & ~(size_t)(2 * MBY - 1));
  rec_kernel<<<256, 256, 0, stream>>>(ws, x, mem0, Wq, Wk, Wv, Wo, w_r, w_gate, w_up, w_down, out);
}
```

```cpp
#define RD2 220
#include <hip/hip_runtime.h>
#include <hip/hip_bf16.h>

typedef unsigned short u16;
typedef unsigned long long u64;
typedef __attribute__((ext_vector_type(8))) short bf16x8;
typedef __attribute__((ext_vector_type(4))) float f32x4;
typedef __attribute__((ext_vector_type(4))) unsigned u32x4;
#define GAS __attribute__((address_space(1)))
typedef GAS u64 gu64;
typedef GAS unsigned gu32;
#define RLX_AGENT __ATOMIC_RELAXED, __HIP_MEMORY_SCOPE_AGENT

constexpr int S_ = 2048, DM = 512, NH = 32, DH = 128, MM = 8, SEG = 32, NSEG = 64;
constexpr int HD = NH * DH;
constexpr int QKVN = 3 * HD;
constexpr int FF = 1792;

__device__ __forceinline__ float bf2f(u16 v) { return __builtin_bit_cast(float, (unsigned)v << 16); }
__device__ __forceinline__ float rbf(float f) { return (float)(__bf16)f; }

template <int CTRL> __device__ __forceinline__ float dpp_add(float v) {
  return v + __builtin_bit_cast(float, __builtin_amdgcn_update_dpp(0, __builtin_bit_cast(int, v), CTRL, 0xf, 0xf, false));
}
__device__ __forceinline__ float wave_sum(float v) {
  v = dpp_add<0x121>(v); v = dpp_add<0x122>(v); v = dpp_add<0x124>(v); v = dpp_add<0x128>(v);
  auto r = __builtin_amdgcn_permlane16_swap(__builtin_bit_cast(unsigned, v), __builtin_bit_cast(unsigned, v), false, false);
  const unsigned r0 = r[0], r1 = r[1];
  v = __builtin_bit_cast(float, r0) + __builtin_bit_cast(float, r1);
  auto q = __builtin_amdgcn_permlane32_swap(__builtin_bit_cast(unsigned, v), __builtin_bit_cast(unsigned, v), false, false);
  const unsigned q0 = q[0], q1 = q[1];
  return __builtin_bit_cast(float, q0) + __builtin_bit_cast(float, q1);
}
typedef __bf16 bf16x2_t __attribute__((ext_vector_type(2)));
typedef float f32x2_t __attribute__((ext_vector_type(2)));
__device__ __forceinline__ unsigned pk2(float a, float b) {
  f32x2_t v; v.x = a; v.y = b;
  return __builtin_bit_cast(unsigned, __builtin_convertvector(v, bf16x2_t));
}
__device__ __forceinline__ void rbf2(float& a, float& b) {
  const unsigned p = pk2(a, b);
  a = __builtin_bit_cast(float, p << 16); b = __builtin_bit_cast(float, p & 0xffff0000u);
}
__device__ __forceinline__ void ln_row(const float (&x)[8], float (&y)[8]) {
  float s = ((x[0] + x[1]) + (x[2] + x[3])) + ((x[4] + x[5]) + (x[6] + x[7]));
  s = wave_sum(s);
  const float mean = s * (1.0f / DM);
  float q = 0.f;
#pragma unroll
  for (int i = 0; i < 8; ++i) { float d = x[i] - mean; q += d * d; }
  q = wave_sum(q);
  float var = q * (1.0f / DM), mb = mean;
  rbf2(var, mb);
  const float sd = rbf(__builtin_amdgcn_sqrtf(rbf(var + 1e-5f)));
  const float inv = __builtin_amdgcn_rcpf(sd);
#pragma unroll
  for (int i = 0; i < 8; i += 2) {
    float a = x[i] - mb, b = x[i + 1] - mb;
    rbf2(a, b);
    a *= inv; b *= inv;
    rbf2(a, b);
    y[i] = a; y[i + 1] = b;
  }
}

constexpr size_t MBY = 1u << 20;
constexpr size_t OFF_G3 = 0, OFF_SYNC = 2 * MBY, OFF_G2 = 116 * MBY;
constexpr size_t OFF_EP = OFF_SYNC, OFF_ERR = OFF_SYNC + 512, OFF_RDY = OFF_SYNC + 0x1000, OFF_ACT = OFF_SYNC + 0x140000, OFF_RC2 = OFF_SYNC + 0x128000;
constexpr size_t OFF_CLM = OFF_SYNC + 0x10000, OFF_TAB = OFF_SYNC + 0xC000, OFF_XFL = OFF_SYNC + 0xE000;
constexpr size_t OFF_PIL = OFF_G3 + 0x80000, OFF_FLAGS = OFF_SYNC + 0x22000, OFF_OFL = OFF_SYNC + 0xA2000, OFF_HFL = OFF_SYNC + 0xE2000;
constexpr size_t OFF_RC = OFF_SYNC + 0x102000, OFF_PAR = OFF_SYNC + 0x112000;
constexpr unsigned SENT = 0xFFBFFFBFu;
constexpr size_t OFF_KF = 21 * MBY, OFF_VF = 37 * MBY, OFF_KM = 100 * MBY, OFF_VM = 108 * MBY;
constexpr size_t OFF_QF = 61 * MBY, OFF_XN = 77 * MBY, OFF_O = 79 * MBY, OFF_HP = 148 * MBY;

__device__ __forceinline__ unsigned tl_idx() { unsigned xcc; asm volatile("s_getreg_b32 %0, hwreg(HW_REG_XCC_ID)" : "=s"(xcc)); return (blockIdx.x & ~7u) | (xcc & 7u); }
#define GS(i) do {} while (0)
#define TS2(i) do {} while (0)
#define TSD() do {} while (0)
#define TL(i) do {} while (0)
#define TLP() do {} while (0)
__device__ __forceinline__ u64 now_clk() { u64 t; asm volatile("s_memtime %0\n\ts_waitcnt lgkmcnt(0)" : "=s"(t) :: "memory"); return t; }

constexpr int NREC = 64;

__device__ __forceinline__ bool spin_bail(unsigned& spins, char* ws, u64 T) {
  ++spins;
  if ((spins & 255u) == 0u) {
    gu64* err = (gu64*)(ws + OFF_ERR);
    if (__hip_atomic_load(err, RLX_AGENT) == T) return true;
    if (spins > (1u << 20)) { __hip_atomic_store(err, T, RLX_AGENT); return true; }
  }
  return false;
}
__device__ __forceinline__ void set_flag(char* ws, size_t off, u64 T, unsigned v) { __hip_atomic_store((gu64*)(ws + off), T + v, RLX_AGENT); }
__device__ __forceinline__ bf16x8 mk8(unsigned a, unsigned b, unsigned c, unsigned d) {
  u32x4 v; v.x = a; v.y = b; v.z = c; v.w = d; return __builtin_bit_cast(bf16x8, v);
}
__device__ __forceinline__ void store16_sc1(const void* base, unsigned off, u32x4 v) {
  asm volatile("global_store_dwordx4 %0, %1, %2 sc1\n\ts_nop 1" :: "v"(off), "v"(v), "s"(base) : "memory");
}

#define NSPEC (59 * 32)
#define RUP 40u
#define RLOOK 800
#define RPW 600
#define RPP 600
#define ASL 24
#define QSL 1
#define NSIX 42
#define XROT 0
#define NPROBE 6
#define PSL 4
#define HSL 10
#define LAS __attribute__((address_space(3)))
__device__ __forceinline__ void unpack8(const u32x4 v, float (&f)[8]) {
  f[0] = bf2f(v.x & 0xffff); f[1] = bf2f(v.x >> 16); f[2] = bf2f(v.y & 0xffff); f[3] = bf2f(v.y >> 16);
  f[4] = bf2f(v.z & 0xffff); f[5] = bf2f(v.z >> 16); f[6] = bf2f(v.w & 0xffff); f[7] = bf2f(v.w >> 16);
}
template <int SL>
__device__ __forceinline__ void wait_flag(char* ws, unsigned off, u64 T, unsigned target) {
  gu64* p = (gu64*)(ws + off);
  unsigned spins = 0;
  while (!__all(__hip_atomic_load(p, RLX_AGENT) - T - target < 64u - target)) {
    __builtin_amdgcn_s_sleep(SL);
    if (spin_bail(spins, ws, T)) break;
  }
  __builtin_amdgcn_fence(__ATOMIC_ACQUIRE, "wavefront");
}
typedef __attribute__((ext_vector_type(4))) short s16x4;
__device__ __forceinline__ bf16x8 tr_frag(const char* p0, const char* p1) {
  const s16x4 a = __builtin_amdgcn_ds_read_tr16_b64_v4i16((LAS s16x4*)p0), b = __builtin_amdgcn_ds_read_tr16_b64_v4i16((LAS s16x4*)p1);
  return __builtin_shufflevector(a, b, 0, 1, 2, 3, 4, 5, 6, 7);
}

__device__ __forceinline__ float sum_g(float v) {
  auto r = __builtin_amdgcn_permlane16_swap(__builtin_bit_cast(unsigned, v), __builtin_bit_cast(unsigned, v), false, false);
  const unsigned r0 = r[0], r1 = r[1];
  v = __builtin_bit_cast(float, r0) + __builtin_bit_cast(float, r1);
  auto q = __builtin_amdgcn_permlane32_swap(__builtin_bit_cast(unsigned, v), __builtin_bit_cast(unsigned, v), false, false);
  const unsigned q0 = q[0], q1 = q[1];
  return __builtin_bit_cast(float, q0) + __builtin_bit_cast(float, q1);
}
__device__ __forceinline__ float max_g(float v) {
  auto r = __builtin_amdgcn_permlane16_swap(__builtin_bit_cast(unsigned, v), __builtin_bit_cast(unsigned, v), false, false);
  const unsigned r0 = r[0], r1 = r[1];
  v = fmaxf(__builtin_bit_cast(float, r0), __builtin_bit_cast(float, r1));
  auto q = __builtin_amdgcn_permlane32_swap(__builtin_bit_cast(unsigned, v), __builtin_bit_cast(unsigned, v), false, false);
  const unsigned q0 = q[0], q1 = q[1];
  return fmaxf(__builtin_bit_cast(float, q0), __builtin_bit_cast(float, q1));
}
template <class F>
__device__ __forceinline__ void ln_tile(u32x4 (&xr)[16], F hook) {
  auto up = [](unsigned u) { f32x2_t v; v.x = __builtin_bit_cast(float, u << 16); v.y = __builtin_bit_cast(float, u & 0xffff0000u); return v; };
  f32x2_t f[16][4], sb[4] = {{0.f, 0.f}, {0.f, 0.f}, {0.f, 0.f}, {0.f, 0.f}};
#pragma unroll
  for (int k = 0; k < 16; ++k) {
#pragma unroll
    for (int e = 0; e < 4; ++e) { const unsigned u = xr[k][e]; f[k][e] = up(u); sb[e] += f[k][e]; }
    if (k & 1) hook(k >> 1);
  }
  const f32x2_t sa = (sb[0] + sb[1]) + (sb[2] + sb[3]);
  const float mean = sum_g(sa.x + sa.y) * (1.0f / DM);
  f32x2_t qb[4] = {{0.f, 0.f}, {0.f, 0.f}, {0.f, 0.f}, {0.f, 0.f}};
#pragma unroll
  for (int k = 0; k < 16; ++k) {
#pragma unroll
    for (int e = 0; e < 4; ++e) { const f32x2_t d = f[k][e] - mean; qb[e] += d * d; }
    if (k & 1) hook(8 + (k >> 1));
  }
  const f32x2_t qa = (qb[0] + qb[1]) + (qb[2] + qb[3]);
  float var = sum_g(qa.x + qa.y) * (1.0f / DM), mb = mean;
  rbf2(var, mb);
  const float inv = __builtin_amdgcn_rcpf(rbf(__builtin_amdgcn_sqrtf(rbf(var + 1e-5f))));
#pragma unroll
  for (int k = 0; k < 16; ++k)
#pragma unroll
    for (int e = 0; e < 4; ++e) {
      const f32x2_t d = f[k][e] - mb;
      const f32x2_t y = up(pk2(d.x, d.y)) * inv;
      xr[k][e] = pk2(y.x, y.y);
    }
}
__device__ __forceinline__ void probe(char* ws, int q, u64 T) {
      unsigned best[2] = {~0u, ~0u};
#pragma unroll 1
      for (int i = 0; i < NPROBE; ++i) {
        gu32* p = (gu32*)(ws + (q < 16 ? OFF_G2 + q * 2 * MBY + MBY : OFF_G3 + 0xC0000) + (i & 1) * 8192);
        const u64 t0 = now_clk();
        __hip_atomic_store(p, SENT, RLX_AGENT);
        asm volatile("s_waitcnt vmcnt(0)" ::: "memory");
        const unsigned d = (unsigned)(now_clk() - t0);
        if (d < best[i & 1]) best[i & 1] = d;
      }
      set_flag(ws, OFF_PAR + q * 128, T, best[1] < best[0] ? 2u : 1u);
}
__device__ __forceinline__ u64 tag_of(u64 ep) { u64 T = (ep + 1) << 6; asm volatile("" : "+s"(T)); return T; }
__device__ __forceinline__ u64 ln1_phase(char* ws, const u16* __restrict__ x, int q, u64 ep0, char* lds, int tid) {
  const int lane = tid & 63, wv = tid >> 6;
  const int q2 = q;
  if (wv < 2) {
    u32x4 xr[16];
#pragma unroll
    for (int k = 0; k < 16; ++k) xr[k] = *(const u32x4*)(x + (size_t)(q2 * 32 + wv * 16 + (lane & 15)) * DM + k * 32 + (lane >> 4) * 8);
    ln_tile(xr, [](int) {});
#pragma unroll
    for (int k = 0; k < 16; ++k) store16_sc1(ws + OFF_XN, (unsigned)((q2 * 2 + wv) * 16384 + k * 1024 + lane * 16), xr[k]);
  } else {
    const u32x4 z = (u32x4){0u, 0u, 0u, 0u};
    store16_sc1(ws, (unsigned)((wv == 2 ? OFF_RC : OFF_RC2) + q * 1024 + lane * 16), z);
    const u32x4 p = (u32x4){SENT, SENT, SENT, SENT};
    store16_sc1(ws + OFF_ACT, (unsigned)(q * 4096 + (wv - 2) * 2048 + lane * 16), p);
    store16_sc1(ws + OFF_ACT, (unsigned)(q * 4096 + (wv - 2) * 2048 + 1024 + lane * 16), p);
  }
  asm volatile("s_waitcnt vmcnt(0)" ::: "memory");
  __syncthreads();
  const u64 T = tag_of(ep0);
  if (tid == 0) set_flag(ws, OFF_XFL + q * 128, T, 1);
  TL(6);
  return T;
}

__device__ __forceinline__ void load_h(const __amdgpu_buffer_rsrc_t rs, const u32x4 xv, int row, int lane, float (&f)[8]) {
  f32x4 p[4][2];
#pragma unroll
  for (int k = 0; k < 4; ++k)
#pragma unroll
    for (int u = 0; u < 2; ++u)
      p[k][u] = __builtin_bit_cast(f32x4, __builtin_amdgcn_raw_buffer_load_b128(rs, (int)OFF_HP + (((row >> 5) * 4 + k) * 4 + (lane >> 4)) * 16384 + (row & 31) * 512 + (lane & 15) * 32 + u * 16, 0, 16));
  float xf[8];
  unpack8(xv, xf);
#pragma unroll
  for (int i = 0; i < 8; ++i) f[i] = rbf(xf[i] + rbf((p[0][i >> 2][i & 3] + p[1][i >> 2][i & 3]) + (p[2][i >> 2][i & 3] + p[3][i >> 2][i & 3])));
}

__device__ __forceinline__ unsigned wait_par(char* ws, u64 T, int lane) {
  unsigned spins = 0; u64 p;
  for (;;) {
    p = __hip_atomic_load((gu64*)(ws + OFF_PAR + min(lane, 16) * 128), RLX_AGENT) - T;
    if (__all(p - 1 < 2)) break;
    __builtin_amdgcn_s_sleep(8);
    if (spin_bail(spins, ws, T)) break;
  }
  return (unsigned)__ballot(p == 2);
}
template <int MAT>
__device__ __forceinline__ u64 qkv_phase(char* ws, const u16* __restrict__ x, const u16* __restrict__ W, int h, u64 ep0, char* lds, int tid) {
  const int lane = tid & 63, wv = __builtin_amdgcn_readfirstlane(tid >> 6), l15 = lane & 15, g = lane >> 4;
  constexpr int TS = 16384 + 32, BS = 4 * TS;
  bf16x8 wf[4][16];
  const int wr = wv >> 1, wc = wv & 1;
  u32x4 xr[16];
#pragma unroll
  for (int i = 0; i < 16; ++i)
    if (MAT != 0) asm volatile("global_load_dwordx4 %0, %1, off" : "=v"(xr[i]) : "v"(x + (size_t)(wv * 16 + l15) * DM + i * 32 + g * 8) : "memory");
  auto wdma = [&](int i) {
    const int r = (i >> 2) * 64 + wv * 16 + (i & 3) * 4 + g, f = (r & 3) | ((r >> 1) & 4);
    __builtin_amdgcn_sched_barrier(0);
    __builtin_amdgcn_global_load_lds((const GAS void*)(W + (unsigned)(r * HD + h * DH + ((l15 ^ (f << 1)) * 8))),
                                     (LAS void*)(lds + (i >> 4) * BS + ((i >> 2) & 3) * 16384 + (wv * 4 + (i & 3)) * 1024), 16, 0, 0);
    __builtin_amdgcn_sched_barrier(0);
  };
#pragma unroll
  for (int i = 0; i < 16; ++i) wdma(i);
  if (MAT != 0) {
  asm volatile("s_waitcnt vmcnt(16)" : "+v"(xr[0]), "+v"(xr[1]), "+v"(xr[2]), "+v"(xr[3]), "+v"(xr[4]), "+v"(xr[5]), "+v"(xr[6]), "+v"(xr[7]),
               "+v"(xr[8]), "+v"(xr[9]), "+v"(xr[10]), "+v"(xr[11]), "+v"(xr[12]), "+v"(xr[13]), "+v"(xr[14]), "+v"(xr[15]) :: "memory");
  TS2(3);
  ln_tile(xr, [&](int i) { wdma(16 + i); });
#pragma unroll
  for (int i = 0; i < 16; ++i) asm volatile("" : "+v"(xr[i]));
  } else {
#pragma unroll
    for (int i = 16; i < 32; ++i) wdma(i);
  }
  TS2(4);
  {
    const int qq = l15 >> 2, f = qq | ((g & 1) << 2);
    const char* rp = lds + (g * 8 + qq) * 256 + (l15 & 3) * 8;
    asm volatile("s_waitcnt vmcnt(0)" ::: "memory");
    __builtin_amdgcn_s_barrier();
    asm volatile("" ::: "memory");
    TS2(5);
#pragma unroll
    for (int j = 0; j < 4; ++j) {
#pragma unroll
      for (int ks = 0; ks < 16; ++ks) {
        const char* p = rp + (ks >> 3) * BS + (ks & 7) * 8192 + (((wc * 4 + j) ^ f) * 32);
        wf[j][ks] = tr_frag(p, p + 1024);
      }
#pragma unroll
      for (int ks = 0; ks < 16; ++ks) asm volatile("" : "+a"(wf[j][ks]));
    }
    asm volatile("s_waitcnt lgkmcnt(0)" ::: "memory");
    __builtin_amdgcn_s_barrier();
    asm volatile("" ::: "memory");
    if (MAT != 0) {
#pragma unroll
      for (int i = 0; i < 16; ++i) *(u32x4*)(lds + wv * TS + i * 1024 + lane * 16) = xr[i];
    } else {
      wait_flag<4>(ws, (unsigned)OFF_XFL + lane * 128, tag_of(ep0), 1u);
#pragma unroll
      for (int i = 0; i < 16; ++i)
        __builtin_amdgcn_global_load_lds((const GAS void*)(ws + OFF_XN + wv * 16384 + i * 1024 + lane * 16), (LAS void*)(lds + wv * TS + i * 1024), 16, 0, 16);
    }
  }
  const int aoff = MAT == 1 ? (l15 >> 3) * TS + (g * 16 + ((l15 >> 1) & 3) * 4 + (l15 & 1)) * 16 : lane * 16;
  constexpr int ASTEP = MAT == 1 ? 32 : TS;
  const u64 T = tag_of(ep0);
  const unsigned rdy = (unsigned)OFF_RDY + (MAT * 32 + h) * 128;
  constexpr int NST = MAT == 0 ? 4 : 6;
  const unsigned par = MAT != 0 ? wait_par(ws, T, lane) : 0u;
  asm volatile("s_waitcnt lgkmcnt(0)" ::: "memory");
  TS2(6);
#pragma unroll 1
  for (int k = 0; k < 32; ++k) {
    const int c = k, buf = k & 1;
    GS(0);
    if (k <= 2) asm volatile("s_waitcnt vmcnt(0)" ::: "memory");
    else if (NST == 4) asm volatile("s_waitcnt vmcnt(4)" ::: "memory");
    else asm volatile("s_waitcnt vmcnt(6)" ::: "memory");
    __builtin_amdgcn_s_barrier();
    asm volatile("" ::: "memory");
    if (k == 0) TS2(7);
    if (k >= 2 && tid == 0) set_flag(ws, rdy, T, k == 2 ? 2 : k - 1);
    GS(1);
    f32x4 acc[2][4];
    const unsigned ab = (unsigned)(size_t)(LAS char*)lds + buf * BS + wr * 2 * TS + aoff;
    const unsigned dl = __builtin_amdgcn_readfirstlane((unsigned)(size_t)(LAS char*)lds + (buf ^ 1) * BS + wv * TS);
    const unsigned dv = (unsigned)OFF_XN + (min(c + 1, 31) * 4 + wv) * 16384 + lane * 16;
    bf16x8 af[4][2];
#define RDA(d, ks) asm volatile("ds_read_b128 %0, %2 offset:%3\n\tds_read_b128 %1, %2 offset:%4" : "=&v"(af[d][0]), "=&v"(af[d][1]) : "v"(ab), "i"((ks) * 1024), "i"((ks) * 1024 + ASTEP) : "memory")
    RDA(0, 0); RDA(1, 1); RDA(2, 2);
#pragma unroll
    for (int ks = 0; ks < 16; ++ks) {
      if (ks + 3 < 16) {
        switch (ks) {
          case 0: RDA(3, 3); break; case 1: RDA(0, 4); break; case 2: RDA(1, 5); break; case 3: RDA(2, 6); break; case 4: RDA(3, 7); break;
          case 5: RDA(0, 8); break; case 6: RDA(1, 9); break; case 7: RDA(2, 10); break; case 8: RDA(3, 11); break; case 9: RDA(0, 12); break;
          case 10: RDA(1, 13); break; case 11: RDA(2, 14); break; default: RDA(3, 15); break;
        }
      }
      if (k > 0 && ks < 8) asm volatile("s_add_u32 m0, %0, %2\n\ts_nop 0\n\tglobal_load_lds_dwordx4 %1, %3 sc1\n\tglobal_load_lds_dwordx4 %1, %3 offset:1024 sc1"
                                        :: "s"(dl), "v"(dv + ks * 2048), "i"(ks * 2048), "s"(ws) : "memory", "scc");
      if (ks < 13) asm volatile("s_waitcnt lgkmcnt(6)" : "+v"(af[ks & 3][0]), "+v"(af[ks & 3][1]));
      else if (ks == 13) asm volatile("s_waitcnt lgkmcnt(4)" : "+v"(af[1][0]), "+v"(af[1][1]));
      else if (ks == 14) asm volatile("s_waitcnt lgkmcnt(2)" : "+v"(af[2][0]), "+v"(af[2][1]));
      else asm volatile("s_waitcnt lgkmcnt(0)" : "+v"(af[3][0]), "+v"(af[3][1]));
#pragma unroll
      for (int i = 0; i < 2; ++i)
#pragma unroll
        for (int j = 0; j < 4; ++j) {
          if (ks == 0) {
            if (MAT == 2) asm volatile("v_mfma_f32_16x16x32_bf16 %0, %1, %2, 0" : "=&v"(acc[i][j]) : "v"(af[0][i]), "a"(wf[j][0]));
            else asm volatile("v_mfma_f32_16x16x32_bf16 %0, %2, %1, 0" : "=&v"(acc[i][j]) : "v"(af[0][i]), "a"(wf[j][0]));
          } else {
            if (MAT == 2) asm volatile("v_mfma_f32_16x16x32_bf16 %0, %1, %2, %0" : "+v"(acc[i][j]) : "v"(af[ks & 3][i]), "a"(wf[j][ks]));
            else asm volatile("v_mfma_f32_16x16x32_bf16 %0, %2, %1, %0" : "+v"(acc[i][j]) : "v"(af[ks & 3][i]), "a"(wf[j][ks]));
          }
        }
    }
#undef RDA
    GS(2);
    asm volatile("s_nop 7\n\ts_nop 3" : "+v"(acc[0][0]), "+v"(acc[0][1]), "+v"(acc[0][2]), "+v"(acc[0][3]), "+v"(acc[1][0]), "+v"(acc[1][1]), "+v"(acc[1][2]), "+v"(acc[1][3]));
#pragma unroll
    for (int q = 0; q < 4; ++q) {
      const f32x4 a = MAT == 2 ? acc[0][q] : acc[q & 1][(q >> 1) * 2], b = MAT == 2 ? acc[1][q] : acc[q & 1][(q >> 1) * 2 + 1];
      u32x4 v; v.x = pk2(a[0], a[1]); v.y = pk2(a[2], a[3]); v.z = pk2(b[0], b[1]); v.w = pk2(b[2], b[3]);
      store16_sc1(ws + (MAT == 0 ? OFF_QF : (MAT == 1 ? OFF_KF : OFF_VF)), (unsigned)(((c * 2 + wr) * 32 + h) * 8192 + wc * 4096 + q * 1024 + lane * 16), v);
    }
    if (MAT != 0) {
      const u32x4 p = (u32x4){SENT, SENT, SENT, SENT};
#pragma unroll
      for (int i = 0; i < 2; ++i) store16_sc1(ws + OFF_G2, (unsigned)((c * 2 + MAT - 1) * 524288 + (i * 16 + (tid >> 4)) * 16384 + ((par >> (c >> 1)) & 1u) * 8192 + ((tid >> 3) & 1) * 4096 + (h ^ (i * 16 + (tid >> 4))) * 128 + (tid & 7) * 16), p);
    }
    GS(3);
    if (k == 0) {
      asm volatile("s_waitcnt vmcnt(0)" ::: "memory");
      __builtin_amdgcn_s_barrier();
      if (tid == 0) set_flag(ws, rdy, T, 1);
      TS2(1); TSD();
      {
      wait_flag<4>(ws, (unsigned)OFF_XFL + lane * 128, T, 1u);
#pragma unroll
      for (int ks = 0; ks < 16; ++ks)
        asm volatile("s_add_u32 m0, %0, %2\n\ts_nop 0\n\tglobal_load_lds_dwordx4 %1, %3 sc1" :: "s"(dl), "v"(dv + ks * 1024), "i"(ks * 1024), "s"(ws) : "memory", "scc");
      }
    }
  }
  asm volatile("s_waitcnt vmcnt(0)" ::: "memory");
  __builtin_amdgcn_s_barrier();
  if (tid == 0) set_flag(ws, rdy, T, 32);
  return T;
}

__device__ __forceinline__ void xa_phase(char* ws, int grp, int h, u64 T, int lane) {
  const int l15 = lane & 15, g = lane >> 4;
  const float scale = 0.08838834764831845f;
  const __amdgpu_buffer_rsrc_t rs = __builtin_amdgcn_make_buffer_rsrc(ws, 0, (int)(164 * MBY), 0x00020000);
  auto ld16 = [&](int off) { return __builtin_bit_cast(bf16x8, __builtin_amdgcn_raw_buffer_load_b128(rs, off, 0, 16)); };
#pragma unroll 1
  for (int s = grp; s < NSEG; s += 4) {
    wait_flag<8>(ws, (unsigned)OFF_RDY + ((lane < 3 ? lane : 0) * 32 + h) * 128, T, (unsigned)(s >> 1) + 1u);
    const int qp = (int)OFF_QF + (s * 32 + h) * 8192 + lane * 16;
    const int kp = (int)OFF_KF + (s * 32 + h) * 8192 + ((l15 >> 1) & 1) * 1024 + (g * 16 + (l15 >> 2) * 2 + (l15 & 1)) * 16;
    const int vp = (int)OFF_VF + (s * 32 + h) * 8192 + lane * 16;
    const int km = (int)OFF_KM + (s * 32 + h) * 4096;
    const int vm = (int)OFF_VM + (s * 32 + h) * 4096;
    bf16x8 qf[4][2], vf[8];
    f32x4 sc[2][3];
#pragma unroll
    for (int nt = 0; nt < 2; ++nt)
#pragma unroll
      for (int t = 0; t < 3; ++t) sc[nt][t] = (f32x4){0.f, 0.f, 0.f, 0.f};
#pragma unroll
    for (int ks = 0; ks < 4; ++ks) {
      bf16x8 kf[2];
#pragma unroll
      for (int nt = 0; nt < 2; ++nt) qf[ks][nt] = ld16(qp + ks * 2048 + nt * 1024);
#pragma unroll
      for (int t = 0; t < 2; ++t) kf[t] = ld16(kp + ks * 2048 + t * 128);
#pragma unroll
      for (int nt = 0; nt < 2; ++nt)
#pragma unroll
        for (int t = 0; t < 2; ++t) sc[nt][t] = __builtin_amdgcn_mfma_f32_16x16x32_bf16(kf[t], qf[ks][nt], sc[nt][t], 0, 0, 0);
    }
#pragma unroll
    for (int e = 0; e < 8; ++e) vf[e] = ld16(vp + e * 1024);
    float mt[2], lt[2];
    f32x4 acc[2][8];
#pragma unroll
    for (int nt = 0; nt < 2; ++nt) {
      const int qi = nt * 16 + l15;
      float v[2][4], m = -3.0e38f;
#pragma unroll
      for (int t = 0; t < 2; ++t)
#pragma unroll
        for (int r = 0; r < 4; ++r) {
          v[t][r] = (t * 16 + g * 4 + r <= qi) ? rbf(rbf(sc[nt][t][r]) * scale) : -3.0e38f;
          m = fmaxf(m, v[t][r]);
        }
      m = max_g(m);
#pragma unroll
      for (int t = 0; t < 2; ++t)
#pragma unroll
        for (int r = 0; r < 4; r += 2) {
          v[t][r] = (t * 16 + g * 4 + r <= qi) ? __expf(v[t][r] - m) : 0.f;
          v[t][r + 1] = (t * 16 + g * 4 + r + 1 <= qi) ? __expf(v[t][r + 1] - m) : 0.f;
          rbf2(v[t][r], v[t][r + 1]);
        }
      mt[nt] = m;
      lt[nt] = sum_g(((v[0][0] + v[0][1]) + (v[0][2] + v[0][3])) + ((v[1][0] + v[1][1]) + (v[1][2] + v[1][3])));
      const bf16x8 pf = mk8(pk2(v[0][0], v[0][1]), pk2(v[0][2], v[0][3]), pk2(v[1][0], v[1][1]), pk2(v[1][2], v[1][3]));
#pragma unroll
      for (int e = 0; e < 8; ++e) acc[nt][e] = __builtin_amdgcn_mfma_f32_16x16x32_bf16(vf[e], pf, (f32x4){0.f, 0.f, 0.f, 0.f}, 0, 0, 0);
    }
#pragma unroll
    for (int e = 0; e < 8; ++e) asm volatile("" : "+v"(acc[0][e]), "+v"(acc[1][e]));
    asm volatile("" : "+v"(mt[0]), "+v"(mt[1]), "+v"(lt[0]), "+v"(lt[1]));
    wait_flag<PSL>(ws, (unsigned)OFF_FLAGS + (s * 64 + h + (lane & 1) * 32) * 128, T, 1u);
    if (s == 63) TL(4);
    bf16x8 k2[4];
    unsigned mv[4][2];
#pragma unroll
    for (int ks = 0; ks < 4; ++ks) k2[ks] = ld16(km + ks * 1024 + (g * 16 + ((l15 >> 3) | ((l15 >> 1) & 2)) * 4 + (l15 & 1)) * 16);
#pragma unroll
    for (int w = 0; w < 4; ++w) { const auto t = __builtin_amdgcn_raw_buffer_load_b64(rs, vm + w * 512 + lane * 8, 0, 16); mv[w][0] = t[0]; mv[w][1] = t[1]; }
#pragma unroll
    for (int ks = 0; ks < 4; ++ks)
#pragma unroll
      for (int nt = 0; nt < 2; ++nt) sc[nt][2] = __builtin_amdgcn_mfma_f32_16x16x32_bf16(k2[ks], qf[ks][nt], sc[nt][2], 0, 0, 0);
#pragma unroll
    for (int nt = 0; nt < 2; ++nt) {
      float a = sc[nt][2][0], b = sc[nt][2][1];
      rbf2(a, b); a *= scale; b *= scale; rbf2(a, b);
      const float m = fmaxf(mt[nt], max_g(fmaxf(a, b)));
      const float al = __expf(mt[nt] - m);
      a = __expf(a - m); b = __expf(b - m);
      const float inv = __builtin_amdgcn_rcpf(al * lt[nt] + sum_g(a + b));
      const float sa = al * inv;
      const bf16x8 pf1 = mk8(pk2(a * inv, b * inv), 0u, 0u, 0u);
#pragma unroll
      for (int e = 0; e < 8; ++e) acc[nt][e] = __builtin_amdgcn_mfma_f32_16x16x32_bf16(mk8(mv[e >> 1][e & 1], 0u, 0u, 0u), pf1, acc[nt][e] * sa, 0, 0, 0);
#pragma unroll
      for (int w = 0; w < 4; ++w) {
        const f32x4 a0 = acc[nt][2 * w], a1 = acc[nt][2 * w + 1];
        u32x4 o; o.x = pk2(a0[0], a0[1]); o.y = pk2(a1[0], a1[1]); o.z = pk2(a0[2], a0[3]); o.w = pk2(a1[2], a1[3]);
        store16_sc1(ws + OFF_O, (unsigned)(((s * 32 + h) * 8 + nt * 4 + w) * 1024 + lane * 16), o);
      }
    }
    asm volatile("s_waitcnt vmcnt(0)" ::: "memory");
    if (lane == 0) set_flag(ws, OFF_OFL + (s * 32 + h) * 128, T, 1);
  }
}

__device__ __forceinline__ void op_phase(char* ws, const u16* __restrict__ x, const u16* __restrict__ w_r, u16* out, const u16* __restrict__ Wo, int grp, int ks4, int cs, u64 T, char* lds, int tid) {
  const int lane = tid & 63, wv = __builtin_amdgcn_readfirstlane(tid >> 6), l15 = lane & 15, g = lane >> 4;
  const __amdgpu_buffer_rsrc_t rs = __builtin_amdgcn_make_buffer_rsrc(ws, 0, (int)(164 * MBY), 0x00020000);
  const int h0 = ks4 * 8 + wv * 2;
  bf16x8 wo[2][32];
#pragma unroll
  for (int hh = 0; hh < 2; ++hh) {
#pragma unroll
    for (int i = 0; i < 32; ++i) {
      const int r = i * 4 + g, f = (r & 1) | ((r >> 1) & 2) | ((r >> 2) & 4);
      __builtin_amdgcn_global_load_lds((const GAS void*)(Wo + (size_t)((h0 + hh) * DH + r) * DM + cs * 128 + ((l15 ^ (f << 1)) * 8)),
                                       (LAS void*)(lds + wv * 32768 + i * 1024), 16, 0, 0);
    }
    asm volatile("s_waitcnt vmcnt(0)" ::: "memory");
    const int qq = l15 >> 2, f = (qq & 1) | ((g & 1) << 1) | ((qq >> 1) << 2);
    const char* rp = lds + wv * 32768 + (g * 4 + (qq & 1) + (qq >> 1) * 16) * 256 + (l15 & 3) * 8;
#pragma unroll
    for (int j = 0; j < 8; ++j)
#pragma unroll
      for (int ks = 0; ks < 4; ++ks) {
        const char* p = rp + ks * 8192 + ((j ^ f) * 32);
        wo[hh][j * 4 + ks] = tr_frag(p, p + 512);
      }
#pragma unroll
    for (int f2 = 0; f2 < 32; ++f2) asm volatile("" : "+a"(wo[hh][f2]));
    asm volatile("s_waitcnt lgkmcnt(0)" ::: "memory");
  }
  __syncthreads();
  constexpr int RS = 528, RW = 32 * RS;
  auto ln2 = [&](int s, bool fresh) {
      const int row = s * 32 + (ks4 * 4 + cs) * 2 + wv;
      const u32x4 xv = *(const u32x4*)(x + (size_t)row * DM + lane * 8);
      float wr[8];
      unpack8(*(const u32x4*)(w_r + lane * 8), wr);
      if (fresh) __builtin_amdgcn_s_sleep(HSL);
      wait_flag<PSL>(ws, (unsigned)OFF_HFL + s * 2048 + l15 * 128, T, 1u);
      if (s == 63) TL(7);
      float f[8], y[8];
      load_h(rs, xv, row, lane, f);
      ln_row(f, y);
      float r = 0.f;
#pragma unroll
      for (int i = 0; i < 8; ++i) r += y[i] * wr[i];
      r = wave_sum(r);
      u32x4 z;
      z.x = pk2(f[0] + y[0], f[1] + y[1]); z.y = pk2(f[2] + y[2], f[3] + y[3]); z.z = pk2(f[4] + y[4], f[5] + y[5]); z.w = pk2(f[6] + y[6], f[7] + y[7]);
      const unsigned rw = (pk2(r, r) & 0xffff0000u) | 1u;
      if (lane < 8 && row >= NSPEC) __hip_atomic_store((gu32*)(ws + OFF_RC2 + (lane * 64 + ((row - NSPEC) >> 2)) * 128 + (row & 3) * 4), rw, RLX_AGENT);
      store16_sc1(out, (unsigned)(row * 1024 + lane * 16), z);
      asm volatile("s_waitcnt vmcnt(0)" ::: "memory");
      if (lane == 0) __hip_atomic_store((gu32*)(ws + OFF_RC + (row >> 2) * 128 + (row & 3) * 4), rw, RLX_AGENT);
  };
  int pend = -1;
#pragma unroll 1
  for (int s = grp; s < NSEG; s = s + 6 < NSIX ? s + 6 : (s < NSIX ? (grp < 4 ? NSIX + grp : NSEG) : s + 4)) {
    f32x4 acc[2][8];
#pragma unroll
    for (int i = 0; i < 2; ++i)
#pragma unroll
      for (int j = 0; j < 8; ++j) { acc[i][j] = (f32x4){0.f, 0.f, 0.f, 0.f}; asm volatile("" : "+v"(acc[i][j])); }
    if (pend >= 0 && !__all(__hip_atomic_load((gu64*)(ws + OFF_OFL + (s * 32 + h0 + (lane & 1)) * 128), RLX_AGENT) == T + 1)) { ln2(pend, true); pend = -1; }
    wait_flag<PSL>(ws, (unsigned)OFF_OFL + (s * 32 + h0 + (lane & 1)) * 128, T, 1u);
    if (s == 63) TL(1);
    const int ob = (int)OFF_O + (s * 32 + h0) * 8192 + lane * 16;
    bf16x8 of[16];
#pragma unroll
    for (int f = 0; f < 16; ++f) of[f] = __builtin_bit_cast(bf16x8, __builtin_amdgcn_raw_buffer_load_b128(rs, ob + f * 1024, 0, 16));
    __builtin_amdgcn_sched_barrier(0);
#pragma unroll
    for (int f = 0; f < 16; ++f)
#pragma unroll
      for (int j = 0; j < 8; ++j)
        asm volatile("v_mfma_f32_16x16x32_bf16 %0, %1, %2, %0" : "+v"(acc[(f >> 2) & 1][j]) : "a"(wo[f >> 3][j * 4 + (f & 3)]), "v"(of[f]));
    asm volatile("s_nop 7\n\ts_nop 3" : "+v"(acc[1][4]), "+v"(acc[1][5]), "+v"(acc[1][6]), "+v"(acc[1][7]), "+v"(acc[1][0]), "+v"(acc[1][1]), "+v"(acc[1][2]), "+v"(acc[1][3]));
    __builtin_amdgcn_sched_barrier(0);
    if (s == 63) TL(3);
#pragma unroll
    for (int tt = 0; tt < 2; ++tt)
#pragma unroll
      for (int j = 0; j < 8; ++j) *(f32x4*)(lds + wv * RW + (tt * 16 + l15) * RS + j * 64 + g * 16) = acc[tt][j];
    __syncthreads();
#pragma unroll
    for (int i = 0; i < 4; ++i) {
      const int row = i * 8 + (tid >> 5);
      const char* p = lds + row * RS + (tid & 31) * 16;
      const f32x4 a = (*(const f32x4*)p + *(const f32x4*)(p + RW)) + (*(const f32x4*)(p + 2 * RW) + *(const f32x4*)(p + 3 * RW));
      store16_sc1(ws + OFF_HP, (unsigned)(((s * 4 + ks4) * 4 + cs) * 16384 + i * 4096 + tid * 16), __builtin_bit_cast(u32x4, a));
    }
    asm volatile("s_waitcnt vmcnt(0)" ::: "memory");
    __syncthreads();
    if (tid == 0) set_flag(ws, OFF_HFL + (s * 16 + ks4 * 4 + cs) * 128, T, 1);
    if (s == 63) TL(6);
    if (pend >= 0) ln2(pend, false);
    pend = wv < 2 ? s : -1;
  }
  if (pend >= 0) ln2(pend, true);
}

__device__ __forceinline__ unsigned rkey(unsigned w, int idx) { return ((w ^ (((int)w >> 31) | 0x80000000u)) & 0xffff0000u) | (unsigned)(0xffff - idx); }
__device__ __forceinline__ int key_row(unsigned k) { return (0xffff - (int)(k & 0xffffu)) & 2047; }
__device__ __forceinline__ float key_val(unsigned k) { k &= 0xffff0000u; return __builtin_bit_cast(float, (k & 0x80000000u) ? k ^ 0x80000000u : (~k) & 0xffff0000u); }
__device__ __forceinline__ unsigned wave_umax(unsigned v) {
  v = max(v, (unsigned)__builtin_amdgcn_update_dpp(0, (int)v, 0x121, 0xf, 0xf, false)); v = max(v, (unsigned)__builtin_amdgcn_update_dpp(0, (int)v, 0x122, 0xf, 0xf, false));
  v = max(v, (unsigned)__builtin_amdgcn_update_dpp(0, (int)v, 0x124, 0xf, 0xf, false)); v = max(v, (unsigned)__builtin_amdgcn_update_dpp(0, (int)v, 0x128, 0xf, 0xf, false));
  return max(max((unsigned)__builtin_amdgcn_readlane((int)v, 0), (unsigned)__builtin_amdgcn_readlane((int)v, 16)),
             max((unsigned)__builtin_amdgcn_readlane((int)v, 32), (unsigned)__builtin_amdgcn_readlane((int)v, 48)));
}
__device__ __forceinline__ void push2(unsigned& a, unsigned& b, unsigned k) { b = max(b, min(a, k)); a = max(a, k); }
__device__ __forceinline__ void wave_top2(unsigned& a, unsigned& b) {
  const unsigned m = wave_umax(a);
  b = wave_umax(a == m ? b : a); a = m;
}
__device__ __forceinline__ void top2(char* ws, u64 T, int nrows, int tid, unsigned& k0, unsigned& k1, unsigned* sk) {
  const __amdgpu_buffer_rsrc_t rs = __builtin_amdgcn_make_buffer_rsrc(ws, 0, (int)(164 * MBY), 0x00020000);
  const int lane = tid & 63, wv = tid >> 6;
  const bool use0 = tid * 4 < nrows, use1 = (256 + tid) * 4 < nrows;
  u32x4 c[2];
  unsigned spins = 0;
  if (wv == 0) { wait_flag<12>(ws, (unsigned)OFF_HFL + ((nrows >> 5) - 1) * 2048, T, 1u); __builtin_amdgcn_s_sleep(48); }
  __syncthreads();
  for (;;) {
    c[0] = __builtin_bit_cast(u32x4, __builtin_amdgcn_raw_buffer_load_b128(rs, (int)OFF_RC + (use0 ? tid : 0) * 128, 0, 16));
    c[1] = __builtin_bit_cast(u32x4, __builtin_amdgcn_raw_buffer_load_b128(rs, (int)OFF_RC + (use1 ? 256 + tid : 0) * 128, 0, 16));
    if (__all(c[0].x & c[0].y & c[0].z & c[0].w & c[1].x & c[1].y & c[1].z & c[1].w & 1u)) break;
    __builtin_amdgcn_s_sleep(8);
    if (spin_bail(spins, ws, T)) break;
    asm volatile("" ::: "memory");
  }
  unsigned a = 0, b = 0;
#pragma unroll
  for (int k = 0; k < 2; ++k)
#pragma unroll
    for (int e = 0; e < 4; ++e) { const unsigned w = c[k][e]; push2(a, b, (k == 0 ? use0 : use1) ? rkey(w, (k * 256 + tid) * 4 + e) : 0u); }
  wave_top2(a, b);
  if (lane == 0) { sk[wv * 2] = a; sk[wv * 2 + 1] = b; }
  __syncthreads();
  a = 0; b = 0;
#pragma unroll
  for (int i = 0; i < 8; ++i) push2(a, b, sk[i]);
  k0 = a; k1 = b;
}
__device__ __forceinline__ bool spec_holds(char* ws, u64 T, unsigned xcc, unsigned& k0, unsigned& k1, unsigned* sk, int tid) {
  static_assert(S_ - NSPEC <= 256, "one line per lane");
  if (tid < 64) {
    const __amdgpu_buffer_rsrc_t rs = __builtin_amdgcn_make_buffer_rsrc(ws, 0, (int)(164 * MBY), 0x00020000);
    const bool use = tid < (S_ - NSPEC) / 4;
    u32x4 c;
    unsigned spins = 0;
    for (;;) {
      c = __builtin_bit_cast(u32x4, __builtin_amdgcn_raw_buffer_load_b128(rs, (int)OFF_RC2 + (xcc * 64 + (use ? tid : 0)) * 128, 0, 16));
      if (__all(c.x & c.y & c.z & c.w & 1u)) break;
      __builtin_amdgcn_s_sleep(2);
      if (spin_bail(spins, ws, T)) break;
      asm volatile("" ::: "memory");
    }
    unsigned a = 0, b = 0;
#pragma unroll
    for (int e = 0; e < 4; ++e) { const unsigned w = c[e]; push2(a, b, use ? rkey(w, NSPEC + tid * 4 + e) : 0u); }
    wave_top2(a, b);
    push2(a, b, k0); push2(a, b, k1);
    if (tid == 0) { sk[8] = a; sk[9] = b; }
  }
  __syncthreads();
  const bool same = sk[8] == k0 && sk[9] == k1;
  k0 = sk[8]; k1 = sk[9];
  return same;
}
__device__ __forceinline__ void sel_rows(char* ws, const u16* __restrict__ x, u64 T, int tid, int i0, int i1, float* hs, float* ys, char* yb) {
  const __amdgpu_buffer_rsrc_t rs = __builtin_amdgcn_make_buffer_rsrc(ws, 0, (int)(164 * MBY), 0x00020000);
  const int lane = tid & 63, wv = tid >> 6;
  if (wv < 2) {
    const int row = wv ? i1 : i0;
    for (unsigned spins = 0; __hip_atomic_load((gu32*)(ws + OFF_RC + (row >> 2) * 128 + (row & 3) * 4), RLX_AGENT) == 0u;)
      if (spin_bail(spins, ws, T)) break;
    float f[8], y[8];
    load_h(rs, *(const u32x4*)(x + (size_t)row * DM + lane * 8), row, lane, f);
    ln_row(f, y);
#pragma unroll
    for (int i = 0; i < 8; ++i) { hs[wv * DM + lane * 8 + i] = f[i]; ys[wv * DM + lane * 8 + i] = y[i]; }
    u32x4 o; o.x = pk2(y[0], y[1]); o.y = pk2(y[2], y[3]); o.z = pk2(y[4], y[5]); o.w = pk2(y[6], y[7]);
    *(u32x4*)(yb + wv * 1040 + lane * 16) = o;
  }
  __syncthreads();
}
template <int NF>
__device__ __forceinline__ void load_frags(bf16x8 (&w)[NF], const u16* __restrict__ src, int pitch, char* st, int lane) {
  const int l15 = lane & 15, g = lane >> 4, qq = l15 >> 2;
#pragma unroll
  for (int b = 0; b < NF / 2; b += 16) {
    constexpr int NK = 16;
#pragma unroll
    for (int i = 0; i < NK * 2; ++i) {
      if (b * 2 + i < NF) {
        const int r = i * 16 + (lane >> 2);
        __builtin_amdgcn_global_load_lds((const GAS void*)(src + (size_t)(b * 32 + r) * pitch + (((lane & 3) ^ ((r >> 2) & 2)) * 8)), (LAS void*)(st + i * 1024), 16, 0, 0);
      }
    }
    asm volatile("s_waitcnt vmcnt(0)" ::: "memory");
#pragma unroll
    for (int ks = 0; ks < NK; ++ks)
#pragma unroll
      for (int j = 0; j < 2; ++j)
        if ((b + ks) * 2 < NF) {
          const char* p = st + (ks * 32 + g * 8 + qq) * 64 + ((j ^ (g & 1)) * 32) + (l15 & 3) * 8;
          w[(b + ks) * 2 + j] = tr_frag(p, p + 256);
        }
#pragma unroll
    for (int f = 0; f < NK * 2; ++f) if (b * 2 + f < NF) asm volatile("" : "+a"(w[b * 2 + f]));
    asm volatile("s_waitcnt lgkmcnt(0)" ::: "memory");
  }
}

__device__ __forceinline__ void mlp1_phase(char* ws, const u16* __restrict__ x, const u16* __restrict__ wg, const u16* __restrict__ wu, int m, u64 T, char* lds, int tid) {
  unsigned* sk = (unsigned*)lds; float* hs = (float*)(lds + 128); float* xs = hs + 2 * DM; char* yb = (char*)(xs + 2 * DM);
  const int lane = tid & 63, wv = __builtin_amdgcn_readfirstlane(tid >> 6), l15 = lane & 15, g = lane >> 4;
  bf16x8 a[32], b[32];
  load_frags<32>(a, wg + m * 128 + wv * 32, FF, lds + 16384 + wv * 32768, lane);
  load_frags<32>(b, wu + m * 128 + wv * 32, FF, lds + 16384 + wv * 32768, lane);
  unsigned xcc, k0, k1;
  asm volatile("s_getreg_b32 %0, hwreg(HW_REG_XCC_ID)" : "=s"(xcc));
  xcc &= 7u;
#pragma unroll 1
  for (int round = 1; round < 3; ++round) {
    if (round < 2) top2(ws, T, NSPEC, tid, k0, k1, sk);
    const int i0 = key_row(k0), i1 = key_row(k1);
    TL(1);
    sel_rows(ws, x, T, tid, i0, i1, hs, xs, yb);
    TL(3);
    bf16x8 xf[16];
#pragma unroll
    for (int ks = 0; ks < 16; ++ks) xf[ks] = *(const bf16x8*)(yb + (l15 & 1) * 1040 + ks * 64 + g * 16);
    f32x4 acc[4];
#pragma unroll
    for (int j = 0; j < 4; ++j) { acc[j] = (f32x4){0.f, 0.f, 0.f, 0.f}; asm volatile("" : "+v"(acc[j])); }
    asm volatile("s_nop 1" ::: "memory");
#pragma unroll
    for (int ks = 0; ks < 16; ++ks)
#pragma unroll
      for (int j = 0; j < 2; ++j) {
        asm volatile("v_mfma_f32_16x16x32_bf16 %0, %1, %2, %0" : "+v"(acc[j]) : "v"(xf[ks]), "a"(a[ks * 2 + j]));
        asm volatile("v_mfma_f32_16x16x32_bf16 %0, %1, %2, %0" : "+v"(acc[2 + j]) : "v"(xf[ks]), "a"(b[ks * 2 + j]));
      }
    asm volatile("s_nop 7\n\ts_nop 3" : "+v"(acc[0]), "+v"(acc[1]), "+v"(acc[2]), "+v"(acc[3]));
    {
      const int t = g >> 1, j = g & 1;
      const float g0 = j ? acc[1][0] : acc[0][0], g1 = j ? acc[1][1] : acc[0][1], u0 = j ? acc[3][0] : acc[2][0], u1 = j ? acc[3][1] : acc[2][1];
      const float gt = rbf(t ? g1 : g0), u = rbf(t ? u1 : u0);
      const float sg = rbf(gt / (1.0f + __expf(-gt)));
      const float av = sg * u;
      u16* ab = (u16*)(lds + 12288) + wv * 64;
      ab[lane] = (u16)pk2(av, av);
      __builtin_amdgcn_fence(__ATOMIC_RELEASE, "wavefront"); __builtin_amdgcn_wave_barrier(); __builtin_amdgcn_fence(__ATOMIC_ACQUIRE, "wavefront");
      store16_sc1(ws + OFF_ACT, (unsigned)(round * 65536 + (lane >> 3) * 8192 + (m * 4 + wv) * 128 + (lane & 7) * 16), *(const u32x4*)(ab + (lane & 7) * 8));
    }
    TL(6);
    if (round == 1 && spec_holds(ws, T, xcc, k0, k1, sk, tid)) break;
  }
}

__device__ __forceinline__ void mlp2_phase(char* ws, const u16* __restrict__ x, const u16* __restrict__ wd, u16* out, int m, u64 T, char* lds, int tid) {
  unsigned* sk = (unsigned*)lds; float* hs = (float*)(lds + 128); float* xs = hs + 2 * DM; char* yb = (char*)(xs + 2 * DM);
  float* part = (float*)(lds + 12288);
  const __amdgpu_buffer_rsrc_t rs = __builtin_amdgcn_make_buffer_rsrc(ws, 0, (int)(164 * MBY), 0x00020000);
  const int lane = tid & 63, wv = __builtin_amdgcn_readfirstlane(tid >> 6), l15 = lane & 15, g = lane >> 4;
  bf16x8 a[28];
  load_frags<28>(a, wd + (size_t)wv * 448 * DM + m * 32, DM, lds + 16384 + wv * 32768, lane);
  unsigned xcc, k0, k1;
  asm volatile("s_getreg_b32 %0, hwreg(HW_REG_XCC_ID)" : "=s"(xcc));
  xcc &= 7u;
#pragma unroll 1
  for (int round = 1; round < 3; ++round) {
    if (round < 2) top2(ws, T, NSPEC, tid, k0, k1, sk);
    const int i0 = key_row(k0), i1 = key_row(k1);
    TL(1);
    sel_rows(ws, x, T, tid, i0, i1, hs, xs, yb);
    TL(3);
    u32x4 xf[14];
    unsigned spins = 0;
    __builtin_amdgcn_s_sleep(ASL);
    for (;;) {
      bool ok = true;
#pragma unroll
      for (int ks = 0; ks < 14; ++ks) {
        xf[ks] = __builtin_bit_cast(u32x4, __builtin_amdgcn_raw_buffer_load_b128(rs, (int)OFF_ACT + round * 65536 + xcc * 8192 + (wv * 14 + ks) * 128 + (l15 & 1) * 64 + g * 16, 0, 16));
        ok = ok && xf[ks].x != SENT && xf[ks].y != SENT && xf[ks].z != SENT && xf[ks].w != SENT;
      }
      if (__all(ok)) break;
      __builtin_amdgcn_s_sleep(2);
      if (spin_bail(spins, ws, T)) break;
      asm volatile("" ::: "memory");
    }
    TL(7);
    f32x4 acc[2];
#pragma unroll
    for (int j = 0; j < 2; ++j) { acc[j] = (f32x4){0.f, 0.f, 0.f, 0.f}; asm volatile("" : "+v"(acc[j])); }
    asm volatile("s_nop 1" ::: "memory");
#pragma unroll
    for (int ks = 0; ks < 14; ++ks)
#pragma unroll
      for (int j = 0; j < 2; ++j) asm volatile("v_mfma_f32_16x16x32_bf16 %0, %1, %2, %0" : "+v"(acc[j]) : "v"(xf[ks]), "a"(a[ks * 2 + j]));
    asm volatile("s_nop 7\n\ts_nop 3" : "+v"(acc[0]), "+v"(acc[1]));
    {
      const float e0 = (g & 1) ? acc[1][0] : acc[0][0], e1 = (g & 1) ? acc[1][1] : acc[0][1];
      part[tid] = (g >> 1) ? e1 : e0;
    }
    __syncthreads();
    const int t = (tid >> 4) & 1, cc = (tid & 15) * 2, idx = t ? i1 : i0, col = m * 32 + cc;
    const float r = key_val(t ? k1 : k0);
    const float sg = rbf(1.0f / (1.0f + __expf(-r)));
    float o[2];
#pragma unroll
    for (int e = 0; e < 2; ++e) {
      const int q = t * 32 + cc + e;
      const float y = (part[q] + part[64 + q]) + (part[128 + q] + part[192 + q]);
      const float upd = rbf(rbf(y) * sg);
      o[e] = hs[t * DM + col + e] + rbf(xs[t * DM + col + e] + upd);
    }
    TL(4);
    gu32* op = (gu32*)(out + (size_t)idx * DM + col);
    if (tid < 32) __hip_atomic_store(op, pk2(o[0], o[1]), RLX_AGENT);
    if (round == 2 || spec_holds(ws, T, xcc, k0, k1, sk, tid)) break;
    if (tid < 32) __hip_atomic_store(op, pk2(hs[t * DM + col] + xs[t * DM + col], hs[t * DM + col + 1] + xs[t * DM + col + 1]), RLX_AGENT);
    asm volatile("s_waitcnt vmcnt(0)" ::: "memory");
    __syncthreads();
  }
}

__device__ __forceinline__ unsigned clk32() { return (unsigned)now_clk(); }
__device__ __forceinline__ unsigned clock_offset() {
  u64 c, r0, r1, r2, r3, r4, r5;
  asm volatile("s_memtime %0\n\ts_memrealtime %1\n\ts_nop 3\n\ts_memrealtime %2\n\ts_nop 3\n\ts_memrealtime %3\n\ts_nop 3\n\ts_memrealtime %4\n\ts_nop 3\n\t"
               "s_memrealtime %5\n\ts_nop 3\n\ts_memrealtime %6\n\ts_waitcnt lgkmcnt(0)"
               : "=&s"(c), "=&s"(r0), "=&s"(r1), "=&s"(r2), "=&s"(r3), "=&s"(r4), "=&s"(r5) :: "memory");
  const unsigned z = (unsigned)r0;
  return z * 24u - (unsigned)c - 20u * (unsigned)((z == (unsigned)r1) + (z == (unsigned)r2) + (z == (unsigned)r3) + (z == (unsigned)r4) + (z == (unsigned)r5));
}
__device__ __forceinline__ void wait_clk(unsigned target) {
  int rem = (int)(target - clk32()) - 60;
  rem = __builtin_amdgcn_readfirstlane(min(max(rem, 0), 8192));
  asm volatile("1:\n\ts_sub_u32 %0, %0, 28\n\ts_cbranch_scc0 1b" : "+s"(rem) :: "scc", "memory");
}
__device__ __forceinline__ void red_phase(char* ws, int b, u64 T, unsigned par, int lane) {
  const __amdgpu_buffer_rsrc_t rs = __builtin_amdgcn_make_buffer_rsrc(ws, 0, (int)(164 * MBY), 0x00020000);
  unsigned xcc; asm volatile("s_getreg_b32 %0, hwreg(HW_REG_XCC_ID)" : "=s"(xcc));
  const int g = lane >> 4;
  const unsigned near3 = ((par >> 16) & 1u) * 8192u;
  const unsigned own = (xcc & 3u) * 16384u + near3;
  const unsigned so = (((lane >> 3) & 1) + (lane >> 5) * 2) * 16384 + (b ^ ((b >> 1) & 1)) * 128 + (((((lane >> 1) & 2) | (g & 1)) ^ (b & 3)) * 32) + (lane & 3) * 8 + near3;
  const unsigned io = (unsigned)OFF_G2 + (b >> 1) * 16384 + (b & 1) * 4096 + lane * 16;
  const unsigned one = (lane & 1) ? 0x3f800000u : 0x3f80u;
  const bf16x8 sel = mk8((lane & 6) == 0 ? one : 0u, (lane & 6) == 2 ? one : 0u, (lane & 6) == 4 ? one : 0u, (lane & 6) == 6 ? one : 0u);
  unsigned spins = 0, off = 0, rd2 = RD2;
#pragma unroll 1
  for (int s = 0; s < NSEG - 1; ++s) {
    unsigned tgt;
    for (;;) {
      const u32x4 pv = __builtin_bit_cast(u32x4, __builtin_amdgcn_raw_buffer_load_b128(rs, (int)(OFF_PIL + (s & 3) * 65536 + own + lane * 64), 0, 16));
      off = clock_offset();
      const unsigned ref = __builtin_amdgcn_readfirstlane(pv.w);
      int d = (int)(pv.w - ref);
      d = max(d, __builtin_amdgcn_update_dpp(0, d, 0x121, 0xf, 0xf, false)); d = max(d, __builtin_amdgcn_update_dpp(0, d, 0x122, 0xf, 0xf, false));
      d = max(d, __builtin_amdgcn_update_dpp(0, d, 0x124, 0xf, 0xf, false)); d = max(d, __builtin_amdgcn_update_dpp(0, d, 0x128, 0xf, 0xf, false));
      d = max(max(__builtin_amdgcn_readlane(d, 0), __builtin_amdgcn_readlane(d, 16)), max(__builtin_amdgcn_readlane(d, 32), __builtin_amdgcn_readlane(d, 48)));
      tgt = ref + (unsigned)d + rd2 - off;
      if (__all(pv.x == (unsigned)T + 1u && pv.y == (unsigned)(T >> 32) && pv.z == (unsigned)s)) {
        break;
      } else __builtin_amdgcn_s_sleep(3);
      if (spin_bail(spins, ws, T)) break;
      asm volatile("" ::: "memory");
    }
    const int ib = (int)(io + s * 524288u + ((par >> (s >> 2)) & 1u) * 8192u);
    gu64* wp = (gu64*)(ws + OFF_G3 + ((s + 1) & 3) * 131072 + so);
    int done;
    {
      int rem;
      { const unsigned nw = clk32(); tgt = __builtin_amdgcn_readfirstlane(nw + (unsigned)min(max((int)(tgt - nw), 0), 8192)); }
      const char* wb = ws + OFF_G3 + ((s + 1) & 3) * 131072;
      asm volatile(
          "1:\n\ts_memtime s[92:93]\n\ts_waitcnt lgkmcnt(0)\n\ts_sub_i32 %1, %8, s92\n\ts_cmp_gt_i32 %1, 110\n\ts_cbranch_scc1 1b\n\t"
          "3:\n\ts_sub_i32 %1, %1, 32\n\ts_cmp_gt_i32 %1, 0\n\ts_cbranch_scc1 3b\n\t"
          "global_load_dwordx4 v[68:71], %2, %3 sc1\n\tglobal_load_dwordx4 v[72:75], %2, %3 offset:1024 sc1\n\t"
          "global_load_dwordx4 v[76:79], %2, %3 offset:2048 sc1\n\tglobal_load_dwordx4 v[80:83], %2, %3 offset:3072 sc1\n\t"
          "s_mov_b32 %0, 0\n\t"
          "s_waitcnt vmcnt(3)\n\tv_mfma_f32_16x16x32_bf16 v[64:67], %7, v[68:71], 0\n\t"
          "s_waitcnt vmcnt(2)\n\tv_mfma_f32_16x16x32_bf16 v[64:67], %7, v[72:75], v[64:67]\n\t"
          "s_waitcnt vmcnt(1)\n\tv_mfma_f32_16x16x32_bf16 v[64:67], %7, v[76:79], v[64:67]\n\t"
          "s_waitcnt vmcnt(0)\n\tv_mfma_f32_16x16x32_bf16 v[64:67], %7, v[80:83], v[64:67]\n\t"
          "s_nop 7\n\t"
          "v_add_f32_dpp v64, v64, v64 row_ror:8 row_mask:0xf bank_mask:0xf\n\tv_add_f32_dpp v65, v65, v65 row_ror:8 row_mask:0xf bank_mask:0xf\n\t"
          "v_add_f32_dpp v66, v66, v66 row_ror:8 row_mask:0xf bank_mask:0xf\n\tv_add_f32_dpp v67, v67, v67 row_ror:8 row_mask:0xf bank_mask:0xf\n\t"
          "v_cmp_u_f32 vcc, v64, v65\n\tv_cmp_u_f32 s[92:93], v66, v67\n\t"
          "v_cvt_pk_bf16_f32 v68, v64, v65\n\tv_cvt_pk_bf16_f32 v69, v66, v67\n\t"
          "s_or_b64 vcc, vcc, s[92:93]\n\ts_cbranch_scc1 2f\n\t"
          "global_store_dwordx2 %4, v[68:69], %6 sc1\n\t"
          "global_store_dwordx2 %5, v[68:69], %6 sc1\n\t"
          "s_mov_b32 %0, 1\n\t"
          "2:"
          : "=&s"(done), "=&s"(rem) : "v"(ib), "s"(ws), "v"(so), "v"(so + 65536u), "s"(wb), "v"(sel), "s"(tgt)
          : "v64", "v65", "v66", "v67", "v68", "v69", "v70", "v71", "v72", "v73", "v74", "v75", "v76", "v77", "v78", "v79", "v80", "v81", "v82", "v83",
            "v84", "v85", "v86", "v87", "v88", "v89", "v90", "v91", "v92", "v93", "v94", "v95", "s92", "s93", "s94", "s95", "vcc", "scc", "memory");
    }
    rd2 = done ? max(rd2 - 1u, (unsigned)RD2) : min(rd2 + RUP, RD2 + 160u);
    if (!done) {
    ++spins;
    unsigned lo, hi;
    for (unsigned tries = 1;; ++tries) {
      u32x4 v[4];
#pragma unroll
      for (int i = 0; i < 4; ++i) v[i] = __builtin_bit_cast(u32x4, __builtin_amdgcn_raw_buffer_load_b128(rs, ib + i * 1024, 0, 16));
      __builtin_amdgcn_sched_barrier(0);
      f32x4 a = (f32x4){0.f, 0.f, 0.f, 0.f};
#pragma unroll
      for (int i = 0; i < 4; ++i) a = __builtin_amdgcn_mfma_f32_16x16x32_bf16(sel, __builtin_bit_cast(bf16x8, v[i]), a, 0, 0, 0);
      float r[4];
#pragma unroll
      for (int e = 0; e < 4; ++e) { const float t = a[e]; r[e] = dpp_add<0x128>(t); }
      lo = pk2(r[0], r[1]); hi = pk2(r[2], r[3]);
      if (!__any(__builtin_isunordered(r[0], r[1]) || __builtin_isunordered(r[2], r[3]))) break;
      if ((tries & 15u) == 0u) {
        bool mark = false;
#pragma unroll
        for (int i = 0; i < 4; ++i) mark |= v[i].x == SENT || v[i].y == SENT || v[i].z == SENT || v[i].w == SENT;
        if (!__any(mark)) break;
      }
      if (spin_bail(spins, ws, T)) break;
      asm volatile("" ::: "memory");
    }
    __hip_atomic_store(wp, ((u64)hi << 32) | lo, RLX_AGENT);
    __hip_atomic_store(wp + 8192, ((u64)hi << 32) | lo, RLX_AGENT);
    }
    wait_clk(clk32() + RPP);
    {
      gu64* pp = (gu64*)(ws + OFF_G3 + ((s + 3) & 3) * 131072 + so);
      __hip_atomic_store(pp, ((u64)SENT << 32) | SENT, RLX_AGENT);
      __hip_atomic_store(pp + 8192, ((u64)SENT << 32) | SENT, RLX_AGENT);
    }
    wait_clk(clk32() + RPW);
  }
}

__global__ void __launch_bounds__(256, 1) rec_kernel(char* ws, const u16* __restrict__ x, const u16* __restrict__ mem0, const u16* __restrict__ Wq,
                                                     const u16* __restrict__ Wk, const u16* __restrict__ Wv, const u16* __restrict__ Wo,
                                                     const u16* __restrict__ w_r, const u16* __restrict__ wg, const u16* __restrict__ wu,
                                                     const u16* __restrict__ wd, u16* out) {
  __shared__ __attribute__((aligned(1024))) char lds[157696];
  const int tid = threadIdx.x, lane = tid & 63, wv = tid >> 6;
  TL(0);
  const u64 ep0 = *(const __attribute__((address_space(4))) u64*)(ws + OFF_EP);
  int role;
  {
    unsigned xcc; asm volatile("s_getreg_b32 %0, hwreg(HW_REG_XCC_ID)" : "=s"(xcc));
    role = (int)((blockIdx.x & ~7u) | ((xcc + XROT) & 7u));
    if (tid == 0) {
      const u64 T0 = tag_of(ep0);
      while (__hip_atomic_exchange((gu64*)(ws + OFF_CLM + role * 128), T0, RLX_AGENT) == T0) role = (role + 1) & 255;
      *(int*)(lds + 157680) = role;
    }
    __syncthreads();
    role = __builtin_amdgcn_readfirstlane(*(int*)(lds + 157680));
  }
  const int bx = role & 7, by = role >> 3;
  if (bx >= 4 || by >= 16) {
    const int xc = bx & 3;
    if (bx < 4) {
      const int i = (by - 16) * 4 + bx;
      if (i >= 47) {
        if (tid == 0) probe(ws, i - 47, tag_of(ep0));
        __syncthreads();
      }
      const u64 T = ln1_phase(ws, x, i, ep0, lds, tid);
      const int p = lane >> 4, q = lane & 3;
      unsigned lo = SENT, hi = SENT;
      if (wv == 0) {
        u16 el[4];
#pragma unroll
        for (int k = 0; k < 4; ++k) {
          u16 t = mem0[(2 * p + (k & 1)) * DM + i * 8 + (k >> 1) * 4 + q];
          if ((t & 0x7f80) == 0x7f80 && (t & 0x7f)) t |= 0x40;
          el[k] = t;
        }
        lo = el[0] | ((unsigned)el[1] << 16); hi = el[2] | ((unsigned)el[3] << 16);
      }
      const unsigned par = wait_par(ws, T, lane);
      gu64* gp = (gu64*)(ws + OFF_G3 + wv * 131072 + ((lane >> 2) & 3) * 16384 + ((par >> 16) & 1u) * 8192 + (i ^ ((i >> 1) & 1)) * 128 + ((p ^ (i & 3)) * 32) + q * 8);
      __hip_atomic_store(gp, ((u64)hi << 32) | lo, RLX_AGENT);
      __hip_atomic_store(gp + 8192, ((u64)hi << 32) | lo, RLX_AGENT);
      asm volatile("s_waitcnt vmcnt(0)" ::: "memory");
      __syncthreads();
      if (wv == 0) {
        if (lane == 0) set_flag(ws, OFF_TAB + i * 128, T, 1u);
        red_phase(ws, i, T, par, lane);
      }
    } else if (by < 8) {
      xa_phase(ws, by & 3, (xc * 2 + (by >> 2)) * 4 + wv, tag_of(ep0), lane);
    } else {
      const int jj = by - 8, h = xc * 8 + (jj & 7), m = (jj & 7) * 4 + xc;
      u64 T;
      if (jj < 8) T = qkv_phase<1>(ws, x, Wk, h, ep0, lds, tid);
      else if (jj < 16) T = qkv_phase<2>(ws, x, Wv, h, ep0, lds, tid);
      else T = qkv_phase<0>(ws, x, Wq, h, ep0, lds, tid);
      TL(2);
      op_phase(ws, x, w_r, out, Wo, jj >> 2, xc, jj & 3, T, lds, tid);
      if (jj >= 16) __syncthreads();
      if (jj < 16) {}
      else if (m < 14) mlp1_phase(ws, x, wg, wu, m, T, lds, tid);
      else if (m >= 16) {
        mlp2_phase(ws, x, wd, out, m - 16, T, lds, tid);
        if (m == 31 && tid == 0) __hip_atomic_store((gu64*)(ws + OFF_EP), T >> 6, RLX_AGENT);
      }
    }
    TL(5);
    return;
  }
  const int wave = __builtin_amdgcn_readfirstlane(wv);
  const unsigned ldsb = (unsigned)(size_t)(LAS char*)lds;
  asm volatile(
    "s_mov_b64 s[36:37], %0\n"
    "s_mov_b32 s38, %1\n"
    "s_mov_b32 s39, %2\n"
    "s_mov_b32 s40, %3\n"
    "s_load_dwordx2 s[96:97], %4, 0x0\n"
    "s_mov_b64 s[22:23], %5\n"
    "s_mov_b64 s[24:25], %6\n"
    "s_mov_b64 s[26:27], %7\n"
    "s_mov_b64 s[28:29], %8\n"
    "s_mov_b64 exec, -1\n"
    "s_mov_b32 s90, 0\n"
    "s_getreg_b32 s10, hwreg(HW_REG_XCC_ID)\n"
    "s_and_b32 s41, s38, 3\n"
    "s_lshl_b32 s41, s41, 3\n"
    "s_bfe_u32 s42, s38, 0x30002\n"
    "s_add_u32 s41, s41, s42\n"
    "s_lshr_b32 s42, s38, 5\n"
    "v_mbcnt_lo_u32_b32 v0, -1, 0\n"
    "v_mbcnt_hi_u32_b32 v0, -1, v0\n"
    "v_and_b32 v1, 15, v0\n"
    "v_lshrrev_b32 v2, 4, v0\n"
    "v_lshlrev_b32 v71, 4, v0\n"
    "s_and_b32 s11, s10, 7\n"
    "s_lshr_b32 s10, s10, 2\n"
    "s_and_b32 s10, s10, 1\n"
    "s_add_u32 s68, s36, 0x200200\n"
    "s_addc_u32 s69, s37, 0\n"
    "s_lshl_b32 s78, s39, 4\n"
    "v_add_u32 v8, s78, v2\n"
    "v_and_b32 v9, 1, v2\n"
    "v_lshlrev_b32 v9, 1, v9\n"
    "v_xor_b32 v9, v9, v1\n"
    "s_and_b32 s79, s39, 1\n"
    "s_lshl_b32 s79, s79, 3\n"
    "v_add_u32 v10, 0, v8\n"
    "v_xor_b32 v11, 0, v9\n"
    "v_lshlrev_b32 v11, 4, v11\n"
    "v_lshl_add_u32 v84, v10, 13, v11\n"
    "v_xor_b32 v11, 0, v9\n"
    "v_xor_b32 v11, s79, v11\n"
    "v_lshlrev_b32 v11, 4, v11\n"
    "v_lshl_add_u32 v88, v10, 10, v11\n"
    "v_add_u32 v10, 4, v8\n"
    "v_xor_b32 v11, 4, v9\n"
    "v_lshlrev_b32 v11, 4, v11\n"
    "v_lshl_add_u32 v85, v10, 13, v11\n"
    "v_xor_b32 v11, 4, v9\n"
    "v_xor_b32 v11, s79, v11\n"
    "v_lshlrev_b32 v11, 4, v11\n"
    "v_lshl_add_u32 v89, v10, 10, v11\n"
    "v_add_u32 v10, 8, v8\n"
    "v_xor_b32 v11, 8, v9\n"
    "v_lshlrev_b32 v11, 4, v11\n"
    "v_lshl_add_u32 v86, v10, 13, v11\n"
    "v_xor_b32 v11, 0, v9\n"
    "v_xor_b32 v11, s79, v11\n"
    "v_lshlrev_b32 v11, 4, v11\n"
    "v_lshl_add_u32 v90, v10, 10, v11\n"
    "v_add_u32 v10, 12, v8\n"
    "v_xor_b32 v11, 12, v9\n"
    "v_lshlrev_b32 v11, 4, v11\n"
    "v_lshl_add_u32 v87, v10, 13, v11\n"
    "v_xor_b32 v11, 4, v9\n"
    "v_xor_b32 v11, s79, v11\n"
    "v_lshlrev_b32 v11, 4, v11\n"
    "v_lshl_add_u32 v91, v10, 10, v11\n"
    "s_lshl_b32 s78, s41, 8\n"
    "s_add_u32 s22, s22, s78\n"
    "s_addc_u32 s23, s23, 0\n"
    "s_add_u32 s24, s24, s78\n"
    "s_addc_u32 s25, s25, 0\n"
    "s_add_u32 s26, s26, s78\n"
    "s_addc_u32 s27, s27, 0\n"
    "s_lshl_b32 s78, s41, 17\n"
    "s_lshl_b32 s79, s42, 9\n"
    "s_add_u32 s78, s78, s79\n"
    "s_add_u32 s28, s28, s78\n"
    "s_addc_u32 s29, s29, 0\n"
    "s_lshl_b32 s30, s39, 12\n"
    "s_add_u32 s30, s30, s40\n"
    "s_add_u32 m0, s30, 0\n"
    "s_nop 0\n"
    "global_load_lds_dwordx4 v88, s[28:29]\n"
    "s_add_u32 m0, s30, 1024\n"
    "s_nop 0\n"
    "global_load_lds_dwordx4 v89, s[28:29]\n"
    "s_add_u32 m0, s30, 2048\n"
    "s_nop 0\n"
    "global_load_lds_dwordx4 v90, s[28:29]\n"
    "s_add_u32 m0, s30, 3072\n"
    "s_nop 0\n"
    "global_load_lds_dwordx4 v91, s[28:29]\n"
    "s_add_u32 s28, s28, 256\n"
    "s_addc_u32 s29, s29, 0\n"
    "s_add_u32 m0, s30, 16384\n"
    "s_nop 0\n"
    "global_load_lds_dwordx4 v88, s[28:29]\n"
    "s_add_u32 m0, s30, 17408\n"
    "s_nop 0\n"
    "global_load_lds_dwordx4 v89, s[28:29]\n"
    "s_add_u32 m0, s30, 18432\n"
    "s_nop 0\n"
    "global_load_lds_dwordx4 v90, s[28:29]\n"
    "s_add_u32 m0, s30, 19456\n"
    "s_nop 0\n"
    "global_load_lds_dwordx4 v91, s[28:29]\n"
    "s_add_u32 s28, s28, 65280\n"
    "s_addc_u32 s29, s29, 0\n"
    "s_add_u32 m0, s30, 32768\n"
    "s_nop 0\n"
    "global_load_lds_dwordx4 v88, s[28:29]\n"
    "s_add_u32 m0, s30, 33792\n"
    "s_nop 0\n"
    "global_load_lds_dwordx4 v89, s[28:29]\n"
    "s_add_u32 m0, s30, 34816\n"
    "s_nop 0\n"
    "global_load_lds_dwordx4 v90, s[28:29]\n"
    "s_add_u32 m0, s30, 35840\n"
    "s_nop 0\n"
    "global_load_lds_dwordx4 v91, s[28:29]\n"
    "s_add_u32 s28, s28, 256\n"
    "s_addc_u32 s29, s29, 0\n"
    "s_add_u32 m0, s30, 49152\n"
    "s_nop 0\n"
    "global_load_lds_dwordx4 v88, s[28:29]\n"
    "s_add_u32 m0, s30, 50176\n"
    "s_nop 0\n"
    "global_load_lds_dwordx4 v89, s[28:29]\n"
    "s_add_u32 m0, s30, 51200\n"
    "s_nop 0\n"
    "global_load_lds_dwordx4 v90, s[28:29]\n"
    "s_add_u32 m0, s30, 52224\n"
    "s_nop 0\n"
    "global_load_lds_dwordx4 v91, s[28:29]\n"
    "s_add_u32 s28, s28, 65280\n"
    "s_addc_u32 s29, s29, 0\n"
    "s_add_u32 m0, s30, 65536\n"
    "s_nop 0\n"
    "global_load_lds_dwordx4 v84, s[24:25]\n"
    "s_add_u32 m0, s30, 66560\n"
    "s_nop 0\n"
    "global_load_lds_dwordx4 v85, s[24:25]\n"
    "s_add_u32 m0, s30, 67584\n"
    "s_nop 0\n"
    "global_load_lds_dwordx4 v86, s[24:25]\n"
    "s_add_u32 m0, s30, 68608\n"
    "s_nop 0\n"
    "global_load_lds_dwordx4 v87, s[24:25]\n"
    "s_add_u32 s24, s24, 0x80000\n"
    "s_addc_u32 s25, s25, 0\n"
    "s_add_u32 m0, s30, 81920\n"
    "s_nop 0\n"
    "global_load_lds_dwordx4 v84, s[24:25]\n"
    "s_add_u32 m0, s30, 82944\n"
    "s_nop 0\n"
    "global_load_lds_dwordx4 v85, s[24:25]\n"
    "s_add_u32 m0, s30, 83968\n"
    "s_nop 0\n"
    "global_load_lds_dwordx4 v86, s[24:25]\n"
    "s_add_u32 m0, s30, 84992\n"
    "s_nop 0\n"
    "global_load_lds_dwordx4 v87, s[24:25]\n"
    "s_add_u32 s24, s24, 0x80000\n"
    "s_addc_u32 s25, s25, 0\n"
    "s_add_u32 m0, s30, 98304\n"
    "s_nop 0\n"
    "global_load_lds_dwordx4 v84, s[24:25]\n"
    "s_add_u32 m0, s30, 99328\n"
    "s_nop 0\n"
    "global_load_lds_dwordx4 v85, s[24:25]\n"
    "s_add_u32 m0, s30, 100352\n"
    "s_nop 0\n"
    "global_load_lds_dwordx4 v86, s[24:25]\n"
    "s_add_u32 m0, s30, 101376\n"
    "s_nop 0\n"
    "global_load_lds_dwordx4 v87, s[24:25]\n"
    "s_add_u32 s24, s24, 0x80000\n"
    "s_addc_u32 s25, s25, 0\n"
    "v_lshrrev_b32 v8, 2, v1\n"
    "v_and_b32 v9, 1, v8\n"
    "v_lshrrev_b32 v10, 1, v8\n"
    "v_and_b32 v11, 1, v2\n"
    "v_and_b32 v12, 3, v1\n"
    "v_lshl_add_u32 v13, v2, 3, v9\n"
    "v_lshl_add_u32 v13, v10, 2, v13\n"
    "v_lshl_add_u32 v14, v11, 2, v8\n"
    "s_lshl_b32 s78, s39, 1\n"
    "v_xor_b32 v14, s78, v14\n"
    "v_lshlrev_b32 v14, 5, v14\n"
    "v_lshl_add_u32 v14, v13, 8, v14\n"
    "v_lshl_add_u32 v14, v12, 3, v14\n"
    "v_xor_b32 v15, 32, v14\n"
    "v_add_u32 v92, s40, v14\n"
    "v_add_u32 v93, s40, v15\n"
    "v_add_u32 v94, 0x10000, v92\n"
    "v_add_u32 v95, 0x10000, v93\n"
    "v_lshl_add_u32 v13, v2, 2, v9\n"
    "v_lshl_add_u32 v13, v10, 4, v13\n"
    "v_lshl_add_u32 v14, v11, 1, v9\n"
    "v_lshl_add_u32 v14, v10, 2, v14\n"
    "v_lshlrev_b32 v14, 1, v14\n"
    "v_xor_b32 v14, v14, v12\n"
    "s_and_b32 s78, s39, 1\n"
    "s_lshl_b32 s78, s78, 3\n"
    "v_xor_b32 v14, s78, v14\n"
    "v_lshlrev_b32 v14, 4, v14\n"
    "v_lshl_add_u32 v14, v13, 8, v14\n"
    "s_lshr_b32 s78, s39, 1\n"
    "s_lshl_b32 s78, s78, 14\n"
    "s_add_u32 s78, s78, s40\n"
    "v_xor_b32 v15, 64, v14\n"
    "v_add_u32 v104, s78, v14\n"
    "v_add_u32 v105, s78, v15\n"
    "s_mul_i32 s78, s39, 34816\n"
    "s_add_u32 s78, s78, s40\n"
    "v_add_u32 v119, s78, v71\n"
    "s_mov_b32 s70, 0xcccccccc\n"
    "s_mov_b32 s71, 0xcccccccc\n"
    "s_mov_b32 s72, 0x33333333\n"
    "s_mov_b32 s73, 0x0\n"
    "s_mov_b32 s74, 0x0\n"
    "s_mov_b32 s75, 0x33333333\n"
    "v_mov_b32 v9, 0x4444\n"
    "v_mov_b32 v10, 0xeeee\n"
    "v_cndmask_b32 v67, v9, v10, s[70:71]\n"
    "s_mov_b32 s76, 0x3e0293ee\n"
    "s_mov_b32 s77, 0x3e0293ee\n"
    "v_and_b32 v8, 1, v1\n"
    "v_lshlrev_b32 v8, 4, v8\n"
    "v_mov_b32 v9, 0x3f80\n"
    "v_lshlrev_b32 v9, v8, v9\n"
    "v_lshrrev_b32 v10, 2, v1\n"
    "v_cmp_eq_u32 vcc, v10, v2\n"
    "s_nop 1\n"
    "v_cndmask_b32 v96, 0, v9, vcc\n"
    "v_cndmask_b32 v99, 0, v9, vcc\n"
    "v_lshrrev_b32 v11, 3, v1\n"
    "v_and_b32 v10, 1, v10\n"
    "v_lshl_add_u32 v10, v10, 1, v11\n"
    "v_cmp_eq_u32 vcc, v10, v2\n"
    "s_nop 1\n"
    "v_cndmask_b32 v100, 0, v9, vcc\n"
    "v_cndmask_b32 v103, 0, v9, vcc\n"
    "v_mov_b32 v97, 0\n"
    "v_mov_b32 v98, 0\n"
    "v_mov_b32 v101, 0\n"
    "v_mov_b32 v102, 0\n"
    "v_mov_b32 v80, 0x3f803f80\n"
    "v_mov_b32 v81, 0x3f803f80\n"
    "v_mov_b32 v82, 0x3f803f80\n"
    "v_mov_b32 v83, 0x3f803f80\n"
    "v_lshrrev_b32 v8, 2, v1\n"
    "v_lshlrev_b32 v8, 3, v8\n"
    "v_lshl_add_u32 v8, v2, 8, v8\n"
    "s_add_u32 s80, s40, 0x22000\n"
    "v_add_u32 v8, s80, v8\n"
    "v_and_b32 v9, 1, v2\n"
    "v_lshlrev_b32 v10, 1, v9\n"
    "v_and_b32 v11, 3, v1\n"
    "v_xor_b32 v10, v10, v11\n"
    "v_lshl_add_u32 v13, v10, 5, v8\n"
    "v_lshl_add_u32 v112, v9, 7, v13\n"
    "v_xor_b32 v10, 1, v10\n"
    "v_xor_b32 v9, 1, v9\n"
    "v_lshl_add_u32 v13, v10, 5, v8\n"
    "v_lshl_add_u32 v113, v9, 7, v13\n"
    "s_add_u32 s80, s40, 0x24000\n"
    "v_add_u32 v8, s80, v71\n"
    "s_lshl_b32 s78, s39, 11\n"
    "v_add_u32 v116, s78, v8\n"
    "s_xor_b32 s79, s78, 0x800\n"
    "v_add_u32 v117, s79, v8\n"
    "s_xor_b32 s79, s78, 0x1000\n"
    "s_andn2_b32 s79, s79, 0x800\n"
    "v_add_u32 v122, s79, v8\n"
    "v_lshrrev_b32 v8, 2, v1\n"
    "v_lshrrev_b32 v9, 3, v1\n"
    "v_xor_b32 v9, v9, v8\n"
    "v_lshlrev_b32 v9, 7, v9\n"
    "v_xor_b32 v8, v8, v2\n"
    "v_lshl_add_u32 v9, v8, 5, v9\n"
    "v_and_b32 v10, 3, v1\n"
    "v_lshl_add_u32 v9, v10, 3, v9\n"
    "s_lshl_b32 s78, s39, 9\n"
    "s_add_u32 s78, s78, s40\n"
    "s_add_u32 s78, s78, 0x26000\n"
    "v_add_u32 v118, s78, v9\n"
    "s_lshl_b32 s78, s39, 11\n"
    "v_add_u32 v75, s78, v71\n"
    "s_lshl_b32 s78, s39, 10\n"
    "v_add_u32 v79, s78, v71\n"
    "v_lshrrev_b32 v79, s42, v79\n"
    "s_add_u32 s62, s36, 0x63e0000\n"
    "s_addc_u32 s63, s37, 0\n"
    "s_lshl_b32 s78, s42, 23\n"
    "s_add_u32 s62, s62, s78\n"
    "s_addc_u32 s63, s63, 0\n"
    "s_lshl_b32 s78, s41, 12\n"
    "s_add_u32 s62, s62, s78\n"
    "s_addc_u32 s63, s63, 0\n"
    "s_lshl_b32 s78, s41, 13\n"
    "s_add_u32 s58, s36, 0x1500000\n"
    "s_addc_u32 s59, s37, 0\n"
    "s_add_u32 s58, s58, s78\n"
    "s_addc_u32 s59, s59, 0\n"
    "s_add_u32 s60, s36, 0x2500000\n"
    "s_addc_u32 s61, s37, 0\n"
    "s_add_u32 s60, s60, s78\n"
    "s_addc_u32 s61, s61, 0\n"
    "s_add_u32 s12, s36, 0x80000\n"
    "s_addc_u32 s13, s37, 0\n"
    "s_add_u32 s66, s36, 0x202000\n"
    "s_addc_u32 s67, s37, 0\n"
    "s_lshl_b32 s78, s41, 7\n"
    "s_add_u32 s66, s66, s78\n"
    "s_addc_u32 s67, s67, 0\n"
    "s_add_u32 s44, s36, 0x7400000\n"
    "s_addc_u32 s45, s37, 0\n"
    "s_waitcnt lgkmcnt(0)\n"
    "s_add_u32 s96, s96, 1\n"
    "s_addc_u32 s97, s97, 0\n"
    "s_lshl_b64 s[96:97], s[96:97], 6\n"
    "s_waitcnt vmcnt(24) lgkmcnt(0)\n"
    "s_barrier\n"
    "s_add_u32 m0, s30, 114688\n"
    "s_nop 0\n"
    "global_load_lds_dwordx4 v84, s[24:25]\n"
    "s_add_u32 m0, s30, 115712\n"
    "s_nop 0\n"
    "global_load_lds_dwordx4 v85, s[24:25]\n"
    "s_add_u32 m0, s30, 116736\n"
    "s_nop 0\n"
    "global_load_lds_dwordx4 v86, s[24:25]\n"
    "s_add_u32 m0, s30, 117760\n"
    "s_nop 0\n"
    "global_load_lds_dwordx4 v87, s[24:25]\n"
    "s_add_u32 s24, s24, 0x80000\n"
    "s_addc_u32 s25, s25, 0\n"
    "s_waitcnt vmcnt(24) lgkmcnt(0)\n"
    "s_barrier\n"
    "ds_read_b64_tr_b16 v[0:1], v104 offset:0\n"
    "ds_read_b64_tr_b16 v[2:3], v104 offset:512\n"
    "ds_read_b64_tr_b16 v[4:5], v104 offset:8192\n"
    "ds_read_b64_tr_b16 v[6:7], v104 offset:8704\n"
    "ds_read_b64_tr_b16 v[16:17], v104 offset:8\n"
    "ds_read_b64_tr_b16 v[18:19], v104 offset:520\n"
    "ds_read_b64_tr_b16 v[20:21], v104 offset:8200\n"
    "ds_read_b64_tr_b16 v[22:23], v104 offset:8712\n"
    "s_waitcnt lgkmcnt(4)\n"
    "ds_read_b64_tr_b16 v[32:33], v105 offset:0\n"
    "ds_read_b64_tr_b16 v[34:35], v105 offset:512\n"
    "ds_read_b64_tr_b16 v[36:37], v105 offset:8192\n"
    "ds_read_b64_tr_b16 v[38:39], v105 offset:8704\n"
    "ds_read_b64_tr_b16 v[48:49], v105 offset:8\n"
    "ds_read_b64_tr_b16 v[50:51], v105 offset:520\n"
    "ds_read_b64_tr_b16 v[52:53], v105 offset:8200\n"
    "ds_read_b64_tr_b16 v[54:55], v105 offset:8712\n"
    "s_waitcnt vmcnt(20) lgkmcnt(0)\n"
    "s_barrier\n"
    "s_add_u32 m0, s30, 0\n"
    "s_nop 0\n"
    "global_load_lds_dwordx4 v84, s[24:25]\n"
    "s_add_u32 m0, s30, 1024\n"
    "s_nop 0\n"
    "global_load_lds_dwordx4 v85, s[24:25]\n"
    "s_add_u32 m0, s30, 2048\n"
    "s_nop 0\n"
    "global_load_lds_dwordx4 v86, s[24:25]\n"
    "s_add_u32 m0, s30, 3072\n"
    "s_nop 0\n"
    "global_load_lds_dwordx4 v87, s[24:25]\n"
    "s_add_u32 s24, s24, 0x80000\n"
    "s_addc_u32 s25, s25, 0\n"
    "s_add_u32 m0, s30, 16384\n"
    "s_nop 0\n"
    "global_load_lds_dwordx4 v84, s[24:25]\n"
    "s_add_u32 m0, s30, 17408\n"
    "s_nop 0\n"
    "global_load_lds_dwordx4 v85, s[24:25]\n"
    "s_add_u32 m0, s30, 18432\n"
    "s_nop 0\n"
    "global_load_lds_dwordx4 v86, s[24:25]\n"
    "s_add_u32 m0, s30, 19456\n"
    "s_nop 0\n"
    "global_load_lds_dwordx4 v87, s[24:25]\n"
    "s_add_u32 s24, s24, 0x80000\n"
    "s_addc_u32 s25, s25, 0\n"
    "s_waitcnt vmcnt(24) lgkmcnt(0)\n"
    "s_barrier\n"
    "ds_read_b64_tr_b16 v[8:9], v104 offset:32768\n"
    "ds_read_b64_tr_b16 v[10:11], v104 offset:33280\n"
    "ds_read_b64_tr_b16 v[12:13], v104 offset:40960\n"
    "ds_read_b64_tr_b16 v[14:15], v104 offset:41472\n"
    "ds_read_b64_tr_b16 v[24:25], v104 offset:32776\n"
    "ds_read_b64_tr_b16 v[26:27], v104 offset:33288\n"
    "ds_read_b64_tr_b16 v[28:29], v104 offset:40968\n"
    "ds_read_b64_tr_b16 v[30:31], v104 offset:41480\n"
    "s_waitcnt lgkmcnt(4)\n"
    "ds_read_b64_tr_b16 v[40:41], v105 offset:32768\n"
    "ds_read_b64_tr_b16 v[42:43], v105 offset:33280\n"
    "ds_read_b64_tr_b16 v[44:45], v105 offset:40960\n"
    "ds_read_b64_tr_b16 v[46:47], v105 offset:41472\n"
    "ds_read_b64_tr_b16 v[56:57], v105 offset:32776\n"
    "ds_read_b64_tr_b16 v[58:59], v105 offset:33288\n"
    "ds_read_b64_tr_b16 v[60:61], v105 offset:40968\n"
    "ds_read_b64_tr_b16 v[62:63], v105 offset:41480\n"
    "s_waitcnt vmcnt(20) lgkmcnt(0)\n"
    "s_barrier\n"
    "s_add_u32 m0, s30, 32768\n"
    "s_nop 0\n"
    "global_load_lds_dwordx4 v84, s[24:25]\n"
    "s_add_u32 m0, s30, 33792\n"
    "s_nop 0\n"
    "global_load_lds_dwordx4 v85, s[24:25]\n"
    "s_add_u32 m0, s30, 34816\n"
    "s_nop 0\n"
    "global_load_lds_dwordx4 v86, s[24:25]\n"
    "s_add_u32 m0, s30, 35840\n"
    "s_nop 0\n"
    "global_load_lds_dwordx4 v87, s[24:25]\n"
    "s_add_u32 s24, s24, 0x80000\n"
    "s_addc_u32 s25, s25, 0\n"
    "s_add_u32 m0, s30, 49152\n"
    "s_nop 0\n"
    "global_load_lds_dwordx4 v84, s[24:25]\n"
    "s_add_u32 m0, s30, 50176\n"
    "s_nop 0\n"
    "global_load_lds_dwordx4 v85, s[24:25]\n"
    "s_add_u32 m0, s30, 51200\n"
    "s_nop 0\n"
    "global_load_lds_dwordx4 v86, s[24:25]\n"
    "s_add_u32 m0, s30, 52224\n"
    "s_nop 0\n"
    "global_load_lds_dwordx4 v87, s[24:25]\n"
    "s_add_u32 s24, s24, 0x80000\n"
    "s_addc_u32 s25, s25, 0\n"
    "ds_read_b64_tr_b16 a[0:1], v94 offset:0\n"
    "ds_read_b64_tr_b16 a[2:3], v94 offset:512\n"
    "ds_read_b64_tr_b16 a[4:5], v94 offset:8192\n"
    "ds_read_b64_tr_b16 a[6:7], v94 offset:8704\n"
    "ds_read_b64_tr_b16 a[64:65], v95 offset:0\n"
    "ds_read_b64_tr_b16 a[66:67], v95 offset:512\n"
    "ds_read_b64_tr_b16 a[68:69], v95 offset:8192\n"
    "ds_read_b64_tr_b16 a[70:71], v95 offset:8704\n"
    "s_waitcnt vmcnt(24) lgkmcnt(0)\n"
    "s_barrier\n"
    "s_add_u32 m0, s30, 65536\n"
    "s_nop 0\n"
    "global_load_lds_dwordx4 v84, s[26:27]\n"
    "s_add_u32 m0, s30, 66560\n"
    "s_nop 0\n"
    "global_load_lds_dwordx4 v85, s[26:27]\n"
    "s_add_u32 m0, s30, 67584\n"
    "s_nop 0\n"
    "global_load_lds_dwordx4 v86, s[26:27]\n"
    "s_add_u32 m0, s30, 68608\n"
    "s_nop 0\n"
    "global_load_lds_dwordx4 v87, s[26:27]\n"
    "s_add_u32 s26, s26, 0x80000\n"
    "s_addc_u32 s27, s27, 0\n"
    "ds_read_b64_tr_b16 a[8:9], v94 offset:16384\n"
    "ds_read_b64_tr_b16 a[10:11], v94 offset:16896\n"
    "ds_read_b64_tr_b16 a[12:13], v94 offset:24576\n"
    "ds_read_b64_tr_b16 a[14:15], v94 offset:25088\n"
    "ds_read_b64_tr_b16 a[72:73], v95 offset:16384\n"
    "ds_read_b64_tr_b16 a[74:75], v95 offset:16896\n"
    "ds_read_b64_tr_b16 a[76:77], v95 offset:24576\n"
    "ds_read_b64_tr_b16 a[78:79], v95 offset:25088\n"
    "s_waitcnt vmcnt(24) lgkmcnt(0)\n"
    "s_barrier\n"
    "s_add_u32 m0, s30, 81920\n"
    "s_nop 0\n"
    "global_load_lds_dwordx4 v84, s[26:27]\n"
    "s_add_u32 m0, s30, 82944\n"
    "s_nop 0\n"
    "global_load_lds_dwordx4 v85, s[26:27]\n"
    "s_add_u32 m0, s30, 83968\n"
    "s_nop 0\n"
    "global_load_lds_dwordx4 v86, s[26:27]\n"
    "s_add_u32 m0, s30, 84992\n"
    "s_nop 0\n"
    "global_load_lds_dwordx4 v87, s[26:27]\n"
    "s_add_u32 s26, s26, 0x80000\n"
    "s_addc_u32 s27, s27, 0\n"
    "ds_read_b64_tr_b16 a[16:17], v94 offset:32768\n"
    "ds_read_b64_tr_b16 a[18:19], v94 offset:33280\n"
    "ds_read_b64_tr_b16 a[20:21], v94 offset:40960\n"
    "ds_read_b64_tr_b16 a[22:23], v94 offset:41472\n"
    "ds_read_b64_tr_b16 a[80:81], v95 offset:32768\n"
    "ds_read_b64_tr_b16 a[82:83], v95 offset:33280\n"
    "ds_read_b64_tr_b16 a[84:85], v95 offset:40960\n"
    "ds_read_b64_tr_b16 a[86:87], v95 offset:41472\n"
    "s_waitcnt vmcnt(24) lgkmcnt(0)\n"
    "s_barrier\n"
    "s_add_u32 m0, s30, 98304\n"
    "s_nop 0\n"
    "global_load_lds_dwordx4 v84, s[26:27]\n"
    "s_add_u32 m0, s30, 99328\n"
    "s_nop 0\n"
    "global_load_lds_dwordx4 v85, s[26:27]\n"
    "s_add_u32 m0, s30, 100352\n"
    "s_nop 0\n"
    "global_load_lds_dwordx4 v86, s[26:27]\n"
    "s_add_u32 m0, s30, 101376\n"
    "s_nop 0\n"
    "global_load_lds_dwordx4 v87, s[26:27]\n"
    "s_add_u32 s26, s26, 0x80000\n"
    "s_addc_u32 s27, s27, 0\n"
    "ds_read_b64_tr_b16 a[24:25], v94 offset:49152\n"
    "ds_read_b64_tr_b16 a[26:27], v94 offset:49664\n"
    "ds_read_b64_tr_b16 a[28:29], v94 offset:57344\n"
    "ds_read_b64_tr_b16 a[30:31], v94 offset:57856\n"
    "ds_read_b64_tr_b16 a[88:89], v95 offset:49152\n"
    "ds_read_b64_tr_b16 a[90:91], v95 offset:49664\n"
    "ds_read_b64_tr_b16 a[92:93], v95 offset:57344\n"
    "ds_read_b64_tr_b16 a[94:95], v95 offset:57856\n"
    "s_waitcnt vmcnt(24) lgkmcnt(0)\n"
    "s_barrier\n"
    "s_add_u32 m0, s30, 114688\n"
    "s_nop 0\n"
    "global_load_lds_dwordx4 v84, s[26:27]\n"
    "s_add_u32 m0, s30, 115712\n"
    "s_nop 0\n"
    "global_load_lds_dwordx4 v85, s[26:27]\n"
    "s_add_u32 m0, s30, 116736\n"
    "s_nop 0\n"
    "global_load_lds_dwordx4 v86, s[26:27]\n"
    "s_add_u32 m0, s30, 117760\n"
    "s_nop 0\n"
    "global_load_lds_dwordx4 v87, s[26:27]\n"
    "s_add_u32 s26, s26, 0x80000\n"
    "s_addc_u32 s27, s27, 0\n"
    "ds_read_b64_tr_b16 a[32:33], v92 offset:0\n"
    "ds_read_b64_tr_b16 a[34:35], v92 offset:512\n"
    "ds_read_b64_tr_b16 a[36:37], v92 offset:8192\n"
    "ds_read_b64_tr_b16 a[38:39], v92 offset:8704\n"
    "ds_read_b64_tr_b16 a[96:97], v93 offset:0\n"
    "ds_read_b64_tr_b16 a[98:99], v93 offset:512\n"
    "ds_read_b64_tr_b16 a[100:101], v93 offset:8192\n"
    "ds_read_b64_tr_b16 a[102:103], v93 offset:8704\n"
    "s_waitcnt vmcnt(24) lgkmcnt(0)\n"
    "s_barrier\n"
    "s_add_u32 m0, s30, 0\n"
    "s_nop 0\n"
    "global_load_lds_dwordx4 v84, s[26:27]\n"
    "s_add_u32 m0, s30, 1024\n"
    "s_nop 0\n"
    "global_load_lds_dwordx4 v85, s[26:27]\n"
    "s_add_u32 m0, s30, 2048\n"
    "s_nop 0\n"
    "global_load_lds_dwordx4 v86, s[26:27]\n"
    "s_add_u32 m0, s30, 3072\n"
    "s_nop 0\n"
    "global_load_lds_dwordx4 v87, s[26:27]\n"
    "s_add_u32 s26, s26, 0x80000\n"
    "s_addc_u32 s27, s27, 0\n"
    "ds_read_b64_tr_b16 a[40:41], v92 offset:16384\n"
    "ds_read_b64_tr_b16 a[42:43], v92 offset:16896\n"
    "ds_read_b64_tr_b16 a[44:45], v92 offset:24576\n"
    "ds_read_b64_tr_b16 a[46:47], v92 offset:25088\n"
    "ds_read_b64_tr_b16 a[104:105], v93 offset:16384\n"
    "ds_read_b64_tr_b16 a[106:107], v93 offset:16896\n"
    "ds_read_b64_tr_b16 a[108:109], v93 offset:24576\n"
    "ds_read_b64_tr_b16 a[110:111], v93 offset:25088\n"
    "s_waitcnt vmcnt(24) lgkmcnt(0)\n"
    "s_barrier\n"
    "s_add_u32 m0, s30, 16384\n"
    "s_nop 0\n"
    "global_load_lds_dwordx4 v84, s[26:27]\n"
    "s_add_u32 m0, s30, 17408\n"
    "s_nop 0\n"
    "global_load_lds_dwordx4 v85, s[26:27]\n"
    "s_add_u32 m0, s30, 18432\n"
    "s_nop 0\n"
    "global_load_lds_dwordx4 v86, s[26:27]\n"
    "s_add_u32 m0, s30, 19456\n"
    "s_nop 0\n"
    "global_load_lds_dwordx4 v87, s[26:27]\n"
    "s_add_u32 s26, s26, 0x80000\n"
    "s_addc_u32 s27, s27, 0\n"
    "ds_read_b64_tr_b16 a[48:49], v92 offset:32768\n"
    "ds_read_b64_tr_b16 a[50:51], v92 offset:33280\n"
    "ds_read_b64_tr_b16 a[52:53], v92 offset:40960\n"
    "ds_read_b64_tr_b16 a[54:55], v92 offset:41472\n"
    "ds_read_b64_tr_b16 a[112:113], v93 offset:32768\n"
    "ds_read_b64_tr_b16 a[114:115], v93 offset:33280\n"
    "ds_read_b64_tr_b16 a[116:117], v93 offset:40960\n"
    "ds_read_b64_tr_b16 a[118:119], v93 offset:41472\n"
    "s_waitcnt vmcnt(24) lgkmcnt(0)\n"
    "s_barrier\n"
    "s_add_u32 m0, s30, 32768\n"
    "s_nop 0\n"
    "global_load_lds_dwordx4 v84, s[26:27]\n"
    "s_add_u32 m0, s30, 33792\n"
    "s_nop 0\n"
    "global_load_lds_dwordx4 v85, s[26:27]\n"
    "s_add_u32 m0, s30, 34816\n"
    "s_nop 0\n"
    "global_load_lds_dwordx4 v86, s[26:27]\n"
    "s_add_u32 m0, s30, 35840\n"
    "s_nop 0\n"
    "global_load_lds_dwordx4 v87, s[26:27]\n"
    "s_add_u32 s26, s26, 0x80000\n"
    "s_addc_u32 s27, s27, 0\n"
    "ds_read_b64_tr_b16 a[56:57], v92 offset:49152\n"
    "ds_read_b64_tr_b16 a[58:59], v92 offset:49664\n"
    "ds_read_b64_tr_b16 a[60:61], v92 offset:57344\n"
    "ds_read_b64_tr_b16 a[62:63], v92 offset:57856\n"
    "ds_read_b64_tr_b16 a[120:121], v93 offset:49152\n"
    "ds_read_b64_tr_b16 a[122:123], v93 offset:49664\n"
    "ds_read_b64_tr_b16 a[124:125], v93 offset:57344\n"
    "ds_read_b64_tr_b16 a[126:127], v93 offset:57856\n"
    "s_waitcnt vmcnt(24) lgkmcnt(0)\n"
    "s_barrier\n"
    "s_add_u32 m0, s30, 49152\n"
    "s_nop 0\n"
    "global_load_lds_dwordx4 v84, s[26:27]\n"
    "s_add_u32 m0, s30, 50176\n"
    "s_nop 0\n"
    "global_load_lds_dwordx4 v85, s[26:27]\n"
    "s_add_u32 m0, s30, 51200\n"
    "s_nop 0\n"
    "global_load_lds_dwordx4 v86, s[26:27]\n"
    "s_add_u32 m0, s30, 52224\n"
    "s_nop 0\n"
    "global_load_lds_dwordx4 v87, s[26:27]\n"
    "s_add_u32 s26, s26, 0x80000\n"
    "s_addc_u32 s27, s27, 0\n"
    "ds_read_b64_tr_b16 a[128:129], v94 offset:0\n"
    "ds_read_b64_tr_b16 a[130:131], v94 offset:512\n"
    "ds_read_b64_tr_b16 a[132:133], v94 offset:8192\n"
    "ds_read_b64_tr_b16 a[134:135], v94 offset:8704\n"
    "ds_read_b64_tr_b16 a[192:193], v95 offset:0\n"
    "ds_read_b64_tr_b16 a[194:195], v95 offset:512\n"
    "ds_read_b64_tr_b16 a[196:197], v95 offset:8192\n"
    "ds_read_b64_tr_b16 a[198:199], v95 offset:8704\n"
    "s_waitcnt vmcnt(24) lgkmcnt(0)\n"
    "s_barrier\n"
    "s_add_u32 m0, s30, 65536\n"
    "s_nop 0\n"
    "global_load_lds_dwordx4 v84, s[22:23]\n"
    "s_add_u32 m0, s30, 66560\n"
    "s_nop 0\n"
    "global_load_lds_dwordx4 v85, s[22:23]\n"
    "s_add_u32 m0, s30, 67584\n"
    "s_nop 0\n"
    "global_load_lds_dwordx4 v86, s[22:23]\n"
    "s_add_u32 m0, s30, 68608\n"
    "s_nop 0\n"
    "global_load_lds_dwordx4 v87, s[22:23]\n"
    "s_add_u32 s22, s22, 0x80000\n"
    "s_addc_u32 s23, s23, 0\n"
    "ds_read_b64_tr_b16 a[136:137], v94 offset:16384\n"
    "ds_read_b64_tr_b16 a[138:139], v94 offset:16896\n"
    "ds_read_b64_tr_b16 a[140:141], v94 offset:24576\n"
    "ds_read_b64_tr_b16 a[142:143], v94 offset:25088\n"
    "ds_read_b64_tr_b16 a[200:201], v95 offset:16384\n"
    "ds_read_b64_tr_b16 a[202:203], v95 offset:16896\n"
    "ds_read_b64_tr_b16 a[204:205], v95 offset:24576\n"
    "ds_read_b64_tr_b16 a[206:207], v95 offset:25088\n"
    "s_waitcnt vmcnt(24) lgkmcnt(0)\n"
    "s_barrier\n"
    "s_add_u32 m0, s30, 81920\n"
    "s_nop 0\n"
    "global_load_lds_dwordx4 v84, s[22:23]\n"
    "s_add_u32 m0, s30, 82944\n"
    "s_nop 0\n"
    "global_load_lds_dwordx4 v85, s[22:23]\n"
    "s_add_u32 m0, s30, 83968\n"
    "s_nop 0\n"
    "global_load_lds_dwordx4 v86, s[22:23]\n"
    "s_add_u32 m0, s30, 84992\n"
    "s_nop 0\n"
    "global_load_lds_dwordx4 v87, s[22:23]\n"
    "s_add_u32 s22, s22, 0x80000\n"
    "s_addc_u32 s23, s23, 0\n"
    "ds_read_b64_tr_b16 a[144:145], v94 offset:32768\n"
    "ds_read_b64_tr_b16 a[146:147], v94 offset:33280\n"
    "ds_read_b64_tr_b16 a[148:149], v94 offset:40960\n"
    "ds_read_b64_tr_b16 a[150:151], v94 offset:41472\n"
    "ds_read_b64_tr_b16 a[208:209], v95 offset:32768\n"
    "ds_read_b64_tr_b16 a[210:211], v95 offset:33280\n"
    "ds_read_b64_tr_b16 a[212:213], v95 offset:40960\n"
    "ds_read_b64_tr_b16 a[214:215], v95 offset:41472\n"
    "s_waitcnt vmcnt(24) lgkmcnt(0)\n"
    "s_barrier\n"
    "s_add_u32 m0, s30, 98304\n"
    "s_nop 0\n"
    "global_load_lds_dwordx4 v84, s[22:23]\n"
    "s_add_u32 m0, s30, 99328\n"
    "s_nop 0\n"
    "global_load_lds_dwordx4 v85, s[22:23]\n"
    "s_add_u32 m0, s30, 100352\n"
    "s_nop 0\n"
    "global_load_lds_dwordx4 v86, s[22:23]\n"
    "s_add_u32 m0, s30, 101376\n"
    "s_nop 0\n"
    "global_load_lds_dwordx4 v87, s[22:23]\n"
    "s_add_u32 s22, s22, 0x80000\n"
    "s_addc_u32 s23, s23, 0\n"
    "ds_read_b64_tr_b16 a[152:153], v94 offset:49152\n"
    "ds_read_b64_tr_b16 a[154:155], v94 offset:49664\n"
    "ds_read_b64_tr_b16 a[156:157], v94 offset:57344\n"
    "ds_read_b64_tr_b16 a[158:159], v94 offset:57856\n"
    "ds_read_b64_tr_b16 a[216:217], v95 offset:49152\n"
    "ds_read_b64_tr_b16 a[218:219], v95 offset:49664\n"
    "ds_read_b64_tr_b16 a[220:221], v95 offset:57344\n"
    "ds_read_b64_tr_b16 a[222:223], v95 offset:57856\n"
    "s_waitcnt vmcnt(24) lgkmcnt(0)\n"
    "s_barrier\n"
    "s_add_u32 m0, s30, 114688\n"
    "s_nop 0\n"
    "global_load_lds_dwordx4 v84, s[22:23]\n"
    "s_add_u32 m0, s30, 115712\n"
    "s_nop 0\n"
    "global_load_lds_dwordx4 v85, s[22:23]\n"
    "s_add_u32 m0, s30, 116736\n"
    "s_nop 0\n"
    "global_load_lds_dwordx4 v86, s[22:23]\n"
    "s_add_u32 m0, s30, 117760\n"
    "s_nop 0\n"
    "global_load_lds_dwordx4 v87, s[22:23]\n"
    "s_add_u32 s22, s22, 0x80000\n"
    "s_addc_u32 s23, s23, 0\n"
    "ds_read_b64_tr_b16 a[160:161], v92 offset:0\n"
    "ds_read_b64_tr_b16 a[162:163], v92 offset:512\n"
    "ds_read_b64_tr_b16 a[164:165], v92 offset:8192\n"
    "ds_read_b64_tr_b16 a[166:167], v92 offset:8704\n"
    "ds_read_b64_tr_b16 a[224:225], v93 offset:0\n"
    "ds_read_b64_tr_b16 a[226:227], v93 offset:512\n"
    "ds_read_b64_tr_b16 a[228:229], v93 offset:8192\n"
    "ds_read_b64_tr_b16 a[230:231], v93 offset:8704\n"
    "s_waitcnt vmcnt(24) lgkmcnt(0)\n"
    "s_barrier\n"
    "s_add_u32 m0, s30, 0\n"
    "s_nop 0\n"
    "global_load_lds_dwordx4 v84, s[22:23]\n"
    "s_add_u32 m0, s30, 1024\n"
    "s_nop 0\n"
    "global_load_lds_dwordx4 v85, s[22:23]\n"
    "s_add_u32 m0, s30, 2048\n"
    "s_nop 0\n"
    "global_load_lds_dwordx4 v86, s[22:23]\n"
    "s_add_u32 m0, s30, 3072\n"
    "s_nop 0\n"
    "global_load_lds_dwordx4 v87, s[22:23]\n"
    "s_add_u32 s22, s22, 0x80000\n"
    "s_addc_u32 s23, s23, 0\n"
    "ds_read_b64_tr_b16 a[168:169], v92 offset:16384\n"
    "ds_read_b64_tr_b16 a[170:171], v92 offset:16896\n"
    "ds_read_b64_tr_b16 a[172:173], v92 offset:24576\n"
    "ds_read_b64_tr_b16 a[174:175], v92 offset:25088\n"
    "ds_read_b64_tr_b16 a[232:233], v93 offset:16384\n"
    "ds_read_b64_tr_b16 a[234:235], v93 offset:16896\n"
    "ds_read_b64_tr_b16 a[236:237], v93 offset:24576\n"
    "ds_read_b64_tr_b16 a[238:239], v93 offset:25088\n"
    "s_waitcnt vmcnt(24) lgkmcnt(0)\n"
    "s_barrier\n"
    "s_add_u32 m0, s30, 16384\n"
    "s_nop 0\n"
    "global_load_lds_dwordx4 v84, s[22:23]\n"
    "s_add_u32 m0, s30, 17408\n"
    "s_nop 0\n"
    "global_load_lds_dwordx4 v85, s[22:23]\n"
    "s_add_u32 m0, s30, 18432\n"
    "s_nop 0\n"
    "global_load_lds_dwordx4 v86, s[22:23]\n"
    "s_add_u32 m0, s30, 19456\n"
    "s_nop 0\n"
    "global_load_lds_dwordx4 v87, s[22:23]\n"
    "s_add_u32 s22, s22, 0x80000\n"
    "s_addc_u32 s23, s23, 0\n"
    "ds_read_b64_tr_b16 a[176:177], v92 offset:32768\n"
    "ds_read_b64_tr_b16 a[178:179], v92 offset:33280\n"
    "ds_read_b64_tr_b16 a[180:181], v92 offset:40960\n"
    "ds_read_b64_tr_b16 a[182:183], v92 offset:41472\n"
    "ds_read_b64_tr_b16 a[240:241], v93 offset:32768\n"
    "ds_read_b64_tr_b16 a[242:243], v93 offset:33280\n"
    "ds_read_b64_tr_b16 a[244:245], v93 offset:40960\n"
    "ds_read_b64_tr_b16 a[246:247], v93 offset:41472\n"
    "s_waitcnt vmcnt(24) lgkmcnt(0)\n"
    "s_barrier\n"
    "s_add_u32 m0, s30, 32768\n"
    "s_nop 0\n"
    "global_load_lds_dwordx4 v84, s[22:23]\n"
    "s_add_u32 m0, s30, 33792\n"
    "s_nop 0\n"
    "global_load_lds_dwordx4 v85, s[22:23]\n"
    "s_add_u32 m0, s30, 34816\n"
    "s_nop 0\n"
    "global_load_lds_dwordx4 v86, s[22:23]\n"
    "s_add_u32 m0, s30, 35840\n"
    "s_nop 0\n"
    "global_load_lds_dwordx4 v87, s[22:23]\n"
    "s_add_u32 s22, s22, 0x80000\n"
    "s_addc_u32 s23, s23, 0\n"
    "ds_read_b64_tr_b16 a[184:185], v92 offset:49152\n"
    "ds_read_b64_tr_b16 a[186:187], v92 offset:49664\n"
    "ds_read_b64_tr_b16 a[188:189], v92 offset:57344\n"
    "ds_read_b64_tr_b16 a[190:191], v92 offset:57856\n"
    "ds_read_b64_tr_b16 a[248:249], v93 offset:49152\n"
    "ds_read_b64_tr_b16 a[250:251], v93 offset:49664\n"
    "ds_read_b64_tr_b16 a[252:253], v93 offset:57344\n"
    "ds_read_b64_tr_b16 a[254:255], v93 offset:57856\n"
    "s_waitcnt vmcnt(24) lgkmcnt(0)\n"
    "s_barrier\n"
    "s_add_u32 m0, s30, 49152\n"
    "s_nop 0\n"
    "global_load_lds_dwordx4 v84, s[22:23]\n"
    "s_add_u32 m0, s30, 50176\n"
    "s_nop 0\n"
    "global_load_lds_dwordx4 v85, s[22:23]\n"
    "s_add_u32 m0, s30, 51200\n"
    "s_nop 0\n"
    "global_load_lds_dwordx4 v86, s[22:23]\n"
    "s_add_u32 m0, s30, 52224\n"
    "s_nop 0\n"
    "global_load_lds_dwordx4 v87, s[22:23]\n"
    "s_add_u32 s22, s22, 0x80000\n"
    "s_addc_u32 s23, s23, 0\n"
    "ds_read_b64_tr_b16 v[184:185], v94 offset:0\n"
    "ds_read_b64_tr_b16 v[186:187], v94 offset:512\n"
    "ds_read_b64_tr_b16 v[188:189], v94 offset:8192\n"
    "ds_read_b64_tr_b16 v[190:191], v94 offset:8704\n"
    "ds_read_b64_tr_b16 v[192:193], v95 offset:0\n"
    "ds_read_b64_tr_b16 v[194:195], v95 offset:512\n"
    "ds_read_b64_tr_b16 v[196:197], v95 offset:8192\n"
    "ds_read_b64_tr_b16 v[198:199], v95 offset:8704\n"
    "s_waitcnt vmcnt(24) lgkmcnt(0)\n"
    "s_barrier\n"
    "ds_read_b64_tr_b16 v[200:201], v94 offset:16384\n"
    "ds_read_b64_tr_b16 v[202:203], v94 offset:16896\n"
    "ds_read_b64_tr_b16 v[204:205], v94 offset:24576\n"
    "ds_read_b64_tr_b16 v[206:207], v94 offset:25088\n"
    "ds_read_b64_tr_b16 v[208:209], v95 offset:16384\n"
    "ds_read_b64_tr_b16 v[210:211], v95 offset:16896\n"
    "ds_read_b64_tr_b16 v[212:213], v95 offset:24576\n"
    "ds_read_b64_tr_b16 v[214:215], v95 offset:25088\n"
    "s_waitcnt vmcnt(20) lgkmcnt(0)\n"
    "s_barrier\n"
    "ds_read_b64_tr_b16 v[216:217], v94 offset:32768\n"
    "ds_read_b64_tr_b16 v[218:219], v94 offset:33280\n"
    "ds_read_b64_tr_b16 v[220:221], v94 offset:40960\n"
    "ds_read_b64_tr_b16 v[222:223], v94 offset:41472\n"
    "ds_read_b64_tr_b16 v[224:225], v95 offset:32768\n"
    "ds_read_b64_tr_b16 v[226:227], v95 offset:33280\n"
    "ds_read_b64_tr_b16 v[228:229], v95 offset:40960\n"
    "ds_read_b64_tr_b16 v[230:231], v95 offset:41472\n"
    "s_waitcnt vmcnt(16) lgkmcnt(0)\n"
    "s_barrier\n"
    "ds_read_b64_tr_b16 v[232:233], v94 offset:49152\n"
    "ds_read_b64_tr_b16 v[234:235], v94 offset:49664\n"
    "ds_read_b64_tr_b16 v[236:237], v94 offset:57344\n"
    "ds_read_b64_tr_b16 v[238:239], v94 offset:57856\n"
    "ds_read_b64_tr_b16 v[240:241], v95 offset:49152\n"
    "ds_read_b64_tr_b16 v[242:243], v95 offset:49664\n"
    "ds_read_b64_tr_b16 v[244:245], v95 offset:57344\n"
    "ds_read_b64_tr_b16 v[246:247], v95 offset:57856\n"
    "s_waitcnt vmcnt(12) lgkmcnt(0)\n"
    "s_barrier\n"
    "ds_read_b64_tr_b16 v[248:249], v92 offset:0\n"
    "ds_read_b64_tr_b16 v[250:251], v92 offset:512\n"
    "ds_read_b64_tr_b16 v[252:253], v92 offset:8192\n"
    "ds_read_b64_tr_b16 v[254:255], v92 offset:8704\n"
    "ds_read_b64_tr_b16 v[128:129], v93 offset:0\n"
    "ds_read_b64_tr_b16 v[130:131], v93 offset:512\n"
    "ds_read_b64_tr_b16 v[132:133], v93 offset:8192\n"
    "ds_read_b64_tr_b16 v[134:135], v93 offset:8704\n"
    "s_waitcnt vmcnt(8) lgkmcnt(0)\n"
    "s_barrier\n"
    "v_lshlrev_b32 v88, 3, v71\n"
    "v_min_u32 v89, 0x800, v88\n"
    "s_add_u32 s82, s36, 0x20c000\n"
    "s_addc_u32 s83, s37, 0\n"
    "s_add_u32 s84, s36, 0x312000\n"
    "s_addc_u32 s85, s37, 0\n"
    "v_and_b32 v126, 16, v71\n"
    "v_lshlrev_b32 v126, 8, v126\n"
    "global_load_dwordx2 v[84:85], v88, s[82:83] sc1\n"
    "global_load_dwordx2 v[86:87], v89, s[84:85] sc1\n"
    "global_load_dwordx2 v[124:125], v126, s[66:67] sc1\n"
    "ds_read_b64_tr_b16 v[136:137], v92 offset:16384\n"
    "ds_read_b64_tr_b16 v[138:139], v92 offset:16896\n"
    "ds_read_b64_tr_b16 v[140:141], v92 offset:24576\n"
    "ds_read_b64_tr_b16 v[142:143], v92 offset:25088\n"
    "ds_read_b64_tr_b16 v[144:145], v93 offset:16384\n"
    "ds_read_b64_tr_b16 v[146:147], v93 offset:16896\n"
    "ds_read_b64_tr_b16 v[148:149], v93 offset:24576\n"
    "ds_read_b64_tr_b16 v[150:151], v93 offset:25088\n"
    "s_waitcnt vmcnt(7) lgkmcnt(0)\n"
    "s_barrier\n"
    "ds_read_b64_tr_b16 v[152:153], v92 offset:32768\n"
    "ds_read_b64_tr_b16 v[154:155], v92 offset:33280\n"
    "ds_read_b64_tr_b16 v[156:157], v92 offset:40960\n"
    "ds_read_b64_tr_b16 v[158:159], v92 offset:41472\n"
    "ds_read_b64_tr_b16 v[160:161], v93 offset:32768\n"
    "ds_read_b64_tr_b16 v[162:163], v93 offset:33280\n"
    "ds_read_b64_tr_b16 v[164:165], v93 offset:40960\n"
    "ds_read_b64_tr_b16 v[166:167], v93 offset:41472\n"
    "s_waitcnt vmcnt(3) lgkmcnt(0)\n"
    "s_barrier\n"
    "ds_read_b64_tr_b16 v[168:169], v92 offset:49152\n"
    "ds_read_b64_tr_b16 v[170:171], v92 offset:49664\n"
    "ds_read_b64_tr_b16 v[172:173], v92 offset:57344\n"
    "ds_read_b64_tr_b16 v[174:175], v92 offset:57856\n"
    "ds_read_b64_tr_b16 v[176:177], v93 offset:49152\n"
    "ds_read_b64_tr_b16 v[178:179], v93 offset:49664\n"
    "ds_read_b64_tr_b16 v[180:181], v93 offset:57344\n"
    "ds_read_b64_tr_b16 v[182:183], v93 offset:57856\n"
    "s_waitcnt vmcnt(0) lgkmcnt(0)\n"
    "s_barrier\n"
    "v_xor_b32 v124, s96, v124\n"
    "v_xor_b32 v125, s97, v125\n"
    "v_cmp_gt_u64 vcc, 64, v[124:125]\n"
    "s_nop 1\n"
    "v_cndmask_b32 v124, 0, v124, vcc\n"
    "s_nop 0\n"
    "v_readlane_b32 s80, v124, 0\n"
    "v_readlane_b32 s81, v124, 1\n"
    "ds_write_b128 v119, v[60:63] offset:15360\n"
    "ds_write_b128 v119, v[56:59] offset:14336\n"
    "ds_write_b128 v119, v[52:55] offset:13312\n"
    "ds_write_b128 v119, v[48:51] offset:12288\n"
    "ds_write_b128 v119, v[44:47] offset:11264\n"
    "ds_write_b128 v119, v[40:43] offset:10240\n"
    "ds_write_b128 v119, v[36:39] offset:9216\n"
    "ds_write_b128 v119, v[32:35] offset:8192\n"
    "s_waitcnt lgkmcnt(4)\n"
    "ds_write_b128 v119, v[28:31] offset:7168\n"
    "ds_write_b128 v119, v[24:27] offset:6144\n"
    "ds_write_b128 v119, v[20:23] offset:5120\n"
    "ds_write_b128 v119, v[16:19] offset:4096\n"
    "ds_write_b128 v119, v[12:15] offset:3072\n"
    "ds_write_b128 v119, v[8:11] offset:2048\n"
    "ds_write_b128 v119, v[4:7] offset:1024\n"
    "ds_write_b128 v119, v[0:3] offset:0\n"
    "s_waitcnt lgkmcnt(4)\n"
    "s_min_u32 s92, s80, s81\n"
    "s_mov_b32 s43, 0\n"
    "s_mov_b32 s19, 0\n"
    "s_cmp_eq_u32 s92, 0\n"
    "s_cbranch_scc1 L_nokv%=\n"
    "global_load_dwordx4 v[56:59], v75, s[58:59] offset:0 sc1\n"
    "global_load_dwordx4 v[60:63], v75, s[58:59] offset:1024 sc1\n"
    "global_load_dwordx2 v[64:65], v75, s[60:61] offset:0 sc1\n"
    "global_load_dwordx2 v[68:69], v75, s[60:61] offset:8 sc1\n"
    "global_load_dwordx2 v[72:73], v75, s[60:61] offset:1024 sc1\n"
    "global_load_dwordx2 v[76:77], v75, s[60:61] offset:1032 sc1\n"
    "s_mov_b32 s19, 1\n"
    "L_nokv%=:\n"
    "ds_write_b128 v119, v[184:187] offset:16384\n"
    "ds_write_b128 v119, v[188:191] offset:17408\n"
    "ds_write_b128 v119, v[192:195] offset:18432\n"
    "ds_write_b128 v119, v[196:199] offset:19456\n"
    "ds_write_b128 v119, v[200:203] offset:20480\n"
    "ds_write_b128 v119, v[204:207] offset:21504\n"
    "ds_write_b128 v119, v[208:211] offset:22528\n"
    "ds_write_b128 v119, v[212:215] offset:23552\n"
    "s_waitcnt lgkmcnt(4)\n"
    "ds_write_b128 v119, v[216:219] offset:24576\n"
    "ds_write_b128 v119, v[220:223] offset:25600\n"
    "ds_write_b128 v119, v[224:227] offset:26624\n"
    "ds_write_b128 v119, v[228:231] offset:27648\n"
    "ds_write_b128 v119, v[232:235] offset:28672\n"
    "ds_write_b128 v119, v[236:239] offset:29696\n"
    "ds_write_b128 v119, v[240:243] offset:30720\n"
    "ds_write_b128 v119, v[244:247] offset:31744\n"
    "s_waitcnt lgkmcnt(4)\n"
    "ds_write_b128 v119, v[248:251] offset:32768\n"
    "ds_write_b128 v119, v[252:255] offset:33792\n"
    "v_lshrrev_b32 v0, 4, v71\n"
    "v_and_b32 v1, 15, v0\n"
    "v_lshrrev_b32 v2, 4, v0\n"
    "s_branch L_tabc%=\n"
    "L_tab%=:\n"
    "global_load_dwordx2 v[84:85], v88, s[82:83] sc1\n"
    "global_load_dwordx2 v[86:87], v89, s[84:85] sc1\n"
    "s_waitcnt vmcnt(0)\n"
    "L_tabc%=:\n"
    "v_xor_b32 v84, s96, v84\n"
    "v_xor_b32 v85, s97, v85\n"
    "v_cmp_gt_u64 vcc, 64, v[84:85]\n"
    "s_nop 1\n"
    "v_cndmask_b32 v84, 0, v84, vcc\n"
    "v_xor_b32 v86, s96, v86\n"
    "v_xor_b32 v87, s97, v87\n"
    "v_cmp_gt_u64 vcc, 64, v[86:87]\n"
    "s_nop 1\n"
    "v_cndmask_b32 v86, 0, v86, vcc\n"
    "v_min_u32 v9, v84, v86\n"
    "v_cmp_eq_u32 vcc, 0, v9\n"
    "s_nop 0\n"
    "s_cbranch_vccz L_tabok%=\n"
    "s_sleep 2\n"
    "s_add_u32 s90, s90, 1\n"
    "s_and_b32 s78, s90, 0xff\n"
    "s_cmp_lg_u32 s78, 0\n"
    "s_cbranch_scc1 L_tab%=\n"
    "v_mov_b32 v126, 0\n"
    "global_load_dwordx2 v[126:127], v126, s[68:69] sc1\n"
    "s_waitcnt vmcnt(0)\n"
    "v_cmp_eq_u64 vcc, s[96:97], v[126:127]\n"
    "s_nop 1\n"
    "s_cbranch_vccnz L_bail_tab%=\n"
    "s_cmp_lt_u32 s90, 0x40000\n"
    "s_cbranch_scc1 L_tab%=\n"
    "v_mov_b32 v126, s96\n"
    "v_mov_b32 v127, s97\n"
    "s_mov_b64 exec, 1\n"
    "global_store_dwordx2 v71, v[126:127], s[68:69] sc1\n"
    "s_mov_b64 exec, -1\n"
    "L_bail_tab%=:\n"
    "L_tabok%=:\n"
    "v_cmp_eq_u32 s[80:81], 2, v84\n"
    "v_cmp_eq_u32 vcc, 2, v86\n"
    "s_nop 0\n"
    "s_mov_b32 s93, vcc_lo\n"
    "s_lshl_b32 s78, s42, 4\n"
    "s_lshl_b32 s79, s39, 2\n"
    "s_add_u32 s78, s78, s79\n"
    "v_and_b32 v8, 1, v2\n"
    "v_lshrrev_b32 v9, 3, v1\n"
    "v_lshl_add_u32 v8, v8, 1, v9\n"
    "v_add_u32 v8, s78, v8\n"
    "v_lshlrev_b32 v10, 14, v8\n"
    "v_xor_b32 v8, s41, v8\n"
    "v_lshl_add_u32 v10, v8, 7, v10\n"
    "v_and_b32 v8, 4, v1\n"
    "v_lshl_add_u32 v10, v8, 10, v10\n"
    "v_lshrrev_b32 v8, 1, v2\n"
    "v_lshl_add_u32 v10, v8, 6, v10\n"
    "v_and_b32 v8, 3, v1\n"
    "v_lshl_add_u32 v10, v8, 4, v10\n"
    "s_and_b32 s78, s93, 1\n"
    "s_lshl_b32 s78, s78, 13\n"
    "v_add_u32 v120, s78, v10\n"
    "s_lshl_b32 s78, s42, 5\n"
    "s_add_u32 s78, s78, s41\n"
    "s_lshl_b32 s78, s78, 7\n"
    "s_add_u32 s64, s36, 0x220000\n"
    "s_addc_u32 s65, s37, 0\n"
    "s_add_u32 s64, s64, s78\n"
    "s_addc_u32 s65, s65, 0\n"
    "s_cmp_eq_u32 s39, 0\n"
    "s_cselect_b32 s52, 1, 15\n"
    "s_mov_b32 s53, 0\n"
    "s_add_u32 s80, s40, 0x22000\n"
    "v_add_u32 v114, s80, v75\n"
    "s_lshr_b32 s81, s93, 16\n"
    "s_and_b32 s81, s81, 1\n"
    "s_lshl_b32 s81, s81, 13\n"
    "s_and_b32 s79, s11, 3\n"
    "s_lshl_b32 s78, s42, 2\n"
    "s_add_u32 s79, s79, s78\n"
    "s_lshl_b32 s79, s79, 14\n"
    "s_add_u32 s78, s81, s79\n"
    "s_add_u32 s54, s36, 0x0\n"
    "s_addc_u32 s55, s37, 0\n"
    "s_add_u32 s54, s54, s78\n"
    "s_addc_u32 s55, s55, 0\n"
    "s_sub_u32 s54, s54, s80\n"
    "s_subb_u32 s55, s55, 0\n"
    "s_mov_b64 s[48:49], s[54:55]\n"
    "s_lshl_b32 s79, s38, 6\n"
    "s_add_u32 s79, s79, s81\n"
    "s_add_u32 s56, s12, s79\n"
    "s_addc_u32 s57, s13, 0\n"
    "s_and_b32 s78, s78, 0xffff\n"
    "s_add_u32 s12, s12, s78\n"
    "s_addc_u32 s13, s13, 0\n"
    "v_lshlrev_b32 v115, 10, v71\n"
    "v_lshlrev_b32 v121, 2, v71\n"
    "s_mov_b32 s43, 0\n"
    "v_mov_b64 v[104:105], 0\n"
    "v_mov_b64 v[106:107], 0\n"
    "v_mov_b64 v[108:109], 0\n"
    "v_mov_b64 v[110:111], 0\n"
    "v_mov_b64 v[32:33], 0\n"
    "v_mov_b64 v[34:35], 0\n"
    "v_mov_b64 v[36:37], 0\n"
    "v_mov_b64 v[38:39], 0\n"
    "v_mov_b64 v[40:41], 0\n"
    "v_mov_b64 v[42:43], 0\n"
    "v_mov_b64 v[44:45], 0\n"
    "v_mov_b64 v[46:47], 0\n"
    "v_mov_b64 v[48:49], 0\n"
    "v_mov_b64 v[50:51], 0\n"
    "v_mov_b64 v[52:53], 0\n"
    "v_mov_b64 v[54:55], 0\n"
    "s_cmp_lg_u32 s19, 0\n"
    "s_cbranch_scc1 L_kvdone%=\n"
    "s_add_u32 s79, s43, 1\n"
    "s_min_u32 s79, s79, 63\n"
    "s_lshr_b32 s79, s79, 1\n"
    "s_cmp_lt_u32 s79, s92\n"
    "s_cbranch_scc1 L_cntokp%=\n"
    "L_cntp%=:\n"
    "v_and_b32 v126, 16, v71\n"
    "v_lshlrev_b32 v126, 8, v126\n"
    "global_load_dwordx2 v[126:127], v126, s[66:67] sc1\n"
    "s_waitcnt vmcnt(0)\n"
    "v_xor_b32 v126, s96, v126\n"
    "v_xor_b32 v127, s97, v127\n"
    "v_cmp_gt_u64 vcc, 64, v[126:127]\n"
    "s_nop 1\n"
    "v_cndmask_b32 v126, 0, v126, vcc\n"
    "s_nop 0\n"
    "v_readlane_b32 s80, v126, 0\n"
    "v_readlane_b32 s81, v126, 1\n"
    "s_nop 1\n"
    "s_min_u32 s92, s80, s81\n"
    "s_cmp_lt_u32 s79, s92\n"
    "s_cbranch_scc1 L_cntokp%=\n"
    "s_sleep 2\n"
    "s_add_u32 s90, s90, 1\n"
    "s_and_b32 s78, s90, 0xff\n"
    "s_cmp_lg_u32 s78, 0\n"
    "s_cbranch_scc1 L_cntp%=\n"
    "v_mov_b32 v126, 0\n"
    "global_load_dwordx2 v[126:127], v126, s[68:69] sc1\n"
    "s_waitcnt vmcnt(0)\n"
    "v_cmp_eq_u64 vcc, s[96:97], v[126:127]\n"
    "s_nop 1\n"
    "s_cbranch_vccnz L_bail_cntp%=\n"
    "s_cmp_lt_u32 s90, 0x40000\n"
    "s_cbranch_scc1 L_cntp%=\n"
    "v_mov_b32 v126, s96\n"
    "v_mov_b32 v127, s97\n"
    "s_mov_b64 exec, 1\n"
    "global_store_dwordx2 v71, v[126:127], s[68:69] sc1\n"
    "s_mov_b64 exec, -1\n"
    "L_bail_cntp%=:\n"
    "L_cntokp%=:\n"
    "global_load_dwordx4 v[56:59], v75, s[58:59] offset:0 sc1\n"
    "global_load_dwordx4 v[60:63], v75, s[58:59] offset:1024 sc1\n"
    "global_load_dwordx2 v[64:65], v75, s[60:61] offset:0 sc1\n"
    "global_load_dwordx2 v[68:69], v75, s[60:61] offset:8 sc1\n"
    "global_load_dwordx2 v[72:73], v75, s[60:61] offset:1024 sc1\n"
    "global_load_dwordx2 v[76:77], v75, s[60:61] offset:1032 sc1\n"
    "L_kvdone%=:\n"
    "s_add_u32 s58, s58, 0x40000\n"
    "s_addc_u32 s59, s59, 0\n"
    "s_add_u32 s60, s60, 0x40000\n"
    "s_addc_u32 s61, s61, 0\n"
    "s_waitcnt lgkmcnt(0)\n"
    "s_memtime s[6:7]\n"
    "s_memrealtime s[78:79]\n"
    "s_nop 3\n"
    "s_memrealtime s[80:81]\n"
    "s_nop 3\n"
    "s_memrealtime s[82:83]\n"
    "s_nop 3\n"
    "s_memrealtime s[84:85]\n"
    "s_nop 3\n"
    "s_memrealtime s[86:87]\n"
    "s_nop 3\n"
    "s_memrealtime s[88:89]\n"
    "s_mov_b32 s14, 0\n"
    "s_waitcnt lgkmcnt(0)\n"
    "s_mul_i32 s91, s78, 24\n"
    "s_cmp_eq_u32 s78, s80\n"
    "s_addc_u32 s14, s14, 0\n"
    "s_cmp_eq_u32 s78, s82\n"
    "s_addc_u32 s14, s14, 0\n"
    "s_cmp_eq_u32 s78, s84\n"
    "s_addc_u32 s14, s14, 0\n"
    "s_cmp_eq_u32 s78, s86\n"
    "s_addc_u32 s14, s14, 0\n"
    "s_cmp_eq_u32 s78, s88\n"
    "s_addc_u32 s14, s14, 0\n"
    "s_mul_i32 s14, s14, 20\n"
    "s_sub_u32 s91, s91, s6\n"
    "s_sub_u32 s91, s91, s14\n"
    "s_mov_b32 s94, 0\n"
    "s_mov_b32 s78, 0\n"
    "s_mov_b32 s19, 2350\n"
    "s_add_u32 s46, s91, s78\n"
    "s_add_u32 s46, s46, s19\n"
    "s_cmp_lt_u32 s43, 29\n"
    "s_cselect_b32 s79, 70, 0\n"
    "s_cselect_b32 s95, 20, 0\n"
    "s_add_u32 s46, s46, s79\n"
    "s_add_u32 s95, s95, 1190\n"
    "s_ashr_i32 s78, s78, 2\n"
    "s_add_u32 s78, s78, s91\n"
    "s_sub_u32 s10, s16, s78\n"
    "s_add_u32 s10, s10, s95\n"
    "s_mov_b32 s47, s91\n"
    "s_mov_b32 s18, s6\n"
    "v_or_b32 v84, 1, s96\n"
    "v_mov_b32 v85, s97\n"
    "v_mov_b32 v86, s43\n"
    "s_branch L_g3poll%=\n"
    ".p2align 6\n"
    "L_g3poll%=:\n"
    "global_load_dwordx4 v[0:3], v114, s[54:55] sc1\n"
    "global_load_dwordx4 v[4:7], v114, s[54:55] offset:1024 sc1\n"
    "s_waitcnt vmcnt(0)\n"
    "s_branch L_g3w%=\n"
    ".p2align 6\n"
    "L_g3w%=:\n"
    "s_waitcnt vmcnt(2)\n"
    "ds_write_b128 v114, v[0:3]\n"
    "v_cmp_u_f32 s[78:79], v0, v1\n"
    "v_cmp_u_f32 s[80:81], v2, v3\n"
    "s_waitcnt vmcnt(1)\n"
    "ds_write_b128 v114, v[4:7] offset:1024\n"
    "L_g3x%=:\n"
    "v_cmp_u_f32 s[82:83], v4, v5\n"
    "v_cmp_u_f32 vcc, v6, v7\n"
    "s_or_b64 s[78:79], s[78:79], s[80:81]\n"
    "s_or_b64 s[78:79], s[78:79], s[82:83]\n"
    "s_or_b64 s[78:79], s[78:79], vcc\n"
    "s_cbranch_scc1 L_g3slow%=\n"
    "L_g3ok%=:\n"
    "s_cmp_lg_u32 s39, 1\n"
    "s_waitcnt lgkmcnt(0)\n"
    "s_barrier\n"
    "s_memtime s[16:17]\n"
    "ds_read_b64_tr_b16 v[0:1], v112 offset:0\n"
    "ds_read_b64_tr_b16 v[2:3], v113 offset:0\n"
    "ds_read_b64_tr_b16 v[4:5], v112 offset:1024\n"
    "ds_read_b64_tr_b16 v[6:7], v113 offset:1024\n"
    "ds_read_b64_tr_b16 v[8:9], v112 offset:2048\n"
    "ds_read_b64_tr_b16 v[10:11], v113 offset:2048\n"
    "s_cbranch_scc1 L_q%=\n"
    "s_add_u32 s14, s16, s46\n"
    "s_mov_b64 exec, 15\n"
    "v_mov_b32 v87, s14\n"
    "global_store_dwordx4 v115, v[84:87], s[56:57] sc1\n"
    "s_mov_b64 exec, -1\n"
    "L_q%=:\n"
    "s_waitcnt lgkmcnt(4)\n"
    "v_smfmac_f32_16x16x64_bf16 v[32:35], v[0:3], v[184:191], v67\n"
    "ds_read_b64_tr_b16 v[12:13], v112 offset:3072\n"
    "v_smfmac_f32_16x16x64_bf16 v[36:39], v[0:3], v[192:199], v67\n"
    "ds_read_b64_tr_b16 v[14:15], v113 offset:3072\n"
    "s_waitcnt lgkmcnt(4)\n"
    "v_smfmac_f32_16x16x64_bf16 v[32:35], v[4:7], v[200:207], v67\n"
    "ds_read_b64_tr_b16 v[16:17], v112 offset:4096\n"
    "v_smfmac_f32_16x16x64_bf16 v[36:39], v[4:7], v[208:215], v67\n"
    "ds_read_b64_tr_b16 v[18:19], v113 offset:4096\n"
    "s_waitcnt lgkmcnt(4)\n"
    "v_smfmac_f32_16x16x64_bf16 v[32:35], v[8:11], v[216:223], v67\n"
    "ds_read_b64_tr_b16 v[20:21], v112 offset:5120\n"
    "v_smfmac_f32_16x16x64_bf16 v[36:39], v[8:11], v[224:231], v67\n"
    "ds_read_b64_tr_b16 v[22:23], v113 offset:5120\n"
    "s_waitcnt lgkmcnt(4)\n"
    "v_smfmac_f32_16x16x64_bf16 v[32:35], v[12:15], v[232:239], v67\n"
    "ds_read_b64_tr_b16 v[24:25], v112 offset:6144\n"
    "v_smfmac_f32_16x16x64_bf16 v[36:39], v[12:15], v[240:247], v67\n"
    "ds_read_b64_tr_b16 v[26:27], v113 offset:6144\n"
    "s_waitcnt lgkmcnt(4)\n"
    "v_smfmac_f32_16x16x64_bf16 v[32:35], v[16:19], v[248:255], v67\n"
    "ds_read_b64_tr_b16 v[28:29], v112 offset:7168\n"
    "v_smfmac_f32_16x16x64_bf16 v[36:39], v[16:19], v[128:135], v67\n"
    "ds_read_b64_tr_b16 v[30:31], v113 offset:7168\n"
    "s_waitcnt lgkmcnt(4)\n"
    "v_smfmac_f32_16x16x64_bf16 v[32:35], v[20:23], v[136:143], v67\n"
    "ds_read_b128 v[184:187], v119 offset:0\n"
    "v_smfmac_f32_16x16x64_bf16 v[36:39], v[20:23], v[144:151], v67\n"
    "s_waitcnt lgkmcnt(1)\n"
    "v_smfmac_f32_16x16x64_bf16 v[32:35], v[24:27], v[152:159], v67\n"
    "ds_read_b128 v[188:191], v119 offset:1024\n"
    "v_smfmac_f32_16x16x64_bf16 v[36:39], v[24:27], v[160:167], v67\n"
    "ds_read_b128 v[192:195], v119 offset:4096\n"
    "v_smfmac_f32_16x16x64_bf16 v[32:35], v[28:31], v[168:175], v67\n"
    "ds_read_b128 v[196:199], v119 offset:5120\n"
    "v_smfmac_f32_16x16x64_bf16 v[36:39], v[28:31], v[176:183], v67\n"
    "ds_read_b128 v[200:203], v119 offset:2048\n"
    "v_smfmac_f32_16x16x64_bf16 v[40:43], v[0:3], a[0:7], v67\n"
    "ds_read_b128 v[204:207], v119 offset:3072\n"
    "v_smfmac_f32_16x16x64_bf16 v[44:47], v[0:3], a[64:71], v67\n"
    "v_add_f32 v32, v32, v34\n"
    "v_add_f32 v33, v33, v35\n"
    "v_smfmac_f32_16x16x64_bf16 v[40:43], v[4:7], a[8:15], v67\n"
    "v_add_f32 v36, v36, v38\n"
    "v_add_f32 v37, v37, v39\n"
    "v_smfmac_f32_16x16x64_bf16 v[44:47], v[4:7], a[72:79], v67\n"
    "v_cvt_pk_bf16_f32 v92, v32, v33\n"
    "v_cvt_pk_bf16_f32 v93, v36, v37\n"
    "v_smfmac_f32_16x16x64_bf16 v[40:43], v[8:11], a[16:23], v67\n"
    "ds_read_b128 v[208:211], v119 offset:6144\n"
    "v_mfma_f32_16x16x16_bf16 v[32:35], v[92:93], v[96:97], 0\n"
    "ds_read_b128 v[212:215], v119 offset:7168\n"
    "ds_read_b128 v[216:219], v119 offset:8192\n"
    "v_mfma_f32_16x16x16_bf16 v[36:39], v[92:93], v[98:99], 0\n"
    "ds_read_b128 v[220:223], v119 offset:9216\n"
    "ds_read_b128 v[224:227], v119 offset:12288\n"
    "v_smfmac_f32_16x16x64_bf16 v[44:47], v[8:11], a[80:87], v67\n"
    "ds_read_b128 v[228:231], v119 offset:13312\n"
    "v_smfmac_f32_16x16x64_bf16 v[40:43], v[12:15], a[24:31], v67\n"
    "v_cvt_pk_bf16_f32 v88, v32, v33\n"
    "v_cvt_pk_bf16_f32 v89, v34, v35\n"
    "v_smfmac_f32_16x16x64_bf16 v[44:47], v[12:15], a[88:95], v67\n"
    "v_cvt_pk_bf16_f32 v90, v36, v37\n"
    "v_cvt_pk_bf16_f32 v91, v38, v39\n"
    "v_smfmac_f32_16x16x64_bf16 v[40:43], v[16:19], a[32:39], v67\n"
    "ds_read_b128 v[232:235], v119 offset:10240\n"
    "v_mfma_f32_16x16x32_bf16 v[32:35], v[56:59], v[88:91], 0\n"
    "global_load_dwordx4 v[56:59], v75, s[58:59] offset:0 sc1\n"
    "ds_read_b128 v[236:239], v119 offset:11264\n"
    "v_mfma_f32_16x16x32_bf16 v[36:39], v[60:63], v[88:91], 0\n"
    "global_load_dwordx4 v[60:63], v75, s[58:59] offset:1024 sc1\n"
    "ds_read_b128 v[240:243], v119 offset:14336\n"
    "v_smfmac_f32_16x16x64_bf16 v[44:47], v[16:19], a[96:103], v67\n"
    "ds_read_b128 v[244:247], v119 offset:15360\n"
    "global_load_dwordx4 v[248:251], v75, s[60:61] offset:0 sc1\n"
    "v_smfmac_f32_16x16x64_bf16 v[40:43], v[20:23], a[40:47], v67\n"
    "global_load_dwordx4 v[252:255], v75, s[60:61] offset:1024 sc1\n"
    "v_smfmac_f32_16x16x64_bf16 v[44:47], v[20:23], a[104:111], v67\n"
    "v_cndmask_b32 v32, v32, v36, s[70:71]\n"
    "v_smfmac_f32_16x16x64_bf16 v[40:43], v[24:27], a[48:55], v67\n"
    "v_cndmask_b32 v33, v33, v37, s[70:71]\n"
    "v_smfmac_f32_16x16x64_bf16 v[44:47], v[24:27], a[112:119], v67\n"
    "v_cndmask_b32 v34, v34, v38, s[70:71]\n"
    "v_smfmac_f32_16x16x64_bf16 v[40:43], v[28:31], a[56:63], v67\n"
    "v_cndmask_b32 v35, v35, v39, s[70:71]\n"
    "v_smfmac_f32_16x16x64_bf16 v[44:47], v[28:31], a[120:127], v67\n"
    "ds_write_b128 v116, v[32:35]\n"
    "v_smfmac_f32_16x16x64_bf16 v[48:51], v[0:3], a[128:135], v67\n"
    "s_nop 1\n"
    "v_add_f32 v40, v40, v42\n"
    "v_add_f32 v41, v41, v43\n"
    "v_cvt_pk_bf16_f32 v84, v40, v41\n"
    "v_add_f32 v44, v44, v46\n"
    "v_add_f32 v45, v45, v47\n"
    "v_cvt_pk_bf16_f32 v85, v44, v45\n"
    "s_nop 1\n"
    "v_mfma_f32_16x16x16_bf16 v[40:43], v[84:85], v[100:101], 0\n"
    "v_mfma_f32_16x16x16_bf16 v[44:47], v[84:85], v[102:103], 0\n"
    "v_smfmac_f32_16x16x64_bf16 v[52:55], v[0:3], a[192:199], v67\n"
    "v_smfmac_f32_16x16x64_bf16 v[48:51], v[4:7], a[136:143], v67\n"
    "v_cvt_pk_bf16_f32 v92, v40, v41\n"
    "v_cvt_pk_bf16_f32 v93, v42, v43\n"
    "v_cvt_pk_bf16_f32 v94, v44, v45\n"
    "v_cvt_pk_bf16_f32 v95, v46, v47\n"
    "v_mov_b32 v0, 0\n"
    "v_mov_b32 v1, 0\n"
    "v_mfma_f32_16x16x32_bf16 v[40:43], v[92:95], v[88:91], 0\n"
    "v_mov_b32 v2, 0\n"
    "v_smfmac_f32_16x16x64_bf16 v[52:55], v[4:7], a[200:207], v67\n"
    "v_mov_b32 v3, 0\n"
    "v_smfmac_f32_16x16x64_bf16 v[48:51], v[8:11], a[144:151], v67\n"
    "v_mov_b32 v4, 0\n"
    "s_nop 0\n"
    "ds_write_b64 v116, v[40:41] offset:1024\n"
    "v_smfmac_f32_16x16x64_bf16 v[52:55], v[8:11], a[208:215], v67\n"
    "v_mov_b32 v5, 0\n"
    "v_smfmac_f32_16x16x64_bf16 v[48:51], v[12:15], a[152:159], v67\n"
    "v_mov_b32 v6, 0\n"
    "v_smfmac_f32_16x16x64_bf16 v[52:55], v[12:15], a[216:223], v67\n"
    "v_mov_b32 v7, 0\n"
    "v_smfmac_f32_16x16x64_bf16 v[48:51], v[16:19], a[160:167], v67\n"
    "v_mov_b32 v8, 0\n"
    "v_smfmac_f32_16x16x64_bf16 v[52:55], v[16:19], a[224:231], v67\n"
    "v_mov_b32 v9, 0\n"
    "v_smfmac_f32_16x16x64_bf16 v[48:51], v[20:23], a[168:175], v67\n"
    "v_mov_b32 v10, 0\n"
    "v_smfmac_f32_16x16x64_bf16 v[52:55], v[20:23], a[232:239], v67\n"
    "v_mov_b32 v11, 0\n"
    "s_waitcnt lgkmcnt(0)\n"
    "s_barrier\n"
    "ds_read_b64 v[42:43], v117 offset:1024\n"
    "ds_read_b64 v[84:85], v122 offset:1024\n"
    "ds_read_b64 v[86:87], v122 offset:3072\n"
    "ds_read_b128 v[36:39], v117\n"
    "ds_read_b128 v[44:47], v122\n"
    "ds_read_b128 v[88:91], v122 offset:2048\n"
    "v_smfmac_f32_16x16x64_bf16 v[48:51], v[24:27], a[176:183], v67\n"
    "v_mov_b32 v12, 0\n"
    "v_smfmac_f32_16x16x64_bf16 v[52:55], v[24:27], a[240:247], v67\n"
    "v_mov_b32 v13, 0\n"
    "v_smfmac_f32_16x16x64_bf16 v[48:51], v[28:31], a[184:191], v67\n"
    "v_mov_b32 v14, 0\n"
    "v_smfmac_f32_16x16x64_bf16 v[52:55], v[28:31], a[248:255], v67\n"
    "v_mov_b32 v15, 0\n"
    "s_waitcnt lgkmcnt(3)\n"
    "v_pk_add_f32 v[40:41], v[40:41], v[42:43]\n"
    "v_pk_add_f32 v[84:85], v[84:85], v[86:87]\n"
    "s_waitcnt lgkmcnt(2)\n"
    "v_pk_add_f32 v[32:33], v[32:33], v[36:37]\n"
    "v_pk_add_f32 v[34:35], v[34:35], v[38:39]\n"
    "v_pk_add_f32 v[40:41], v[40:41], v[84:85]\n"
    "s_waitcnt lgkmcnt(0)\n"
    "v_pk_add_f32 v[44:45], v[44:45], v[88:89]\n"
    "v_pk_add_f32 v[46:47], v[46:47], v[90:91]\n"
    "v_pk_add_f32 v[32:33], v[32:33], v[44:45]\n"
    "v_pk_add_f32 v[34:35], v[34:35], v[46:47]\n"
    "v_max3_f32 v36, v32, v40, v41\n"
    "v_max3_f32 v37, v33, v34, v35\n"
    "v_max_f32 v36, v36, v37\n"
    "v_pk_add_f32 v[48:49], v[48:49], v[50:51]\n"
    "v_pk_add_f32 v[52:53], v[52:53], v[54:55]\n"
    "v_max_f32_dpp v37, v36, v36 quad_perm:[2,3,0,1] row_mask:0xf bank_mask:0xf\n"
    "v_max_f32_dpp v38, v36, v36 quad_perm:[2,3,0,1] row_mask:0xf bank_mask:0xf\n"
    "v_cvt_pk_bf16_f32 v66, v48, v49\n"
    "v_cvt_pk_bf16_f32 v70, v48, v49\n"
    "v_permlane16_swap_b32 v37, v38\n"
    "v_max_f32 v36, v37, v38\n"
    "v_max_f32 v37, v37, v38\n"
    "v_cvt_pk_bf16_f32 v74, v52, v53\n"
    "v_cvt_pk_bf16_f32 v78, v52, v53\n"
    "v_permlane32_swap_b32 v36, v37\n"
    "v_max_f32 v36, v36, v37\n"
    "v_mul_f32 v36, s76, v36\n"
    "v_pk_fma_f32 v[40:41], v[40:41], s[76:77], v[36:37] op_sel_hi:[1,1,0] neg_lo:[0,0,1] neg_hi:[0,0,1]\n"
    "v_pk_fma_f32 v[32:33], v[32:33], s[76:77], v[36:37] op_sel_hi:[1,1,0] neg_lo:[0,0,1] neg_hi:[0,0,1]\n"
    "v_pk_fma_f32 v[34:35], v[34:35], s[76:77], v[36:37] op_sel_hi:[1,1,0] neg_lo:[0,0,1] neg_hi:[0,0,1]\n"
    "v_exp_f32 v40, v40\n"
    "v_exp_f32 v41, v41\n"
    "v_exp_f32 v32, v32\n"
    "v_exp_f32 v33, v33\n"
    "v_exp_f32 v34, v34\n"
    "v_exp_f32 v35, v35\n"
    "v_cvt_pk_bf16_f32 v126, v40, v41\n"
    "v_cvt_pk_bf16_f32 v44, v32, v33\n"
    "v_cvt_pk_bf16_f32 v46, v34, v35\n"
    "v_cndmask_b32 v45, 0, v126, s[72:73]\n"
    "v_cndmask_b32 v47, 0, v126, s[74:75]\n"
    "s_cmp_eq_u32 s43, 63\n"
    "s_cbranch_scc1 L_last%=\n"
    "v_smfmac_f32_16x16x64_bf16 v[104:107], v[44:47], v[64:71], v67\n"
    "v_smfmac_f32_16x16x64_bf16 v[108:111], v[44:47], v[72:79], v67\n"
    "v_mfma_f32_16x16x32_bf16 v[84:87], v[44:47], v[80:83], 0\n"
    "s_nop 1\n"
    "v_add_f32 v104, v104, v106\n"
    "v_add_f32 v105, v105, v107\n"
    "v_cvt_pk_bf16_f32 v104, v104, v105\n"
    "v_add_f32 v108, v108, v110\n"
    "v_add_f32 v109, v109, v111\n"
    "v_cvt_pk_bf16_f32 v105, v108, v109\n"
    "ds_write_b64 v118, v[104:105]\n"
    "v_pk_add_f32 v[124:125], v[84:85], v[86:87]\n"
    "v_rcp_f32 v124, v124\n"
    "v_rcp_f32 v125, v125\n"
    "s_waitcnt lgkmcnt(0)\n"
    "s_barrier\n"
    "ds_read_b64_tr_b16 v[32:33], v112 offset:16384\n"
    "ds_read_b64_tr_b16 v[34:35], v113 offset:16384\n"
    "ds_read_b64_tr_b16 v[36:37], v112 offset:17408\n"
    "ds_read_b64_tr_b16 v[38:39], v113 offset:17408\n"
    "s_waitcnt lgkmcnt(2)\n"
    "v_smfmac_f32_16x16x64_bf16 v[0:3], v[32:35], v[184:191], v67\n"
    "v_smfmac_f32_16x16x64_bf16 v[4:7], v[32:35], v[192:199], v67\n"
    "s_waitcnt lgkmcnt(0)\n"
    "v_smfmac_f32_16x16x64_bf16 v[0:3], v[36:39], v[200:207], v67\n"
    "v_smfmac_f32_16x16x64_bf16 v[4:7], v[36:39], v[208:215], v67\n"
    "v_smfmac_f32_16x16x64_bf16 v[8:11], v[32:35], v[216:223], v67\n"
    "v_smfmac_f32_16x16x64_bf16 v[12:15], v[32:35], v[224:231], v67\n"
    "v_add_f32 v0, v0, v2\n"
    "v_smfmac_f32_16x16x64_bf16 v[8:11], v[36:39], v[232:239], v67\n"
    "v_add_f32 v1, v1, v3\n"
    "v_smfmac_f32_16x16x64_bf16 v[12:15], v[36:39], v[240:247], v67\n"
    "v_add_f32 v4, v4, v6\n"
    "v_add_f32 v5, v5, v7\n"
    "v_mul_f32 v0, v0, v124\n"
    "v_mul_f32 v1, v1, v125\n"
    "v_mul_f32 v4, v4, v124\n"
    "v_mul_f32 v5, v5, v125\n"
    "v_cvt_pk_bf16_f32 v40, v0, v1\n"
    "v_pk_add_f32 v[8:9], v[8:9], v[10:11]\n"
    "v_cvt_pk_bf16_f32 v41, v4, v5\n"
    "v_pk_add_f32 v[12:13], v[12:13], v[14:15]\n"
    "v_pk_mul_f32 v[8:9], v[8:9], v[124:125]\n"
    "v_pk_mul_f32 v[12:13], v[12:13], v[124:125]\n"
    "v_cvt_pk_bf16_f32 v42, v8, v9\n"
    "v_cvt_pk_bf16_f32 v43, v12, v13\n"
    "s_nop 0\n"
    "v_permlane16_swap_b32 v40, v42\n"
    "v_permlane16_swap_b32 v41, v43\n"
    "global_store_dwordx4 v120, v[40:43], s[44:45] sc1\n"
    "s_memtime s[8:9]\n"
    "s_cmp_lg_u32 s39, 1\n"
    "s_waitcnt vmcnt(1)\n"
    "v_mov_b64 v[64:65], v[248:249]\n"
    "v_mov_b64 v[68:69], v[250:251]\n"
    "v_mov_b64 v[72:73], v[252:253]\n"
    "v_mov_b64 v[76:77], v[254:255]\n"
    "s_cbranch_scc1 L_nopl%=\n"
    "global_load_dword v123, v121, s[12:13] offset:12 sc1\n"
    "s_waitcnt lgkmcnt(0)\n"
    "s_sub_u32 s19, s8, s16\n"
    "s_add_u32 s16, s16, s46\n"
    "s_memtime s[6:7]\n"
    "s_memrealtime s[78:79]\n"
    "s_nop 3\n"
    "s_memrealtime s[80:81]\n"
    "s_nop 3\n"
    "s_memrealtime s[82:83]\n"
    "s_nop 3\n"
    "s_memrealtime s[84:85]\n"
    "s_nop 3\n"
    "s_memrealtime s[86:87]\n"
    "s_nop 3\n"
    "s_memrealtime s[88:89]\n"
    "s_mov_b32 s14, 0\n"
    "s_waitcnt lgkmcnt(0)\n"
    "s_mul_i32 s91, s78, 24\n"
    "s_cmp_eq_u32 s78, s80\n"
    "s_addc_u32 s14, s14, 0\n"
    "s_cmp_eq_u32 s78, s82\n"
    "s_addc_u32 s14, s14, 0\n"
    "s_cmp_eq_u32 s78, s84\n"
    "s_addc_u32 s14, s14, 0\n"
    "s_cmp_eq_u32 s78, s86\n"
    "s_addc_u32 s14, s14, 0\n"
    "s_cmp_eq_u32 s78, s88\n"
    "s_addc_u32 s14, s14, 0\n"
    "s_mul_i32 s14, s14, 20\n"
    "s_sub_u32 s91, s91, s6\n"
    "s_sub_u32 s91, s91, s14\n"
    "s_sub_i32 s78, s91, s47\n"
    "s_sub_u32 s79, s6, s18\n"
    "v_cvt_f32_u32 v127, s79\n"
    "v_cvt_f32_i32 v126, s78\n"
    "v_rcp_f32 v127, v127\n"
    "v_mul_f32 v126, 0x457a0000, v126\n"
    "v_mul_f32 v126, v126, v127\n"
    "v_cvt_i32_f32 v126, v126\n"
    "s_nop 0\n"
    "v_readfirstlane_b32 s78, v126\n"
    "s_min_u32 s19, s19, 2150\n"
    "s_max_i32 s78, s78, -200\n"
    "s_min_i32 s78, s78, 1200\n"
    "s_add_i32 s78, s78, s94\n"
    "s_ashr_i32 s78, s78, 1\n"
    "s_mov_b32 s94, s78\n"
    "s_add_u32 s46, s91, s78\n"
    "s_add_u32 s46, s46, s19\n"
    "s_cmp_lt_u32 s43, 29\n"
    "s_cselect_b32 s79, 70, 0\n"
    "s_cselect_b32 s95, 20, 0\n"
    "s_add_u32 s46, s46, s79\n"
    "s_add_u32 s95, s95, 1190\n"
    "s_ashr_i32 s78, s78, 2\n"
    "s_add_u32 s78, s78, s91\n"
    "s_sub_u32 s10, s16, s78\n"
    "s_add_u32 s10, s10, s95\n"
    "s_mov_b32 s47, s91\n"
    "s_mov_b32 s18, s6\n"
    "L_nopl%=:\n"
    "s_add_u32 s43, s43, 1\n"
    "s_cmp_lg_u32 s39, 2\n"
    "s_cbranch_scc1 L_cntokl%=\n"
    "s_add_u32 s79, s43, 1\n"
    "s_min_u32 s79, s79, 63\n"
    "s_lshr_b32 s79, s79, 1\n"
    "s_cmp_lt_u32 s79, s92\n"
    "s_cbranch_scc1 L_cntokl%=\n"
    "L_cntl%=:\n"
    "v_and_b32 v126, 16, v71\n"
    "v_lshlrev_b32 v126, 8, v126\n"
    "global_load_dwordx2 v[126:127], v126, s[66:67] sc1\n"
    "s_waitcnt vmcnt(0)\n"
    "v_xor_b32 v126, s96, v126\n"
    "v_xor_b32 v127, s97, v127\n"
    "v_cmp_gt_u64 vcc, 64, v[126:127]\n"
    "s_nop 1\n"
    "v_cndmask_b32 v126, 0, v126, vcc\n"
    "s_nop 0\n"
    "v_readlane_b32 s80, v126, 0\n"
    "v_readlane_b32 s81, v126, 1\n"
    "s_nop 1\n"
    "s_min_u32 s92, s80, s81\n"
    "s_cmp_lt_u32 s79, s92\n"
    "s_cbranch_scc1 L_cntokl%=\n"
    "s_sleep 2\n"
    "s_add_u32 s90, s90, 1\n"
    "s_and_b32 s78, s90, 0xff\n"
    "s_cmp_lg_u32 s78, 0\n"
    "s_cbranch_scc1 L_cntl%=\n"
    "v_mov_b32 v126, 0\n"
    "global_load_dwordx2 v[126:127], v126, s[68:69] sc1\n"
    "s_waitcnt vmcnt(0)\n"
    "v_cmp_eq_u64 vcc, s[96:97], v[126:127]\n"
    "s_nop 1\n"
    "s_cbranch_vccnz L_bail_cntl%=\n"
    "s_cmp_lt_u32 s90, 0x40000\n"
    "s_cbranch_scc1 L_cntl%=\n"
    "v_mov_b32 v126, s96\n"
    "v_mov_b32 v127, s97\n"
    "s_mov_b64 exec, 1\n"
    "global_store_dwordx2 v71, v[126:127], s[68:69] sc1\n"
    "s_mov_b64 exec, -1\n"
    "L_bail_cntl%=:\n"
    "L_cntokl%=:\n"
    "ds_read_b128 v[184:187], v119 offset:16384\n"
    "ds_read_b128 v[188:191], v119 offset:17408\n"
    "ds_read_b128 v[192:195], v119 offset:18432\n"
    "ds_read_b128 v[196:199], v119 offset:19456\n"
    "ds_read_b128 v[200:203], v119 offset:20480\n"
    "ds_read_b128 v[204:207], v119 offset:21504\n"
    "ds_read_b128 v[208:211], v119 offset:22528\n"
    "ds_read_b128 v[212:215], v119 offset:23552\n"
    "ds_read_b128 v[216:219], v119 offset:24576\n"
    "ds_read_b128 v[220:223], v119 offset:25600\n"
    "ds_read_b128 v[224:227], v119 offset:26624\n"
    "ds_read_b128 v[228:231], v119 offset:27648\n"
    "ds_read_b128 v[232:235], v119 offset:28672\n"
    "ds_read_b128 v[236:239], v119 offset:29696\n"
    "s_waitcnt lgkmcnt(8)\n"
    "ds_read_b128 v[240:243], v119 offset:30720\n"
    "ds_read_b128 v[244:247], v119 offset:31744\n"
    "ds_read_b128 v[248:251], v119 offset:32768\n"
    "ds_read_b128 v[252:255], v119 offset:33792\n"
    "v_mov_b64 v[32:33], 0\n"
    "v_mov_b64 v[34:35], 0\n"
    "v_mov_b64 v[36:37], 0\n"
    "v_mov_b64 v[38:39], 0\n"
    "v_mov_b64 v[40:41], 0\n"
    "v_mov_b64 v[42:43], 0\n"
    "v_mov_b64 v[44:45], 0\n"
    "v_mov_b64 v[46:47], 0\n"
    "v_mov_b64 v[48:49], 0\n"
    "v_mov_b64 v[50:51], 0\n"
    "v_mov_b64 v[52:53], 0\n"
    "v_mov_b64 v[54:55], 0\n"
    "v_mov_b64 v[104:105], 0\n"
    "v_mov_b64 v[106:107], 0\n"
    "v_mov_b64 v[108:109], 0\n"
    "v_mov_b64 v[110:111], 0\n"
    "s_sub_u32 s78, s43, 1\n"
    "s_lshr_b32 s78, s78, 2\n"
    "s_lshr_b32 s79, s43, 2\n"
    "s_lshr_b32 s78, s93, s78\n"
    "s_lshr_b32 s79, s93, s79\n"
    "s_xor_b32 s78, s78, s79\n"
    "s_and_b32 s78, s78, 1\n"
    "s_lshl_b32 s78, s78, 13\n"
    "v_xor_b32 v120, s78, v120\n"
    "s_add_u32 s44, s44, 0x80000\n"
    "s_addc_u32 s45, s45, 0\n"
    "s_add_u32 s62, s62, 0x20000\n"
    "s_addc_u32 s63, s63, 0\n"
    "s_add_u32 s12, s12, 0x10000\n"
    "s_andn2_b32 s12, s12, 0x40000\n"
    "s_add_u32 s56, s56, 0x10000\n"
    "s_andn2_b32 s56, s56, 0x40000\n"
    "s_add_u32 s58, s58, 0x40000\n"
    "s_addc_u32 s59, s59, 0\n"
    "s_add_u32 s60, s60, 0x40000\n"
    "s_addc_u32 s61, s61, 0\n"
    "s_and_b32 s78, s43, 3\n"
    "s_lshl_b32 s78, s78, 17\n"
    "s_add_u32 s54, s48, s78\n"
    "s_addc_u32 s55, s49, 0\n"
    "v_or_b32 v84, 1, s96\n"
    "v_mov_b32 v85, s97\n"
    "v_mov_b32 v86, s43\n"
    "s_waitcnt vmcnt(0)\n"
    "s_cmp_lg_u32 s39, 1\n"
    "s_cbranch_scc1 L_notime%=\n"
    "v_subrev_u32 v123, s16, v123\n"
    "v_cmp_gt_u32 vcc, 0x600, v123\n"
    "s_nop 1\n"
    "v_cndmask_b32 v123, 0, v123, vcc\n"
    "s_nop 1\n"
    "v_max_u32_dpp v123, v123, v123 row_ror:1 row_mask:0xf bank_mask:0xf\n"
    "s_nop 1\n"
    "v_max_u32_dpp v123, v123, v123 row_ror:2 row_mask:0xf bank_mask:0xf\n"
    "s_nop 1\n"
    "v_max_u32_dpp v123, v123, v123 row_ror:4 row_mask:0xf bank_mask:0xf\n"
    "s_nop 1\n"
    "v_max_u32_dpp v123, v123, v123 row_ror:8 row_mask:0xf bank_mask:0xf\n"
    "s_nop 0\n"
    "v_readlane_b32 s78, v123, 0\n"
    "v_readlane_b32 s79, v123, 16\n"
    "v_readlane_b32 s80, v123, 32\n"
    "v_readlane_b32 s81, v123, 48\n"
    "s_max_u32 s78, s78, s79\n"
    "s_max_u32 s80, s80, s81\n"
    "s_max_u32 s78, s78, s80\n"
    "s_add_u32 s8, s10, s78\n"
    "L_wug%=:\n"
    "s_memtime s[6:7]\n"
    "s_waitcnt lgkmcnt(0)\n"
    "s_sub_i32 s78, s8, s6\n"
    "s_sub_u32 s79, s78, 111\n"
    "s_cmp_lt_u32 s79, 0x1000\n"
    "s_cbranch_scc1 L_wug%=\n"
    "s_min_i32 s78, s78, 110\n"
    "L_wvg%=:\n"
    "s_sub_i32 s78, s78, 32\n"
    "s_cmp_gt_i32 s78, 0\n"
    "s_cbranch_scc1 L_wvg%=\n"
    "L_notime%=:\n"
    "s_barrier\n"
    "global_load_dwordx4 v[0:3], v114, s[54:55] sc1\n"
    "global_load_dwordx4 v[4:7], v114, s[54:55] offset:1024 sc1\n"
    "s_nop 15\n"
    "s_cmp_lg_u32 s42, 0\n"
    "s_cbranch_scc1 L_vmb%=\n"
    "global_store_dwordx4 v79, v[92:95], s[62:63] sc1\n"
    "s_branch L_kvb%=\n"
    "L_vmb%=:\n"
    "v_mov_b32 v126, v66\n"
    "v_mov_b32 v127, v74\n"
    "global_store_dwordx2 v79, v[126:127], s[62:63] sc1\n"
    "L_kvb%=:\n"
    "s_cmp_lg_u32 s39, 0\n"
    "s_cbranch_scc1 L_g3w%=\n"
    "s_mov_b64 exec, 1\n"
    "global_store_dwordx2 v71, v[84:85], s[64:65] sc1\n"
    "s_mov_b64 exec, -1\n"
    "s_add_u32 s64, s64, 8192\n"
    "s_addc_u32 s65, s65, 0\n"
    "s_waitcnt vmcnt(3)\n"
    "ds_write_b128 v114, v[0:3]\n"
    "v_cmp_u_f32 s[78:79], v0, v1\n"
    "v_cmp_u_f32 s[80:81], v2, v3\n"
    "s_waitcnt vmcnt(2)\n"
    "ds_write_b128 v114, v[4:7] offset:1024\n"
    "s_branch L_g3x%=\n"
    "L_g3slow%=:\n"
    "s_and_b32 s78, s90, 15\n"
    "s_cmp_lg_u32 s78, 15\n"
    "s_cbranch_scc1 L_g3retry%=\n"
    "s_mov_b32 s80, 0xffbfffbf\n"
    "s_mov_b64 s[82:83], 0\n"
    "v_cmp_eq_u32 vcc, s80, v0\n"
    "s_nop 0\n"
    "s_or_b64 s[82:83], s[82:83], vcc\n"
    "v_cmp_eq_u32 vcc, s80, v1\n"
    "s_nop 0\n"
    "s_or_b64 s[82:83], s[82:83], vcc\n"
    "v_cmp_eq_u32 vcc, s80, v2\n"
    "s_nop 0\n"
    "s_or_b64 s[82:83], s[82:83], vcc\n"
    "v_cmp_eq_u32 vcc, s80, v3\n"
    "s_nop 0\n"
    "s_or_b64 s[82:83], s[82:83], vcc\n"
    "v_cmp_eq_u32 vcc, s80, v4\n"
    "s_nop 0\n"
    "s_or_b64 s[82:83], s[82:83], vcc\n"
    "v_cmp_eq_u32 vcc, s80, v5\n"
    "s_nop 0\n"
    "s_or_b64 s[82:83], s[82:83], vcc\n"
    "v_cmp_eq_u32 vcc, s80, v6\n"
    "s_nop 0\n"
    "s_or_b64 s[82:83], s[82:83], vcc\n"
    "v_cmp_eq_u32 vcc, s80, v7\n"
    "s_nop 0\n"
    "s_or_b64 s[82:83], s[82:83], vcc\n"
    "s_cmp_eq_u64 s[82:83], 0\n"
    "s_cbranch_scc1 L_g3ok%=\n"
    "L_g3retry%=:\n"
    "s_add_u32 s90, s90, 1\n"
    "s_and_b32 s78, s90, 0xff\n"
    "s_cmp_lg_u32 s78, 0\n"
    "s_cbranch_scc1 L_g3poll%=\n"
    "v_mov_b32 v126, 0\n"
    "global_load_dwordx2 v[126:127], v126, s[68:69] sc1\n"
    "s_waitcnt vmcnt(0)\n"
    "v_cmp_eq_u64 vcc, s[96:97], v[126:127]\n"
    "s_nop 1\n"
    "s_cbranch_vccnz L_bail_g3%=\n"
    "s_cmp_lt_u32 s90, 0x40000\n"
    "s_cbranch_scc1 L_g3poll%=\n"
    "v_mov_b32 v126, s96\n"
    "v_mov_b32 v127, s97\n"
    "s_mov_b64 exec, 1\n"
    "global_store_dwordx2 v71, v[126:127], s[68:69] sc1\n"
    "s_mov_b64 exec, -1\n"
    "L_bail_g3%=:\n"
    "s_branch L_g3ok%=\n"
    "L_last%=:\n"
    "s_add_u32 s62, s62, 0x20000\n"
    "s_addc_u32 s63, s63, 0\n"
    "s_cmp_lg_u32 s42, 0\n"
    "s_cbranch_scc1 L_vmc%=\n"
    "global_store_dwordx4 v79, v[92:95], s[62:63] sc1\n"
    "s_branch L_kvc%=\n"
    "L_vmc%=:\n"
    "v_mov_b32 v126, v66\n"
    "v_mov_b32 v127, v74\n"
    "global_store_dwordx2 v79, v[126:127], s[62:63] sc1\n"
    "L_kvc%=:\n"
    "s_waitcnt vmcnt(0) lgkmcnt(0)\n"
    "s_barrier\n"
    "s_cmp_lg_u32 s39, 0\n"
    "s_cbranch_scc1 L_end%=\n"
    "v_or_b32 v86, 1, s96\n"
    "v_mov_b32 v87, s97\n"
    "s_mov_b64 exec, 1\n"
    "global_store_dwordx2 v71, v[86:87], s[64:65] sc1\n"
    "s_add_u32 s64, s64, 8192\n"
    "s_addc_u32 s65, s65, 0\n"
    "global_store_dwordx2 v71, v[86:87], s[64:65] sc1\n"
    "s_waitcnt vmcnt(0)\n"
    "L_end%=:\n"
    "s_endpgm\n"
    :: "s"(ws), "s"(__builtin_amdgcn_readfirstlane(by * 4 + bx)), "s"(wave), "s"(ldsb), "s"(ws + OFF_EP), "s"(Wq), "s"(Wk), "s"(Wv), "s"(Wo)
    : "memory", "vcc", "v255", "a255", "s36", "s37", "s38", "s39", "s40", "s96", "s97", "s99", "s22", "s23", "s24", "s25", "s26", "s27", "s28", "s29", "scc");
  __builtin_unreachable();
}

extern "C" void kernel_launch(void* const* d_in, const int* in_sizes, int n_in, void* d_out, int out_size, void* d_ws, size_t ws_size,
                              hipStream_t stream) {
  const u16* x = (const u16*)d_in[0];
  const u16* mem0 = (const u16*)d_in[1];
  const u16* Wq = (const u16*)d_in[2];
  const u16* Wk = (const u16*)d_in[3];
  const u16* Wv = (const u16*)d_in[4];
  const u16* Wo = (const u16*)d_in[5];
  const u16* w_r = (const u16*)d_in[8];
  const u16* w_gate = (const u16*)d_in[9];
  const u16* w_up = (const u16*)d_in[10];
  const u16* w_down = (const u16*)d_in[11];
  u16* out = (u16*)d_out;
  char* ws = (char*)(((size_t)d_ws + 2 * MBY - 1) & ~(size_t)(2 * MBY - 1));
  rec_kernel<<<256, 256, 0, stream>>>(ws, x, mem0, Wq, Wk, Wv, Wo, w_r, w_gate, w_up, w_down, out);
}
```

```cpp
#include <hip/hip_runtime.h>
#include <hip/hip_bf16.h>

typedef unsigned short u16;
typedef unsigned long long u64;
typedef __attribute__((ext_vector_type(8))) short bf16x8;
typedef __attribute__((ext_vector_type(4))) float f32x4;
typedef __attribute__((ext_vector_type(4))) unsigned u32x4;
#define GAS __attribute__((address_space(1)))
typedef GAS u64 gu64;
typedef GAS unsigned gu32;
#define RLX_AGENT __ATOMIC_RELAXED, __HIP_MEMORY_SCOPE_AGENT

constexpr int S_ = 2048, DM = 512, NH = 32, DH = 128, MM = 8, SEG = 32, NSEG = 64;
constexpr int HD = NH * DH;
constexpr int QKVN = 3 * HD;
constexpr int FF = 1792;

__device__ __forceinline__ float bf2f(u16 v) { return __builtin_bit_cast(float, (unsigned)v << 16); }
__device__ __forceinline__ float rbf(float f) { return (float)(__bf16)f; }

template <int CTRL> __device__ __forceinline__ float dpp_add(float v) {
  return v + __builtin_bit_cast(float, __builtin_amdgcn_update_dpp(0, __builtin_bit_cast(int, v), CTRL, 0xf, 0xf, false));
}
__device__ __forceinline__ float wave_sum(float v) {
  v = dpp_add<0x121>(v); v = dpp_add<0x122>(v); v = dpp_add<0x124>(v); v = dpp_add<0x128>(v);
  auto r = __builtin_amdgcn_permlane16_swap(__builtin_bit_cast(unsigned, v), __builtin_bit_cast(unsigned, v), false, false);
  const unsigned r0 = r[0], r1 = r[1];
  v = __builtin_bit_cast(float, r0) + __builtin_bit_cast(float, r1);
  auto q = __builtin_amdgcn_permlane32_swap(__builtin_bit_cast(unsigned, v), __builtin_bit_cast(unsigned, v), false, false);
  const unsigned q0 = q[0], q1 = q[1];
  return __builtin_bit_cast(float, q0) + __builtin_bit_cast(float, q1);
}
typedef __bf16 bf16x2_t __attribute__((ext_vector_type(2)));
typedef float f32x2_t __attribute__((ext_vector_type(2)));
__device__ __forceinline__ unsigned pk2(float a, float b) {
  f32x2_t v; v.x = a; v.y = b;
  return __builtin_bit_cast(unsigned, __builtin_convertvector(v, bf16x2_t));
}
__device__ __forceinline__ void rbf2(float& a, float& b) {
  const unsigned p = pk2(a, b);
  a = __builtin_bit_cast(float, p << 16); b = __builtin_bit_cast(float, p & 0xffff0000u);
}
__device__ __forceinline__ void ln_row(const float (&x)[8], float (&y)[8]) {
  float s = ((x[0] + x[1]) + (x[2] + x[3])) + ((x[4] + x[5]) + (x[6] + x[7]));
  s = wave_sum(s);
  const float mean = s * (1.0f / DM);
  float q = 0.f;
#pragma unroll
  for (int i = 0; i < 8; ++i) { float d = x[i] - mean; q += d * d; }
  q = wave_sum(q);
  float var = q * (1.0f / DM), mb = mean;
  rbf2(var, mb);
  const float sd = rbf(__builtin_amdgcn_sqrtf(rbf(var + 1e-5f)));
  const float inv = __builtin_amdgcn_rcpf(sd);
#pragma unroll
  for (int i = 0; i < 8; i += 2) {
    float a = x[i] - mb, b = x[i + 1] - mb;
    rbf2(a, b);
    a *= inv; b *= inv;
    rbf2(a, b);
    y[i] = a; y[i + 1] = b;
  }
}

constexpr size_t MBY = 1u << 20;
constexpr size_t OFF_G2 = 0, OFF_G3 = MBY, OFF_SYNC = 2 * MBY;
constexpr size_t OFF_EP = OFF_SYNC, OFF_ERR = OFF_SYNC + 512, OFF_RDY = OFF_SYNC + 0x1000, OFF_ACT = OFF_SYNC + 0x140000, OFF_RC2 = OFF_SYNC + 0x128000;
constexpr size_t OFF_CLM = OFF_SYNC + 0x10000, OFF_TAB = OFF_SYNC + 0xC000, OFF_XFL = OFF_SYNC + 0xE000;
constexpr size_t OFF_PIL = OFF_G3 + 0x80000, OFF_FLAGS = OFF_SYNC + 0x22000, OFF_OFL = OFF_SYNC + 0xA2000, OFF_HFL = OFF_SYNC + 0xE2000;
constexpr size_t OFF_RC = OFF_SYNC + 0x102000, OFF_PAR = OFF_SYNC + 0x112000;
constexpr unsigned SENT = 0xFFBFFFBFu;
constexpr size_t OFF_KF = 21 * MBY, OFF_VF = 37 * MBY, OFF_KM = 100 * MBY, OFF_VM = 108 * MBY;
constexpr size_t OFF_QF = 61 * MBY, OFF_XN = 77 * MBY, OFF_O = 79 * MBY, OFF_HP = 148 * MBY;

__device__ __forceinline__ unsigned tl_idx() { unsigned xcc; asm volatile("s_getreg_b32 %0, hwreg(HW_REG_XCC_ID)" : "=s"(xcc)); return (blockIdx.x & ~7u) | (xcc & 7u); }
#define GS(i) do {} while (0)
#define TS2(i) do {} while (0)
#define TSD() do {} while (0)
#define TL(i) do {} while (0)
#define TLP() do {} while (0)
#define TLX(i) do {} while (0)
__device__ __forceinline__ u64 now_clk() { u64 t; asm volatile("s_memtime %0\n\ts_waitcnt lgkmcnt(0)" : "=s"(t) :: "memory"); return t; }

constexpr int NREC = 64;

__device__ __forceinline__ bool spin_bail(unsigned& spins, char* ws, u64 T) {
  ++spins;
  if ((spins & 255u) == 0u) {
    gu64* err = (gu64*)(ws + OFF_ERR);
    if (__hip_atomic_load(err, RLX_AGENT) == T) return true;
    if (spins > (1u << 20)) { __hip_atomic_store(err, T, RLX_AGENT); return true; }
  }
  return false;
}
__device__ __forceinline__ void set_flag(char* ws, size_t off, u64 T, unsigned v) { __hip_atomic_store((gu64*)(ws + off), T + v, RLX_AGENT); }
__device__ __forceinline__ bf16x8 mk8(unsigned a, unsigned b, unsigned c, unsigned d) {
  u32x4 v; v.x = a; v.y = b; v.z = c; v.w = d; return __builtin_bit_cast(bf16x8, v);
}
__device__ __forceinline__ void store16_sc1(const void* base, unsigned off, u32x4 v) {
  asm volatile("global_store_dwordx4 %0, %1, %2 sc1\n\ts_nop 1" :: "v"(off), "v"(v), "s"(base) : "memory");
}

#define NSPEC (59 * 32)
#define ASL 24
#define QSL 1
#define ADL 0
#define ATN 0
#define QDL 90
#define NSIX 42
#define XROT 0
#define NPROBE 12
#define PSL 4
#define HSL 10
#define LAS __attribute__((address_space(3)))
__device__ __forceinline__ void unpack8(const u32x4 v, float (&f)[8]) {
  f[0] = bf2f(v.x & 0xffff); f[1] = bf2f(v.x >> 16); f[2] = bf2f(v.y & 0xffff); f[3] = bf2f(v.y >> 16);
  f[4] = bf2f(v.z & 0xffff); f[5] = bf2f(v.z >> 16); f[6] = bf2f(v.w & 0xffff); f[7] = bf2f(v.w >> 16);
}
template <int SL>
__device__ __forceinline__ void wait_flag(char* ws, unsigned off, u64 T, unsigned target) {
  gu64* p = (gu64*)(ws + off);
  unsigned spins = 0;
  while (!__all(__hip_atomic_load(p, RLX_AGENT) - T - target < 64u - target)) {
    __builtin_amdgcn_s_sleep(SL);
    if (spin_bail(spins, ws, T)) break;
  }
  __builtin_amdgcn_fence(__ATOMIC_ACQUIRE, "wavefront");
}
typedef __attribute__((ext_vector_type(4))) short s16x4;
__device__ __forceinline__ bf16x8 tr_frag(const char* p0, const char* p1) {
  const s16x4 a = __builtin_amdgcn_ds_read_tr16_b64_v4i16((LAS s16x4*)p0), b = __builtin_amdgcn_ds_read_tr16_b64_v4i16((LAS s16x4*)p1);
  return __builtin_shufflevector(a, b, 0, 1, 2, 3, 4, 5, 6, 7);
}

__device__ __forceinline__ float sum_g(float v) {
  auto r = __builtin_amdgcn_permlane16_swap(__builtin_bit_cast(unsigned, v), __builtin_bit_cast(unsigned, v), false, false);
  const unsigned r0 = r[0], r1 = r[1];
  v = __builtin_bit_cast(float, r0) + __builtin_bit_cast(float, r1);
  auto q = __builtin_amdgcn_permlane32_swap(__builtin_bit_cast(unsigned, v), __builtin_bit_cast(unsigned, v), false, false);
  const unsigned q0 = q[0], q1 = q[1];
  return __builtin_bit_cast(float, q0) + __builtin_bit_cast(float, q1);
}
__device__ __forceinline__ float max_g(float v) {
  auto r = __builtin_amdgcn_permlane16_swap(__builtin_bit_cast(unsigned, v), __builtin_bit_cast(unsigned, v), false, false);
  const unsigned r0 = r[0], r1 = r[1];
  v = fmaxf(__builtin_bit_cast(float, r0), __builtin_bit_cast(float, r1));
  auto q = __builtin_amdgcn_permlane32_swap(__builtin_bit_cast(unsigned, v), __builtin_bit_cast(unsigned, v), false, false);
  const unsigned q0 = q[0], q1 = q[1];
  return fmaxf(__builtin_bit_cast(float, q0), __builtin_bit_cast(float, q1));
}
__device__ __forceinline__ void ln_tile(u32x4 (&xr)[8]) {
  auto up = [](unsigned u) { f32x2_t v; v.x = __builtin_bit_cast(float, u << 16); v.y = __builtin_bit_cast(float, u & 0xffff0000u); return v; };
  f32x2_t f[8][4], sb[4] = {{0.f, 0.f}, {0.f, 0.f}, {0.f, 0.f}, {0.f, 0.f}};
#pragma unroll
  for (int k = 0; k < 8; ++k)
#pragma unroll
    for (int e = 0; e < 4; ++e) { const unsigned u = xr[k][e]; f[k][e] = up(u); sb[e] += f[k][e]; }
  const f32x2_t sa = (sb[0] + sb[1]) + (sb[2] + sb[3]);
  const float mean = sum_g(dpp_add<0x128>(sa.x + sa.y)) * (1.0f / DM);
  f32x2_t qb[4] = {{0.f, 0.f}, {0.f, 0.f}, {0.f, 0.f}, {0.f, 0.f}};
#pragma unroll
  for (int k = 0; k < 8; ++k)
#pragma unroll
    for (int e = 0; e < 4; ++e) { const f32x2_t d = f[k][e] - mean; qb[e] += d * d; }
  const f32x2_t qa = (qb[0] + qb[1]) + (qb[2] + qb[3]);
  float var = sum_g(dpp_add<0x128>(qa.x + qa.y)) * (1.0f / DM), mb = mean;
  rbf2(var, mb);
  const float inv = __builtin_amdgcn_rcpf(rbf(__builtin_amdgcn_sqrtf(rbf(var + 1e-5f))));
#pragma unroll
  for (int k = 0; k < 8; ++k)
#pragma unroll
    for (int e = 0; e < 4; ++e) {
      const f32x2_t d = f[k][e] - mb;
      const f32x2_t y = up(pk2(d.x, d.y)) * inv;
      xr[k][e] = pk2(y.x, y.y);
    }
}
__device__ __forceinline__ unsigned pgc(unsigned c) { return ((c & 1u) << 2) | (c >> 1); }
__device__ __forceinline__ unsigned probe(char* ws, int q, unsigned c, int lane) {
  const char* p = ws + OFF_G3 + 0xC0000 + (q & 7) * 32768 + q * 128;
  unsigned best = ~0u, near = 0, z = 0;
  asm volatile("global_atomic_or %0, %1, %0, %2 sc0 sc1\n\ts_waitcnt vmcnt(0)" : "+v"(z) : "v"((lane & 7) * 4096), "s"(p) : "memory");
#pragma unroll 1
  for (int i = 0; i < NPROBE; ++i) {
    if (i == 4) {
      unsigned m[2] = {~0u, ~0u};
#pragma unroll
      for (int k = 0; k < 8; ++k) m[(k >> 1) & 1] = min(m[(k >> 1) & 1], (unsigned)__builtin_amdgcn_readlane((int)best, k));
      near = m[1] < m[0] ? 2u : 0u;
    }
    const unsigned j = i + q, pg = i < 4 ? (pgc(j & 3u) | (((j ^ (j >> 1) ^ (q >> 2)) & 1u) << 1)) : (pgc(j & 3u) | near);
    u64 t0, t1;
    asm volatile("s_mov_b64 exec, 1\n\tv_mov_b32 %0, 0\n\ts_memtime %1\n\ts_waitcnt lgkmcnt(0)\n\tglobal_atomic_or %0, %0, %0, %3 sc0 sc1\n\ts_waitcnt vmcnt(0)\n\t"
                 "s_memtime %2\n\ts_waitcnt lgkmcnt(0)\n\ts_mov_b64 exec, -1" : "=&v"(z), "=&s"(t0), "=&s"(t1) : "s"(p + pg * 4096) : "memory");
    if (lane == pg) best = min(best, (unsigned)(t1 - t0));
  }
  const unsigned a = __builtin_amdgcn_readlane((int)best, near), b = __builtin_amdgcn_readlane((int)best, near | 1), d = __builtin_amdgcn_readlane((int)best, near | 4), e = __builtin_amdgcn_readlane((int)best, near | 5);
  return 1u + ((near | (d + e < a + b ? 4u : 0u) | (b + e < a + d ? 1u : 0u)) ^ pgc(c) ^ 5u);
}
__device__ __forceinline__ u64 uni(u64 v) { return ((u64)(unsigned)__builtin_amdgcn_readfirstlane((unsigned)(v >> 32)) << 32) | (u64)(unsigned)__builtin_amdgcn_readfirstlane((unsigned)v); }
__device__ __forceinline__ u64 tag_of(u64 ep) { return (ep + 1) << 6; }
__device__ __forceinline__ void ln1_phase(char* ws, int q, u64 T, char* lds, int tid) {
  const int lane = tid & 63, wv = tid >> 6;
  u32x4 xr[8];
#pragma unroll
  for (int k = 0; k < 8; ++k) xr[k] = *(const u32x4*)(lds + (wv * 8 + (lane & 7)) * 1040 + (lane >> 3) * 128 + k * 16);
  ln_tile(xr);
#pragma unroll
  for (int k = 0; k < 8; ++k) store16_sc1(ws + OFF_XN, (unsigned)((q * 2 + (wv >> 1)) * 16384 + ((lane >> 3) * 8 + k) * 256 + ((wv & 1) * 8 + (lane & 7)) * 16), xr[k]);
  if (wv < 2) store16_sc1(ws, (unsigned)((wv == 0 ? OFF_RC : OFF_RC2) + q * 1024 + lane * 16), (u32x4){0u, 0u, 0u, 0u});
  store16_sc1(ws + OFF_ACT, (unsigned)(q * 4096 + tid * 16), (u32x4){SENT, SENT, SENT, SENT});
  asm volatile("s_waitcnt vmcnt(0)" ::: "memory");
  __syncthreads();
  if (tid == 0) set_flag(ws, OFF_XFL + q * 128, T, 1);
  TL(6);
}

__device__ __forceinline__ void load_h(const __amdgpu_buffer_rsrc_t rs, const u32x4 xv, int row, int lane, float (&f)[8]) {
  f32x4 p[4][2];
#pragma unroll
  for (int k = 0; k < 4; ++k)
#pragma unroll
    for (int u = 0; u < 2; ++u)
      p[k][u] = __builtin_bit_cast(f32x4, __builtin_amdgcn_raw_buffer_load_b128(rs, (int)OFF_HP + (((row >> 5) * 4 + k) * 4 + (lane >> 4)) * 16384 + (row & 31) * 512 + (lane & 15) * 32 + u * 16, 0, 16));
  float xf[8];
  unpack8(xv, xf);
#pragma unroll
  for (int i = 0; i < 8; ++i) f[i] = rbf(xf[i] + rbf((p[0][i >> 2][i & 3] + p[1][i >> 2][i & 3]) + (p[2][i >> 2][i & 3] + p[3][i >> 2][i & 3])));
}

__device__ __forceinline__ unsigned wait_par(char* ws, u64 T, int lane) {
  unsigned spins = 0; u64 p;
  for (;;) {
    p = __hip_atomic_load((gu64*)(ws + OFF_PAR + min(lane, 10) * 128), RLX_AGENT) - T - 1;
    if (__all(p < 8)) break;
    __builtin_amdgcn_s_sleep(8);
    if (spin_bail(spins, ws, T)) break;
  }
  unsigned psi = 0;
#pragma unroll
  for (int k = 1; k < 8; k *= 2) psi |= __builtin_popcount((unsigned)__ballot(p & k) & 0x7ffu) >= 6 ? k : 0;
  return psi;
}
#define GCLOB "memory", "vcc", "scc", "s40", "s41", "s42", "s43", "s44", "s45", "s46", "s47", "s48", "s49", "s50", "s51", "s52", "s53", "s54", "s55", "s56", "s57", "s58", "s59", "s60", "s61", "s62", "s63", "s64", "s65", "s66", "s67", "s68", "s69", "s70", "s71", "v109", "v110", "v111", "v112", "v113", "v114", "v115", "v116", "v117", "v118", "v119", "v120", "v121", "v122", "v123", "v124", "v125", "v126", "v127", "v128", "v129", "v130", "v131", "v132", "v133", "v134", "v135", "v136", "v137", "v138", "v139", "v140", "v141", "v142", "v143", "v144", "v145", "v146", "v147", "v148", "v149", "v150", "v151", "v152", "v153", "v154", "v155", "v156", "v157", "v158", "v159", "v160", "v161", "v162", "v163", "v164", "v165", "v166", "v167", "v168", "v169", "v170", "v171", "v172", "v173", "v174", "v175", "v176", "v177", "v178", "v179", "v180", "v181", "v182", "v183", "v184", "v185", "v186", "v187", "v188", "v189", "v190", "v191", "v192", "v193", "v194", "v195", "v196", "v197", "v198", "v199", "v200", "v201", "v202", "v203", "v204", "v205", "v206", "v207", "v208", "v209", "v210", "v211", "v212", "v213", "v214", "v215", "v216", "v217", "v218", "v219", "v220", "v221", "v222", "v223", "v224", "v225", "v226", "v227", "v228", "v229", "v230", "v231", "v232", "v233", "v234", "v235", "v236", "v237", "v238", "v239", "v240", "v241", "v242", "v243", "v244", "v245", "v246", "v247", "v248", "v249", "v250", "v251", "v252", "v253", "v254", "v255", "a0", "a1", "a2", "a3", "a4", "a5", "a6", "a7", "a8", "a9", "a10", "a11", "a12", "a13", "a14", "a15", "a16", "a17", "a18", "a19", "a20", "a21", "a22", "a23", "a24", "a25", "a26", "a27", "a28", "a29", "a30", "a31", "a32", "a33", "a34", "a35", "a36", "a37", "a38", "a39", "a40", "a41", "a42", "a43", "a44", "a45", "a46", "a47", "a48", "a49", "a50", "a51", "a52", "a53", "a54", "a55", "a56", "a57", "a58", "a59", "a60", "a61", "a62", "a63", "a64", "a65", "a66", "a67", "a68", "a69", "a70", "a71", "a72", "a73", "a74", "a75", "a76", "a77", "a78", "a79", "a80", "a81", "a82", "a83", "a84", "a85", "a86", "a87", "a88", "a89", "a90", "a91", "a92", "a93", "a94", "a95", "a96", "a97", "a98", "a99", "a100", "a101", "a102", "a103", "a104", "a105", "a106", "a107", "a108", "a109", "a110", "a111", "a112", "a113", "a114", "a115", "a116", "a117", "a118", "a119", "a120", "a121", "a122", "a123", "a124", "a125", "a126", "a127", "a128", "a129", "a130", "a131", "a132", "a133", "a134", "a135", "a136", "a137", "a138", "a139", "a140", "a141", "a142", "a143", "a144", "a145", "a146", "a147", "a148", "a149", "a150", "a151", "a152", "a153", "a154", "a155", "a156", "a157", "a158", "a159", "a160", "a161", "a162", "a163", "a164", "a165", "a166", "a167", "a168", "a169", "a170", "a171", "a172", "a173", "a174", "a175", "a176", "a177", "a178", "a179", "a180", "a181", "a182", "a183", "a184", "a185", "a186", "a187", "a188", "a189", "a190", "a191", "a192", "a193", "a194", "a195", "a196", "a197", "a198", "a199", "a200", "a201", "a202", "a203", "a204", "a205", "a206", "a207", "a208", "a209", "a210", "a211", "a212", "a213", "a214", "a215", "a216", "a217", "a218", "a219", "a220", "a221", "a222", "a223", "a224", "a225", "a226", "a227", "a228", "a229", "a230", "a231", "a232", "a233", "a234", "a235", "a236", "a237", "a238", "a239", "a240", "a241", "a242", "a243", "a244", "a245", "a246", "a247", "a248", "a249", "a250", "a251", "a252", "a253", "a254", "a255"
template <int MAT>
__device__ __forceinline__ u64 qkv_phase(char* ws, int h, u64 ep0, int tid) {
  const int lane = tid & 63, wv = __builtin_amdgcn_readfirstlane(tid >> 6), l15 = lane & 15, g = lane >> 4;
  const int wr = wv >> 1, wc = wv & 1;
  TLX(512 + (MAT * 32 + h) * 16 + 1);
  const u64 T = tag_of(ep0);
  TLX(512 + (MAT * 32 + h) * 16 + 2);
  const int qq = l15 >> 2, f = qq | ((g & 1) << 2);
  const unsigned rp = (g * 8 + qq) * 256 + (l15 & 3) * 8 + ((wc * 4) ^ f) * 32;
  char* op = ws + (MAT == 0 ? OFF_QF : (MAT == 1 ? OFF_KF : OFF_VF)) + (wr * 32 + h) * 8192 + wc * 4096;
  char* rdy = ws + OFF_RDY + (MAT * 32 + h) * 128;
  asm volatile("s_waitcnt vmcnt(0)\n\ts_barrier" ::: "memory");
#define GIN "s"(ws), "s"(uni(T)), "s"(op), "s"(rdy), "s"(wv), "v"(lane * 16), "v"(rp)
  if constexpr (MAT == 0) asm volatile(
    "s_mov_b64 s[40:41], %0\n"
    "s_mov_b64 s[42:43], %1\n"
    "s_mov_b64 s[44:45], %2\n"
    "s_mov_b64 s[54:55], %3\n"
    "s_mov_b32 s56, %4\n"
    "v_mov_b32 v121, %5\n"
    "s_add_u32 s58, s40, 0x20e000\n"
    "s_addc_u32 s59, s41, 0\n"
    "v_lshlrev_b32 v225, 3, v121\n"
    "v_and_b32 v224, 0x380, v225\n"
    "global_load_dwordx2 v[226:227], v224, s[58:59] sc1\n"
    "v_xor_b32 v112, 0, %6\n"
    "v_xor_b32 v113, 32, %6\n"
    "v_xor_b32 v114, 64, %6\n"
    "v_xor_b32 v115, 96, %6\n"
    "ds_read_b64_tr_b16 a[0:1], v112 offset:0\n"
    "ds_read_b64_tr_b16 a[2:3], v112 offset:1024\n"
    "ds_read_b64_tr_b16 a[4:5], v112 offset:8192\n"
    "ds_read_b64_tr_b16 a[6:7], v112 offset:9216\n"
    "ds_read_b64_tr_b16 a[8:9], v112 offset:16384\n"
    "ds_read_b64_tr_b16 a[10:11], v112 offset:17408\n"
    "ds_read_b64_tr_b16 a[12:13], v112 offset:24576\n"
    "ds_read_b64_tr_b16 a[14:15], v112 offset:25600\n"
    "s_waitcnt lgkmcnt(6)\n"
    "ds_read_b64_tr_b16 a[16:17], v112 offset:32768\n"
    "ds_read_b64_tr_b16 a[18:19], v112 offset:33792\n"
    "ds_read_b64_tr_b16 a[20:21], v112 offset:40960\n"
    "ds_read_b64_tr_b16 a[22:23], v112 offset:41984\n"
    "ds_read_b64_tr_b16 a[24:25], v112 offset:49152\n"
    "ds_read_b64_tr_b16 a[26:27], v112 offset:50176\n"
    "ds_read_b64_tr_b16 a[28:29], v112 offset:57344\n"
    "ds_read_b64_tr_b16 a[30:31], v112 offset:58368\n"
    "s_waitcnt lgkmcnt(6)\n"
    "ds_read_b64_tr_b16 a[64:65], v113 offset:0\n"
    "ds_read_b64_tr_b16 a[66:67], v113 offset:1024\n"
    "ds_read_b64_tr_b16 a[68:69], v113 offset:8192\n"
    "ds_read_b64_tr_b16 a[70:71], v113 offset:9216\n"
    "ds_read_b64_tr_b16 a[72:73], v113 offset:16384\n"
    "ds_read_b64_tr_b16 a[74:75], v113 offset:17408\n"
    "ds_read_b64_tr_b16 a[76:77], v113 offset:24576\n"
    "ds_read_b64_tr_b16 a[78:79], v113 offset:25600\n"
    "s_waitcnt lgkmcnt(6)\n"
    "ds_read_b64_tr_b16 a[80:81], v113 offset:32768\n"
    "ds_read_b64_tr_b16 a[82:83], v113 offset:33792\n"
    "ds_read_b64_tr_b16 a[84:85], v113 offset:40960\n"
    "ds_read_b64_tr_b16 a[86:87], v113 offset:41984\n"
    "ds_read_b64_tr_b16 a[88:89], v113 offset:49152\n"
    "ds_read_b64_tr_b16 a[90:91], v113 offset:50176\n"
    "ds_read_b64_tr_b16 a[92:93], v113 offset:57344\n"
    "ds_read_b64_tr_b16 a[94:95], v113 offset:58368\n"
    "s_waitcnt lgkmcnt(6)\n"
    "ds_read_b64_tr_b16 a[128:129], v114 offset:0\n"
    "ds_read_b64_tr_b16 a[130:131], v114 offset:1024\n"
    "ds_read_b64_tr_b16 a[132:133], v114 offset:8192\n"
    "ds_read_b64_tr_b16 a[134:135], v114 offset:9216\n"
    "ds_read_b64_tr_b16 a[136:137], v114 offset:16384\n"
    "ds_read_b64_tr_b16 a[138:139], v114 offset:17408\n"
    "ds_read_b64_tr_b16 a[140:141], v114 offset:24576\n"
    "ds_read_b64_tr_b16 a[142:143], v114 offset:25600\n"
    "s_waitcnt lgkmcnt(6)\n"
    "ds_read_b64_tr_b16 a[144:145], v114 offset:32768\n"
    "ds_read_b64_tr_b16 a[146:147], v114 offset:33792\n"
    "ds_read_b64_tr_b16 a[148:149], v114 offset:40960\n"
    "ds_read_b64_tr_b16 a[150:151], v114 offset:41984\n"
    "ds_read_b64_tr_b16 a[152:153], v114 offset:49152\n"
    "ds_read_b64_tr_b16 a[154:155], v114 offset:50176\n"
    "ds_read_b64_tr_b16 a[156:157], v114 offset:57344\n"
    "ds_read_b64_tr_b16 a[158:159], v114 offset:58368\n"
    "s_waitcnt lgkmcnt(6)\n"
    "ds_read_b64_tr_b16 a[192:193], v115 offset:0\n"
    "ds_read_b64_tr_b16 a[194:195], v115 offset:1024\n"
    "ds_read_b64_tr_b16 a[196:197], v115 offset:8192\n"
    "ds_read_b64_tr_b16 a[198:199], v115 offset:9216\n"
    "ds_read_b64_tr_b16 a[200:201], v115 offset:16384\n"
    "ds_read_b64_tr_b16 a[202:203], v115 offset:17408\n"
    "ds_read_b64_tr_b16 a[204:205], v115 offset:24576\n"
    "ds_read_b64_tr_b16 a[206:207], v115 offset:25600\n"
    "s_waitcnt lgkmcnt(6)\n"
    "ds_read_b64_tr_b16 a[208:209], v115 offset:32768\n"
    "ds_read_b64_tr_b16 a[210:211], v115 offset:33792\n"
    "ds_read_b64_tr_b16 a[212:213], v115 offset:40960\n"
    "ds_read_b64_tr_b16 a[214:215], v115 offset:41984\n"
    "ds_read_b64_tr_b16 a[216:217], v115 offset:49152\n"
    "ds_read_b64_tr_b16 a[218:219], v115 offset:50176\n"
    "ds_read_b64_tr_b16 a[220:221], v115 offset:57344\n"
    "ds_read_b64_tr_b16 a[222:223], v115 offset:58368\n"
    "s_waitcnt lgkmcnt(6)\n"
    "v_xor_b32 v112, 0, %6\n"
    "v_add_u32 v112, 0x10000, v112\n"
    "v_xor_b32 v113, 32, %6\n"
    "v_add_u32 v113, 0x10000, v113\n"
    "v_xor_b32 v114, 64, %6\n"
    "v_add_u32 v114, 0x10000, v114\n"
    "v_xor_b32 v115, 96, %6\n"
    "v_add_u32 v115, 0x10000, v115\n"
    "ds_read_b64_tr_b16 a[32:33], v112 offset:0\n"
    "ds_read_b64_tr_b16 a[34:35], v112 offset:1024\n"
    "ds_read_b64_tr_b16 a[36:37], v112 offset:8192\n"
    "ds_read_b64_tr_b16 a[38:39], v112 offset:9216\n"
    "ds_read_b64_tr_b16 a[40:41], v112 offset:16384\n"
    "ds_read_b64_tr_b16 a[42:43], v112 offset:17408\n"
    "ds_read_b64_tr_b16 a[44:45], v112 offset:24576\n"
    "ds_read_b64_tr_b16 a[46:47], v112 offset:25600\n"
    "s_waitcnt lgkmcnt(6)\n"
    "ds_read_b64_tr_b16 a[48:49], v112 offset:32768\n"
    "ds_read_b64_tr_b16 a[50:51], v112 offset:33792\n"
    "ds_read_b64_tr_b16 a[52:53], v112 offset:40960\n"
    "ds_read_b64_tr_b16 a[54:55], v112 offset:41984\n"
    "ds_read_b64_tr_b16 a[56:57], v112 offset:49152\n"
    "ds_read_b64_tr_b16 a[58:59], v112 offset:50176\n"
    "ds_read_b64_tr_b16 a[60:61], v112 offset:57344\n"
    "ds_read_b64_tr_b16 a[62:63], v112 offset:58368\n"
    "s_waitcnt lgkmcnt(6)\n"
    "ds_read_b64_tr_b16 a[96:97], v113 offset:0\n"
    "ds_read_b64_tr_b16 a[98:99], v113 offset:1024\n"
    "ds_read_b64_tr_b16 a[100:101], v113 offset:8192\n"
    "ds_read_b64_tr_b16 a[102:103], v113 offset:9216\n"
    "ds_read_b64_tr_b16 a[104:105], v113 offset:16384\n"
    "ds_read_b64_tr_b16 a[106:107], v113 offset:17408\n"
    "ds_read_b64_tr_b16 a[108:109], v113 offset:24576\n"
    "ds_read_b64_tr_b16 a[110:111], v113 offset:25600\n"
    "s_waitcnt lgkmcnt(6)\n"
    "ds_read_b64_tr_b16 a[112:113], v113 offset:32768\n"
    "ds_read_b64_tr_b16 a[114:115], v113 offset:33792\n"
    "ds_read_b64_tr_b16 a[116:117], v113 offset:40960\n"
    "ds_read_b64_tr_b16 a[118:119], v113 offset:41984\n"
    "ds_read_b64_tr_b16 a[120:121], v113 offset:49152\n"
    "ds_read_b64_tr_b16 a[122:123], v113 offset:50176\n"
    "ds_read_b64_tr_b16 a[124:125], v113 offset:57344\n"
    "ds_read_b64_tr_b16 a[126:127], v113 offset:58368\n"
    "s_waitcnt lgkmcnt(6)\n"
    "ds_read_b64_tr_b16 a[160:161], v114 offset:0\n"
    "ds_read_b64_tr_b16 a[162:163], v114 offset:1024\n"
    "ds_read_b64_tr_b16 a[164:165], v114 offset:8192\n"
    "ds_read_b64_tr_b16 a[166:167], v114 offset:9216\n"
    "ds_read_b64_tr_b16 a[168:169], v114 offset:16384\n"
    "ds_read_b64_tr_b16 a[170:171], v114 offset:17408\n"
    "ds_read_b64_tr_b16 a[172:173], v114 offset:24576\n"
    "ds_read_b64_tr_b16 a[174:175], v114 offset:25600\n"
    "s_waitcnt lgkmcnt(6)\n"
    "ds_read_b64_tr_b16 a[176:177], v114 offset:32768\n"
    "ds_read_b64_tr_b16 a[178:179], v114 offset:33792\n"
    "ds_read_b64_tr_b16 a[180:181], v114 offset:40960\n"
    "ds_read_b64_tr_b16 a[182:183], v114 offset:41984\n"
    "ds_read_b64_tr_b16 a[184:185], v114 offset:49152\n"
    "ds_read_b64_tr_b16 a[186:187], v114 offset:50176\n"
    "ds_read_b64_tr_b16 a[188:189], v114 offset:57344\n"
    "ds_read_b64_tr_b16 a[190:191], v114 offset:58368\n"
    "s_waitcnt lgkmcnt(6)\n"
    "ds_read_b64_tr_b16 a[224:225], v115 offset:0\n"
    "ds_read_b64_tr_b16 a[226:227], v115 offset:1024\n"
    "ds_read_b64_tr_b16 a[228:229], v115 offset:8192\n"
    "ds_read_b64_tr_b16 a[230:231], v115 offset:9216\n"
    "ds_read_b64_tr_b16 a[232:233], v115 offset:16384\n"
    "ds_read_b64_tr_b16 a[234:235], v115 offset:17408\n"
    "ds_read_b64_tr_b16 a[236:237], v115 offset:24576\n"
    "ds_read_b64_tr_b16 a[238:239], v115 offset:25600\n"
    "s_waitcnt lgkmcnt(6)\n"
    "ds_read_b64_tr_b16 a[240:241], v115 offset:32768\n"
    "ds_read_b64_tr_b16 a[242:243], v115 offset:33792\n"
    "ds_read_b64_tr_b16 a[244:245], v115 offset:40960\n"
    "ds_read_b64_tr_b16 a[246:247], v115 offset:41984\n"
    "ds_read_b64_tr_b16 a[248:249], v115 offset:49152\n"
    "ds_read_b64_tr_b16 a[250:251], v115 offset:50176\n"
    "ds_read_b64_tr_b16 a[252:253], v115 offset:57344\n"
    "ds_read_b64_tr_b16 a[254:255], v115 offset:58368\n"
    "s_waitcnt lgkmcnt(6)\n"
    "s_lshr_b32 s58, s56, 1\n"
    "s_lshl_b32 s58, s58, 15\n"
    "v_add_u32 v122, s58, v121\n"
    "v_add_u32 v123, 0x10000, v122\n"
    "s_lshl_b32 s50, s56, 14\n"
    "v_mov_b32 v118, 0\n"
    "v_mov_b32 v117, s43\n"
    "s_add_u32 s58, s50, 0x4d00000\n"
    "s_add_u32 s48, s40, s58\n"
    "s_addc_u32 s49, s41, 0\n"
    "s_mov_b32 s57, 0\n"
    "s_mov_b32 s51, 0\n"
    "s_waitcnt lgkmcnt(0)\n"
    "s_barrier\n"
    "s_add_u32 s58, s40, 0x20e000\n"
    "s_addc_u32 s59, s41, 0\n"
    "s_add_u32 s60, s40, 0x200200\n"
    "s_addc_u32 s61, s41, 0\n"
    "s_or_b32 s62, s42, 1\n"
    "s_mov_b32 s63, s43\n"
    "s_branch G_xc0%=\n"
    "G_xw0%=:\n"
    "global_load_dwordx2 v[226:227], v224, s[58:59] sc1\n"
    "G_xc0%=:\n"
    "s_waitcnt vmcnt(0)\n"
    "v_cmp_ne_u64 vcc, s[62:63], v[226:227]\n"
    "s_nop 0\n"
    "s_cbranch_vccz G_xok0%=\n"
    "s_sleep 4\n"
    "s_add_u32 s57, s57, 1\n"
    "s_and_b32 s68, s57, 0xff\n"
    "s_cbranch_scc1 G_xw0%=\n"
    "global_load_dwordx2 v[226:227], v118, s[60:61] sc1\n"
    "s_waitcnt vmcnt(0)\n"
    "v_cmp_eq_u64 vcc, s[42:43], v[226:227]\n"
    "s_nop 0\n"
    "s_cbranch_vccnz G_xok0%=\n"
    "s_cmp_lt_u32 s57, 0x40000\n"
    "s_cbranch_scc1 G_xw0%=\n"
    "v_mov_b32 v116, s42\n"
    "global_store_dwordx2 v118, v[116:117], s[60:61] sc1\n"
    "G_xok0%=:\n"
    "s_mov_b32 vcc_lo, 0xcccccccc\n"
    "s_mov_b32 vcc_hi, 0xcccccccc\n"
    "s_add_u32 m0, s50, 0\n"
    "s_add_u32 s64, s48, 0\n"
    "s_addc_u32 s65, s49, 0\n"
    "global_load_lds_dwordx4 v121, s[64:65] offset:0 sc1\n"
    "global_load_lds_dwordx4 v121, s[64:65] offset:1024 sc1\n"
    "global_load_lds_dwordx4 v121, s[64:65] offset:2048 sc1\n"
    "global_load_lds_dwordx4 v121, s[64:65] offset:3072 sc1\n"
    "s_add_u32 m0, s50, 4096\n"
    "s_add_u32 s64, s48, 4096\n"
    "s_addc_u32 s65, s49, 0\n"
    "global_load_lds_dwordx4 v121, s[64:65] offset:0 sc1\n"
    "global_load_lds_dwordx4 v121, s[64:65] offset:1024 sc1\n"
    "global_load_lds_dwordx4 v121, s[64:65] offset:2048 sc1\n"
    "global_load_lds_dwordx4 v121, s[64:65] offset:3072 sc1\n"
    "s_add_u32 m0, s50, 8192\n"
    "s_add_u32 s64, s48, 8192\n"
    "s_addc_u32 s65, s49, 0\n"
    "global_load_lds_dwordx4 v121, s[64:65] offset:0 sc1\n"
    "global_load_lds_dwordx4 v121, s[64:65] offset:1024 sc1\n"
    "global_load_lds_dwordx4 v121, s[64:65] offset:2048 sc1\n"
    "global_load_lds_dwordx4 v121, s[64:65] offset:3072 sc1\n"
    "s_add_u32 m0, s50, 12288\n"
    "s_add_u32 s64, s48, 12288\n"
    "s_addc_u32 s65, s49, 0\n"
    "s_add_u32 s48, s48, 0x10000\n"
    "s_addc_u32 s49, s49, 0\n"
    "global_load_lds_dwordx4 v121, s[64:65] offset:0 sc1\n"
    "global_load_lds_dwordx4 v121, s[64:65] offset:1024 sc1\n"
    "global_load_lds_dwordx4 v121, s[64:65] offset:2048 sc1\n"
    "global_load_lds_dwordx4 v121, s[64:65] offset:3072 sc1\n"
    "s_add_u32 m0, s50, 65536\n"
    "s_add_u32 s64, s48, 0\n"
    "s_addc_u32 s65, s49, 0\n"
    "global_load_lds_dwordx4 v121, s[64:65] offset:0 sc1\n"
    "global_load_lds_dwordx4 v121, s[64:65] offset:1024 sc1\n"
    "global_load_lds_dwordx4 v121, s[64:65] offset:2048 sc1\n"
    "global_load_lds_dwordx4 v121, s[64:65] offset:3072 sc1\n"
    "s_add_u32 m0, s50, 69632\n"
    "s_add_u32 s64, s48, 4096\n"
    "s_addc_u32 s65, s49, 0\n"
    "global_load_lds_dwordx4 v121, s[64:65] offset:0 sc1\n"
    "global_load_lds_dwordx4 v121, s[64:65] offset:1024 sc1\n"
    "global_load_lds_dwordx4 v121, s[64:65] offset:2048 sc1\n"
    "global_load_lds_dwordx4 v121, s[64:65] offset:3072 sc1\n"
    "s_add_u32 m0, s50, 73728\n"
    "s_add_u32 s64, s48, 8192\n"
    "s_addc_u32 s65, s49, 0\n"
    "global_load_lds_dwordx4 v121, s[64:65] offset:0 sc1\n"
    "global_load_lds_dwordx4 v121, s[64:65] offset:1024 sc1\n"
    "global_load_lds_dwordx4 v121, s[64:65] offset:2048 sc1\n"
    "s_mov_b64 s[66:67], s[44:45]\n"
    "s_add_u32 s44, s44, 0x6f00000\n"
    "s_addc_u32 s45, s45, 0\n"
    "s_waitcnt vmcnt(19) lgkmcnt(0)\n"
    "s_barrier\n"
    "ds_read_b128 v[192:195], v122 offset:0\n"
    "ds_read_b128 v[196:199], v122 offset:16384\n"
    "ds_read_b128 v[200:203], v122 offset:1024\n"
    "ds_read_b128 v[204:207], v122 offset:17408\n"
    "ds_read_b128 v[208:211], v122 offset:2048\n"
    "ds_read_b128 v[212:215], v122 offset:18432\n"
    "s_waitcnt lgkmcnt(4)\n"
    "s_branch G_even%=\n"
    ".p2align 6\n"
    "G_loop%=:\n"
    "v_mfma_f32_16x16x32_bf16 v[160:163], a[0:3], v[192:195], 0\n"
    "ds_read_b128 v[216:219], v123 offset:3072\n"
    "v_mfma_f32_16x16x32_bf16 v[164:167], a[64:67], v[192:195], 0\n"
    "ds_read_b128 v[220:223], v123 offset:19456\n"
    "v_mfma_f32_16x16x32_bf16 v[168:171], a[128:131], v[192:195], 0\n"
    "v_mfma_f32_16x16x32_bf16 v[172:175], a[192:195], v[192:195], 0\n"
    "v_cvt_pk_bf16_f32 v224, v128, v129\n"
    "v_cvt_pk_bf16_f32 v225, v130, v131\n"
    "v_mfma_f32_16x16x32_bf16 v[176:179], a[0:3], v[196:199], 0\n"
    "global_load_lds_dwordx4 v121, s[64:65] offset:3072 sc1\n"
    "v_mfma_f32_16x16x32_bf16 v[180:183], a[64:67], v[196:199], 0\n"
    "v_cvt_pk_bf16_f32 v226, v132, v133\n"
    "v_cvt_pk_bf16_f32 v227, v134, v135\n"
    "v_mfma_f32_16x16x32_bf16 v[184:187], a[128:131], v[196:199], 0\n"
    "v_cvt_pk_bf16_f32 v228, v144, v145\n"
    "v_cvt_pk_bf16_f32 v229, v146, v147\n"
    "v_mfma_f32_16x16x32_bf16 v[188:191], a[192:195], v[196:199], 0\n"
    "v_cvt_pk_bf16_f32 v230, v148, v149\n"
    "v_cvt_pk_bf16_f32 v231, v150, v151\n"
    "s_waitcnt lgkmcnt(4)\n"
    "v_mfma_f32_16x16x32_bf16 v[160:163], a[4:7], v[200:203], v[160:163]\n"
    "ds_read_b128 v[192:195], v123 offset:4096\n"
    "v_mfma_f32_16x16x32_bf16 v[164:167], a[68:71], v[200:203], v[164:167]\n"
    "ds_read_b128 v[196:199], v123 offset:20480\n"
    "v_mfma_f32_16x16x32_bf16 v[168:171], a[132:135], v[200:203], v[168:171]\n"
    "s_add_u32 m0, s50, 12288\n"
    "s_add_u32 s64, s48, 12288\n"
    "s_addc_u32 s65, s49, 0\n"
    "v_cvt_pk_bf16_f32 v232, v136, v137\n"
    "v_cvt_pk_bf16_f32 v233, v138, v139\n"
    "v_mfma_f32_16x16x32_bf16 v[172:175], a[196:199], v[200:203], v[172:175]\n"
    "s_add_u32 s48, s48, 0x10000\n"
    "s_addc_u32 s49, s49, 0\n"
    "v_cvt_pk_bf16_f32 v234, v140, v141\n"
    "v_cvt_pk_bf16_f32 v235, v142, v143\n"
    "v_mfma_f32_16x16x32_bf16 v[176:179], a[4:7], v[204:207], v[176:179]\n"
    "global_load_lds_dwordx4 v121, s[64:65] offset:0 sc1\n"
    "v_mfma_f32_16x16x32_bf16 v[180:183], a[68:71], v[204:207], v[180:183]\n"
    "v_cvt_pk_bf16_f32 v236, v152, v153\n"
    "v_cvt_pk_bf16_f32 v237, v154, v155\n"
    "v_mfma_f32_16x16x32_bf16 v[184:187], a[132:135], v[204:207], v[184:187]\n"
    "global_store_dwordx4 v121, v[224:227], s[44:45] offset:0 sc1\n"
    "v_mfma_f32_16x16x32_bf16 v[188:191], a[196:199], v[204:207], v[188:191]\n"
    "v_cvt_pk_bf16_f32 v238, v156, v157\n"
    "v_cvt_pk_bf16_f32 v239, v158, v159\n"
    "s_waitcnt lgkmcnt(4)\n"
    "v_mfma_f32_16x16x32_bf16 v[160:163], a[8:11], v[208:211], v[160:163]\n"
    "ds_read_b128 v[200:203], v123 offset:5120\n"
    "v_mfma_f32_16x16x32_bf16 v[164:167], a[72:75], v[208:211], v[164:167]\n"
    "ds_read_b128 v[204:207], v123 offset:21504\n"
    "v_mfma_f32_16x16x32_bf16 v[168:171], a[136:139], v[208:211], v[168:171]\n"
    "v_mfma_f32_16x16x32_bf16 v[172:175], a[200:203], v[208:211], v[172:175]\n"
    "v_mfma_f32_16x16x32_bf16 v[176:179], a[8:11], v[212:215], v[176:179]\n"
    "global_load_lds_dwordx4 v121, s[64:65] offset:1024 sc1\n"
    "v_mfma_f32_16x16x32_bf16 v[180:183], a[72:75], v[212:215], v[180:183]\n"
    "v_mfma_f32_16x16x32_bf16 v[184:187], a[136:139], v[212:215], v[184:187]\n"
    "global_store_dwordx4 v121, v[228:231], s[44:45] offset:1024 sc1\n"
    "v_mfma_f32_16x16x32_bf16 v[188:191], a[200:203], v[212:215], v[188:191]\n"
    "s_waitcnt lgkmcnt(4)\n"
    "v_mfma_f32_16x16x32_bf16 v[160:163], a[12:15], v[216:219], v[160:163]\n"
    "ds_read_b128 v[208:211], v123 offset:6144\n"
    "v_mfma_f32_16x16x32_bf16 v[164:167], a[76:79], v[216:219], v[164:167]\n"
    "ds_read_b128 v[212:215], v123 offset:22528\n"
    "v_mfma_f32_16x16x32_bf16 v[168:171], a[140:143], v[216:219], v[168:171]\n"
    "v_mfma_f32_16x16x32_bf16 v[172:175], a[204:207], v[216:219], v[172:175]\n"
    "v_mfma_f32_16x16x32_bf16 v[176:179], a[12:15], v[220:223], v[176:179]\n"
    "global_load_lds_dwordx4 v121, s[64:65] offset:2048 sc1\n"
    "v_mfma_f32_16x16x32_bf16 v[180:183], a[76:79], v[220:223], v[180:183]\n"
    "v_mfma_f32_16x16x32_bf16 v[184:187], a[140:143], v[220:223], v[184:187]\n"
    "global_store_dwordx4 v121, v[232:235], s[44:45] offset:2048 sc1\n"
    "v_mfma_f32_16x16x32_bf16 v[188:191], a[204:207], v[220:223], v[188:191]\n"
    "s_waitcnt lgkmcnt(4)\n"
    "v_mfma_f32_16x16x32_bf16 v[160:163], a[16:19], v[192:195], v[160:163]\n"
    "ds_read_b128 v[216:219], v123 offset:7168\n"
    "v_mfma_f32_16x16x32_bf16 v[164:167], a[80:83], v[192:195], v[164:167]\n"
    "ds_read_b128 v[220:223], v123 offset:23552\n"
    "v_mfma_f32_16x16x32_bf16 v[168:171], a[144:147], v[192:195], v[168:171]\n"
    "v_mfma_f32_16x16x32_bf16 v[172:175], a[208:211], v[192:195], v[172:175]\n"
    "v_mfma_f32_16x16x32_bf16 v[176:179], a[16:19], v[196:199], v[176:179]\n"
    "global_load_lds_dwordx4 v121, s[64:65] offset:3072 sc1\n"
    "v_mfma_f32_16x16x32_bf16 v[180:183], a[80:83], v[196:199], v[180:183]\n"
    "v_mfma_f32_16x16x32_bf16 v[184:187], a[144:147], v[196:199], v[184:187]\n"
    "global_store_dwordx4 v121, v[236:239], s[44:45] offset:3072 sc1\n"
    "v_mfma_f32_16x16x32_bf16 v[188:191], a[208:211], v[196:199], v[188:191]\n"
    "s_waitcnt lgkmcnt(4)\n"
    "v_mfma_f32_16x16x32_bf16 v[160:163], a[20:23], v[200:203], v[160:163]\n"
    "s_waitcnt vmcnt(12) lgkmcnt(0)\n"
    "s_barrier\n"
    "v_mfma_f32_16x16x32_bf16 v[164:167], a[84:87], v[200:203], v[164:167]\n"
    "ds_read_b128 v[192:195], v123 offset:8192\n"
    "v_mfma_f32_16x16x32_bf16 v[168:171], a[148:151], v[200:203], v[168:171]\n"
    "ds_read_b128 v[196:199], v123 offset:24576\n"
    "s_add_u32 m0, s50, 65536\n"
    "s_add_u32 s64, s48, 0\n"
    "s_addc_u32 s65, s49, 0\n"
    "v_mfma_f32_16x16x32_bf16 v[172:175], a[212:215], v[200:203], v[172:175]\n"
    "v_mfma_f32_16x16x32_bf16 v[176:179], a[20:23], v[204:207], v[176:179]\n"
    "global_load_lds_dwordx4 v121, s[64:65] offset:0 sc1\n"
    "v_mfma_f32_16x16x32_bf16 v[180:183], a[84:87], v[204:207], v[180:183]\n"
    "v_mfma_f32_16x16x32_bf16 v[184:187], a[148:151], v[204:207], v[184:187]\n"
    "v_mfma_f32_16x16x32_bf16 v[188:191], a[212:215], v[204:207], v[188:191]\n"
    "s_waitcnt lgkmcnt(4)\n"
    "v_mfma_f32_16x16x32_bf16 v[160:163], a[24:27], v[208:211], v[160:163]\n"
    "ds_read_b128 v[200:203], v123 offset:9216\n"
    "v_mfma_f32_16x16x32_bf16 v[164:167], a[88:91], v[208:211], v[164:167]\n"
    "ds_read_b128 v[204:207], v123 offset:25600\n"
    "v_mfma_f32_16x16x32_bf16 v[168:171], a[152:155], v[208:211], v[168:171]\n"
    "v_mfma_f32_16x16x32_bf16 v[172:175], a[216:219], v[208:211], v[172:175]\n"
    "v_mfma_f32_16x16x32_bf16 v[176:179], a[24:27], v[212:215], v[176:179]\n"
    "global_load_lds_dwordx4 v121, s[64:65] offset:1024 sc1\n"
    "v_mfma_f32_16x16x32_bf16 v[180:183], a[88:91], v[212:215], v[180:183]\n"
    "v_mfma_f32_16x16x32_bf16 v[184:187], a[152:155], v[212:215], v[184:187]\n"
    "v_mfma_f32_16x16x32_bf16 v[188:191], a[216:219], v[212:215], v[188:191]\n"
    "s_waitcnt lgkmcnt(4)\n"
    "v_mfma_f32_16x16x32_bf16 v[160:163], a[28:31], v[216:219], v[160:163]\n"
    "ds_read_b128 v[208:211], v123 offset:10240\n"
    "v_mfma_f32_16x16x32_bf16 v[164:167], a[92:95], v[216:219], v[164:167]\n"
    "ds_read_b128 v[212:215], v123 offset:26624\n"
    "v_mfma_f32_16x16x32_bf16 v[168:171], a[156:159], v[216:219], v[168:171]\n"
    "v_mfma_f32_16x16x32_bf16 v[172:175], a[220:223], v[216:219], v[172:175]\n"
    "v_mfma_f32_16x16x32_bf16 v[176:179], a[28:31], v[220:223], v[176:179]\n"
    "global_load_lds_dwordx4 v121, s[64:65] offset:2048 sc1\n"
    "v_mfma_f32_16x16x32_bf16 v[180:183], a[92:95], v[220:223], v[180:183]\n"
    "s_mov_b64 s[44:45], s[66:67]\n"
    "s_add_u32 s66, s66, 0x80000\n"
    "s_addc_u32 s67, s67, 0\n"
    "v_mfma_f32_16x16x32_bf16 v[184:187], a[156:159], v[220:223], v[184:187]\n"
    "v_mfma_f32_16x16x32_bf16 v[188:191], a[220:223], v[220:223], v[188:191]\n"
    "s_waitcnt lgkmcnt(4)\n"
    "v_mfma_f32_16x16x32_bf16 v[160:163], a[32:35], v[192:195], v[160:163]\n"
    "ds_read_b128 v[216:219], v123 offset:11264\n"
    "v_mfma_f32_16x16x32_bf16 v[164:167], a[96:99], v[192:195], v[164:167]\n"
    "ds_read_b128 v[220:223], v123 offset:27648\n"
    "v_mfma_f32_16x16x32_bf16 v[168:171], a[160:163], v[192:195], v[168:171]\n"
    "v_mfma_f32_16x16x32_bf16 v[172:175], a[224:227], v[192:195], v[172:175]\n"
    "v_mfma_f32_16x16x32_bf16 v[176:179], a[32:35], v[196:199], v[176:179]\n"
    "global_load_lds_dwordx4 v121, s[64:65] offset:3072 sc1\n"
    "v_mfma_f32_16x16x32_bf16 v[180:183], a[96:99], v[196:199], v[180:183]\n"
    "v_mfma_f32_16x16x32_bf16 v[184:187], a[160:163], v[196:199], v[184:187]\n"
    "v_mfma_f32_16x16x32_bf16 v[188:191], a[224:227], v[196:199], v[188:191]\n"
    "s_waitcnt lgkmcnt(4)\n"
    "v_mfma_f32_16x16x32_bf16 v[160:163], a[36:39], v[200:203], v[160:163]\n"
    "ds_read_b128 v[192:195], v123 offset:12288\n"
    "v_mfma_f32_16x16x32_bf16 v[164:167], a[100:103], v[200:203], v[164:167]\n"
    "ds_read_b128 v[196:199], v123 offset:28672\n"
    "v_mfma_f32_16x16x32_bf16 v[168:171], a[164:167], v[200:203], v[168:171]\n"
    "s_add_u32 m0, s50, 69632\n"
    "s_add_u32 s64, s48, 4096\n"
    "s_addc_u32 s65, s49, 0\n"
    "v_mfma_f32_16x16x32_bf16 v[172:175], a[228:231], v[200:203], v[172:175]\n"
    "v_mfma_f32_16x16x32_bf16 v[176:179], a[36:39], v[204:207], v[176:179]\n"
    "global_load_lds_dwordx4 v121, s[64:65] offset:0 sc1\n"
    "v_mfma_f32_16x16x32_bf16 v[180:183], a[100:103], v[204:207], v[180:183]\n"
    "v_mfma_f32_16x16x32_bf16 v[184:187], a[164:167], v[204:207], v[184:187]\n"
    "v_mfma_f32_16x16x32_bf16 v[188:191], a[228:231], v[204:207], v[188:191]\n"
    "s_waitcnt lgkmcnt(4)\n"
    "v_mfma_f32_16x16x32_bf16 v[160:163], a[40:43], v[208:211], v[160:163]\n"
    "ds_read_b128 v[200:203], v123 offset:13312\n"
    "v_mfma_f32_16x16x32_bf16 v[164:167], a[104:107], v[208:211], v[164:167]\n"
    "ds_read_b128 v[204:207], v123 offset:29696\n"
    "v_mfma_f32_16x16x32_bf16 v[168:171], a[168:171], v[208:211], v[168:171]\n"
    "v_mfma_f32_16x16x32_bf16 v[172:175], a[232:235], v[208:211], v[172:175]\n"
    "v_mfma_f32_16x16x32_bf16 v[176:179], a[40:43], v[212:215], v[176:179]\n"
    "global_load_lds_dwordx4 v121, s[64:65] offset:1024 sc1\n"
    "v_mfma_f32_16x16x32_bf16 v[180:183], a[104:107], v[212:215], v[180:183]\n"
    "v_mfma_f32_16x16x32_bf16 v[184:187], a[168:171], v[212:215], v[184:187]\n"
    "v_mfma_f32_16x16x32_bf16 v[188:191], a[232:235], v[212:215], v[188:191]\n"
    "s_waitcnt lgkmcnt(4)\n"
    "v_mfma_f32_16x16x32_bf16 v[160:163], a[44:47], v[216:219], v[160:163]\n"
    "ds_read_b128 v[208:211], v123 offset:14336\n"
    "v_mfma_f32_16x16x32_bf16 v[164:167], a[108:111], v[216:219], v[164:167]\n"
    "ds_read_b128 v[212:215], v123 offset:30720\n"
    "v_mfma_f32_16x16x32_bf16 v[168:171], a[172:175], v[216:219], v[168:171]\n"
    "v_mfma_f32_16x16x32_bf16 v[172:175], a[236:239], v[216:219], v[172:175]\n"
    "v_mfma_f32_16x16x32_bf16 v[176:179], a[44:47], v[220:223], v[176:179]\n"
    "global_load_lds_dwordx4 v121, s[64:65] offset:2048 sc1\n"
    "v_mfma_f32_16x16x32_bf16 v[180:183], a[108:111], v[220:223], v[180:183]\n"
    "v_mfma_f32_16x16x32_bf16 v[184:187], a[172:175], v[220:223], v[184:187]\n"
    "v_mfma_f32_16x16x32_bf16 v[188:191], a[236:239], v[220:223], v[188:191]\n"
    "s_waitcnt lgkmcnt(4)\n"
    "v_mfma_f32_16x16x32_bf16 v[160:163], a[48:51], v[192:195], v[160:163]\n"
    "ds_read_b128 v[216:219], v123 offset:15360\n"
    "v_mfma_f32_16x16x32_bf16 v[164:167], a[112:115], v[192:195], v[164:167]\n"
    "ds_read_b128 v[220:223], v123 offset:31744\n"
    "v_mfma_f32_16x16x32_bf16 v[168:171], a[176:179], v[192:195], v[168:171]\n"
    "v_mfma_f32_16x16x32_bf16 v[172:175], a[240:243], v[192:195], v[172:175]\n"
    "v_mfma_f32_16x16x32_bf16 v[176:179], a[48:51], v[196:199], v[176:179]\n"
    "global_load_lds_dwordx4 v121, s[64:65] offset:3072 sc1\n"
    "v_mfma_f32_16x16x32_bf16 v[180:183], a[112:115], v[196:199], v[180:183]\n"
    "v_mfma_f32_16x16x32_bf16 v[184:187], a[176:179], v[196:199], v[184:187]\n"
    "v_mfma_f32_16x16x32_bf16 v[188:191], a[240:243], v[196:199], v[188:191]\n"
    "s_waitcnt lgkmcnt(4)\n"
    "v_mfma_f32_16x16x32_bf16 v[160:163], a[52:55], v[200:203], v[160:163]\n"
    "s_waitcnt vmcnt(8) lgkmcnt(0)\n"
    "s_barrier\n"
    "v_mfma_f32_16x16x32_bf16 v[164:167], a[116:119], v[200:203], v[164:167]\n"
    "ds_read_b128 v[192:195], v122 offset:0\n"
    "v_mfma_f32_16x16x32_bf16 v[168:171], a[180:183], v[200:203], v[168:171]\n"
    "ds_read_b128 v[196:199], v122 offset:16384\n"
    "s_add_u32 m0, s50, 73728\n"
    "s_add_u32 s64, s48, 8192\n"
    "s_addc_u32 s65, s49, 0\n"
    "v_mfma_f32_16x16x32_bf16 v[172:175], a[244:247], v[200:203], v[172:175]\n"
    "v_mfma_f32_16x16x32_bf16 v[176:179], a[52:55], v[204:207], v[176:179]\n"
    "global_load_lds_dwordx4 v121, s[64:65] offset:0 sc1\n"
    "v_mfma_f32_16x16x32_bf16 v[180:183], a[116:119], v[204:207], v[180:183]\n"
    "s_cmp_lg_u32 s56, 0\n"
    "s_cbranch_scc1 G_f1%=\n"
    "s_or_b32 s58, s42, s51\n"
    "s_mov_b64 exec, 1\n"
    "v_mov_b32 v116, s58\n"
    "global_store_dwordx2 v118, v[116:117], s[54:55] sc1\n"
    "s_mov_b64 exec, -1\n"
    "G_f1%=:\n"
    "v_mfma_f32_16x16x32_bf16 v[184:187], a[180:183], v[204:207], v[184:187]\n"
    "v_mfma_f32_16x16x32_bf16 v[188:191], a[244:247], v[204:207], v[188:191]\n"
    "s_waitcnt lgkmcnt(4)\n"
    "v_mfma_f32_16x16x32_bf16 v[160:163], a[56:59], v[208:211], v[160:163]\n"
    "ds_read_b128 v[200:203], v122 offset:1024\n"
    "v_mfma_f32_16x16x32_bf16 v[164:167], a[120:123], v[208:211], v[164:167]\n"
    "ds_read_b128 v[204:207], v122 offset:17408\n"
    "v_mfma_f32_16x16x32_bf16 v[168:171], a[184:187], v[208:211], v[168:171]\n"
    "v_mfma_f32_16x16x32_bf16 v[172:175], a[248:251], v[208:211], v[172:175]\n"
    "v_mfma_f32_16x16x32_bf16 v[176:179], a[56:59], v[212:215], v[176:179]\n"
    "global_load_lds_dwordx4 v121, s[64:65] offset:1024 sc1\n"
    "v_mfma_f32_16x16x32_bf16 v[180:183], a[120:123], v[212:215], v[180:183]\n"
    "v_mfma_f32_16x16x32_bf16 v[184:187], a[184:187], v[212:215], v[184:187]\n"
    "v_mfma_f32_16x16x32_bf16 v[188:191], a[248:251], v[212:215], v[188:191]\n"
    "s_add_u32 s51, s51, 1\n"
    "s_waitcnt lgkmcnt(4)\n"
    "v_mfma_f32_16x16x32_bf16 v[160:163], a[60:63], v[216:219], v[160:163]\n"
    "ds_read_b128 v[208:211], v122 offset:2048\n"
    "v_mfma_f32_16x16x32_bf16 v[164:167], a[124:127], v[216:219], v[164:167]\n"
    "ds_read_b128 v[212:215], v122 offset:18432\n"
    "v_mfma_f32_16x16x32_bf16 v[168:171], a[188:191], v[216:219], v[168:171]\n"
    "v_mfma_f32_16x16x32_bf16 v[172:175], a[252:255], v[216:219], v[172:175]\n"
    "v_mfma_f32_16x16x32_bf16 v[176:179], a[60:63], v[220:223], v[176:179]\n"
    "global_load_lds_dwordx4 v121, s[64:65] offset:2048 sc1\n"
    "v_mfma_f32_16x16x32_bf16 v[180:183], a[124:127], v[220:223], v[180:183]\n"
    "v_mfma_f32_16x16x32_bf16 v[184:187], a[188:191], v[220:223], v[184:187]\n"
    "v_mfma_f32_16x16x32_bf16 v[188:191], a[252:255], v[220:223], v[188:191]\n"
    "s_add_u32 s58, s51, -3\n"
    "s_cmp_gt_u32 s58, 28\n"
    "s_waitcnt lgkmcnt(4)\n"
    "s_cbranch_scc1 G_t1%=\n"
    "G_even%=:\n"
    "v_mfma_f32_16x16x32_bf16 v[128:131], a[0:3], v[192:195], 0\n"
    "ds_read_b128 v[216:219], v122 offset:3072\n"
    "v_mfma_f32_16x16x32_bf16 v[132:135], a[64:67], v[192:195], 0\n"
    "ds_read_b128 v[220:223], v122 offset:19456\n"
    "v_mfma_f32_16x16x32_bf16 v[136:139], a[128:131], v[192:195], 0\n"
    "v_mfma_f32_16x16x32_bf16 v[140:143], a[192:195], v[192:195], 0\n"
    "v_cvt_pk_bf16_f32 v224, v160, v161\n"
    "v_cvt_pk_bf16_f32 v225, v162, v163\n"
    "v_mfma_f32_16x16x32_bf16 v[144:147], a[0:3], v[196:199], 0\n"
    "global_load_lds_dwordx4 v121, s[64:65] offset:3072 sc1\n"
    "v_mfma_f32_16x16x32_bf16 v[148:151], a[64:67], v[196:199], 0\n"
    "v_cvt_pk_bf16_f32 v226, v164, v165\n"
    "v_cvt_pk_bf16_f32 v227, v166, v167\n"
    "v_mfma_f32_16x16x32_bf16 v[152:155], a[128:131], v[196:199], 0\n"
    "v_cvt_pk_bf16_f32 v228, v176, v177\n"
    "v_cvt_pk_bf16_f32 v229, v178, v179\n"
    "v_mfma_f32_16x16x32_bf16 v[156:159], a[192:195], v[196:199], 0\n"
    "v_cvt_pk_bf16_f32 v230, v180, v181\n"
    "v_cvt_pk_bf16_f32 v231, v182, v183\n"
    "s_waitcnt lgkmcnt(4)\n"
    "v_mfma_f32_16x16x32_bf16 v[128:131], a[4:7], v[200:203], v[128:131]\n"
    "ds_read_b128 v[192:195], v122 offset:4096\n"
    "v_mfma_f32_16x16x32_bf16 v[132:135], a[68:71], v[200:203], v[132:135]\n"
    "ds_read_b128 v[196:199], v122 offset:20480\n"
    "v_mfma_f32_16x16x32_bf16 v[136:139], a[132:135], v[200:203], v[136:139]\n"
    "s_add_u32 m0, s50, 77824\n"
    "s_add_u32 s64, s48, 12288\n"
    "s_addc_u32 s65, s49, 0\n"
    "v_cvt_pk_bf16_f32 v232, v168, v169\n"
    "v_cvt_pk_bf16_f32 v233, v170, v171\n"
    "v_mfma_f32_16x16x32_bf16 v[140:143], a[196:199], v[200:203], v[140:143]\n"
    "s_add_u32 s48, s48, 0x10000\n"
    "s_addc_u32 s49, s49, 0\n"
    "v_cvt_pk_bf16_f32 v234, v172, v173\n"
    "v_cvt_pk_bf16_f32 v235, v174, v175\n"
    "v_mfma_f32_16x16x32_bf16 v[144:147], a[4:7], v[204:207], v[144:147]\n"
    "global_load_lds_dwordx4 v121, s[64:65] offset:0 sc1\n"
    "v_mfma_f32_16x16x32_bf16 v[148:151], a[68:71], v[204:207], v[148:151]\n"
    "v_cvt_pk_bf16_f32 v236, v184, v185\n"
    "v_cvt_pk_bf16_f32 v237, v186, v187\n"
    "v_mfma_f32_16x16x32_bf16 v[152:155], a[132:135], v[204:207], v[152:155]\n"
    "global_store_dwordx4 v121, v[224:227], s[44:45] offset:0 sc1\n"
    "v_mfma_f32_16x16x32_bf16 v[156:159], a[196:199], v[204:207], v[156:159]\n"
    "v_cvt_pk_bf16_f32 v238, v188, v189\n"
    "v_cvt_pk_bf16_f32 v239, v190, v191\n"
    "s_waitcnt lgkmcnt(4)\n"
    "v_mfma_f32_16x16x32_bf16 v[128:131], a[8:11], v[208:211], v[128:131]\n"
    "ds_read_b128 v[200:203], v122 offset:5120\n"
    "v_mfma_f32_16x16x32_bf16 v[132:135], a[72:75], v[208:211], v[132:135]\n"
    "ds_read_b128 v[204:207], v122 offset:21504\n"
    "v_mfma_f32_16x16x32_bf16 v[136:139], a[136:139], v[208:211], v[136:139]\n"
    "v_mfma_f32_16x16x32_bf16 v[140:143], a[200:203], v[208:211], v[140:143]\n"
    "v_mfma_f32_16x16x32_bf16 v[144:147], a[8:11], v[212:215], v[144:147]\n"
    "global_load_lds_dwordx4 v121, s[64:65] offset:1024 sc1\n"
    "v_mfma_f32_16x16x32_bf16 v[148:151], a[72:75], v[212:215], v[148:151]\n"
    "v_mfma_f32_16x16x32_bf16 v[152:155], a[136:139], v[212:215], v[152:155]\n"
    "global_store_dwordx4 v121, v[228:231], s[44:45] offset:1024 sc1\n"
    "v_mfma_f32_16x16x32_bf16 v[156:159], a[200:203], v[212:215], v[156:159]\n"
    "s_waitcnt lgkmcnt(4)\n"
    "v_mfma_f32_16x16x32_bf16 v[128:131], a[12:15], v[216:219], v[128:131]\n"
    "ds_read_b128 v[208:211], v122 offset:6144\n"
    "v_mfma_f32_16x16x32_bf16 v[132:135], a[76:79], v[216:219], v[132:135]\n"
    "ds_read_b128 v[212:215], v122 offset:22528\n"
    "v_mfma_f32_16x16x32_bf16 v[136:139], a[140:143], v[216:219], v[136:139]\n"
    "v_mfma_f32_16x16x32_bf16 v[140:143], a[204:207], v[216:219], v[140:143]\n"
    "v_mfma_f32_16x16x32_bf16 v[144:147], a[12:15], v[220:223], v[144:147]\n"
    "global_load_lds_dwordx4 v121, s[64:65] offset:2048 sc1\n"
    "v_mfma_f32_16x16x32_bf16 v[148:151], a[76:79], v[220:223], v[148:151]\n"
    "v_mfma_f32_16x16x32_bf16 v[152:155], a[140:143], v[220:223], v[152:155]\n"
    "global_store_dwordx4 v121, v[232:235], s[44:45] offset:2048 sc1\n"
    "v_mfma_f32_16x16x32_bf16 v[156:159], a[204:207], v[220:223], v[156:159]\n"
    "s_waitcnt lgkmcnt(4)\n"
    "v_mfma_f32_16x16x32_bf16 v[128:131], a[16:19], v[192:195], v[128:131]\n"
    "ds_read_b128 v[216:219], v122 offset:7168\n"
    "v_mfma_f32_16x16x32_bf16 v[132:135], a[80:83], v[192:195], v[132:135]\n"
    "ds_read_b128 v[220:223], v122 offset:23552\n"
    "v_mfma_f32_16x16x32_bf16 v[136:139], a[144:147], v[192:195], v[136:139]\n"
    "v_mfma_f32_16x16x32_bf16 v[140:143], a[208:211], v[192:195], v[140:143]\n"
    "v_mfma_f32_16x16x32_bf16 v[144:147], a[16:19], v[196:199], v[144:147]\n"
    "global_load_lds_dwordx4 v121, s[64:65] offset:3072 sc1\n"
    "v_mfma_f32_16x16x32_bf16 v[148:151], a[80:83], v[196:199], v[148:151]\n"
    "v_mfma_f32_16x16x32_bf16 v[152:155], a[144:147], v[196:199], v[152:155]\n"
    "global_store_dwordx4 v121, v[236:239], s[44:45] offset:3072 sc1\n"
    "v_mfma_f32_16x16x32_bf16 v[156:159], a[208:211], v[196:199], v[156:159]\n"
    "s_waitcnt lgkmcnt(4)\n"
    "v_mfma_f32_16x16x32_bf16 v[128:131], a[20:23], v[200:203], v[128:131]\n"
    "s_waitcnt vmcnt(12) lgkmcnt(0)\n"
    "s_barrier\n"
    "v_mfma_f32_16x16x32_bf16 v[132:135], a[84:87], v[200:203], v[132:135]\n"
    "ds_read_b128 v[192:195], v122 offset:8192\n"
    "v_mfma_f32_16x16x32_bf16 v[136:139], a[148:151], v[200:203], v[136:139]\n"
    "ds_read_b128 v[196:199], v122 offset:24576\n"
    "s_add_u32 m0, s50, 0\n"
    "s_add_u32 s64, s48, 0\n"
    "s_addc_u32 s65, s49, 0\n"
    "v_mfma_f32_16x16x32_bf16 v[140:143], a[212:215], v[200:203], v[140:143]\n"
    "v_mfma_f32_16x16x32_bf16 v[144:147], a[20:23], v[204:207], v[144:147]\n"
    "global_load_lds_dwordx4 v121, s[64:65] offset:0 sc1\n"
    "v_mfma_f32_16x16x32_bf16 v[148:151], a[84:87], v[204:207], v[148:151]\n"
    "v_mfma_f32_16x16x32_bf16 v[152:155], a[148:151], v[204:207], v[152:155]\n"
    "v_mfma_f32_16x16x32_bf16 v[156:159], a[212:215], v[204:207], v[156:159]\n"
    "s_waitcnt lgkmcnt(4)\n"
    "v_mfma_f32_16x16x32_bf16 v[128:131], a[24:27], v[208:211], v[128:131]\n"
    "ds_read_b128 v[200:203], v122 offset:9216\n"
    "v_mfma_f32_16x16x32_bf16 v[132:135], a[88:91], v[208:211], v[132:135]\n"
    "ds_read_b128 v[204:207], v122 offset:25600\n"
    "v_mfma_f32_16x16x32_bf16 v[136:139], a[152:155], v[208:211], v[136:139]\n"
    "v_mfma_f32_16x16x32_bf16 v[140:143], a[216:219], v[208:211], v[140:143]\n"
    "v_mfma_f32_16x16x32_bf16 v[144:147], a[24:27], v[212:215], v[144:147]\n"
    "global_load_lds_dwordx4 v121, s[64:65] offset:1024 sc1\n"
    "v_mfma_f32_16x16x32_bf16 v[148:151], a[88:91], v[212:215], v[148:151]\n"
    "v_mfma_f32_16x16x32_bf16 v[152:155], a[152:155], v[212:215], v[152:155]\n"
    "v_mfma_f32_16x16x32_bf16 v[156:159], a[216:219], v[212:215], v[156:159]\n"
    "s_waitcnt lgkmcnt(4)\n"
    "v_mfma_f32_16x16x32_bf16 v[128:131], a[28:31], v[216:219], v[128:131]\n"
    "ds_read_b128 v[208:211], v122 offset:10240\n"
    "v_mfma_f32_16x16x32_bf16 v[132:135], a[92:95], v[216:219], v[132:135]\n"
    "ds_read_b128 v[212:215], v122 offset:26624\n"
    "v_mfma_f32_16x16x32_bf16 v[136:139], a[156:159], v[216:219], v[136:139]\n"
    "v_mfma_f32_16x16x32_bf16 v[140:143], a[220:223], v[216:219], v[140:143]\n"
    "v_mfma_f32_16x16x32_bf16 v[144:147], a[28:31], v[220:223], v[144:147]\n"
    "global_load_lds_dwordx4 v121, s[64:65] offset:2048 sc1\n"
    "v_mfma_f32_16x16x32_bf16 v[148:151], a[92:95], v[220:223], v[148:151]\n"
    "s_mov_b64 s[44:45], s[66:67]\n"
    "s_add_u32 s66, s66, 0x80000\n"
    "s_addc_u32 s67, s67, 0\n"
    "v_mfma_f32_16x16x32_bf16 v[152:155], a[156:159], v[220:223], v[152:155]\n"
    "v_mfma_f32_16x16x32_bf16 v[156:159], a[220:223], v[220:223], v[156:159]\n"
    "s_waitcnt lgkmcnt(4)\n"
    "v_mfma_f32_16x16x32_bf16 v[128:131], a[32:35], v[192:195], v[128:131]\n"
    "ds_read_b128 v[216:219], v122 offset:11264\n"
    "v_mfma_f32_16x16x32_bf16 v[132:135], a[96:99], v[192:195], v[132:135]\n"
    "ds_read_b128 v[220:223], v122 offset:27648\n"
    "v_mfma_f32_16x16x32_bf16 v[136:139], a[160:163], v[192:195], v[136:139]\n"
    "v_mfma_f32_16x16x32_bf16 v[140:143], a[224:227], v[192:195], v[140:143]\n"
    "v_mfma_f32_16x16x32_bf16 v[144:147], a[32:35], v[196:199], v[144:147]\n"
    "global_load_lds_dwordx4 v121, s[64:65] offset:3072 sc1\n"
    "v_mfma_f32_16x16x32_bf16 v[148:151], a[96:99], v[196:199], v[148:151]\n"
    "v_mfma_f32_16x16x32_bf16 v[152:155], a[160:163], v[196:199], v[152:155]\n"
    "v_mfma_f32_16x16x32_bf16 v[156:159], a[224:227], v[196:199], v[156:159]\n"
    "s_waitcnt lgkmcnt(4)\n"
    "v_mfma_f32_16x16x32_bf16 v[128:131], a[36:39], v[200:203], v[128:131]\n"
    "ds_read_b128 v[192:195], v122 offset:12288\n"
    "v_mfma_f32_16x16x32_bf16 v[132:135], a[100:103], v[200:203], v[132:135]\n"
    "ds_read_b128 v[196:199], v122 offset:28672\n"
    "v_mfma_f32_16x16x32_bf16 v[136:139], a[164:167], v[200:203], v[136:139]\n"
    "s_add_u32 m0, s50, 4096\n"
    "s_add_u32 s64, s48, 4096\n"
    "s_addc_u32 s65, s49, 0\n"
    "v_mfma_f32_16x16x32_bf16 v[140:143], a[228:231], v[200:203], v[140:143]\n"
    "v_mfma_f32_16x16x32_bf16 v[144:147], a[36:39], v[204:207], v[144:147]\n"
    "global_load_lds_dwordx4 v121, s[64:65] offset:0 sc1\n"
    "v_mfma_f32_16x16x32_bf16 v[148:151], a[100:103], v[204:207], v[148:151]\n"
    "v_mfma_f32_16x16x32_bf16 v[152:155], a[164:167], v[204:207], v[152:155]\n"
    "v_mfma_f32_16x16x32_bf16 v[156:159], a[228:231], v[204:207], v[156:159]\n"
    "s_waitcnt lgkmcnt(4)\n"
    "v_mfma_f32_16x16x32_bf16 v[128:131], a[40:43], v[208:211], v[128:131]\n"
    "ds_read_b128 v[200:203], v122 offset:13312\n"
    "v_mfma_f32_16x16x32_bf16 v[132:135], a[104:107], v[208:211], v[132:135]\n"
    "ds_read_b128 v[204:207], v122 offset:29696\n"
    "v_mfma_f32_16x16x32_bf16 v[136:139], a[168:171], v[208:211], v[136:139]\n"
    "v_mfma_f32_16x16x32_bf16 v[140:143], a[232:235], v[208:211], v[140:143]\n"
    "v_mfma_f32_16x16x32_bf16 v[144:147], a[40:43], v[212:215], v[144:147]\n"
    "global_load_lds_dwordx4 v121, s[64:65] offset:1024 sc1\n"
    "v_mfma_f32_16x16x32_bf16 v[148:151], a[104:107], v[212:215], v[148:151]\n"
    "v_mfma_f32_16x16x32_bf16 v[152:155], a[168:171], v[212:215], v[152:155]\n"
    "v_mfma_f32_16x16x32_bf16 v[156:159], a[232:235], v[212:215], v[156:159]\n"
    "s_waitcnt lgkmcnt(4)\n"
    "v_mfma_f32_16x16x32_bf16 v[128:131], a[44:47], v[216:219], v[128:131]\n"
    "ds_read_b128 v[208:211], v122 offset:14336\n"
    "v_mfma_f32_16x16x32_bf16 v[132:135], a[108:111], v[216:219], v[132:135]\n"
    "ds_read_b128 v[212:215], v122 offset:30720\n"
    "v_mfma_f32_16x16x32_bf16 v[136:139], a[172:175], v[216:219], v[136:139]\n"
    "v_mfma_f32_16x16x32_bf16 v[140:143], a[236:239], v[216:219], v[140:143]\n"
    "v_mfma_f32_16x16x32_bf16 v[144:147], a[44:47], v[220:223], v[144:147]\n"
    "global_load_lds_dwordx4 v121, s[64:65] offset:2048 sc1\n"
    "v_mfma_f32_16x16x32_bf16 v[148:151], a[108:111], v[220:223], v[148:151]\n"
    "v_mfma_f32_16x16x32_bf16 v[152:155], a[172:175], v[220:223], v[152:155]\n"
    "v_mfma_f32_16x16x32_bf16 v[156:159], a[236:239], v[220:223], v[156:159]\n"
    "s_waitcnt lgkmcnt(4)\n"
    "v_mfma_f32_16x16x32_bf16 v[128:131], a[48:51], v[192:195], v[128:131]\n"
    "ds_read_b128 v[216:219], v122 offset:15360\n"
    "v_mfma_f32_16x16x32_bf16 v[132:135], a[112:115], v[192:195], v[132:135]\n"
    "ds_read_b128 v[220:223], v122 offset:31744\n"
    "v_mfma_f32_16x16x32_bf16 v[136:139], a[176:179], v[192:195], v[136:139]\n"
    "v_mfma_f32_16x16x32_bf16 v[140:143], a[240:243], v[192:195], v[140:143]\n"
    "v_mfma_f32_16x16x32_bf16 v[144:147], a[48:51], v[196:199], v[144:147]\n"
    "global_load_lds_dwordx4 v121, s[64:65] offset:3072 sc1\n"
    "v_mfma_f32_16x16x32_bf16 v[148:151], a[112:115], v[196:199], v[148:151]\n"
    "v_mfma_f32_16x16x32_bf16 v[152:155], a[176:179], v[196:199], v[152:155]\n"
    "v_mfma_f32_16x16x32_bf16 v[156:159], a[240:243], v[196:199], v[156:159]\n"
    "s_waitcnt lgkmcnt(4)\n"
    "v_mfma_f32_16x16x32_bf16 v[128:131], a[52:55], v[200:203], v[128:131]\n"
    "s_waitcnt vmcnt(8) lgkmcnt(0)\n"
    "s_barrier\n"
    "v_mfma_f32_16x16x32_bf16 v[132:135], a[116:119], v[200:203], v[132:135]\n"
    "ds_read_b128 v[192:195], v123 offset:0\n"
    "v_mfma_f32_16x16x32_bf16 v[136:139], a[180:183], v[200:203], v[136:139]\n"
    "ds_read_b128 v[196:199], v123 offset:16384\n"
    "s_add_u32 m0, s50, 8192\n"
    "s_add_u32 s64, s48, 8192\n"
    "s_addc_u32 s65, s49, 0\n"
    "v_mfma_f32_16x16x32_bf16 v[140:143], a[244:247], v[200:203], v[140:143]\n"
    "v_mfma_f32_16x16x32_bf16 v[144:147], a[52:55], v[204:207], v[144:147]\n"
    "global_load_lds_dwordx4 v121, s[64:65] offset:0 sc1\n"
    "v_mfma_f32_16x16x32_bf16 v[148:151], a[116:119], v[204:207], v[148:151]\n"
    "s_cmp_lg_u32 s56, 0\n"
    "s_cbranch_scc1 G_f2%=\n"
    "s_or_b32 s58, s42, s51\n"
    "s_mov_b64 exec, 1\n"
    "v_mov_b32 v116, s58\n"
    "global_store_dwordx2 v118, v[116:117], s[54:55] sc1\n"
    "s_mov_b64 exec, -1\n"
    "G_f2%=:\n"
    "v_mfma_f32_16x16x32_bf16 v[152:155], a[180:183], v[204:207], v[152:155]\n"
    "v_mfma_f32_16x16x32_bf16 v[156:159], a[244:247], v[204:207], v[156:159]\n"
    "s_waitcnt lgkmcnt(4)\n"
    "v_mfma_f32_16x16x32_bf16 v[128:131], a[56:59], v[208:211], v[128:131]\n"
    "ds_read_b128 v[200:203], v123 offset:1024\n"
    "v_mfma_f32_16x16x32_bf16 v[132:135], a[120:123], v[208:211], v[132:135]\n"
    "ds_read_b128 v[204:207], v123 offset:17408\n"
    "v_mfma_f32_16x16x32_bf16 v[136:139], a[184:187], v[208:211], v[136:139]\n"
    "v_mfma_f32_16x16x32_bf16 v[140:143], a[248:251], v[208:211], v[140:143]\n"
    "v_mfma_f32_16x16x32_bf16 v[144:147], a[56:59], v[212:215], v[144:147]\n"
    "global_load_lds_dwordx4 v121, s[64:65] offset:1024 sc1\n"
    "v_mfma_f32_16x16x32_bf16 v[148:151], a[120:123], v[212:215], v[148:151]\n"
    "v_mfma_f32_16x16x32_bf16 v[152:155], a[184:187], v[212:215], v[152:155]\n"
    "v_mfma_f32_16x16x32_bf16 v[156:159], a[248:251], v[212:215], v[156:159]\n"
    "s_add_u32 s51, s51, 1\n"
    "s_waitcnt lgkmcnt(4)\n"
    "v_mfma_f32_16x16x32_bf16 v[128:131], a[60:63], v[216:219], v[128:131]\n"
    "ds_read_b128 v[208:211], v123 offset:2048\n"
    "v_mfma_f32_16x16x32_bf16 v[132:135], a[124:127], v[216:219], v[132:135]\n"
    "ds_read_b128 v[212:215], v123 offset:18432\n"
    "v_mfma_f32_16x16x32_bf16 v[136:139], a[188:191], v[216:219], v[136:139]\n"
    "v_mfma_f32_16x16x32_bf16 v[140:143], a[252:255], v[216:219], v[140:143]\n"
    "v_mfma_f32_16x16x32_bf16 v[144:147], a[60:63], v[220:223], v[144:147]\n"
    "global_load_lds_dwordx4 v121, s[64:65] offset:2048 sc1\n"
    "v_mfma_f32_16x16x32_bf16 v[148:151], a[124:127], v[220:223], v[148:151]\n"
    "v_mfma_f32_16x16x32_bf16 v[152:155], a[188:191], v[220:223], v[152:155]\n"
    "v_mfma_f32_16x16x32_bf16 v[156:159], a[252:255], v[220:223], v[156:159]\n"
    "s_add_u32 s58, s51, -3\n"
    "s_cmp_gt_u32 s58, 28\n"
    "s_waitcnt lgkmcnt(4)\n"
    "s_cbranch_scc0 G_loop%=\n"
    "s_nop 7\n"
    "v_cvt_pk_bf16_f32 v224, v128, v129\n"
    "v_cvt_pk_bf16_f32 v225, v130, v131\n"
    "v_cvt_pk_bf16_f32 v226, v132, v133\n"
    "v_cvt_pk_bf16_f32 v227, v134, v135\n"
    "v_cvt_pk_bf16_f32 v228, v144, v145\n"
    "v_cvt_pk_bf16_f32 v229, v146, v147\n"
    "v_cvt_pk_bf16_f32 v230, v148, v149\n"
    "v_cvt_pk_bf16_f32 v231, v150, v151\n"
    "v_cvt_pk_bf16_f32 v232, v136, v137\n"
    "v_cvt_pk_bf16_f32 v233, v138, v139\n"
    "v_cvt_pk_bf16_f32 v234, v140, v141\n"
    "v_cvt_pk_bf16_f32 v235, v142, v143\n"
    "v_cvt_pk_bf16_f32 v236, v152, v153\n"
    "v_cvt_pk_bf16_f32 v237, v154, v155\n"
    "v_cvt_pk_bf16_f32 v238, v156, v157\n"
    "v_cvt_pk_bf16_f32 v239, v158, v159\n"
    "global_store_dwordx4 v121, v[224:227], s[44:45] offset:0 sc1\n"
    "global_store_dwordx4 v121, v[228:231], s[44:45] offset:1024 sc1\n"
    "global_store_dwordx4 v121, v[232:235], s[44:45] offset:2048 sc1\n"
    "global_store_dwordx4 v121, v[236:239], s[44:45] offset:3072 sc1\n"
    "s_waitcnt vmcnt(0)\n"
    "s_barrier\n"
    "s_cmp_lg_u32 s56, 0\n"
    "s_cbranch_scc1 G_f3%=\n"
    "s_or_b32 s58, s42, s51\n"
    "s_mov_b64 exec, 1\n"
    "v_mov_b32 v116, s58\n"
    "global_store_dwordx2 v118, v[116:117], s[54:55] sc1\n"
    "s_mov_b64 exec, -1\n"
    "G_f3%=:\n"
    "s_branch G_loop%=\n"
    "G_t1%=:\n"
    "s_nop 7\n"
    "v_cvt_pk_bf16_f32 v224, v160, v161\n"
    "v_cvt_pk_bf16_f32 v225, v162, v163\n"
    "v_cvt_pk_bf16_f32 v226, v164, v165\n"
    "v_cvt_pk_bf16_f32 v227, v166, v167\n"
    "v_cvt_pk_bf16_f32 v228, v176, v177\n"
    "v_cvt_pk_bf16_f32 v229, v178, v179\n"
    "v_cvt_pk_bf16_f32 v230, v180, v181\n"
    "v_cvt_pk_bf16_f32 v231, v182, v183\n"
    "v_cvt_pk_bf16_f32 v232, v168, v169\n"
    "v_cvt_pk_bf16_f32 v233, v170, v171\n"
    "v_cvt_pk_bf16_f32 v234, v172, v173\n"
    "v_cvt_pk_bf16_f32 v235, v174, v175\n"
    "v_cvt_pk_bf16_f32 v236, v184, v185\n"
    "v_cvt_pk_bf16_f32 v237, v186, v187\n"
    "v_cvt_pk_bf16_f32 v238, v188, v189\n"
    "v_cvt_pk_bf16_f32 v239, v190, v191\n"
    "global_store_dwordx4 v121, v[224:227], s[44:45] offset:0 sc1\n"
    "global_store_dwordx4 v121, v[228:231], s[44:45] offset:1024 sc1\n"
    "global_store_dwordx4 v121, v[232:235], s[44:45] offset:2048 sc1\n"
    "global_store_dwordx4 v121, v[236:239], s[44:45] offset:3072 sc1\n"
    "s_waitcnt vmcnt(0)\n"
    "s_barrier\n"
    "s_cmp_lg_u32 s56, 0\n"
    "s_cbranch_scc1 G_f4%=\n"
    "s_or_b32 s58, s42, s51\n"
    "s_mov_b64 exec, 1\n"
    "v_mov_b32 v116, s58\n"
    "global_store_dwordx2 v118, v[116:117], s[54:55] sc1\n"
    "s_mov_b64 exec, -1\n"
    "G_f4%=:\n"
    "s_cmp_eq_u32 s51, 32\n"
    "s_cbranch_scc1 G_end%=\n"
    "s_add_u32 s58, s40, 0x20e000\n"
    "s_addc_u32 s59, s41, 0\n"
    "s_add_u32 s60, s40, 0x200200\n"
    "s_addc_u32 s61, s41, 0\n"
    "s_or_b32 s62, s42, 1\n"
    "s_mov_b32 s63, s43\n"
    "v_lshlrev_b32 v224, 3, v121\n"
    "G_xw1%=:\n"
    "global_load_dwordx2 v[226:227], v224, s[58:59] sc1\n"
    "G_xc1%=:\n"
    "s_waitcnt vmcnt(0)\n"
    "v_cmp_ne_u64 vcc, s[62:63], v[226:227]\n"
    "s_nop 0\n"
    "s_cbranch_vccz G_xok1%=\n"
    "s_sleep 4\n"
    "s_add_u32 s57, s57, 1\n"
    "s_and_b32 s68, s57, 0xff\n"
    "s_cbranch_scc1 G_xw1%=\n"
    "global_load_dwordx2 v[226:227], v118, s[60:61] sc1\n"
    "s_waitcnt vmcnt(0)\n"
    "v_cmp_eq_u64 vcc, s[42:43], v[226:227]\n"
    "s_nop 0\n"
    "s_cbranch_vccnz G_xok1%=\n"
    "s_cmp_lt_u32 s57, 0x40000\n"
    "s_cbranch_scc1 G_xw1%=\n"
    "v_mov_b32 v116, s42\n"
    "global_store_dwordx2 v118, v[116:117], s[60:61] sc1\n"
    "G_xok1%=:\n"
    "s_mov_b32 vcc_lo, 0xcccccccc\n"
    "s_mov_b32 vcc_hi, 0xcccccccc\n"
    "s_branch G_even%=\n"
    "G_end%=:\n"
    "s_waitcnt lgkmcnt(0)\n"
    :: GIN : GCLOB);
  else if constexpr (MAT == 1) asm volatile(
    "s_mov_b64 s[40:41], %0\n"
    "s_mov_b64 s[42:43], %1\n"
    "s_mov_b64 s[44:45], %2\n"
    "s_mov_b64 s[54:55], %3\n"
    "s_mov_b32 s56, %4\n"
    "v_mov_b32 v121, %5\n"
    "s_add_u32 s58, s40, 0x20e000\n"
    "s_addc_u32 s59, s41, 0\n"
    "v_lshlrev_b32 v225, 3, v121\n"
    "v_and_b32 v224, 0x380, v225\n"
    "global_load_dwordx2 v[226:227], v224, s[58:59] sc1\n"
    "v_xor_b32 v112, 0, %6\n"
    "v_xor_b32 v113, 32, %6\n"
    "v_xor_b32 v114, 64, %6\n"
    "v_xor_b32 v115, 96, %6\n"
    "ds_read_b64_tr_b16 a[0:1], v112 offset:0\n"
    "ds_read_b64_tr_b16 a[2:3], v112 offset:1024\n"
    "ds_read_b64_tr_b16 a[4:5], v112 offset:8192\n"
    "ds_read_b64_tr_b16 a[6:7], v112 offset:9216\n"
    "ds_read_b64_tr_b16 a[8:9], v112 offset:16384\n"
    "ds_read_b64_tr_b16 a[10:11], v112 offset:17408\n"
    "ds_read_b64_tr_b16 a[12:13], v112 offset:24576\n"
    "ds_read_b64_tr_b16 a[14:15], v112 offset:25600\n"
    "s_waitcnt lgkmcnt(6)\n"
    "ds_read_b64_tr_b16 a[16:17], v112 offset:32768\n"
    "ds_read_b64_tr_b16 a[18:19], v112 offset:33792\n"
    "ds_read_b64_tr_b16 a[20:21], v112 offset:40960\n"
    "ds_read_b64_tr_b16 a[22:23], v112 offset:41984\n"
    "ds_read_b64_tr_b16 a[24:25], v112 offset:49152\n"
    "ds_read_b64_tr_b16 a[26:27], v112 offset:50176\n"
    "ds_read_b64_tr_b16 a[28:29], v112 offset:57344\n"
    "ds_read_b64_tr_b16 a[30:31], v112 offset:58368\n"
    "s_waitcnt lgkmcnt(6)\n"
    "ds_read_b64_tr_b16 a[64:65], v113 offset:0\n"
    "ds_read_b64_tr_b16 a[66:67], v113 offset:1024\n"
    "ds_read_b64_tr_b16 a[68:69], v113 offset:8192\n"
    "ds_read_b64_tr_b16 a[70:71], v113 offset:9216\n"
    "ds_read_b64_tr_b16 a[72:73], v113 offset:16384\n"
    "ds_read_b64_tr_b16 a[74:75], v113 offset:17408\n"
    "ds_read_b64_tr_b16 a[76:77], v113 offset:24576\n"
    "ds_read_b64_tr_b16 a[78:79], v113 offset:25600\n"
    "s_waitcnt lgkmcnt(6)\n"
    "ds_read_b64_tr_b16 a[80:81], v113 offset:32768\n"
    "ds_read_b64_tr_b16 a[82:83], v113 offset:33792\n"
    "ds_read_b64_tr_b16 a[84:85], v113 offset:40960\n"
    "ds_read_b64_tr_b16 a[86:87], v113 offset:41984\n"
    "ds_read_b64_tr_b16 a[88:89], v113 offset:49152\n"
    "ds_read_b64_tr_b16 a[90:91], v113 offset:50176\n"
    "ds_read_b64_tr_b16 a[92:93], v113 offset:57344\n"
    "ds_read_b64_tr_b16 a[94:95], v113 offset:58368\n"
    "s_waitcnt lgkmcnt(6)\n"
    "ds_read_b64_tr_b16 a[128:129], v114 offset:0\n"
    "ds_read_b64_tr_b16 a[130:131], v114 offset:1024\n"
    "ds_read_b64_tr_b16 a[132:133], v114 offset:8192\n"
    "ds_read_b64_tr_b16 a[134:135], v114 offset:9216\n"
    "ds_read_b64_tr_b16 a[136:137], v114 offset:16384\n"
    "ds_read_b64_tr_b16 a[138:139], v114 offset:17408\n"
    "ds_read_b64_tr_b16 a[140:141], v114 offset:24576\n"
    "ds_read_b64_tr_b16 a[142:143], v114 offset:25600\n"
    "s_waitcnt lgkmcnt(6)\n"
    "ds_read_b64_tr_b16 a[144:145], v114 offset:32768\n"
    "ds_read_b64_tr_b16 a[146:147], v114 offset:33792\n"
    "ds_read_b64_tr_b16 a[148:149], v114 offset:40960\n"
    "ds_read_b64_tr_b16 a[150:151], v114 offset:41984\n"
    "ds_read_b64_tr_b16 a[152:153], v114 offset:49152\n"
    "ds_read_b64_tr_b16 a[154:155], v114 offset:50176\n"
    "ds_read_b64_tr_b16 a[156:157], v114 offset:57344\n"
    "ds_read_b64_tr_b16 a[158:159], v114 offset:58368\n"
    "s_waitcnt lgkmcnt(6)\n"
    "ds_read_b64_tr_b16 a[192:193], v115 offset:0\n"
    "ds_read_b64_tr_b16 a[194:195], v115 offset:1024\n"
    "ds_read_b64_tr_b16 a[196:197], v115 offset:8192\n"
    "ds_read_b64_tr_b16 a[198:199], v115 offset:9216\n"
    "ds_read_b64_tr_b16 a[200:201], v115 offset:16384\n"
    "ds_read_b64_tr_b16 a[202:203], v115 offset:17408\n"
    "ds_read_b64_tr_b16 a[204:205], v115 offset:24576\n"
    "ds_read_b64_tr_b16 a[206:207], v115 offset:25600\n"
    "s_waitcnt lgkmcnt(6)\n"
    "ds_read_b64_tr_b16 a[208:209], v115 offset:32768\n"
    "ds_read_b64_tr_b16 a[210:211], v115 offset:33792\n"
    "ds_read_b64_tr_b16 a[212:213], v115 offset:40960\n"
    "ds_read_b64_tr_b16 a[214:215], v115 offset:41984\n"
    "ds_read_b64_tr_b16 a[216:217], v115 offset:49152\n"
    "ds_read_b64_tr_b16 a[218:219], v115 offset:50176\n"
    "ds_read_b64_tr_b16 a[220:221], v115 offset:57344\n"
    "ds_read_b64_tr_b16 a[222:223], v115 offset:58368\n"
    "s_waitcnt lgkmcnt(6)\n"
    "v_xor_b32 v112, 0, %6\n"
    "v_add_u32 v112, 0x10000, v112\n"
    "v_xor_b32 v113, 32, %6\n"
    "v_add_u32 v113, 0x10000, v113\n"
    "v_xor_b32 v114, 64, %6\n"
    "v_add_u32 v114, 0x10000, v114\n"
    "v_xor_b32 v115, 96, %6\n"
    "v_add_u32 v115, 0x10000, v115\n"
    "ds_read_b64_tr_b16 a[32:33], v112 offset:0\n"
    "ds_read_b64_tr_b16 a[34:35], v112 offset:1024\n"
    "ds_read_b64_tr_b16 a[36:37], v112 offset:8192\n"
    "ds_read_b64_tr_b16 a[38:39], v112 offset:9216\n"
    "ds_read_b64_tr_b16 a[40:41], v112 offset:16384\n"
    "ds_read_b64_tr_b16 a[42:43], v112 offset:17408\n"
    "ds_read_b64_tr_b16 a[44:45], v112 offset:24576\n"
    "ds_read_b64_tr_b16 a[46:47], v112 offset:25600\n"
    "s_waitcnt lgkmcnt(6)\n"
    "ds_read_b64_tr_b16 a[48:49], v112 offset:32768\n"
    "ds_read_b64_tr_b16 a[50:51], v112 offset:33792\n"
    "ds_read_b64_tr_b16 a[52:53], v112 offset:40960\n"
    "ds_read_b64_tr_b16 a[54:55], v112 offset:41984\n"
    "ds_read_b64_tr_b16 a[56:57], v112 offset:49152\n"
    "ds_read_b64_tr_b16 a[58:59], v112 offset:50176\n"
    "ds_read_b64_tr_b16 a[60:61], v112 offset:57344\n"
    "ds_read_b64_tr_b16 a[62:63], v112 offset:58368\n"
    "s_waitcnt lgkmcnt(6)\n"
    "ds_read_b64_tr_b16 a[96:97], v113 offset:0\n"
    "ds_read_b64_tr_b16 a[98:99], v113 offset:1024\n"
    "ds_read_b64_tr_b16 a[100:101], v113 offset:8192\n"
    "ds_read_b64_tr_b16 a[102:103], v113 offset:9216\n"
    "ds_read_b64_tr_b16 a[104:105], v113 offset:16384\n"
    "ds_read_b64_tr_b16 a[106:107], v113 offset:17408\n"
    "ds_read_b64_tr_b16 a[108:109], v113 offset:24576\n"
    "ds_read_b64_tr_b16 a[110:111], v113 offset:25600\n"
    "s_waitcnt lgkmcnt(6)\n"
    "ds_read_b64_tr_b16 a[112:113], v113 offset:32768\n"
    "ds_read_b64_tr_b16 a[114:115], v113 offset:33792\n"
    "ds_read_b64_tr_b16 a[116:117], v113 offset:40960\n"
    "ds_read_b64_tr_b16 a[118:119], v113 offset:41984\n"
    "ds_read_b64_tr_b16 a[120:121], v113 offset:49152\n"
    "ds_read_b64_tr_b16 a[122:123], v113 offset:50176\n"
    "ds_read_b64_tr_b16 a[124:125], v113 offset:57344\n"
    "ds_read_b64_tr_b16 a[126:127], v113 offset:58368\n"
    "s_waitcnt lgkmcnt(6)\n"
    "ds_read_b64_tr_b16 a[160:161], v114 offset:0\n"
    "ds_read_b64_tr_b16 a[162:163], v114 offset:1024\n"
    "ds_read_b64_tr_b16 a[164:165], v114 offset:8192\n"
    "ds_read_b64_tr_b16 a[166:167], v114 offset:9216\n"
    "ds_read_b64_tr_b16 a[168:169], v114 offset:16384\n"
    "ds_read_b64_tr_b16 a[170:171], v114 offset:17408\n"
    "ds_read_b64_tr_b16 a[172:173], v114 offset:24576\n"
    "ds_read_b64_tr_b16 a[174:175], v114 offset:25600\n"
    "s_waitcnt lgkmcnt(6)\n"
    "ds_read_b64_tr_b16 a[176:177], v114 offset:32768\n"
    "ds_read_b64_tr_b16 a[178:179], v114 offset:33792\n"
    "ds_read_b64_tr_b16 a[180:181], v114 offset:40960\n"
    "ds_read_b64_tr_b16 a[182:183], v114 offset:41984\n"
    "ds_read_b64_tr_b16 a[184:185], v114 offset:49152\n"
    "ds_read_b64_tr_b16 a[186:187], v114 offset:50176\n"
    "ds_read_b64_tr_b16 a[188:189], v114 offset:57344\n"
    "ds_read_b64_tr_b16 a[190:191], v114 offset:58368\n"
    "s_waitcnt lgkmcnt(6)\n"
    "ds_read_b64_tr_b16 a[224:225], v115 offset:0\n"
    "ds_read_b64_tr_b16 a[226:227], v115 offset:1024\n"
    "ds_read_b64_tr_b16 a[228:229], v115 offset:8192\n"
    "ds_read_b64_tr_b16 a[230:231], v115 offset:9216\n"
    "ds_read_b64_tr_b16 a[232:233], v115 offset:16384\n"
    "ds_read_b64_tr_b16 a[234:235], v115 offset:17408\n"
    "ds_read_b64_tr_b16 a[236:237], v115 offset:24576\n"
    "ds_read_b64_tr_b16 a[238:239], v115 offset:25600\n"
    "s_waitcnt lgkmcnt(6)\n"
    "ds_read_b64_tr_b16 a[240:241], v115 offset:32768\n"
    "ds_read_b64_tr_b16 a[242:243], v115 offset:33792\n"
    "ds_read_b64_tr_b16 a[244:245], v115 offset:40960\n"
    "ds_read_b64_tr_b16 a[246:247], v115 offset:41984\n"
    "ds_read_b64_tr_b16 a[248:249], v115 offset:49152\n"
    "ds_read_b64_tr_b16 a[250:251], v115 offset:50176\n"
    "ds_read_b64_tr_b16 a[252:253], v115 offset:57344\n"
    "ds_read_b64_tr_b16 a[254:255], v115 offset:58368\n"
    "s_waitcnt lgkmcnt(6)\n"
    "s_lshr_b32 s58, s56, 1\n"
    "s_lshl_b32 s58, s58, 15\n"
    "v_add_u32 v122, s58, v121\n"
    "v_add_u32 v123, 0x10000, v122\n"
    "s_lshl_b32 s50, s56, 14\n"
    "v_mov_b32 v118, 0\n"
    "v_mov_b32 v117, s43\n"
    "s_add_u32 s58, s50, 0x4d00000\n"
    "s_add_u32 s48, s40, s58\n"
    "s_addc_u32 s49, s41, 0\n"
    "s_mov_b32 s57, 0\n"
    "s_mov_b32 s51, 0\n"
    "s_waitcnt lgkmcnt(0)\n"
    "s_barrier\n"
    "s_add_u32 s58, s40, 0x20e000\n"
    "s_addc_u32 s59, s41, 0\n"
    "s_add_u32 s60, s40, 0x200200\n"
    "s_addc_u32 s61, s41, 0\n"
    "s_or_b32 s62, s42, 1\n"
    "s_mov_b32 s63, s43\n"
    "s_branch G_xc0%=\n"
    "G_xw0%=:\n"
    "global_load_dwordx2 v[226:227], v224, s[58:59] sc1\n"
    "G_xc0%=:\n"
    "s_waitcnt vmcnt(0)\n"
    "v_cmp_ne_u64 vcc, s[62:63], v[226:227]\n"
    "s_nop 0\n"
    "s_cbranch_vccz G_xok0%=\n"
    "s_sleep 4\n"
    "s_add_u32 s57, s57, 1\n"
    "s_and_b32 s68, s57, 0xff\n"
    "s_cbranch_scc1 G_xw0%=\n"
    "global_load_dwordx2 v[226:227], v118, s[60:61] sc1\n"
    "s_waitcnt vmcnt(0)\n"
    "v_cmp_eq_u64 vcc, s[42:43], v[226:227]\n"
    "s_nop 0\n"
    "s_cbranch_vccnz G_xok0%=\n"
    "s_cmp_lt_u32 s57, 0x40000\n"
    "s_cbranch_scc1 G_xw0%=\n"
    "v_mov_b32 v116, s42\n"
    "global_store_dwordx2 v118, v[116:117], s[60:61] sc1\n"
    "G_xok0%=:\n"
    "s_mov_b32 vcc_lo, 0xcccccccc\n"
    "s_mov_b32 vcc_hi, 0xcccccccc\n"
    "s_add_u32 m0, s50, 0\n"
    "s_add_u32 s64, s48, 0\n"
    "s_addc_u32 s65, s49, 0\n"
    "global_load_lds_dwordx4 v121, s[64:65] offset:0 sc1\n"
    "global_load_lds_dwordx4 v121, s[64:65] offset:1024 sc1\n"
    "global_load_lds_dwordx4 v121, s[64:65] offset:2048 sc1\n"
    "global_load_lds_dwordx4 v121, s[64:65] offset:3072 sc1\n"
    "s_add_u32 m0, s50, 4096\n"
    "s_add_u32 s64, s48, 4096\n"
    "s_addc_u32 s65, s49, 0\n"
    "global_load_lds_dwordx4 v121, s[64:65] offset:0 sc1\n"
    "global_load_lds_dwordx4 v121, s[64:65] offset:1024 sc1\n"
    "global_load_lds_dwordx4 v121, s[64:65] offset:2048 sc1\n"
    "global_load_lds_dwordx4 v121, s[64:65] offset:3072 sc1\n"
    "s_add_u32 m0, s50, 8192\n"
    "s_add_u32 s64, s48, 8192\n"
    "s_addc_u32 s65, s49, 0\n"
    "global_load_lds_dwordx4 v121, s[64:65] offset:0 sc1\n"
    "global_load_lds_dwordx4 v121, s[64:65] offset:1024 sc1\n"
    "global_load_lds_dwordx4 v121, s[64:65] offset:2048 sc1\n"
    "global_load_lds_dwordx4 v121, s[64:65] offset:3072 sc1\n"
    "s_add_u32 m0, s50, 12288\n"
    "s_add_u32 s64, s48, 12288\n"
    "s_addc_u32 s65, s49, 0\n"
    "s_add_u32 s48, s48, 0x10000\n"
    "s_addc_u32 s49, s49, 0\n"
    "global_load_lds_dwordx4 v121, s[64:65] offset:0 sc1\n"
    "global_load_lds_dwordx4 v121, s[64:65] offset:1024 sc1\n"
    "global_load_lds_dwordx4 v121, s[64:65] offset:2048 sc1\n"
    "global_load_lds_dwordx4 v121, s[64:65] offset:3072 sc1\n"
    "s_add_u32 m0, s50, 65536\n"
    "s_add_u32 s64, s48, 0\n"
    "s_addc_u32 s65, s49, 0\n"
    "global_load_lds_dwordx4 v121, s[64:65] offset:0 sc1\n"
    "global_load_lds_dwordx4 v121, s[64:65] offset:1024 sc1\n"
    "global_load_lds_dwordx4 v121, s[64:65] offset:2048 sc1\n"
    "global_load_lds_dwordx4 v121, s[64:65] offset:3072 sc1\n"
    "s_add_u32 m0, s50, 69632\n"
    "s_add_u32 s64, s48, 4096\n"
    "s_addc_u32 s65, s49, 0\n"
    "global_load_lds_dwordx4 v121, s[64:65] offset:0 sc1\n"
    "global_load_lds_dwordx4 v121, s[64:65] offset:1024 sc1\n"
    "global_load_lds_dwordx4 v121, s[64:65] offset:2048 sc1\n"
    "global_load_lds_dwordx4 v121, s[64:65] offset:3072 sc1\n"
    "s_add_u32 m0, s50, 73728\n"
    "s_add_u32 s64, s48, 8192\n"
    "s_addc_u32 s65, s49, 0\n"
    "global_load_lds_dwordx4 v121, s[64:65] offset:0 sc1\n"
    "global_load_lds_dwordx4 v121, s[64:65] offset:1024 sc1\n"
    "global_load_lds_dwordx4 v121, s[64:65] offset:2048 sc1\n"
    "s_mov_b64 s[66:67], s[44:45]\n"
    "s_add_u32 s44, s44, 0x9800000\n"
    "s_addc_u32 s45, s45, 0\n"
    "s_waitcnt vmcnt(19) lgkmcnt(0)\n"
    "s_barrier\n"
    "ds_read_b128 v[192:195], v122 offset:0\n"
    "ds_read_b128 v[196:199], v122 offset:16384\n"
    "ds_read_b128 v[200:203], v122 offset:1024\n"
    "ds_read_b128 v[204:207], v122 offset:17408\n"
    "ds_read_b128 v[208:211], v122 offset:2048\n"
    "ds_read_b128 v[212:215], v122 offset:18432\n"
    "s_waitcnt lgkmcnt(4)\n"
    "s_branch G_even%=\n"
    ".p2align 6\n"
    "G_loop%=:\n"
    "v_mfma_f32_16x16x32_bf16 v[160:163], a[0:3], v[192:195], 0\n"
    "ds_read_b128 v[216:219], v123 offset:3072\n"
    "v_mfma_f32_16x16x32_bf16 v[164:167], a[64:67], v[192:195], 0\n"
    "ds_read_b128 v[220:223], v123 offset:19456\n"
    "v_mfma_f32_16x16x32_bf16 v[168:171], a[128:131], v[192:195], 0\n"
    "v_mfma_f32_16x16x32_bf16 v[172:175], a[192:195], v[192:195], 0\n"
    "v_cvt_pk_bf16_f32 v224, v128, v129\n"
    "v_cvt_pk_bf16_f32 v225, v130, v131\n"
    "v_mfma_f32_16x16x32_bf16 v[176:179], a[0:3], v[196:199], 0\n"
    "global_load_lds_dwordx4 v121, s[64:65] offset:3072 sc1\n"
    "v_mfma_f32_16x16x32_bf16 v[180:183], a[64:67], v[196:199], 0\n"
    "v_cvt_pk_bf16_f32 v226, v132, v133\n"
    "v_cvt_pk_bf16_f32 v227, v134, v135\n"
    "v_mfma_f32_16x16x32_bf16 v[184:187], a[128:131], v[196:199], 0\n"
    "v_cvt_pk_bf16_f32 v228, v144, v145\n"
    "v_cvt_pk_bf16_f32 v229, v146, v147\n"
    "v_mfma_f32_16x16x32_bf16 v[188:191], a[192:195], v[196:199], 0\n"
    "v_cvt_pk_bf16_f32 v230, v148, v149\n"
    "v_cvt_pk_bf16_f32 v231, v150, v151\n"
    "s_waitcnt lgkmcnt(4)\n"
    "v_mfma_f32_16x16x32_bf16 v[160:163], a[4:7], v[200:203], v[160:163]\n"
    "ds_read_b128 v[192:195], v123 offset:4096\n"
    "v_mfma_f32_16x16x32_bf16 v[164:167], a[68:71], v[200:203], v[164:167]\n"
    "ds_read_b128 v[196:199], v123 offset:20480\n"
    "v_mfma_f32_16x16x32_bf16 v[168:171], a[132:135], v[200:203], v[168:171]\n"
    "s_add_u32 m0, s50, 12288\n"
    "s_add_u32 s64, s48, 12288\n"
    "s_addc_u32 s65, s49, 0\n"
    "v_cvt_pk_bf16_f32 v232, v136, v137\n"
    "v_cvt_pk_bf16_f32 v233, v138, v139\n"
    "v_mfma_f32_16x16x32_bf16 v[172:175], a[196:199], v[200:203], v[172:175]\n"
    "s_add_u32 s48, s48, 0x10000\n"
    "s_addc_u32 s49, s49, 0\n"
    "v_cvt_pk_bf16_f32 v234, v140, v141\n"
    "v_cvt_pk_bf16_f32 v235, v142, v143\n"
    "v_mfma_f32_16x16x32_bf16 v[176:179], a[4:7], v[204:207], v[176:179]\n"
    "global_load_lds_dwordx4 v121, s[64:65] offset:0 sc1\n"
    "v_mfma_f32_16x16x32_bf16 v[180:183], a[68:71], v[204:207], v[180:183]\n"
    "v_cvt_pk_bf16_f32 v236, v152, v153\n"
    "v_cvt_pk_bf16_f32 v237, v154, v155\n"
    "v_mfma_f32_16x16x32_bf16 v[184:187], a[132:135], v[204:207], v[184:187]\n"
    "v_mfma_f32_16x16x32_bf16 v[188:191], a[196:199], v[204:207], v[188:191]\n"
    "v_cvt_pk_bf16_f32 v238, v156, v157\n"
    "v_cvt_pk_bf16_f32 v239, v158, v159\n"
    "s_waitcnt lgkmcnt(4)\n"
    "v_mfma_f32_16x16x32_bf16 v[160:163], a[8:11], v[208:211], v[160:163]\n"
    "ds_read_b128 v[200:203], v123 offset:5120\n"
    "v_mfma_f32_16x16x32_bf16 v[164:167], a[72:75], v[208:211], v[164:167]\n"
    "ds_read_b128 v[204:207], v123 offset:21504\n"
    "v_mfma_f32_16x16x32_bf16 v[168:171], a[136:139], v[208:211], v[168:171]\n"
    "s_not_b64 vcc, vcc\n"
    "v_cndmask_b32_dpp v240, v232, v224, vcc quad_perm:[0,1,0,1] row_mask:0xf bank_mask:0xf\n"
    "v_cndmask_b32_dpp v241, v233, v225, vcc quad_perm:[0,1,0,1] row_mask:0xf bank_mask:0xf\n"
    "v_mfma_f32_16x16x32_bf16 v[172:175], a[200:203], v[208:211], v[172:175]\n"
    "v_cndmask_b32_dpp v242, v234, v226, vcc quad_perm:[0,1,0,1] row_mask:0xf bank_mask:0xf\n"
    "v_cndmask_b32_dpp v243, v235, v227, vcc quad_perm:[0,1,0,1] row_mask:0xf bank_mask:0xf\n"
    "v_mfma_f32_16x16x32_bf16 v[176:179], a[8:11], v[212:215], v[176:179]\n"
    "global_load_lds_dwordx4 v121, s[64:65] offset:1024 sc1\n"
    "v_mfma_f32_16x16x32_bf16 v[180:183], a[72:75], v[212:215], v[180:183]\n"
    "v_cndmask_b32_dpp v248, v236, v228, vcc quad_perm:[0,1,0,1] row_mask:0xf bank_mask:0xf\n"
    "v_cndmask_b32_dpp v249, v237, v229, vcc quad_perm:[0,1,0,1] row_mask:0xf bank_mask:0xf\n"
    "v_mfma_f32_16x16x32_bf16 v[184:187], a[136:139], v[212:215], v[184:187]\n"
    "global_store_dwordx4 v121, v[240:243], s[44:45] offset:0 sc1\n"
    "v_mfma_f32_16x16x32_bf16 v[188:191], a[200:203], v[212:215], v[188:191]\n"
    "v_cndmask_b32_dpp v250, v238, v230, vcc quad_perm:[0,1,0,1] row_mask:0xf bank_mask:0xf\n"
    "v_cndmask_b32_dpp v251, v239, v231, vcc quad_perm:[0,1,0,1] row_mask:0xf bank_mask:0xf\n"
    "s_waitcnt lgkmcnt(4)\n"
    "v_mfma_f32_16x16x32_bf16 v[160:163], a[12:15], v[216:219], v[160:163]\n"
    "ds_read_b128 v[208:211], v123 offset:6144\n"
    "v_mfma_f32_16x16x32_bf16 v[164:167], a[76:79], v[216:219], v[164:167]\n"
    "ds_read_b128 v[212:215], v123 offset:22528\n"
    "v_mfma_f32_16x16x32_bf16 v[168:171], a[140:143], v[216:219], v[168:171]\n"
    "s_not_b64 vcc, vcc\n"
    "v_cndmask_b32_dpp v244, v224, v232, vcc quad_perm:[2,3,2,3] row_mask:0xf bank_mask:0xf\n"
    "v_cndmask_b32_dpp v245, v225, v233, vcc quad_perm:[2,3,2,3] row_mask:0xf bank_mask:0xf\n"
    "v_mfma_f32_16x16x32_bf16 v[172:175], a[204:207], v[216:219], v[172:175]\n"
    "v_cndmask_b32_dpp v246, v226, v234, vcc quad_perm:[2,3,2,3] row_mask:0xf bank_mask:0xf\n"
    "v_cndmask_b32_dpp v247, v227, v235, vcc quad_perm:[2,3,2,3] row_mask:0xf bank_mask:0xf\n"
    "v_mfma_f32_16x16x32_bf16 v[176:179], a[12:15], v[220:223], v[176:179]\n"
    "global_load_lds_dwordx4 v121, s[64:65] offset:2048 sc1\n"
    "v_mfma_f32_16x16x32_bf16 v[180:183], a[76:79], v[220:223], v[180:183]\n"
    "v_cndmask_b32_dpp v252, v228, v236, vcc quad_perm:[2,3,2,3] row_mask:0xf bank_mask:0xf\n"
    "v_cndmask_b32_dpp v253, v229, v237, vcc quad_perm:[2,3,2,3] row_mask:0xf bank_mask:0xf\n"
    "v_mfma_f32_16x16x32_bf16 v[184:187], a[140:143], v[220:223], v[184:187]\n"
    "global_store_dwordx4 v121, v[248:251], s[44:45] offset:2048 sc1\n"
    "v_mfma_f32_16x16x32_bf16 v[188:191], a[204:207], v[220:223], v[188:191]\n"
    "v_cndmask_b32_dpp v254, v230, v238, vcc quad_perm:[2,3,2,3] row_mask:0xf bank_mask:0xf\n"
    "v_cndmask_b32_dpp v255, v231, v239, vcc quad_perm:[2,3,2,3] row_mask:0xf bank_mask:0xf\n"
    "s_waitcnt lgkmcnt(4)\n"
    "v_mfma_f32_16x16x32_bf16 v[160:163], a[16:19], v[192:195], v[160:163]\n"
    "ds_read_b128 v[216:219], v123 offset:7168\n"
    "v_mfma_f32_16x16x32_bf16 v[164:167], a[80:83], v[192:195], v[164:167]\n"
    "ds_read_b128 v[220:223], v123 offset:23552\n"
    "v_mfma_f32_16x16x32_bf16 v[168:171], a[144:147], v[192:195], v[168:171]\n"
    "global_store_dwordx4 v121, v[244:247], s[44:45] offset:1024 sc1\n"
    "v_mfma_f32_16x16x32_bf16 v[172:175], a[208:211], v[192:195], v[172:175]\n"
    "v_mfma_f32_16x16x32_bf16 v[176:179], a[16:19], v[196:199], v[176:179]\n"
    "global_load_lds_dwordx4 v121, s[64:65] offset:3072 sc1\n"
    "v_mfma_f32_16x16x32_bf16 v[180:183], a[80:83], v[196:199], v[180:183]\n"
    "v_mfma_f32_16x16x32_bf16 v[184:187], a[144:147], v[196:199], v[184:187]\n"
    "global_store_dwordx4 v121, v[252:255], s[44:45] offset:3072 sc1\n"
    "v_mfma_f32_16x16x32_bf16 v[188:191], a[208:211], v[196:199], v[188:191]\n"
    "s_waitcnt lgkmcnt(4)\n"
    "v_mfma_f32_16x16x32_bf16 v[160:163], a[20:23], v[200:203], v[160:163]\n"
    "s_waitcnt vmcnt(12) lgkmcnt(0)\n"
    "s_barrier\n"
    "v_mfma_f32_16x16x32_bf16 v[164:167], a[84:87], v[200:203], v[164:167]\n"
    "ds_read_b128 v[192:195], v123 offset:8192\n"
    "v_mfma_f32_16x16x32_bf16 v[168:171], a[148:151], v[200:203], v[168:171]\n"
    "ds_read_b128 v[196:199], v123 offset:24576\n"
    "s_add_u32 m0, s50, 65536\n"
    "s_add_u32 s64, s48, 0\n"
    "s_addc_u32 s65, s49, 0\n"
    "v_mfma_f32_16x16x32_bf16 v[172:175], a[212:215], v[200:203], v[172:175]\n"
    "v_mfma_f32_16x16x32_bf16 v[176:179], a[20:23], v[204:207], v[176:179]\n"
    "global_load_lds_dwordx4 v121, s[64:65] offset:0 sc1\n"
    "v_mfma_f32_16x16x32_bf16 v[180:183], a[84:87], v[204:207], v[180:183]\n"
    "v_mfma_f32_16x16x32_bf16 v[184:187], a[148:151], v[204:207], v[184:187]\n"
    "v_mfma_f32_16x16x32_bf16 v[188:191], a[212:215], v[204:207], v[188:191]\n"
    "s_waitcnt lgkmcnt(4)\n"
    "v_mfma_f32_16x16x32_bf16 v[160:163], a[24:27], v[208:211], v[160:163]\n"
    "ds_read_b128 v[200:203], v123 offset:9216\n"
    "v_mfma_f32_16x16x32_bf16 v[164:167], a[88:91], v[208:211], v[164:167]\n"
    "ds_read_b128 v[204:207], v123 offset:25600\n"
    "v_mfma_f32_16x16x32_bf16 v[168:171], a[152:155], v[208:211], v[168:171]\n"
    "v_mfma_f32_16x16x32_bf16 v[172:175], a[216:219], v[208:211], v[172:175]\n"
    "v_mfma_f32_16x16x32_bf16 v[176:179], a[24:27], v[212:215], v[176:179]\n"
    "global_load_lds_dwordx4 v121, s[64:65] offset:1024 sc1\n"
    "v_mfma_f32_16x16x32_bf16 v[180:183], a[88:91], v[212:215], v[180:183]\n"
    "v_mfma_f32_16x16x32_bf16 v[184:187], a[152:155], v[212:215], v[184:187]\n"
    "v_mfma_f32_16x16x32_bf16 v[188:191], a[216:219], v[212:215], v[188:191]\n"
    "s_waitcnt lgkmcnt(4)\n"
    "v_mfma_f32_16x16x32_bf16 v[160:163], a[28:31], v[216:219], v[160:163]\n"
    "ds_read_b128 v[208:211], v123 offset:10240\n"
    "v_mfma_f32_16x16x32_bf16 v[164:167], a[92:95], v[216:219], v[164:167]\n"
    "ds_read_b128 v[212:215], v123 offset:26624\n"
    "v_mfma_f32_16x16x32_bf16 v[168:171], a[156:159], v[216:219], v[168:171]\n"
    "v_mfma_f32_16x16x32_bf16 v[172:175], a[220:223], v[216:219], v[172:175]\n"
    "v_mfma_f32_16x16x32_bf16 v[176:179], a[28:31], v[220:223], v[176:179]\n"
    "global_load_lds_dwordx4 v121, s[64:65] offset:2048 sc1\n"
    "v_mfma_f32_16x16x32_bf16 v[180:183], a[92:95], v[220:223], v[180:183]\n"
    "s_mov_b64 s[44:45], s[66:67]\n"
    "s_add_u32 s66, s66, 0x80000\n"
    "s_addc_u32 s67, s67, 0\n"
    "v_mfma_f32_16x16x32_bf16 v[184:187], a[156:159], v[220:223], v[184:187]\n"
    "v_mfma_f32_16x16x32_bf16 v[188:191], a[220:223], v[220:223], v[188:191]\n"
    "s_waitcnt lgkmcnt(4)\n"
    "v_mfma_f32_16x16x32_bf16 v[160:163], a[32:35], v[192:195], v[160:163]\n"
    "ds_read_b128 v[216:219], v123 offset:11264\n"
    "v_mfma_f32_16x16x32_bf16 v[164:167], a[96:99], v[192:195], v[164:167]\n"
    "ds_read_b128 v[220:223], v123 offset:27648\n"
    "v_mfma_f32_16x16x32_bf16 v[168:171], a[160:163], v[192:195], v[168:171]\n"
    "v_mfma_f32_16x16x32_bf16 v[172:175], a[224:227], v[192:195], v[172:175]\n"
    "v_mfma_f32_16x16x32_bf16 v[176:179], a[32:35], v[196:199], v[176:179]\n"
    "global_load_lds_dwordx4 v121, s[64:65] offset:3072 sc1\n"
    "v_mfma_f32_16x16x32_bf16 v[180:183], a[96:99], v[196:199], v[180:183]\n"
    "v_mfma_f32_16x16x32_bf16 v[184:187], a[160:163], v[196:199], v[184:187]\n"
    "v_mfma_f32_16x16x32_bf16 v[188:191], a[224:227], v[196:199], v[188:191]\n"
    "s_waitcnt lgkmcnt(4)\n"
    "v_mfma_f32_16x16x32_bf16 v[160:163], a[36:39], v[200:203], v[160:163]\n"
    "ds_read_b128 v[192:195], v123 offset:12288\n"
    "v_mfma_f32_16x16x32_bf16 v[164:167], a[100:103], v[200:203], v[164:167]\n"
    "ds_read_b128 v[196:199], v123 offset:28672\n"
    "v_mfma_f32_16x16x32_bf16 v[168:171], a[164:167], v[200:203], v[168:171]\n"
    "s_add_u32 m0, s50, 69632\n"
    "s_add_u32 s64, s48, 4096\n"
    "s_addc_u32 s65, s49, 0\n"
    "v_mfma_f32_16x16x32_bf16 v[172:175], a[228:231], v[200:203], v[172:175]\n"
    "v_mfma_f32_16x16x32_bf16 v[176:179], a[36:39], v[204:207], v[176:179]\n"
    "global_load_lds_dwordx4 v121, s[64:65] offset:0 sc1\n"
    "v_mfma_f32_16x16x32_bf16 v[180:183], a[100:103], v[204:207], v[180:183]\n"
    "v_mfma_f32_16x16x32_bf16 v[184:187], a[164:167], v[204:207], v[184:187]\n"
    "v_mfma_f32_16x16x32_bf16 v[188:191], a[228:231], v[204:207], v[188:191]\n"
    "s_waitcnt lgkmcnt(4)\n"
    "v_mfma_f32_16x16x32_bf16 v[160:163], a[40:43], v[208:211], v[160:163]\n"
    "ds_read_b128 v[200:203], v123 offset:13312\n"
    "v_mfma_f32_16x16x32_bf16 v[164:167], a[104:107], v[208:211], v[164:167]\n"
    "ds_read_b128 v[204:207], v123 offset:29696\n"
    "v_mfma_f32_16x16x32_bf16 v[168:171], a[168:171], v[208:211], v[168:171]\n"
    "v_mfma_f32_16x16x32_bf16 v[172:175], a[232:235], v[208:211], v[172:175]\n"
    "v_mfma_f32_16x16x32_bf16 v[176:179], a[40:43], v[212:215], v[176:179]\n"
    "global_load_lds_dwordx4 v121, s[64:65] offset:1024 sc1\n"
    "v_mfma_f32_16x16x32_bf16 v[180:183], a[104:107], v[212:215], v[180:183]\n"
    "v_mfma_f32_16x16x32_bf16 v[184:187], a[168:171], v[212:215], v[184:187]\n"
    "v_mfma_f32_16x16x32_bf16 v[188:191], a[232:235], v[212:215], v[188:191]\n"
    "s_waitcnt lgkmcnt(4)\n"
    "v_mfma_f32_16x16x32_bf16 v[160:163], a[44:47], v[216:219], v[160:163]\n"
    "ds_read_b128 v[208:211], v123 offset:14336\n"
    "v_mfma_f32_16x16x32_bf16 v[164:167], a[108:111], v[216:219], v[164:167]\n"
    "ds_read_b128 v[212:215], v123 offset:30720\n"
    "v_mfma_f32_16x16x32_bf16 v[168:171], a[172:175], v[216:219], v[168:171]\n"
    "v_mfma_f32_16x16x32_bf16 v[172:175], a[236:239], v[216:219], v[172:175]\n"
    "v_mfma_f32_16x16x32_bf16 v[176:179], a[44:47], v[220:223], v[176:179]\n"
    "global_load_lds_dwordx4 v121, s[64:65] offset:2048 sc1\n"
    "v_mfma_f32_16x16x32_bf16 v[180:183], a[108:111], v[220:223], v[180:183]\n"
    "v_mfma_f32_16x16x32_bf16 v[184:187], a[172:175], v[220:223], v[184:187]\n"
    "v_mfma_f32_16x16x32_bf16 v[188:191], a[236:239], v[220:223], v[188:191]\n"
    "s_waitcnt lgkmcnt(4)\n"
    "v_mfma_f32_16x16x32_bf16 v[160:163], a[48:51], v[192:195], v[160:163]\n"
    "ds_read_b128 v[216:219], v123 offset:15360\n"
    "v_mfma_f32_16x16x32_bf16 v[164:167], a[112:115], v[192:195], v[164:167]\n"
    "ds_read_b128 v[220:223], v123 offset:31744\n"
    "v_mfma_f32_16x16x32_bf16 v[168:171], a[176:179], v[192:195], v[168:171]\n"
    "v_mfma_f32_16x16x32_bf16 v[172:175], a[240:243], v[192:195], v[172:175]\n"
    "v_mfma_f32_16x16x32_bf16 v[176:179], a[48:51], v[196:199], v[176:179]\n"
    "global_load_lds_dwordx4 v121, s[64:65] offset:3072 sc1\n"
    "v_mfma_f32_16x16x32_bf16 v[180:183], a[112:115], v[196:199], v[180:183]\n"
    "v_mfma_f32_16x16x32_bf16 v[184:187], a[176:179], v[196:199], v[184:187]\n"
    "v_mfma_f32_16x16x32_bf16 v[188:191], a[240:243], v[196:199], v[188:191]\n"
    "s_waitcnt lgkmcnt(4)\n"
    "v_mfma_f32_16x16x32_bf16 v[160:163], a[52:55], v[200:203], v[160:163]\n"
    "s_waitcnt vmcnt(8) lgkmcnt(0)\n"
    "s_barrier\n"
    "v_mfma_f32_16x16x32_bf16 v[164:167], a[116:119], v[200:203], v[164:167]\n"
    "ds_read_b128 v[192:195], v122 offset:0\n"
    "v_mfma_f32_16x16x32_bf16 v[168:171], a[180:183], v[200:203], v[168:171]\n"
    "ds_read_b128 v[196:199], v122 offset:16384\n"
    "s_add_u32 m0, s50, 73728\n"
    "s_add_u32 s64, s48, 8192\n"
    "s_addc_u32 s65, s49, 0\n"
    "v_mfma_f32_16x16x32_bf16 v[172:175], a[244:247], v[200:203], v[172:175]\n"
    "v_mfma_f32_16x16x32_bf16 v[176:179], a[52:55], v[204:207], v[176:179]\n"
    "global_load_lds_dwordx4 v121, s[64:65] offset:0 sc1\n"
    "v_mfma_f32_16x16x32_bf16 v[180:183], a[116:119], v[204:207], v[180:183]\n"
    "s_cmp_lg_u32 s56, 0\n"
    "s_cbranch_scc1 G_f1%=\n"
    "s_or_b32 s58, s42, s51\n"
    "s_mov_b64 exec, 1\n"
    "v_mov_b32 v116, s58\n"
    "global_store_dwordx2 v118, v[116:117], s[54:55] sc1\n"
    "s_mov_b64 exec, -1\n"
    "G_f1%=:\n"
    "v_mfma_f32_16x16x32_bf16 v[184:187], a[180:183], v[204:207], v[184:187]\n"
    "v_mfma_f32_16x16x32_bf16 v[188:191], a[244:247], v[204:207], v[188:191]\n"
    "s_waitcnt lgkmcnt(4)\n"
    "v_mfma_f32_16x16x32_bf16 v[160:163], a[56:59], v[208:211], v[160:163]\n"
    "ds_read_b128 v[200:203], v122 offset:1024\n"
    "v_mfma_f32_16x16x32_bf16 v[164:167], a[120:123], v[208:211], v[164:167]\n"
    "ds_read_b128 v[204:207], v122 offset:17408\n"
    "v_mfma_f32_16x16x32_bf16 v[168:171], a[184:187], v[208:211], v[168:171]\n"
    "v_mfma_f32_16x16x32_bf16 v[172:175], a[248:251], v[208:211], v[172:175]\n"
    "v_mfma_f32_16x16x32_bf16 v[176:179], a[56:59], v[212:215], v[176:179]\n"
    "global_load_lds_dwordx4 v121, s[64:65] offset:1024 sc1\n"
    "v_mfma_f32_16x16x32_bf16 v[180:183], a[120:123], v[212:215], v[180:183]\n"
    "v_mfma_f32_16x16x32_bf16 v[184:187], a[184:187], v[212:215], v[184:187]\n"
    "v_mfma_f32_16x16x32_bf16 v[188:191], a[248:251], v[212:215], v[188:191]\n"
    "s_add_u32 s51, s51, 1\n"
    "s_waitcnt lgkmcnt(4)\n"
    "v_mfma_f32_16x16x32_bf16 v[160:163], a[60:63], v[216:219], v[160:163]\n"
    "ds_read_b128 v[208:211], v122 offset:2048\n"
    "v_mfma_f32_16x16x32_bf16 v[164:167], a[124:127], v[216:219], v[164:167]\n"
    "ds_read_b128 v[212:215], v122 offset:18432\n"
    "v_mfma_f32_16x16x32_bf16 v[168:171], a[188:191], v[216:219], v[168:171]\n"
    "v_mfma_f32_16x16x32_bf16 v[172:175], a[252:255], v[216:219], v[172:175]\n"
    "v_mfma_f32_16x16x32_bf16 v[176:179], a[60:63], v[220:223], v[176:179]\n"
    "global_load_lds_dwordx4 v121, s[64:65] offset:2048 sc1\n"
    "v_mfma_f32_16x16x32_bf16 v[180:183], a[124:127], v[220:223], v[180:183]\n"
    "v_mfma_f32_16x16x32_bf16 v[184:187], a[188:191], v[220:223], v[184:187]\n"
    "v_mfma_f32_16x16x32_bf16 v[188:191], a[252:255], v[220:223], v[188:191]\n"
    "s_add_u32 s58, s51, -3\n"
    "s_cmp_gt_u32 s58, 28\n"
    "s_waitcnt lgkmcnt(4)\n"
    "s_cbranch_scc1 G_t1%=\n"
    "G_even%=:\n"
    "v_mfma_f32_16x16x32_bf16 v[128:131], a[0:3], v[192:195], 0\n"
    "ds_read_b128 v[216:219], v122 offset:3072\n"
    "v_mfma_f32_16x16x32_bf16 v[132:135], a[64:67], v[192:195], 0\n"
    "ds_read_b128 v[220:223], v122 offset:19456\n"
    "v_mfma_f32_16x16x32_bf16 v[136:139], a[128:131], v[192:195], 0\n"
    "v_mfma_f32_16x16x32_bf16 v[140:143], a[192:195], v[192:195], 0\n"
    "v_cvt_pk_bf16_f32 v224, v160, v161\n"
    "v_cvt_pk_bf16_f32 v225, v162, v163\n"
    "v_mfma_f32_16x16x32_bf16 v[144:147], a[0:3], v[196:199], 0\n"
    "global_load_lds_dwordx4 v121, s[64:65] offset:3072 sc1\n"
    "v_mfma_f32_16x16x32_bf16 v[148:151], a[64:67], v[196:199], 0\n"
    "v_cvt_pk_bf16_f32 v226, v164, v165\n"
    "v_cvt_pk_bf16_f32 v227, v166, v167\n"
    "v_mfma_f32_16x16x32_bf16 v[152:155], a[128:131], v[196:199], 0\n"
    "v_cvt_pk_bf16_f32 v228, v176, v177\n"
    "v_cvt_pk_bf16_f32 v229, v178, v179\n"
    "v_mfma_f32_16x16x32_bf16 v[156:159], a[192:195], v[196:199], 0\n"
    "v_cvt_pk_bf16_f32 v230, v180, v181\n"
    "v_cvt_pk_bf16_f32 v231, v182, v183\n"
    "s_waitcnt lgkmcnt(4)\n"
    "v_mfma_f32_16x16x32_bf16 v[128:131], a[4:7], v[200:203], v[128:131]\n"
    "ds_read_b128 v[192:195], v122 offset:4096\n"
    "v_mfma_f32_16x16x32_bf16 v[132:135], a[68:71], v[200:203], v[132:135]\n"
    "ds_read_b128 v[196:199], v122 offset:20480\n"
    "v_mfma_f32_16x16x32_bf16 v[136:139], a[132:135], v[200:203], v[136:139]\n"
    "s_add_u32 m0, s50, 77824\n"
    "s_add_u32 s64, s48, 12288\n"
    "s_addc_u32 s65, s49, 0\n"
    "v_cvt_pk_bf16_f32 v232, v168, v169\n"
    "v_cvt_pk_bf16_f32 v233, v170, v171\n"
    "v_mfma_f32_16x16x32_bf16 v[140:143], a[196:199], v[200:203], v[140:143]\n"
    "s_add_u32 s48, s48, 0x10000\n"
    "s_addc_u32 s49, s49, 0\n"
    "v_cvt_pk_bf16_f32 v234, v172, v173\n"
    "v_cvt_pk_bf16_f32 v235, v174, v175\n"
    "v_mfma_f32_16x16x32_bf16 v[144:147], a[4:7], v[204:207], v[144:147]\n"
    "global_load_lds_dwordx4 v121, s[64:65] offset:0 sc1\n"
    "v_mfma_f32_16x16x32_bf16 v[148:151], a[68:71], v[204:207], v[148:151]\n"
    "v_cvt_pk_bf16_f32 v236, v184, v185\n"
    "v_cvt_pk_bf16_f32 v237, v186, v187\n"
    "v_mfma_f32_16x16x32_bf16 v[152:155], a[132:135], v[204:207], v[152:155]\n"
    "v_mfma_f32_16x16x32_bf16 v[156:159], a[196:199], v[204:207], v[156:159]\n"
    "v_cvt_pk_bf16_f32 v238, v188, v189\n"
    "v_cvt_pk_bf16_f32 v239, v190, v191\n"
    "s_waitcnt lgkmcnt(4)\n"
    "v_mfma_f32_16x16x32_bf16 v[128:131], a[8:11], v[208:211], v[128:131]\n"
    "ds_read_b128 v[200:203], v122 offset:5120\n"
    "v_mfma_f32_16x16x32_bf16 v[132:135], a[72:75], v[208:211], v[132:135]\n"
    "ds_read_b128 v[204:207], v122 offset:21504\n"
    "v_mfma_f32_16x16x32_bf16 v[136:139], a[136:139], v[208:211], v[136:139]\n"
    "s_not_b64 vcc, vcc\n"
    "v_cndmask_b32_dpp v240, v232, v224, vcc quad_perm:[0,1,0,1] row_mask:0xf bank_mask:0xf\n"
    "v_cndmask_b32_dpp v241, v233, v225, vcc quad_perm:[0,1,0,1] row_mask:0xf bank_mask:0xf\n"
    "v_mfma_f32_16x16x32_bf16 v[140:143], a[200:203], v[208:211], v[140:143]\n"
    "v_cndmask_b32_dpp v242, v234, v226, vcc quad_perm:[0,1,0,1] row_mask:0xf bank_mask:0xf\n"
    "v_cndmask_b32_dpp v243, v235, v227, vcc quad_perm:[0,1,0,1] row_mask:0xf bank_mask:0xf\n"
    "v_mfma_f32_16x16x32_bf16 v[144:147], a[8:11], v[212:215], v[144:147]\n"
    "global_load_lds_dwordx4 v121, s[64:65] offset:1024 sc1\n"
    "v_mfma_f32_16x16x32_bf16 v[148:151], a[72:75], v[212:215], v[148:151]\n"
    "v_cndmask_b32_dpp v248, v236, v228, vcc quad_perm:[0,1,0,1] row_mask:0xf bank_mask:0xf\n"
    "v_cndmask_b32_dpp v249, v237, v229, vcc quad_perm:[0,1,0,1] row_mask:0xf bank_mask:0xf\n"
    "v_mfma_f32_16x16x32_bf16 v[152:155], a[136:139], v[212:215], v[152:155]\n"
    "global_store_dwordx4 v121, v[240:243], s[44:45] offset:0 sc1\n"
    "v_mfma_f32_16x16x32_bf16 v[156:159], a[200:203], v[212:215], v[156:159]\n"
    "v_cndmask_b32_dpp v250, v238, v230, vcc quad_perm:[0,1,0,1] row_mask:0xf bank_mask:0xf\n"
    "v_cndmask_b32_dpp v251, v239, v231, vcc quad_perm:[0,1,0,1] row_mask:0xf bank_mask:0xf\n"
    "s_waitcnt lgkmcnt(4)\n"
    "v_mfma_f32_16x16x32_bf16 v[128:131], a[12:15], v[216:219], v[128:131]\n"
    "ds_read_b128 v[208:211], v122 offset:6144\n"
    "v_mfma_f32_16x16x32_bf16 v[132:135], a[76:79], v[216:219], v[132:135]\n"
    "ds_read_b128 v[212:215], v122 offset:22528\n"
    "v_mfma_f32_16x16x32_bf16 v[136:139], a[140:143], v[216:219], v[136:139]\n"
    "s_not_b64 vcc, vcc\n"
    "v_cndmask_b32_dpp v244, v224, v232, vcc quad_perm:[2,3,2,3] row_mask:0xf bank_mask:0xf\n"
    "v_cndmask_b32_dpp v245, v225, v233, vcc quad_perm:[2,3,2,3] row_mask:0xf bank_mask:0xf\n"
    "v_mfma_f32_16x16x32_bf16 v[140:143], a[204:207], v[216:219], v[140:143]\n"
    "v_cndmask_b32_dpp v246, v226, v234, vcc quad_perm:[2,3,2,3] row_mask:0xf bank_mask:0xf\n"
    "v_cndmask_b32_dpp v247, v227, v235, vcc quad_perm:[2,3,2,3] row_mask:0xf bank_mask:0xf\n"
    "v_mfma_f32_16x16x32_bf16 v[144:147], a[12:15], v[220:223], v[144:147]\n"
    "global_load_lds_dwordx4 v121, s[64:65] offset:2048 sc1\n"
    "v_mfma_f32_16x16x32_bf16 v[148:151], a[76:79], v[220:223], v[148:151]\n"
    "v_cndmask_b32_dpp v252, v228, v236, vcc quad_perm:[2,3,2,3] row_mask:0xf bank_mask:0xf\n"
    "v_cndmask_b32_dpp v253, v229, v237, vcc quad_perm:[2,3,2,3] row_mask:0xf bank_mask:0xf\n"
    "v_mfma_f32_16x16x32_bf16 v[152:155], a[140:143], v[220:223], v[152:155]\n"
    "global_store_dwordx4 v121, v[248:251], s[44:45] offset:2048 sc1\n"
    "v_mfma_f32_16x16x32_bf16 v[156:159], a[204:207], v[220:223], v[156:159]\n"
    "v_cndmask_b32_dpp v254, v230, v238, vcc quad_perm:[2,3,2,3] row_mask:0xf bank_mask:0xf\n"
    "v_cndmask_b32_dpp v255, v231, v239, vcc quad_perm:[2,3,2,3] row_mask:0xf bank_mask:0xf\n"
    "s_waitcnt lgkmcnt(4)\n"
    "v_mfma_f32_16x16x32_bf16 v[128:131], a[16:19], v[192:195], v[128:131]\n"
    "ds_read_b128 v[216:219], v122 offset:7168\n"
    "v_mfma_f32_16x16x32_bf16 v[132:135], a[80:83], v[192:195], v[132:135]\n"
    "ds_read_b128 v[220:223], v122 offset:23552\n"
    "v_mfma_f32_16x16x32_bf16 v[136:139], a[144:147], v[192:195], v[136:139]\n"
    "global_store_dwordx4 v121, v[244:247], s[44:45] offset:1024 sc1\n"
    "v_mfma_f32_16x16x32_bf16 v[140:143], a[208:211], v[192:195], v[140:143]\n"
    "v_mfma_f32_16x16x32_bf16 v[144:147], a[16:19], v[196:199], v[144:147]\n"
    "global_load_lds_dwordx4 v121, s[64:65] offset:3072 sc1\n"
    "v_mfma_f32_16x16x32_bf16 v[148:151], a[80:83], v[196:199], v[148:151]\n"
    "v_mfma_f32_16x16x32_bf16 v[152:155], a[144:147], v[196:199], v[152:155]\n"
    "global_store_dwordx4 v121, v[252:255], s[44:45] offset:3072 sc1\n"
    "v_mfma_f32_16x16x32_bf16 v[156:159], a[208:211], v[196:199], v[156:159]\n"
    "s_waitcnt lgkmcnt(4)\n"
    "v_mfma_f32_16x16x32_bf16 v[128:131], a[20:23], v[200:203], v[128:131]\n"
    "s_waitcnt vmcnt(12) lgkmcnt(0)\n"
    "s_barrier\n"
    "v_mfma_f32_16x16x32_bf16 v[132:135], a[84:87], v[200:203], v[132:135]\n"
    "ds_read_b128 v[192:195], v122 offset:8192\n"
    "v_mfma_f32_16x16x32_bf16 v[136:139], a[148:151], v[200:203], v[136:139]\n"
    "ds_read_b128 v[196:199], v122 offset:24576\n"
    "s_add_u32 m0, s50, 0\n"
    "s_add_u32 s64, s48, 0\n"
    "s_addc_u32 s65, s49, 0\n"
    "v_mfma_f32_16x16x32_bf16 v[140:143], a[212:215], v[200:203], v[140:143]\n"
    "v_mfma_f32_16x16x32_bf16 v[144:147], a[20:23], v[204:207], v[144:147]\n"
    "global_load_lds_dwordx4 v121, s[64:65] offset:0 sc1\n"
    "v_mfma_f32_16x16x32_bf16 v[148:151], a[84:87], v[204:207], v[148:151]\n"
    "v_mfma_f32_16x16x32_bf16 v[152:155], a[148:151], v[204:207], v[152:155]\n"
    "v_mfma_f32_16x16x32_bf16 v[156:159], a[212:215], v[204:207], v[156:159]\n"
    "s_waitcnt lgkmcnt(4)\n"
    "v_mfma_f32_16x16x32_bf16 v[128:131], a[24:27], v[208:211], v[128:131]\n"
    "ds_read_b128 v[200:203], v122 offset:9216\n"
    "v_mfma_f32_16x16x32_bf16 v[132:135], a[88:91], v[208:211], v[132:135]\n"
    "ds_read_b128 v[204:207], v122 offset:25600\n"
    "v_mfma_f32_16x16x32_bf16 v[136:139], a[152:155], v[208:211], v[136:139]\n"
    "v_mfma_f32_16x16x32_bf16 v[140:143], a[216:219], v[208:211], v[140:143]\n"
    "v_mfma_f32_16x16x32_bf16 v[144:147], a[24:27], v[212:215], v[144:147]\n"
    "global_load_lds_dwordx4 v121, s[64:65] offset:1024 sc1\n"
    "v_mfma_f32_16x16x32_bf16 v[148:151], a[88:91], v[212:215], v[148:151]\n"
    "v_mfma_f32_16x16x32_bf16 v[152:155], a[152:155], v[212:215], v[152:155]\n"
    "v_mfma_f32_16x16x32_bf16 v[156:159], a[216:219], v[212:215], v[156:159]\n"
    "s_waitcnt lgkmcnt(4)\n"
    "v_mfma_f32_16x16x32_bf16 v[128:131], a[28:31], v[216:219], v[128:131]\n"
    "ds_read_b128 v[208:211], v122 offset:10240\n"
    "v_mfma_f32_16x16x32_bf16 v[132:135], a[92:95], v[216:219], v[132:135]\n"
    "ds_read_b128 v[212:215], v122 offset:26624\n"
    "v_mfma_f32_16x16x32_bf16 v[136:139], a[156:159], v[216:219], v[136:139]\n"
    "v_mfma_f32_16x16x32_bf16 v[140:143], a[220:223], v[216:219], v[140:143]\n"
    "v_mfma_f32_16x16x32_bf16 v[144:147], a[28:31], v[220:223], v[144:147]\n"
    "global_load_lds_dwordx4 v121, s[64:65] offset:2048 sc1\n"
    "v_mfma_f32_16x16x32_bf16 v[148:151], a[92:95], v[220:223], v[148:151]\n"
    "s_mov_b64 s[44:45], s[66:67]\n"
    "s_add_u32 s66, s66, 0x80000\n"
    "s_addc_u32 s67, s67, 0\n"
    "v_mfma_f32_16x16x32_bf16 v[152:155], a[156:159], v[220:223], v[152:155]\n"
    "v_mfma_f32_16x16x32_bf16 v[156:159], a[220:223], v[220:223], v[156:159]\n"
    "s_waitcnt lgkmcnt(4)\n"
    "v_mfma_f32_16x16x32_bf16 v[128:131], a[32:35], v[192:195], v[128:131]\n"
    "ds_read_b128 v[216:219], v122 offset:11264\n"
    "v_mfma_f32_16x16x32_bf16 v[132:135], a[96:99], v[192:195], v[132:135]\n"
    "ds_read_b128 v[220:223], v122 offset:27648\n"
    "v_mfma_f32_16x16x32_bf16 v[136:139], a[160:163], v[192:195], v[136:139]\n"
    "v_mfma_f32_16x16x32_bf16 v[140:143], a[224:227], v[192:195], v[140:143]\n"
    "v_mfma_f32_16x16x32_bf16 v[144:147], a[32:35], v[196:199], v[144:147]\n"
    "global_load_lds_dwordx4 v121, s[64:65] offset:3072 sc1\n"
    "v_mfma_f32_16x16x32_bf16 v[148:151], a[96:99], v[196:199], v[148:151]\n"
    "v_mfma_f32_16x16x32_bf16 v[152:155], a[160:163], v[196:199], v[152:155]\n"
    "v_mfma_f32_16x16x32_bf16 v[156:159], a[224:227], v[196:199], v[156:159]\n"
    "s_waitcnt lgkmcnt(4)\n"
    "v_mfma_f32_16x16x32_bf16 v[128:131], a[36:39], v[200:203], v[128:131]\n"
    "ds_read_b128 v[192:195], v122 offset:12288\n"
    "v_mfma_f32_16x16x32_bf16 v[132:135], a[100:103], v[200:203], v[132:135]\n"
    "ds_read_b128 v[196:199], v122 offset:28672\n"
    "v_mfma_f32_16x16x32_bf16 v[136:139], a[164:167], v[200:203], v[136:139]\n"
    "s_add_u32 m0, s50, 4096\n"
    "s_add_u32 s64, s48, 4096\n"
    "s_addc_u32 s65, s49, 0\n"
    "v_mfma_f32_16x16x32_bf16 v[140:143], a[228:231], v[200:203], v[140:143]\n"
    "v_mfma_f32_16x16x32_bf16 v[144:147], a[36:39], v[204:207], v[144:147]\n"
    "global_load_lds_dwordx4 v121, s[64:65] offset:0 sc1\n"
    "v_mfma_f32_16x16x32_bf16 v[148:151], a[100:103], v[204:207], v[148:151]\n"
    "v_mfma_f32_16x16x32_bf16 v[152:155], a[164:167], v[204:207], v[152:155]\n"
    "v_mfma_f32_16x16x32_bf16 v[156:159], a[228:231], v[204:207], v[156:159]\n"
    "s_waitcnt lgkmcnt(4)\n"
    "v_mfma_f32_16x16x32_bf16 v[128:131], a[40:43], v[208:211], v[128:131]\n"
    "ds_read_b128 v[200:203], v122 offset:13312\n"
    "v_mfma_f32_16x16x32_bf16 v[132:135], a[104:107], v[208:211], v[132:135]\n"
    "ds_read_b128 v[204:207], v122 offset:29696\n"
    "v_mfma_f32_16x16x32_bf16 v[136:139], a[168:171], v[208:211], v[136:139]\n"
    "v_mfma_f32_16x16x32_bf16 v[140:143], a[232:235], v[208:211], v[140:143]\n"
    "v_mfma_f32_16x16x32_bf16 v[144:147], a[40:43], v[212:215], v[144:147]\n"
    "global_load_lds_dwordx4 v121, s[64:65] offset:1024 sc1\n"
    "v_mfma_f32_16x16x32_bf16 v[148:151], a[104:107], v[212:215], v[148:151]\n"
    "v_mfma_f32_16x16x32_bf16 v[152:155], a[168:171], v[212:215], v[152:155]\n"
    "v_mfma_f32_16x16x32_bf16 v[156:159], a[232:235], v[212:215], v[156:159]\n"
    "s_waitcnt lgkmcnt(4)\n"
    "v_mfma_f32_16x16x32_bf16 v[128:131], a[44:47], v[216:219], v[128:131]\n"
    "ds_read_b128 v[208:211], v122 offset:14336\n"
    "v_mfma_f32_16x16x32_bf16 v[132:135], a[108:111], v[216:219], v[132:135]\n"
    "ds_read_b128 v[212:215], v122 offset:30720\n"
    "v_mfma_f32_16x16x32_bf16 v[136:139], a[172:175], v[216:219], v[136:139]\n"
    "v_mfma_f32_16x16x32_bf16 v[140:143], a[236:239], v[216:219], v[140:143]\n"
    "v_mfma_f32_16x16x32_bf16 v[144:147], a[44:47], v[220:223], v[144:147]\n"
    "global_load_lds_dwordx4 v121, s[64:65] offset:2048 sc1\n"
    "v_mfma_f32_16x16x32_bf16 v[148:151], a[108:111], v[220:223], v[148:151]\n"
    "v_mfma_f32_16x16x32_bf16 v[152:155], a[172:175], v[220:223], v[152:155]\n"
    "v_mfma_f32_16x16x32_bf16 v[156:159], a[236:239], v[220:223], v[156:159]\n"
    "s_waitcnt lgkmcnt(4)\n"
    "v_mfma_f32_16x16x32_bf16 v[128:131], a[48:51], v[192:195], v[128:131]\n"
    "ds_read_b128 v[216:219], v122 offset:15360\n"
    "v_mfma_f32_16x16x32_bf16 v[132:135], a[112:115], v[192:195], v[132:135]\n"
    "ds_read_b128 v[220:223], v122 offset:31744\n"
    "v_mfma_f32_16x16x32_bf16 v[136:139], a[176:179], v[192:195], v[136:139]\n"
    "v_mfma_f32_16x16x32_bf16 v[140:143], a[240:243], v[192:195], v[140:143]\n"
    "v_mfma_f32_16x16x32_bf16 v[144:147], a[48:51], v[196:199], v[144:147]\n"
    "global_load_lds_dwordx4 v121, s[64:65] offset:3072 sc1\n"
    "v_mfma_f32_16x16x32_bf16 v[148:151], a[112:115], v[196:199], v[148:151]\n"
    "v_mfma_f32_16x16x32_bf16 v[152:155], a[176:179], v[196:199], v[152:155]\n"
    "v_mfma_f32_16x16x32_bf16 v[156:159], a[240:243], v[196:199], v[156:159]\n"
    "s_waitcnt lgkmcnt(4)\n"
    "v_mfma_f32_16x16x32_bf16 v[128:131], a[52:55], v[200:203], v[128:131]\n"
    "s_waitcnt vmcnt(8) lgkmcnt(0)\n"
    "s_barrier\n"
    "v_mfma_f32_16x16x32_bf16 v[132:135], a[116:119], v[200:203], v[132:135]\n"
    "ds_read_b128 v[192:195], v123 offset:0\n"
    "v_mfma_f32_16x16x32_bf16 v[136:139], a[180:183], v[200:203], v[136:139]\n"
    "ds_read_b128 v[196:199], v123 offset:16384\n"
    "s_add_u32 m0, s50, 8192\n"
    "s_add_u32 s64, s48, 8192\n"
    "s_addc_u32 s65, s49, 0\n"
    "v_mfma_f32_16x16x32_bf16 v[140:143], a[244:247], v[200:203], v[140:143]\n"
    "v_mfma_f32_16x16x32_bf16 v[144:147], a[52:55], v[204:207], v[144:147]\n"
    "global_load_lds_dwordx4 v121, s[64:65] offset:0 sc1\n"
    "v_mfma_f32_16x16x32_bf16 v[148:151], a[116:119], v[204:207], v[148:151]\n"
    "s_cmp_lg_u32 s56, 0\n"
    "s_cbranch_scc1 G_f2%=\n"
    "s_or_b32 s58, s42, s51\n"
    "s_mov_b64 exec, 1\n"
    "v_mov_b32 v116, s58\n"
    "global_store_dwordx2 v118, v[116:117], s[54:55] sc1\n"
    "s_mov_b64 exec, -1\n"
    "G_f2%=:\n"
    "v_mfma_f32_16x16x32_bf16 v[152:155], a[180:183], v[204:207], v[152:155]\n"
    "v_mfma_f32_16x16x32_bf16 v[156:159], a[244:247], v[204:207], v[156:159]\n"
    "s_waitcnt lgkmcnt(4)\n"
    "v_mfma_f32_16x16x32_bf16 v[128:131], a[56:59], v[208:211], v[128:131]\n"
    "ds_read_b128 v[200:203], v123 offset:1024\n"
    "v_mfma_f32_16x16x32_bf16 v[132:135], a[120:123], v[208:211], v[132:135]\n"
    "ds_read_b128 v[204:207], v123 offset:17408\n"
    "v_mfma_f32_16x16x32_bf16 v[136:139], a[184:187], v[208:211], v[136:139]\n"
    "v_mfma_f32_16x16x32_bf16 v[140:143], a[248:251], v[208:211], v[140:143]\n"
    "v_mfma_f32_16x16x32_bf16 v[144:147], a[56:59], v[212:215], v[144:147]\n"
    "global_load_lds_dwordx4 v121, s[64:65] offset:1024 sc1\n"
    "v_mfma_f32_16x16x32_bf16 v[148:151], a[120:123], v[212:215], v[148:151]\n"
    "v_mfma_f32_16x16x32_bf16 v[152:155], a[184:187], v[212:215], v[152:155]\n"
    "v_mfma_f32_16x16x32_bf16 v[156:159], a[248:251], v[212:215], v[156:159]\n"
    "s_add_u32 s51, s51, 1\n"
    "s_waitcnt lgkmcnt(4)\n"
    "v_mfma_f32_16x16x32_bf16 v[128:131], a[60:63], v[216:219], v[128:131]\n"
    "ds_read_b128 v[208:211], v123 offset:2048\n"
    "v_mfma_f32_16x16x32_bf16 v[132:135], a[124:127], v[216:219], v[132:135]\n"
    "ds_read_b128 v[212:215], v123 offset:18432\n"
    "v_mfma_f32_16x16x32_bf16 v[136:139], a[188:191], v[216:219], v[136:139]\n"
    "v_mfma_f32_16x16x32_bf16 v[140:143], a[252:255], v[216:219], v[140:143]\n"
    "v_mfma_f32_16x16x32_bf16 v[144:147], a[60:63], v[220:223], v[144:147]\n"
    "global_load_lds_dwordx4 v121, s[64:65] offset:2048 sc1\n"
    "v_mfma_f32_16x16x32_bf16 v[148:151], a[124:127], v[220:223], v[148:151]\n"
    "v_mfma_f32_16x16x32_bf16 v[152:155], a[188:191], v[220:223], v[152:155]\n"
    "v_mfma_f32_16x16x32_bf16 v[156:159], a[252:255], v[220:223], v[156:159]\n"
    "s_add_u32 s58, s51, -3\n"
    "s_cmp_gt_u32 s58, 28\n"
    "s_waitcnt lgkmcnt(4)\n"
    "s_cbranch_scc0 G_loop%=\n"
    "s_nop 7\n"
    "v_cvt_pk_bf16_f32 v224, v128, v129\n"
    "v_cvt_pk_bf16_f32 v225, v130, v131\n"
    "v_cvt_pk_bf16_f32 v226, v132, v133\n"
    "v_cvt_pk_bf16_f32 v227, v134, v135\n"
    "v_cvt_pk_bf16_f32 v228, v144, v145\n"
    "v_cvt_pk_bf16_f32 v229, v146, v147\n"
    "v_cvt_pk_bf16_f32 v230, v148, v149\n"
    "v_cvt_pk_bf16_f32 v231, v150, v151\n"
    "v_cvt_pk_bf16_f32 v232, v136, v137\n"
    "v_cvt_pk_bf16_f32 v233, v138, v139\n"
    "v_cvt_pk_bf16_f32 v234, v140, v141\n"
    "v_cvt_pk_bf16_f32 v235, v142, v143\n"
    "v_cvt_pk_bf16_f32 v236, v152, v153\n"
    "v_cvt_pk_bf16_f32 v237, v154, v155\n"
    "v_cvt_pk_bf16_f32 v238, v156, v157\n"
    "v_cvt_pk_bf16_f32 v239, v158, v159\n"
    "s_not_b64 vcc, vcc\n"
    "v_cndmask_b32_dpp v240, v232, v224, vcc quad_perm:[0,1,0,1] row_mask:0xf bank_mask:0xf\n"
    "v_cndmask_b32_dpp v241, v233, v225, vcc quad_perm:[0,1,0,1] row_mask:0xf bank_mask:0xf\n"
    "v_cndmask_b32_dpp v242, v234, v226, vcc quad_perm:[0,1,0,1] row_mask:0xf bank_mask:0xf\n"
    "v_cndmask_b32_dpp v243, v235, v227, vcc quad_perm:[0,1,0,1] row_mask:0xf bank_mask:0xf\n"
    "v_cndmask_b32_dpp v248, v236, v228, vcc quad_perm:[0,1,0,1] row_mask:0xf bank_mask:0xf\n"
    "v_cndmask_b32_dpp v249, v237, v229, vcc quad_perm:[0,1,0,1] row_mask:0xf bank_mask:0xf\n"
    "v_cndmask_b32_dpp v250, v238, v230, vcc quad_perm:[0,1,0,1] row_mask:0xf bank_mask:0xf\n"
    "v_cndmask_b32_dpp v251, v239, v231, vcc quad_perm:[0,1,0,1] row_mask:0xf bank_mask:0xf\n"
    "s_not_b64 vcc, vcc\n"
    "v_cndmask_b32_dpp v244, v224, v232, vcc quad_perm:[2,3,2,3] row_mask:0xf bank_mask:0xf\n"
    "v_cndmask_b32_dpp v245, v225, v233, vcc quad_perm:[2,3,2,3] row_mask:0xf bank_mask:0xf\n"
    "v_cndmask_b32_dpp v246, v226, v234, vcc quad_perm:[2,3,2,3] row_mask:0xf bank_mask:0xf\n"
    "v_cndmask_b32_dpp v247, v227, v235, vcc quad_perm:[2,3,2,3] row_mask:0xf bank_mask:0xf\n"
    "v_cndmask_b32_dpp v252, v228, v236, vcc quad_perm:[2,3,2,3] row_mask:0xf bank_mask:0xf\n"
    "v_cndmask_b32_dpp v253, v229, v237, vcc quad_perm:[2,3,2,3] row_mask:0xf bank_mask:0xf\n"
    "v_cndmask_b32_dpp v254, v230, v238, vcc quad_perm:[2,3,2,3] row_mask:0xf bank_mask:0xf\n"
    "v_cndmask_b32_dpp v255, v231, v239, vcc quad_perm:[2,3,2,3] row_mask:0xf bank_mask:0xf\n"
    "global_store_dwordx4 v121, v[240:243], s[44:45] offset:0 sc1\n"
    "global_store_dwordx4 v121, v[248:251], s[44:45] offset:2048 sc1\n"
    "global_store_dwordx4 v121, v[244:247], s[44:45] offset:1024 sc1\n"
    "global_store_dwordx4 v121, v[252:255], s[44:45] offset:3072 sc1\n"
    "s_waitcnt vmcnt(0)\n"
    "s_barrier\n"
    "s_cmp_lg_u32 s56, 0\n"
    "s_cbranch_scc1 G_f3%=\n"
    "s_or_b32 s58, s42, s51\n"
    "s_mov_b64 exec, 1\n"
    "v_mov_b32 v116, s58\n"
    "global_store_dwordx2 v118, v[116:117], s[54:55] sc1\n"
    "s_mov_b64 exec, -1\n"
    "G_f3%=:\n"
    "s_branch G_loop%=\n"
    "G_t1%=:\n"
    "s_nop 7\n"
    "v_cvt_pk_bf16_f32 v224, v160, v161\n"
    "v_cvt_pk_bf16_f32 v225, v162, v163\n"
    "v_cvt_pk_bf16_f32 v226, v164, v165\n"
    "v_cvt_pk_bf16_f32 v227, v166, v167\n"
    "v_cvt_pk_bf16_f32 v228, v176, v177\n"
    "v_cvt_pk_bf16_f32 v229, v178, v179\n"
    "v_cvt_pk_bf16_f32 v230, v180, v181\n"
    "v_cvt_pk_bf16_f32 v231, v182, v183\n"
    "v_cvt_pk_bf16_f32 v232, v168, v169\n"
    "v_cvt_pk_bf16_f32 v233, v170, v171\n"
    "v_cvt_pk_bf16_f32 v234, v172, v173\n"
    "v_cvt_pk_bf16_f32 v235, v174, v175\n"
    "v_cvt_pk_bf16_f32 v236, v184, v185\n"
    "v_cvt_pk_bf16_f32 v237, v186, v187\n"
    "v_cvt_pk_bf16_f32 v238, v188, v189\n"
    "v_cvt_pk_bf16_f32 v239, v190, v191\n"
    "s_not_b64 vcc, vcc\n"
    "v_cndmask_b32_dpp v240, v232, v224, vcc quad_perm:[0,1,0,1] row_mask:0xf bank_mask:0xf\n"
    "v_cndmask_b32_dpp v241, v233, v225, vcc quad_perm:[0,1,0,1] row_mask:0xf bank_mask:0xf\n"
    "v_cndmask_b32_dpp v242, v234, v226, vcc quad_perm:[0,1,0,1] row_mask:0xf bank_mask:0xf\n"
    "v_cndmask_b32_dpp v243, v235, v227, vcc quad_perm:[0,1,0,1] row_mask:0xf bank_mask:0xf\n"
    "v_cndmask_b32_dpp v248, v236, v228, vcc quad_perm:[0,1,0,1] row_mask:0xf bank_mask:0xf\n"
    "v_cndmask_b32_dpp v249, v237, v229, vcc quad_perm:[0,1,0,1] row_mask:0xf bank_mask:0xf\n"
    "v_cndmask_b32_dpp v250, v238, v230, vcc quad_perm:[0,1,0,1] row_mask:0xf bank_mask:0xf\n"
    "v_cndmask_b32_dpp v251, v239, v231, vcc quad_perm:[0,1,0,1] row_mask:0xf bank_mask:0xf\n"
    "s_not_b64 vcc, vcc\n"
    "v_cndmask_b32_dpp v244, v224, v232, vcc quad_perm:[2,3,2,3] row_mask:0xf bank_mask:0xf\n"
    "v_cndmask_b32_dpp v245, v225, v233, vcc quad_perm:[2,3,2,3] row_mask:0xf bank_mask:0xf\n"
    "v_cndmask_b32_dpp v246, v226, v234, vcc quad_perm:[2,3,2,3] row_mask:0xf bank_mask:0xf\n"
    "v_cndmask_b32_dpp v247, v227, v235, vcc quad_perm:[2,3,2,3] row_mask:0xf bank_mask:0xf\n"
    "v_cndmask_b32_dpp v252, v228, v236, vcc quad_perm:[2,3,2,3] row_mask:0xf bank_mask:0xf\n"
    "v_cndmask_b32_dpp v253, v229, v237, vcc quad_perm:[2,3,2,3] row_mask:0xf bank_mask:0xf\n"
    "v_cndmask_b32_dpp v254, v230, v238, vcc quad_perm:[2,3,2,3] row_mask:0xf bank_mask:0xf\n"
    "v_cndmask_b32_dpp v255, v231, v239, vcc quad_perm:[2,3,2,3] row_mask:0xf bank_mask:0xf\n"
    "global_store_dwordx4 v121, v[240:243], s[44:45] offset:0 sc1\n"
    "global_store_dwordx4 v121, v[248:251], s[44:45] offset:2048 sc1\n"
    "global_store_dwordx4 v121, v[244:247], s[44:45] offset:1024 sc1\n"
    "global_store_dwordx4 v121, v[252:255], s[44:45] offset:3072 sc1\n"
    "s_waitcnt vmcnt(0)\n"
    "s_barrier\n"
    "s_cmp_lg_u32 s56, 0\n"
    "s_cbranch_scc1 G_f4%=\n"
    "s_or_b32 s58, s42, s51\n"
    "s_mov_b64 exec, 1\n"
    "v_mov_b32 v116, s58\n"
    "global_store_dwordx2 v118, v[116:117], s[54:55] sc1\n"
    "s_mov_b64 exec, -1\n"
    "G_f4%=:\n"
    "s_cmp_eq_u32 s51, 32\n"
    "s_cbranch_scc1 G_end%=\n"
    "s_add_u32 s58, s40, 0x20e000\n"
    "s_addc_u32 s59, s41, 0\n"
    "s_add_u32 s60, s40, 0x200200\n"
    "s_addc_u32 s61, s41, 0\n"
    "s_or_b32 s62, s42, 1\n"
    "s_mov_b32 s63, s43\n"
    "v_lshlrev_b32 v224, 3, v121\n"
    "G_xw1%=:\n"
    "global_load_dwordx2 v[226:227], v224, s[58:59] sc1\n"
    "G_xc1%=:\n"
    "s_waitcnt vmcnt(0)\n"
    "v_cmp_ne_u64 vcc, s[62:63], v[226:227]\n"
    "s_nop 0\n"
    "s_cbranch_vccz G_xok1%=\n"
    "s_sleep 4\n"
    "s_add_u32 s57, s57, 1\n"
    "s_and_b32 s68, s57, 0xff\n"
    "s_cbranch_scc1 G_xw1%=\n"
    "global_load_dwordx2 v[226:227], v118, s[60:61] sc1\n"
    "s_waitcnt vmcnt(0)\n"
    "v_cmp_eq_u64 vcc, s[42:43], v[226:227]\n"
    "s_nop 0\n"
    "s_cbranch_vccnz G_xok1%=\n"
    "s_cmp_lt_u32 s57, 0x40000\n"
    "s_cbranch_scc1 G_xw1%=\n"
    "v_mov_b32 v116, s42\n"
    "global_store_dwordx2 v118, v[116:117], s[60:61] sc1\n"
    "G_xok1%=:\n"
    "s_mov_b32 vcc_lo, 0xcccccccc\n"
    "s_mov_b32 vcc_hi, 0xcccccccc\n"
    "s_branch G_even%=\n"
    "G_end%=:\n"
    "s_waitcnt lgkmcnt(0)\n"
    :: GIN : GCLOB);
  else asm volatile(
    "s_mov_b64 s[40:41], %0\n"
    "s_mov_b64 s[42:43], %1\n"
    "s_mov_b64 s[44:45], %2\n"
    "s_mov_b64 s[54:55], %3\n"
    "s_mov_b32 s56, %4\n"
    "v_mov_b32 v121, %5\n"
    "s_add_u32 s58, s40, 0x20e000\n"
    "s_addc_u32 s59, s41, 0\n"
    "v_lshlrev_b32 v225, 3, v121\n"
    "v_and_b32 v224, 0x380, v225\n"
    "global_load_dwordx2 v[226:227], v224, s[58:59] sc1\n"
    "v_xor_b32 v112, 0, %6\n"
    "v_xor_b32 v113, 32, %6\n"
    "v_xor_b32 v114, 64, %6\n"
    "v_xor_b32 v115, 96, %6\n"
    "ds_read_b64_tr_b16 a[0:1], v112 offset:0\n"
    "ds_read_b64_tr_b16 a[2:3], v112 offset:1024\n"
    "ds_read_b64_tr_b16 a[4:5], v112 offset:8192\n"
    "ds_read_b64_tr_b16 a[6:7], v112 offset:9216\n"
    "ds_read_b64_tr_b16 a[8:9], v112 offset:16384\n"
    "ds_read_b64_tr_b16 a[10:11], v112 offset:17408\n"
    "ds_read_b64_tr_b16 a[12:13], v112 offset:24576\n"
    "ds_read_b64_tr_b16 a[14:15], v112 offset:25600\n"
    "s_waitcnt lgkmcnt(6)\n"
    "ds_read_b64_tr_b16 a[16:17], v112 offset:32768\n"
    "ds_read_b64_tr_b16 a[18:19], v112 offset:33792\n"
    "ds_read_b64_tr_b16 a[20:21], v112 offset:40960\n"
    "ds_read_b64_tr_b16 a[22:23], v112 offset:41984\n"
    "ds_read_b64_tr_b16 a[24:25], v112 offset:49152\n"
    "ds_read_b64_tr_b16 a[26:27], v112 offset:50176\n"
    "ds_read_b64_tr_b16 a[28:29], v112 offset:57344\n"
    "ds_read_b64_tr_b16 a[30:31], v112 offset:58368\n"
    "s_waitcnt lgkmcnt(6)\n"
    "ds_read_b64_tr_b16 a[64:65], v113 offset:0\n"
    "ds_read_b64_tr_b16 a[66:67], v113 offset:1024\n"
    "ds_read_b64_tr_b16 a[68:69], v113 offset:8192\n"
    "ds_read_b64_tr_b16 a[70:71], v113 offset:9216\n"
    "ds_read_b64_tr_b16 a[72:73], v113 offset:16384\n"
    "ds_read_b64_tr_b16 a[74:75], v113 offset:17408\n"
    "ds_read_b64_tr_b16 a[76:77], v113 offset:24576\n"
    "ds_read_b64_tr_b16 a[78:79], v113 offset:25600\n"
    "s_waitcnt lgkmcnt(6)\n"
    "ds_read_b64_tr_b16 a[80:81], v113 offset:32768\n"
    "ds_read_b64_tr_b16 a[82:83], v113 offset:33792\n"
    "ds_read_b64_tr_b16 a[84:85], v113 offset:40960\n"
    "ds_read_b64_tr_b16 a[86:87], v113 offset:41984\n"
    "ds_read_b64_tr_b16 a[88:89], v113 offset:49152\n"
    "ds_read_b64_tr_b16 a[90:91], v113 offset:50176\n"
    "ds_read_b64_tr_b16 a[92:93], v113 offset:57344\n"
    "ds_read_b64_tr_b16 a[94:95], v113 offset:58368\n"
    "s_waitcnt lgkmcnt(6)\n"
    "ds_read_b64_tr_b16 a[128:129], v114 offset:0\n"
    "ds_read_b64_tr_b16 a[130:131], v114 offset:1024\n"
    "ds_read_b64_tr_b16 a[132:133], v114 offset:8192\n"
    "ds_read_b64_tr_b16 a[134:135], v114 offset:9216\n"
    "ds_read_b64_tr_b16 a[136:137], v114 offset:16384\n"
    "ds_read_b64_tr_b16 a[138:139], v114 offset:17408\n"
    "ds_read_b64_tr_b16 a[140:141], v114 offset:24576\n"
    "ds_read_b64_tr_b16 a[142:143], v114 offset:25600\n"
    "s_waitcnt lgkmcnt(6)\n"
    "ds_read_b64_tr_b16 a[144:145], v114 offset:32768\n"
    "ds_read_b64_tr_b16 a[146:147], v114 offset:33792\n"
    "ds_read_b64_tr_b16 a[148:149], v114 offset:40960\n"
    "ds_read_b64_tr_b16 a[150:151], v114 offset:41984\n"
    "ds_read_b64_tr_b16 a[152:153], v114 offset:49152\n"
    "ds_read_b64_tr_b16 a[154:155], v114 offset:50176\n"
    "ds_read_b64_tr_b16 a[156:157], v114 offset:57344\n"
    "ds_read_b64_tr_b16 a[158:159], v114 offset:58368\n"
    "s_waitcnt lgkmcnt(6)\n"
    "ds_read_b64_tr_b16 a[192:193], v115 offset:0\n"
    "ds_read_b64_tr_b16 a[194:195], v115 offset:1024\n"
    "ds_read_b64_tr_b16 a[196:197], v115 offset:8192\n"
    "ds_read_b64_tr_b16 a[198:199], v115 offset:9216\n"
    "ds_read_b64_tr_b16 a[200:201], v115 offset:16384\n"
    "ds_read_b64_tr_b16 a[202:203], v115 offset:17408\n"
    "ds_read_b64_tr_b16 a[204:205], v115 offset:24576\n"
    "ds_read_b64_tr_b16 a[206:207], v115 offset:25600\n"
    "s_waitcnt lgkmcnt(6)\n"
    "ds_read_b64_tr_b16 a[208:209], v115 offset:32768\n"
    "ds_read_b64_tr_b16 a[210:211], v115 offset:33792\n"
    "ds_read_b64_tr_b16 a[212:213], v115 offset:40960\n"
    "ds_read_b64_tr_b16 a[214:215], v115 offset:41984\n"
    "ds_read_b64_tr_b16 a[216:217], v115 offset:49152\n"
    "ds_read_b64_tr_b16 a[218:219], v115 offset:50176\n"
    "ds_read_b64_tr_b16 a[220:221], v115 offset:57344\n"
    "ds_read_b64_tr_b16 a[222:223], v115 offset:58368\n"
    "s_waitcnt lgkmcnt(6)\n"
    "v_xor_b32 v112, 0, %6\n"
    "v_add_u32 v112, 0x10000, v112\n"
    "v_xor_b32 v113, 32, %6\n"
    "v_add_u32 v113, 0x10000, v113\n"
    "v_xor_b32 v114, 64, %6\n"
    "v_add_u32 v114, 0x10000, v114\n"
    "v_xor_b32 v115, 96, %6\n"
    "v_add_u32 v115, 0x10000, v115\n"
    "ds_read_b64_tr_b16 a[32:33], v112 offset:0\n"
    "ds_read_b64_tr_b16 a[34:35], v112 offset:1024\n"
    "ds_read_b64_tr_b16 a[36:37], v112 offset:8192\n"
    "ds_read_b64_tr_b16 a[38:39], v112 offset:9216\n"
    "ds_read_b64_tr_b16 a[40:41], v112 offset:16384\n"
    "ds_read_b64_tr_b16 a[42:43], v112 offset:17408\n"
    "ds_read_b64_tr_b16 a[44:45], v112 offset:24576\n"
    "ds_read_b64_tr_b16 a[46:47], v112 offset:25600\n"
    "s_waitcnt lgkmcnt(6)\n"
    "ds_read_b64_tr_b16 a[48:49], v112 offset:32768\n"
    "ds_read_b64_tr_b16 a[50:51], v112 offset:33792\n"
    "ds_read_b64_tr_b16 a[52:53], v112 offset:40960\n"
    "ds_read_b64_tr_b16 a[54:55], v112 offset:41984\n"
    "ds_read_b64_tr_b16 a[56:57], v112 offset:49152\n"
    "ds_read_b64_tr_b16 a[58:59], v112 offset:50176\n"
    "ds_read_b64_tr_b16 a[60:61], v112 offset:57344\n"
    "ds_read_b64_tr_b16 a[62:63], v112 offset:58368\n"
    "s_waitcnt lgkmcnt(6)\n"
    "ds_read_b64_tr_b16 a[96:97], v113 offset:0\n"
    "ds_read_b64_tr_b16 a[98:99], v113 offset:1024\n"
    "ds_read_b64_tr_b16 a[100:101], v113 offset:8192\n"
    "ds_read_b64_tr_b16 a[102:103], v113 offset:9216\n"
    "ds_read_b64_tr_b16 a[104:105], v113 offset:16384\n"
    "ds_read_b64_tr_b16 a[106:107], v113 offset:17408\n"
    "ds_read_b64_tr_b16 a[108:109], v113 offset:24576\n"
    "ds_read_b64_tr_b16 a[110:111], v113 offset:25600\n"
    "s_waitcnt lgkmcnt(6)\n"
    "ds_read_b64_tr_b16 a[112:113], v113 offset:32768\n"
    "ds_read_b64_tr_b16 a[114:115], v113 offset:33792\n"
    "ds_read_b64_tr_b16 a[116:117], v113 offset:40960\n"
    "ds_read_b64_tr_b16 a[118:119], v113 offset:41984\n"
    "ds_read_b64_tr_b16 a[120:121], v113 offset:49152\n"
    "ds_read_b64_tr_b16 a[122:123], v113 offset:50176\n"
    "ds_read_b64_tr_b16 a[124:125], v113 offset:57344\n"
    "ds_read_b64_tr_b16 a[126:127], v113 offset:58368\n"
    "s_waitcnt lgkmcnt(6)\n"
    "ds_read_b64_tr_b16 a[160:161], v114 offset:0\n"
    "ds_read_b64_tr_b16 a[162:163], v114 offset:1024\n"
    "ds_read_b64_tr_b16 a[164:165], v114 offset:8192\n"
    "ds_read_b64_tr_b16 a[166:167], v114 offset:9216\n"
    "ds_read_b64_tr_b16 a[168:169], v114 offset:16384\n"
    "ds_read_b64_tr_b16 a[170:171], v114 offset:17408\n"
    "ds_read_b64_tr_b16 a[172:173], v114 offset:24576\n"
    "ds_read_b64_tr_b16 a[174:175], v114 offset:25600\n"
    "s_waitcnt lgkmcnt(6)\n"
    "ds_read_b64_tr_b16 a[176:177], v114 offset:32768\n"
    "ds_read_b64_tr_b16 a[178:179], v114 offset:33792\n"
    "ds_read_b64_tr_b16 a[180:181], v114 offset:40960\n"
    "ds_read_b64_tr_b16 a[182:183], v114 offset:41984\n"
    "ds_read_b64_tr_b16 a[184:185], v114 offset:49152\n"
    "ds_read_b64_tr_b16 a[186:187], v114 offset:50176\n"
    "ds_read_b64_tr_b16 a[188:189], v114 offset:57344\n"
    "ds_read_b64_tr_b16 a[190:191], v114 offset:58368\n"
    "s_waitcnt lgkmcnt(6)\n"
    "ds_read_b64_tr_b16 a[224:225], v115 offset:0\n"
    "ds_read_b64_tr_b16 a[226:227], v115 offset:1024\n"
    "ds_read_b64_tr_b16 a[228:229], v115 offset:8192\n"
    "ds_read_b64_tr_b16 a[230:231], v115 offset:9216\n"
    "ds_read_b64_tr_b16 a[232:233], v115 offset:16384\n"
    "ds_read_b64_tr_b16 a[234:235], v115 offset:17408\n"
    "ds_read_b64_tr_b16 a[236:237], v115 offset:24576\n"
    "ds_read_b64_tr_b16 a[238:239], v115 offset:25600\n"
    "s_waitcnt lgkmcnt(6)\n"
    "ds_read_b64_tr_b16 a[240:241], v115 offset:32768\n"
    "ds_read_b64_tr_b16 a[242:243], v115 offset:33792\n"
    "ds_read_b64_tr_b16 a[244:245], v115 offset:40960\n"
    "ds_read_b64_tr_b16 a[246:247], v115 offset:41984\n"
    "ds_read_b64_tr_b16 a[248:249], v115 offset:49152\n"
    "ds_read_b64_tr_b16 a[250:251], v115 offset:50176\n"
    "ds_read_b64_tr_b16 a[252:253], v115 offset:57344\n"
    "ds_read_b64_tr_b16 a[254:255], v115 offset:58368\n"
    "s_waitcnt lgkmcnt(6)\n"
    "s_lshr_b32 s58, s56, 1\n"
    "s_lshl_b32 s58, s58, 15\n"
    "v_add_u32 v122, s58, v121\n"
    "v_add_u32 v123, 0x10000, v122\n"
    "s_lshl_b32 s50, s56, 14\n"
    "v_mov_b32 v118, 0\n"
    "v_mov_b32 v117, s43\n"
    "s_add_u32 s58, s50, 0x4d00000\n"
    "s_add_u32 s48, s40, s58\n"
    "s_addc_u32 s49, s41, 0\n"
    "s_mov_b32 s57, 0\n"
    "s_mov_b32 s51, 0\n"
    "s_waitcnt lgkmcnt(0)\n"
    "s_barrier\n"
    "s_add_u32 s58, s40, 0x20e000\n"
    "s_addc_u32 s59, s41, 0\n"
    "s_add_u32 s60, s40, 0x200200\n"
    "s_addc_u32 s61, s41, 0\n"
    "s_or_b32 s62, s42, 1\n"
    "s_mov_b32 s63, s43\n"
    "s_branch G_xc0%=\n"
    "G_xw0%=:\n"
    "global_load_dwordx2 v[226:227], v224, s[58:59] sc1\n"
    "G_xc0%=:\n"
    "s_waitcnt vmcnt(0)\n"
    "v_cmp_ne_u64 vcc, s[62:63], v[226:227]\n"
    "s_nop 0\n"
    "s_cbranch_vccz G_xok0%=\n"
    "s_sleep 4\n"
    "s_add_u32 s57, s57, 1\n"
    "s_and_b32 s68, s57, 0xff\n"
    "s_cbranch_scc1 G_xw0%=\n"
    "global_load_dwordx2 v[226:227], v118, s[60:61] sc1\n"
    "s_waitcnt vmcnt(0)\n"
    "v_cmp_eq_u64 vcc, s[42:43], v[226:227]\n"
    "s_nop 0\n"
    "s_cbranch_vccnz G_xok0%=\n"
    "s_cmp_lt_u32 s57, 0x40000\n"
    "s_cbranch_scc1 G_xw0%=\n"
    "v_mov_b32 v116, s42\n"
    "global_store_dwordx2 v118, v[116:117], s[60:61] sc1\n"
    "G_xok0%=:\n"
    "s_mov_b32 vcc_lo, 0xcccccccc\n"
    "s_mov_b32 vcc_hi, 0xcccccccc\n"
    "s_add_u32 m0, s50, 0\n"
    "s_add_u32 s64, s48, 0\n"
    "s_addc_u32 s65, s49, 0\n"
    "global_load_lds_dwordx4 v121, s[64:65] offset:0 sc1\n"
    "global_load_lds_dwordx4 v121, s[64:65] offset:1024 sc1\n"
    "global_load_lds_dwordx4 v121, s[64:65] offset:2048 sc1\n"
    "global_load_lds_dwordx4 v121, s[64:65] offset:3072 sc1\n"
    "s_add_u32 m0, s50, 4096\n"
    "s_add_u32 s64, s48, 4096\n"
    "s_addc_u32 s65, s49, 0\n"
    "global_load_lds_dwordx4 v121, s[64:65] offset:0 sc1\n"
    "global_load_lds_dwordx4 v121, s[64:65] offset:1024 sc1\n"
    "global_load_lds_dwordx4 v121, s[64:65] offset:2048 sc1\n"
    "global_load_lds_dwordx4 v121, s[64:65] offset:3072 sc1\n"
    "s_add_u32 m0, s50, 8192\n"
    "s_add_u32 s64, s48, 8192\n"
    "s_addc_u32 s65, s49, 0\n"
    "global_load_lds_dwordx4 v121, s[64:65] offset:0 sc1\n"
    "global_load_lds_dwordx4 v121, s[64:65] offset:1024 sc1\n"
    "global_load_lds_dwordx4 v121, s[64:65] offset:2048 sc1\n"
    "global_load_lds_dwordx4 v121, s[64:65] offset:3072 sc1\n"
    "s_add_u32 m0, s50, 12288\n"
    "s_add_u32 s64, s48, 12288\n"
    "s_addc_u32 s65, s49, 0\n"
    "s_add_u32 s48, s48, 0x10000\n"
    "s_addc_u32 s49, s49, 0\n"
    "global_load_lds_dwordx4 v121, s[64:65] offset:0 sc1\n"
    "global_load_lds_dwordx4 v121, s[64:65] offset:1024 sc1\n"
    "global_load_lds_dwordx4 v121, s[64:65] offset:2048 sc1\n"
    "global_load_lds_dwordx4 v121, s[64:65] offset:3072 sc1\n"
    "s_add_u32 m0, s50, 65536\n"
    "s_add_u32 s64, s48, 0\n"
    "s_addc_u32 s65, s49, 0\n"
    "global_load_lds_dwordx4 v121, s[64:65] offset:0 sc1\n"
    "global_load_lds_dwordx4 v121, s[64:65] offset:1024 sc1\n"
    "global_load_lds_dwordx4 v121, s[64:65] offset:2048 sc1\n"
    "global_load_lds_dwordx4 v121, s[64:65] offset:3072 sc1\n"
    "s_add_u32 m0, s50, 69632\n"
    "s_add_u32 s64, s48, 4096\n"
    "s_addc_u32 s65, s49, 0\n"
    "global_load_lds_dwordx4 v121, s[64:65] offset:0 sc1\n"
    "global_load_lds_dwordx4 v121, s[64:65] offset:1024 sc1\n"
    "global_load_lds_dwordx4 v121, s[64:65] offset:2048 sc1\n"
    "global_load_lds_dwordx4 v121, s[64:65] offset:3072 sc1\n"
    "s_add_u32 m0, s50, 73728\n"
    "s_add_u32 s64, s48, 8192\n"
    "s_addc_u32 s65, s49, 0\n"
    "global_load_lds_dwordx4 v121, s[64:65] offset:0 sc1\n"
    "global_load_lds_dwordx4 v121, s[64:65] offset:1024 sc1\n"
    "global_load_lds_dwordx4 v121, s[64:65] offset:2048 sc1\n"
    "s_mov_b64 s[66:67], s[44:45]\n"
    "s_add_u32 s44, s44, 0x8900000\n"
    "s_addc_u32 s45, s45, 0\n"
    "s_waitcnt vmcnt(19) lgkmcnt(0)\n"
    "s_barrier\n"
    "ds_read_b128 v[192:195], v122 offset:0\n"
    "ds_read_b128 v[196:199], v122 offset:16384\n"
    "ds_read_b128 v[200:203], v122 offset:1024\n"
    "ds_read_b128 v[204:207], v122 offset:17408\n"
    "ds_read_b128 v[208:211], v122 offset:2048\n"
    "ds_read_b128 v[212:215], v122 offset:18432\n"
    "s_waitcnt lgkmcnt(4)\n"
    "s_branch G_even%=\n"
    ".p2align 6\n"
    "G_loop%=:\n"
    "v_mfma_f32_16x16x32_bf16 v[160:163], v[192:195], a[0:3], 0\n"
    "ds_read_b128 v[216:219], v123 offset:3072\n"
    "v_mfma_f32_16x16x32_bf16 v[164:167], v[192:195], a[64:67], 0\n"
    "ds_read_b128 v[220:223], v123 offset:19456\n"
    "v_mfma_f32_16x16x32_bf16 v[168:171], v[192:195], a[128:131], 0\n"
    "v_mfma_f32_16x16x32_bf16 v[172:175], v[192:195], a[192:195], 0\n"
    "v_cvt_pk_bf16_f32 v224, v128, v129\n"
    "v_cvt_pk_bf16_f32 v225, v130, v131\n"
    "v_mfma_f32_16x16x32_bf16 v[176:179], v[196:199], a[0:3], 0\n"
    "global_load_lds_dwordx4 v121, s[64:65] offset:3072 sc1\n"
    "v_mfma_f32_16x16x32_bf16 v[180:183], v[196:199], a[64:67], 0\n"
    "v_cvt_pk_bf16_f32 v226, v144, v145\n"
    "v_cvt_pk_bf16_f32 v227, v146, v147\n"
    "v_mfma_f32_16x16x32_bf16 v[184:187], v[196:199], a[128:131], 0\n"
    "v_cvt_pk_bf16_f32 v228, v132, v133\n"
    "v_cvt_pk_bf16_f32 v229, v134, v135\n"
    "v_mfma_f32_16x16x32_bf16 v[188:191], v[196:199], a[192:195], 0\n"
    "v_cvt_pk_bf16_f32 v230, v148, v149\n"
    "v_cvt_pk_bf16_f32 v231, v150, v151\n"
    "s_waitcnt lgkmcnt(4)\n"
    "v_mfma_f32_16x16x32_bf16 v[160:163], v[200:203], a[4:7], v[160:163]\n"
    "ds_read_b128 v[192:195], v123 offset:4096\n"
    "v_mfma_f32_16x16x32_bf16 v[164:167], v[200:203], a[68:71], v[164:167]\n"
    "ds_read_b128 v[196:199], v123 offset:20480\n"
    "v_mfma_f32_16x16x32_bf16 v[168:171], v[200:203], a[132:135], v[168:171]\n"
    "s_add_u32 m0, s50, 12288\n"
    "s_add_u32 s64, s48, 12288\n"
    "s_addc_u32 s65, s49, 0\n"
    "v_cvt_pk_bf16_f32 v232, v136, v137\n"
    "v_cvt_pk_bf16_f32 v233, v138, v139\n"
    "v_mfma_f32_16x16x32_bf16 v[172:175], v[200:203], a[196:199], v[172:175]\n"
    "s_add_u32 s48, s48, 0x10000\n"
    "s_addc_u32 s49, s49, 0\n"
    "v_cvt_pk_bf16_f32 v234, v152, v153\n"
    "v_cvt_pk_bf16_f32 v235, v154, v155\n"
    "v_mfma_f32_16x16x32_bf16 v[176:179], v[204:207], a[4:7], v[176:179]\n"
    "global_load_lds_dwordx4 v121, s[64:65] offset:0 sc1\n"
    "v_mfma_f32_16x16x32_bf16 v[180:183], v[204:207], a[68:71], v[180:183]\n"
    "v_cvt_pk_bf16_f32 v236, v140, v141\n"
    "v_cvt_pk_bf16_f32 v237, v142, v143\n"
    "v_mfma_f32_16x16x32_bf16 v[184:187], v[204:207], a[132:135], v[184:187]\n"
    "global_store_dwordx4 v121, v[224:227], s[44:45] offset:0 sc1\n"
    "v_mfma_f32_16x16x32_bf16 v[188:191], v[204:207], a[196:199], v[188:191]\n"
    "v_cvt_pk_bf16_f32 v238, v156, v157\n"
    "v_cvt_pk_bf16_f32 v239, v158, v159\n"
    "s_waitcnt lgkmcnt(4)\n"
    "v_mfma_f32_16x16x32_bf16 v[160:163], v[208:211], a[8:11], v[160:163]\n"
    "ds_read_b128 v[200:203], v123 offset:5120\n"
    "v_mfma_f32_16x16x32_bf16 v[164:167], v[208:211], a[72:75], v[164:167]\n"
    "ds_read_b128 v[204:207], v123 offset:21504\n"
    "v_mfma_f32_16x16x32_bf16 v[168:171], v[208:211], a[136:139], v[168:171]\n"
    "v_mfma_f32_16x16x32_bf16 v[172:175], v[208:211], a[200:203], v[172:175]\n"
    "v_mfma_f32_16x16x32_bf16 v[176:179], v[212:215], a[8:11], v[176:179]\n"
    "global_load_lds_dwordx4 v121, s[64:65] offset:1024 sc1\n"
    "v_mfma_f32_16x16x32_bf16 v[180:183], v[212:215], a[72:75], v[180:183]\n"
    "v_mfma_f32_16x16x32_bf16 v[184:187], v[212:215], a[136:139], v[184:187]\n"
    "global_store_dwordx4 v121, v[228:231], s[44:45] offset:1024 sc1\n"
    "v_mfma_f32_16x16x32_bf16 v[188:191], v[212:215], a[200:203], v[188:191]\n"
    "s_waitcnt lgkmcnt(4)\n"
    "v_mfma_f32_16x16x32_bf16 v[160:163], v[216:219], a[12:15], v[160:163]\n"
    "ds_read_b128 v[208:211], v123 offset:6144\n"
    "v_mfma_f32_16x16x32_bf16 v[164:167], v[216:219], a[76:79], v[164:167]\n"
    "ds_read_b128 v[212:215], v123 offset:22528\n"
    "v_mfma_f32_16x16x32_bf16 v[168:171], v[216:219], a[140:143], v[168:171]\n"
    "v_mfma_f32_16x16x32_bf16 v[172:175], v[216:219], a[204:207], v[172:175]\n"
    "v_mfma_f32_16x16x32_bf16 v[176:179], v[220:223], a[12:15], v[176:179]\n"
    "global_load_lds_dwordx4 v121, s[64:65] offset:2048 sc1\n"
    "v_mfma_f32_16x16x32_bf16 v[180:183], v[220:223], a[76:79], v[180:183]\n"
    "v_mfma_f32_16x16x32_bf16 v[184:187], v[220:223], a[140:143], v[184:187]\n"
    "global_store_dwordx4 v121, v[232:235], s[44:45] offset:2048 sc1\n"
    "v_mfma_f32_16x16x32_bf16 v[188:191], v[220:223], a[204:207], v[188:191]\n"
    "s_waitcnt lgkmcnt(4)\n"
    "v_mfma_f32_16x16x32_bf16 v[160:163], v[192:195], a[16:19], v[160:163]\n"
    "ds_read_b128 v[216:219], v123 offset:7168\n"
    "v_mfma_f32_16x16x32_bf16 v[164:167], v[192:195], a[80:83], v[164:167]\n"
    "ds_read_b128 v[220:223], v123 offset:23552\n"
    "v_mfma_f32_16x16x32_bf16 v[168:171], v[192:195], a[144:147], v[168:171]\n"
    "v_mfma_f32_16x16x32_bf16 v[172:175], v[192:195], a[208:211], v[172:175]\n"
    "v_mfma_f32_16x16x32_bf16 v[176:179], v[196:199], a[16:19], v[176:179]\n"
    "global_load_lds_dwordx4 v121, s[64:65] offset:3072 sc1\n"
    "v_mfma_f32_16x16x32_bf16 v[180:183], v[196:199], a[80:83], v[180:183]\n"
    "v_mfma_f32_16x16x32_bf16 v[184:187], v[196:199], a[144:147], v[184:187]\n"
    "global_store_dwordx4 v121, v[236:239], s[44:45] offset:3072 sc1\n"
    "v_mfma_f32_16x16x32_bf16 v[188:191], v[196:199], a[208:211], v[188:191]\n"
    "s_waitcnt lgkmcnt(4)\n"
    "v_mfma_f32_16x16x32_bf16 v[160:163], v[200:203], a[20:23], v[160:163]\n"
    "s_waitcnt vmcnt(12) lgkmcnt(0)\n"
    "s_barrier\n"
    "v_mfma_f32_16x16x32_bf16 v[164:167], v[200:203], a[84:87], v[164:167]\n"
    "ds_read_b128 v[192:195], v123 offset:8192\n"
    "v_mfma_f32_16x16x32_bf16 v[168:171], v[200:203], a[148:151], v[168:171]\n"
    "ds_read_b128 v[196:199], v123 offset:24576\n"
    "s_add_u32 m0, s50, 65536\n"
    "s_add_u32 s64, s48, 0\n"
    "s_addc_u32 s65, s49, 0\n"
    "v_mfma_f32_16x16x32_bf16 v[172:175], v[200:203], a[212:215], v[172:175]\n"
    "v_mfma_f32_16x16x32_bf16 v[176:179], v[204:207], a[20:23], v[176:179]\n"
    "global_load_lds_dwordx4 v121, s[64:65] offset:0 sc1\n"
    "v_mfma_f32_16x16x32_bf16 v[180:183], v[204:207], a[84:87], v[180:183]\n"
    "v_mfma_f32_16x16x32_bf16 v[184:187], v[204:207], a[148:151], v[184:187]\n"
    "v_mfma_f32_16x16x32_bf16 v[188:191], v[204:207], a[212:215], v[188:191]\n"
    "s_waitcnt lgkmcnt(4)\n"
    "v_mfma_f32_16x16x32_bf16 v[160:163], v[208:211], a[24:27], v[160:163]\n"
    "ds_read_b128 v[200:203], v123 offset:9216\n"
    "v_mfma_f32_16x16x32_bf16 v[164:167], v[208:211], a[88:91], v[164:167]\n"
    "ds_read_b128 v[204:207], v123 offset:25600\n"
    "v_mfma_f32_16x16x32_bf16 v[168:171], v[208:211], a[152:155], v[168:171]\n"
    "v_mfma_f32_16x16x32_bf16 v[172:175], v[208:211], a[216:219], v[172:175]\n"
    "v_mfma_f32_16x16x32_bf16 v[176:179], v[212:215], a[24:27], v[176:179]\n"
    "global_load_lds_dwordx4 v121, s[64:65] offset:1024 sc1\n"
    "v_mfma_f32_16x16x32_bf16 v[180:183], v[212:215], a[88:91], v[180:183]\n"
    "v_mfma_f32_16x16x32_bf16 v[184:187], v[212:215], a[152:155], v[184:187]\n"
    "v_mfma_f32_16x16x32_bf16 v[188:191], v[212:215], a[216:219], v[188:191]\n"
    "s_waitcnt lgkmcnt(4)\n"
    "v_mfma_f32_16x16x32_bf16 v[160:163], v[216:219], a[28:31], v[160:163]\n"
    "ds_read_b128 v[208:211], v123 offset:10240\n"
    "v_mfma_f32_16x16x32_bf16 v[164:167], v[216:219], a[92:95], v[164:167]\n"
    "ds_read_b128 v[212:215], v123 offset:26624\n"
    "v_mfma_f32_16x16x32_bf16 v[168:171], v[216:219], a[156:159], v[168:171]\n"
    "v_mfma_f32_16x16x32_bf16 v[172:175], v[216:219], a[220:223], v[172:175]\n"
    "v_mfma_f32_16x16x32_bf16 v[176:179], v[220:223], a[28:31], v[176:179]\n"
    "global_load_lds_dwordx4 v121, s[64:65] offset:2048 sc1\n"
    "v_mfma_f32_16x16x32_bf16 v[180:183], v[220:223], a[92:95], v[180:183]\n"
    "s_mov_b64 s[44:45], s[66:67]\n"
    "s_add_u32 s66, s66, 0x80000\n"
    "s_addc_u32 s67, s67, 0\n"
    "v_mfma_f32_16x16x32_bf16 v[184:187], v[220:223], a[156:159], v[184:187]\n"
    "v_mfma_f32_16x16x32_bf16 v[188:191], v[220:223], a[220:223], v[188:191]\n"
    "s_waitcnt lgkmcnt(4)\n"
    "v_mfma_f32_16x16x32_bf16 v[160:163], v[192:195], a[32:35], v[160:163]\n"
    "ds_read_b128 v[216:219], v123 offset:11264\n"
    "v_mfma_f32_16x16x32_bf16 v[164:167], v[192:195], a[96:99], v[164:167]\n"
    "ds_read_b128 v[220:223], v123 offset:27648\n"
    "v_mfma_f32_16x16x32_bf16 v[168:171], v[192:195], a[160:163], v[168:171]\n"
    "v_mfma_f32_16x16x32_bf16 v[172:175], v[192:195], a[224:227], v[172:175]\n"
    "v_mfma_f32_16x16x32_bf16 v[176:179], v[196:199], a[32:35], v[176:179]\n"
    "global_load_lds_dwordx4 v121, s[64:65] offset:3072 sc1\n"
    "v_mfma_f32_16x16x32_bf16 v[180:183], v[196:199], a[96:99], v[180:183]\n"
    "v_mfma_f32_16x16x32_bf16 v[184:187], v[196:199], a[160:163], v[184:187]\n"
    "v_mfma_f32_16x16x32_bf16 v[188:191], v[196:199], a[224:227], v[188:191]\n"
    "s_waitcnt lgkmcnt(4)\n"
    "v_mfma_f32_16x16x32_bf16 v[160:163], v[200:203], a[36:39], v[160:163]\n"
    "ds_read_b128 v[192:195], v123 offset:12288\n"
    "v_mfma_f32_16x16x32_bf16 v[164:167], v[200:203], a[100:103], v[164:167]\n"
    "ds_read_b128 v[196:199], v123 offset:28672\n"
    "v_mfma_f32_16x16x32_bf16 v[168:171], v[200:203], a[164:167], v[168:171]\n"
    "s_add_u32 m0, s50, 69632\n"
    "s_add_u32 s64, s48, 4096\n"
    "s_addc_u32 s65, s49, 0\n"
    "v_mfma_f32_16x16x32_bf16 v[172:175], v[200:203], a[228:231], v[172:175]\n"
    "v_mfma_f32_16x16x32_bf16 v[176:179], v[204:207], a[36:39], v[176:179]\n"
    "global_load_lds_dwordx4 v121, s[64:65] offset:0 sc1\n"
    "v_mfma_f32_16x16x32_bf16 v[180:183], v[204:207], a[100:103], v[180:183]\n"
    "v_mfma_f32_16x16x32_bf16 v[184:187], v[204:207], a[164:167], v[184:187]\n"
    "v_mfma_f32_16x16x32_bf16 v[188:191], v[204:207], a[228:231], v[188:191]\n"
    "s_waitcnt lgkmcnt(4)\n"
    "v_mfma_f32_16x16x32_bf16 v[160:163], v[208:211], a[40:43], v[160:163]\n"
    "ds_read_b128 v[200:203], v123 offset:13312\n"
    "v_mfma_f32_16x16x32_bf16 v[164:167], v[208:211], a[104:107], v[164:167]\n"
    "ds_read_b128 v[204:207], v123 offset:29696\n"
    "v_mfma_f32_16x16x32_bf16 v[168:171], v[208:211], a[168:171], v[168:171]\n"
    "v_mfma_f32_16x16x32_bf16 v[172:175], v[208:211], a[232:235], v[172:175]\n"
    "v_mfma_f32_16x16x32_bf16 v[176:179], v[212:215], a[40:43], v[176:179]\n"
    "global_load_lds_dwordx4 v121, s[64:65] offset:1024 sc1\n"
    "v_mfma_f32_16x16x32_bf16 v[180:183], v[212:215], a[104:107], v[180:183]\n"
    "v_mfma_f32_16x16x32_bf16 v[184:187], v[212:215], a[168:171], v[184:187]\n"
    "v_mfma_f32_16x16x32_bf16 v[188:191], v[212:215], a[232:235], v[188:191]\n"
    "s_waitcnt lgkmcnt(4)\n"
    "v_mfma_f32_16x16x32_bf16 v[160:163], v[216:219], a[44:47], v[160:163]\n"
    "ds_read_b128 v[208:211], v123 offset:14336\n"
    "v_mfma_f32_16x16x32_bf16 v[164:167], v[216:219], a[108:111], v[164:167]\n"
    "ds_read_b128 v[212:215], v123 offset:30720\n"
    "v_mfma_f32_16x16x32_bf16 v[168:171], v[216:219], a[172:175], v[168:171]\n"
    "v_mfma_f32_16x16x32_bf16 v[172:175], v[216:219], a[236:239], v[172:175]\n"
    "v_mfma_f32_16x16x32_bf16 v[176:179], v[220:223], a[44:47], v[176:179]\n"
    "global_load_lds_dwordx4 v121, s[64:65] offset:2048 sc1\n"
    "v_mfma_f32_16x16x32_bf16 v[180:183], v[220:223], a[108:111], v[180:183]\n"
    "v_mfma_f32_16x16x32_bf16 v[184:187], v[220:223], a[172:175], v[184:187]\n"
    "v_mfma_f32_16x16x32_bf16 v[188:191], v[220:223], a[236:239], v[188:191]\n"
    "s_waitcnt lgkmcnt(4)\n"
    "v_mfma_f32_16x16x32_bf16 v[160:163], v[192:195], a[48:51], v[160:163]\n"
    "ds_read_b128 v[216:219], v123 offset:15360\n"
    "v_mfma_f32_16x16x32_bf16 v[164:167], v[192:195], a[112:115], v[164:167]\n"
    "ds_read_b128 v[220:223], v123 offset:31744\n"
    "v_mfma_f32_16x16x32_bf16 v[168:171], v[192:195], a[176:179], v[168:171]\n"
    "v_mfma_f32_16x16x32_bf16 v[172:175], v[192:195], a[240:243], v[172:175]\n"
    "v_mfma_f32_16x16x32_bf16 v[176:179], v[196:199], a[48:51], v[176:179]\n"
    "global_load_lds_dwordx4 v121, s[64:65] offset:3072 sc1\n"
    "v_mfma_f32_16x16x32_bf16 v[180:183], v[196:199], a[112:115], v[180:183]\n"
    "v_mfma_f32_16x16x32_bf16 v[184:187], v[196:199], a[176:179], v[184:187]\n"
    "v_mfma_f32_16x16x32_bf16 v[188:191], v[196:199], a[240:243], v[188:191]\n"
    "s_waitcnt lgkmcnt(4)\n"
    "v_mfma_f32_16x16x32_bf16 v[160:163], v[200:203], a[52:55], v[160:163]\n"
    "s_waitcnt vmcnt(8) lgkmcnt(0)\n"
    "s_barrier\n"
    "v_mfma_f32_16x16x32_bf16 v[164:167], v[200:203], a[116:119], v[164:167]\n"
    "ds_read_b128 v[192:195], v122 offset:0\n"
    "v_mfma_f32_16x16x32_bf16 v[168:171], v[200:203], a[180:183], v[168:171]\n"
    "ds_read_b128 v[196:199], v122 offset:16384\n"
    "s_add_u32 m0, s50, 73728\n"
    "s_add_u32 s64, s48, 8192\n"
    "s_addc_u32 s65, s49, 0\n"
    "v_mfma_f32_16x16x32_bf16 v[172:175], v[200:203], a[244:247], v[172:175]\n"
    "v_mfma_f32_16x16x32_bf16 v[176:179], v[204:207], a[52:55], v[176:179]\n"
    "global_load_lds_dwordx4 v121, s[64:65] offset:0 sc1\n"
    "v_mfma_f32_16x16x32_bf16 v[180:183], v[204:207], a[116:119], v[180:183]\n"
    "s_cmp_lg_u32 s56, 0\n"
    "s_cbranch_scc1 G_f1%=\n"
    "s_or_b32 s58, s42, s51\n"
    "s_mov_b64 exec, 1\n"
    "v_mov_b32 v116, s58\n"
    "global_store_dwordx2 v118, v[116:117], s[54:55] sc1\n"
    "s_mov_b64 exec, -1\n"
    "G_f1%=:\n"
    "v_mfma_f32_16x16x32_bf16 v[184:187], v[204:207], a[180:183], v[184:187]\n"
    "v_mfma_f32_16x16x32_bf16 v[188:191], v[204:207], a[244:247], v[188:191]\n"
    "s_waitcnt lgkmcnt(4)\n"
    "v_mfma_f32_16x16x32_bf16 v[160:163], v[208:211], a[56:59], v[160:163]\n"
    "ds_read_b128 v[200:203], v122 offset:1024\n"
    "v_mfma_f32_16x16x32_bf16 v[164:167], v[208:211], a[120:123], v[164:167]\n"
    "ds_read_b128 v[204:207], v122 offset:17408\n"
    "v_mfma_f32_16x16x32_bf16 v[168:171], v[208:211], a[184:187], v[168:171]\n"
    "v_mfma_f32_16x16x32_bf16 v[172:175], v[208:211], a[248:251], v[172:175]\n"
    "v_mfma_f32_16x16x32_bf16 v[176:179], v[212:215], a[56:59], v[176:179]\n"
    "global_load_lds_dwordx4 v121, s[64:65] offset:1024 sc1\n"
    "v_mfma_f32_16x16x32_bf16 v[180:183], v[212:215], a[120:123], v[180:183]\n"
    "v_mfma_f32_16x16x32_bf16 v[184:187], v[212:215], a[184:187], v[184:187]\n"
    "v_mfma_f32_16x16x32_bf16 v[188:191], v[212:215], a[248:251], v[188:191]\n"
    "s_add_u32 s51, s51, 1\n"
    "s_waitcnt lgkmcnt(4)\n"
    "v_mfma_f32_16x16x32_bf16 v[160:163], v[216:219], a[60:63], v[160:163]\n"
    "ds_read_b128 v[208:211], v122 offset:2048\n"
    "v_mfma_f32_16x16x32_bf16 v[164:167], v[216:219], a[124:127], v[164:167]\n"
    "ds_read_b128 v[212:215], v122 offset:18432\n"
    "v_mfma_f32_16x16x32_bf16 v[168:171], v[216:219], a[188:191], v[168:171]\n"
    "v_mfma_f32_16x16x32_bf16 v[172:175], v[216:219], a[252:255], v[172:175]\n"
    "v_mfma_f32_16x16x32_bf16 v[176:179], v[220:223], a[60:63], v[176:179]\n"
    "global_load_lds_dwordx4 v121, s[64:65] offset:2048 sc1\n"
    "v_mfma_f32_16x16x32_bf16 v[180:183], v[220:223], a[124:127], v[180:183]\n"
    "v_mfma_f32_16x16x32_bf16 v[184:187], v[220:223], a[188:191], v[184:187]\n"
    "v_mfma_f32_16x16x32_bf16 v[188:191], v[220:223], a[252:255], v[188:191]\n"
    "s_add_u32 s58, s51, -3\n"
    "s_cmp_gt_u32 s58, 28\n"
    "s_waitcnt lgkmcnt(4)\n"
    "s_cbranch_scc1 G_t1%=\n"
    "G_even%=:\n"
    "v_mfma_f32_16x16x32_bf16 v[128:131], v[192:195], a[0:3], 0\n"
    "ds_read_b128 v[216:219], v122 offset:3072\n"
    "v_mfma_f32_16x16x32_bf16 v[132:135], v[192:195], a[64:67], 0\n"
    "ds_read_b128 v[220:223], v122 offset:19456\n"
    "v_mfma_f32_16x16x32_bf16 v[136:139], v[192:195], a[128:131], 0\n"
    "v_mfma_f32_16x16x32_bf16 v[140:143], v[192:195], a[192:195], 0\n"
    "v_cvt_pk_bf16_f32 v224, v160, v161\n"
    "v_cvt_pk_bf16_f32 v225, v162, v163\n"
    "v_mfma_f32_16x16x32_bf16 v[144:147], v[196:199], a[0:3], 0\n"
    "global_load_lds_dwordx4 v121, s[64:65] offset:3072 sc1\n"
    "v_mfma_f32_16x16x32_bf16 v[148:151], v[196:199], a[64:67], 0\n"
    "v_cvt_pk_bf16_f32 v226, v176, v177\n"
    "v_cvt_pk_bf16_f32 v227, v178, v179\n"
    "v_mfma_f32_16x16x32_bf16 v[152:155], v[196:199], a[128:131], 0\n"
    "v_cvt_pk_bf16_f32 v228, v164, v165\n"
    "v_cvt_pk_bf16_f32 v229, v166, v167\n"
    "v_mfma_f32_16x16x32_bf16 v[156:159], v[196:199], a[192:195], 0\n"
    "v_cvt_pk_bf16_f32 v230, v180, v181\n"
    "v_cvt_pk_bf16_f32 v231, v182, v183\n"
    "s_waitcnt lgkmcnt(4)\n"
    "v_mfma_f32_16x16x32_bf16 v[128:131], v[200:203], a[4:7], v[128:131]\n"
    "ds_read_b128 v[192:195], v122 offset:4096\n"
    "v_mfma_f32_16x16x32_bf16 v[132:135], v[200:203], a[68:71], v[132:135]\n"
    "ds_read_b128 v[196:199], v122 offset:20480\n"
    "v_mfma_f32_16x16x32_bf16 v[136:139], v[200:203], a[132:135], v[136:139]\n"
    "s_add_u32 m0, s50, 77824\n"
    "s_add_u32 s64, s48, 12288\n"
    "s_addc_u32 s65, s49, 0\n"
    "v_cvt_pk_bf16_f32 v232, v168, v169\n"
    "v_cvt_pk_bf16_f32 v233, v170, v171\n"
    "v_mfma_f32_16x16x32_bf16 v[140:143], v[200:203], a[196:199], v[140:143]\n"
    "s_add_u32 s48, s48, 0x10000\n"
    "s_addc_u32 s49, s49, 0\n"
    "v_cvt_pk_bf16_f32 v234, v184, v185\n"
    "v_cvt_pk_bf16_f32 v235, v186, v187\n"
    "v_mfma_f32_16x16x32_bf16 v[144:147], v[204:207], a[4:7], v[144:147]\n"
    "global_load_lds_dwordx4 v121, s[64:65] offset:0 sc1\n"
    "v_mfma_f32_16x16x32_bf16 v[148:151], v[204:207], a[68:71], v[148:151]\n"
    "v_cvt_pk_bf16_f32 v236, v172, v173\n"
    "v_cvt_pk_bf16_f32 v237, v174, v175\n"
    "v_mfma_f32_16x16x32_bf16 v[152:155], v[204:207], a[132:135], v[152:155]\n"
    "global_store_dwordx4 v121, v[224:227], s[44:45] offset:0 sc1\n"
    "v_mfma_f32_16x16x32_bf16 v[156:159], v[204:207], a[196:199], v[156:159]\n"
    "v_cvt_pk_bf16_f32 v238, v188, v189\n"
    "v_cvt_pk_bf16_f32 v239, v190, v191\n"
    "s_waitcnt lgkmcnt(4)\n"
    "v_mfma_f32_16x16x32_bf16 v[128:131], v[208:211], a[8:11], v[128:131]\n"
    "ds_read_b128 v[200:203], v122 offset:5120\n"
    "v_mfma_f32_16x16x32_bf16 v[132:135], v[208:211], a[72:75], v[132:135]\n"
    "ds_read_b128 v[204:207], v122 offset:21504\n"
    "v_mfma_f32_16x16x32_bf16 v[136:139], v[208:211], a[136:139], v[136:139]\n"
    "v_mfma_f32_16x16x32_bf16 v[140:143], v[208:211], a[200:203], v[140:143]\n"
    "v_mfma_f32_16x16x32_bf16 v[144:147], v[212:215], a[8:11], v[144:147]\n"
    "global_load_lds_dwordx4 v121, s[64:65] offset:1024 sc1\n"
    "v_mfma_f32_16x16x32_bf16 v[148:151], v[212:215], a[72:75], v[148:151]\n"
    "v_mfma_f32_16x16x32_bf16 v[152:155], v[212:215], a[136:139], v[152:155]\n"
    "global_store_dwordx4 v121, v[228:231], s[44:45] offset:1024 sc1\n"
    "v_mfma_f32_16x16x32_bf16 v[156:159], v[212:215], a[200:203], v[156:159]\n"
    "s_waitcnt lgkmcnt(4)\n"
    "v_mfma_f32_16x16x32_bf16 v[128:131], v[216:219], a[12:15], v[128:131]\n"
    "ds_read_b128 v[208:211], v122 offset:6144\n"
    "v_mfma_f32_16x16x32_bf16 v[132:135], v[216:219], a[76:79], v[132:135]\n"
    "ds_read_b128 v[212:215], v122 offset:22528\n"
    "v_mfma_f32_16x16x32_bf16 v[136:139], v[216:219], a[140:143], v[136:139]\n"
    "v_mfma_f32_16x16x32_bf16 v[140:143], v[216:219], a[204:207], v[140:143]\n"
    "v_mfma_f32_16x16x32_bf16 v[144:147], v[220:223], a[12:15], v[144:147]\n"
    "global_load_lds_dwordx4 v121, s[64:65] offset:2048 sc1\n"
    "v_mfma_f32_16x16x32_bf16 v[148:151], v[220:223], a[76:79], v[148:151]\n"
    "v_mfma_f32_16x16x32_bf16 v[152:155], v[220:223], a[140:143], v[152:155]\n"
    "global_store_dwordx4 v121, v[232:235], s[44:45] offset:2048 sc1\n"
    "v_mfma_f32_16x16x32_bf16 v[156:159], v[220:223], a[204:207], v[156:159]\n"
    "s_waitcnt lgkmcnt(4)\n"
    "v_mfma_f32_16x16x32_bf16 v[128:131], v[192:195], a[16:19], v[128:131]\n"
    "ds_read_b128 v[216:219], v122 offset:7168\n"
    "v_mfma_f32_16x16x32_bf16 v[132:135], v[192:195], a[80:83], v[132:135]\n"
    "ds_read_b128 v[220:223], v122 offset:23552\n"
    "v_mfma_f32_16x16x32_bf16 v[136:139], v[192:195], a[144:147], v[136:139]\n"
    "v_mfma_f32_16x16x32_bf16 v[140:143], v[192:195], a[208:211], v[140:143]\n"
    "v_mfma_f32_16x16x32_bf16 v[144:147], v[196:199], a[16:19], v[144:147]\n"
    "global_load_lds_dwordx4 v121, s[64:65] offset:3072 sc1\n"
    "v_mfma_f32_16x16x32_bf16 v[148:151], v[196:199], a[80:83], v[148:151]\n"
    "v_mfma_f32_16x16x32_bf16 v[152:155], v[196:199], a[144:147], v[152:155]\n"
    "global_store_dwordx4 v121, v[236:239], s[44:45] offset:3072 sc1\n"
    "v_mfma_f32_16x16x32_bf16 v[156:159], v[196:199], a[208:211], v[156:159]\n"
    "s_waitcnt lgkmcnt(4)\n"
    "v_mfma_f32_16x16x32_bf16 v[128:131], v[200:203], a[20:23], v[128:131]\n"
    "s_waitcnt vmcnt(12) lgkmcnt(0)\n"
    "s_barrier\n"
    "v_mfma_f32_16x16x32_bf16 v[132:135], v[200:203], a[84:87], v[132:135]\n"
    "ds_read_b128 v[192:195], v122 offset:8192\n"
    "v_mfma_f32_16x16x32_bf16 v[136:139], v[200:203], a[148:151], v[136:139]\n"
    "ds_read_b128 v[196:199], v122 offset:24576\n"
    "s_add_u32 m0, s50, 0\n"
    "s_add_u32 s64, s48, 0\n"
    "s_addc_u32 s65, s49, 0\n"
    "v_mfma_f32_16x16x32_bf16 v[140:143], v[200:203], a[212:215], v[140:143]\n"
    "v_mfma_f32_16x16x32_bf16 v[144:147], v[204:207], a[20:23], v[144:147]\n"
    "global_load_lds_dwordx4 v121, s[64:65] offset:0 sc1\n"
    "v_mfma_f32_16x16x32_bf16 v[148:151], v[204:207], a[84:87], v[148:151]\n"
    "v_mfma_f32_16x16x32_bf16 v[152:155], v[204:207], a[148:151], v[152:155]\n"
    "v_mfma_f32_16x16x32_bf16 v[156:159], v[204:207], a[212:215], v[156:159]\n"
    "s_waitcnt lgkmcnt(4)\n"
    "v_mfma_f32_16x16x32_bf16 v[128:131], v[208:211], a[24:27], v[128:131]\n"
    "ds_read_b128 v[200:203], v122 offset:9216\n"
    "v_mfma_f32_16x16x32_bf16 v[132:135], v[208:211], a[88:91], v[132:135]\n"
    "ds_read_b128 v[204:207], v122 offset:25600\n"
    "v_mfma_f32_16x16x32_bf16 v[136:139], v[208:211], a[152:155], v[136:139]\n"
    "v_mfma_f32_16x16x32_bf16 v[140:143], v[208:211], a[216:219], v[140:143]\n"
    "v_mfma_f32_16x16x32_bf16 v[144:147], v[212:215], a[24:27], v[144:147]\n"
    "global_load_lds_dwordx4 v121, s[64:65] offset:1024 sc1\n"
    "v_mfma_f32_16x16x32_bf16 v[148:151], v[212:215], a[88:91], v[148:151]\n"
    "v_mfma_f32_16x16x32_bf16 v[152:155], v[212:215], a[152:155], v[152:155]\n"
    "v_mfma_f32_16x16x32_bf16 v[156:159], v[212:215], a[216:219], v[156:159]\n"
    "s_waitcnt lgkmcnt(4)\n"
    "v_mfma_f32_16x16x32_bf16 v[128:131], v[216:219], a[28:31], v[128:131]\n"
    "ds_read_b128 v[208:211], v122 offset:10240\n"
    "v_mfma_f32_16x16x32_bf16 v[132:135], v[216:219], a[92:95], v[132:135]\n"
    "ds_read_b128 v[212:215], v122 offset:26624\n"
    "v_mfma_f32_16x16x32_bf16 v[136:139], v[216:219], a[156:159], v[136:139]\n"
    "v_mfma_f32_16x16x32_bf16 v[140:143], v[216:219], a[220:223], v[140:143]\n"
    "v_mfma_f32_16x16x32_bf16 v[144:147], v[220:223], a[28:31], v[144:147]\n"
    "global_load_lds_dwordx4 v121, s[64:65] offset:2048 sc1\n"
    "v_mfma_f32_16x16x32_bf16 v[148:151], v[220:223], a[92:95], v[148:151]\n"
    "s_mov_b64 s[44:45], s[66:67]\n"
    "s_add_u32 s66, s66, 0x80000\n"
    "s_addc_u32 s67, s67, 0\n"
    "v_mfma_f32_16x16x32_bf16 v[152:155], v[220:223], a[156:159], v[152:155]\n"
    "v_mfma_f32_16x16x32_bf16 v[156:159], v[220:223], a[220:223], v[156:159]\n"
    "s_waitcnt lgkmcnt(4)\n"
    "v_mfma_f32_16x16x32_bf16 v[128:131], v[192:195], a[32:35], v[128:131]\n"
    "ds_read_b128 v[216:219], v122 offset:11264\n"
    "v_mfma_f32_16x16x32_bf16 v[132:135], v[192:195], a[96:99], v[132:135]\n"
    "ds_read_b128 v[220:223], v122 offset:27648\n"
    "v_mfma_f32_16x16x32_bf16 v[136:139], v[192:195], a[160:163], v[136:139]\n"
    "v_mfma_f32_16x16x32_bf16 v[140:143], v[192:195], a[224:227], v[140:143]\n"
    "v_mfma_f32_16x16x32_bf16 v[144:147], v[196:199], a[32:35], v[144:147]\n"
    "global_load_lds_dwordx4 v121, s[64:65] offset:3072 sc1\n"
    "v_mfma_f32_16x16x32_bf16 v[148:151], v[196:199], a[96:99], v[148:151]\n"
    "v_mfma_f32_16x16x32_bf16 v[152:155], v[196:199], a[160:163], v[152:155]\n"
    "v_mfma_f32_16x16x32_bf16 v[156:159], v[196:199], a[224:227], v[156:159]\n"
    "s_waitcnt lgkmcnt(4)\n"
    "v_mfma_f32_16x16x32_bf16 v[128:131], v[200:203], a[36:39], v[128:131]\n"
    "ds_read_b128 v[192:195], v122 offset:12288\n"
    "v_mfma_f32_16x16x32_bf16 v[132:135], v[200:203], a[100:103], v[132:135]\n"
    "ds_read_b128 v[196:199], v122 offset:28672\n"
    "v_mfma_f32_16x16x32_bf16 v[136:139], v[200:203], a[164:167], v[136:139]\n"
    "s_add_u32 m0, s50, 4096\n"
    "s_add_u32 s64, s48, 4096\n"
    "s_addc_u32 s65, s49, 0\n"
    "v_mfma_f32_16x16x32_bf16 v[140:143], v[200:203], a[228:231], v[140:143]\n"
    "v_mfma_f32_16x16x32_bf16 v[144:147], v[204:207], a[36:39], v[144:147]\n"
    "global_load_lds_dwordx4 v121, s[64:65] offset:0 sc1\n"
    "v_mfma_f32_16x16x32_bf16 v[148:151], v[204:207], a[100:103], v[148:151]\n"
    "v_mfma_f32_16x16x32_bf16 v[152:155], v[204:207], a[164:167], v[152:155]\n"
    "v_mfma_f32_16x16x32_bf16 v[156:159], v[204:207], a[228:231], v[156:159]\n"
    "s_waitcnt lgkmcnt(4)\n"
    "v_mfma_f32_16x16x32_bf16 v[128:131], v[208:211], a[40:43], v[128:131]\n"
    "ds_read_b128 v[200:203], v122 offset:13312\n"
    "v_mfma_f32_16x16x32_bf16 v[132:135], v[208:211], a[104:107], v[132:135]\n"
    "ds_read_b128 v[204:207], v122 offset:29696\n"
    "v_mfma_f32_16x16x32_bf16 v[136:139], v[208:211], a[168:171], v[136:139]\n"
    "v_mfma_f32_16x16x32_bf16 v[140:143], v[208:211], a[232:235], v[140:143]\n"
    "v_mfma_f32_16x16x32_bf16 v[144:147], v[212:215], a[40:43], v[144:147]\n"
    "global_load_lds_dwordx4 v121, s[64:65] offset:1024 sc1\n"
    "v_mfma_f32_16x16x32_bf16 v[148:151], v[212:215], a[104:107], v[148:151]\n"
    "v_mfma_f32_16x16x32_bf16 v[152:155], v[212:215], a[168:171], v[152:155]\n"
    "v_mfma_f32_16x16x32_bf16 v[156:159], v[212:215], a[232:235], v[156:159]\n"
    "s_waitcnt lgkmcnt(4)\n"
    "v_mfma_f32_16x16x32_bf16 v[128:131], v[216:219], a[44:47], v[128:131]\n"
    "ds_read_b128 v[208:211], v122 offset:14336\n"
    "v_mfma_f32_16x16x32_bf16 v[132:135], v[216:219], a[108:111], v[132:135]\n"
    "ds_read_b128 v[212:215], v122 offset:30720\n"
    "v_mfma_f32_16x16x32_bf16 v[136:139], v[216:219], a[172:175], v[136:139]\n"
    "v_mfma_f32_16x16x32_bf16 v[140:143], v[216:219], a[236:239], v[140:143]\n"
    "v_mfma_f32_16x16x32_bf16 v[144:147], v[220:223], a[44:47], v[144:147]\n"
    "global_load_lds_dwordx4 v121, s[64:65] offset:2048 sc1\n"
    "v_mfma_f32_16x16x32_bf16 v[148:151], v[220:223], a[108:111], v[148:151]\n"
    "v_mfma_f32_16x16x32_bf16 v[152:155], v[220:223], a[172:175], v[152:155]\n"
    "v_mfma_f32_16x16x32_bf16 v[156:159], v[220:223], a[236:239], v[156:159]\n"
    "s_waitcnt lgkmcnt(4)\n"
    "v_mfma_f32_16x16x32_bf16 v[128:131], v[192:195], a[48:51], v[128:131]\n"
    "ds_read_b128 v[216:219], v122 offset:15360\n"
    "v_mfma_f32_16x16x32_bf16 v[132:135], v[192:195], a[112:115], v[132:135]\n"
    "ds_read_b128 v[220:223], v122 offset:31744\n"
    "v_mfma_f32_16x16x32_bf16 v[136:139], v[192:195], a[176:179], v[136:139]\n"
    "v_mfma_f32_16x16x32_bf16 v[140:143], v[192:195], a[240:243], v[140:143]\n"
    "v_mfma_f32_16x16x32_bf16 v[144:147], v[196:199], a[48:51], v[144:147]\n"
    "global_load_lds_dwordx4 v121, s[64:65] offset:3072 sc1\n"
    "v_mfma_f32_16x16x32_bf16 v[148:151], v[196:199], a[112:115], v[148:151]\n"
    "v_mfma_f32_16x16x32_bf16 v[152:155], v[196:199], a[176:179], v[152:155]\n"
    "v_mfma_f32_16x16x32_bf16 v[156:159], v[196:199], a[240:243], v[156:159]\n"
    "s_waitcnt lgkmcnt(4)\n"
    "v_mfma_f32_16x16x32_bf16 v[128:131], v[200:203], a[52:55], v[128:131]\n"
    "s_waitcnt vmcnt(8) lgkmcnt(0)\n"
    "s_barrier\n"
    "v_mfma_f32_16x16x32_bf16 v[132:135], v[200:203], a[116:119], v[132:135]\n"
    "ds_read_b128 v[192:195], v123 offset:0\n"
    "v_mfma_f32_16x16x32_bf16 v[136:139], v[200:203], a[180:183], v[136:139]\n"
    "ds_read_b128 v[196:199], v123 offset:16384\n"
    "s_add_u32 m0, s50, 8192\n"
    "s_add_u32 s64, s48, 8192\n"
    "s_addc_u32 s65, s49, 0\n"
    "v_mfma_f32_16x16x32_bf16 v[140:143], v[200:203], a[244:247], v[140:143]\n"
    "v_mfma_f32_16x16x32_bf16 v[144:147], v[204:207], a[52:55], v[144:147]\n"
    "global_load_lds_dwordx4 v121, s[64:65] offset:0 sc1\n"
    "v_mfma_f32_16x16x32_bf16 v[148:151], v[204:207], a[116:119], v[148:151]\n"
    "s_cmp_lg_u32 s56, 0\n"
    "s_cbranch_scc1 G_f2%=\n"
    "s_or_b32 s58, s42, s51\n"
    "s_mov_b64 exec, 1\n"
    "v_mov_b32 v116, s58\n"
    "global_store_dwordx2 v118, v[116:117], s[54:55] sc1\n"
    "s_mov_b64 exec, -1\n"
    "G_f2%=:\n"
    "v_mfma_f32_16x16x32_bf16 v[152:155], v[204:207], a[180:183], v[152:155]\n"
    "v_mfma_f32_16x16x32_bf16 v[156:159], v[204:207], a[244:247], v[156:159]\n"
    "s_waitcnt lgkmcnt(4)\n"
    "v_mfma_f32_16x16x32_bf16 v[128:131], v[208:211], a[56:59], v[128:131]\n"
    "ds_read_b128 v[200:203], v123 offset:1024\n"
    "v_mfma_f32_16x16x32_bf16 v[132:135], v[208:211], a[120:123], v[132:135]\n"
    "ds_read_b128 v[204:207], v123 offset:17408\n"
    "v_mfma_f32_16x16x32_bf16 v[136:139], v[208:211], a[184:187], v[136:139]\n"
    "v_mfma_f32_16x16x32_bf16 v[140:143], v[208:211], a[248:251], v[140:143]\n"
    "v_mfma_f32_16x16x32_bf16 v[144:147], v[212:215], a[56:59], v[144:147]\n"
    "global_load_lds_dwordx4 v121, s[64:65] offset:1024 sc1\n"
    "v_mfma_f32_16x16x32_bf16 v[148:151], v[212:215], a[120:123], v[148:151]\n"
    "v_mfma_f32_16x16x32_bf16 v[152:155], v[212:215], a[184:187], v[152:155]\n"
    "v_mfma_f32_16x16x32_bf16 v[156:159], v[212:215], a[248:251], v[156:159]\n"
    "s_add_u32 s51, s51, 1\n"
    "s_waitcnt lgkmcnt(4)\n"
    "v_mfma_f32_16x16x32_bf16 v[128:131], v[216:219], a[60:63], v[128:131]\n"
    "ds_read_b128 v[208:211], v123 offset:2048\n"
    "v_mfma_f32_16x16x32_bf16 v[132:135], v[216:219], a[124:127], v[132:135]\n"
    "ds_read_b128 v[212:215], v123 offset:18432\n"
    "v_mfma_f32_16x16x32_bf16 v[136:139], v[216:219], a[188:191], v[136:139]\n"
    "v_mfma_f32_16x16x32_bf16 v[140:143], v[216:219], a[252:255], v[140:143]\n"
    "v_mfma_f32_16x16x32_bf16 v[144:147], v[220:223], a[60:63], v[144:147]\n"
    "global_load_lds_dwordx4 v121, s[64:65] offset:2048 sc1\n"
    "v_mfma_f32_16x16x32_bf16 v[148:151], v[220:223], a[124:127], v[148:151]\n"
    "v_mfma_f32_16x16x32_bf16 v[152:155], v[220:223], a[188:191], v[152:155]\n"
    "v_mfma_f32_16x16x32_bf16 v[156:159], v[220:223], a[252:255], v[156:159]\n"
    "s_add_u32 s58, s51, -3\n"
    "s_cmp_gt_u32 s58, 28\n"
    "s_waitcnt lgkmcnt(4)\n"
    "s_cbranch_scc0 G_loop%=\n"
    "s_nop 7\n"
    "v_cvt_pk_bf16_f32 v224, v128, v129\n"
    "v_cvt_pk_bf16_f32 v225, v130, v131\n"
    "v_cvt_pk_bf16_f32 v226, v144, v145\n"
    "v_cvt_pk_bf16_f32 v227, v146, v147\n"
    "v_cvt_pk_bf16_f32 v228, v132, v133\n"
    "v_cvt_pk_bf16_f32 v229, v134, v135\n"
    "v_cvt_pk_bf16_f32 v230, v148, v149\n"
    "v_cvt_pk_bf16_f32 v231, v150, v151\n"
    "v_cvt_pk_bf16_f32 v232, v136, v137\n"
    "v_cvt_pk_bf16_f32 v233, v138, v139\n"
    "v_cvt_pk_bf16_f32 v234, v152, v153\n"
    "v_cvt_pk_bf16_f32 v235, v154, v155\n"
    "v_cvt_pk_bf16_f32 v236, v140, v141\n"
    "v_cvt_pk_bf16_f32 v237, v142, v143\n"
    "v_cvt_pk_bf16_f32 v238, v156, v157\n"
    "v_cvt_pk_bf16_f32 v239, v158, v159\n"
    "global_store_dwordx4 v121, v[224:227], s[44:45] offset:0 sc1\n"
    "global_store_dwordx4 v121, v[228:231], s[44:45] offset:1024 sc1\n"
    "global_store_dwordx4 v121, v[232:235], s[44:45] offset:2048 sc1\n"
    "global_store_dwordx4 v121, v[236:239], s[44:45] offset:3072 sc1\n"
    "s_waitcnt vmcnt(0)\n"
    "s_barrier\n"
    "s_cmp_lg_u32 s56, 0\n"
    "s_cbranch_scc1 G_f3%=\n"
    "s_or_b32 s58, s42, s51\n"
    "s_mov_b64 exec, 1\n"
    "v_mov_b32 v116, s58\n"
    "global_store_dwordx2 v118, v[116:117], s[54:55] sc1\n"
    "s_mov_b64 exec, -1\n"
    "G_f3%=:\n"
    "s_branch G_loop%=\n"
    "G_t1%=:\n"
    "s_nop 7\n"
    "v_cvt_pk_bf16_f32 v224, v160, v161\n"
    "v_cvt_pk_bf16_f32 v225, v162, v163\n"
    "v_cvt_pk_bf16_f32 v226, v176, v177\n"
    "v_cvt_pk_bf16_f32 v227, v178, v179\n"
    "v_cvt_pk_bf16_f32 v228, v164, v165\n"
    "v_cvt_pk_bf16_f32 v229, v166, v167\n"
    "v_cvt_pk_bf16_f32 v230, v180, v181\n"
    "v_cvt_pk_bf16_f32 v231, v182, v183\n"
    "v_cvt_pk_bf16_f32 v232, v168, v169\n"
    "v_cvt_pk_bf16_f32 v233, v170, v171\n"
    "v_cvt_pk_bf16_f32 v234, v184, v185\n"
    "v_cvt_pk_bf16_f32 v235, v186, v187\n"
    "v_cvt_pk_bf16_f32 v236, v172, v173\n"
    "v_cvt_pk_bf16_f32 v237, v174, v175\n"
    "v_cvt_pk_bf16_f32 v238, v188, v189\n"
    "v_cvt_pk_bf16_f32 v239, v190, v191\n"
    "global_store_dwordx4 v121, v[224:227], s[44:45] offset:0 sc1\n"
    "global_store_dwordx4 v121, v[228:231], s[44:45] offset:1024 sc1\n"
    "global_store_dwordx4 v121, v[232:235], s[44:45] offset:2048 sc1\n"
    "global_store_dwordx4 v121, v[236:239], s[44:45] offset:3072 sc1\n"
    "s_waitcnt vmcnt(0)\n"
    "s_barrier\n"
    "s_cmp_lg_u32 s56, 0\n"
    "s_cbranch_scc1 G_f4%=\n"
    "s_or_b32 s58, s42, s51\n"
    "s_mov_b64 exec, 1\n"
    "v_mov_b32 v116, s58\n"
    "global_store_dwordx2 v118, v[116:117], s[54:55] sc1\n"
    "s_mov_b64 exec, -1\n"
    "G_f4%=:\n"
    "s_cmp_eq_u32 s51, 32\n"
    "s_cbranch_scc1 G_end%=\n"
    "s_add_u32 s58, s40, 0x20e000\n"
    "s_addc_u32 s59, s41, 0\n"
    "s_add_u32 s60, s40, 0x200200\n"
    "s_addc_u32 s61, s41, 0\n"
    "s_or_b32 s62, s42, 1\n"
    "s_mov_b32 s63, s43\n"
    "v_lshlrev_b32 v224, 3, v121\n"
    "G_xw1%=:\n"
    "global_load_dwordx2 v[226:227], v224, s[58:59] sc1\n"
    "G_xc1%=:\n"
    "s_waitcnt vmcnt(0)\n"
    "v_cmp_ne_u64 vcc, s[62:63], v[226:227]\n"
    "s_nop 0\n"
    "s_cbranch_vccz G_xok1%=\n"
    "s_sleep 4\n"
    "s_add_u32 s57, s57, 1\n"
    "s_and_b32 s68, s57, 0xff\n"
    "s_cbranch_scc1 G_xw1%=\n"
    "global_load_dwordx2 v[226:227], v118, s[60:61] sc1\n"
    "s_waitcnt vmcnt(0)\n"
    "v_cmp_eq_u64 vcc, s[42:43], v[226:227]\n"
    "s_nop 0\n"
    "s_cbranch_vccnz G_xok1%=\n"
    "s_cmp_lt_u32 s57, 0x40000\n"
    "s_cbranch_scc1 G_xw1%=\n"
    "v_mov_b32 v116, s42\n"
    "global_store_dwordx2 v118, v[116:117], s[60:61] sc1\n"
    "G_xok1%=:\n"
    "s_mov_b32 vcc_lo, 0xcccccccc\n"
    "s_mov_b32 vcc_hi, 0xcccccccc\n"
    "s_branch G_even%=\n"
    "G_end%=:\n"
    "s_waitcnt lgkmcnt(0)\n"
    :: GIN : GCLOB);
  return T;
}

__device__ __forceinline__ void xa_phase(char* ws, int grp, int h, u64 T, int lane) {
  const int l15 = lane & 15, g = lane >> 4;
  const float scale = 0.08838834764831845f;
  const __amdgpu_buffer_rsrc_t rs = __builtin_amdgcn_make_buffer_rsrc(ws, 0, (int)(164 * MBY), 0x00020000);
  auto ld16 = [&](int off) { return __builtin_bit_cast(bf16x8, __builtin_amdgcn_raw_buffer_load_b128(rs, off, 0, 16)); };
#pragma unroll 1
  for (int s = grp; s < NSEG; s += 4) {
    wait_flag<8>(ws, (unsigned)OFF_RDY + ((lane < 3 ? lane : 0) * 32 + h) * 128, T, (unsigned)(s >> 1) + 1u);
    const int qp = (int)OFF_QF + (s * 32 + h) * 8192 + lane * 16;
    const int kp = (int)OFF_KF + (s * 32 + h) * 8192 + ((l15 >> 1) & 1) * 1024 + (g * 16 + (l15 & 13)) * 16;
    const int vp = (int)OFF_VF + (s * 32 + h) * 8192 + lane * 16;
    const int km = (int)OFF_KM + (s * 32 + h) * 4096;
    const int vm = (int)OFF_VM + (s * 32 + h) * 4096;
    bf16x8 qf[4][2], vf[8];
    f32x4 sc[2][3];
#pragma unroll
    for (int nt = 0; nt < 2; ++nt)
#pragma unroll
      for (int t = 0; t < 3; ++t) sc[nt][t] = (f32x4){0.f, 0.f, 0.f, 0.f};
#pragma unroll
    for (int ks = 0; ks < 4; ++ks) {
      bf16x8 kf[2];
#pragma unroll
      for (int nt = 0; nt < 2; ++nt) qf[ks][nt] = ld16(qp + ks * 2048 + nt * 1024);
#pragma unroll
      for (int t = 0; t < 2; ++t) kf[t] = ld16(kp + (ks >> 1) * 4096 + (ks & 1) * 32 + t * 2048);
#pragma unroll
      for (int nt = 0; nt < 2; ++nt)
#pragma unroll
        for (int t = 0; t < 2; ++t) sc[nt][t] = __builtin_amdgcn_mfma_f32_16x16x32_bf16(kf[t], qf[ks][nt], sc[nt][t], 0, 0, 0);
    }
#pragma unroll
    for (int e = 0; e < 8; ++e) vf[e] = ld16(vp + e * 1024);
    float mt[2], lt[2];
    f32x4 acc[2][8];
#pragma unroll
    for (int nt = 0; nt < 2; ++nt) {
      const int qi = nt * 16 + l15;
      float v[2][4], m = -3.0e38f;
#pragma unroll
      for (int t = 0; t < 2; ++t)
#pragma unroll
        for (int r = 0; r < 4; ++r) {
          v[t][r] = (t * 16 + g * 4 + r <= qi) ? rbf(rbf(sc[nt][t][r]) * scale) : -3.0e38f;
          m = fmaxf(m, v[t][r]);
        }
      m = max_g(m);
#pragma unroll
      for (int t = 0; t < 2; ++t)
#pragma unroll
        for (int r = 0; r < 4; r += 2) {
          v[t][r] = (t * 16 + g * 4 + r <= qi) ? __expf(v[t][r] - m) : 0.f;
          v[t][r + 1] = (t * 16 + g * 4 + r + 1 <= qi) ? __expf(v[t][r + 1] - m) : 0.f;
          rbf2(v[t][r], v[t][r + 1]);
        }
      mt[nt] = m;
      lt[nt] = sum_g(((v[0][0] + v[0][1]) + (v[0][2] + v[0][3])) + ((v[1][0] + v[1][1]) + (v[1][2] + v[1][3])));
      const bf16x8 pf = mk8(pk2(v[0][0], v[0][1]), pk2(v[0][2], v[0][3]), pk2(v[1][0], v[1][1]), pk2(v[1][2], v[1][3]));
#pragma unroll
      for (int e = 0; e < 8; ++e) acc[nt][e] = __builtin_amdgcn_mfma_f32_16x16x32_bf16(vf[e], pf, (f32x4){0.f, 0.f, 0.f, 0.f}, 0, 0, 0);
    }
#pragma unroll
    for (int e = 0; e < 8; ++e) asm volatile("" : "+v"(acc[0][e]), "+v"(acc[1][e]));
    asm volatile("" : "+v"(mt[0]), "+v"(mt[1]), "+v"(lt[0]), "+v"(lt[1]));
    if (h == 5) TLX(s * 8);
    wait_flag<PSL>(ws, (unsigned)OFF_FLAGS + (s * 64 + h + (lane & 1) * 32) * 128, T, 1u);
    if (s == 63) TL(4);
    if (h == 5) TLX(s * 8 + 1);
    bf16x8 k2[4];
    unsigned mv[4][2];
#pragma unroll
    for (int ks = 0; ks < 4; ++ks) k2[ks] = ld16(km + (ks >> 1) * 1024 + (g * 16 + ((l15 >> 3) | ((l15 >> 1) & 2)) * 4 + (ks & 1) * 2 + (l15 & 1)) * 16);
#pragma unroll
    for (int w = 0; w < 4; ++w) { const auto t = __builtin_amdgcn_raw_buffer_load_b64(rs, vm + w * 512 + lane * 8, 0, 16); mv[w][0] = t[0]; mv[w][1] = t[1]; }
#pragma unroll
    for (int ks = 0; ks < 4; ++ks)
#pragma unroll
      for (int nt = 0; nt < 2; ++nt) sc[nt][2] = __builtin_amdgcn_mfma_f32_16x16x32_bf16(k2[ks], qf[ks][nt], sc[nt][2], 0, 0, 0);
#pragma unroll
    for (int nt = 0; nt < 2; ++nt) {
      float a = sc[nt][2][0], b = sc[nt][2][1];
      rbf2(a, b); a *= scale; b *= scale; rbf2(a, b);
      const float m = fmaxf(mt[nt], max_g(fmaxf(a, b)));
      const float al = __expf(mt[nt] - m);
      a = __expf(a - m); b = __expf(b - m);
      const float inv = __builtin_amdgcn_rcpf(al * lt[nt] + sum_g(a + b));
      const float sa = al * inv;
      const bf16x8 pf1 = mk8(pk2(a * inv, b * inv), 0u, 0u, 0u);
#pragma unroll
      for (int e = 0; e < 8; ++e) acc[nt][e] = __builtin_amdgcn_mfma_f32_16x16x32_bf16(mk8(mv[e >> 1][e & 1], 0u, 0u, 0u), pf1, acc[nt][e] * sa, 0, 0, 0);
#pragma unroll
      for (int w = 0; w < 4; ++w) {
        const f32x4 a0 = acc[nt][2 * w], a1 = acc[nt][2 * w + 1];
        u32x4 o; o.x = pk2(a0[0], a0[1]); o.y = pk2(a1[0], a1[1]); o.z = pk2(a0[2], a0[3]); o.w = pk2(a1[2], a1[3]);
        store16_sc1(ws + OFF_O, (unsigned)(((s * 32 + h) * 8 + nt * 4 + w) * 1024 + lane * 16), o);
      }
    }
    asm volatile("s_waitcnt vmcnt(0)" ::: "memory");
    if (lane == 0) set_flag(ws, OFF_OFL + (s * 32 + h) * 128, T, 1);
    if (h == 5) TLX(s * 8 + 2);
  }
}

__device__ __forceinline__ void op_phase(char* ws, const u16* __restrict__ x, const u16* __restrict__ w_r, u16* out, const u16* __restrict__ Wo, int grp, int ks4, int cs, u64 T, char* lds, int tid) {
  const int lane = tid & 63, wv = __builtin_amdgcn_readfirstlane(tid >> 6), l15 = lane & 15, g = lane >> 4;
  const __amdgpu_buffer_rsrc_t rs = __builtin_amdgcn_make_buffer_rsrc(ws, 0, (int)(164 * MBY), 0x00020000);
  const int h0 = ks4 * 8 + wv * 2;
  bf16x8 wo[2][32];
#pragma unroll
  for (int hh = 0; hh < 2; ++hh) {
#pragma unroll
    for (int i = 0; i < 32; ++i) {
      const int r = i * 4 + g, f = (r & 1) | ((r >> 1) & 2) | ((r >> 2) & 4);
      __builtin_amdgcn_global_load_lds((const GAS void*)(Wo + (size_t)((h0 + hh) * DH + r) * DM + cs * 128 + ((l15 ^ (f << 1)) * 8)),
                                       (LAS void*)(lds + wv * 32768 + i * 1024), 16, 0, 0);
    }
    asm volatile("s_waitcnt vmcnt(0)" ::: "memory");
    const int qq = l15 >> 2, f = (qq & 1) | ((g & 1) << 1) | ((qq >> 1) << 2);
    const char* rp = lds + wv * 32768 + (g * 4 + (qq & 1) + (qq >> 1) * 16) * 256 + (l15 & 3) * 8;
#pragma unroll
    for (int j = 0; j < 8; ++j)
#pragma unroll
      for (int ks = 0; ks < 4; ++ks) {
        const char* p = rp + ks * 8192 + ((j ^ f) * 32);
        wo[hh][j * 4 + ks] = tr_frag(p, p + 512);
      }
#pragma unroll
    for (int f2 = 0; f2 < 32; ++f2) asm volatile("" : "+a"(wo[hh][f2]));
    asm volatile("s_waitcnt lgkmcnt(0)" ::: "memory");
  }
  __syncthreads();
  constexpr int RS = 528, RW = 32 * RS;
  auto ln2 = [&](int s, bool fresh) {
      const int row = s * 32 + (ks4 * 4 + cs) * 2 + (wv & 1);
      const u32x4 xv = *(const u32x4*)(x + (size_t)row * DM + lane * 8);
      float wr[8];
      unpack8(*(const u32x4*)(w_r + lane * 8), wr);
      if (fresh) __builtin_amdgcn_s_sleep(HSL);
      wait_flag<PSL>(ws, (unsigned)OFF_HFL + s * 2048 + l15 * 128, T, 1u);
      if (s == 63) TL(7);
      float f[8], y[8];
      load_h(rs, xv, row, lane, f);
      ln_row(f, y);
      float r = 0.f;
#pragma unroll
      for (int i = 0; i < 8; ++i) r += y[i] * wr[i];
      r = wave_sum(r);
      u32x4 z;
      z.x = pk2(f[0] + y[0], f[1] + y[1]); z.y = pk2(f[2] + y[2], f[3] + y[3]); z.z = pk2(f[4] + y[4], f[5] + y[5]); z.w = pk2(f[6] + y[6], f[7] + y[7]);
      const unsigned rw = (pk2(r, r) & 0xffff0000u) | 1u;
      if (lane < 8 && row >= NSPEC) __hip_atomic_store((gu32*)(ws + OFF_RC2 + (lane * 64 + ((row - NSPEC) >> 2)) * 128 + (row & 3) * 4), rw, RLX_AGENT);
      store16_sc1(out, (unsigned)(row * 1024 + lane * 16), z);
      asm volatile("s_waitcnt vmcnt(0)" ::: "memory");
      if (lane == 0) __hip_atomic_store((gu32*)(ws + OFF_RC + (row >> 2) * 128 + (row & 3) * 4), rw, RLX_AGENT);
      if ((row & 31) == 0) TLX(s * 8 + 6);
  };
  int pend = -1, turn = 0;
  bool have = false;
  bf16x8 of[16];
  auto load_o = [&](int s) {
    const int ob = (int)OFF_O + (s * 32 + h0) * 8192 + lane * 16;
#pragma unroll
    for (int f = 0; f < 16; ++f) of[f] = __builtin_bit_cast(bf16x8, __builtin_amdgcn_raw_buffer_load_b128(rs, ob + f * 1024, 0, 16));
  };
#pragma unroll 1
  for (int s = grp, sn; s < NSEG; s = sn) {
    sn = s + 6 < NSIX ? s + 6 : (s < NSIX ? (grp < 4 ? NSIX + grp : NSEG) : s + 4);
    f32x4 acc[2][8];
#pragma unroll
    for (int i = 0; i < 2; ++i)
#pragma unroll
      for (int j = 0; j < 8; ++j) { acc[i][j] = (f32x4){0.f, 0.f, 0.f, 0.f}; asm volatile("" : "+v"(acc[i][j])); }
    if (!have) {
      if (pend >= 0) { ln2(pend, true); pend = -1; }
      wait_flag<PSL>(ws, (unsigned)OFF_OFL + (s * 32 + h0 + (lane & 1)) * 128, T, 1u);
      if (s == 63) TL(1);
      load_o(s);
    }
    if (ks4 == 0 && cs == 0 && tid == 0) TLX(s * 8 + 3);
    const u64 nf = __hip_atomic_load((gu64*)(ws + OFF_OFL + (min(sn, NSEG - 1) * 32 + h0 + (lane & 1)) * 128), RLX_AGENT);
    __builtin_amdgcn_sched_barrier(0);
#pragma unroll
    for (int f = 0; f < 16; ++f)
#pragma unroll
      for (int j = 0; j < 8; ++j)
        asm volatile("v_mfma_f32_16x16x32_bf16 %0, %1, %2, %0" : "+v"(acc[(f >> 2) & 1][j]) : "a"(wo[f >> 3][j * 4 + (f & 3)]), "v"(of[f]));
    __builtin_amdgcn_sched_barrier(0);
    have = sn < NSEG && __all(nf == T + 1);
    if (have) load_o(sn);
    asm volatile("s_nop 7\n\ts_nop 3" : "+v"(acc[1][4]), "+v"(acc[1][5]), "+v"(acc[1][6]), "+v"(acc[1][7]), "+v"(acc[1][0]), "+v"(acc[1][1]), "+v"(acc[1][2]), "+v"(acc[1][3]));
    __builtin_amdgcn_sched_barrier(0);
    if (s == 63) TL(3);
    if (ks4 == 0 && cs == 0 && tid == 0) TLX(s * 8 + 4);
#pragma unroll
    for (int tt = 0; tt < 2; ++tt)
#pragma unroll
      for (int j = 0; j < 8; ++j) *(f32x4*)(lds + wv * RW + (tt * 16 + l15) * RS + j * 64 + g * 16) = acc[tt][j];
    asm volatile("s_waitcnt lgkmcnt(0)" ::: "memory");
    __builtin_amdgcn_s_barrier();
    asm volatile("" ::: "memory");
#pragma unroll
    for (int i = 0; i < 4; ++i) {
      const int row = i * 8 + (tid >> 5);
      const char* p = lds + row * RS + (tid & 31) * 16;
      const f32x4 a = (*(const f32x4*)p + *(const f32x4*)(p + RW)) + (*(const f32x4*)(p + 2 * RW) + *(const f32x4*)(p + 3 * RW));
      store16_sc1(ws + OFF_HP, (unsigned)(((s * 4 + ks4) * 4 + cs) * 16384 + i * 4096 + tid * 16), __builtin_bit_cast(u32x4, a));
    }
    asm volatile("s_waitcnt vmcnt(0)" ::: "memory");
    __syncthreads();
    if (tid == 0) set_flag(ws, OFF_HFL + (s * 16 + ks4 * 4 + cs) * 128, T, 1);
    if (s == 63) TL(6);
    if (ks4 == 0 && cs == 0 && tid == 0) TLX(s * 8 + 5);
    if (turn == 0 && pend >= 0) { ln2(pend, false); pend = -1; }
    if ((wv >> 1) == turn) pend = s;
    turn ^= 1;
  }
  if (pend >= 0) ln2(pend, true);
}

__device__ __forceinline__ unsigned rkey(unsigned w, int idx) { return ((w ^ (((int)w >> 31) | 0x80000000u)) & 0xffff0000u) | (unsigned)(0xffff - idx); }
__device__ __forceinline__ int key_row(unsigned k) { return (0xffff - (int)(k & 0xffffu)) & 2047; }
__device__ __forceinline__ float key_val(unsigned k) { k &= 0xffff0000u; return __builtin_bit_cast(float, (k & 0x80000000u) ? k ^ 0x80000000u : (~k) & 0xffff0000u); }
__device__ __forceinline__ unsigned wave_umax(unsigned v) {
  v = max(v, (unsigned)__builtin_amdgcn_update_dpp(0, (int)v, 0x121, 0xf, 0xf, false)); v = max(v, (unsigned)__builtin_amdgcn_update_dpp(0, (int)v, 0x122, 0xf, 0xf, false));
  v = max(v, (unsigned)__builtin_amdgcn_update_dpp(0, (int)v, 0x124, 0xf, 0xf, false)); v = max(v, (unsigned)__builtin_amdgcn_update_dpp(0, (int)v, 0x128, 0xf, 0xf, false));
  return max(max((unsigned)__builtin_amdgcn_readlane((int)v, 0), (unsigned)__builtin_amdgcn_readlane((int)v, 16)),
             max((unsigned)__builtin_amdgcn_readlane((int)v, 32), (unsigned)__builtin_amdgcn_readlane((int)v, 48)));
}
__device__ __forceinline__ void push2(unsigned& a, unsigned& b, unsigned k) { b = max(b, min(a, k)); a = max(a, k); }
__device__ __forceinline__ void wave_top2(unsigned& a, unsigned& b) {
  const unsigned m = wave_umax(a);
  b = wave_umax(a == m ? b : a); a = m;
}
__device__ __forceinline__ void top2(char* ws, u64 T, int nrows, int tid, unsigned& k0, unsigned& k1, unsigned* sk) {
  const __amdgpu_buffer_rsrc_t rs = __builtin_amdgcn_make_buffer_rsrc(ws, 0, (int)(164 * MBY), 0x00020000);
  const int lane = tid & 63, wv = tid >> 6;
  const bool use0 = tid * 4 < nrows, use1 = (256 + tid) * 4 < nrows;
  u32x4 c[2];
  unsigned spins = 0;
  if (wv == 0) { wait_flag<12>(ws, (unsigned)OFF_HFL + ((nrows >> 5) - 1) * 2048, T, 1u); __builtin_amdgcn_s_sleep(48); }
  __syncthreads();
  for (;;) {
    c[0] = __builtin_bit_cast(u32x4, __builtin_amdgcn_raw_buffer_load_b128(rs, (int)OFF_RC + (use0 ? tid : 0) * 128, 0, 16));
    c[1] = __builtin_bit_cast(u32x4, __builtin_amdgcn_raw_buffer_load_b128(rs, (int)OFF_RC + (use1 ? 256 + tid : 0) * 128, 0, 16));
    if (__all(c[0].x & c[0].y & c[0].z & c[0].w & c[1].x & c[1].y & c[1].z & c[1].w & 1u)) break;
    __builtin_amdgcn_s_sleep(8);
    if (spin_bail(spins, ws, T)) break;
    asm volatile("" ::: "memory");
  }
  unsigned a = 0, b = 0;
#pragma unroll
  for (int k = 0; k < 2; ++k)
#pragma unroll
    for (int e = 0; e < 4; ++e) { const unsigned w = c[k][e]; push2(a, b, (k == 0 ? use0 : use1) ? rkey(w, (k * 256 + tid) * 4 + e) : 0u); }
  wave_top2(a, b);
  if (lane == 0) { sk[wv * 2] = a; sk[wv * 2 + 1] = b; }
  __syncthreads();
  a = 0; b = 0;
#pragma unroll
  for (int i = 0; i < 8; ++i) push2(a, b, sk[i]);
  k0 = a; k1 = b;
}
__device__ __forceinline__ bool spec_holds(char* ws, u64 T, unsigned xcc, unsigned& k0, unsigned& k1, unsigned* sk, int tid) {
  static_assert(S_ - NSPEC <= 256, "one line per lane");
  if (tid < 64) {
    const __amdgpu_buffer_rsrc_t rs = __builtin_amdgcn_make_buffer_rsrc(ws, 0, (int)(164 * MBY), 0x00020000);
    const bool use = tid < (S_ - NSPEC) / 4;
    u32x4 c;
    unsigned spins = 0;
    for (;;) {
      c = __builtin_bit_cast(u32x4, __builtin_amdgcn_raw_buffer_load_b128(rs, (int)OFF_RC2 + (xcc * 64 + (use ? tid : 0)) * 128, 0, 16));
      if (__all(c.x & c.y & c.z & c.w & 1u)) break;
      __builtin_amdgcn_s_sleep(2);
      if (spin_bail(spins, ws, T)) break;
      asm volatile("" ::: "memory");
    }
    unsigned a = 0, b = 0;
#pragma unroll
    for (int e = 0; e < 4; ++e) { const unsigned w = c[e]; push2(a, b, use ? rkey(w, NSPEC + tid * 4 + e) : 0u); }
    wave_top2(a, b);
    push2(a, b, k0); push2(a, b, k1);
    if (tid == 0) { sk[8] = a; sk[9] = b; }
  }
  __syncthreads();
  const bool same = sk[8] == k0 && sk[9] == k1;
  k0 = sk[8]; k1 = sk[9];
  return same;
}
__device__ __forceinline__ void sel_rows(char* ws, const u16* __restrict__ x, u64 T, int tid, int i0, int i1, float* hs, float* ys, char* yb) {
  const __amdgpu_buffer_rsrc_t rs = __builtin_amdgcn_make_buffer_rsrc(ws, 0, (int)(164 * MBY), 0x00020000);
  const int lane = tid & 63, wv = tid >> 6;
  if (wv < 2) {
    const int row = wv ? i1 : i0;
    for (unsigned spins = 0; __hip_atomic_load((gu32*)(ws + OFF_RC + (row >> 2) * 128 + (row & 3) * 4), RLX_AGENT) == 0u;)
      if (spin_bail(spins, ws, T)) break;
    float f[8], y[8];
    load_h(rs, *(const u32x4*)(x + (size_t)row * DM + lane * 8), row, lane, f);
    ln_row(f, y);
#pragma unroll
    for (int i = 0; i < 8; ++i) { hs[wv * DM + lane * 8 + i] = f[i]; ys[wv * DM + lane * 8 + i] = y[i]; }
    u32x4 o; o.x = pk2(y[0], y[1]); o.y = pk2(y[2], y[3]); o.z = pk2(y[4], y[5]); o.w = pk2(y[6], y[7]);
    *(u32x4*)(yb + wv * 1040 + lane * 16) = o;
  }
  __syncthreads();
}
template <int NF>
__device__ __forceinline__ void load_frags(bf16x8 (&w)[NF], const u16* __restrict__ src, int pitch, char* st, int lane) {
  const int l15 = lane & 15, g = lane >> 4, qq = l15 >> 2;
#pragma unroll
  for (int b = 0; b < NF / 2; b += 16) {
    constexpr int NK = 16;
#pragma unroll
    for (int i = 0; i < NK * 2; ++i) {
      if (b * 2 + i < NF) {
        const int r = i * 16 + (lane >> 2);
        __builtin_amdgcn_global_load_lds((const GAS void*)(src + (size_t)(b * 32 + r) * pitch + (((lane & 3) ^ ((r >> 2) & 2)) * 8)), (LAS void*)(st + i * 1024), 16, 0, 0);
      }
    }
    asm volatile("s_waitcnt vmcnt(0)" ::: "memory");
#pragma unroll
    for (int ks = 0; ks < NK; ++ks)
#pragma unroll
      for (int j = 0; j < 2; ++j)
        if ((b + ks) * 2 < NF) {
          const char* p = st + (ks * 32 + g * 8 + qq) * 64 + ((j ^ (g & 1)) * 32) + (l15 & 3) * 8;
          w[(b + ks) * 2 + j] = tr_frag(p, p + 256);
        }
#pragma unroll
    for (int f = 0; f < NK * 2; ++f) if (b * 2 + f < NF) asm volatile("" : "+a"(w[b * 2 + f]));
    asm volatile("s_waitcnt lgkmcnt(0)" ::: "memory");
  }
}

__device__ __forceinline__ void mlp1_phase(char* ws, const u16* __restrict__ x, const u16* __restrict__ wg, const u16* __restrict__ wu, int m, u64 T, char* lds, int tid) {
  unsigned* sk = (unsigned*)lds; float* hs = (float*)(lds + 128); float* xs = hs + 2 * DM; char* yb = (char*)(xs + 2 * DM);
  const int lane = tid & 63, wv = __builtin_amdgcn_readfirstlane(tid >> 6), l15 = lane & 15, g = lane >> 4;
  bf16x8 a[32], b[32];
  load_frags<32>(a, wg + m * 128 + wv * 32, FF, lds + 16384 + wv * 32768, lane);
  load_frags<32>(b, wu + m * 128 + wv * 32, FF, lds + 16384 + wv * 32768, lane);
  unsigned xcc, k0, k1;
  asm volatile("s_getreg_b32 %0, hwreg(HW_REG_XCC_ID)" : "=s"(xcc));
  xcc &= 7u;
#pragma unroll 1
  for (int round = 1; round < 3; ++round) {
    if (round < 2) top2(ws, T, NSPEC, tid, k0, k1, sk);
    const int i0 = key_row(k0), i1 = key_row(k1);
    TL(1);
    sel_rows(ws, x, T, tid, i0, i1, hs, xs, yb);
    TL(3);
    bf16x8 xf[16];
#pragma unroll
    for (int ks = 0; ks < 16; ++ks) xf[ks] = *(const bf16x8*)(yb + (l15 & 1) * 1040 + ks * 64 + g * 16);
    f32x4 acc[4];
#pragma unroll
    for (int j = 0; j < 4; ++j) { acc[j] = (f32x4){0.f, 0.f, 0.f, 0.f}; asm volatile("" : "+v"(acc[j])); }
    asm volatile("s_nop 1" ::: "memory");
#pragma unroll
    for (int ks = 0; ks < 16; ++ks)
#pragma unroll
      for (int j = 0; j < 2; ++j) {
        asm volatile("v_mfma_f32_16x16x32_bf16 %0, %1, %2, %0" : "+v"(acc[j]) : "v"(xf[ks]), "a"(a[ks * 2 + j]));
        asm volatile("v_mfma_f32_16x16x32_bf16 %0, %1, %2, %0" : "+v"(acc[2 + j]) : "v"(xf[ks]), "a"(b[ks * 2 + j]));
      }
    asm volatile("s_nop 7\n\ts_nop 3" : "+v"(acc[0]), "+v"(acc[1]), "+v"(acc[2]), "+v"(acc[3]));
    {
      const int t = g >> 1, j = g & 1;
      const float g0 = j ? acc[1][0] : acc[0][0], g1 = j ? acc[1][1] : acc[0][1], u0 = j ? acc[3][0] : acc[2][0], u1 = j ? acc[3][1] : acc[2][1];
      const float gt = rbf(t ? g1 : g0), u = rbf(t ? u1 : u0);
      const float sg = rbf(gt / (1.0f + __expf(-gt)));
      const float av = sg * u;
      u16* ab = (u16*)(lds + 12288) + wv * 64;
      ab[lane] = (u16)pk2(av, av);
      __builtin_amdgcn_fence(__ATOMIC_RELEASE, "wavefront"); __builtin_amdgcn_wave_barrier(); __builtin_amdgcn_fence(__ATOMIC_ACQUIRE, "wavefront");
      store16_sc1(ws + OFF_ACT, (unsigned)(round * 65536 + (lane >> 3) * 8192 + (m * 4 + wv) * 128 + (lane & 7) * 16), *(const u32x4*)(ab + (lane & 7) * 8));
    }
    TL(6);
    if (round == 1 && spec_holds(ws, T, xcc, k0, k1, sk, tid)) break;
  }
}

__device__ __forceinline__ void mlp2_phase(char* ws, const u16* __restrict__ x, const u16* __restrict__ wd, u16* out, int m, u64 T, char* lds, int tid) {
  unsigned* sk = (unsigned*)lds; float* hs = (float*)(lds + 128); float* xs = hs + 2 * DM; char* yb = (char*)(xs + 2 * DM);
  float* part = (float*)(lds + 12288);
  const __amdgpu_buffer_rsrc_t rs = __builtin_amdgcn_make_buffer_rsrc(ws, 0, (int)(164 * MBY), 0x00020000);
  const int lane = tid & 63, wv = __builtin_amdgcn_readfirstlane(tid >> 6), l15 = lane & 15, g = lane >> 4;
  bf16x8 a[28];
  load_frags<28>(a, wd + (size_t)wv * 448 * DM + m * 32, DM, lds + 16384 + wv * 32768, lane);
  unsigned xcc, k0, k1;
  asm volatile("s_getreg_b32 %0, hwreg(HW_REG_XCC_ID)" : "=s"(xcc));
  xcc &= 7u;
#pragma unroll 1
  for (int round = 1; round < 3; ++round) {
    if (round < 2) top2(ws, T, NSPEC, tid, k0, k1, sk);
    const int i0 = key_row(k0), i1 = key_row(k1);
    TL(1);
    sel_rows(ws, x, T, tid, i0, i1, hs, xs, yb);
    TL(3);
    u32x4 xf[14];
    unsigned spins = 0;
    __builtin_amdgcn_s_sleep(ASL);
    for (;;) {
      bool ok = true;
#pragma unroll
      for (int ks = 0; ks < 14; ++ks) {
        xf[ks] = __builtin_bit_cast(u32x4, __builtin_amdgcn_raw_buffer_load_b128(rs, (int)OFF_ACT + round * 65536 + xcc * 8192 + (wv * 14 + ks) * 128 + (l15 & 1) * 64 + g * 16, 0, 16));
        ok = ok && xf[ks].x != SENT && xf[ks].y != SENT && xf[ks].z != SENT && xf[ks].w != SENT;
      }
      if (__all(ok)) break;
      __builtin_amdgcn_s_sleep(2);
      if (spin_bail(spins, ws, T)) break;
      asm volatile("" ::: "memory");
    }
    TL(7);
    f32x4 acc[2];
#pragma unroll
    for (int j = 0; j < 2; ++j) { acc[j] = (f32x4){0.f, 0.f, 0.f, 0.f}; asm volatile("" : "+v"(acc[j])); }
    asm volatile("s_nop 1" ::: "memory");
#pragma unroll
    for (int ks = 0; ks < 14; ++ks)
#pragma unroll
      for (int j = 0; j < 2; ++j) asm volatile("v_mfma_f32_16x16x32_bf16 %0, %1, %2, %0" : "+v"(acc[j]) : "v"(xf[ks]), "a"(a[ks * 2 + j]));
    asm volatile("s_nop 7\n\ts_nop 3" : "+v"(acc[0]), "+v"(acc[1]));
    {
      const float e0 = (g & 1) ? acc[1][0] : acc[0][0], e1 = (g & 1) ? acc[1][1] : acc[0][1];
      part[tid] = (g >> 1) ? e1 : e0;
    }
    __syncthreads();
    const int t = (tid >> 4) & 1, cc = (tid & 15) * 2, idx = t ? i1 : i0, col = m * 32 + cc;
    const float r = key_val(t ? k1 : k0);
    const float sg = rbf(1.0f / (1.0f + __expf(-r)));
    float o[2];
#pragma unroll
    for (int e = 0; e < 2; ++e) {
      const int q = t * 32 + cc + e;
      const float y = (part[q] + part[64 + q]) + (part[128 + q] + part[192 + q]);
      const float upd = rbf(rbf(y) * sg);
      o[e] = hs[t * DM + col + e] + rbf(xs[t * DM + col + e] + upd);
    }
    TL(4);
    gu32* op = (gu32*)(out + (size_t)idx * DM + col);
    if (tid < 32) __hip_atomic_store(op, pk2(o[0], o[1]), RLX_AGENT);
    if (round == 2 || spec_holds(ws, T, xcc, k0, k1, sk, tid)) break;
    if (tid < 32) __hip_atomic_store(op, pk2(hs[t * DM + col] + xs[t * DM + col], hs[t * DM + col + 1] + xs[t * DM + col + 1]), RLX_AGENT);
    asm volatile("s_waitcnt vmcnt(0)" ::: "memory");
    __syncthreads();
  }
}

__device__ __forceinline__ void red_phase(char* ws, int b, u64 T, unsigned psi, int lane) {
  unsigned xcc; asm volatile("s_getreg_b32 %0, hwreg(HW_REG_XCC_ID)" : "=s"(xcc));
  const int g = lane >> 4;
  const unsigned own = (psi ^ pgc(xcc & 3u) ^ 5u) * 4096u;
  const unsigned so = (psi ^ pgc(((lane >> 3) & 1) + (lane >> 5) * 2) ^ 1u) * 4096 + (b >> 5) * 32768 + (b & 31) * 128 + ((g & 1) + (lane & 1) * 2 + (lane & 4)) * 16 + (lane & 2) * 4;
  const unsigned io = (unsigned)OFF_G2 + (b >> 2) * 65536 + (psi ^ pgc(b & 3) ^ ((b >> 5) << 2)) * 4096 + lane * 16;
  const unsigned one = (lane & 4) ? 0x3f800000u : 0x3f80u;
  asm volatile(
    "s_mov_b64 s[36:37], %0\n"
    "s_mov_b64 s[38:39], %1\n"
    "v_mov_b32 v31, %2\n"
    "v_mov_b32 v32, %3\n"
    "v_mov_b32 v33, %4\n"
    "v_mov_b32 v34, %5\n"
    "v_mov_b32 v35, %6\n"
    "v_mov_b32 v36, %7\n"
    "v_mov_b32 v37, %8\n"
    "v_mov_b32 v39, %9\n"
    "v_add_u32 v38, 0x10000, v37\n"
    "v_mov_b32 v40, 0xffbfffbf\n"
    "v_mov_b32 v41, 0xffbfffbf\n"
    "v_mov_b32 v42, 0xffbfffbf\n"
    "v_mov_b32 v43, 0xffbfffbf\n"
    "s_or_b32 s38, s38, 1\n"
    "s_mov_b32 s41, 0\n"
    "s_mov_b32 s47, 0\n"
    "s_add_u32 s64, s36, 0x200200\n"
    "s_addc_u32 s65, s37, 0\n"
    "s_getpc_b64 s[60:61]\n"
    "R_pc%=:\n"
    "s_add_u32 s60, s60, R_go%=-R_pc%=\n"
    "s_addc_u32 s61, s61, 0\n"
    "R_step%=:\n"
    "s_and_b32 s70, s41, 3\n"
    "s_lshl_b32 s71, s70, 16\n"
    "s_add_u32 s71, s71, 0x180000\n"
    "s_add_u32 s48, s36, s71\n"
    "s_addc_u32 s49, s37, 0\n"
    "s_add_u32 s71, s70, 1\n"
    "s_and_b32 s71, s71, 3\n"
    "s_lshl_b32 s71, s71, 17\n"
    "s_add_u32 s71, s71, 0x100000\n"
    "s_add_u32 s52, s36, s71\n"
    "s_addc_u32 s53, s37, 0\n"
    "s_xor_b32 s71, s71, 0x40000\n"
    "s_add_u32 s54, s36, s71\n"
    "s_addc_u32 s55, s37, 0\n"
    "s_and_b32 s70, s41, 1\n"
    "s_lshl_b32 s70, s70, 15\n"
    "s_add_u32 s50, s36, s70\n"
    "s_addc_u32 s51, s37, 0\n"
    "s_mov_b32 s45, 0\n"
    "s_mov_b32 s46, 0\n"
    "s_cmp_eq_u32 s41, 0\n"
    "s_cbranch_scc1 R_look%=\n"
    "s_add_u32 s44, s44, 2920\n"
    "R_lookat%=:\n"
    "R_w1%=:\n"
    "s_memtime s[56:57]\n"
    "s_waitcnt lgkmcnt(0)\n"
    "s_sub_u32 s70, s44, s56\n"
    "s_sub_u32 s71, s70, 800\n"
    "s_cmp_lt_u32 s71, 0x2000\n"
    "s_cbranch_scc1 R_w1%=\n"
    "R_look%=:\n"
    "global_load_dwordx4 v[64:67], v39, s[48:49] sc1\n"
    "s_sleep 3\n"
    "s_memtime s[56:57]\n"
    "s_memrealtime s[70:71]\n"
    "s_nop 3\n"
    "s_memrealtime s[72:73]\n"
    "s_nop 3\n"
    "s_memrealtime s[74:75]\n"
    "s_nop 3\n"
    "s_memrealtime s[76:77]\n"
    "s_nop 3\n"
    "s_memrealtime s[78:79]\n"
    "s_nop 3\n"
    "s_memrealtime s[80:81]\n"
    "s_mov_b32 s59, 0\n"
    "s_waitcnt lgkmcnt(0)\n"
    "s_mul_i32 s58, s70, 24\n"
    "s_cmp_eq_u32 s70, s72\n"
    "s_addc_u32 s59, s59, 0\n"
    "s_cmp_eq_u32 s70, s74\n"
    "s_addc_u32 s59, s59, 0\n"
    "s_cmp_eq_u32 s70, s76\n"
    "s_addc_u32 s59, s59, 0\n"
    "s_cmp_eq_u32 s70, s78\n"
    "s_addc_u32 s59, s59, 0\n"
    "s_cmp_eq_u32 s70, s80\n"
    "s_addc_u32 s59, s59, 0\n"
    "s_mul_i32 s59, s59, 20\n"
    "s_sub_u32 s58, s58, s59\n"
    "s_sub_u32 s58, s58, 300\n"
    "s_waitcnt vmcnt(0)\n"
    "v_subrev_u32 v67, s58, v67\n"
    "v_cmp_ne_u64 s[74:75], s[38:39], v[64:65]\n"
    "v_cmp_ne_u32 vcc, s41, v66\n"
    "v_max_i32_dpp v67, v67, v67 row_ror:1 row_mask:0xf bank_mask:0xf\n"
    "s_nop 1\n"
    "v_max_i32_dpp v67, v67, v67 row_ror:2 row_mask:0xf bank_mask:0xf\n"
    "s_nop 1\n"
    "v_max_i32_dpp v67, v67, v67 row_ror:4 row_mask:0xf bank_mask:0xf\n"
    "s_nop 1\n"
    "v_max_i32_dpp v67, v67, v67 row_ror:8 row_mask:0xf bank_mask:0xf\n"
    "s_nop 0\n"
    "v_readlane_b32 s70, v67, 0\n"
    "v_readlane_b32 s71, v67, 16\n"
    "v_readlane_b32 s72, v67, 32\n"
    "v_readlane_b32 s73, v67, 48\n"
    "s_or_b64 vcc, vcc, s[74:75]\n"
    "s_cbranch_scc1 R_notyet%=\n"
    "s_max_i32 s70, s70, s71\n"
    "s_max_i32 s72, s72, s73\n"
    "s_max_i32 s70, s70, s72\n"
    "s_min_i32 s70, s70, 8000\n"
    "s_add_u32 s44, s56, s70\n"
    "s_cmp_gt_i32 s70, 1210\n"
    "s_cbranch_scc1 R_far%=\n"
    "R_timed%=:\n"
    "R_w2%=:\n"
    "s_memtime s[56:57]\n"
    "s_waitcnt lgkmcnt(0)\n"
    "s_sub_u32 s70, s44, s56\n"
    "s_sub_u32 s71, s70, 200\n"
    "s_cmp_lt_u32 s71, 0x2000\n"
    "s_cbranch_scc1 R_w2%=\n"
    "s_sub_i32 s70, s70, 76\n"
    "s_max_i32 s70, s70, 0\n"
    "s_min_i32 s70, s70, 124\n"
    "s_andn2_b32 s70, s70, 3\n"
    "s_sub_u32 s72, s60, s70\n"
    "s_subb_u32 s73, s61, 0\n"
    "s_setpc_b64 s[72:73]\n"
    ".p2align 6\n"
    "s_nop 0\n"
    "s_nop 0\n"
    "s_nop 0\n"
    "s_nop 0\n"
    "s_nop 0\n"
    "s_nop 0\n"
    "s_nop 0\n"
    "s_nop 0\n"
    "s_nop 0\n"
    "s_nop 0\n"
    "s_nop 0\n"
    "s_nop 0\n"
    "s_nop 0\n"
    "s_nop 0\n"
    "s_nop 0\n"
    "s_nop 0\n"
    "s_nop 0\n"
    "s_nop 0\n"
    "s_nop 0\n"
    "s_nop 0\n"
    "s_nop 0\n"
    "s_nop 0\n"
    "s_nop 0\n"
    "s_nop 0\n"
    "s_nop 0\n"
    "s_nop 0\n"
    "s_nop 0\n"
    "s_nop 0\n"
    "s_nop 0\n"
    "s_nop 0\n"
    "s_nop 0\n"
    "s_nop 0\n"
    "R_go%=:\n"
    "global_load_dwordx4 v[48:51], v36, s[50:51] offset:0 sc1\n"
    "global_load_dwordx4 v[52:55], v36, s[50:51] offset:1024 sc1\n"
    "global_load_dwordx4 v[56:59], v36, s[50:51] offset:2048 sc1\n"
    "global_load_dwordx4 v[60:63], v36, s[50:51] offset:3072 sc1\n"
    "s_waitcnt vmcnt(3)\n"
    "v_mfma_f32_16x16x32_bf16 v[44:47], v[32:35], v[48:51], 0\n"
    "s_waitcnt vmcnt(2)\n"
    "v_mfma_f32_16x16x32_bf16 v[44:47], v[32:35], v[52:55], v[44:47]\n"
    "s_waitcnt vmcnt(1)\n"
    "v_mfma_f32_16x16x32_bf16 v[44:47], v[32:35], v[56:59], v[44:47]\n"
    "s_waitcnt vmcnt(0)\n"
    "v_mfma_f32_16x16x32_bf16 v[44:47], v[32:35], v[60:63], v[44:47]\n"
    "s_nop 7\n"
    "v_add_f32_dpp v44, v44, v44 row_ror:8 row_mask:0xf bank_mask:0xf\n"
    "v_add_f32_dpp v45, v45, v45 row_ror:8 row_mask:0xf bank_mask:0xf\n"
    "v_add_f32_dpp v46, v46, v46 row_ror:8 row_mask:0xf bank_mask:0xf\n"
    "v_add_f32_dpp v47, v47, v47 row_ror:8 row_mask:0xf bank_mask:0xf\n"
    "v_cmpx_o_f32 vcc, v44, v45\n"
    "R_pub%=:\n"
    "v_cvt_pk_bf16_f32 v64, v44, v45\n"
    "v_cvt_pk_bf16_f32 v65, v46, v47\n"
    "global_store_dwordx2 v37, v[64:65], s[52:53] sc1\n"
    "global_store_dwordx2 v38, v[64:65], s[52:53] sc1\n"
    "s_memtime s[58:59]\n"
    "s_cmp_eq_u64 exec, -1\n"
    "s_mov_b64 exec, -1\n"
    "s_cbranch_scc0 R_miss%=\n"
    "s_waitcnt lgkmcnt(0)\n"
    "s_cmp_eq_u32 s45, 0\n"
    "s_cbranch_scc1 R_nomiss%=\n"
    "s_add_u32 s48, s48, 0x10000\n"
    "s_andn2_b32 s48, s48, 0x40000\n"
    "s_add_u32 s70, s41, 1\n"
    "v_mov_b32 v66, s70\n"
    "s_mov_b64 exec, 15\n"
    "global_store_dword v31, v66, s[48:49] offset:16 sc1\n"
    "s_mov_b64 exec, -1\n"
    "R_nomiss%=:\n"
    "s_add_u32 s58, s58, 600\n"
    "R_w3%=:\n"
    "s_memtime s[56:57]\n"
    "s_waitcnt lgkmcnt(0)\n"
    "s_sub_u32 s70, s58, s56\n"
    "s_sub_u32 s71, s70, 0\n"
    "s_cmp_lt_u32 s71, 0x2000\n"
    "s_cbranch_scc1 R_w3%=\n"
    "global_store_dwordx2 v37, v[40:41], s[54:55] sc1\n"
    "global_store_dwordx2 v38, v[40:41], s[54:55] sc1\n"
    "global_store_dwordx4 v36, v[40:43], s[50:51] offset:0 sc1\n"
    "global_store_dwordx4 v36, v[40:43], s[50:51] offset:1024 sc1\n"
    "global_store_dwordx4 v36, v[40:43], s[50:51] offset:2048 sc1\n"
    "global_store_dwordx4 v36, v[40:43], s[50:51] offset:3072 sc1\n"
    "s_add_u32 s41, s41, 1\n"
    "s_cmp_lt_u32 s41, 63\n"
    "s_cbranch_scc1 R_step%=\n"
    "s_endpgm\n"
    "R_far%=:\n"
    "s_add_u32 s46, s46, 1\n"
    "s_cmp_lt_u32 s46, 16\n"
    "s_cbranch_scc1 R_lookat%=\n"
    "s_branch R_timed%=\n"
    "R_notyet%=:\n"
    "s_add_u32 s47, s47, 1\n"
    "s_and_b32 s70, s47, 0xff\n"
    "s_cbranch_scc1 R_look%=\n"
    "v_mov_b32 v64, 0\n"
    "global_load_dwordx2 v[66:67], v64, s[64:65] sc1\n"
    "s_sub_u32 s70, s38, 1\n"
    "s_mov_b32 s71, s39\n"
    "s_waitcnt vmcnt(0)\n"
    "v_cmp_eq_u64 vcc, s[70:71], v[66:67]\n"
    "s_nop 1\n"
    "s_cbranch_vccnz R_go%=\n"
    "s_cmp_lt_u32 s47, 0x40000\n"
    "s_cbranch_scc1 R_look%=\n"
    "v_mov_b32 v66, s70\n"
    "v_mov_b32 v67, s71\n"
    "global_store_dwordx2 v64, v[66:67], s[64:65] sc1\n"
    "s_branch R_go%=\n"
    "R_miss%=:\n"
    "s_add_u32 s45, s45, 1\n"
    "s_cmp_eq_u32 s45, 1\n"
    "s_cbranch_scc1 R_go%=\n"
    "s_mov_b64 s[72:73], 0\n"
    "v_cmp_eq_u32 vcc, v40, v48\n"
    "s_nop 0\n"
    "s_or_b64 s[72:73], s[72:73], vcc\n"
    "v_cmp_eq_u32 vcc, v40, v49\n"
    "s_nop 0\n"
    "s_or_b64 s[72:73], s[72:73], vcc\n"
    "v_cmp_eq_u32 vcc, v40, v50\n"
    "s_nop 0\n"
    "s_or_b64 s[72:73], s[72:73], vcc\n"
    "v_cmp_eq_u32 vcc, v40, v51\n"
    "s_nop 0\n"
    "s_or_b64 s[72:73], s[72:73], vcc\n"
    "v_cmp_eq_u32 vcc, v40, v52\n"
    "s_nop 0\n"
    "s_or_b64 s[72:73], s[72:73], vcc\n"
    "v_cmp_eq_u32 vcc, v40, v53\n"
    "s_nop 0\n"
    "s_or_b64 s[72:73], s[72:73], vcc\n"
    "v_cmp_eq_u32 vcc, v40, v54\n"
    "s_nop 0\n"
    "s_or_b64 s[72:73], s[72:73], vcc\n"
    "v_cmp_eq_u32 vcc, v40, v55\n"
    "s_nop 0\n"
    "s_or_b64 s[72:73], s[72:73], vcc\n"
    "v_cmp_eq_u32 vcc, v40, v56\n"
    "s_nop 0\n"
    "s_or_b64 s[72:73], s[72:73], vcc\n"
    "v_cmp_eq_u32 vcc, v40, v57\n"
    "s_nop 0\n"
    "s_or_b64 s[72:73], s[72:73], vcc\n"
    "v_cmp_eq_u32 vcc, v40, v58\n"
    "s_nop 0\n"
    "s_or_b64 s[72:73], s[72:73], vcc\n"
    "v_cmp_eq_u32 vcc, v40, v59\n"
    "s_nop 0\n"
    "s_or_b64 s[72:73], s[72:73], vcc\n"
    "v_cmp_eq_u32 vcc, v40, v60\n"
    "s_nop 0\n"
    "s_or_b64 s[72:73], s[72:73], vcc\n"
    "v_cmp_eq_u32 vcc, v40, v61\n"
    "s_nop 0\n"
    "s_or_b64 s[72:73], s[72:73], vcc\n"
    "v_cmp_eq_u32 vcc, v40, v62\n"
    "s_nop 0\n"
    "s_or_b64 s[72:73], s[72:73], vcc\n"
    "v_cmp_eq_u32 vcc, v40, v63\n"
    "s_nop 0\n"
    "s_or_b64 s[72:73], s[72:73], vcc\n"
    "s_cbranch_scc0 R_pub%=\n"
    "s_add_u32 s47, s47, 1\n"
    "s_and_b32 s70, s47, 0xff\n"
    "s_cbranch_scc1 R_look%=\n"
    "v_mov_b32 v64, 0\n"
    "global_load_dwordx2 v[66:67], v64, s[64:65] sc1\n"
    "s_sub_u32 s70, s38, 1\n"
    "s_mov_b32 s71, s39\n"
    "s_waitcnt vmcnt(0)\n"
    "v_cmp_eq_u64 vcc, s[70:71], v[66:67]\n"
    "s_nop 1\n"
    "s_cbranch_vccnz R_pub%=\n"
    "s_cmp_lt_u32 s47, 0x40000\n"
    "s_cbranch_scc1 R_look%=\n"
    "v_mov_b32 v66, s70\n"
    "v_mov_b32 v67, s71\n"
    "global_store_dwordx2 v64, v[66:67], s[64:65] sc1\n"
    "s_branch R_pub%=\n"
    "mk_code_end:\n"
    :: "s"(ws), "s"(uni(T)), "v"((psi ^ pgc(lane & 3) ^ 5u) * 4096 + b * 64), "v"((lane & 3) == 0 ? one : 0u), "v"((lane & 3) == 1 ? one : 0u), "v"((lane & 3) == 2 ? one : 0u), "v"((lane & 3) == 3 ? one : 0u),
       "v"(io), "v"(so), "v"(own + lane * 64), "s"(ws + 204 * MBY + b * 2048)
    : "memory", "vcc", "scc", "s36", "s37", "s38", "s39", "s40", "s62", "s63", "s99", "v31", "v32", "v33", "v34", "v35", "v36", "v37", "v38", "v39", "v67");
  __builtin_unreachable();
}

__global__ void __launch_bounds__(256, 1) rec_kernel(char* ws, const u16* __restrict__ x, const u16* __restrict__ mem0, const u16* __restrict__ Wq,
                                                     const u16* __restrict__ Wk, const u16* __restrict__ Wv, const u16* __restrict__ Wo,
                                                     const u16* __restrict__ w_r, const u16* __restrict__ wg, const u16* __restrict__ wu,
                                                     const u16* __restrict__ wd, u16* out) {
  __shared__ __attribute__((aligned(1024))) char lds[158720];
  const int tid = threadIdx.x, lane = tid & 63, wv = tid >> 6;
#define PADOP
  TL(0);
  u64 ep0;
  int role;
  {
    unsigned xcc; asm volatile("s_getreg_b32 %0, hwreg(HW_REG_XCC_ID)" : "=s"(xcc));
    role = (int)((blockIdx.x & ~7u) | ((xcc + XROT) & 7u));
  }
  if ((role >> 5) == ((role & 4) ? 0 : 6)) {
    unsigned tmp;
    asm volatile("s_getpc_b64 s[40:41]\n"
                 "mk_pc:\n\t"
                 "s_mov_b32 m0, 0x24000\n\t"
                 "v_min_u32 %0, mk_code_end-mk_pc-4, %1\n\t"
                 "global_load_lds_dword %0, s[40:41]" : "=&v"(tmp) : "v"((((blockIdx.x >> 3) & 3) * 256 + tid) * 128) : "s40", "s41", "memory");
  }
  const int wave = __builtin_amdgcn_readfirstlane(wv);
  unsigned near = 0;
#pragma unroll 1
  for (int pass = 0;; ++pass) {
    role = __builtin_amdgcn_readfirstlane(role);
    const int bx = role & 7, by = role >> 3;
    const bool red = bx < 4 && by >= 16, att = bx >= 4 && by < 8;
    const char* src = red ? (const char*)(x + (size_t)((by - 16) * 4 + bx) * 32 * DM) : att ? (const char*)(Wo + (size_t)((bx & 3) * 8 + by) * DH * DM)
                    : (const char*)((bx < 4 ? (by < 8 ? Wk : Wq) : (by < 16 ? Wk : (by < 24 ? Wv : Wq))) + ((bx & 3) * 8 + (by & 7)) * DH);
    const unsigned sstep = red || att ? 16384u : 64u * HD * 2u, lstep = red ? 1040u : 1024u;
    unsigned vo[4];
#pragma unroll
    for (int i = 0; i < 4; ++i) {
      const int r = wave * 16 + i * 4 + (lane >> 4), f = (r & 3) | ((bx < 4 ? r : r >> 1) & 4);
      vo[i] = red || att ? (wave * 4 + i) * 1024 + lane * 16 : r * (HD * 2) + (((lane & 15) ^ (f << 1)) * 16);
    }
    if (att) __builtin_amdgcn_s_sleep(ADL);
    if (bx >= 4 && by >= 24) __builtin_amdgcn_s_sleep(QDL);
    if (red && (by - 16) * 4 + bx >= 53 && wave == 3) near = probe(ws, (by - 16) * 4 + bx - 53, bx, lane);
    unsigned olo, ohi;
#define FD4 "global_load_lds_dwordx4 %3, s[42:43]\n\ts_add_u32 m0, m0, %10\n\ts_nop 0\n\tglobal_load_lds_dwordx4 %4, s[42:43]\n\ts_add_u32 m0, m0, %10\n\ts_nop 0\n\t" \
            "global_load_lds_dwordx4 %5, s[42:43]\n\ts_add_u32 m0, m0, %10\n\ts_nop 0\n\tglobal_load_lds_dwordx4 %6, s[42:43]\n\t" \
            "s_add_u32 m0, m0, s44\n\ts_add_u32 s42, s42, %9\n\ts_addc_u32 s43, s43, 0\n\t"
    asm volatile("s_load_dwordx2 %0, %11, 0x0\n\ts_mov_b64 s[42:43], %7\n\ts_mov_b32 m0, %8\n\ts_mul_i32 s44, %10, 13\n\ts_mov_b32 s45, %13\n\t"
                 FD4 FD4
                 "s_waitcnt lgkmcnt(0)\n\ts_mov_b64 s[46:47], %0\n\ts_add_u32 s46, s46, 1\n\ts_addc_u32 s47, s47, 0\n\ts_lshl_b64 s[46:47], s[46:47], 6\n\t"
                 "v_mov_b32 v40, s46\n\tv_mov_b32 v41, s47\n\tv_mov_b32 v42, 0\n\t"
                 "s_cmp_eq_u32 %14, 0\n\ts_cbranch_scc0 2f\n\t"
                 "s_mov_b64 exec, 1\n\tglobal_atomic_swap_x2 v[40:41], v42, v[40:41], %12 sc0\n\ts_mov_b64 exec, -1\n"
                 "2:\n\ts_cmp_eq_u32 s45, 0\n\ts_cbranch_scc0 1f\n\ts_waitcnt vmcnt(0)\n\ts_branch 3f\n"
                 "1:\n\t" FD4 "s_sub_u32 s45, s45, 1\n\ts_cmp_lg_u32 s45, 0\n\ts_cbranch_scc1 1b\n\ts_waitcnt vmcnt(24)\n"
                 "3:\n\tv_mov_b32 %1, v40\n\tv_mov_b32 %2, v41"
                 : "=&s"(ep0), "=v"(olo), "=v"(ohi)
                 : "v"(vo[0]), "v"(vo[1]), "v"(vo[2]), "v"(vo[3]), "s"(uni((u64)src)), "s"(wave * 4 * lstep), "s"(sstep), "s"(lstep), "s"(ws + OFF_EP), "s"(ws + OFF_CLM + role * 128),
                   "s"(red ? 0 : (att ? ATN : 6)), "s"(wave + pass)
                 : "memory", "scc", "s42", "s43", "s44", "s45", "s46", "s47", "v40", "v41", "v42");
    ep0 = uni(ep0);
    u64 old = ((u64)ohi << 32) | olo;
    int got = role;
    if (tid == 0 && pass == 0) {
      const u64 T0 = tag_of(ep0);
      while (old == T0) { got = (got + 1) & 255; old = __hip_atomic_exchange((gu64*)(ws + OFF_CLM + got * 128), T0, RLX_AGENT); }
      *(int*)(lds + 158704) = got;
    }
    asm volatile("s_waitcnt lgkmcnt(0)\n\ts_barrier" ::: "memory");
    got = __builtin_amdgcn_readfirstlane(*(int*)(lds + 158704));
    if (got == role) break;
    asm volatile("s_waitcnt vmcnt(0)\n\ts_barrier" ::: "memory");
    role = got;
  }
  const int bx = role & 7, by = role >> 3;
  if (bx >= 4 || by >= 16) {
    const int xc = bx & 3;
    if (bx < 4) {
      const int i = (by - 16) * 4 + bx;
      if (i >= 53 && tid == 192) set_flag(ws, OFF_PAR + (i - 53) * 128, tag_of(ep0), near);
      const u64 T = tag_of(ep0);
      if (i < 53) ln1_phase(ws, i, T, lds, tid);
      const int p = lane >> 4, q = lane & 3;
      unsigned lo = SENT, hi = SENT;
      if (wv == 0) {
        u16 el[4];
#pragma unroll
        for (int k = 0; k < 4; ++k) {
          u16 t = mem0[((i & 1) * 4 + p) * DM + (i >> 1) * 16 + k * 4 + q];
          if ((t & 0x7f80) == 0x7f80 && (t & 0x7f)) t |= 0x40;
          el[k] = t;
        }
        lo = el[0] | ((unsigned)el[1] << 16); hi = el[2] | ((unsigned)el[3] << 16);
      }
      const unsigned par = wait_par(ws, T, lane);
      gu64* gp = (gu64*)(ws + OFF_G3 + wv * 131072 + (par ^ pgc((lane >> 2) & 3) ^ 1u) * 4096 + (i >> 5) * 32768 + (i & 31) * 128 + ((p & 1) + (q & 1) * 2 + (p & 2) * 2) * 16 + (q & 2) * 4);
      __hip_atomic_store(gp, ((u64)hi << 32) | lo, RLX_AGENT);
      __hip_atomic_store(gp + 8192, ((u64)hi << 32) | lo, RLX_AGENT);
#pragma unroll
      for (int k = 0; k < 2; ++k)
        store16_sc1(ws + OFF_G2, (unsigned)(k * 32768 + (i >> 2) * 65536 + (par ^ pgc(i & 3) ^ ((i >> 5) << 2)) * 4096 + tid * 16), (u32x4){SENT, SENT, SENT, SENT});
      asm volatile("s_waitcnt vmcnt(0)" ::: "memory");
      __syncthreads();
      if (tid == 0) set_flag(ws, OFF_TAB + i * 128, T, 1u + par);
      if (i >= 53) ln1_phase(ws, i, T, lds, tid);
      if (wv == 0) red_phase(ws, i, T, par, lane);
    } else if (by < 8) {
      xa_phase(ws, by & 3, (xc * 2 + (by >> 2)) * 4 + wv, tag_of(ep0), lane);
    } else {
      const int jj = by - 8, h = xc * 8 + (jj & 7), m = (jj & 7) * 4 + xc;
      u64 T;
      if (jj < 8) T = qkv_phase<1>(ws, h, ep0, tid);
      else if (jj < 16) T = qkv_phase<2>(ws, h, ep0, tid);
      else T = qkv_phase<0>(ws, h, ep0, tid);
      TL(2);
      op_phase(ws, x, w_r, out, Wo, jj >> 2, xc, jj & 3, T, lds, tid);
      if (jj >= 16) __syncthreads();
      if (jj < 16) {}
      else if (m < 14) mlp1_phase(ws, x, wg, wu, m, T, lds, tid);
      else if (m >= 16) {
        mlp2_phase(ws, x, wd, out, m - 16, T, lds, tid);
        if (m == 31 && tid == 0) __hip_atomic_store((gu64*)(ws + OFF_EP), T >> 6, RLX_AGENT);
      }
    }
    TL(5);
    return;
  }
  const unsigned ldsb = (unsigned)(size_t)(LAS char*)lds;
  asm volatile(
    "s_mov_b64 s[36:37], %0\n"
    "s_mov_b32 s38, %1\n"
    "s_mov_b32 s39, %2\n"
    "s_mov_b32 s40, %3\n"
    "s_load_dwordx2 s[96:97], %4, 0x0\n"
    "s_mov_b64 s[22:23], %5\n"
    "s_mov_b64 s[24:25], %6\n"
    "s_mov_b64 s[26:27], %7\n"
    "s_mov_b64 s[28:29], %8\n"
    "s_mov_b64 exec, -1\n"
    "s_mov_b32 s90, 0\n"
    "s_getreg_b32 s10, hwreg(HW_REG_XCC_ID)\n"
    "s_and_b32 s41, s38, 3\n"
    "s_lshl_b32 s41, s41, 3\n"
    "s_bfe_u32 s42, s38, 0x30002\n"
    "s_add_u32 s41, s41, s42\n"
    "s_lshr_b32 s42, s38, 5\n"
    "v_mbcnt_lo_u32_b32 v0, -1, 0\n"
    "v_mbcnt_hi_u32_b32 v0, -1, v0\n"
    "v_and_b32 v1, 15, v0\n"
    "v_lshrrev_b32 v2, 4, v0\n"
    "v_lshlrev_b32 v71, 4, v0\n"
    "s_and_b32 s11, s10, 7\n"
    "s_add_u32 s68, s36, 0x200200\n"
    "s_addc_u32 s69, s37, 0\n"
    "s_lshl_b32 s78, s39, 4\n"
    "v_add_u32 v8, s78, v2\n"
    "v_lshlrev_b32 v12, 1, v2\n"
    "v_xor_b32 v12, v12, v1\n"
    "v_and_b32 v9, 1, v2\n"
    "v_lshlrev_b32 v9, 1, v9\n"
    "v_xor_b32 v9, v9, v1\n"
    "s_and_b32 s79, s39, 1\n"
    "s_lshl_b32 s79, s79, 3\n"
    "v_add_u32 v10, 0, v8\n"
    "v_xor_b32 v11, 0, v12\n"
    "v_lshlrev_b32 v11, 4, v11\n"
    "v_lshl_add_u32 v84, v10, 13, v11\n"
    "v_xor_b32 v11, 0, v9\n"
    "v_xor_b32 v11, s79, v11\n"
    "v_lshlrev_b32 v11, 4, v11\n"
    "v_lshl_add_u32 v88, v10, 10, v11\n"
    "v_add_u32 v10, 4, v8\n"
    "v_xor_b32 v11, 8, v12\n"
    "v_lshlrev_b32 v11, 4, v11\n"
    "v_lshl_add_u32 v85, v10, 13, v11\n"
    "v_xor_b32 v11, 4, v9\n"
    "v_xor_b32 v11, s79, v11\n"
    "v_lshlrev_b32 v11, 4, v11\n"
    "v_lshl_add_u32 v89, v10, 10, v11\n"
    "v_add_u32 v10, 8, v8\n"
    "v_xor_b32 v11, 0, v12\n"
    "v_lshlrev_b32 v11, 4, v11\n"
    "v_lshl_add_u32 v86, v10, 13, v11\n"
    "v_xor_b32 v11, 0, v9\n"
    "v_xor_b32 v11, s79, v11\n"
    "v_lshlrev_b32 v11, 4, v11\n"
    "v_lshl_add_u32 v90, v10, 10, v11\n"
    "v_add_u32 v10, 12, v8\n"
    "v_xor_b32 v11, 8, v12\n"
    "v_lshlrev_b32 v11, 4, v11\n"
    "v_lshl_add_u32 v87, v10, 13, v11\n"
    "v_xor_b32 v11, 4, v9\n"
    "v_xor_b32 v11, s79, v11\n"
    "v_lshlrev_b32 v11, 4, v11\n"
    "v_lshl_add_u32 v91, v10, 10, v11\n"
    "s_lshl_b32 s78, s41, 8\n"
    "s_add_u32 s22, s22, s78\n"
    "s_addc_u32 s23, s23, 0\n"
    "s_add_u32 s24, s24, s78\n"
    "s_addc_u32 s25, s25, 0\n"
    "s_add_u32 s26, s26, s78\n"
    "s_addc_u32 s27, s27, 0\n"
    "s_lshl_b32 s78, s41, 17\n"
    "s_lshl_b32 s79, s42, 9\n"
    "s_add_u32 s78, s78, s79\n"
    "s_add_u32 s28, s28, s78\n"
    "s_addc_u32 s29, s29, 0\n"
    "s_lshl_b32 s30, s39, 12\n"
    "s_add_u32 s30, s30, s40\n"
    "v_lshrrev_b32 v8, 2, v1\n"
    "v_and_b32 v9, 1, v8\n"
    "v_lshrrev_b32 v10, 1, v8\n"
    "v_and_b32 v11, 1, v2\n"
    "v_and_b32 v12, 3, v1\n"
    "v_lshl_add_u32 v14, v11, 1, v10\n"
    "v_lshl_add_u32 v14, v9, 2, v14\n"
    "v_lshrrev_b32 v13, 1, v2\n"
    "v_lshl_add_u32 v13, v13, 4, v14\n"
    "v_lshlrev_b32 v13, 8, v13\n"
    "v_add_u32 v13, s40, v13\n"
    "v_lshlrev_b32 v14, 5, v14\n"
    "s_lshl_b32 s78, s39, 6\n"
    "v_lshl_add_u32 v15, v12, 3, s78\n"
    "v_xor_b32 v15, v15, v14\n"
    "v_add_u32 v92, v15, v13\n"
    "v_xor_b32 v15, 32, v15\n"
    "v_add_u32 v93, v15, v13\n"
    "s_lshr_b32 s78, s39, 1\n"
    "s_lshl_b32 s78, s78, 7\n"
    "s_and_b32 s79, s39, 1\n"
    "s_lshl_b32 s79, s79, 4\n"
    "s_add_u32 s78, s78, s79\n"
    "v_and_b32 v15, 1, v12\n"
    "v_lshl_add_u32 v15, v15, 5, s78\n"
    "v_lshrrev_b32 v16, 1, v12\n"
    "v_lshl_add_u32 v15, v16, 3, v15\n"
    "v_xor_b32 v15, v15, v14\n"
    "v_add_u32 v96, v15, v13\n"
    "v_xor_b32 v15, 64, v15\n"
    "v_add_u32 v97, v15, v13\n"
    "v_add_u32 v94, 0x10000, v92\n"
    "v_add_u32 v95, 0x10000, v93\n"
    "v_add_u32 v98, 0x10000, v96\n"
    "v_add_u32 v99, 0x10000, v97\n"
    "v_lshl_add_u32 v13, v2, 2, v9\n"
    "v_lshl_add_u32 v13, v10, 4, v13\n"
    "v_lshl_add_u32 v14, v11, 1, v9\n"
    "v_lshl_add_u32 v14, v10, 2, v14\n"
    "v_xor_b32 v14, v14, v12\n"
    "v_lshlrev_b32 v14, 1, v14\n"
    "s_and_b32 s78, s39, 1\n"
    "s_lshl_b32 s78, s78, 3\n"
    "v_xor_b32 v14, s78, v14\n"
    "v_lshlrev_b32 v14, 4, v14\n"
    "v_lshl_add_u32 v14, v13, 8, v14\n"
    "s_lshr_b32 s78, s39, 1\n"
    "s_lshl_b32 s78, s78, 14\n"
    "s_add_u32 s78, s78, 0x10000\n"
    "v_add_u32 v104, s78, v14\n"
    "s_mul_i32 s78, s39, 34816\n"
    "s_add_u32 s78, s78, 18688\n"
    "v_add_u32 v116, s78, v71\n"
    "s_cmp_lg_u32 s42, 0\n"
    "s_cbranch_scc1 L_rb0%=\n"
    "ds_read_b64_tr_b16 a[0:1], v96 offset:0\n"
    "ds_read_b64_tr_b16 a[2:3], v96 offset:2048\n"
    "ds_read_b64_tr_b16 a[4:5], v96 offset:8192\n"
    "ds_read_b64_tr_b16 a[6:7], v96 offset:10240\n"
    "ds_read_b64_tr_b16 a[64:65], v97 offset:0\n"
    "ds_read_b64_tr_b16 a[66:67], v97 offset:2048\n"
    "ds_read_b64_tr_b16 a[68:69], v97 offset:8192\n"
    "ds_read_b64_tr_b16 a[70:71], v97 offset:10240\n"
    "s_waitcnt vmcnt(24) lgkmcnt(0)\n"
    "s_barrier\n"
    "s_add_u32 s32, s26, 0x0\n"
    "s_addc_u32 s33, s27, 0\n"
    "s_add_u32 m0, s30, 0\n"
    "s_nop 0\n"
    "global_load_lds_dwordx4 v84, s[32:33]\n"
    "s_add_u32 m0, s30, 1024\n"
    "s_nop 0\n"
    "global_load_lds_dwordx4 v85, s[32:33]\n"
    "s_add_u32 m0, s30, 2048\n"
    "s_nop 0\n"
    "global_load_lds_dwordx4 v86, s[32:33]\n"
    "s_add_u32 m0, s30, 3072\n"
    "s_nop 0\n"
    "global_load_lds_dwordx4 v87, s[32:33]\n"
    "ds_read_b64_tr_b16 a[8:9], v96 offset:16384\n"
    "ds_read_b64_tr_b16 a[10:11], v96 offset:18432\n"
    "ds_read_b64_tr_b16 a[12:13], v96 offset:24576\n"
    "ds_read_b64_tr_b16 a[14:15], v96 offset:26624\n"
    "ds_read_b64_tr_b16 a[72:73], v97 offset:16384\n"
    "ds_read_b64_tr_b16 a[74:75], v97 offset:18432\n"
    "ds_read_b64_tr_b16 a[76:77], v97 offset:24576\n"
    "ds_read_b64_tr_b16 a[78:79], v97 offset:26624\n"
    "s_waitcnt vmcnt(24) lgkmcnt(0)\n"
    "s_barrier\n"
    "s_add_u32 s32, s26, 0x80000\n"
    "s_addc_u32 s33, s27, 0\n"
    "s_add_u32 m0, s30, 16384\n"
    "s_nop 0\n"
    "global_load_lds_dwordx4 v84, s[32:33]\n"
    "s_add_u32 m0, s30, 17408\n"
    "s_nop 0\n"
    "global_load_lds_dwordx4 v85, s[32:33]\n"
    "s_add_u32 m0, s30, 18432\n"
    "s_nop 0\n"
    "global_load_lds_dwordx4 v86, s[32:33]\n"
    "s_add_u32 m0, s30, 19456\n"
    "s_nop 0\n"
    "global_load_lds_dwordx4 v87, s[32:33]\n"
    "ds_read_b64_tr_b16 a[16:17], v96 offset:32768\n"
    "ds_read_b64_tr_b16 a[18:19], v96 offset:34816\n"
    "ds_read_b64_tr_b16 a[20:21], v96 offset:40960\n"
    "ds_read_b64_tr_b16 a[22:23], v96 offset:43008\n"
    "ds_read_b64_tr_b16 a[80:81], v97 offset:32768\n"
    "ds_read_b64_tr_b16 a[82:83], v97 offset:34816\n"
    "ds_read_b64_tr_b16 a[84:85], v97 offset:40960\n"
    "ds_read_b64_tr_b16 a[86:87], v97 offset:43008\n"
    "s_waitcnt vmcnt(24) lgkmcnt(0)\n"
    "s_barrier\n"
    "s_add_u32 s32, s26, 0x100000\n"
    "s_addc_u32 s33, s27, 0\n"
    "s_add_u32 m0, s30, 32768\n"
    "s_nop 0\n"
    "global_load_lds_dwordx4 v84, s[32:33]\n"
    "s_add_u32 m0, s30, 33792\n"
    "s_nop 0\n"
    "global_load_lds_dwordx4 v85, s[32:33]\n"
    "s_add_u32 m0, s30, 34816\n"
    "s_nop 0\n"
    "global_load_lds_dwordx4 v86, s[32:33]\n"
    "s_add_u32 m0, s30, 35840\n"
    "s_nop 0\n"
    "global_load_lds_dwordx4 v87, s[32:33]\n"
    "ds_read_b64_tr_b16 a[24:25], v96 offset:49152\n"
    "ds_read_b64_tr_b16 a[26:27], v96 offset:51200\n"
    "ds_read_b64_tr_b16 a[28:29], v96 offset:57344\n"
    "ds_read_b64_tr_b16 a[30:31], v96 offset:59392\n"
    "ds_read_b64_tr_b16 a[88:89], v97 offset:49152\n"
    "ds_read_b64_tr_b16 a[90:91], v97 offset:51200\n"
    "ds_read_b64_tr_b16 a[92:93], v97 offset:57344\n"
    "ds_read_b64_tr_b16 a[94:95], v97 offset:59392\n"
    "s_waitcnt vmcnt(24) lgkmcnt(0)\n"
    "s_barrier\n"
    "s_add_u32 s32, s26, 0x180000\n"
    "s_addc_u32 s33, s27, 0\n"
    "s_add_u32 m0, s30, 49152\n"
    "s_nop 0\n"
    "global_load_lds_dwordx4 v84, s[32:33]\n"
    "s_add_u32 m0, s30, 50176\n"
    "s_nop 0\n"
    "global_load_lds_dwordx4 v85, s[32:33]\n"
    "s_add_u32 m0, s30, 51200\n"
    "s_nop 0\n"
    "global_load_lds_dwordx4 v86, s[32:33]\n"
    "s_add_u32 m0, s30, 52224\n"
    "s_nop 0\n"
    "global_load_lds_dwordx4 v87, s[32:33]\n"
    "ds_read_b64_tr_b16 a[32:33], v98 offset:0\n"
    "ds_read_b64_tr_b16 a[34:35], v98 offset:2048\n"
    "ds_read_b64_tr_b16 a[36:37], v98 offset:8192\n"
    "ds_read_b64_tr_b16 a[38:39], v98 offset:10240\n"
    "ds_read_b64_tr_b16 a[96:97], v99 offset:0\n"
    "ds_read_b64_tr_b16 a[98:99], v99 offset:2048\n"
    "ds_read_b64_tr_b16 a[100:101], v99 offset:8192\n"
    "ds_read_b64_tr_b16 a[102:103], v99 offset:10240\n"
    "s_waitcnt vmcnt(24) lgkmcnt(0)\n"
    "s_barrier\n"
    "s_add_u32 s32, s28, 0x0\n"
    "s_addc_u32 s33, s29, 0\n"
    "s_add_u32 m0, s30, 65536\n"
    "s_nop 0\n"
    "global_load_lds_dwordx4 v88, s[32:33]\n"
    "s_add_u32 m0, s30, 66560\n"
    "s_nop 0\n"
    "global_load_lds_dwordx4 v89, s[32:33]\n"
    "s_add_u32 m0, s30, 67584\n"
    "s_nop 0\n"
    "global_load_lds_dwordx4 v90, s[32:33]\n"
    "s_add_u32 m0, s30, 68608\n"
    "s_nop 0\n"
    "global_load_lds_dwordx4 v91, s[32:33]\n"
    "ds_read_b64_tr_b16 a[40:41], v98 offset:16384\n"
    "ds_read_b64_tr_b16 a[42:43], v98 offset:18432\n"
    "ds_read_b64_tr_b16 a[44:45], v98 offset:24576\n"
    "ds_read_b64_tr_b16 a[46:47], v98 offset:26624\n"
    "ds_read_b64_tr_b16 a[104:105], v99 offset:16384\n"
    "ds_read_b64_tr_b16 a[106:107], v99 offset:18432\n"
    "ds_read_b64_tr_b16 a[108:109], v99 offset:24576\n"
    "ds_read_b64_tr_b16 a[110:111], v99 offset:26624\n"
    "s_waitcnt vmcnt(24) lgkmcnt(0)\n"
    "s_barrier\n"
    "s_add_u32 s32, s28, 0x100\n"
    "s_addc_u32 s33, s29, 0\n"
    "s_add_u32 m0, s30, 81920\n"
    "s_nop 0\n"
    "global_load_lds_dwordx4 v88, s[32:33]\n"
    "s_add_u32 m0, s30, 82944\n"
    "s_nop 0\n"
    "global_load_lds_dwordx4 v89, s[32:33]\n"
    "s_add_u32 m0, s30, 83968\n"
    "s_nop 0\n"
    "global_load_lds_dwordx4 v90, s[32:33]\n"
    "s_add_u32 m0, s30, 84992\n"
    "s_nop 0\n"
    "global_load_lds_dwordx4 v91, s[32:33]\n"
    "ds_read_b64_tr_b16 a[48:49], v98 offset:32768\n"
    "ds_read_b64_tr_b16 a[50:51], v98 offset:34816\n"
    "ds_read_b64_tr_b16 a[52:53], v98 offset:40960\n"
    "ds_read_b64_tr_b16 a[54:55], v98 offset:43008\n"
    "ds_read_b64_tr_b16 a[112:113], v99 offset:32768\n"
    "ds_read_b64_tr_b16 a[114:115], v99 offset:34816\n"
    "ds_read_b64_tr_b16 a[116:117], v99 offset:40960\n"
    "ds_read_b64_tr_b16 a[118:119], v99 offset:43008\n"
    "s_waitcnt vmcnt(24) lgkmcnt(0)\n"
    "s_barrier\n"
    "s_add_u32 s32, s28, 0x10000\n"
    "s_addc_u32 s33, s29, 0\n"
    "s_add_u32 m0, s30, 98304\n"
    "s_nop 0\n"
    "global_load_lds_dwordx4 v88, s[32:33]\n"
    "s_add_u32 m0, s30, 99328\n"
    "s_nop 0\n"
    "global_load_lds_dwordx4 v89, s[32:33]\n"
    "s_add_u32 m0, s30, 100352\n"
    "s_nop 0\n"
    "global_load_lds_dwordx4 v90, s[32:33]\n"
    "s_add_u32 m0, s30, 101376\n"
    "s_nop 0\n"
    "global_load_lds_dwordx4 v91, s[32:33]\n"
    "ds_read_b64_tr_b16 a[56:57], v98 offset:49152\n"
    "ds_read_b64_tr_b16 a[58:59], v98 offset:51200\n"
    "ds_read_b64_tr_b16 a[60:61], v98 offset:57344\n"
    "ds_read_b64_tr_b16 a[62:63], v98 offset:59392\n"
    "ds_read_b64_tr_b16 a[120:121], v99 offset:49152\n"
    "ds_read_b64_tr_b16 a[122:123], v99 offset:51200\n"
    "ds_read_b64_tr_b16 a[124:125], v99 offset:57344\n"
    "ds_read_b64_tr_b16 a[126:127], v99 offset:59392\n"
    "s_branch L_rc0%=\n"
    "L_rb0%=:\n"
    "ds_read_b64_tr_b16 v[128:129], v96 offset:0\n"
    "ds_read_b64_tr_b16 v[130:131], v96 offset:2048\n"
    "ds_read_b64_tr_b16 v[132:133], v96 offset:8192\n"
    "ds_read_b64_tr_b16 v[134:135], v96 offset:10240\n"
    "ds_read_b64_tr_b16 v[136:137], v97 offset:0\n"
    "ds_read_b64_tr_b16 v[138:139], v97 offset:2048\n"
    "ds_read_b64_tr_b16 v[140:141], v97 offset:8192\n"
    "ds_read_b64_tr_b16 v[142:143], v97 offset:10240\n"
    "s_waitcnt vmcnt(24) lgkmcnt(0)\n"
    "s_barrier\n"
    "s_add_u32 s32, s26, 0x200000\n"
    "s_addc_u32 s33, s27, 0\n"
    "s_add_u32 m0, s30, 0\n"
    "s_nop 0\n"
    "global_load_lds_dwordx4 v84, s[32:33]\n"
    "s_add_u32 m0, s30, 1024\n"
    "s_nop 0\n"
    "global_load_lds_dwordx4 v85, s[32:33]\n"
    "s_add_u32 m0, s30, 2048\n"
    "s_nop 0\n"
    "global_load_lds_dwordx4 v86, s[32:33]\n"
    "s_add_u32 m0, s30, 3072\n"
    "s_nop 0\n"
    "global_load_lds_dwordx4 v87, s[32:33]\n"
    "ds_read_b64_tr_b16 v[144:145], v96 offset:16384\n"
    "ds_read_b64_tr_b16 v[146:147], v96 offset:18432\n"
    "ds_read_b64_tr_b16 v[148:149], v96 offset:24576\n"
    "ds_read_b64_tr_b16 v[150:151], v96 offset:26624\n"
    "ds_read_b64_tr_b16 v[152:153], v97 offset:16384\n"
    "ds_read_b64_tr_b16 v[154:155], v97 offset:18432\n"
    "ds_read_b64_tr_b16 v[156:157], v97 offset:24576\n"
    "ds_read_b64_tr_b16 v[158:159], v97 offset:26624\n"
    "s_waitcnt vmcnt(24) lgkmcnt(0)\n"
    "s_barrier\n"
    "s_add_u32 s32, s26, 0x280000\n"
    "s_addc_u32 s33, s27, 0\n"
    "s_add_u32 m0, s30, 16384\n"
    "s_nop 0\n"
    "global_load_lds_dwordx4 v84, s[32:33]\n"
    "s_add_u32 m0, s30, 17408\n"
    "s_nop 0\n"
    "global_load_lds_dwordx4 v85, s[32:33]\n"
    "s_add_u32 m0, s30, 18432\n"
    "s_nop 0\n"
    "global_load_lds_dwordx4 v86, s[32:33]\n"
    "s_add_u32 m0, s30, 19456\n"
    "s_nop 0\n"
    "global_load_lds_dwordx4 v87, s[32:33]\n"
    "ds_read_b64_tr_b16 v[160:161], v96 offset:32768\n"
    "ds_read_b64_tr_b16 v[162:163], v96 offset:34816\n"
    "ds_read_b64_tr_b16 v[164:165], v96 offset:40960\n"
    "ds_read_b64_tr_b16 v[166:167], v96 offset:43008\n"
    "ds_read_b64_tr_b16 v[168:169], v97 offset:32768\n"
    "ds_read_b64_tr_b16 v[170:171], v97 offset:34816\n"
    "ds_read_b64_tr_b16 v[172:173], v97 offset:40960\n"
    "ds_read_b64_tr_b16 v[174:175], v97 offset:43008\n"
    "s_waitcnt vmcnt(24) lgkmcnt(0)\n"
    "s_barrier\n"
    "s_add_u32 s32, s26, 0x300000\n"
    "s_addc_u32 s33, s27, 0\n"
    "s_add_u32 m0, s30, 32768\n"
    "s_nop 0\n"
    "global_load_lds_dwordx4 v84, s[32:33]\n"
    "s_add_u32 m0, s30, 33792\n"
    "s_nop 0\n"
    "global_load_lds_dwordx4 v85, s[32:33]\n"
    "s_add_u32 m0, s30, 34816\n"
    "s_nop 0\n"
    "global_load_lds_dwordx4 v86, s[32:33]\n"
    "s_add_u32 m0, s30, 35840\n"
    "s_nop 0\n"
    "global_load_lds_dwordx4 v87, s[32:33]\n"
    "ds_read_b64_tr_b16 v[176:177], v96 offset:49152\n"
    "ds_read_b64_tr_b16 v[178:179], v96 offset:51200\n"
    "ds_read_b64_tr_b16 v[180:181], v96 offset:57344\n"
    "ds_read_b64_tr_b16 v[182:183], v96 offset:59392\n"
    "ds_read_b64_tr_b16 v[184:185], v97 offset:49152\n"
    "ds_read_b64_tr_b16 v[186:187], v97 offset:51200\n"
    "ds_read_b64_tr_b16 v[188:189], v97 offset:57344\n"
    "ds_read_b64_tr_b16 v[190:191], v97 offset:59392\n"
    "s_waitcnt vmcnt(24) lgkmcnt(0)\n"
    "s_barrier\n"
    "s_add_u32 s32, s26, 0x380000\n"
    "s_addc_u32 s33, s27, 0\n"
    "s_add_u32 m0, s30, 49152\n"
    "s_nop 0\n"
    "global_load_lds_dwordx4 v84, s[32:33]\n"
    "s_add_u32 m0, s30, 50176\n"
    "s_nop 0\n"
    "global_load_lds_dwordx4 v85, s[32:33]\n"
    "s_add_u32 m0, s30, 51200\n"
    "s_nop 0\n"
    "global_load_lds_dwordx4 v86, s[32:33]\n"
    "s_add_u32 m0, s30, 52224\n"
    "s_nop 0\n"
    "global_load_lds_dwordx4 v87, s[32:33]\n"
    "ds_read_b64_tr_b16 v[192:193], v98 offset:0\n"
    "ds_read_b64_tr_b16 v[194:195], v98 offset:2048\n"
    "ds_read_b64_tr_b16 v[196:197], v98 offset:8192\n"
    "ds_read_b64_tr_b16 v[198:199], v98 offset:10240\n"
    "ds_read_b64_tr_b16 v[200:201], v99 offset:0\n"
    "ds_read_b64_tr_b16 v[202:203], v99 offset:2048\n"
    "ds_read_b64_tr_b16 v[204:205], v99 offset:8192\n"
    "ds_read_b64_tr_b16 v[206:207], v99 offset:10240\n"
    "s_waitcnt vmcnt(24) lgkmcnt(0)\n"
    "s_barrier\n"
    "s_add_u32 s32, s28, 0x0\n"
    "s_addc_u32 s33, s29, 0\n"
    "s_add_u32 m0, s30, 65536\n"
    "s_nop 0\n"
    "global_load_lds_dwordx4 v88, s[32:33]\n"
    "s_add_u32 m0, s30, 66560\n"
    "s_nop 0\n"
    "global_load_lds_dwordx4 v89, s[32:33]\n"
    "s_add_u32 m0, s30, 67584\n"
    "s_nop 0\n"
    "global_load_lds_dwordx4 v90, s[32:33]\n"
    "s_add_u32 m0, s30, 68608\n"
    "s_nop 0\n"
    "global_load_lds_dwordx4 v91, s[32:33]\n"
    "ds_read_b64_tr_b16 v[208:209], v98 offset:16384\n"
    "ds_read_b64_tr_b16 v[210:211], v98 offset:18432\n"
    "ds_read_b64_tr_b16 v[212:213], v98 offset:24576\n"
    "ds_read_b64_tr_b16 v[214:215], v98 offset:26624\n"
    "ds_read_b64_tr_b16 v[216:217], v99 offset:16384\n"
    "ds_read_b64_tr_b16 v[218:219], v99 offset:18432\n"
    "ds_read_b64_tr_b16 v[220:221], v99 offset:24576\n"
    "ds_read_b64_tr_b16 v[222:223], v99 offset:26624\n"
    "s_waitcnt vmcnt(24) lgkmcnt(0)\n"
    "s_barrier\n"
    "s_add_u32 s32, s28, 0x100\n"
    "s_addc_u32 s33, s29, 0\n"
    "s_add_u32 m0, s30, 81920\n"
    "s_nop 0\n"
    "global_load_lds_dwordx4 v88, s[32:33]\n"
    "s_add_u32 m0, s30, 82944\n"
    "s_nop 0\n"
    "global_load_lds_dwordx4 v89, s[32:33]\n"
    "s_add_u32 m0, s30, 83968\n"
    "s_nop 0\n"
    "global_load_lds_dwordx4 v90, s[32:33]\n"
    "s_add_u32 m0, s30, 84992\n"
    "s_nop 0\n"
    "global_load_lds_dwordx4 v91, s[32:33]\n"
    "ds_read_b64_tr_b16 v[224:225], v98 offset:32768\n"
    "ds_read_b64_tr_b16 v[226:227], v98 offset:34816\n"
    "ds_read_b64_tr_b16 v[228:229], v98 offset:40960\n"
    "ds_read_b64_tr_b16 v[230:231], v98 offset:43008\n"
    "ds_read_b64_tr_b16 v[232:233], v99 offset:32768\n"
    "ds_read_b64_tr_b16 v[234:235], v99 offset:34816\n"
    "ds_read_b64_tr_b16 v[236:237], v99 offset:40960\n"
    "ds_read_b64_tr_b16 v[238:239], v99 offset:43008\n"
    "s_waitcnt vmcnt(24) lgkmcnt(0)\n"
    "s_barrier\n"
    "s_add_u32 s32, s28, 0x10000\n"
    "s_addc_u32 s33, s29, 0\n"
    "s_add_u32 m0, s30, 98304\n"
    "s_nop 0\n"
    "global_load_lds_dwordx4 v88, s[32:33]\n"
    "s_add_u32 m0, s30, 99328\n"
    "s_nop 0\n"
    "global_load_lds_dwordx4 v89, s[32:33]\n"
    "s_add_u32 m0, s30, 100352\n"
    "s_nop 0\n"
    "global_load_lds_dwordx4 v90, s[32:33]\n"
    "s_add_u32 m0, s30, 101376\n"
    "s_nop 0\n"
    "global_load_lds_dwordx4 v91, s[32:33]\n"
    "ds_read_b64_tr_b16 v[240:241], v98 offset:49152\n"
    "ds_read_b64_tr_b16 v[242:243], v98 offset:51200\n"
    "ds_read_b64_tr_b16 v[244:245], v98 offset:57344\n"
    "ds_read_b64_tr_b16 v[246:247], v98 offset:59392\n"
    "ds_read_b64_tr_b16 v[248:249], v99 offset:49152\n"
    "ds_read_b64_tr_b16 v[250:251], v99 offset:51200\n"
    "ds_read_b64_tr_b16 v[252:253], v99 offset:57344\n"
    "ds_read_b64_tr_b16 v[254:255], v99 offset:59392\n"
    "L_rc0%=:\n"
    "s_mov_b32 s70, 0xcccccccc\n"
    "s_mov_b32 s71, 0xcccccccc\n"
    "s_mov_b32 s50, 0xffbfffbf\n"
    "s_mov_b32 s72, 0x33333333\n"
    "s_mov_b32 s73, 0x0\n"
    "s_mov_b32 s74, 0x0\n"
    "s_mov_b32 s75, 0x33333333\n"
    "v_mov_b32 v9, 0x4444\n"
    "v_mov_b32 v10, 0xeeee\n"
    "v_cndmask_b32 v67, v9, v10, s[70:71]\n"
    "s_mov_b32 s76, 0x3e0293ee\n"
    "s_mov_b32 s77, 0x3e0293ee\n"
    "v_mov_b32 v80, 0x3f803f80\n"
    "v_mov_b32 v81, 0x3f803f80\n"
    "v_mov_b32 v82, 0x3f803f80\n"
    "v_mov_b32 v83, 0x3f803f80\n"
    "v_lshrrev_b32 v8, 2, v1\n"
    "v_lshlrev_b32 v8, 3, v8\n"
    "v_lshl_add_u32 v8, v2, 8, v8\n"
    "v_and_b32 v9, 1, v2\n"
    "v_lshlrev_b32 v10, 1, v9\n"
    "v_and_b32 v11, 3, v1\n"
    "v_xor_b32 v10, v10, v11\n"
    "v_lshl_add_u32 v13, v10, 5, v8\n"
    "v_lshl_add_u32 v112, v9, 7, v13\n"
    "v_xor_b32 v10, 1, v10\n"
    "v_xor_b32 v9, 1, v9\n"
    "v_lshl_add_u32 v13, v10, 5, v8\n"
    "v_lshl_add_u32 v113, v9, 7, v13\n"
    "v_and_b32 v8, 13, v1\n"
    "v_lshlrev_b32 v8, 4, v8\n"
    "v_lshrrev_b32 v9, 1, v2\n"
    "v_lshl_add_u32 v8, v9, 8, v8\n"
    "v_and_b32 v9, 1, v2\n"
    "v_lshl_add_u32 v8, v9, 3, v8\n"
    "v_add_u32 v9, 1056, v8\n"
    "v_cndmask_b32 v75, v8, v9, s[70:71]\n"
    "v_lshrrev_b32 v8, 2, v1\n"
    "v_lshrrev_b32 v9, 3, v1\n"
    "v_xor_b32 v9, v9, v8\n"
    "v_lshlrev_b32 v9, 7, v9\n"
    "v_and_b32 v10, 3, v1\n"
    "v_lshl_add_u32 v9, v10, 3, v9\n"
    "v_xor_b32 v10, v8, v2\n"
    "v_lshl_add_u32 v114, v10, 5, v9\n"
    "v_and_b32 v10, 1, v2\n"
    "v_lshrrev_b32 v11, 1, v2\n"
    "v_lshl_add_u32 v10, v10, 1, v11\n"
    "v_xor_b32 v10, v8, v10\n"
    "v_lshl_add_u32 v115, v10, 5, v9\n"
    "v_lshrrev_b32 v79, 1, v71\n"
    "s_add_u32 s62, s36, 0x63e0000\n"
    "s_addc_u32 s63, s37, 0\n"
    "s_lshl_b32 s78, s42, 23\n"
    "s_add_u32 s62, s62, s78\n"
    "s_addc_u32 s63, s63, 0\n"
    "s_lshl_b32 s78, s41, 12\n"
    "s_add_u32 s62, s62, s78\n"
    "s_addc_u32 s63, s63, 0\n"
    "s_lshl_b32 s78, s41, 13\n"
    "s_lshl_b32 s79, s39, 10\n"
    "s_add_u32 s78, s78, s79\n"
    "s_add_u32 s58, s36, 0x1500800\n"
    "s_addc_u32 s59, s37, 0\n"
    "s_add_u32 s58, s58, s78\n"
    "s_addc_u32 s59, s59, 0\n"
    "s_add_u32 s78, s78, s79\n"
    "s_add_u32 s60, s36, 0x2500000\n"
    "s_addc_u32 s61, s37, 0\n"
    "s_add_u32 s60, s60, s78\n"
    "s_addc_u32 s61, s61, 0\n"
    "s_add_u32 s12, s36, 0x180000\n"
    "s_addc_u32 s13, s37, 0\n"
    "s_add_u32 s66, s36, 0x202000\n"
    "s_addc_u32 s67, s37, 0\n"
    "s_lshl_b32 s78, s41, 7\n"
    "s_add_u32 s66, s66, s78\n"
    "s_addc_u32 s67, s67, 0\n"
    "s_add_u32 s44, s36, 0x0\n"
    "s_addc_u32 s45, s37, 0\n"
    "s_waitcnt lgkmcnt(0)\n"
    "s_add_u32 s96, s96, 1\n"
    "s_addc_u32 s97, s97, 0\n"
    "s_lshl_b64 s[96:97], s[96:97], 6\n"
    "s_cmp_lg_u32 s42, 0\n"
    "s_cbranch_scc1 L_rb1%=\n"
    "s_waitcnt vmcnt(24) lgkmcnt(0)\n"
    "s_barrier\n"
    "s_add_u32 s32, s28, 0x10100\n"
    "s_addc_u32 s33, s29, 0\n"
    "s_add_u32 m0, s30, 114688\n"
    "s_nop 0\n"
    "global_load_lds_dwordx4 v88, s[32:33]\n"
    "s_add_u32 m0, s30, 115712\n"
    "s_nop 0\n"
    "global_load_lds_dwordx4 v89, s[32:33]\n"
    "s_add_u32 m0, s30, 116736\n"
    "s_nop 0\n"
    "global_load_lds_dwordx4 v90, s[32:33]\n"
    "s_add_u32 m0, s30, 117760\n"
    "s_nop 0\n"
    "global_load_lds_dwordx4 v91, s[32:33]\n"
    "ds_read_b64_tr_b16 a[128:129], v92 offset:0\n"
    "ds_read_b64_tr_b16 a[130:131], v92 offset:2048\n"
    "ds_read_b64_tr_b16 a[132:133], v92 offset:8192\n"
    "ds_read_b64_tr_b16 a[134:135], v92 offset:10240\n"
    "ds_read_b64_tr_b16 a[192:193], v93 offset:0\n"
    "ds_read_b64_tr_b16 a[194:195], v93 offset:2048\n"
    "ds_read_b64_tr_b16 a[196:197], v93 offset:8192\n"
    "ds_read_b64_tr_b16 a[198:199], v93 offset:10240\n"
    "s_waitcnt vmcnt(24) lgkmcnt(0)\n"
    "s_barrier\n"
    "s_add_u32 s32, s26, 0x200000\n"
    "s_addc_u32 s33, s27, 0\n"
    "s_add_u32 m0, s30, 0\n"
    "s_nop 0\n"
    "global_load_lds_dwordx4 v84, s[32:33]\n"
    "s_add_u32 m0, s30, 1024\n"
    "s_nop 0\n"
    "global_load_lds_dwordx4 v85, s[32:33]\n"
    "s_add_u32 m0, s30, 2048\n"
    "s_nop 0\n"
    "global_load_lds_dwordx4 v86, s[32:33]\n"
    "s_add_u32 m0, s30, 3072\n"
    "s_nop 0\n"
    "global_load_lds_dwordx4 v87, s[32:33]\n"
    "ds_read_b64_tr_b16 a[136:137], v92 offset:16384\n"
    "ds_read_b64_tr_b16 a[138:139], v92 offset:18432\n"
    "ds_read_b64_tr_b16 a[140:141], v92 offset:24576\n"
    "ds_read_b64_tr_b16 a[142:143], v92 offset:26624\n"
    "ds_read_b64_tr_b16 a[200:201], v93 offset:16384\n"
    "ds_read_b64_tr_b16 a[202:203], v93 offset:18432\n"
    "ds_read_b64_tr_b16 a[204:205], v93 offset:24576\n"
    "ds_read_b64_tr_b16 a[206:207], v93 offset:26624\n"
    "s_waitcnt vmcnt(24) lgkmcnt(0)\n"
    "s_barrier\n"
    "s_add_u32 s32, s26, 0x280000\n"
    "s_addc_u32 s33, s27, 0\n"
    "s_add_u32 m0, s30, 16384\n"
    "s_nop 0\n"
    "global_load_lds_dwordx4 v84, s[32:33]\n"
    "s_add_u32 m0, s30, 17408\n"
    "s_nop 0\n"
    "global_load_lds_dwordx4 v85, s[32:33]\n"
    "s_add_u32 m0, s30, 18432\n"
    "s_nop 0\n"
    "global_load_lds_dwordx4 v86, s[32:33]\n"
    "s_add_u32 m0, s30, 19456\n"
    "s_nop 0\n"
    "global_load_lds_dwordx4 v87, s[32:33]\n"
    "ds_read_b64_tr_b16 a[144:145], v92 offset:32768\n"
    "ds_read_b64_tr_b16 a[146:147], v92 offset:34816\n"
    "ds_read_b64_tr_b16 a[148:149], v92 offset:40960\n"
    "ds_read_b64_tr_b16 a[150:151], v92 offset:43008\n"
    "ds_read_b64_tr_b16 a[208:209], v93 offset:32768\n"
    "ds_read_b64_tr_b16 a[210:211], v93 offset:34816\n"
    "ds_read_b64_tr_b16 a[212:213], v93 offset:40960\n"
    "ds_read_b64_tr_b16 a[214:215], v93 offset:43008\n"
    "s_waitcnt vmcnt(24) lgkmcnt(0)\n"
    "s_barrier\n"
    "s_add_u32 s32, s26, 0x300000\n"
    "s_addc_u32 s33, s27, 0\n"
    "s_add_u32 m0, s30, 32768\n"
    "s_nop 0\n"
    "global_load_lds_dwordx4 v84, s[32:33]\n"
    "s_add_u32 m0, s30, 33792\n"
    "s_nop 0\n"
    "global_load_lds_dwordx4 v85, s[32:33]\n"
    "s_add_u32 m0, s30, 34816\n"
    "s_nop 0\n"
    "global_load_lds_dwordx4 v86, s[32:33]\n"
    "s_add_u32 m0, s30, 35840\n"
    "s_nop 0\n"
    "global_load_lds_dwordx4 v87, s[32:33]\n"
    "ds_read_b64_tr_b16 a[152:153], v92 offset:49152\n"
    "ds_read_b64_tr_b16 a[154:155], v92 offset:51200\n"
    "ds_read_b64_tr_b16 a[156:157], v92 offset:57344\n"
    "ds_read_b64_tr_b16 a[158:159], v92 offset:59392\n"
    "ds_read_b64_tr_b16 a[216:217], v93 offset:49152\n"
    "ds_read_b64_tr_b16 a[218:219], v93 offset:51200\n"
    "ds_read_b64_tr_b16 a[220:221], v93 offset:57344\n"
    "ds_read_b64_tr_b16 a[222:223], v93 offset:59392\n"
    "s_waitcnt vmcnt(24) lgkmcnt(0)\n"
    "s_barrier\n"
    "s_add_u32 s32, s26, 0x380000\n"
    "s_addc_u32 s33, s27, 0\n"
    "s_add_u32 m0, s30, 49152\n"
    "s_nop 0\n"
    "global_load_lds_dwordx4 v84, s[32:33]\n"
    "s_add_u32 m0, s30, 50176\n"
    "s_nop 0\n"
    "global_load_lds_dwordx4 v85, s[32:33]\n"
    "s_add_u32 m0, s30, 51200\n"
    "s_nop 0\n"
    "global_load_lds_dwordx4 v86, s[32:33]\n"
    "s_add_u32 m0, s30, 52224\n"
    "s_nop 0\n"
    "global_load_lds_dwordx4 v87, s[32:33]\n"
    "s_waitcnt vmcnt(24) lgkmcnt(0)\n"
    "s_barrier\n"
    "ds_read_b64_tr_b16 v[0:1], v104 offset:0\n"
    "ds_read_b64_tr_b16 v[2:3], v104 offset:512\n"
    "ds_read_b64_tr_b16 v[4:5], v104 offset:8192\n"
    "ds_read_b64_tr_b16 v[6:7], v104 offset:8704\n"
    "ds_read_b64_tr_b16 v[16:17], v104 offset:8\n"
    "ds_read_b64_tr_b16 v[18:19], v104 offset:520\n"
    "ds_read_b64_tr_b16 v[20:21], v104 offset:8200\n"
    "ds_read_b64_tr_b16 v[22:23], v104 offset:8712\n"
    "s_waitcnt lgkmcnt(4)\n"
    "ds_read_b64_tr_b16 v[32:33], v104 offset:16\n"
    "ds_read_b64_tr_b16 v[34:35], v104 offset:528\n"
    "ds_read_b64_tr_b16 v[36:37], v104 offset:8208\n"
    "ds_read_b64_tr_b16 v[38:39], v104 offset:8720\n"
    "ds_read_b64_tr_b16 v[48:49], v104 offset:24\n"
    "ds_read_b64_tr_b16 v[50:51], v104 offset:536\n"
    "ds_read_b64_tr_b16 v[52:53], v104 offset:8216\n"
    "ds_read_b64_tr_b16 v[54:55], v104 offset:8728\n"
    "s_waitcnt vmcnt(20) lgkmcnt(0)\n"
    "s_barrier\n"
    "s_add_u32 s32, s22, 0x0\n"
    "s_addc_u32 s33, s23, 0\n"
    "s_add_u32 m0, s30, 65536\n"
    "s_nop 0\n"
    "global_load_lds_dwordx4 v84, s[32:33]\n"
    "s_add_u32 m0, s30, 66560\n"
    "s_nop 0\n"
    "global_load_lds_dwordx4 v85, s[32:33]\n"
    "s_add_u32 m0, s30, 67584\n"
    "s_nop 0\n"
    "global_load_lds_dwordx4 v86, s[32:33]\n"
    "s_add_u32 m0, s30, 68608\n"
    "s_nop 0\n"
    "global_load_lds_dwordx4 v87, s[32:33]\n"
    "s_add_u32 s32, s22, 0x80000\n"
    "s_addc_u32 s33, s23, 0\n"
    "s_add_u32 m0, s30, 81920\n"
    "s_nop 0\n"
    "global_load_lds_dwordx4 v84, s[32:33]\n"
    "s_add_u32 m0, s30, 82944\n"
    "s_nop 0\n"
    "global_load_lds_dwordx4 v85, s[32:33]\n"
    "s_add_u32 m0, s30, 83968\n"
    "s_nop 0\n"
    "global_load_lds_dwordx4 v86, s[32:33]\n"
    "s_add_u32 m0, s30, 84992\n"
    "s_nop 0\n"
    "global_load_lds_dwordx4 v87, s[32:33]\n"
    "s_waitcnt vmcnt(24) lgkmcnt(0)\n"
    "s_barrier\n"
    "ds_read_b64_tr_b16 v[8:9], v104 offset:32768\n"
    "ds_read_b64_tr_b16 v[10:11], v104 offset:33280\n"
    "ds_read_b64_tr_b16 v[12:13], v104 offset:40960\n"
    "ds_read_b64_tr_b16 v[14:15], v104 offset:41472\n"
    "ds_read_b64_tr_b16 v[24:25], v104 offset:32776\n"
    "ds_read_b64_tr_b16 v[26:27], v104 offset:33288\n"
    "ds_read_b64_tr_b16 v[28:29], v104 offset:40968\n"
    "ds_read_b64_tr_b16 v[30:31], v104 offset:41480\n"
    "s_waitcnt lgkmcnt(4)\n"
    "ds_read_b64_tr_b16 v[40:41], v104 offset:32784\n"
    "ds_read_b64_tr_b16 v[42:43], v104 offset:33296\n"
    "ds_read_b64_tr_b16 v[44:45], v104 offset:40976\n"
    "ds_read_b64_tr_b16 v[46:47], v104 offset:41488\n"
    "ds_read_b64_tr_b16 v[56:57], v104 offset:32792\n"
    "ds_read_b64_tr_b16 v[58:59], v104 offset:33304\n"
    "ds_read_b64_tr_b16 v[60:61], v104 offset:40984\n"
    "ds_read_b64_tr_b16 v[62:63], v104 offset:41496\n"
    "s_waitcnt vmcnt(20) lgkmcnt(0)\n"
    "s_barrier\n"
    "s_add_u32 s32, s22, 0x100000\n"
    "s_addc_u32 s33, s23, 0\n"
    "s_add_u32 m0, s30, 98304\n"
    "s_nop 0\n"
    "global_load_lds_dwordx4 v84, s[32:33]\n"
    "s_add_u32 m0, s30, 99328\n"
    "s_nop 0\n"
    "global_load_lds_dwordx4 v85, s[32:33]\n"
    "s_add_u32 m0, s30, 100352\n"
    "s_nop 0\n"
    "global_load_lds_dwordx4 v86, s[32:33]\n"
    "s_add_u32 m0, s30, 101376\n"
    "s_nop 0\n"
    "global_load_lds_dwordx4 v87, s[32:33]\n"
    "s_add_u32 s32, s22, 0x180000\n"
    "s_addc_u32 s33, s23, 0\n"
    "s_add_u32 m0, s30, 114688\n"
    "s_nop 0\n"
    "global_load_lds_dwordx4 v84, s[32:33]\n"
    "s_add_u32 m0, s30, 115712\n"
    "s_nop 0\n"
    "global_load_lds_dwordx4 v85, s[32:33]\n"
    "s_add_u32 m0, s30, 116736\n"
    "s_nop 0\n"
    "global_load_lds_dwordx4 v86, s[32:33]\n"
    "s_add_u32 m0, s30, 117760\n"
    "s_nop 0\n"
    "global_load_lds_dwordx4 v87, s[32:33]\n"
    "ds_read_b64_tr_b16 a[160:161], v92 offset:0\n"
    "ds_read_b64_tr_b16 a[162:163], v92 offset:2048\n"
    "ds_read_b64_tr_b16 a[164:165], v92 offset:8192\n"
    "ds_read_b64_tr_b16 a[166:167], v92 offset:10240\n"
    "ds_read_b64_tr_b16 a[224:225], v93 offset:0\n"
    "ds_read_b64_tr_b16 a[226:227], v93 offset:2048\n"
    "ds_read_b64_tr_b16 a[228:229], v93 offset:8192\n"
    "ds_read_b64_tr_b16 a[230:231], v93 offset:10240\n"
    "s_waitcnt vmcnt(24) lgkmcnt(0)\n"
    "s_barrier\n"
    "s_add_u32 s32, s22, 0x200000\n"
    "s_addc_u32 s33, s23, 0\n"
    "s_add_u32 m0, s30, 0\n"
    "s_nop 0\n"
    "global_load_lds_dwordx4 v84, s[32:33]\n"
    "s_add_u32 m0, s30, 1024\n"
    "s_nop 0\n"
    "global_load_lds_dwordx4 v85, s[32:33]\n"
    "s_add_u32 m0, s30, 2048\n"
    "s_nop 0\n"
    "global_load_lds_dwordx4 v86, s[32:33]\n"
    "s_add_u32 m0, s30, 3072\n"
    "s_nop 0\n"
    "global_load_lds_dwordx4 v87, s[32:33]\n"
    "ds_read_b64_tr_b16 a[168:169], v92 offset:16384\n"
    "ds_read_b64_tr_b16 a[170:171], v92 offset:18432\n"
    "ds_read_b64_tr_b16 a[172:173], v92 offset:24576\n"
    "ds_read_b64_tr_b16 a[174:175], v92 offset:26624\n"
    "ds_read_b64_tr_b16 a[232:233], v93 offset:16384\n"
    "ds_read_b64_tr_b16 a[234:235], v93 offset:18432\n"
    "ds_read_b64_tr_b16 a[236:237], v93 offset:24576\n"
    "ds_read_b64_tr_b16 a[238:239], v93 offset:26624\n"
    "s_waitcnt vmcnt(24) lgkmcnt(0)\n"
    "s_barrier\n"
    "s_add_u32 s32, s22, 0x280000\n"
    "s_addc_u32 s33, s23, 0\n"
    "s_add_u32 m0, s30, 16384\n"
    "s_nop 0\n"
    "global_load_lds_dwordx4 v84, s[32:33]\n"
    "s_add_u32 m0, s30, 17408\n"
    "s_nop 0\n"
    "global_load_lds_dwordx4 v85, s[32:33]\n"
    "s_add_u32 m0, s30, 18432\n"
    "s_nop 0\n"
    "global_load_lds_dwordx4 v86, s[32:33]\n"
    "s_add_u32 m0, s30, 19456\n"
    "s_nop 0\n"
    "global_load_lds_dwordx4 v87, s[32:33]\n"
    "ds_read_b64_tr_b16 a[176:177], v92 offset:32768\n"
    "ds_read_b64_tr_b16 a[178:179], v92 offset:34816\n"
    "ds_read_b64_tr_b16 a[180:181], v92 offset:40960\n"
    "ds_read_b64_tr_b16 a[182:183], v92 offset:43008\n"
    "ds_read_b64_tr_b16 a[240:241], v93 offset:32768\n"
    "ds_read_b64_tr_b16 a[242:243], v93 offset:34816\n"
    "ds_read_b64_tr_b16 a[244:245], v93 offset:40960\n"
    "ds_read_b64_tr_b16 a[246:247], v93 offset:43008\n"
    "s_waitcnt vmcnt(24) lgkmcnt(0)\n"
    "s_barrier\n"
    "s_add_u32 s32, s22, 0x300000\n"
    "s_addc_u32 s33, s23, 0\n"
    "s_add_u32 m0, s30, 32768\n"
    "s_nop 0\n"
    "global_load_lds_dwordx4 v84, s[32:33]\n"
    "s_add_u32 m0, s30, 33792\n"
    "s_nop 0\n"
    "global_load_lds_dwordx4 v85, s[32:33]\n"
    "s_add_u32 m0, s30, 34816\n"
    "s_nop 0\n"
    "global_load_lds_dwordx4 v86, s[32:33]\n"
    "s_add_u32 m0, s30, 35840\n"
    "s_nop 0\n"
    "global_load_lds_dwordx4 v87, s[32:33]\n"
    "ds_read_b64_tr_b16 a[184:185], v92 offset:49152\n"
    "ds_read_b64_tr_b16 a[186:187], v92 offset:51200\n"
    "ds_read_b64_tr_b16 a[188:189], v92 offset:57344\n"
    "ds_read_b64_tr_b16 a[190:191], v92 offset:59392\n"
    "ds_read_b64_tr_b16 a[248:249], v93 offset:49152\n"
    "ds_read_b64_tr_b16 a[250:251], v93 offset:51200\n"
    "ds_read_b64_tr_b16 a[252:253], v93 offset:57344\n"
    "ds_read_b64_tr_b16 a[254:255], v93 offset:59392\n"
    "s_waitcnt vmcnt(24) lgkmcnt(0)\n"
    "s_barrier\n"
    "s_add_u32 s32, s22, 0x380000\n"
    "s_addc_u32 s33, s23, 0\n"
    "s_add_u32 m0, s30, 49152\n"
    "s_nop 0\n"
    "global_load_lds_dwordx4 v84, s[32:33]\n"
    "s_add_u32 m0, s30, 50176\n"
    "s_nop 0\n"
    "global_load_lds_dwordx4 v85, s[32:33]\n"
    "s_add_u32 m0, s30, 51200\n"
    "s_nop 0\n"
    "global_load_lds_dwordx4 v86, s[32:33]\n"
    "s_add_u32 m0, s30, 52224\n"
    "s_nop 0\n"
    "global_load_lds_dwordx4 v87, s[32:33]\n"
    "ds_read_b64_tr_b16 v[128:129], v98 offset:0\n"
    "ds_read_b64_tr_b16 v[130:131], v98 offset:2048\n"
    "ds_read_b64_tr_b16 v[132:133], v98 offset:8192\n"
    "ds_read_b64_tr_b16 v[134:135], v98 offset:10240\n"
    "ds_read_b64_tr_b16 v[136:137], v99 offset:0\n"
    "ds_read_b64_tr_b16 v[138:139], v99 offset:2048\n"
    "ds_read_b64_tr_b16 v[140:141], v99 offset:8192\n"
    "ds_read_b64_tr_b16 v[142:143], v99 offset:10240\n"
    "s_waitcnt vmcnt(24) lgkmcnt(0)\n"
    "s_barrier\n"
    "ds_read_b64_tr_b16 v[144:145], v98 offset:16384\n"
    "ds_read_b64_tr_b16 v[146:147], v98 offset:18432\n"
    "ds_read_b64_tr_b16 v[148:149], v98 offset:24576\n"
    "ds_read_b64_tr_b16 v[150:151], v98 offset:26624\n"
    "ds_read_b64_tr_b16 v[152:153], v99 offset:16384\n"
    "ds_read_b64_tr_b16 v[154:155], v99 offset:18432\n"
    "ds_read_b64_tr_b16 v[156:157], v99 offset:24576\n"
    "ds_read_b64_tr_b16 v[158:159], v99 offset:26624\n"
    "s_waitcnt vmcnt(20) lgkmcnt(0)\n"
    "s_barrier\n"
    "ds_read_b64_tr_b16 v[160:161], v98 offset:32768\n"
    "ds_read_b64_tr_b16 v[162:163], v98 offset:34816\n"
    "ds_read_b64_tr_b16 v[164:165], v98 offset:40960\n"
    "ds_read_b64_tr_b16 v[166:167], v98 offset:43008\n"
    "ds_read_b64_tr_b16 v[168:169], v99 offset:32768\n"
    "ds_read_b64_tr_b16 v[170:171], v99 offset:34816\n"
    "ds_read_b64_tr_b16 v[172:173], v99 offset:40960\n"
    "ds_read_b64_tr_b16 v[174:175], v99 offset:43008\n"
    "s_waitcnt vmcnt(16) lgkmcnt(0)\n"
    "s_barrier\n"
    "ds_read_b64_tr_b16 v[176:177], v98 offset:49152\n"
    "ds_read_b64_tr_b16 v[178:179], v98 offset:51200\n"
    "ds_read_b64_tr_b16 v[180:181], v98 offset:57344\n"
    "ds_read_b64_tr_b16 v[182:183], v98 offset:59392\n"
    "ds_read_b64_tr_b16 v[184:185], v99 offset:49152\n"
    "ds_read_b64_tr_b16 v[186:187], v99 offset:51200\n"
    "ds_read_b64_tr_b16 v[188:189], v99 offset:57344\n"
    "ds_read_b64_tr_b16 v[190:191], v99 offset:59392\n"
    "s_waitcnt vmcnt(12) lgkmcnt(0)\n"
    "s_barrier\n"
    "ds_read_b64_tr_b16 v[192:193], v96 offset:0\n"
    "ds_read_b64_tr_b16 v[194:195], v96 offset:2048\n"
    "ds_read_b64_tr_b16 v[196:197], v96 offset:8192\n"
    "ds_read_b64_tr_b16 v[198:199], v96 offset:10240\n"
    "ds_read_b64_tr_b16 v[200:201], v97 offset:0\n"
    "ds_read_b64_tr_b16 v[202:203], v97 offset:2048\n"
    "ds_read_b64_tr_b16 v[204:205], v97 offset:8192\n"
    "ds_read_b64_tr_b16 v[206:207], v97 offset:10240\n"
    "s_waitcnt vmcnt(8) lgkmcnt(0)\n"
    "s_barrier\n"
    "v_lshlrev_b32 v100, 3, v71\n"
    "s_add_u32 s82, s36, 0x20c000\n"
    "s_addc_u32 s83, s37, 0\n"
    "v_and_b32 v126, 16, v71\n"
    "v_lshlrev_b32 v126, 8, v126\n"
    "global_load_dwordx2 v[84:85], v100, s[82:83] sc1\n"
    "global_load_dwordx2 v[124:125], v126, s[66:67] sc1\n"
    "ds_read_b64_tr_b16 v[208:209], v96 offset:16384\n"
    "ds_read_b64_tr_b16 v[210:211], v96 offset:18432\n"
    "ds_read_b64_tr_b16 v[212:213], v96 offset:24576\n"
    "ds_read_b64_tr_b16 v[214:215], v96 offset:26624\n"
    "ds_read_b64_tr_b16 v[216:217], v97 offset:16384\n"
    "ds_read_b64_tr_b16 v[218:219], v97 offset:18432\n"
    "ds_read_b64_tr_b16 v[220:221], v97 offset:24576\n"
    "ds_read_b64_tr_b16 v[222:223], v97 offset:26624\n"
    "s_waitcnt vmcnt(6) lgkmcnt(0)\n"
    "s_barrier\n"
    "ds_read_b64_tr_b16 v[224:225], v96 offset:32768\n"
    "ds_read_b64_tr_b16 v[226:227], v96 offset:34816\n"
    "ds_read_b64_tr_b16 v[228:229], v96 offset:40960\n"
    "ds_read_b64_tr_b16 v[230:231], v96 offset:43008\n"
    "ds_read_b64_tr_b16 v[232:233], v97 offset:32768\n"
    "ds_read_b64_tr_b16 v[234:235], v97 offset:34816\n"
    "ds_read_b64_tr_b16 v[236:237], v97 offset:40960\n"
    "ds_read_b64_tr_b16 v[238:239], v97 offset:43008\n"
    "s_waitcnt vmcnt(2) lgkmcnt(0)\n"
    "s_barrier\n"
    "ds_read_b64_tr_b16 v[240:241], v96 offset:49152\n"
    "ds_read_b64_tr_b16 v[242:243], v96 offset:51200\n"
    "ds_read_b64_tr_b16 v[244:245], v96 offset:57344\n"
    "ds_read_b64_tr_b16 v[246:247], v96 offset:59392\n"
    "ds_read_b64_tr_b16 v[248:249], v97 offset:49152\n"
    "ds_read_b64_tr_b16 v[250:251], v97 offset:51200\n"
    "ds_read_b64_tr_b16 v[252:253], v97 offset:57344\n"
    "ds_read_b64_tr_b16 v[254:255], v97 offset:59392\n"
    "s_branch L_rc1%=\n"
    "L_rb1%=:\n"
    "s_waitcnt vmcnt(24) lgkmcnt(0)\n"
    "s_barrier\n"
    "s_add_u32 s32, s28, 0x10100\n"
    "s_addc_u32 s33, s29, 0\n"
    "s_add_u32 m0, s30, 114688\n"
    "s_nop 0\n"
    "global_load_lds_dwordx4 v88, s[32:33]\n"
    "s_add_u32 m0, s30, 115712\n"
    "s_nop 0\n"
    "global_load_lds_dwordx4 v89, s[32:33]\n"
    "s_add_u32 m0, s30, 116736\n"
    "s_nop 0\n"
    "global_load_lds_dwordx4 v90, s[32:33]\n"
    "s_add_u32 m0, s30, 117760\n"
    "s_nop 0\n"
    "global_load_lds_dwordx4 v91, s[32:33]\n"
    "ds_read_b64_tr_b16 a[160:161], v92 offset:0\n"
    "ds_read_b64_tr_b16 a[162:163], v92 offset:2048\n"
    "ds_read_b64_tr_b16 a[164:165], v92 offset:8192\n"
    "ds_read_b64_tr_b16 a[166:167], v92 offset:10240\n"
    "ds_read_b64_tr_b16 a[224:225], v93 offset:0\n"
    "ds_read_b64_tr_b16 a[226:227], v93 offset:2048\n"
    "ds_read_b64_tr_b16 a[228:229], v93 offset:8192\n"
    "ds_read_b64_tr_b16 a[230:231], v93 offset:10240\n"
    "s_waitcnt vmcnt(24) lgkmcnt(0)\n"
    "s_barrier\n"
    "s_add_u32 s32, s26, 0x0\n"
    "s_addc_u32 s33, s27, 0\n"
    "s_add_u32 m0, s30, 0\n"
    "s_nop 0\n"
    "global_load_lds_dwordx4 v84, s[32:33]\n"
    "s_add_u32 m0, s30, 1024\n"
    "s_nop 0\n"
    "global_load_lds_dwordx4 v85, s[32:33]\n"
    "s_add_u32 m0, s30, 2048\n"
    "s_nop 0\n"
    "global_load_lds_dwordx4 v86, s[32:33]\n"
    "s_add_u32 m0, s30, 3072\n"
    "s_nop 0\n"
    "global_load_lds_dwordx4 v87, s[32:33]\n"
    "ds_read_b64_tr_b16 a[168:169], v92 offset:16384\n"
    "ds_read_b64_tr_b16 a[170:171], v92 offset:18432\n"
    "ds_read_b64_tr_b16 a[172:173], v92 offset:24576\n"
    "ds_read_b64_tr_b16 a[174:175], v92 offset:26624\n"
    "ds_read_b64_tr_b16 a[232:233], v93 offset:16384\n"
    "ds_read_b64_tr_b16 a[234:235], v93 offset:18432\n"
    "ds_read_b64_tr_b16 a[236:237], v93 offset:24576\n"
    "ds_read_b64_tr_b16 a[238:239], v93 offset:26624\n"
    "s_waitcnt vmcnt(24) lgkmcnt(0)\n"
    "s_barrier\n"
    "s_add_u32 s32, s26, 0x80000\n"
    "s_addc_u32 s33, s27, 0\n"
    "s_add_u32 m0, s30, 16384\n"
    "s_nop 0\n"
    "global_load_lds_dwordx4 v84, s[32:33]\n"
    "s_add_u32 m0, s30, 17408\n"
    "s_nop 0\n"
    "global_load_lds_dwordx4 v85, s[32:33]\n"
    "s_add_u32 m0, s30, 18432\n"
    "s_nop 0\n"
    "global_load_lds_dwordx4 v86, s[32:33]\n"
    "s_add_u32 m0, s30, 19456\n"
    "s_nop 0\n"
    "global_load_lds_dwordx4 v87, s[32:33]\n"
    "ds_read_b64_tr_b16 a[176:177], v92 offset:32768\n"
    "ds_read_b64_tr_b16 a[178:179], v92 offset:34816\n"
    "ds_read_b64_tr_b16 a[180:181], v92 offset:40960\n"
    "ds_read_b64_tr_b16 a[182:183], v92 offset:43008\n"
    "ds_read_b64_tr_b16 a[240:241], v93 offset:32768\n"
    "ds_read_b64_tr_b16 a[242:243], v93 offset:34816\n"
    "ds_read_b64_tr_b16 a[244:245], v93 offset:40960\n"
    "ds_read_b64_tr_b16 a[246:247], v93 offset:43008\n"
    "s_waitcnt vmcnt(24) lgkmcnt(0)\n"
    "s_barrier\n"
    "s_add_u32 s32, s26, 0x100000\n"
    "s_addc_u32 s33, s27, 0\n"
    "s_add_u32 m0, s30, 32768\n"
    "s_nop 0\n"
    "global_load_lds_dwordx4 v84, s[32:33]\n"
    "s_add_u32 m0, s30, 33792\n"
    "s_nop 0\n"
    "global_load_lds_dwordx4 v85, s[32:33]\n"
    "s_add_u32 m0, s30, 34816\n"
    "s_nop 0\n"
    "global_load_lds_dwordx4 v86, s[32:33]\n"
    "s_add_u32 m0, s30, 35840\n"
    "s_nop 0\n"
    "global_load_lds_dwordx4 v87, s[32:33]\n"
    "ds_read_b64_tr_b16 a[184:185], v92 offset:49152\n"
    "ds_read_b64_tr_b16 a[186:187], v92 offset:51200\n"
    "ds_read_b64_tr_b16 a[188:189], v92 offset:57344\n"
    "ds_read_b64_tr_b16 a[190:191], v92 offset:59392\n"
    "ds_read_b64_tr_b16 a[248:249], v93 offset:49152\n"
    "ds_read_b64_tr_b16 a[250:251], v93 offset:51200\n"
    "ds_read_b64_tr_b16 a[252:253], v93 offset:57344\n"
    "ds_read_b64_tr_b16 a[254:255], v93 offset:59392\n"
    "s_waitcnt vmcnt(24) lgkmcnt(0)\n"
    "s_barrier\n"
    "s_add_u32 s32, s26, 0x180000\n"
    "s_addc_u32 s33, s27, 0\n"
    "s_add_u32 m0, s30, 49152\n"
    "s_nop 0\n"
    "global_load_lds_dwordx4 v84, s[32:33]\n"
    "s_add_u32 m0, s30, 50176\n"
    "s_nop 0\n"
    "global_load_lds_dwordx4 v85, s[32:33]\n"
    "s_add_u32 m0, s30, 51200\n"
    "s_nop 0\n"
    "global_load_lds_dwordx4 v86, s[32:33]\n"
    "s_add_u32 m0, s30, 52224\n"
    "s_nop 0\n"
    "global_load_lds_dwordx4 v87, s[32:33]\n"
    "s_waitcnt vmcnt(24) lgkmcnt(0)\n"
    "s_barrier\n"
    "ds_read_b64_tr_b16 v[0:1], v104 offset:0\n"
    "ds_read_b64_tr_b16 v[2:3], v104 offset:512\n"
    "ds_read_b64_tr_b16 v[4:5], v104 offset:8192\n"
    "ds_read_b64_tr_b16 v[6:7], v104 offset:8704\n"
    "ds_read_b64_tr_b16 v[16:17], v104 offset:8\n"
    "ds_read_b64_tr_b16 v[18:19], v104 offset:520\n"
    "ds_read_b64_tr_b16 v[20:21], v104 offset:8200\n"
    "ds_read_b64_tr_b16 v[22:23], v104 offset:8712\n"
    "s_waitcnt lgkmcnt(4)\n"
    "ds_read_b64_tr_b16 v[32:33], v104 offset:16\n"
    "ds_read_b64_tr_b16 v[34:35], v104 offset:528\n"
    "ds_read_b64_tr_b16 v[36:37], v104 offset:8208\n"
    "ds_read_b64_tr_b16 v[38:39], v104 offset:8720\n"
    "ds_read_b64_tr_b16 v[48:49], v104 offset:24\n"
    "ds_read_b64_tr_b16 v[50:51], v104 offset:536\n"
    "ds_read_b64_tr_b16 v[52:53], v104 offset:8216\n"
    "ds_read_b64_tr_b16 v[54:55], v104 offset:8728\n"
    "s_waitcnt vmcnt(20) lgkmcnt(0)\n"
    "s_barrier\n"
    "s_add_u32 s32, s24, 0x0\n"
    "s_addc_u32 s33, s25, 0\n"
    "s_add_u32 m0, s30, 65536\n"
    "s_nop 0\n"
    "global_load_lds_dwordx4 v84, s[32:33]\n"
    "s_add_u32 m0, s30, 66560\n"
    "s_nop 0\n"
    "global_load_lds_dwordx4 v85, s[32:33]\n"
    "s_add_u32 m0, s30, 67584\n"
    "s_nop 0\n"
    "global_load_lds_dwordx4 v86, s[32:33]\n"
    "s_add_u32 m0, s30, 68608\n"
    "s_nop 0\n"
    "global_load_lds_dwordx4 v87, s[32:33]\n"
    "s_add_u32 s32, s24, 0x80000\n"
    "s_addc_u32 s33, s25, 0\n"
    "s_add_u32 m0, s30, 81920\n"
    "s_nop 0\n"
    "global_load_lds_dwordx4 v84, s[32:33]\n"
    "s_add_u32 m0, s30, 82944\n"
    "s_nop 0\n"
    "global_load_lds_dwordx4 v85, s[32:33]\n"
    "s_add_u32 m0, s30, 83968\n"
    "s_nop 0\n"
    "global_load_lds_dwordx4 v86, s[32:33]\n"
    "s_add_u32 m0, s30, 84992\n"
    "s_nop 0\n"
    "global_load_lds_dwordx4 v87, s[32:33]\n"
    "s_waitcnt vmcnt(24) lgkmcnt(0)\n"
    "s_barrier\n"
    "ds_read_b64_tr_b16 v[8:9], v104 offset:32768\n"
    "ds_read_b64_tr_b16 v[10:11], v104 offset:33280\n"
    "ds_read_b64_tr_b16 v[12:13], v104 offset:40960\n"
    "ds_read_b64_tr_b16 v[14:15], v104 offset:41472\n"
    "ds_read_b64_tr_b16 v[24:25], v104 offset:32776\n"
    "ds_read_b64_tr_b16 v[26:27], v104 offset:33288\n"
    "ds_read_b64_tr_b16 v[28:29], v104 offset:40968\n"
    "ds_read_b64_tr_b16 v[30:31], v104 offset:41480\n"
    "s_waitcnt lgkmcnt(4)\n"
    "ds_read_b64_tr_b16 v[40:41], v104 offset:32784\n"
    "ds_read_b64_tr_b16 v[42:43], v104 offset:33296\n"
    "ds_read_b64_tr_b16 v[44:45], v104 offset:40976\n"
    "ds_read_b64_tr_b16 v[46:47], v104 offset:41488\n"
    "ds_read_b64_tr_b16 v[56:57], v104 offset:32792\n"
    "ds_read_b64_tr_b16 v[58:59], v104 offset:33304\n"
    "ds_read_b64_tr_b16 v[60:61], v104 offset:40984\n"
    "ds_read_b64_tr_b16 v[62:63], v104 offset:41496\n"
    "s_waitcnt vmcnt(20) lgkmcnt(0)\n"
    "s_barrier\n"
    "s_add_u32 s32, s24, 0x100000\n"
    "s_addc_u32 s33, s25, 0\n"
    "s_add_u32 m0, s30, 98304\n"
    "s_nop 0\n"
    "global_load_lds_dwordx4 v84, s[32:33]\n"
    "s_add_u32 m0, s30, 99328\n"
    "s_nop 0\n"
    "global_load_lds_dwordx4 v85, s[32:33]\n"
    "s_add_u32 m0, s30, 100352\n"
    "s_nop 0\n"
    "global_load_lds_dwordx4 v86, s[32:33]\n"
    "s_add_u32 m0, s30, 101376\n"
    "s_nop 0\n"
    "global_load_lds_dwordx4 v87, s[32:33]\n"
    "s_add_u32 s32, s24, 0x180000\n"
    "s_addc_u32 s33, s25, 0\n"
    "s_add_u32 m0, s30, 114688\n"
    "s_nop 0\n"
    "global_load_lds_dwordx4 v84, s[32:33]\n"
    "s_add_u32 m0, s30, 115712\n"
    "s_nop 0\n"
    "global_load_lds_dwordx4 v85, s[32:33]\n"
    "s_add_u32 m0, s30, 116736\n"
    "s_nop 0\n"
    "global_load_lds_dwordx4 v86, s[32:33]\n"
    "s_add_u32 m0, s30, 117760\n"
    "s_nop 0\n"
    "global_load_lds_dwordx4 v87, s[32:33]\n"
    "ds_read_b64_tr_b16 a[128:129], v92 offset:0\n"
    "ds_read_b64_tr_b16 a[130:131], v92 offset:2048\n"
    "ds_read_b64_tr_b16 a[132:133], v92 offset:8192\n"
    "ds_read_b64_tr_b16 a[134:135], v92 offset:10240\n"
    "ds_read_b64_tr_b16 a[192:193], v93 offset:0\n"
    "ds_read_b64_tr_b16 a[194:195], v93 offset:2048\n"
    "ds_read_b64_tr_b16 a[196:197], v93 offset:8192\n"
    "ds_read_b64_tr_b16 a[198:199], v93 offset:10240\n"
    "s_waitcnt vmcnt(24) lgkmcnt(0)\n"
    "s_barrier\n"
    "s_add_u32 s32, s24, 0x200000\n"
    "s_addc_u32 s33, s25, 0\n"
    "s_add_u32 m0, s30, 0\n"
    "s_nop 0\n"
    "global_load_lds_dwordx4 v84, s[32:33]\n"
    "s_add_u32 m0, s30, 1024\n"
    "s_nop 0\n"
    "global_load_lds_dwordx4 v85, s[32:33]\n"
    "s_add_u32 m0, s30, 2048\n"
    "s_nop 0\n"
    "global_load_lds_dwordx4 v86, s[32:33]\n"
    "s_add_u32 m0, s30, 3072\n"
    "s_nop 0\n"
    "global_load_lds_dwordx4 v87, s[32:33]\n"
    "ds_read_b64_tr_b16 a[136:137], v92 offset:16384\n"
    "ds_read_b64_tr_b16 a[138:139], v92 offset:18432\n"
    "ds_read_b64_tr_b16 a[140:141], v92 offset:24576\n"
    "ds_read_b64_tr_b16 a[142:143], v92 offset:26624\n"
    "ds_read_b64_tr_b16 a[200:201], v93 offset:16384\n"
    "ds_read_b64_tr_b16 a[202:203], v93 offset:18432\n"
    "ds_read_b64_tr_b16 a[204:205], v93 offset:24576\n"
    "ds_read_b64_tr_b16 a[206:207], v93 offset:26624\n"
    "s_waitcnt vmcnt(24) lgkmcnt(0)\n"
    "s_barrier\n"
    "s_add_u32 s32, s24, 0x280000\n"
    "s_addc_u32 s33, s25, 0\n"
    "s_add_u32 m0, s30, 16384\n"
    "s_nop 0\n"
    "global_load_lds_dwordx4 v84, s[32:33]\n"
    "s_add_u32 m0, s30, 17408\n"
    "s_nop 0\n"
    "global_load_lds_dwordx4 v85, s[32:33]\n"
    "s_add_u32 m0, s30, 18432\n"
    "s_nop 0\n"
    "global_load_lds_dwordx4 v86, s[32:33]\n"
    "s_add_u32 m0, s30, 19456\n"
    "s_nop 0\n"
    "global_load_lds_dwordx4 v87, s[32:33]\n"
    "ds_read_b64_tr_b16 a[144:145], v92 offset:32768\n"
    "ds_read_b64_tr_b16 a[146:147], v92 offset:34816\n"
    "ds_read_b64_tr_b16 a[148:149], v92 offset:40960\n"
    "ds_read_b64_tr_b16 a[150:151], v92 offset:43008\n"
    "ds_read_b64_tr_b16 a[208:209], v93 offset:32768\n"
    "ds_read_b64_tr_b16 a[210:211], v93 offset:34816\n"
    "ds_read_b64_tr_b16 a[212:213], v93 offset:40960\n"
    "ds_read_b64_tr_b16 a[214:215], v93 offset:43008\n"
    "s_waitcnt vmcnt(24) lgkmcnt(0)\n"
    "s_barrier\n"
    "s_add_u32 s32, s24, 0x300000\n"
    "s_addc_u32 s33, s25, 0\n"
    "s_add_u32 m0, s30, 32768\n"
    "s_nop 0\n"
    "global_load_lds_dwordx4 v84, s[32:33]\n"
    "s_add_u32 m0, s30, 33792\n"
    "s_nop 0\n"
    "global_load_lds_dwordx4 v85, s[32:33]\n"
    "s_add_u32 m0, s30, 34816\n"
    "s_nop 0\n"
    "global_load_lds_dwordx4 v86, s[32:33]\n"
    "s_add_u32 m0, s30, 35840\n"
    "s_nop 0\n"
    "global_load_lds_dwordx4 v87, s[32:33]\n"
    "ds_read_b64_tr_b16 a[152:153], v92 offset:49152\n"
    "ds_read_b64_tr_b16 a[154:155], v92 offset:51200\n"
    "ds_read_b64_tr_b16 a[156:157], v92 offset:57344\n"
    "ds_read_b64_tr_b16 a[158:159], v92 offset:59392\n"
    "ds_read_b64_tr_b16 a[216:217], v93 offset:49152\n"
    "ds_read_b64_tr_b16 a[218:219], v93 offset:51200\n"
    "ds_read_b64_tr_b16 a[220:221], v93 offset:57344\n"
    "ds_read_b64_tr_b16 a[222:223], v93 offset:59392\n"
    "s_waitcnt vmcnt(24) lgkmcnt(0)\n"
    "s_barrier\n"
    "s_add_u32 s32, s24, 0x380000\n"
    "s_addc_u32 s33, s25, 0\n"
    "s_add_u32 m0, s30, 49152\n"
    "s_nop 0\n"
    "global_load_lds_dwordx4 v84, s[32:33]\n"
    "s_add_u32 m0, s30, 50176\n"
    "s_nop 0\n"
    "global_load_lds_dwordx4 v85, s[32:33]\n"
    "s_add_u32 m0, s30, 51200\n"
    "s_nop 0\n"
    "global_load_lds_dwordx4 v86, s[32:33]\n"
    "s_add_u32 m0, s30, 52224\n"
    "s_nop 0\n"
    "global_load_lds_dwordx4 v87, s[32:33]\n"
    "ds_read_b64_tr_b16 a[0:1], v98 offset:0\n"
    "ds_read_b64_tr_b16 a[2:3], v98 offset:2048\n"
    "ds_read_b64_tr_b16 a[4:5], v98 offset:8192\n"
    "ds_read_b64_tr_b16 a[6:7], v98 offset:10240\n"
    "ds_read_b64_tr_b16 a[64:65], v99 offset:0\n"
    "ds_read_b64_tr_b16 a[66:67], v99 offset:2048\n"
    "ds_read_b64_tr_b16 a[68:69], v99 offset:8192\n"
    "ds_read_b64_tr_b16 a[70:71], v99 offset:10240\n"
    "s_waitcnt vmcnt(24) lgkmcnt(0)\n"
    "s_barrier\n"
    "ds_read_b64_tr_b16 a[8:9], v98 offset:16384\n"
    "ds_read_b64_tr_b16 a[10:11], v98 offset:18432\n"
    "ds_read_b64_tr_b16 a[12:13], v98 offset:24576\n"
    "ds_read_b64_tr_b16 a[14:15], v98 offset:26624\n"
    "ds_read_b64_tr_b16 a[72:73], v99 offset:16384\n"
    "ds_read_b64_tr_b16 a[74:75], v99 offset:18432\n"
    "ds_read_b64_tr_b16 a[76:77], v99 offset:24576\n"
    "ds_read_b64_tr_b16 a[78:79], v99 offset:26624\n"
    "s_waitcnt vmcnt(20) lgkmcnt(0)\n"
    "s_barrier\n"
    "ds_read_b64_tr_b16 a[16:17], v98 offset:32768\n"
    "ds_read_b64_tr_b16 a[18:19], v98 offset:34816\n"
    "ds_read_b64_tr_b16 a[20:21], v98 offset:40960\n"
    "ds_read_b64_tr_b16 a[22:23], v98 offset:43008\n"
    "ds_read_b64_tr_b16 a[80:81], v99 offset:32768\n"
    "ds_read_b64_tr_b16 a[82:83], v99 offset:34816\n"
    "ds_read_b64_tr_b16 a[84:85], v99 offset:40960\n"
    "ds_read_b64_tr_b16 a[86:87], v99 offset:43008\n"
    "s_waitcnt vmcnt(16) lgkmcnt(0)\n"
    "s_barrier\n"
    "ds_read_b64_tr_b16 a[24:25], v98 offset:49152\n"
    "ds_read_b64_tr_b16 a[26:27], v98 offset:51200\n"
    "ds_read_b64_tr_b16 a[28:29], v98 offset:57344\n"
    "ds_read_b64_tr_b16 a[30:31], v98 offset:59392\n"
    "ds_read_b64_tr_b16 a[88:89], v99 offset:49152\n"
    "ds_read_b64_tr_b16 a[90:91], v99 offset:51200\n"
    "ds_read_b64_tr_b16 a[92:93], v99 offset:57344\n"
    "ds_read_b64_tr_b16 a[94:95], v99 offset:59392\n"
    "s_waitcnt vmcnt(12) lgkmcnt(0)\n"
    "s_barrier\n"
    "ds_read_b64_tr_b16 a[32:33], v96 offset:0\n"
    "ds_read_b64_tr_b16 a[34:35], v96 offset:2048\n"
    "ds_read_b64_tr_b16 a[36:37], v96 offset:8192\n"
    "ds_read_b64_tr_b16 a[38:39], v96 offset:10240\n"
    "ds_read_b64_tr_b16 a[96:97], v97 offset:0\n"
    "ds_read_b64_tr_b16 a[98:99], v97 offset:2048\n"
    "ds_read_b64_tr_b16 a[100:101], v97 offset:8192\n"
    "ds_read_b64_tr_b16 a[102:103], v97 offset:10240\n"
    "s_waitcnt vmcnt(8) lgkmcnt(0)\n"
    "s_barrier\n"
    "v_lshlrev_b32 v100, 3, v71\n"
    "s_add_u32 s82, s36, 0x20c000\n"
    "s_addc_u32 s83, s37, 0\n"
    "v_and_b32 v126, 16, v71\n"
    "v_lshlrev_b32 v126, 8, v126\n"
    "global_load_dwordx2 v[84:85], v100, s[82:83] sc1\n"
    "global_load_dwordx2 v[124:125], v126, s[66:67] sc1\n"
    "ds_read_b64_tr_b16 a[40:41], v96 offset:16384\n"
    "ds_read_b64_tr_b16 a[42:43], v96 offset:18432\n"
    "ds_read_b64_tr_b16 a[44:45], v96 offset:24576\n"
    "ds_read_b64_tr_b16 a[46:47], v96 offset:26624\n"
    "ds_read_b64_tr_b16 a[104:105], v97 offset:16384\n"
    "ds_read_b64_tr_b16 a[106:107], v97 offset:18432\n"
    "ds_read_b64_tr_b16 a[108:109], v97 offset:24576\n"
    "ds_read_b64_tr_b16 a[110:111], v97 offset:26624\n"
    "s_waitcnt vmcnt(6) lgkmcnt(0)\n"
    "s_barrier\n"
    "ds_read_b64_tr_b16 a[48:49], v96 offset:32768\n"
    "ds_read_b64_tr_b16 a[50:51], v96 offset:34816\n"
    "ds_read_b64_tr_b16 a[52:53], v96 offset:40960\n"
    "ds_read_b64_tr_b16 a[54:55], v96 offset:43008\n"
    "ds_read_b64_tr_b16 a[112:113], v97 offset:32768\n"
    "ds_read_b64_tr_b16 a[114:115], v97 offset:34816\n"
    "ds_read_b64_tr_b16 a[116:117], v97 offset:40960\n"
    "ds_read_b64_tr_b16 a[118:119], v97 offset:43008\n"
    "s_waitcnt vmcnt(2) lgkmcnt(0)\n"
    "s_barrier\n"
    "ds_read_b64_tr_b16 a[56:57], v96 offset:49152\n"
    "ds_read_b64_tr_b16 a[58:59], v96 offset:51200\n"
    "ds_read_b64_tr_b16 a[60:61], v96 offset:57344\n"
    "ds_read_b64_tr_b16 a[62:63], v96 offset:59392\n"
    "ds_read_b64_tr_b16 a[120:121], v97 offset:49152\n"
    "ds_read_b64_tr_b16 a[122:123], v97 offset:51200\n"
    "ds_read_b64_tr_b16 a[124:125], v97 offset:57344\n"
    "ds_read_b64_tr_b16 a[126:127], v97 offset:59392\n"
    "L_rc1%=:\n"
    "s_waitcnt vmcnt(0) lgkmcnt(0)\n"
    "s_barrier\n"
    "v_xor_b32 v124, s96, v124\n"
    "v_xor_b32 v125, s97, v125\n"
    "v_cmp_gt_u64 vcc, 64, v[124:125]\n"
    "s_nop 1\n"
    "v_cndmask_b32 v124, 0, v124, vcc\n"
    "s_nop 0\n"
    "v_readlane_b32 s80, v124, 0\n"
    "v_readlane_b32 s81, v124, 1\n"
    "ds_write_b128 v116, v[60:63] offset:15360\n"
    "ds_write_b128 v116, v[56:59] offset:14336\n"
    "ds_write_b128 v116, v[52:55] offset:13312\n"
    "ds_write_b128 v116, v[48:51] offset:12288\n"
    "ds_write_b128 v116, v[44:47] offset:11264\n"
    "ds_write_b128 v116, v[40:43] offset:10240\n"
    "ds_write_b128 v116, v[36:39] offset:9216\n"
    "ds_write_b128 v116, v[32:35] offset:8192\n"
    "s_waitcnt lgkmcnt(4)\n"
    "ds_write_b128 v116, v[28:31] offset:7168\n"
    "ds_write_b128 v116, v[24:27] offset:6144\n"
    "ds_write_b128 v116, v[20:23] offset:5120\n"
    "ds_write_b128 v116, v[16:19] offset:4096\n"
    "ds_write_b128 v116, v[12:15] offset:3072\n"
    "ds_write_b128 v116, v[8:11] offset:2048\n"
    "ds_write_b128 v116, v[4:7] offset:1024\n"
    "ds_write_b128 v116, v[0:3] offset:0\n"
    "s_waitcnt lgkmcnt(4)\n"
    "s_min_u32 s92, s80, s81\n"
    "s_mov_b32 s43, 0\n"
    "s_mov_b32 s19, 0\n"
    "s_cmp_eq_u32 s92, 0\n"
    "s_cbranch_scc1 L_nokv%=\n"
    "global_load_dwordx4 v[56:59], v71, s[58:59] offset:-2048 sc1\n"
    "global_load_dwordx4 v[60:63], v71, s[58:59] offset:2048 sc1\n"
    "global_load_dwordx2 v[64:65], v71, s[60:61] offset:0 sc1\n"
    "global_load_dwordx2 v[68:69], v71, s[60:61] offset:8 sc1\n"
    "global_load_dwordx2 v[72:73], v71, s[60:61] offset:1024 sc1\n"
    "global_load_dwordx2 v[76:77], v71, s[60:61] offset:1032 sc1\n"
    "s_mov_b32 s19, 1\n"
    "L_nokv%=:\n"
    "s_cmp_lg_u32 s39, 0\n"
    "s_cbranch_scc1 L_sv1%=\n"
    "ds_write_b128 v116, a[0:3] offset:16384\n"
    "ds_write_b128 v116, a[4:7] offset:17408\n"
    "ds_write_b128 v116, a[64:67] offset:18432\n"
    "ds_write_b128 v116, a[68:71] offset:19456\n"
    "ds_write_b128 v116, a[8:11] offset:20480\n"
    "ds_write_b128 v116, a[12:15] offset:21504\n"
    "ds_write_b128 v116, a[72:75] offset:22528\n"
    "ds_write_b128 v116, a[76:79] offset:23552\n"
    "s_waitcnt lgkmcnt(4)\n"
    "ds_write_b128 v116, a[16:19] offset:24576\n"
    "ds_write_b128 v116, a[20:23] offset:25600\n"
    "ds_write_b128 v116, a[80:83] offset:26624\n"
    "ds_write_b128 v116, a[84:87] offset:27648\n"
    "ds_write_b128 v116, a[32:35] offset:28672\n"
    "ds_write_b128 v116, a[36:39] offset:29696\n"
    "ds_write_b128 v116, a[96:99] offset:30720\n"
    "ds_write_b128 v116, a[100:103] offset:31744\n"
    "s_waitcnt lgkmcnt(4)\n"
    "ds_write_b128 v116, v[128:131] offset:32768\n"
    "ds_write_b128 v116, v[132:135] offset:33792\n"
    "s_branch L_sv4%=\n"
    "L_sv1%=:\n"
    "s_cmp_lg_u32 s39, 1\n"
    "s_cbranch_scc1 L_sv2%=\n"
    "ds_write_b128 v116, a[16:19] offset:16384\n"
    "ds_write_b128 v116, a[20:23] offset:17408\n"
    "ds_write_b128 v116, a[80:83] offset:18432\n"
    "ds_write_b128 v116, a[84:87] offset:19456\n"
    "ds_write_b128 v116, a[24:27] offset:20480\n"
    "ds_write_b128 v116, a[28:31] offset:21504\n"
    "ds_write_b128 v116, a[88:91] offset:22528\n"
    "ds_write_b128 v116, a[92:95] offset:23552\n"
    "s_waitcnt lgkmcnt(4)\n"
    "ds_write_b128 v116, a[0:3] offset:24576\n"
    "ds_write_b128 v116, a[4:7] offset:25600\n"
    "ds_write_b128 v116, a[64:67] offset:26624\n"
    "ds_write_b128 v116, a[68:71] offset:27648\n"
    "ds_write_b128 v116, a[48:51] offset:28672\n"
    "ds_write_b128 v116, a[52:55] offset:29696\n"
    "ds_write_b128 v116, a[112:115] offset:30720\n"
    "ds_write_b128 v116, a[116:119] offset:31744\n"
    "s_waitcnt lgkmcnt(4)\n"
    "ds_write_b128 v116, v[160:163] offset:32768\n"
    "ds_write_b128 v116, v[164:167] offset:33792\n"
    "s_branch L_sv4%=\n"
    "L_sv2%=:\n"
    "s_cmp_lg_u32 s39, 2\n"
    "s_cbranch_scc1 L_sv3%=\n"
    "ds_write_b128 v116, a[32:35] offset:16384\n"
    "ds_write_b128 v116, a[36:39] offset:17408\n"
    "ds_write_b128 v116, a[96:99] offset:18432\n"
    "ds_write_b128 v116, a[100:103] offset:19456\n"
    "ds_write_b128 v116, a[40:43] offset:20480\n"
    "ds_write_b128 v116, a[44:47] offset:21504\n"
    "ds_write_b128 v116, a[104:107] offset:22528\n"
    "ds_write_b128 v116, a[108:111] offset:23552\n"
    "s_waitcnt lgkmcnt(4)\n"
    "ds_write_b128 v116, a[48:51] offset:24576\n"
    "ds_write_b128 v116, a[52:55] offset:25600\n"
    "ds_write_b128 v116, a[112:115] offset:26624\n"
    "ds_write_b128 v116, a[116:119] offset:27648\n"
    "ds_write_b128 v116, a[0:3] offset:28672\n"
    "ds_write_b128 v116, a[4:7] offset:29696\n"
    "ds_write_b128 v116, a[64:67] offset:30720\n"
    "ds_write_b128 v116, a[68:71] offset:31744\n"
    "s_waitcnt lgkmcnt(4)\n"
    "ds_write_b128 v116, v[192:195] offset:32768\n"
    "ds_write_b128 v116, v[196:199] offset:33792\n"
    "s_branch L_sv4%=\n"
    "L_sv3%=:\n"
    "ds_write_b128 v116, a[48:51] offset:16384\n"
    "ds_write_b128 v116, a[52:55] offset:17408\n"
    "ds_write_b128 v116, a[112:115] offset:18432\n"
    "ds_write_b128 v116, a[116:119] offset:19456\n"
    "ds_write_b128 v116, a[56:59] offset:20480\n"
    "ds_write_b128 v116, a[60:63] offset:21504\n"
    "ds_write_b128 v116, a[120:123] offset:22528\n"
    "ds_write_b128 v116, a[124:127] offset:23552\n"
    "s_waitcnt lgkmcnt(4)\n"
    "ds_write_b128 v116, a[32:35] offset:24576\n"
    "ds_write_b128 v116, a[36:39] offset:25600\n"
    "ds_write_b128 v116, a[96:99] offset:26624\n"
    "ds_write_b128 v116, a[100:103] offset:27648\n"
    "ds_write_b128 v116, a[16:19] offset:28672\n"
    "ds_write_b128 v116, a[20:23] offset:29696\n"
    "ds_write_b128 v116, a[80:83] offset:30720\n"
    "ds_write_b128 v116, a[84:87] offset:31744\n"
    "s_waitcnt lgkmcnt(4)\n"
    "ds_write_b128 v116, v[224:227] offset:32768\n"
    "ds_write_b128 v116, v[228:231] offset:33792\n"
    "L_sv4%=:\n"
    "v_lshrrev_b32 v0, 4, v71\n"
    "v_and_b32 v1, 15, v0\n"
    "v_lshrrev_b32 v2, 4, v0\n"
    "s_branch L_tabc%=\n"
    "L_tab%=:\n"
    "global_load_dwordx2 v[84:85], v100, s[82:83] sc1\n"
    "s_waitcnt vmcnt(0)\n"
    "L_tabc%=:\n"
    "v_xor_b32 v84, s96, v84\n"
    "v_xor_b32 v85, s97, v85\n"
    "v_cmp_gt_u64 vcc, 64, v[84:85]\n"
    "s_nop 1\n"
    "v_cndmask_b32 v84, 0, v84, vcc\n"
    "v_cmp_eq_u32 vcc, 0, v84\n"
    "s_nop 0\n"
    "s_cbranch_vccz L_tabok%=\n"
    "s_sleep 2\n"
    "s_add_u32 s90, s90, 1\n"
    "s_and_b32 s78, s90, 0xff\n"
    "s_cmp_lg_u32 s78, 0\n"
    "s_cbranch_scc1 L_tab%=\n"
    "v_mov_b32 v126, 0\n"
    "global_load_dwordx2 v[126:127], v126, s[68:69] sc1\n"
    "s_waitcnt vmcnt(0)\n"
    "v_cmp_eq_u64 vcc, s[96:97], v[126:127]\n"
    "s_nop 1\n"
    "s_cbranch_vccnz L_bail_tab%=\n"
    "s_cmp_lt_u32 s90, 0x40000\n"
    "s_cbranch_scc1 L_tab%=\n"
    "v_mov_b32 v126, s96\n"
    "v_mov_b32 v127, s97\n"
    "s_mov_b64 exec, 1\n"
    "global_store_dwordx2 v71, v[126:127], s[68:69] sc1\n"
    "s_mov_b64 exec, -1\n"
    "L_bail_tab%=:\n"
    "L_tabok%=:\n"
    "v_readfirstlane_b32 s93, v84\n"
    "s_nop 0\n"
    "s_sub_u32 s93, s93, 1\n"
    "s_and_b32 s93, s93, 7\n"
    "s_lshl_b32 s78, s42, 4\n"
    "s_lshl_b32 s79, s39, 2\n"
    "s_add_u32 s78, s78, s79\n"
    "v_lshrrev_b32 v8, 2, v1\n"
    "v_add_u32 v8, s78, v8\n"
    "v_lshrrev_b32 v10, 1, v8\n"
    "v_lshlrev_b32 v10, 16, v10\n"
    "v_and_b32 v9, 1, v8\n"
    "s_lshr_b32 s78, s39, 1\n"
    "s_lshl_b32 s78, s78, 4\n"
    "s_xor_b32 s78, s78, s41\n"
    "v_xor_b32 v8, s78, v8\n"
    "v_lshl_add_u32 v10, v8, 7, v10\n"
    "v_lshrrev_b32 v8, 1, v2\n"
    "v_lshl_add_u32 v9, v8, 2, v9\n"
    "s_lshl_b32 s78, s42, 2\n"
    "s_xor_b32 s78, s78, s93\n"
    "v_xor_b32 v9, s78, v9\n"
    "v_lshl_add_u32 v10, v9, 12, v10\n"
    "v_and_b32 v8, 1, v2\n"
    "v_lshl_add_u32 v10, v8, 6, v10\n"
    "v_and_b32 v8, 3, v1\n"
    "v_lshl_add_u32 v117, v8, 4, v10\n"
    "s_lshl_b32 s78, s42, 5\n"
    "s_add_u32 s78, s78, s41\n"
    "s_lshl_b32 s78, s78, 7\n"
    "s_add_u32 s64, s36, 0x220000\n"
    "s_addc_u32 s65, s37, 0\n"
    "s_add_u32 s64, s64, s78\n"
    "s_addc_u32 s65, s65, 0\n"
    "s_and_b32 s78, s11, 1\n"
    "s_lshl_b32 s78, s78, 2\n"
    "s_bfe_u32 s79, s11, 0x10001\n"
    "s_or_b32 s78, s78, s79\n"
    "s_xor_b32 s78, s78, s93\n"
    "s_xor_b32 s78, s78, 1\n"
    "s_lshl_b32 s78, s78, 12\n"
    "s_lshl_b32 s79, s42, 16\n"
    "s_add_u32 s79, s79, s78\n"
    "s_lshr_b32 s80, s39, 1\n"
    "s_lshl_b32 s80, s80, 15\n"
    "s_add_u32 s79, s79, s80\n"
    "s_and_b32 s80, s39, 1\n"
    "s_lshl_b32 s80, s80, 11\n"
    "s_add_u32 s79, s79, s80\n"
    "s_add_u32 s54, s36, 0x100000\n"
    "s_addc_u32 s55, s37, 0\n"
    "s_add_u32 s54, s54, s79\n"
    "s_addc_u32 s55, s55, 0\n"
    "s_mov_b64 s[48:49], s[54:55]\n"
    "s_lshl_b32 s79, s38, 6\n"
    "s_add_u32 s56, s12, s79\n"
    "s_addc_u32 s57, s13, 0\n"
    "s_xor_b32 s78, s78, 0x4000\n"
    "s_add_u32 s12, s12, s78\n"
    "s_addc_u32 s13, s13, 0\n"
    "v_and_b32 v8, 1, v0\n"
    "v_bfe_u32 v9, v0, 1, 1\n"
    "v_lshl_add_u32 v8, v8, 2, v9\n"
    "s_xor_b32 s78, s93, 5\n"
    "v_xor_b32 v8, s78, v8\n"
    "v_lshlrev_b32 v118, 12, v8\n"
    "v_lshlrev_b32 v119, 2, v71\n"
    "s_mov_b32 s43, 0\n"
    "v_mov_b64 v[104:105], 0\n"
    "v_mov_b64 v[106:107], 0\n"
    "v_mov_b64 v[108:109], 0\n"
    "v_mov_b64 v[110:111], 0\n"
    "v_mov_b64 v[32:33], 0\n"
    "v_mov_b64 v[34:35], 0\n"
    "v_mov_b64 v[36:37], 0\n"
    "v_mov_b64 v[38:39], 0\n"
    "v_mov_b64 v[40:41], 0\n"
    "v_mov_b64 v[42:43], 0\n"
    "v_mov_b64 v[44:45], 0\n"
    "v_mov_b64 v[46:47], 0\n"
    "v_mov_b64 v[48:49], 0\n"
    "v_mov_b64 v[50:51], 0\n"
    "v_mov_b64 v[52:53], 0\n"
    "v_mov_b64 v[54:55], 0\n"
    "s_cmp_lg_u32 s19, 0\n"
    "s_cbranch_scc1 L_kvdone%=\n"
    "s_add_u32 s79, s43, 1\n"
    "s_min_u32 s79, s79, 63\n"
    "s_lshr_b32 s79, s79, 1\n"
    "s_cmp_lt_u32 s79, s92\n"
    "s_cbranch_scc1 L_cntokp%=\n"
    "L_cntp%=:\n"
    "v_and_b32 v126, 16, v71\n"
    "v_lshlrev_b32 v126, 8, v126\n"
    "global_load_dwordx2 v[126:127], v126, s[66:67] sc1\n"
    "s_waitcnt vmcnt(0)\n"
    "v_xor_b32 v126, s96, v126\n"
    "v_xor_b32 v127, s97, v127\n"
    "v_cmp_gt_u64 vcc, 64, v[126:127]\n"
    "s_nop 1\n"
    "v_cndmask_b32 v126, 0, v126, vcc\n"
    "s_nop 0\n"
    "v_readlane_b32 s80, v126, 0\n"
    "v_readlane_b32 s81, v126, 1\n"
    "s_nop 1\n"
    "s_min_u32 s92, s80, s81\n"
    "s_cmp_lt_u32 s79, s92\n"
    "s_cbranch_scc1 L_cntokp%=\n"
    "s_sleep 2\n"
    "s_add_u32 s90, s90, 1\n"
    "s_and_b32 s78, s90, 0xff\n"
    "s_cmp_lg_u32 s78, 0\n"
    "s_cbranch_scc1 L_cntp%=\n"
    "v_mov_b32 v126, 0\n"
    "global_load_dwordx2 v[126:127], v126, s[68:69] sc1\n"
    "s_waitcnt vmcnt(0)\n"
    "v_cmp_eq_u64 vcc, s[96:97], v[126:127]\n"
    "s_nop 1\n"
    "s_cbranch_vccnz L_bail_cntp%=\n"
    "s_cmp_lt_u32 s90, 0x40000\n"
    "s_cbranch_scc1 L_cntp%=\n"
    "v_mov_b32 v126, s96\n"
    "v_mov_b32 v127, s97\n"
    "s_mov_b64 exec, 1\n"
    "global_store_dwordx2 v71, v[126:127], s[68:69] sc1\n"
    "s_mov_b64 exec, -1\n"
    "L_bail_cntp%=:\n"
    "L_cntokp%=:\n"
    "global_load_dwordx4 v[56:59], v71, s[58:59] offset:-2048 sc1\n"
    "global_load_dwordx4 v[60:63], v71, s[58:59] offset:2048 sc1\n"
    "global_load_dwordx2 v[64:65], v71, s[60:61] offset:0 sc1\n"
    "global_load_dwordx2 v[68:69], v71, s[60:61] offset:8 sc1\n"
    "global_load_dwordx2 v[72:73], v71, s[60:61] offset:1024 sc1\n"
    "global_load_dwordx2 v[76:77], v71, s[60:61] offset:1032 sc1\n"
    "L_kvdone%=:\n"
    "s_add_u32 s58, s58, 0x40000\n"
    "s_addc_u32 s59, s59, 0\n"
    "s_add_u32 s60, s60, 0x40000\n"
    "s_addc_u32 s61, s61, 0\n"
    "s_waitcnt lgkmcnt(0)\n"
    "s_memtime s[6:7]\n"
    "s_memrealtime s[78:79]\n"
    "s_nop 3\n"
    "s_memrealtime s[80:81]\n"
    "s_nop 3\n"
    "s_memrealtime s[82:83]\n"
    "s_nop 3\n"
    "s_memrealtime s[84:85]\n"
    "s_nop 3\n"
    "s_memrealtime s[86:87]\n"
    "s_nop 3\n"
    "s_memrealtime s[88:89]\n"
    "s_mov_b32 s10, 0\n"
    "s_waitcnt lgkmcnt(0)\n"
    "s_mul_i32 s91, s78, 24\n"
    "s_cmp_eq_u32 s78, s80\n"
    "s_addc_u32 s10, s10, 0\n"
    "s_cmp_eq_u32 s78, s82\n"
    "s_addc_u32 s10, s10, 0\n"
    "s_cmp_eq_u32 s78, s84\n"
    "s_addc_u32 s10, s10, 0\n"
    "s_cmp_eq_u32 s78, s86\n"
    "s_addc_u32 s10, s10, 0\n"
    "s_cmp_eq_u32 s78, s88\n"
    "s_addc_u32 s10, s10, 0\n"
    "s_mul_i32 s10, s10, 20\n"
    "s_sub_u32 s91, s91, s6\n"
    "s_sub_u32 s91, s91, s10\n"
    "s_mov_b32 s94, 90\n"
    "s_mov_b64 s[0:1], 0\n"
    "s_mov_b64 s[40:41], 0\n"
    "s_mov_b32 s51, 2060\n"
    "s_mov_b32 s15, 740\n"
    "s_mov_b32 s3, 1140\n"
    "s_mov_b32 s16, s6\n"
    "s_add_u32 s46, s91, 2400\n"
    "s_cmp_lt_u32 s43, 29\n"
    "s_cselect_b32 s79, 100, 0\n"
    "s_cselect_b32 s95, 20, 0\n"
    "s_add_u32 s95, s95, 1110\n"
    "s_add_u32 s46, s46, s79\n"
    "s_add_u32 s46, s46, s0\n"
    "s_mov_b32 s47, s91\n"
    "s_mov_b32 s18, s16\n"
    "s_getpc_b64 s[4:5]\n"
    "L_pc%=:\n"
    "s_add_u32 s4, s4, L_sled%=-L_pc%=\n"
    "s_addc_u32 s5, s5, 0\n"
    "v_or_b32 v84, 1, s96\n"
    "v_mov_b32 v85, s97\n"
    "v_mov_b32 v86, s43\n"
    "s_cmp_eq_u32 s39, 1\n"
    "s_cbranch_scc1 L_poll1%=\n"
    "s_cmp_eq_u32 s39, 2\n"
    "s_cbranch_scc1 L_poll2%=\n"
    "s_cmp_eq_u32 s39, 3\n"
    "s_cbranch_scc1 L_poll3%=\n"
    "s_branch L_poll0%=\n"
    ".p2align 6\n"
    "L_poll0%=:\n"
    "global_load_dwordx4 v[0:3], v71, s[54:55] offset:0 sc1\n"
    "global_load_dwordx4 v[4:7], v71, s[54:55] offset:1024 sc1\n"
    "s_waitcnt vmcnt(0)\n"
    "s_branch L_top0%=\n"
    ".p2align 6\n"
    "L_top0%=:\n"
    "s_waitcnt vmcnt(3)\n"
    "v_smfmac_f32_16x16x64_bf16 v[40:43], v[0:3], a[0:7], v67\n"
    "ds_write_b128 v71, v[0:3] offset:0\n"
    "v_smfmac_f32_16x16x64_bf16 v[44:47], v[0:3], a[64:71], v67\n"
    "v_smfmac_f32_16x16x64_bf16 v[32:35], v[0:3], v[128:135], v67\n"
    "v_smfmac_f32_16x16x64_bf16 v[36:39], v[0:3], v[136:143], v67\n"
    "s_waitcnt vmcnt(2)\n"
    "v_smfmac_f32_16x16x64_bf16 v[40:43], v[4:7], a[8:15], v67\n"
    "ds_write_b128 v71, v[4:7] offset:1024\n"
    "s_waitcnt lgkmcnt(1)\n"
    "s_barrier\n"
    "ds_read_b128 v[8:11], v71 offset:2048\n"
    "v_smfmac_f32_16x16x64_bf16 v[44:47], v[4:7], a[72:79], v67\n"
    "ds_read_b128 v[16:19], v71 offset:4096\n"
    "v_smfmac_f32_16x16x64_bf16 v[32:35], v[4:7], v[144:151], v67\n"
    "ds_read_b128 v[24:27], v71 offset:6144\n"
    "v_smfmac_f32_16x16x64_bf16 v[36:39], v[4:7], v[152:159], v67\n"
    "s_waitcnt lgkmcnt(3)\n"
    "s_barrier\n"
    "v_smfmac_f32_16x16x64_bf16 v[48:51], v[0:3], a[128:135], v67\n"
    "s_waitcnt lgkmcnt(2)\n"
    "ds_read_b128 v[12:15], v71 offset:3072\n"
    "v_smfmac_f32_16x16x64_bf16 v[32:35], v[8:11], v[160:167], v67\n"
    "ds_read_b128 v[20:23], v71 offset:5120\n"
    "v_smfmac_f32_16x16x64_bf16 v[36:39], v[8:11], v[168:175], v67\n"
    "s_waitcnt lgkmcnt(2)\n"
    "ds_read_b128 v[28:31], v71 offset:7168\n"
    "v_smfmac_f32_16x16x64_bf16 v[32:35], v[16:19], v[192:199], v67\n"
    "ds_read_b128 a[0:3], v116 offset:0\n"
    "v_smfmac_f32_16x16x64_bf16 v[36:39], v[16:19], v[200:207], v67\n"
    "ds_read_b128 a[4:7], v116 offset:1024\n"
    "v_smfmac_f32_16x16x64_bf16 v[32:35], v[24:27], v[224:231], v67\n"
    "ds_read_b128 a[64:67], v116 offset:4096\n"
    "v_smfmac_f32_16x16x64_bf16 v[36:39], v[24:27], v[232:239], v67\n"
    "s_waitcnt lgkmcnt(5)\n"
    "ds_read_b128 a[68:71], v116 offset:5120\n"
    "v_smfmac_f32_16x16x64_bf16 v[32:35], v[12:15], v[176:183], v67\n"
    "ds_read_b128 a[8:11], v116 offset:2048\n"
    "v_smfmac_f32_16x16x64_bf16 v[36:39], v[12:15], v[184:191], v67\n"
    "s_waitcnt lgkmcnt(5)\n"
    "ds_read_b128 a[12:15], v116 offset:3072\n"
    "v_smfmac_f32_16x16x64_bf16 v[32:35], v[20:23], v[208:215], v67\n"
    "ds_read_b128 a[72:75], v116 offset:6144\n"
    "v_smfmac_f32_16x16x64_bf16 v[36:39], v[20:23], v[216:223], v67\n"
    "ds_read_b128 a[76:79], v116 offset:7168\n"
    "v_smfmac_f32_16x16x64_bf16 v[32:35], v[28:31], v[240:247], v67\n"
    "v_smfmac_f32_16x16x64_bf16 v[36:39], v[28:31], v[248:255], v67\n"
    "v_smfmac_f32_16x16x64_bf16 v[40:43], v[8:11], a[16:23], v67\n"
    "v_add_f32 v32, v32, v34\n"
    "v_add_f32 v33, v33, v35\n"
    "v_smfmac_f32_16x16x64_bf16 v[44:47], v[8:11], a[80:87], v67\n"
    "ds_read_b128 a[16:19], v116 offset:8192\n"
    "v_cvt_pk_bf16_f32 v126, v32, v33\n"
    "v_add_f32 v36, v36, v38\n"
    "v_add_f32 v37, v37, v39\n"
    "v_cvt_pk_bf16_f32 v127, v36, v37\n"
    "v_smfmac_f32_16x16x64_bf16 v[40:43], v[16:19], a[32:39], v67\n"
    "ds_write_b64 v114, v[126:127] offset:8192\n"
    "v_smfmac_f32_16x16x64_bf16 v[44:47], v[16:19], a[96:103], v67\n"
    "ds_read_b128 a[20:23], v116 offset:9216\n"
    "v_cmp_u_f32 vcc, v32, v33\n"
    "v_smfmac_f32_16x16x64_bf16 v[40:43], v[24:27], a[48:55], v67\n"
    "ds_read_b128 a[80:83], v116 offset:12288\n"
    "v_smfmac_f32_16x16x64_bf16 v[44:47], v[24:27], a[112:119], v67\n"
    "s_cbranch_vccnz L_slow0%=\n"
    "L_go0%=:\n"
    "v_smfmac_f32_16x16x64_bf16 v[40:43], v[12:15], a[24:31], v67\n"
    "s_waitcnt lgkmcnt(2)\n"
    "s_barrier\n"
    "ds_read_b64_tr_b16 v[32:33], v112 offset:8192\n"
    "v_smfmac_f32_16x16x64_bf16 v[44:47], v[12:15], a[88:95], v67\n"
    "ds_read_b64_tr_b16 v[34:35], v113 offset:8192\n"
    "ds_read_b64_tr_b16 v[36:37], v112 offset:9216\n"
    "v_smfmac_f32_16x16x64_bf16 v[40:43], v[20:23], a[40:47], v67\n"
    "ds_read_b64_tr_b16 v[38:39], v113 offset:9216\n"
    "v_smfmac_f32_16x16x64_bf16 v[44:47], v[20:23], a[104:111], v67\n"
    "global_load_dwordx4 v[132:135], v71, s[60:61] offset:1024 sc1\n"
    "v_smfmac_f32_16x16x64_bf16 v[40:43], v[28:31], a[56:63], v67\n"
    "ds_read_b128 a[84:87], v116 offset:13312\n"
    "v_smfmac_f32_16x16x64_bf16 v[44:47], v[28:31], a[120:127], v67\n"
    "s_waitcnt lgkmcnt(1)\n"
    "ds_read_b128 a[32:35], v116 offset:10240\n"
    "v_mfma_f32_16x16x32_bf16 v[88:91], v[56:59], v[32:35], 0\n"
    "v_add_f32 v40, v40, v42\n"
    "v_add_f32 v41, v41, v43\n"
    "v_mfma_f32_16x16x32_bf16 v[88:91], v[60:63], v[36:39], v[88:91]\n"
    "v_cvt_pk_bf16_f32 v126, v40, v41\n"
    "v_add_f32 v44, v44, v46\n"
    "v_add_f32 v45, v45, v47\n"
    "v_cvt_pk_bf16_f32 v127, v44, v45\n"
    "v_smfmac_f32_16x16x64_bf16 v[52:55], v[0:3], a[192:199], v67\n"
    "ds_write_b64 v115, v[126:127] offset:10240\n"
    "v_add_f32_dpp v88, v90, v88 quad_perm:[2,3,2,3] row_mask:0xf bank_mask:0xf\n"
    "v_add_f32_dpp v89, v91, v89 quad_perm:[2,3,2,3] row_mask:0xf bank_mask:0xf\n"
    "v_smfmac_f32_16x16x64_bf16 v[48:51], v[4:7], a[136:143], v67\n"
    "ds_write_b64 v75, v[88:89] offset:14336\n"
    "v_smfmac_f32_16x16x64_bf16 v[52:55], v[4:7], a[200:207], v67\n"
    "global_load_dwordx4 v[56:59], v71, s[58:59] offset:-2048 sc1\n"
    "v_smfmac_f32_16x16x64_bf16 v[48:51], v[8:11], a[144:151], v67\n"
    "global_load_dwordx4 v[60:63], v71, s[58:59] offset:2048 sc1\n"
    "v_smfmac_f32_16x16x64_bf16 v[52:55], v[8:11], a[208:215], v67\n"
    "global_load_dwordx4 v[128:131], v71, s[60:61] offset:0 sc1\n"
    "v_smfmac_f32_16x16x64_bf16 v[48:51], v[16:19], a[160:167], v67\n"
    "s_waitcnt lgkmcnt(0)\n"
    "s_barrier\n"
    "ds_read_b64_tr_b16 v[40:41], v112 offset:10240\n"
    "v_smfmac_f32_16x16x64_bf16 v[52:55], v[16:19], a[224:231], v67\n"
    "ds_read_b64_tr_b16 v[42:43], v113 offset:10240\n"
    "ds_read_b64_tr_b16 v[44:45], v112 offset:11264\n"
    "v_smfmac_f32_16x16x64_bf16 v[48:51], v[24:27], a[176:183], v67\n"
    "ds_read_b64_tr_b16 v[46:47], v113 offset:11264\n"
    "v_smfmac_f32_16x16x64_bf16 v[52:55], v[24:27], a[240:247], v67\n"
    "ds_read_b128 v[88:91], v71 offset:14336\n"
    "s_cmp_eq_u32 s43, 63\n"
    "v_smfmac_f32_16x16x64_bf16 v[48:51], v[12:15], a[152:159], v67\n"
    "s_waitcnt lgkmcnt(3)\n"
    "ds_read_b128 a[36:39], v116 offset:11264\n"
    "v_mfma_f32_16x16x32_bf16 v[96:99], v[40:43], v[32:35], 0\n"
    "s_waitcnt lgkmcnt(2)\n"
    "ds_read_b128 a[96:99], v116 offset:14336\n"
    "v_mfma_f32_16x16x32_bf16 v[96:99], v[44:47], v[36:39], v[96:99]\n"
    "s_cbranch_scc1 L_last0%=\n"
    "v_smfmac_f32_16x16x64_bf16 v[52:55], v[12:15], a[216:223], v67\n"
    "s_waitcnt lgkmcnt(2)\n"
    "ds_read_b128 a[100:103], v116 offset:15360\n"
    "v_max3_f32 v122, v88, v89, v90\n"
    "v_max_f32 v122, v122, v91\n"
    "v_add_f32_dpp v96, v98, v96 quad_perm:[2,3,2,3] row_mask:0xf bank_mask:0xf\n"
    "v_add_f32_dpp v97, v99, v97 quad_perm:[2,3,2,3] row_mask:0xf bank_mask:0xf\n"
    "v_max_f32_dpp v123, v122, v122 quad_perm:[2,3,0,1] row_mask:0xf bank_mask:0xf\n"
    "v_max3_f32 v122, v123, v96, v97\n"
    "v_smfmac_f32_16x16x64_bf16 v[48:51], v[20:23], a[168:175], v67\n"
    "v_max_f32_dpp v121, v122, v123 quad_perm:[0,1,0,1] row_mask:0xf bank_mask:0xf\n"
    "v_max_f32_dpp v123, v122, v123 quad_perm:[0,1,0,1] row_mask:0xf bank_mask:0xf\n"
    "v_smfmac_f32_16x16x64_bf16 v[52:55], v[20:23], a[232:239], v67\n"
    "v_permlane16_swap_b32 v123, v121\n"
    "v_max_f32 v122, v123, v121\n"
    "v_max_f32 v123, v123, v121\n"
    "v_smfmac_f32_16x16x64_bf16 v[48:51], v[28:31], a[184:191], v67\n"
    "v_smfmac_f32_16x16x64_bf16 v[52:55], v[28:31], a[248:255], v67\n"
    "v_permlane32_swap_b32 v122, v123\n"
    "v_max_f32 v122, v122, v123\n"
    "v_mul_f32 v122, s76, v122\n"
    "v_pk_fma_f32 v[96:97], v[96:97], s[76:77], v[122:123] op_sel_hi:[1,1,0] neg_lo:[0,0,1] neg_hi:[0,0,1]\n"
    "v_pk_fma_f32 v[88:89], v[88:89], s[76:77], v[122:123] op_sel_hi:[1,1,0] neg_lo:[0,0,1] neg_hi:[0,0,1]\n"
    "v_pk_fma_f32 v[90:91], v[90:91], s[76:77], v[122:123] op_sel_hi:[1,1,0] neg_lo:[0,0,1] neg_hi:[0,0,1]\n"
    "v_exp_f32 v96, v96\n"
    "v_exp_f32 v97, v97\n"
    "v_exp_f32 v88, v88\n"
    "v_exp_f32 v89, v89\n"
    "v_exp_f32 v90, v90\n"
    "v_exp_f32 v91, v91\n"
    "v_pk_add_f32 v[48:49], v[48:49], v[50:51]\n"
    "v_pk_add_f32 v[52:53], v[52:53], v[54:55]\n"
    "v_cvt_pk_bf16_f32 v126, v96, v97\n"
    "v_cvt_pk_bf16_f32 v100, v88, v89\n"
    "v_cvt_pk_bf16_f32 v102, v90, v91\n"
    "v_cndmask_b32 v101, 0, v126, s[72:73]\n"
    "v_cndmask_b32 v103, 0, v126, s[74:75]\n"
    "v_cvt_pk_bf16_f32 v66, v48, v49\n"
    "v_cvt_pk_bf16_f32 v70, v48, v49\n"
    "v_cvt_pk_bf16_f32 v74, v52, v53\n"
    "v_cvt_pk_bf16_f32 v78, v52, v53\n"
    "v_smfmac_f32_16x16x64_bf16 v[104:107], v[100:103], v[64:71], v67\n"
    "v_smfmac_f32_16x16x64_bf16 v[108:111], v[100:103], v[72:79], v67\n"
    "v_mfma_f32_16x16x32_bf16 v[84:87], v[100:103], v[80:83], 0\n"
    "v_add_f32 v104, v104, v106\n"
    "v_add_f32 v105, v105, v107\n"
    "v_cvt_pk_bf16_f32 v104, v104, v105\n"
    "v_add_f32 v108, v108, v110\n"
    "v_add_f32 v109, v109, v111\n"
    "v_cvt_pk_bf16_f32 v105, v108, v109\n"
    "ds_write_b64 v114, v[104:105] offset:12288\n"
    "v_pk_add_f32 v[124:125], v[84:85], v[86:87]\n"
    "v_rcp_f32 v124, v124\n"
    "v_rcp_f32 v125, v125\n"
    "v_mov_b64 v[0:1], 0\n"
    "v_mov_b64 v[2:3], 0\n"
    "v_mov_b64 v[4:5], 0\n"
    "v_mov_b64 v[6:7], 0\n"
    "v_mov_b64 v[8:9], 0\n"
    "v_mov_b64 v[10:11], 0\n"
    "v_mov_b64 v[12:13], 0\n"
    "v_mov_b64 v[14:15], 0\n"
    "v_mfma_f32_16x16x32_bf16 v[96:99], v[100:103], v[80:83], 0\n"
    "s_waitcnt lgkmcnt(0)\n"
    "s_barrier\n"
    "ds_read_b64_tr_b16 v[32:33], v112 offset:12288\n"
    "v_mfma_f32_16x16x32_bf16 v[96:99], v[100:103], v[80:83], 0\n"
    "ds_read_b64_tr_b16 v[34:35], v113 offset:12288\n"
    "ds_read_b64_tr_b16 v[36:37], v112 offset:13312\n"
    "v_mfma_f32_16x16x32_bf16 v[96:99], v[100:103], v[80:83], 0\n"
    "ds_read_b64_tr_b16 v[38:39], v113 offset:13312\n"
    "v_mfma_f32_16x16x32_bf16 v[96:99], v[100:103], v[80:83], 0\n"
    "s_waitcnt lgkmcnt(2)\n"
    "v_smfmac_f32_16x16x64_bf16 v[0:3], v[32:35], a[0:7], v67\n"
    "v_smfmac_f32_16x16x64_bf16 v[4:7], v[32:35], a[64:71], v67\n"
    "s_waitcnt lgkmcnt(0)\n"
    "v_smfmac_f32_16x16x64_bf16 v[0:3], v[36:39], a[8:15], v67\n"
    "v_smfmac_f32_16x16x64_bf16 v[4:7], v[36:39], a[72:79], v67\n"
    "v_smfmac_f32_16x16x64_bf16 v[8:11], v[32:35], a[16:23], v67\n"
    "v_add_f32 v0, v0, v2\n"
    "v_smfmac_f32_16x16x64_bf16 v[12:15], v[32:35], a[80:87], v67\n"
    "v_add_f32 v1, v1, v3\n"
    "v_smfmac_f32_16x16x64_bf16 v[8:11], v[36:39], a[32:39], v67\n"
    "v_add_f32 v4, v4, v6\n"
    "v_smfmac_f32_16x16x64_bf16 v[12:15], v[36:39], a[96:103], v67\n"
    "v_add_f32 v5, v5, v7\n"
    "v_mul_f32 v0, v0, v124\n"
    "v_mul_f32 v1, v1, v125\n"
    "v_cvt_pk_bf16_f32 v88, v0, v1\n"
    "v_pk_mul_f32 v[4:5], v[4:5], v[124:125]\n"
    "v_pk_add_f32 v[8:9], v[8:9], v[10:11]\n"
    "v_pk_add_f32 v[12:13], v[12:13], v[14:15]\n"
    "v_cvt_pk_bf16_f32 v89, v4, v5\n"
    "v_pk_mul_f32 v[8:9], v[8:9], v[124:125]\n"
    "v_pk_mul_f32 v[12:13], v[12:13], v[124:125]\n"
    "v_cvt_pk_bf16_f32 v90, v8, v9\n"
    "v_cvt_pk_bf16_f32 v91, v12, v13\n"
    "global_store_dwordx4 v117, v[88:91], s[44:45] sc1\n"
    "s_add_u32 s43, s43, 1\n"
    "s_sleep 3\n"
    "ds_read_b128 a[0:3], v116 offset:16384\n"
    "ds_read_b128 a[4:7], v116 offset:17408\n"
    "ds_read_b128 a[64:67], v116 offset:18432\n"
    "ds_read_b128 a[68:71], v116 offset:19456\n"
    "ds_read_b128 a[8:11], v116 offset:20480\n"
    "ds_read_b128 a[12:15], v116 offset:21504\n"
    "ds_read_b128 a[72:75], v116 offset:22528\n"
    "ds_read_b128 a[76:79], v116 offset:23552\n"
    "ds_read_b128 a[16:19], v116 offset:24576\n"
    "ds_read_b128 a[20:23], v116 offset:25600\n"
    "ds_read_b128 a[80:83], v116 offset:26624\n"
    "ds_read_b128 a[84:87], v116 offset:27648\n"
    "ds_read_b128 a[32:35], v116 offset:28672\n"
    "s_waitcnt lgkmcnt(8)\n"
    "ds_read_b128 a[36:39], v116 offset:29696\n"
    "ds_read_b128 a[96:99], v116 offset:30720\n"
    "ds_read_b128 a[100:103], v116 offset:31744\n"
    "s_and_b32 s78, s43, 3\n"
    "s_lshl_b32 s78, s78, 17\n"
    "s_add_u32 s54, s48, s78\n"
    "s_addc_u32 s55, s49, 0\n"
    "s_add_u32 s62, s62, 0x20000\n"
    "s_addc_u32 s63, s63, 0\n"
    "v_mov_b64 v[32:33], 0\n"
    "v_mov_b64 v[34:35], 0\n"
    "v_mov_b64 v[36:37], 0\n"
    "v_mov_b64 v[38:39], 0\n"
    "v_mov_b64 v[48:49], 0\n"
    "v_mov_b64 v[50:51], 0\n"
    "v_mov_b64 v[52:53], 0\n"
    "v_mov_b64 v[54:55], 0\n"
    "v_mov_b64 v[104:105], 0\n"
    "v_mov_b64 v[106:107], 0\n"
    "v_mov_b64 v[108:109], 0\n"
    "v_mov_b64 v[110:111], 0\n"
    "s_xor_b32 s44, s44, 0x8000\n"
    "s_add_u32 s58, s58, 0x40000\n"
    "s_addc_u32 s59, s59, 0\n"
    "s_add_u32 s60, s60, 0x40000\n"
    "s_addc_u32 s61, s61, 0\n"
    "v_or_b32 v84, 1, s96\n"
    "v_mov_b32 v85, s97\n"
    "v_mov_b32 v86, s43\n"
    "s_waitcnt vmcnt(1)\n"
    "v_mov_b64 v[64:65], v[128:129]\n"
    "v_mov_b64 v[68:69], v[130:131]\n"
    "v_mov_b64 v[72:73], v[132:133]\n"
    "v_mov_b64 v[76:77], v[134:135]\n"
    "ds_read_b128 v[128:131], v116 offset:32768\n"
    "ds_read_b128 v[132:135], v116 offset:33792\n"
    "s_waitcnt lgkmcnt(0)\n"
    "s_barrier\n"
    "global_load_dwordx4 v[0:3], v71, s[54:55] offset:0 sc1\n"
    "s_nop 3\n"
    "global_load_dwordx4 v[4:7], v71, s[54:55] offset:1024 sc1\n"
    "s_nop 15\n"
    "s_cmp_lg_u32 s42, 0\n"
    "s_cbranch_scc1 L_vmb0%=\n"
    "s_mov_b32 exec_hi, 0\n"
    "global_store_dwordx4 v71, v[40:43], s[62:63] offset:0 sc1\n"
    "s_mov_b64 exec, -1\n"
    "s_branch L_kvb0%=\n"
    "L_vmb0%=:\n"
    "v_mov_b32 v126, v66\n"
    "v_mov_b32 v127, v74\n"
    "global_store_dwordx2 v79, v[126:127], s[62:63] offset:0 sc1\n"
    "L_kvb0%=:\n"
    "s_mov_b64 exec, 1\n"
    "global_store_dwordx2 v71, v[84:85], s[64:65] sc1\n"
    "s_mov_b64 exec, -1\n"
    "s_add_u32 s64, s64, 8192\n"
    "s_addc_u32 s65, s65, 0\n"
    "s_nop 1\n"
    "v_mov_b64 v[40:41], 0\n"
    "v_mov_b64 v[42:43], 0\n"
    "v_mov_b64 v[44:45], 0\n"
    "v_mov_b64 v[46:47], 0\n"
    "s_branch L_top0%=\n"
    "L_slow0%=:\n"
    "v_cmp_eq_u32 s[78:79], s50, v0\n"
    "v_cmp_eq_u32 s[80:81], s50, v1\n"
    "v_cmp_eq_u32 s[82:83], s50, v2\n"
    "v_cmp_eq_u32 s[84:85], s50, v3\n"
    "v_cmp_eq_u32 s[86:87], s50, v4\n"
    "v_cmp_eq_u32 s[88:89], s50, v5\n"
    "v_cmp_eq_u32 s[52:53], s50, v6\n"
    "v_cmp_eq_u32 vcc, s50, v7\n"
    "global_load_dwordx4 v[0:3], v71, s[54:55] offset:0 sc1\n"
    "global_load_dwordx4 v[4:7], v71, s[54:55] offset:1024 sc1\n"
    "s_waitcnt lgkmcnt(0)\n"
    "s_barrier\n"
    "s_or_b64 vcc, vcc, s[78:79]\n"
    "s_or_b64 vcc, vcc, s[80:81]\n"
    "s_or_b64 vcc, vcc, s[82:83]\n"
    "s_or_b64 vcc, vcc, s[84:85]\n"
    "s_or_b64 vcc, vcc, s[86:87]\n"
    "s_or_b64 vcc, vcc, s[88:89]\n"
    "s_or_b64 vcc, vcc, s[52:53]\n"
    "s_cbranch_scc1 L_redo0%=\n"
    "v_cmp_eq_u32 s[78:79], s50, v8\n"
    "v_cmp_eq_u32 s[80:81], s50, v9\n"
    "v_cmp_eq_u32 s[82:83], s50, v10\n"
    "v_cmp_eq_u32 s[84:85], s50, v11\n"
    "v_cmp_eq_u32 s[86:87], s50, v16\n"
    "v_cmp_eq_u32 s[88:89], s50, v17\n"
    "v_cmp_eq_u32 s[52:53], s50, v18\n"
    "v_cmp_eq_u32 vcc, s50, v19\n"
    "s_nop 0\n"
    "s_or_b64 vcc, vcc, s[78:79]\n"
    "s_or_b64 vcc, vcc, s[80:81]\n"
    "s_or_b64 vcc, vcc, s[82:83]\n"
    "s_or_b64 vcc, vcc, s[84:85]\n"
    "s_or_b64 vcc, vcc, s[86:87]\n"
    "s_or_b64 vcc, vcc, s[88:89]\n"
    "s_or_b64 vcc, vcc, s[52:53]\n"
    "s_cbranch_scc1 L_redo0%=\n"
    "v_cmp_eq_u32 s[78:79], s50, v24\n"
    "v_cmp_eq_u32 s[80:81], s50, v25\n"
    "v_cmp_eq_u32 s[82:83], s50, v26\n"
    "v_cmp_eq_u32 s[84:85], s50, v27\n"
    "v_cmp_eq_u32 s[86:87], s50, v12\n"
    "v_cmp_eq_u32 s[88:89], s50, v13\n"
    "v_cmp_eq_u32 s[52:53], s50, v14\n"
    "v_cmp_eq_u32 vcc, s50, v15\n"
    "s_nop 0\n"
    "s_or_b64 vcc, vcc, s[78:79]\n"
    "s_or_b64 vcc, vcc, s[80:81]\n"
    "s_or_b64 vcc, vcc, s[82:83]\n"
    "s_or_b64 vcc, vcc, s[84:85]\n"
    "s_or_b64 vcc, vcc, s[86:87]\n"
    "s_or_b64 vcc, vcc, s[88:89]\n"
    "s_or_b64 vcc, vcc, s[52:53]\n"
    "s_cbranch_scc1 L_redo0%=\n"
    "v_cmp_eq_u32 s[78:79], s50, v20\n"
    "v_cmp_eq_u32 s[80:81], s50, v21\n"
    "v_cmp_eq_u32 s[82:83], s50, v22\n"
    "v_cmp_eq_u32 s[84:85], s50, v23\n"
    "v_cmp_eq_u32 s[86:87], s50, v28\n"
    "v_cmp_eq_u32 s[88:89], s50, v29\n"
    "v_cmp_eq_u32 s[52:53], s50, v30\n"
    "v_cmp_eq_u32 vcc, s50, v31\n"
    "s_nop 0\n"
    "s_or_b64 vcc, vcc, s[78:79]\n"
    "s_or_b64 vcc, vcc, s[80:81]\n"
    "s_or_b64 vcc, vcc, s[82:83]\n"
    "s_or_b64 vcc, vcc, s[84:85]\n"
    "s_or_b64 vcc, vcc, s[86:87]\n"
    "s_or_b64 vcc, vcc, s[88:89]\n"
    "s_or_b64 vcc, vcc, s[52:53]\n"
    "s_cbranch_scc1 L_redo0%=\n"
    "s_waitcnt vmcnt(0)\n"
    "s_branch L_go0%=\n"
    "L_redo0%=:\n"
    "ds_read_b128 a[0:3], v116 offset:16384\n"
    "ds_read_b128 a[4:7], v116 offset:17408\n"
    "ds_read_b128 a[64:67], v116 offset:18432\n"
    "ds_read_b128 a[68:71], v116 offset:19456\n"
    "ds_read_b128 a[8:11], v116 offset:20480\n"
    "ds_read_b128 a[12:15], v116 offset:21504\n"
    "ds_read_b128 a[72:75], v116 offset:22528\n"
    "ds_read_b128 a[76:79], v116 offset:23552\n"
    "ds_read_b128 a[16:19], v116 offset:24576\n"
    "ds_read_b128 a[20:23], v116 offset:25600\n"
    "ds_read_b128 a[80:83], v116 offset:26624\n"
    "ds_read_b128 a[84:87], v116 offset:27648\n"
    "ds_read_b128 a[32:35], v116 offset:28672\n"
    "ds_read_b128 a[36:39], v116 offset:29696\n"
    "s_waitcnt lgkmcnt(8)\n"
    "ds_read_b128 a[96:99], v116 offset:30720\n"
    "ds_read_b128 a[100:103], v116 offset:31744\n"
    "v_mov_b64 v[32:33], 0\n"
    "v_mov_b64 v[34:35], 0\n"
    "v_mov_b64 v[36:37], 0\n"
    "v_mov_b64 v[38:39], 0\n"
    "v_mov_b64 v[40:41], 0\n"
    "v_mov_b64 v[42:43], 0\n"
    "v_mov_b64 v[44:45], 0\n"
    "v_mov_b64 v[46:47], 0\n"
    "v_mov_b64 v[48:49], 0\n"
    "v_mov_b64 v[50:51], 0\n"
    "v_mov_b64 v[52:53], 0\n"
    "v_mov_b64 v[54:55], 0\n"
    "s_add_u32 s90, s90, 1\n"
    "s_and_b32 s78, s90, 0xff\n"
    "s_cmp_lg_u32 s78, 0\n"
    "s_cbranch_scc1 L_re0%=\n"
    "v_mov_b32 v126, 0\n"
    "global_load_dwordx2 v[126:127], v126, s[68:69] sc1\n"
    "s_waitcnt vmcnt(0)\n"
    "v_cmp_eq_u64 vcc, s[96:97], v[126:127]\n"
    "s_nop 1\n"
    "s_cbranch_vccnz L_bail_g0%=\n"
    "s_cmp_lt_u32 s90, 0x40000\n"
    "s_cbranch_scc1 L_re0%=\n"
    "v_mov_b32 v126, s96\n"
    "v_mov_b32 v127, s97\n"
    "s_mov_b64 exec, 1\n"
    "global_store_dwordx2 v71, v[126:127], s[68:69] sc1\n"
    "s_mov_b64 exec, -1\n"
    "L_bail_g0%=:\n"
    "s_branch L_go0%=\n"
    "L_re0%=:\n"
    "s_waitcnt vmcnt(0) lgkmcnt(0)\n"
    "s_branch L_top0%=\n"
    "L_last0%=:\n"
    "v_smfmac_f32_16x16x64_bf16 v[52:55], v[12:15], a[216:223], v67\n"
    "v_smfmac_f32_16x16x64_bf16 v[48:51], v[20:23], a[168:175], v67\n"
    "v_smfmac_f32_16x16x64_bf16 v[52:55], v[20:23], a[232:239], v67\n"
    "v_smfmac_f32_16x16x64_bf16 v[48:51], v[28:31], a[184:191], v67\n"
    "v_smfmac_f32_16x16x64_bf16 v[52:55], v[28:31], a[248:255], v67\n"
    "s_nop 3\n"
    "v_pk_add_f32 v[48:49], v[48:49], v[50:51]\n"
    "s_nop 2\n"
    "v_pk_add_f32 v[52:53], v[52:53], v[54:55]\n"
    "v_cvt_pk_bf16_f32 v66, v48, v49\n"
    "v_cvt_pk_bf16_f32 v74, v52, v53\n"
    "s_add_u32 s62, s62, 0x20000\n"
    "s_addc_u32 s63, s63, 0\n"
    "s_cmp_lg_u32 s42, 0\n"
    "s_cbranch_scc1 L_vmc0%=\n"
    "s_mov_b32 exec_hi, 0\n"
    "global_store_dwordx4 v71, v[40:43], s[62:63] offset:0 sc1\n"
    "s_mov_b64 exec, -1\n"
    "s_branch L_kvc0%=\n"
    "L_vmc0%=:\n"
    "v_mov_b32 v126, v66\n"
    "v_mov_b32 v127, v74\n"
    "global_store_dwordx2 v79, v[126:127], s[62:63] offset:0 sc1\n"
    "L_kvc0%=:\n"
    "s_waitcnt vmcnt(0) lgkmcnt(0)\n"
    "s_barrier\n"
    "v_or_b32 v86, 1, s96\n"
    "v_mov_b32 v87, s97\n"
    "s_mov_b64 exec, 1\n"
    "global_store_dwordx2 v71, v[86:87], s[64:65] sc1\n"
    "s_add_u32 s64, s64, 8192\n"
    "s_addc_u32 s65, s65, 0\n"
    "global_store_dwordx2 v71, v[86:87], s[64:65] sc1\n"
    "s_waitcnt vmcnt(0)\n"
    "s_mov_b64 exec, -1\n"
    "s_endpgm\n"
    ".p2align 6\n"
    "L_poll1%=:\n"
    "global_load_dwordx4 v[8:11], v71, s[54:55] offset:0 sc1\n"
    "global_load_dwordx4 v[12:15], v71, s[54:55] offset:1024 sc1\n"
    "s_memtime s[16:17]\n"
    "s_mov_b32 s38, 0\n"
    "s_waitcnt lgkmcnt(0)\n"
    "s_add_u32 s14, s16, s46\n"
    "s_andn2_b32 s14, s14, 1\n"
    "s_or_b32 s14, s14, s38\n"
    "s_mov_b64 exec, 15\n"
    "v_mov_b32 v87, s14\n"
    "global_store_dwordx4 v118, v[84:87], s[56:57] sc1\n"
    "s_mov_b64 exec, -1\n"
    "s_waitcnt vmcnt(0)\n"
    "s_branch L_top1%=\n"
    ".p2align 6\n"
    "L_top1%=:\n"
    "s_waitcnt vmcnt(3)\n"
    "v_smfmac_f32_16x16x64_bf16 v[40:43], v[8:11], a[16:23], v67\n"
    "ds_write_b128 v71, v[8:11] offset:2048\n"
    "v_smfmac_f32_16x16x64_bf16 v[44:47], v[8:11], a[80:87], v67\n"
    "v_smfmac_f32_16x16x64_bf16 v[32:35], v[8:11], v[160:167], v67\n"
    "v_smfmac_f32_16x16x64_bf16 v[36:39], v[8:11], v[168:175], v67\n"
    "s_waitcnt vmcnt(2)\n"
    "v_smfmac_f32_16x16x64_bf16 v[40:43], v[12:15], a[24:31], v67\n"
    "ds_write_b128 v71, v[12:15] offset:3072\n"
    "s_waitcnt lgkmcnt(1)\n"
    "s_barrier\n"
    "ds_read_b128 v[0:3], v71 offset:0\n"
    "v_smfmac_f32_16x16x64_bf16 v[44:47], v[12:15], a[88:95], v67\n"
    "ds_read_b128 v[24:27], v71 offset:6144\n"
    "v_smfmac_f32_16x16x64_bf16 v[32:35], v[12:15], v[176:183], v67\n"
    "ds_read_b128 v[16:19], v71 offset:4096\n"
    "v_smfmac_f32_16x16x64_bf16 v[36:39], v[12:15], v[184:191], v67\n"
    "s_waitcnt lgkmcnt(3)\n"
    "s_barrier\n"
    "v_smfmac_f32_16x16x64_bf16 v[48:51], v[8:11], a[144:151], v67\n"
    "s_waitcnt lgkmcnt(2)\n"
    "ds_read_b128 v[4:7], v71 offset:1024\n"
    "v_smfmac_f32_16x16x64_bf16 v[32:35], v[0:3], v[128:135], v67\n"
    "ds_read_b128 v[28:31], v71 offset:7168\n"
    "v_smfmac_f32_16x16x64_bf16 v[36:39], v[0:3], v[136:143], v67\n"
    "s_waitcnt lgkmcnt(2)\n"
    "ds_read_b128 v[20:23], v71 offset:5120\n"
    "v_smfmac_f32_16x16x64_bf16 v[32:35], v[24:27], v[224:231], v67\n"
    "s_memtime s[6:7]\n"
    "ds_read_b128 a[16:19], v116 offset:0\n"
    "v_smfmac_f32_16x16x64_bf16 v[36:39], v[24:27], v[232:239], v67\n"
    "ds_read_b128 a[20:23], v116 offset:1024\n"
    "v_smfmac_f32_16x16x64_bf16 v[32:35], v[16:19], v[192:199], v67\n"
    "ds_read_b128 a[80:83], v116 offset:4096\n"
    "v_smfmac_f32_16x16x64_bf16 v[36:39], v[16:19], v[200:207], v67\n"
    "s_waitcnt lgkmcnt(5)\n"
    "ds_read_b128 a[84:87], v116 offset:5120\n"
    "v_smfmac_f32_16x16x64_bf16 v[32:35], v[4:7], v[144:151], v67\n"
    "s_sub_i32 s2, s6, s16\n"
    "ds_read_b128 a[24:27], v116 offset:2048\n"
    "v_smfmac_f32_16x16x64_bf16 v[36:39], v[4:7], v[152:159], v67\n"
    "s_waitcnt lgkmcnt(5)\n"
    "ds_read_b128 a[28:31], v116 offset:3072\n"
    "v_smfmac_f32_16x16x64_bf16 v[32:35], v[28:31], v[240:247], v67\n"
    "s_cmp_gt_i32 s2, s3\n"
    "ds_read_b128 a[88:91], v116 offset:6144\n"
    "v_smfmac_f32_16x16x64_bf16 v[36:39], v[28:31], v[248:255], v67\n"
    "ds_read_b128 a[92:95], v116 offset:7168\n"
    "v_smfmac_f32_16x16x64_bf16 v[32:35], v[20:23], v[208:215], v67\n"
    "s_cbranch_scc1 L_fix%=\n"
    "L_fixed%=:\n"
    "v_smfmac_f32_16x16x64_bf16 v[36:39], v[20:23], v[216:223], v67\n"
    "v_smfmac_f32_16x16x64_bf16 v[40:43], v[0:3], a[0:7], v67\n"
    "v_add_f32 v32, v32, v34\n"
    "v_add_f32 v33, v33, v35\n"
    "v_smfmac_f32_16x16x64_bf16 v[44:47], v[0:3], a[64:71], v67\n"
    "ds_read_b128 a[0:3], v116 offset:8192\n"
    "v_cvt_pk_bf16_f32 v126, v32, v33\n"
    "v_add_f32 v36, v36, v38\n"
    "v_add_f32 v37, v37, v39\n"
    "v_cvt_pk_bf16_f32 v127, v36, v37\n"
    "v_smfmac_f32_16x16x64_bf16 v[40:43], v[24:27], a[48:55], v67\n"
    "ds_write_b64 v114, v[126:127] offset:8704\n"
    "v_smfmac_f32_16x16x64_bf16 v[44:47], v[24:27], a[112:119], v67\n"
    "ds_read_b128 a[4:7], v116 offset:9216\n"
    "v_cmp_u_f32 vcc, v32, v33\n"
    "v_smfmac_f32_16x16x64_bf16 v[40:43], v[16:19], a[32:39], v67\n"
    "ds_read_b128 a[64:67], v116 offset:12288\n"
    "v_smfmac_f32_16x16x64_bf16 v[44:47], v[16:19], a[96:103], v67\n"
    "s_cbranch_vccnz L_slow1%=\n"
    "L_go1%=:\n"
    "v_smfmac_f32_16x16x64_bf16 v[40:43], v[4:7], a[8:15], v67\n"
    "s_waitcnt lgkmcnt(2)\n"
    "s_barrier\n"
    "ds_read_b64_tr_b16 v[32:33], v112 offset:8192\n"
    "v_smfmac_f32_16x16x64_bf16 v[44:47], v[4:7], a[72:79], v67\n"
    "ds_read_b64_tr_b16 v[34:35], v113 offset:8192\n"
    "ds_read_b64_tr_b16 v[36:37], v112 offset:9216\n"
    "v_smfmac_f32_16x16x64_bf16 v[40:43], v[28:31], a[56:63], v67\n"
    "ds_read_b64_tr_b16 v[38:39], v113 offset:9216\n"
    "v_smfmac_f32_16x16x64_bf16 v[44:47], v[28:31], a[120:127], v67\n"
    "global_load_dwordx4 v[164:167], v71, s[60:61] offset:1024 sc1\n"
    "v_smfmac_f32_16x16x64_bf16 v[40:43], v[20:23], a[40:47], v67\n"
    "ds_read_b128 a[68:71], v116 offset:13312\n"
    "v_smfmac_f32_16x16x64_bf16 v[44:47], v[20:23], a[104:111], v67\n"
    "s_waitcnt lgkmcnt(1)\n"
    "ds_read_b128 a[48:51], v116 offset:10240\n"
    "v_mfma_f32_16x16x32_bf16 v[88:91], v[56:59], v[32:35], 0\n"
    "v_add_f32 v40, v40, v42\n"
    "v_add_f32 v41, v41, v43\n"
    "v_mfma_f32_16x16x32_bf16 v[88:91], v[60:63], v[36:39], v[88:91]\n"
    "v_cvt_pk_bf16_f32 v126, v40, v41\n"
    "v_add_f32 v44, v44, v46\n"
    "v_add_f32 v45, v45, v47\n"
    "v_cvt_pk_bf16_f32 v127, v44, v45\n"
    "v_smfmac_f32_16x16x64_bf16 v[52:55], v[8:11], a[208:215], v67\n"
    "ds_write_b64 v115, v[126:127] offset:10752\n"
    "v_add_f32_dpp v88, v90, v88 quad_perm:[2,3,2,3] row_mask:0xf bank_mask:0xf\n"
    "v_add_f32_dpp v89, v91, v89 quad_perm:[2,3,2,3] row_mask:0xf bank_mask:0xf\n"
    "v_smfmac_f32_16x16x64_bf16 v[48:51], v[12:15], a[152:159], v67\n"
    "ds_write_b64 v75, v[88:89] offset:14368\n"
    "v_smfmac_f32_16x16x64_bf16 v[52:55], v[12:15], a[216:223], v67\n"
    "global_load_dwordx4 v[56:59], v71, s[58:59] offset:-2048 sc1\n"
    "v_smfmac_f32_16x16x64_bf16 v[48:51], v[0:3], a[128:135], v67\n"
    "global_load_dwordx4 v[60:63], v71, s[58:59] offset:2048 sc1\n"
    "v_smfmac_f32_16x16x64_bf16 v[52:55], v[0:3], a[192:199], v67\n"
    "global_load_dwordx4 v[160:163], v71, s[60:61] offset:0 sc1\n"
    "v_smfmac_f32_16x16x64_bf16 v[48:51], v[24:27], a[176:183], v67\n"
    "s_waitcnt lgkmcnt(0)\n"
    "s_barrier\n"
    "ds_read_b64_tr_b16 v[40:41], v112 offset:10240\n"
    "v_smfmac_f32_16x16x64_bf16 v[52:55], v[24:27], a[240:247], v67\n"
    "ds_read_b64_tr_b16 v[42:43], v113 offset:10240\n"
    "ds_read_b64_tr_b16 v[44:45], v112 offset:11264\n"
    "v_smfmac_f32_16x16x64_bf16 v[48:51], v[16:19], a[160:167], v67\n"
    "ds_read_b64_tr_b16 v[46:47], v113 offset:11264\n"
    "v_smfmac_f32_16x16x64_bf16 v[52:55], v[16:19], a[224:231], v67\n"
    "ds_read_b128 v[88:91], v71 offset:14336\n"
    "s_cmp_eq_u32 s43, 63\n"
    "v_smfmac_f32_16x16x64_bf16 v[48:51], v[4:7], a[136:143], v67\n"
    "s_waitcnt lgkmcnt(3)\n"
    "ds_read_b128 a[52:55], v116 offset:11264\n"
    "v_mfma_f32_16x16x32_bf16 v[96:99], v[40:43], v[32:35], 0\n"
    "s_waitcnt lgkmcnt(2)\n"
    "ds_read_b128 a[112:115], v116 offset:14336\n"
    "v_mfma_f32_16x16x32_bf16 v[96:99], v[44:47], v[36:39], v[96:99]\n"
    "s_cbranch_scc1 L_last1%=\n"
    "v_smfmac_f32_16x16x64_bf16 v[52:55], v[4:7], a[200:207], v67\n"
    "s_waitcnt lgkmcnt(2)\n"
    "ds_read_b128 a[116:119], v116 offset:15360\n"
    "v_max3_f32 v122, v88, v89, v90\n"
    "v_max_f32 v122, v122, v91\n"
    "v_add_f32_dpp v96, v98, v96 quad_perm:[2,3,2,3] row_mask:0xf bank_mask:0xf\n"
    "v_add_f32_dpp v97, v99, v97 quad_perm:[2,3,2,3] row_mask:0xf bank_mask:0xf\n"
    "v_max_f32_dpp v123, v122, v122 quad_perm:[2,3,0,1] row_mask:0xf bank_mask:0xf\n"
    "v_max3_f32 v122, v123, v96, v97\n"
    "v_smfmac_f32_16x16x64_bf16 v[48:51], v[28:31], a[184:191], v67\n"
    "v_max_f32_dpp v121, v122, v123 quad_perm:[0,1,0,1] row_mask:0xf bank_mask:0xf\n"
    "v_max_f32_dpp v123, v122, v123 quad_perm:[0,1,0,1] row_mask:0xf bank_mask:0xf\n"
    "v_smfmac_f32_16x16x64_bf16 v[52:55], v[28:31], a[248:255], v67\n"
    "v_permlane16_swap_b32 v123, v121\n"
    "v_max_f32 v122, v123, v121\n"
    "v_max_f32 v123, v123, v121\n"
    "v_smfmac_f32_16x16x64_bf16 v[48:51], v[20:23], a[168:175], v67\n"
    "v_smfmac_f32_16x16x64_bf16 v[52:55], v[20:23], a[232:239], v67\n"
    "v_permlane32_swap_b32 v122, v123\n"
    "v_max_f32 v122, v122, v123\n"
    "v_mul_f32 v122, s76, v122\n"
    "v_pk_fma_f32 v[96:97], v[96:97], s[76:77], v[122:123] op_sel_hi:[1,1,0] neg_lo:[0,0,1] neg_hi:[0,0,1]\n"
    "v_pk_fma_f32 v[88:89], v[88:89], s[76:77], v[122:123] op_sel_hi:[1,1,0] neg_lo:[0,0,1] neg_hi:[0,0,1]\n"
    "v_pk_fma_f32 v[90:91], v[90:91], s[76:77], v[122:123] op_sel_hi:[1,1,0] neg_lo:[0,0,1] neg_hi:[0,0,1]\n"
    "v_exp_f32 v96, v96\n"
    "v_exp_f32 v97, v97\n"
    "v_exp_f32 v88, v88\n"
    "v_exp_f32 v89, v89\n"
    "v_exp_f32 v90, v90\n"
    "v_exp_f32 v91, v91\n"
    "v_pk_add_f32 v[48:49], v[48:49], v[50:51]\n"
    "v_pk_add_f32 v[52:53], v[52:53], v[54:55]\n"
    "v_cvt_pk_bf16_f32 v126, v96, v97\n"
    "v_cvt_pk_bf16_f32 v100, v88, v89\n"
    "v_cvt_pk_bf16_f32 v102, v90, v91\n"
    "v_cndmask_b32 v101, 0, v126, s[72:73]\n"
    "v_cndmask_b32 v103, 0, v126, s[74:75]\n"
    "v_cvt_pk_bf16_f32 v66, v48, v49\n"
    "v_cvt_pk_bf16_f32 v70, v48, v49\n"
    "v_cvt_pk_bf16_f32 v74, v52, v53\n"
    "v_cvt_pk_bf16_f32 v78, v52, v53\n"
    "v_smfmac_f32_16x16x64_bf16 v[104:107], v[100:103], v[64:71], v67\n"
    "v_smfmac_f32_16x16x64_bf16 v[108:111], v[100:103], v[72:79], v67\n"
    "v_mfma_f32_16x16x32_bf16 v[84:87], v[100:103], v[80:83], 0\n"
    "v_add_f32 v104, v104, v106\n"
    "v_add_f32 v105, v105, v107\n"
    "v_cvt_pk_bf16_f32 v104, v104, v105\n"
    "v_add_f32 v108, v108, v110\n"
    "v_add_f32 v109, v109, v111\n"
    "v_cvt_pk_bf16_f32 v105, v108, v109\n"
    "ds_write_b64 v114, v[104:105] offset:12800\n"
    "global_load_dwordx2 v[120:121], v119, s[12:13] offset:12 sc1\n"
    "v_pk_add_f32 v[124:125], v[84:85], v[86:87]\n"
    "v_rcp_f32 v124, v124\n"
    "v_rcp_f32 v125, v125\n"
    "v_mov_b64 v[0:1], 0\n"
    "v_mov_b64 v[2:3], 0\n"
    "v_mov_b64 v[4:5], 0\n"
    "v_mov_b64 v[6:7], 0\n"
    "v_mov_b64 v[8:9], 0\n"
    "v_mov_b64 v[10:11], 0\n"
    "v_mov_b64 v[12:13], 0\n"
    "v_mov_b64 v[14:15], 0\n"
    "v_mfma_f32_16x16x32_bf16 v[96:99], v[100:103], v[80:83], 0\n"
    "s_waitcnt lgkmcnt(0)\n"
    "s_barrier\n"
    "ds_read_b64_tr_b16 v[32:33], v112 offset:12288\n"
    "v_mfma_f32_16x16x32_bf16 v[96:99], v[100:103], v[80:83], 0\n"
    "ds_read_b64_tr_b16 v[34:35], v113 offset:12288\n"
    "ds_read_b64_tr_b16 v[36:37], v112 offset:13312\n"
    "v_mfma_f32_16x16x32_bf16 v[96:99], v[100:103], v[80:83], 0\n"
    "ds_read_b64_tr_b16 v[38:39], v113 offset:13312\n"
    "v_mfma_f32_16x16x32_bf16 v[96:99], v[100:103], v[80:83], 0\n"
    "s_waitcnt lgkmcnt(2)\n"
    "v_smfmac_f32_16x16x64_bf16 v[0:3], v[32:35], a[16:23], v67\n"
    "v_smfmac_f32_16x16x64_bf16 v[4:7], v[32:35], a[80:87], v67\n"
    "s_waitcnt lgkmcnt(0)\n"
    "v_smfmac_f32_16x16x64_bf16 v[0:3], v[36:39], a[24:31], v67\n"
    "v_smfmac_f32_16x16x64_bf16 v[4:7], v[36:39], a[88:95], v67\n"
    "v_smfmac_f32_16x16x64_bf16 v[8:11], v[32:35], a[0:7], v67\n"
    "v_add_f32 v0, v0, v2\n"
    "v_smfmac_f32_16x16x64_bf16 v[12:15], v[32:35], a[64:71], v67\n"
    "v_add_f32 v1, v1, v3\n"
    "v_smfmac_f32_16x16x64_bf16 v[8:11], v[36:39], a[48:55], v67\n"
    "v_add_f32 v4, v4, v6\n"
    "v_smfmac_f32_16x16x64_bf16 v[12:15], v[36:39], a[112:119], v67\n"
    "v_add_f32 v5, v5, v7\n"
    "v_mul_f32 v0, v0, v124\n"
    "v_mul_f32 v1, v1, v125\n"
    "v_cvt_pk_bf16_f32 v88, v0, v1\n"
    "v_pk_mul_f32 v[4:5], v[4:5], v[124:125]\n"
    "v_pk_add_f32 v[8:9], v[8:9], v[10:11]\n"
    "v_pk_add_f32 v[12:13], v[12:13], v[14:15]\n"
    "v_cvt_pk_bf16_f32 v89, v4, v5\n"
    "v_pk_mul_f32 v[8:9], v[8:9], v[124:125]\n"
    "v_pk_mul_f32 v[12:13], v[12:13], v[124:125]\n"
    "v_cvt_pk_bf16_f32 v90, v8, v9\n"
    "v_cvt_pk_bf16_f32 v91, v12, v13\n"
    "global_store_dwordx4 v117, v[88:91], s[44:45] sc1\n"
    "s_memtime s[8:9]\n"
    "s_add_u32 s43, s43, 1\n"
    "ds_read_b128 a[16:19], v116 offset:16384\n"
    "ds_read_b128 a[20:23], v116 offset:17408\n"
    "ds_read_b128 a[80:83], v116 offset:18432\n"
    "ds_read_b128 a[84:87], v116 offset:19456\n"
    "ds_read_b128 a[24:27], v116 offset:20480\n"
    "ds_read_b128 a[28:31], v116 offset:21504\n"
    "ds_read_b128 a[88:91], v116 offset:22528\n"
    "ds_read_b128 a[92:95], v116 offset:23552\n"
    "ds_read_b128 a[0:3], v116 offset:24576\n"
    "ds_read_b128 a[4:7], v116 offset:25600\n"
    "ds_read_b128 a[64:67], v116 offset:26624\n"
    "ds_read_b128 a[68:71], v116 offset:27648\n"
    "ds_read_b128 a[48:51], v116 offset:28672\n"
    "s_waitcnt lgkmcnt(8)\n"
    "ds_read_b128 a[52:55], v116 offset:29696\n"
    "ds_read_b128 a[112:115], v116 offset:30720\n"
    "ds_read_b128 a[116:119], v116 offset:31744\n"
    "s_and_b32 s78, s43, 3\n"
    "s_lshl_b32 s78, s78, 17\n"
    "s_add_u32 s54, s48, s78\n"
    "s_addc_u32 s55, s49, 0\n"
    "s_add_u32 s62, s62, 0x20000\n"
    "s_addc_u32 s63, s63, 0\n"
    "v_mov_b64 v[32:33], 0\n"
    "v_mov_b64 v[34:35], 0\n"
    "v_mov_b64 v[36:37], 0\n"
    "v_mov_b64 v[38:39], 0\n"
    "v_mov_b64 v[48:49], 0\n"
    "v_mov_b64 v[50:51], 0\n"
    "v_mov_b64 v[52:53], 0\n"
    "v_mov_b64 v[54:55], 0\n"
    "v_mov_b64 v[104:105], 0\n"
    "v_mov_b64 v[106:107], 0\n"
    "v_mov_b64 v[108:109], 0\n"
    "v_mov_b64 v[110:111], 0\n"
    "s_xor_b32 s44, s44, 0x8000\n"
    "s_add_u32 s58, s58, 0x40000\n"
    "s_addc_u32 s59, s59, 0\n"
    "s_add_u32 s60, s60, 0x40000\n"
    "s_addc_u32 s61, s61, 0\n"
    "v_or_b32 v84, 1, s96\n"
    "v_mov_b32 v85, s97\n"
    "v_mov_b32 v86, s43\n"
    "s_waitcnt vmcnt(1)\n"
    "v_mov_b64 v[64:65], v[160:161]\n"
    "v_mov_b64 v[68:69], v[162:163]\n"
    "v_mov_b64 v[72:73], v[164:165]\n"
    "v_mov_b64 v[76:77], v[166:167]\n"
    "ds_read_b128 v[160:163], v116 offset:32768\n"
    "ds_read_b128 v[164:167], v116 offset:33792\n"
    "s_sub_u32 s19, s8, s16\n"
    "v_add_u32 v121, 1, v121\n"
    "v_cmp_eq_u32 s[86:87], s43, v121\n"
    "v_and_b32 v121, 1, v120\n"
    "v_cmp_eq_u32 s[88:89], 1, v121\n"
    "L_lk%=:\n"
    "v_subrev_u32 v120, s14, v120\n"
    "v_cmp_gt_u32 vcc, 0xe00, v120\n"
    "s_nop 1\n"
    "v_cndmask_b32 v120, 0, v120, vcc\n"
    "s_nop 1\n"
    "v_max_u32_dpp v120, v120, v120 row_ror:1 row_mask:0xf bank_mask:0xf\n"
    "s_nop 1\n"
    "v_max_u32_dpp v120, v120, v120 row_ror:2 row_mask:0xf bank_mask:0xf\n"
    "s_nop 1\n"
    "v_max_u32_dpp v120, v120, v120 row_ror:4 row_mask:0xf bank_mask:0xf\n"
    "s_nop 1\n"
    "v_max_u32_dpp v120, v120, v120 row_ror:8 row_mask:0xf bank_mask:0xf\n"
    "s_memtime s[6:7]\n"
    "v_readlane_b32 s78, v120, 0\n"
    "v_readlane_b32 s79, v120, 16\n"
    "v_readlane_b32 s80, v120, 32\n"
    "v_readlane_b32 s81, v120, 48\n"
    "s_add_u32 s9, s14, s95\n"
    "s_max_u32 s78, s78, s79\n"
    "s_max_u32 s80, s80, s81\n"
    "s_max_u32 s78, s78, s80\n"
    "s_add_u32 s9, s9, s78\n"
    "s_min_u32 s3, s78, 100\n"
    "s_add_u32 s3, s3, 30\n"
    "s_sub_u32 s8, s9, s91\n"
    "s_sub_u32 s8, s8, s94\n"
    "s_waitcnt lgkmcnt(0)\n"
    "s_sub_i32 s78, s8, s6\n"
    "s_cmp_gt_i32 s78, 1300\n"
    "s_cbranch_scc1 L_far%=\n"
    "L_near%=:\n"
    "s_cmp_lg_u64 s[86:87], 0\n"
    "s_cselect_b64 s[88:89], 0, s[88:89]\n"
    "s_lshl_b32 s79, s0, 1\n"
    "s_sub_u32 s79, s79, s40\n"
    "s_add_u32 s79, s79, 24\n"
    "s_min_u32 s79, s79, 700\n"
    "s_add_u32 s80, s0, 8\n"
    "s_cmp_eq_u32 s0, 0\n"
    "s_cselect_b32 s80, 0, s80\n"
    "s_sub_i32 s0, s0, 4\n"
    "s_max_i32 s0, s0, s40\n"
    "s_cmp_lg_u64 s[86:87], 0\n"
    "s_cselect_b32 s0, s79, s0\n"
    "s_cselect_b32 s40, s80, s40\n"
    "s_lshl_b32 s79, s1, 1\n"
    "s_sub_u32 s79, s79, s41\n"
    "s_add_u32 s79, s79, 24\n"
    "s_min_u32 s79, s79, 700\n"
    "s_add_u32 s80, s1, 8\n"
    "s_cmp_eq_u32 s1, 0\n"
    "s_cselect_b32 s80, 0, s80\n"
    "s_sub_i32 s1, s1, 4\n"
    "s_max_i32 s1, s1, s41\n"
    "s_cmp_lg_u64 s[88:89], 0\n"
    "s_cselect_b32 s1, s79, s1\n"
    "s_cselect_b32 s41, s80, s41\n"
    "s_mov_b32 s38, 0\n"
    "s_add_u32 s8, s8, s1\n"
    "s_add_u32 s9, s9, s1\n"
    "L_wa%=:\n"
    "s_memtime s[6:7]\n"
    "s_waitcnt lgkmcnt(0)\n"
    "s_sub_i32 s78, s8, s6\n"
    "s_sub_u32 s79, s78, 520\n"
    "s_cmp_lt_u32 s79, 0x1400\n"
    "s_cbranch_scc1 L_wa%=\n"
    "s_memtime s[6:7]\n"
    "s_memrealtime s[78:79]\n"
    "s_nop 3\n"
    "s_memrealtime s[80:81]\n"
    "s_nop 3\n"
    "s_memrealtime s[82:83]\n"
    "s_nop 3\n"
    "s_memrealtime s[84:85]\n"
    "s_nop 3\n"
    "s_memrealtime s[86:87]\n"
    "s_nop 3\n"
    "s_memrealtime s[88:89]\n"
    "s_mov_b32 s10, 0\n"
    "s_waitcnt lgkmcnt(0)\n"
    "s_mul_i32 s91, s78, 24\n"
    "s_cmp_eq_u32 s78, s80\n"
    "s_addc_u32 s10, s10, 0\n"
    "s_cmp_eq_u32 s78, s82\n"
    "s_addc_u32 s10, s10, 0\n"
    "s_cmp_eq_u32 s78, s84\n"
    "s_addc_u32 s10, s10, 0\n"
    "s_cmp_eq_u32 s78, s86\n"
    "s_addc_u32 s10, s10, 0\n"
    "s_cmp_eq_u32 s78, s88\n"
    "s_addc_u32 s10, s10, 0\n"
    "s_mul_i32 s10, s10, 20\n"
    "s_sub_u32 s91, s91, s6\n"
    "s_sub_u32 s91, s91, s10\n"
    "s_sub_u32 s8, s9, s91\n"
    "L_wb%=:\n"
    "s_memtime s[6:7]\n"
    "s_waitcnt lgkmcnt(0)\n"
    "s_sub_i32 s78, s8, s6\n"
    "s_sub_u32 s79, s78, 200\n"
    "s_cmp_lt_u32 s79, 0x1400\n"
    "s_cbranch_scc1 L_wb%=\n"
    "s_sub_i32 s78, s78, 84\n"
    "s_max_i32 s78, s78, 0\n"
    "s_min_i32 s78, s78, 124\n"
    "s_andn2_b32 s78, s78, 3\n"
    "s_sub_u32 s80, s4, s78\n"
    "s_subb_u32 s81, s5, 0\n"
    "s_setpc_b64 s[80:81]\n"
    "L_far%=:\n"
    "L_wc%=:\n"
    "s_memtime s[6:7]\n"
    "s_waitcnt lgkmcnt(0)\n"
    "s_sub_i32 s78, s8, s6\n"
    "s_sub_u32 s79, s78, 1300\n"
    "s_cmp_lt_u32 s79, 0x1400\n"
    "s_cbranch_scc1 L_wc%=\n"
    "global_load_dword v120, v119, s[12:13] offset:12 sc1\n"
    "s_cmp_gt_i32 s78, 1300\n"
    "s_waitcnt vmcnt(0)\n"
    "s_cbranch_scc0 L_lk%=\n"
    "s_branch L_near%=\n"
    ".p2align 6\n"
    "s_nop 0\n"
    "s_nop 0\n"
    "s_nop 0\n"
    "s_nop 0\n"
    "s_nop 0\n"
    "s_nop 0\n"
    "s_nop 0\n"
    "s_nop 0\n"
    "s_nop 0\n"
    "s_nop 0\n"
    "s_nop 0\n"
    "s_nop 0\n"
    "s_nop 0\n"
    "s_nop 0\n"
    "s_nop 0\n"
    "s_nop 0\n"
    "s_nop 0\n"
    "s_nop 0\n"
    "s_nop 0\n"
    "s_nop 0\n"
    "s_nop 0\n"
    "s_nop 0\n"
    "s_nop 0\n"
    "s_nop 0\n"
    "s_nop 0\n"
    "s_nop 0\n"
    "s_nop 0\n"
    "s_nop 0\n"
    "s_nop 0\n"
    "s_nop 0\n"
    "s_nop 0\n"
    "s_nop 0\n"
    "L_sled%=:\n"
    "s_barrier\n"
    "global_load_dwordx4 v[8:11], v71, s[54:55] offset:0 sc1\n"
    "s_nop 3\n"
    "global_load_dwordx4 v[12:15], v71, s[54:55] offset:1024 sc1\n"
    "s_memtime s[16:17]\n"
    "s_nop 15\n"
    "s_cmp_lg_u32 s42, 0\n"
    "s_cbranch_scc1 L_vmb1%=\n"
    "s_mov_b32 exec_lo, 0\n"
    "global_store_dwordx4 v71, v[40:43], s[62:63] offset:0 sc1\n"
    "s_mov_b64 exec, -1\n"
    "s_branch L_kvb1%=\n"
    "L_vmb1%=:\n"
    "v_mov_b32 v126, v66\n"
    "v_mov_b32 v127, v74\n"
    "global_store_dwordx2 v79, v[126:127], s[62:63] offset:512 sc1\n"
    "L_kvb1%=:\n"
    "s_nop 1\n"
    "v_mov_b64 v[40:41], 0\n"
    "v_mov_b64 v[42:43], 0\n"
    "v_mov_b64 v[44:45], 0\n"
    "v_mov_b64 v[46:47], 0\n"
    "s_add_u32 s12, s12, 0x10000\n"
    "s_andn2_b32 s12, s12, 0x40000\n"
    "s_add_u32 s56, s56, 0x10000\n"
    "s_andn2_b32 s56, s56, 0x40000\n"
    "s_waitcnt lgkmcnt(0)\n"
    "s_sub_i32 s78, s91, s47\n"
    "s_sub_u32 s79, s16, s18\n"
    "v_cvt_f32_u32 v127, s79\n"
    "v_cvt_f32_i32 v126, s78\n"
    "v_rcp_f32 v127, v127\n"
    "v_mul_f32 v126, 0x450fc000, v126\n"
    "v_mul_f32 v126, v126, v127\n"
    "v_cvt_i32_f32 v126, v126\n"
    "s_nop 0\n"
    "v_readfirstlane_b32 s78, v126\n"
    "s_sub_i32 s19, s19, s51\n"
    "s_max_i32 s19, s19, -64\n"
    "s_min_i32 s19, s19, 64\n"
    "s_ashr_i32 s19, s19, 1\n"
    "s_add_i32 s51, s51, s19\n"
    "s_sub_i32 s2, s2, s15\n"
    "s_max_i32 s2, s2, -64\n"
    "s_min_i32 s2, s2, 64\n"
    "s_ashr_i32 s2, s2, 1\n"
    "s_add_i32 s15, s15, s2\n"
    "s_add_u32 s3, s3, s15\n"
    "s_max_i32 s78, s78, -200\n"
    "s_min_i32 s78, s78, 900\n"
    "s_add_i32 s78, s78, s94\n"
    "s_ashr_i32 s94, s78, 1\n"
    "s_add_u32 s46, s91, s94\n"
    "s_add_u32 s46, s46, s51\n"
    "s_cmp_lt_u32 s43, 29\n"
    "s_cselect_b32 s79, 100, 0\n"
    "s_cselect_b32 s95, 20, 0\n"
    "s_add_u32 s95, s95, 1110\n"
    "s_add_u32 s46, s46, s79\n"
    "s_add_u32 s46, s46, s0\n"
    "s_mov_b32 s47, s91\n"
    "s_mov_b32 s18, s16\n"
    "s_add_u32 s14, s16, s46\n"
    "s_andn2_b32 s14, s14, 1\n"
    "s_or_b32 s14, s14, s38\n"
    "s_mov_b64 exec, 15\n"
    "v_mov_b32 v87, s14\n"
    "global_store_dwordx4 v118, v[84:87], s[56:57] sc1\n"
    "s_mov_b64 exec, -1\n"
    "s_branch L_top1%=\n"
    "L_fix%=:\n"
    "s_sub_u32 s16, s6, s15\n"
    "s_add_u32 s16, s16, 60\n"
    "s_add_u32 s14, s16, s46\n"
    "s_andn2_b32 s14, s14, 1\n"
    "s_or_b32 s14, s14, s38\n"
    "s_mov_b64 exec, 15\n"
    "v_mov_b32 v87, s14\n"
    "global_store_dwordx4 v118, v[84:87], s[56:57] sc1\n"
    "s_mov_b64 exec, -1\n"
    "s_branch L_fixed%=\n"
    "L_slow1%=:\n"
    "v_cmp_eq_u32 s[78:79], s50, v8\n"
    "v_cmp_eq_u32 s[80:81], s50, v9\n"
    "v_cmp_eq_u32 s[82:83], s50, v10\n"
    "v_cmp_eq_u32 s[84:85], s50, v11\n"
    "v_cmp_eq_u32 s[86:87], s50, v12\n"
    "v_cmp_eq_u32 s[88:89], s50, v13\n"
    "v_cmp_eq_u32 s[52:53], s50, v14\n"
    "v_cmp_eq_u32 vcc, s50, v15\n"
    "global_load_dwordx4 v[8:11], v71, s[54:55] offset:0 sc1\n"
    "global_load_dwordx4 v[12:15], v71, s[54:55] offset:1024 sc1\n"
    "s_memtime s[16:17]\n"
    "s_waitcnt lgkmcnt(0)\n"
    "s_barrier\n"
    "s_add_u32 s16, s16, 400\n"
    "s_mov_b32 s38, 1\n"
    "s_add_u32 s14, s16, s46\n"
    "s_andn2_b32 s14, s14, 1\n"
    "s_or_b32 s14, s14, s38\n"
    "s_mov_b64 exec, 15\n"
    "v_mov_b32 v87, s14\n"
    "global_store_dwordx4 v118, v[84:87], s[56:57] sc1\n"
    "s_mov_b64 exec, -1\n"
    "s_or_b64 vcc, vcc, s[78:79]\n"
    "s_or_b64 vcc, vcc, s[80:81]\n"
    "s_or_b64 vcc, vcc, s[82:83]\n"
    "s_or_b64 vcc, vcc, s[84:85]\n"
    "s_or_b64 vcc, vcc, s[86:87]\n"
    "s_or_b64 vcc, vcc, s[88:89]\n"
    "s_or_b64 vcc, vcc, s[52:53]\n"
    "s_cbranch_scc1 L_redo1%=\n"
    "v_cmp_eq_u32 s[78:79], s50, v0\n"
    "v_cmp_eq_u32 s[80:81], s50, v1\n"
    "v_cmp_eq_u32 s[82:83], s50, v2\n"
    "v_cmp_eq_u32 s[84:85], s50, v3\n"
    "v_cmp_eq_u32 s[86:87], s50, v24\n"
    "v_cmp_eq_u32 s[88:89], s50, v25\n"
    "v_cmp_eq_u32 s[52:53], s50, v26\n"
    "v_cmp_eq_u32 vcc, s50, v27\n"
    "s_nop 0\n"
    "s_or_b64 vcc, vcc, s[78:79]\n"
    "s_or_b64 vcc, vcc, s[80:81]\n"
    "s_or_b64 vcc, vcc, s[82:83]\n"
    "s_or_b64 vcc, vcc, s[84:85]\n"
    "s_or_b64 vcc, vcc, s[86:87]\n"
    "s_or_b64 vcc, vcc, s[88:89]\n"
    "s_or_b64 vcc, vcc, s[52:53]\n"
    "s_cbranch_scc1 L_redo1%=\n"
    "v_cmp_eq_u32 s[78:79], s50, v16\n"
    "v_cmp_eq_u32 s[80:81], s50, v17\n"
    "v_cmp_eq_u32 s[82:83], s50, v18\n"
    "v_cmp_eq_u32 s[84:85], s50, v19\n"
    "v_cmp_eq_u32 s[86:87], s50, v4\n"
    "v_cmp_eq_u32 s[88:89], s50, v5\n"
    "v_cmp_eq_u32 s[52:53], s50, v6\n"
    "v_cmp_eq_u32 vcc, s50, v7\n"
    "s_nop 0\n"
    "s_or_b64 vcc, vcc, s[78:79]\n"
    "s_or_b64 vcc, vcc, s[80:81]\n"
    "s_or_b64 vcc, vcc, s[82:83]\n"
    "s_or_b64 vcc, vcc, s[84:85]\n"
    "s_or_b64 vcc, vcc, s[86:87]\n"
    "s_or_b64 vcc, vcc, s[88:89]\n"
    "s_or_b64 vcc, vcc, s[52:53]\n"
    "s_cbranch_scc1 L_redo1%=\n"
    "v_cmp_eq_u32 s[78:79], s50, v28\n"
    "v_cmp_eq_u32 s[80:81], s50, v29\n"
    "v_cmp_eq_u32 s[82:83], s50, v30\n"
    "v_cmp_eq_u32 s[84:85], s50, v31\n"
    "v_cmp_eq_u32 s[86:87], s50, v20\n"
    "v_cmp_eq_u32 s[88:89], s50, v21\n"
    "v_cmp_eq_u32 s[52:53], s50, v22\n"
    "v_cmp_eq_u32 vcc, s50, v23\n"
    "s_nop 0\n"
    "s_or_b64 vcc, vcc, s[78:79]\n"
    "s_or_b64 vcc, vcc, s[80:81]\n"
    "s_or_b64 vcc, vcc, s[82:83]\n"
    "s_or_b64 vcc, vcc, s[84:85]\n"
    "s_or_b64 vcc, vcc, s[86:87]\n"
    "s_or_b64 vcc, vcc, s[88:89]\n"
    "s_or_b64 vcc, vcc, s[52:53]\n"
    "s_cbranch_scc1 L_redo1%=\n"
    "s_waitcnt vmcnt(0)\n"
    "s_branch L_go1%=\n"
    "L_redo1%=:\n"
    "ds_read_b128 a[16:19], v116 offset:16384\n"
    "ds_read_b128 a[20:23], v116 offset:17408\n"
    "ds_read_b128 a[80:83], v116 offset:18432\n"
    "ds_read_b128 a[84:87], v116 offset:19456\n"
    "ds_read_b128 a[24:27], v116 offset:20480\n"
    "ds_read_b128 a[28:31], v116 offset:21504\n"
    "ds_read_b128 a[88:91], v116 offset:22528\n"
    "ds_read_b128 a[92:95], v116 offset:23552\n"
    "ds_read_b128 a[0:3], v116 offset:24576\n"
    "ds_read_b128 a[4:7], v116 offset:25600\n"
    "ds_read_b128 a[64:67], v116 offset:26624\n"
    "ds_read_b128 a[68:71], v116 offset:27648\n"
    "ds_read_b128 a[48:51], v116 offset:28672\n"
    "ds_read_b128 a[52:55], v116 offset:29696\n"
    "s_waitcnt lgkmcnt(8)\n"
    "ds_read_b128 a[112:115], v116 offset:30720\n"
    "ds_read_b128 a[116:119], v116 offset:31744\n"
    "v_mov_b64 v[32:33], 0\n"
    "v_mov_b64 v[34:35], 0\n"
    "v_mov_b64 v[36:37], 0\n"
    "v_mov_b64 v[38:39], 0\n"
    "v_mov_b64 v[40:41], 0\n"
    "v_mov_b64 v[42:43], 0\n"
    "v_mov_b64 v[44:45], 0\n"
    "v_mov_b64 v[46:47], 0\n"
    "v_mov_b64 v[48:49], 0\n"
    "v_mov_b64 v[50:51], 0\n"
    "v_mov_b64 v[52:53], 0\n"
    "v_mov_b64 v[54:55], 0\n"
    "s_add_u32 s90, s90, 1\n"
    "s_and_b32 s78, s90, 0xff\n"
    "s_cmp_lg_u32 s78, 0\n"
    "s_cbranch_scc1 L_re1%=\n"
    "v_mov_b32 v126, 0\n"
    "global_load_dwordx2 v[126:127], v126, s[68:69] sc1\n"
    "s_waitcnt vmcnt(0)\n"
    "v_cmp_eq_u64 vcc, s[96:97], v[126:127]\n"
    "s_nop 1\n"
    "s_cbranch_vccnz L_bail_g1%=\n"
    "s_cmp_lt_u32 s90, 0x40000\n"
    "s_cbranch_scc1 L_re1%=\n"
    "v_mov_b32 v126, s96\n"
    "v_mov_b32 v127, s97\n"
    "s_mov_b64 exec, 1\n"
    "global_store_dwordx2 v71, v[126:127], s[68:69] sc1\n"
    "s_mov_b64 exec, -1\n"
    "L_bail_g1%=:\n"
    "s_branch L_go1%=\n"
    "L_re1%=:\n"
    "s_waitcnt vmcnt(1) lgkmcnt(0)\n"
    "s_branch L_top1%=\n"
    "L_last1%=:\n"
    "v_smfmac_f32_16x16x64_bf16 v[52:55], v[4:7], a[200:207], v67\n"
    "v_smfmac_f32_16x16x64_bf16 v[48:51], v[28:31], a[184:191], v67\n"
    "v_smfmac_f32_16x16x64_bf16 v[52:55], v[28:31], a[248:255], v67\n"
    "v_smfmac_f32_16x16x64_bf16 v[48:51], v[20:23], a[168:175], v67\n"
    "v_smfmac_f32_16x16x64_bf16 v[52:55], v[20:23], a[232:239], v67\n"
    "s_nop 3\n"
    "v_pk_add_f32 v[48:49], v[48:49], v[50:51]\n"
    "s_nop 2\n"
    "v_pk_add_f32 v[52:53], v[52:53], v[54:55]\n"
    "v_cvt_pk_bf16_f32 v66, v48, v49\n"
    "v_cvt_pk_bf16_f32 v74, v52, v53\n"
    "s_add_u32 s62, s62, 0x20000\n"
    "s_addc_u32 s63, s63, 0\n"
    "s_cmp_lg_u32 s42, 0\n"
    "s_cbranch_scc1 L_vmc1%=\n"
    "s_mov_b32 exec_lo, 0\n"
    "global_store_dwordx4 v71, v[40:43], s[62:63] offset:0 sc1\n"
    "s_mov_b64 exec, -1\n"
    "s_branch L_kvc1%=\n"
    "L_vmc1%=:\n"
    "v_mov_b32 v126, v66\n"
    "v_mov_b32 v127, v74\n"
    "global_store_dwordx2 v79, v[126:127], s[62:63] offset:512 sc1\n"
    "L_kvc1%=:\n"
    "s_waitcnt vmcnt(0) lgkmcnt(0)\n"
    "s_barrier\n"
    "s_endpgm\n"
    ".p2align 6\n"
    "L_poll2%=:\n"
    "global_load_dwordx4 v[16:19], v71, s[54:55] offset:0 sc1\n"
    "global_load_dwordx4 v[20:23], v71, s[54:55] offset:1024 sc1\n"
    "s_waitcnt vmcnt(0)\n"
    "s_branch L_top2%=\n"
    ".p2align 6\n"
    "L_top2%=:\n"
    "s_waitcnt vmcnt(2)\n"
    "v_smfmac_f32_16x16x64_bf16 v[40:43], v[16:19], a[32:39], v67\n"
    "ds_write_b128 v71, v[16:19] offset:4096\n"
    "v_smfmac_f32_16x16x64_bf16 v[44:47], v[16:19], a[96:103], v67\n"
    "v_smfmac_f32_16x16x64_bf16 v[32:35], v[16:19], v[192:199], v67\n"
    "v_smfmac_f32_16x16x64_bf16 v[36:39], v[16:19], v[200:207], v67\n"
    "s_waitcnt vmcnt(1)\n"
    "v_smfmac_f32_16x16x64_bf16 v[40:43], v[20:23], a[40:47], v67\n"
    "ds_write_b128 v71, v[20:23] offset:5120\n"
    "s_waitcnt lgkmcnt(1)\n"
    "s_barrier\n"
    "ds_read_b128 v[24:27], v71 offset:6144\n"
    "v_smfmac_f32_16x16x64_bf16 v[44:47], v[20:23], a[104:111], v67\n"
    "ds_read_b128 v[0:3], v71 offset:0\n"
    "v_smfmac_f32_16x16x64_bf16 v[32:35], v[20:23], v[208:215], v67\n"
    "ds_read_b128 v[8:11], v71 offset:2048\n"
    "v_smfmac_f32_16x16x64_bf16 v[36:39], v[20:23], v[216:223], v67\n"
    "s_waitcnt lgkmcnt(3)\n"
    "s_barrier\n"
    "v_smfmac_f32_16x16x64_bf16 v[48:51], v[16:19], a[160:167], v67\n"
    "s_waitcnt lgkmcnt(2)\n"
    "ds_read_b128 v[28:31], v71 offset:7168\n"
    "v_smfmac_f32_16x16x64_bf16 v[32:35], v[24:27], v[224:231], v67\n"
    "ds_read_b128 v[4:7], v71 offset:1024\n"
    "v_smfmac_f32_16x16x64_bf16 v[36:39], v[24:27], v[232:239], v67\n"
    "s_waitcnt lgkmcnt(2)\n"
    "ds_read_b128 v[12:15], v71 offset:3072\n"
    "v_smfmac_f32_16x16x64_bf16 v[32:35], v[0:3], v[128:135], v67\n"
    "ds_read_b128 a[32:35], v116 offset:0\n"
    "v_smfmac_f32_16x16x64_bf16 v[36:39], v[0:3], v[136:143], v67\n"
    "ds_read_b128 a[36:39], v116 offset:1024\n"
    "v_smfmac_f32_16x16x64_bf16 v[32:35], v[8:11], v[160:167], v67\n"
    "ds_read_b128 a[96:99], v116 offset:4096\n"
    "v_smfmac_f32_16x16x64_bf16 v[36:39], v[8:11], v[168:175], v67\n"
    "s_waitcnt lgkmcnt(5)\n"
    "ds_read_b128 a[100:103], v116 offset:5120\n"
    "v_smfmac_f32_16x16x64_bf16 v[32:35], v[28:31], v[240:247], v67\n"
    "ds_read_b128 a[40:43], v116 offset:2048\n"
    "v_smfmac_f32_16x16x64_bf16 v[36:39], v[28:31], v[248:255], v67\n"
    "s_waitcnt lgkmcnt(5)\n"
    "ds_read_b128 a[44:47], v116 offset:3072\n"
    "v_smfmac_f32_16x16x64_bf16 v[32:35], v[4:7], v[144:151], v67\n"
    "ds_read_b128 a[104:107], v116 offset:6144\n"
    "v_smfmac_f32_16x16x64_bf16 v[36:39], v[4:7], v[152:159], v67\n"
    "ds_read_b128 a[108:111], v116 offset:7168\n"
    "v_smfmac_f32_16x16x64_bf16 v[32:35], v[12:15], v[176:183], v67\n"
    "v_smfmac_f32_16x16x64_bf16 v[36:39], v[12:15], v[184:191], v67\n"
    "v_smfmac_f32_16x16x64_bf16 v[40:43], v[24:27], a[48:55], v67\n"
    "v_add_f32 v32, v32, v34\n"
    "v_add_f32 v33, v33, v35\n"
    "v_smfmac_f32_16x16x64_bf16 v[44:47], v[24:27], a[112:119], v67\n"
    "ds_read_b128 a[48:51], v116 offset:8192\n"
    "v_cvt_pk_bf16_f32 v126, v32, v33\n"
    "v_add_f32 v36, v36, v38\n"
    "v_add_f32 v37, v37, v39\n"
    "v_cvt_pk_bf16_f32 v127, v36, v37\n"
    "v_smfmac_f32_16x16x64_bf16 v[40:43], v[0:3], a[0:7], v67\n"
    "ds_write_b64 v114, v[126:127] offset:9216\n"
    "v_smfmac_f32_16x16x64_bf16 v[44:47], v[0:3], a[64:71], v67\n"
    "ds_read_b128 a[52:55], v116 offset:9216\n"
    "v_cmp_u_f32 vcc, v32, v33\n"
    "v_smfmac_f32_16x16x64_bf16 v[40:43], v[8:11], a[16:23], v67\n"
    "ds_read_b128 a[112:115], v116 offset:12288\n"
    "v_smfmac_f32_16x16x64_bf16 v[44:47], v[8:11], a[80:87], v67\n"
    "s_cbranch_vccnz L_slow2%=\n"
    "L_go2%=:\n"
    "v_smfmac_f32_16x16x64_bf16 v[40:43], v[28:31], a[56:63], v67\n"
    "s_waitcnt lgkmcnt(2)\n"
    "s_barrier\n"
    "ds_read_b64_tr_b16 v[32:33], v112 offset:8192\n"
    "v_smfmac_f32_16x16x64_bf16 v[44:47], v[28:31], a[120:127], v67\n"
    "ds_read_b64_tr_b16 v[34:35], v113 offset:8192\n"
    "ds_read_b64_tr_b16 v[36:37], v112 offset:9216\n"
    "v_smfmac_f32_16x16x64_bf16 v[40:43], v[4:7], a[8:15], v67\n"
    "ds_read_b64_tr_b16 v[38:39], v113 offset:9216\n"
    "v_smfmac_f32_16x16x64_bf16 v[44:47], v[4:7], a[72:79], v67\n"
    "global_load_dwordx4 v[196:199], v71, s[60:61] offset:1024 sc1\n"
    "v_smfmac_f32_16x16x64_bf16 v[40:43], v[12:15], a[24:31], v67\n"
    "ds_read_b128 a[116:119], v116 offset:13312\n"
    "v_smfmac_f32_16x16x64_bf16 v[44:47], v[12:15], a[88:95], v67\n"
    "s_waitcnt lgkmcnt(1)\n"
    "ds_read_b128 a[0:3], v116 offset:10240\n"
    "v_mfma_f32_16x16x32_bf16 v[88:91], v[56:59], v[32:35], 0\n"
    "v_add_f32 v40, v40, v42\n"
    "v_add_f32 v41, v41, v43\n"
    "v_mfma_f32_16x16x32_bf16 v[88:91], v[60:63], v[36:39], v[88:91]\n"
    "v_cvt_pk_bf16_f32 v126, v40, v41\n"
    "v_add_f32 v44, v44, v46\n"
    "v_add_f32 v45, v45, v47\n"
    "v_cvt_pk_bf16_f32 v127, v44, v45\n"
    "v_smfmac_f32_16x16x64_bf16 v[52:55], v[16:19], a[224:231], v67\n"
    "ds_write_b64 v115, v[126:127] offset:11264\n"
    "v_add_f32_dpp v88, v90, v88 quad_perm:[2,3,2,3] row_mask:0xf bank_mask:0xf\n"
    "v_add_f32_dpp v89, v91, v89 quad_perm:[2,3,2,3] row_mask:0xf bank_mask:0xf\n"
    "v_smfmac_f32_16x16x64_bf16 v[48:51], v[20:23], a[168:175], v67\n"
    "ds_write_b64 v75, v[88:89] offset:14848\n"
    "v_smfmac_f32_16x16x64_bf16 v[52:55], v[20:23], a[232:239], v67\n"
    "global_load_dwordx4 v[56:59], v71, s[58:59] offset:-2048 sc1\n"
    "v_smfmac_f32_16x16x64_bf16 v[48:51], v[24:27], a[176:183], v67\n"
    "global_load_dwordx4 v[60:63], v71, s[58:59] offset:2048 sc1\n"
    "v_smfmac_f32_16x16x64_bf16 v[52:55], v[24:27], a[240:247], v67\n"
    "global_load_dwordx4 v[192:195], v71, s[60:61] offset:0 sc1\n"
    "v_smfmac_f32_16x16x64_bf16 v[48:51], v[0:3], a[128:135], v67\n"
    "s_waitcnt lgkmcnt(0)\n"
    "s_barrier\n"
    "ds_read_b64_tr_b16 v[40:41], v112 offset:10240\n"
    "v_smfmac_f32_16x16x64_bf16 v[52:55], v[0:3], a[192:199], v67\n"
    "ds_read_b64_tr_b16 v[42:43], v113 offset:10240\n"
    "ds_read_b64_tr_b16 v[44:45], v112 offset:11264\n"
    "v_smfmac_f32_16x16x64_bf16 v[48:51], v[8:11], a[144:151], v67\n"
    "ds_read_b64_tr_b16 v[46:47], v113 offset:11264\n"
    "v_smfmac_f32_16x16x64_bf16 v[52:55], v[8:11], a[208:215], v67\n"
    "ds_read_b128 v[88:91], v71 offset:14336\n"
    "s_cmp_eq_u32 s43, 63\n"
    "v_smfmac_f32_16x16x64_bf16 v[48:51], v[28:31], a[184:191], v67\n"
    "s_waitcnt lgkmcnt(3)\n"
    "ds_read_b128 a[4:7], v116 offset:11264\n"
    "v_mfma_f32_16x16x32_bf16 v[96:99], v[40:43], v[32:35], 0\n"
    "s_waitcnt lgkmcnt(2)\n"
    "ds_read_b128 a[64:67], v116 offset:14336\n"
    "v_mfma_f32_16x16x32_bf16 v[96:99], v[44:47], v[36:39], v[96:99]\n"
    "s_cbranch_scc1 L_last2%=\n"
    "v_smfmac_f32_16x16x64_bf16 v[52:55], v[28:31], a[248:255], v67\n"
    "s_waitcnt lgkmcnt(2)\n"
    "ds_read_b128 a[68:71], v116 offset:15360\n"
    "v_max3_f32 v122, v88, v89, v90\n"
    "v_max_f32 v122, v122, v91\n"
    "v_add_f32_dpp v96, v98, v96 quad_perm:[2,3,2,3] row_mask:0xf bank_mask:0xf\n"
    "v_add_f32_dpp v97, v99, v97 quad_perm:[2,3,2,3] row_mask:0xf bank_mask:0xf\n"
    "v_max_f32_dpp v123, v122, v122 quad_perm:[2,3,0,1] row_mask:0xf bank_mask:0xf\n"
    "v_max3_f32 v122, v123, v96, v97\n"
    "v_smfmac_f32_16x16x64_bf16 v[48:51], v[4:7], a[136:143], v67\n"
    "v_max_f32_dpp v121, v122, v123 quad_perm:[0,1,0,1] row_mask:0xf bank_mask:0xf\n"
    "v_max_f32_dpp v123, v122, v123 quad_perm:[0,1,0,1] row_mask:0xf bank_mask:0xf\n"
    "v_smfmac_f32_16x16x64_bf16 v[52:55], v[4:7], a[200:207], v67\n"
    "v_permlane16_swap_b32 v123, v121\n"
    "v_max_f32 v122, v123, v121\n"
    "v_max_f32 v123, v123, v121\n"
    "v_smfmac_f32_16x16x64_bf16 v[48:51], v[12:15], a[152:159], v67\n"
    "v_smfmac_f32_16x16x64_bf16 v[52:55], v[12:15], a[216:223], v67\n"
    "v_permlane32_swap_b32 v122, v123\n"
    "v_max_f32 v122, v122, v123\n"
    "v_mul_f32 v122, s76, v122\n"
    "v_pk_fma_f32 v[96:97], v[96:97], s[76:77], v[122:123] op_sel_hi:[1,1,0] neg_lo:[0,0,1] neg_hi:[0,0,1]\n"
    "v_pk_fma_f32 v[88:89], v[88:89], s[76:77], v[122:123] op_sel_hi:[1,1,0] neg_lo:[0,0,1] neg_hi:[0,0,1]\n"
    "v_pk_fma_f32 v[90:91], v[90:91], s[76:77], v[122:123] op_sel_hi:[1,1,0] neg_lo:[0,0,1] neg_hi:[0,0,1]\n"
    "v_exp_f32 v96, v96\n"
    "v_exp_f32 v97, v97\n"
    "v_exp_f32 v88, v88\n"
    "v_exp_f32 v89, v89\n"
    "v_exp_f32 v90, v90\n"
    "v_exp_f32 v91, v91\n"
    "v_pk_add_f32 v[48:49], v[48:49], v[50:51]\n"
    "v_pk_add_f32 v[52:53], v[52:53], v[54:55]\n"
    "v_cvt_pk_bf16_f32 v126, v96, v97\n"
    "v_cvt_pk_bf16_f32 v100, v88, v89\n"
    "v_cvt_pk_bf16_f32 v102, v90, v91\n"
    "v_cndmask_b32 v101, 0, v126, s[72:73]\n"
    "v_cndmask_b32 v103, 0, v126, s[74:75]\n"
    "v_cvt_pk_bf16_f32 v66, v48, v49\n"
    "v_cvt_pk_bf16_f32 v70, v48, v49\n"
    "v_cvt_pk_bf16_f32 v74, v52, v53\n"
    "v_cvt_pk_bf16_f32 v78, v52, v53\n"
    "v_smfmac_f32_16x16x64_bf16 v[104:107], v[100:103], v[64:71], v67\n"
    "v_smfmac_f32_16x16x64_bf16 v[108:111], v[100:103], v[72:79], v67\n"
    "v_mfma_f32_16x16x32_bf16 v[84:87], v[100:103], v[80:83], 0\n"
    "v_add_f32 v104, v104, v106\n"
    "v_add_f32 v105, v105, v107\n"
    "v_cvt_pk_bf16_f32 v104, v104, v105\n"
    "v_add_f32 v108, v108, v110\n"
    "v_add_f32 v109, v109, v111\n"
    "v_cvt_pk_bf16_f32 v105, v108, v109\n"
    "ds_write_b64 v114, v[104:105] offset:13312\n"
    "v_pk_add_f32 v[124:125], v[84:85], v[86:87]\n"
    "v_rcp_f32 v124, v124\n"
    "v_rcp_f32 v125, v125\n"
    "v_mov_b64 v[0:1], 0\n"
    "v_mov_b64 v[2:3], 0\n"
    "v_mov_b64 v[4:5], 0\n"
    "v_mov_b64 v[6:7], 0\n"
    "v_mov_b64 v[8:9], 0\n"
    "v_mov_b64 v[10:11], 0\n"
    "v_mov_b64 v[12:13], 0\n"
    "v_mov_b64 v[14:15], 0\n"
    "v_mfma_f32_16x16x32_bf16 v[96:99], v[100:103], v[80:83], 0\n"
    "s_waitcnt lgkmcnt(0)\n"
    "s_barrier\n"
    "ds_read_b64_tr_b16 v[32:33], v112 offset:12288\n"
    "v_mfma_f32_16x16x32_bf16 v[96:99], v[100:103], v[80:83], 0\n"
    "ds_read_b64_tr_b16 v[34:35], v113 offset:12288\n"
    "ds_read_b64_tr_b16 v[36:37], v112 offset:13312\n"
    "v_mfma_f32_16x16x32_bf16 v[96:99], v[100:103], v[80:83], 0\n"
    "ds_read_b64_tr_b16 v[38:39], v113 offset:13312\n"
    "v_mfma_f32_16x16x32_bf16 v[96:99], v[100:103], v[80:83], 0\n"
    "s_waitcnt lgkmcnt(2)\n"
    "v_smfmac_f32_16x16x64_bf16 v[0:3], v[32:35], a[32:39], v67\n"
    "v_smfmac_f32_16x16x64_bf16 v[4:7], v[32:35], a[96:103], v67\n"
    "s_waitcnt lgkmcnt(0)\n"
    "v_smfmac_f32_16x16x64_bf16 v[0:3], v[36:39], a[40:47], v67\n"
    "v_smfmac_f32_16x16x64_bf16 v[4:7], v[36:39], a[104:111], v67\n"
    "v_smfmac_f32_16x16x64_bf16 v[8:11], v[32:35], a[48:55], v67\n"
    "v_add_f32 v0, v0, v2\n"
    "v_smfmac_f32_16x16x64_bf16 v[12:15], v[32:35], a[112:119], v67\n"
    "v_add_f32 v1, v1, v3\n"
    "v_smfmac_f32_16x16x64_bf16 v[8:11], v[36:39], a[0:7], v67\n"
    "v_add_f32 v4, v4, v6\n"
    "v_smfmac_f32_16x16x64_bf16 v[12:15], v[36:39], a[64:71], v67\n"
    "v_add_f32 v5, v5, v7\n"
    "v_mul_f32 v0, v0, v124\n"
    "v_mul_f32 v1, v1, v125\n"
    "v_cvt_pk_bf16_f32 v88, v0, v1\n"
    "v_pk_mul_f32 v[4:5], v[4:5], v[124:125]\n"
    "v_pk_add_f32 v[8:9], v[8:9], v[10:11]\n"
    "v_pk_add_f32 v[12:13], v[12:13], v[14:15]\n"
    "v_cvt_pk_bf16_f32 v89, v4, v5\n"
    "v_pk_mul_f32 v[8:9], v[8:9], v[124:125]\n"
    "v_pk_mul_f32 v[12:13], v[12:13], v[124:125]\n"
    "v_cvt_pk_bf16_f32 v90, v8, v9\n"
    "v_cvt_pk_bf16_f32 v91, v12, v13\n"
    "global_store_dwordx4 v117, v[88:91], s[44:45] sc1\n"
    "s_add_u32 s43, s43, 1\n"
    "s_sleep 3\n"
    "s_add_u32 s79, s43, 1\n"
    "s_min_u32 s79, s79, 63\n"
    "s_lshr_b32 s79, s79, 1\n"
    "s_cmp_lt_u32 s79, s92\n"
    "s_cbranch_scc1 L_cntokl%=\n"
    "L_cntl%=:\n"
    "v_and_b32 v126, 16, v71\n"
    "v_lshlrev_b32 v126, 8, v126\n"
    "global_load_dwordx2 v[126:127], v126, s[66:67] sc1\n"
    "s_waitcnt vmcnt(0)\n"
    "v_xor_b32 v126, s96, v126\n"
    "v_xor_b32 v127, s97, v127\n"
    "v_cmp_gt_u64 vcc, 64, v[126:127]\n"
    "s_nop 1\n"
    "v_cndmask_b32 v126, 0, v126, vcc\n"
    "s_nop 0\n"
    "v_readlane_b32 s80, v126, 0\n"
    "v_readlane_b32 s81, v126, 1\n"
    "s_nop 1\n"
    "s_min_u32 s92, s80, s81\n"
    "s_cmp_lt_u32 s79, s92\n"
    "s_cbranch_scc1 L_cntokl%=\n"
    "s_sleep 2\n"
    "s_add_u32 s90, s90, 1\n"
    "s_and_b32 s78, s90, 0xff\n"
    "s_cmp_lg_u32 s78, 0\n"
    "s_cbranch_scc1 L_cntl%=\n"
    "v_mov_b32 v126, 0\n"
    "global_load_dwordx2 v[126:127], v126, s[68:69] sc1\n"
    "s_waitcnt vmcnt(0)\n"
    "v_cmp_eq_u64 vcc, s[96:97], v[126:127]\n"
    "s_nop 1\n"
    "s_cbranch_vccnz L_bail_cntl%=\n"
    "s_cmp_lt_u32 s90, 0x40000\n"
    "s_cbranch_scc1 L_cntl%=\n"
    "v_mov_b32 v126, s96\n"
    "v_mov_b32 v127, s97\n"
    "s_mov_b64 exec, 1\n"
    "global_store_dwordx2 v71, v[126:127], s[68:69] sc1\n"
    "s_mov_b64 exec, -1\n"
    "L_bail_cntl%=:\n"
    "L_cntokl%=:\n"
    "ds_read_b128 a[32:35], v116 offset:16384\n"
    "ds_read_b128 a[36:39], v116 offset:17408\n"
    "ds_read_b128 a[96:99], v116 offset:18432\n"
    "ds_read_b128 a[100:103], v116 offset:19456\n"
    "ds_read_b128 a[40:43], v116 offset:20480\n"
    "ds_read_b128 a[44:47], v116 offset:21504\n"
    "ds_read_b128 a[104:107], v116 offset:22528\n"
    "ds_read_b128 a[108:111], v116 offset:23552\n"
    "ds_read_b128 a[48:51], v116 offset:24576\n"
    "ds_read_b128 a[52:55], v116 offset:25600\n"
    "ds_read_b128 a[112:115], v116 offset:26624\n"
    "ds_read_b128 a[116:119], v116 offset:27648\n"
    "ds_read_b128 a[0:3], v116 offset:28672\n"
    "s_waitcnt lgkmcnt(8)\n"
    "ds_read_b128 a[4:7], v116 offset:29696\n"
    "ds_read_b128 a[64:67], v116 offset:30720\n"
    "ds_read_b128 a[68:71], v116 offset:31744\n"
    "s_and_b32 s78, s43, 3\n"
    "s_lshl_b32 s78, s78, 17\n"
    "s_add_u32 s54, s48, s78\n"
    "s_addc_u32 s55, s49, 0\n"
    "s_add_u32 s62, s62, 0x20000\n"
    "s_addc_u32 s63, s63, 0\n"
    "v_mov_b64 v[32:33], 0\n"
    "v_mov_b64 v[34:35], 0\n"
    "v_mov_b64 v[36:37], 0\n"
    "v_mov_b64 v[38:39], 0\n"
    "v_mov_b64 v[48:49], 0\n"
    "v_mov_b64 v[50:51], 0\n"
    "v_mov_b64 v[52:53], 0\n"
    "v_mov_b64 v[54:55], 0\n"
    "v_mov_b64 v[104:105], 0\n"
    "v_mov_b64 v[106:107], 0\n"
    "v_mov_b64 v[108:109], 0\n"
    "v_mov_b64 v[110:111], 0\n"
    "s_xor_b32 s44, s44, 0x8000\n"
    "s_add_u32 s58, s58, 0x40000\n"
    "s_addc_u32 s59, s59, 0\n"
    "s_add_u32 s60, s60, 0x40000\n"
    "s_addc_u32 s61, s61, 0\n"
    "s_waitcnt vmcnt(1)\n"
    "v_mov_b64 v[64:65], v[192:193]\n"
    "v_mov_b64 v[68:69], v[194:195]\n"
    "v_mov_b64 v[72:73], v[196:197]\n"
    "v_mov_b64 v[76:77], v[198:199]\n"
    "ds_read_b128 v[192:195], v116 offset:32768\n"
    "ds_read_b128 v[196:199], v116 offset:33792\n"
    "s_waitcnt lgkmcnt(0)\n"
    "s_barrier\n"
    "global_load_dwordx4 v[16:19], v71, s[54:55] offset:0 sc1\n"
    "s_nop 3\n"
    "global_load_dwordx4 v[20:23], v71, s[54:55] offset:1024 sc1\n"
    "s_nop 15\n"
    "s_cmp_lg_u32 s42, 0\n"
    "s_cbranch_scc1 L_vmb2%=\n"
    "s_mov_b32 exec_hi, 0\n"
    "global_store_dwordx4 v71, v[44:47], s[62:63] offset:1024 sc1\n"
    "s_mov_b64 exec, -1\n"
    "s_branch L_kvb2%=\n"
    "L_vmb2%=:\n"
    "v_mov_b32 v126, v66\n"
    "v_mov_b32 v127, v74\n"
    "global_store_dwordx2 v79, v[126:127], s[62:63] offset:1024 sc1\n"
    "L_kvb2%=:\n"
    "s_nop 1\n"
    "v_mov_b64 v[40:41], 0\n"
    "v_mov_b64 v[42:43], 0\n"
    "v_mov_b64 v[44:45], 0\n"
    "v_mov_b64 v[46:47], 0\n"
    "s_branch L_top2%=\n"
    "L_slow2%=:\n"
    "v_cmp_eq_u32 s[78:79], s50, v16\n"
    "v_cmp_eq_u32 s[80:81], s50, v17\n"
    "v_cmp_eq_u32 s[82:83], s50, v18\n"
    "v_cmp_eq_u32 s[84:85], s50, v19\n"
    "v_cmp_eq_u32 s[86:87], s50, v20\n"
    "v_cmp_eq_u32 s[88:89], s50, v21\n"
    "v_cmp_eq_u32 s[52:53], s50, v22\n"
    "v_cmp_eq_u32 vcc, s50, v23\n"
    "global_load_dwordx4 v[16:19], v71, s[54:55] offset:0 sc1\n"
    "global_load_dwordx4 v[20:23], v71, s[54:55] offset:1024 sc1\n"
    "s_waitcnt lgkmcnt(0)\n"
    "s_barrier\n"
    "s_or_b64 vcc, vcc, s[78:79]\n"
    "s_or_b64 vcc, vcc, s[80:81]\n"
    "s_or_b64 vcc, vcc, s[82:83]\n"
    "s_or_b64 vcc, vcc, s[84:85]\n"
    "s_or_b64 vcc, vcc, s[86:87]\n"
    "s_or_b64 vcc, vcc, s[88:89]\n"
    "s_or_b64 vcc, vcc, s[52:53]\n"
    "s_cbranch_scc1 L_redo2%=\n"
    "v_cmp_eq_u32 s[78:79], s50, v24\n"
    "v_cmp_eq_u32 s[80:81], s50, v25\n"
    "v_cmp_eq_u32 s[82:83], s50, v26\n"
    "v_cmp_eq_u32 s[84:85], s50, v27\n"
    "v_cmp_eq_u32 s[86:87], s50, v0\n"
    "v_cmp_eq_u32 s[88:89], s50, v1\n"
    "v_cmp_eq_u32 s[52:53], s50, v2\n"
    "v_cmp_eq_u32 vcc, s50, v3\n"
    "s_nop 0\n"
    "s_or_b64 vcc, vcc, s[78:79]\n"
    "s_or_b64 vcc, vcc, s[80:81]\n"
    "s_or_b64 vcc, vcc, s[82:83]\n"
    "s_or_b64 vcc, vcc, s[84:85]\n"
    "s_or_b64 vcc, vcc, s[86:87]\n"
    "s_or_b64 vcc, vcc, s[88:89]\n"
    "s_or_b64 vcc, vcc, s[52:53]\n"
    "s_cbranch_scc1 L_redo2%=\n"
    "v_cmp_eq_u32 s[78:79], s50, v8\n"
    "v_cmp_eq_u32 s[80:81], s50, v9\n"
    "v_cmp_eq_u32 s[82:83], s50, v10\n"
    "v_cmp_eq_u32 s[84:85], s50, v11\n"
    "v_cmp_eq_u32 s[86:87], s50, v28\n"
    "v_cmp_eq_u32 s[88:89], s50, v29\n"
    "v_cmp_eq_u32 s[52:53], s50, v30\n"
    "v_cmp_eq_u32 vcc, s50, v31\n"
    "s_nop 0\n"
    "s_or_b64 vcc, vcc, s[78:79]\n"
    "s_or_b64 vcc, vcc, s[80:81]\n"
    "s_or_b64 vcc, vcc, s[82:83]\n"
    "s_or_b64 vcc, vcc, s[84:85]\n"
    "s_or_b64 vcc, vcc, s[86:87]\n"
    "s_or_b64 vcc, vcc, s[88:89]\n"
    "s_or_b64 vcc, vcc, s[52:53]\n"
    "s_cbranch_scc1 L_redo2%=\n"
    "v_cmp_eq_u32 s[78:79], s50, v4\n"
    "v_cmp_eq_u32 s[80:81], s50, v5\n"
    "v_cmp_eq_u32 s[82:83], s50, v6\n"
    "v_cmp_eq_u32 s[84:85], s50, v7\n"
    "v_cmp_eq_u32 s[86:87], s50, v12\n"
    "v_cmp_eq_u32 s[88:89], s50, v13\n"
    "v_cmp_eq_u32 s[52:53], s50, v14\n"
    "v_cmp_eq_u32 vcc, s50, v15\n"
    "s_nop 0\n"
    "s_or_b64 vcc, vcc, s[78:79]\n"
    "s_or_b64 vcc, vcc, s[80:81]\n"
    "s_or_b64 vcc, vcc, s[82:83]\n"
    "s_or_b64 vcc, vcc, s[84:85]\n"
    "s_or_b64 vcc, vcc, s[86:87]\n"
    "s_or_b64 vcc, vcc, s[88:89]\n"
    "s_or_b64 vcc, vcc, s[52:53]\n"
    "s_cbranch_scc1 L_redo2%=\n"
    "s_waitcnt vmcnt(0)\n"
    "s_branch L_go2%=\n"
    "L_redo2%=:\n"
    "ds_read_b128 a[32:35], v116 offset:16384\n"
    "ds_read_b128 a[36:39], v116 offset:17408\n"
    "ds_read_b128 a[96:99], v116 offset:18432\n"
    "ds_read_b128 a[100:103], v116 offset:19456\n"
    "ds_read_b128 a[40:43], v116 offset:20480\n"
    "ds_read_b128 a[44:47], v116 offset:21504\n"
    "ds_read_b128 a[104:107], v116 offset:22528\n"
    "ds_read_b128 a[108:111], v116 offset:23552\n"
    "ds_read_b128 a[48:51], v116 offset:24576\n"
    "ds_read_b128 a[52:55], v116 offset:25600\n"
    "ds_read_b128 a[112:115], v116 offset:26624\n"
    "ds_read_b128 a[116:119], v116 offset:27648\n"
    "ds_read_b128 a[0:3], v116 offset:28672\n"
    "ds_read_b128 a[4:7], v116 offset:29696\n"
    "s_waitcnt lgkmcnt(8)\n"
    "ds_read_b128 a[64:67], v116 offset:30720\n"
    "ds_read_b128 a[68:71], v116 offset:31744\n"
    "v_mov_b64 v[32:33], 0\n"
    "v_mov_b64 v[34:35], 0\n"
    "v_mov_b64 v[36:37], 0\n"
    "v_mov_b64 v[38:39], 0\n"
    "v_mov_b64 v[40:41], 0\n"
    "v_mov_b64 v[42:43], 0\n"
    "v_mov_b64 v[44:45], 0\n"
    "v_mov_b64 v[46:47], 0\n"
    "v_mov_b64 v[48:49], 0\n"
    "v_mov_b64 v[50:51], 0\n"
    "v_mov_b64 v[52:53], 0\n"
    "v_mov_b64 v[54:55], 0\n"
    "s_add_u32 s90, s90, 1\n"
    "s_and_b32 s78, s90, 0xff\n"
    "s_cmp_lg_u32 s78, 0\n"
    "s_cbranch_scc1 L_re2%=\n"
    "v_mov_b32 v126, 0\n"
    "global_load_dwordx2 v[126:127], v126, s[68:69] sc1\n"
    "s_waitcnt vmcnt(0)\n"
    "v_cmp_eq_u64 vcc, s[96:97], v[126:127]\n"
    "s_nop 1\n"
    "s_cbranch_vccnz L_bail_g2%=\n"
    "s_cmp_lt_u32 s90, 0x40000\n"
    "s_cbranch_scc1 L_re2%=\n"
    "v_mov_b32 v126, s96\n"
    "v_mov_b32 v127, s97\n"
    "s_mov_b64 exec, 1\n"
    "global_store_dwordx2 v71, v[126:127], s[68:69] sc1\n"
    "s_mov_b64 exec, -1\n"
    "L_bail_g2%=:\n"
    "s_branch L_go2%=\n"
    "L_re2%=:\n"
    "s_waitcnt vmcnt(0) lgkmcnt(0)\n"
    "s_branch L_top2%=\n"
    "L_last2%=:\n"
    "v_smfmac_f32_16x16x64_bf16 v[52:55], v[28:31], a[248:255], v67\n"
    "v_smfmac_f32_16x16x64_bf16 v[48:51], v[4:7], a[136:143], v67\n"
    "v_smfmac_f32_16x16x64_bf16 v[52:55], v[4:7], a[200:207], v67\n"
    "v_smfmac_f32_16x16x64_bf16 v[48:51], v[12:15], a[152:159], v67\n"
    "v_smfmac_f32_16x16x64_bf16 v[52:55], v[12:15], a[216:223], v67\n"
    "s_nop 3\n"
    "v_pk_add_f32 v[48:49], v[48:49], v[50:51]\n"
    "s_nop 2\n"
    "v_pk_add_f32 v[52:53], v[52:53], v[54:55]\n"
    "v_cvt_pk_bf16_f32 v66, v48, v49\n"
    "v_cvt_pk_bf16_f32 v74, v52, v53\n"
    "s_add_u32 s62, s62, 0x20000\n"
    "s_addc_u32 s63, s63, 0\n"
    "s_cmp_lg_u32 s42, 0\n"
    "s_cbranch_scc1 L_vmc2%=\n"
    "s_mov_b32 exec_hi, 0\n"
    "global_store_dwordx4 v71, v[44:47], s[62:63] offset:1024 sc1\n"
    "s_mov_b64 exec, -1\n"
    "s_branch L_kvc2%=\n"
    "L_vmc2%=:\n"
    "v_mov_b32 v126, v66\n"
    "v_mov_b32 v127, v74\n"
    "global_store_dwordx2 v79, v[126:127], s[62:63] offset:1024 sc1\n"
    "L_kvc2%=:\n"
    "s_waitcnt vmcnt(0) lgkmcnt(0)\n"
    "s_barrier\n"
    "s_endpgm\n"
    ".p2align 6\n"
    "L_poll3%=:\n"
    "global_load_dwordx4 v[24:27], v71, s[54:55] offset:0 sc1\n"
    "global_load_dwordx4 v[28:31], v71, s[54:55] offset:1024 sc1\n"
    "s_waitcnt vmcnt(0)\n"
    "s_branch L_top3%=\n"
    ".p2align 6\n"
    "L_top3%=:\n"
    "s_waitcnt vmcnt(2)\n"
    "v_smfmac_f32_16x16x64_bf16 v[40:43], v[24:27], a[48:55], v67\n"
    "ds_write_b128 v71, v[24:27] offset:6144\n"
    "v_smfmac_f32_16x16x64_bf16 v[44:47], v[24:27], a[112:119], v67\n"
    "v_smfmac_f32_16x16x64_bf16 v[32:35], v[24:27], v[224:231], v67\n"
    "v_smfmac_f32_16x16x64_bf16 v[36:39], v[24:27], v[232:239], v67\n"
    "s_waitcnt vmcnt(1)\n"
    "v_smfmac_f32_16x16x64_bf16 v[40:43], v[28:31], a[56:63], v67\n"
    "ds_write_b128 v71, v[28:31] offset:7168\n"
    "s_waitcnt lgkmcnt(1)\n"
    "s_barrier\n"
    "ds_read_b128 v[16:19], v71 offset:4096\n"
    "v_smfmac_f32_16x16x64_bf16 v[44:47], v[28:31], a[120:127], v67\n"
    "ds_read_b128 v[8:11], v71 offset:2048\n"
    "v_smfmac_f32_16x16x64_bf16 v[32:35], v[28:31], v[240:247], v67\n"
    "ds_read_b128 v[0:3], v71 offset:0\n"
    "v_smfmac_f32_16x16x64_bf16 v[36:39], v[28:31], v[248:255], v67\n"
    "s_waitcnt lgkmcnt(3)\n"
    "s_barrier\n"
    "v_smfmac_f32_16x16x64_bf16 v[48:51], v[24:27], a[176:183], v67\n"
    "s_waitcnt lgkmcnt(2)\n"
    "ds_read_b128 v[20:23], v71 offset:5120\n"
    "v_smfmac_f32_16x16x64_bf16 v[32:35], v[16:19], v[192:199], v67\n"
    "ds_read_b128 v[12:15], v71 offset:3072\n"
    "v_smfmac_f32_16x16x64_bf16 v[36:39], v[16:19], v[200:207], v67\n"
    "s_waitcnt lgkmcnt(2)\n"
    "ds_read_b128 v[4:7], v71 offset:1024\n"
    "v_smfmac_f32_16x16x64_bf16 v[32:35], v[8:11], v[160:167], v67\n"
    "ds_read_b128 a[48:51], v116 offset:0\n"
    "v_smfmac_f32_16x16x64_bf16 v[36:39], v[8:11], v[168:175], v67\n"
    "ds_read_b128 a[52:55], v116 offset:1024\n"
    "v_smfmac_f32_16x16x64_bf16 v[32:35], v[0:3], v[128:135], v67\n"
    "ds_read_b128 a[112:115], v116 offset:4096\n"
    "v_smfmac_f32_16x16x64_bf16 v[36:39], v[0:3], v[136:143], v67\n"
    "s_waitcnt lgkmcnt(5)\n"
    "ds_read_b128 a[116:119], v116 offset:5120\n"
    "v_smfmac_f32_16x16x64_bf16 v[32:35], v[20:23], v[208:215], v67\n"
    "ds_read_b128 a[56:59], v116 offset:2048\n"
    "v_smfmac_f32_16x16x64_bf16 v[36:39], v[20:23], v[216:223], v67\n"
    "s_waitcnt lgkmcnt(5)\n"
    "ds_read_b128 a[60:63], v116 offset:3072\n"
    "v_smfmac_f32_16x16x64_bf16 v[32:35], v[12:15], v[176:183], v67\n"
    "ds_read_b128 a[120:123], v116 offset:6144\n"
    "v_smfmac_f32_16x16x64_bf16 v[36:39], v[12:15], v[184:191], v67\n"
    "ds_read_b128 a[124:127], v116 offset:7168\n"
    "v_smfmac_f32_16x16x64_bf16 v[32:35], v[4:7], v[144:151], v67\n"
    "v_smfmac_f32_16x16x64_bf16 v[36:39], v[4:7], v[152:159], v67\n"
    "v_smfmac_f32_16x16x64_bf16 v[40:43], v[16:19], a[32:39], v67\n"
    "v_add_f32 v32, v32, v34\n"
    "v_add_f32 v33, v33, v35\n"
    "v_smfmac_f32_16x16x64_bf16 v[44:47], v[16:19], a[96:103], v67\n"
    "ds_read_b128 a[32:35], v116 offset:8192\n"
    "v_cvt_pk_bf16_f32 v126, v32, v33\n"
    "v_add_f32 v36, v36, v38\n"
    "v_add_f32 v37, v37, v39\n"
    "v_cvt_pk_bf16_f32 v127, v36, v37\n"
    "v_smfmac_f32_16x16x64_bf16 v[40:43], v[8:11], a[16:23], v67\n"
    "ds_write_b64 v114, v[126:127] offset:9728\n"
    "v_smfmac_f32_16x16x64_bf16 v[44:47], v[8:11], a[80:87], v67\n"
    "ds_read_b128 a[36:39], v116 offset:9216\n"
    "v_cmp_u_f32 vcc, v32, v33\n"
    "v_smfmac_f32_16x16x64_bf16 v[40:43], v[0:3], a[0:7], v67\n"
    "ds_read_b128 a[96:99], v116 offset:12288\n"
    "v_smfmac_f32_16x16x64_bf16 v[44:47], v[0:3], a[64:71], v67\n"
    "s_cbranch_vccnz L_slow3%=\n"
    "L_go3%=:\n"
    "v_smfmac_f32_16x16x64_bf16 v[40:43], v[20:23], a[40:47], v67\n"
    "s_waitcnt lgkmcnt(2)\n"
    "s_barrier\n"
    "ds_read_b64_tr_b16 v[32:33], v112 offset:8192\n"
    "v_smfmac_f32_16x16x64_bf16 v[44:47], v[20:23], a[104:111], v67\n"
    "ds_read_b64_tr_b16 v[34:35], v113 offset:8192\n"
    "ds_read_b64_tr_b16 v[36:37], v112 offset:9216\n"
    "v_smfmac_f32_16x16x64_bf16 v[40:43], v[12:15], a[24:31], v67\n"
    "ds_read_b64_tr_b16 v[38:39], v113 offset:9216\n"
    "v_smfmac_f32_16x16x64_bf16 v[44:47], v[12:15], a[88:95], v67\n"
    "global_load_dwordx4 v[228:231], v71, s[60:61] offset:1024 sc1\n"
    "v_smfmac_f32_16x16x64_bf16 v[40:43], v[4:7], a[8:15], v67\n"
    "ds_read_b128 a[100:103], v116 offset:13312\n"
    "v_smfmac_f32_16x16x64_bf16 v[44:47], v[4:7], a[72:79], v67\n"
    "s_waitcnt lgkmcnt(1)\n"
    "ds_read_b128 a[16:19], v116 offset:10240\n"
    "v_mfma_f32_16x16x32_bf16 v[88:91], v[56:59], v[32:35], 0\n"
    "v_add_f32 v40, v40, v42\n"
    "v_add_f32 v41, v41, v43\n"
    "v_mfma_f32_16x16x32_bf16 v[88:91], v[60:63], v[36:39], v[88:91]\n"
    "v_cvt_pk_bf16_f32 v126, v40, v41\n"
    "v_add_f32 v44, v44, v46\n"
    "v_add_f32 v45, v45, v47\n"
    "v_cvt_pk_bf16_f32 v127, v44, v45\n"
    "v_smfmac_f32_16x16x64_bf16 v[52:55], v[24:27], a[240:247], v67\n"
    "ds_write_b64 v115, v[126:127] offset:11776\n"
    "v_add_f32_dpp v88, v90, v88 quad_perm:[2,3,2,3] row_mask:0xf bank_mask:0xf\n"
    "v_add_f32_dpp v89, v91, v89 quad_perm:[2,3,2,3] row_mask:0xf bank_mask:0xf\n"
    "v_smfmac_f32_16x16x64_bf16 v[48:51], v[28:31], a[184:191], v67\n"
    "ds_write_b64 v75, v[88:89] offset:14880\n"
    "v_smfmac_f32_16x16x64_bf16 v[52:55], v[28:31], a[248:255], v67\n"
    "global_load_dwordx4 v[56:59], v71, s[58:59] offset:-2048 sc1\n"
    "v_smfmac_f32_16x16x64_bf16 v[48:51], v[16:19], a[160:167], v67\n"
    "global_load_dwordx4 v[60:63], v71, s[58:59] offset:2048 sc1\n"
    "v_smfmac_f32_16x16x64_bf16 v[52:55], v[16:19], a[224:231], v67\n"
    "global_load_dwordx4 v[224:227], v71, s[60:61] offset:0 sc1\n"
    "v_smfmac_f32_16x16x64_bf16 v[48:51], v[8:11], a[144:151], v67\n"
    "s_waitcnt lgkmcnt(0)\n"
    "s_barrier\n"
    "ds_read_b64_tr_b16 v[40:41], v112 offset:10240\n"
    "v_smfmac_f32_16x16x64_bf16 v[52:55], v[8:11], a[208:215], v67\n"
    "ds_read_b64_tr_b16 v[42:43], v113 offset:10240\n"
    "ds_read_b64_tr_b16 v[44:45], v112 offset:11264\n"
    "v_smfmac_f32_16x16x64_bf16 v[48:51], v[0:3], a[128:135], v67\n"
    "ds_read_b64_tr_b16 v[46:47], v113 offset:11264\n"
    "v_smfmac_f32_16x16x64_bf16 v[52:55], v[0:3], a[192:199], v67\n"
    "ds_read_b128 v[88:91], v71 offset:14336\n"
    "s_cmp_eq_u32 s43, 63\n"
    "v_smfmac_f32_16x16x64_bf16 v[48:51], v[20:23], a[168:175], v67\n"
    "s_waitcnt lgkmcnt(3)\n"
    "ds_read_b128 a[20:23], v116 offset:11264\n"
    "v_mfma_f32_16x16x32_bf16 v[96:99], v[40:43], v[32:35], 0\n"
    "s_waitcnt lgkmcnt(2)\n"
    "ds_read_b128 a[80:83], v116 offset:14336\n"
    "v_mfma_f32_16x16x32_bf16 v[96:99], v[44:47], v[36:39], v[96:99]\n"
    "s_cbranch_scc1 L_last3%=\n"
    "v_smfmac_f32_16x16x64_bf16 v[52:55], v[20:23], a[232:239], v67\n"
    "s_waitcnt lgkmcnt(2)\n"
    "ds_read_b128 a[84:87], v116 offset:15360\n"
    "v_max3_f32 v122, v88, v89, v90\n"
    "v_max_f32 v122, v122, v91\n"
    "v_add_f32_dpp v96, v98, v96 quad_perm:[2,3,2,3] row_mask:0xf bank_mask:0xf\n"
    "v_add_f32_dpp v97, v99, v97 quad_perm:[2,3,2,3] row_mask:0xf bank_mask:0xf\n"
    "v_max_f32_dpp v123, v122, v122 quad_perm:[2,3,0,1] row_mask:0xf bank_mask:0xf\n"
    "v_max3_f32 v122, v123, v96, v97\n"
    "v_smfmac_f32_16x16x64_bf16 v[48:51], v[12:15], a[152:159], v67\n"
    "v_max_f32_dpp v121, v122, v123 quad_perm:[0,1,0,1] row_mask:0xf bank_mask:0xf\n"
    "v_max_f32_dpp v123, v122, v123 quad_perm:[0,1,0,1] row_mask:0xf bank_mask:0xf\n"
    "v_smfmac_f32_16x16x64_bf16 v[52:55], v[12:15], a[216:223], v67\n"
    "v_permlane16_swap_b32 v123, v121\n"
    "v_max_f32 v122, v123, v121\n"
    "v_max_f32 v123, v123, v121\n"
    "v_smfmac_f32_16x16x64_bf16 v[48:51], v[4:7], a[136:143], v67\n"
    "v_smfmac_f32_16x16x64_bf16 v[52:55], v[4:7], a[200:207], v67\n"
    "v_permlane32_swap_b32 v122, v123\n"
    "v_max_f32 v122, v122, v123\n"
    "v_mul_f32 v122, s76, v122\n"
    "v_pk_fma_f32 v[96:97], v[96:97], s[76:77], v[122:123] op_sel_hi:[1,1,0] neg_lo:[0,0,1] neg_hi:[0,0,1]\n"
    "v_pk_fma_f32 v[88:89], v[88:89], s[76:77], v[122:123] op_sel_hi:[1,1,0] neg_lo:[0,0,1] neg_hi:[0,0,1]\n"
    "v_pk_fma_f32 v[90:91], v[90:91], s[76:77], v[122:123] op_sel_hi:[1,1,0] neg_lo:[0,0,1] neg_hi:[0,0,1]\n"
    "v_exp_f32 v96, v96\n"
    "v_exp_f32 v97, v97\n"
    "v_exp_f32 v88, v88\n"
    "v_exp_f32 v89, v89\n"
    "v_exp_f32 v90, v90\n"
    "v_exp_f32 v91, v91\n"
    "v_pk_add_f32 v[48:49], v[48:49], v[50:51]\n"
    "v_pk_add_f32 v[52:53], v[52:53], v[54:55]\n"
    "v_cvt_pk_bf16_f32 v126, v96, v97\n"
    "v_cvt_pk_bf16_f32 v100, v88, v89\n"
    "v_cvt_pk_bf16_f32 v102, v90, v91\n"
    "v_cndmask_b32 v101, 0, v126, s[72:73]\n"
    "v_cndmask_b32 v103, 0, v126, s[74:75]\n"
    "v_cvt_pk_bf16_f32 v66, v48, v49\n"
    "v_cvt_pk_bf16_f32 v70, v48, v49\n"
    "v_cvt_pk_bf16_f32 v74, v52, v53\n"
    "v_cvt_pk_bf16_f32 v78, v52, v53\n"
    "v_smfmac_f32_16x16x64_bf16 v[104:107], v[100:103], v[64:71], v67\n"
    "v_smfmac_f32_16x16x64_bf16 v[108:111], v[100:103], v[72:79], v67\n"
    "v_mfma_f32_16x16x32_bf16 v[84:87], v[100:103], v[80:83], 0\n"
    "v_add_f32 v104, v104, v106\n"
    "v_add_f32 v105, v105, v107\n"
    "v_cvt_pk_bf16_f32 v104, v104, v105\n"
    "v_add_f32 v108, v108, v110\n"
    "v_add_f32 v109, v109, v111\n"
    "v_cvt_pk_bf16_f32 v105, v108, v109\n"
    "ds_write_b64 v114, v[104:105] offset:13824\n"
    "v_pk_add_f32 v[124:125], v[84:85], v[86:87]\n"
    "v_rcp_f32 v124, v124\n"
    "v_rcp_f32 v125, v125\n"
    "v_mov_b64 v[0:1], 0\n"
    "v_mov_b64 v[2:3], 0\n"
    "v_mov_b64 v[4:5], 0\n"
    "v_mov_b64 v[6:7], 0\n"
    "v_mov_b64 v[8:9], 0\n"
    "v_mov_b64 v[10:11], 0\n"
    "v_mov_b64 v[12:13], 0\n"
    "v_mov_b64 v[14:15], 0\n"
    "v_mfma_f32_16x16x32_bf16 v[96:99], v[100:103], v[80:83], 0\n"
    "s_waitcnt lgkmcnt(0)\n"
    "s_barrier\n"
    "ds_read_b64_tr_b16 v[32:33], v112 offset:12288\n"
    "v_mfma_f32_16x16x32_bf16 v[96:99], v[100:103], v[80:83], 0\n"
    "ds_read_b64_tr_b16 v[34:35], v113 offset:12288\n"
    "ds_read_b64_tr_b16 v[36:37], v112 offset:13312\n"
    "v_mfma_f32_16x16x32_bf16 v[96:99], v[100:103], v[80:83], 0\n"
    "ds_read_b64_tr_b16 v[38:39], v113 offset:13312\n"
    "v_mfma_f32_16x16x32_bf16 v[96:99], v[100:103], v[80:83], 0\n"
    "s_waitcnt lgkmcnt(2)\n"
    "v_smfmac_f32_16x16x64_bf16 v[0:3], v[32:35], a[48:55], v67\n"
    "v_smfmac_f32_16x16x64_bf16 v[4:7], v[32:35], a[112:119], v67\n"
    "s_waitcnt lgkmcnt(0)\n"
    "v_smfmac_f32_16x16x64_bf16 v[0:3], v[36:39], a[56:63], v67\n"
    "v_smfmac_f32_16x16x64_bf16 v[4:7], v[36:39], a[120:127], v67\n"
    "v_smfmac_f32_16x16x64_bf16 v[8:11], v[32:35], a[32:39], v67\n"
    "v_add_f32 v0, v0, v2\n"
    "v_smfmac_f32_16x16x64_bf16 v[12:15], v[32:35], a[96:103], v67\n"
    "v_add_f32 v1, v1, v3\n"
    "v_smfmac_f32_16x16x64_bf16 v[8:11], v[36:39], a[16:23], v67\n"
    "v_add_f32 v4, v4, v6\n"
    "v_smfmac_f32_16x16x64_bf16 v[12:15], v[36:39], a[80:87], v67\n"
    "v_add_f32 v5, v5, v7\n"
    "v_mul_f32 v0, v0, v124\n"
    "v_mul_f32 v1, v1, v125\n"
    "v_cvt_pk_bf16_f32 v88, v0, v1\n"
    "v_pk_mul_f32 v[4:5], v[4:5], v[124:125]\n"
    "v_pk_add_f32 v[8:9], v[8:9], v[10:11]\n"
    "v_pk_add_f32 v[12:13], v[12:13], v[14:15]\n"
    "v_cvt_pk_bf16_f32 v89, v4, v5\n"
    "v_pk_mul_f32 v[8:9], v[8:9], v[124:125]\n"
    "v_pk_mul_f32 v[12:13], v[12:13], v[124:125]\n"
    "v_cvt_pk_bf16_f32 v90, v8, v9\n"
    "v_cvt_pk_bf16_f32 v91, v12, v13\n"
    "global_store_dwordx4 v117, v[88:91], s[44:45] sc1\n"
    "s_add_u32 s43, s43, 1\n"
    "s_sleep 3\n"
    "ds_read_b128 a[48:51], v116 offset:16384\n"
    "ds_read_b128 a[52:55], v116 offset:17408\n"
    "ds_read_b128 a[112:115], v116 offset:18432\n"
    "ds_read_b128 a[116:119], v116 offset:19456\n"
    "ds_read_b128 a[56:59], v116 offset:20480\n"
    "ds_read_b128 a[60:63], v116 offset:21504\n"
    "ds_read_b128 a[120:123], v116 offset:22528\n"
    "ds_read_b128 a[124:127], v116 offset:23552\n"
    "ds_read_b128 a[32:35], v116 offset:24576\n"
    "ds_read_b128 a[36:39], v116 offset:25600\n"
    "ds_read_b128 a[96:99], v116 offset:26624\n"
    "ds_read_b128 a[100:103], v116 offset:27648\n"
    "ds_read_b128 a[16:19], v116 offset:28672\n"
    "s_waitcnt lgkmcnt(8)\n"
    "ds_read_b128 a[20:23], v116 offset:29696\n"
    "ds_read_b128 a[80:83], v116 offset:30720\n"
    "ds_read_b128 a[84:87], v116 offset:31744\n"
    "s_and_b32 s78, s43, 3\n"
    "s_lshl_b32 s78, s78, 17\n"
    "s_add_u32 s54, s48, s78\n"
    "s_addc_u32 s55, s49, 0\n"
    "s_add_u32 s62, s62, 0x20000\n"
    "s_addc_u32 s63, s63, 0\n"
    "v_mov_b64 v[32:33], 0\n"
    "v_mov_b64 v[34:35], 0\n"
    "v_mov_b64 v[36:37], 0\n"
    "v_mov_b64 v[38:39], 0\n"
    "v_mov_b64 v[48:49], 0\n"
    "v_mov_b64 v[50:51], 0\n"
    "v_mov_b64 v[52:53], 0\n"
    "v_mov_b64 v[54:55], 0\n"
    "v_mov_b64 v[104:105], 0\n"
    "v_mov_b64 v[106:107], 0\n"
    "v_mov_b64 v[108:109], 0\n"
    "v_mov_b64 v[110:111], 0\n"
    "s_xor_b32 s44, s44, 0x8000\n"
    "s_add_u32 s58, s58, 0x40000\n"
    "s_addc_u32 s59, s59, 0\n"
    "s_add_u32 s60, s60, 0x40000\n"
    "s_addc_u32 s61, s61, 0\n"
    "s_waitcnt vmcnt(1)\n"
    "v_mov_b64 v[64:65], v[224:225]\n"
    "v_mov_b64 v[68:69], v[226:227]\n"
    "v_mov_b64 v[72:73], v[228:229]\n"
    "v_mov_b64 v[76:77], v[230:231]\n"
    "ds_read_b128 v[224:227], v116 offset:32768\n"
    "ds_read_b128 v[228:231], v116 offset:33792\n"
    "s_waitcnt lgkmcnt(0)\n"
    "s_barrier\n"
    "global_load_dwordx4 v[24:27], v71, s[54:55] offset:0 sc1\n"
    "s_nop 3\n"
    "global_load_dwordx4 v[28:31], v71, s[54:55] offset:1024 sc1\n"
    "s_nop 15\n"
    "s_cmp_lg_u32 s42, 0\n"
    "s_cbranch_scc1 L_vmb3%=\n"
    "s_mov_b32 exec_lo, 0\n"
    "global_store_dwordx4 v71, v[44:47], s[62:63] offset:1024 sc1\n"
    "s_mov_b64 exec, -1\n"
    "s_branch L_kvb3%=\n"
    "L_vmb3%=:\n"
    "v_mov_b32 v126, v66\n"
    "v_mov_b32 v127, v74\n"
    "global_store_dwordx2 v79, v[126:127], s[62:63] offset:1536 sc1\n"
    "L_kvb3%=:\n"
    "s_nop 1\n"
    "v_mov_b64 v[40:41], 0\n"
    "v_mov_b64 v[42:43], 0\n"
    "v_mov_b64 v[44:45], 0\n"
    "v_mov_b64 v[46:47], 0\n"
    "s_branch L_top3%=\n"
    "L_slow3%=:\n"
    "v_cmp_eq_u32 s[78:79], s50, v24\n"
    "v_cmp_eq_u32 s[80:81], s50, v25\n"
    "v_cmp_eq_u32 s[82:83], s50, v26\n"
    "v_cmp_eq_u32 s[84:85], s50, v27\n"
    "v_cmp_eq_u32 s[86:87], s50, v28\n"
    "v_cmp_eq_u32 s[88:89], s50, v29\n"
    "v_cmp_eq_u32 s[52:53], s50, v30\n"
    "v_cmp_eq_u32 vcc, s50, v31\n"
    "global_load_dwordx4 v[24:27], v71, s[54:55] offset:0 sc1\n"
    "global_load_dwordx4 v[28:31], v71, s[54:55] offset:1024 sc1\n"
    "s_waitcnt lgkmcnt(0)\n"
    "s_barrier\n"
    "s_or_b64 vcc, vcc, s[78:79]\n"
    "s_or_b64 vcc, vcc, s[80:81]\n"
    "s_or_b64 vcc, vcc, s[82:83]\n"
    "s_or_b64 vcc, vcc, s[84:85]\n"
    "s_or_b64 vcc, vcc, s[86:87]\n"
    "s_or_b64 vcc, vcc, s[88:89]\n"
    "s_or_b64 vcc, vcc, s[52:53]\n"
    "s_cbranch_scc1 L_redo3%=\n"
    "v_cmp_eq_u32 s[78:79], s50, v16\n"
    "v_cmp_eq_u32 s[80:81], s50, v17\n"
    "v_cmp_eq_u32 s[82:83], s50, v18\n"
    "v_cmp_eq_u32 s[84:85], s50, v19\n"
    "v_cmp_eq_u32 s[86:87], s50, v8\n"
    "v_cmp_eq_u32 s[88:89], s50, v9\n"
    "v_cmp_eq_u32 s[52:53], s50, v10\n"
    "v_cmp_eq_u32 vcc, s50, v11\n"
    "s_nop 0\n"
    "s_or_b64 vcc, vcc, s[78:79]\n"
    "s_or_b64 vcc, vcc, s[80:81]\n"
    "s_or_b64 vcc, vcc, s[82:83]\n"
    "s_or_b64 vcc, vcc, s[84:85]\n"
    "s_or_b64 vcc, vcc, s[86:87]\n"
    "s_or_b64 vcc, vcc, s[88:89]\n"
    "s_or_b64 vcc, vcc, s[52:53]\n"
    "s_cbranch_scc1 L_redo3%=\n"
    "v_cmp_eq_u32 s[78:79], s50, v0\n"
    "v_cmp_eq_u32 s[80:81], s50, v1\n"
    "v_cmp_eq_u32 s[82:83], s50, v2\n"
    "v_cmp_eq_u32 s[84:85], s50, v3\n"
    "v_cmp_eq_u32 s[86:87], s50, v20\n"
    "v_cmp_eq_u32 s[88:89], s50, v21\n"
    "v_cmp_eq_u32 s[52:53], s50, v22\n"
    "v_cmp_eq_u32 vcc, s50, v23\n"
    "s_nop 0\n"
    "s_or_b64 vcc, vcc, s[78:79]\n"
    "s_or_b64 vcc, vcc, s[80:81]\n"
    "s_or_b64 vcc, vcc, s[82:83]\n"
    "s_or_b64 vcc, vcc, s[84:85]\n"
    "s_or_b64 vcc, vcc, s[86:87]\n"
    "s_or_b64 vcc, vcc, s[88:89]\n"
    "s_or_b64 vcc, vcc, s[52:53]\n"
    "s_cbranch_scc1 L_redo3%=\n"
    "v_cmp_eq_u32 s[78:79], s50, v12\n"
    "v_cmp_eq_u32 s[80:81], s50, v13\n"
    "v_cmp_eq_u32 s[82:83], s50, v14\n"
    "v_cmp_eq_u32 s[84:85], s50, v15\n"
    "v_cmp_eq_u32 s[86:87], s50, v4\n"
    "v_cmp_eq_u32 s[88:89], s50, v5\n"
    "v_cmp_eq_u32 s[52:53], s50, v6\n"
    "v_cmp_eq_u32 vcc, s50, v7\n"
    "s_nop 0\n"
    "s_or_b64 vcc, vcc, s[78:79]\n"
    "s_or_b64 vcc, vcc, s[80:81]\n"
    "s_or_b64 vcc, vcc, s[82:83]\n"
    "s_or_b64 vcc, vcc, s[84:85]\n"
    "s_or_b64 vcc, vcc, s[86:87]\n"
    "s_or_b64 vcc, vcc, s[88:89]\n"
    "s_or_b64 vcc, vcc, s[52:53]\n"
    "s_cbranch_scc1 L_redo3%=\n"
    "s_waitcnt vmcnt(0)\n"
    "s_branch L_go3%=\n"
    "L_redo3%=:\n"
    "ds_read_b128 a[48:51], v116 offset:16384\n"
    "ds_read_b128 a[52:55], v116 offset:17408\n"
    "ds_read_b128 a[112:115], v116 offset:18432\n"
    "ds_read_b128 a[116:119], v116 offset:19456\n"
    "ds_read_b128 a[56:59], v116 offset:20480\n"
    "ds_read_b128 a[60:63], v116 offset:21504\n"
    "ds_read_b128 a[120:123], v116 offset:22528\n"
    "ds_read_b128 a[124:127], v116 offset:23552\n"
    "ds_read_b128 a[32:35], v116 offset:24576\n"
    "ds_read_b128 a[36:39], v116 offset:25600\n"
    "ds_read_b128 a[96:99], v116 offset:26624\n"
    "ds_read_b128 a[100:103], v116 offset:27648\n"
    "ds_read_b128 a[16:19], v116 offset:28672\n"
    "ds_read_b128 a[20:23], v116 offset:29696\n"
    "s_waitcnt lgkmcnt(8)\n"
    "ds_read_b128 a[80:83], v116 offset:30720\n"
    "ds_read_b128 a[84:87], v116 offset:31744\n"
    "v_mov_b64 v[32:33], 0\n"
    "v_mov_b64 v[34:35], 0\n"
    "v_mov_b64 v[36:37], 0\n"
    "v_mov_b64 v[38:39], 0\n"
    "v_mov_b64 v[40:41], 0\n"
    "v_mov_b64 v[42:43], 0\n"
    "v_mov_b64 v[44:45], 0\n"
    "v_mov_b64 v[46:47], 0\n"
    "v_mov_b64 v[48:49], 0\n"
    "v_mov_b64 v[50:51], 0\n"
    "v_mov_b64 v[52:53], 0\n"
    "v_mov_b64 v[54:55], 0\n"
    "s_add_u32 s90, s90, 1\n"
    "s_and_b32 s78, s90, 0xff\n"
    "s_cmp_lg_u32 s78, 0\n"
    "s_cbranch_scc1 L_re3%=\n"
    "v_mov_b32 v126, 0\n"
    "global_load_dwordx2 v[126:127], v126, s[68:69] sc1\n"
    "s_waitcnt vmcnt(0)\n"
    "v_cmp_eq_u64 vcc, s[96:97], v[126:127]\n"
    "s_nop 1\n"
    "s_cbranch_vccnz L_bail_g3%=\n"
    "s_cmp_lt_u32 s90, 0x40000\n"
    "s_cbranch_scc1 L_re3%=\n"
    "v_mov_b32 v126, s96\n"
    "v_mov_b32 v127, s97\n"
    "s_mov_b64 exec, 1\n"
    "global_store_dwordx2 v71, v[126:127], s[68:69] sc1\n"
    "s_mov_b64 exec, -1\n"
    "L_bail_g3%=:\n"
    "s_branch L_go3%=\n"
    "L_re3%=:\n"
    "s_waitcnt vmcnt(0) lgkmcnt(0)\n"
    "s_branch L_top3%=\n"
    "L_last3%=:\n"
    "v_smfmac_f32_16x16x64_bf16 v[52:55], v[20:23], a[232:239], v67\n"
    "v_smfmac_f32_16x16x64_bf16 v[48:51], v[12:15], a[152:159], v67\n"
    "v_smfmac_f32_16x16x64_bf16 v[52:55], v[12:15], a[216:223], v67\n"
    "v_smfmac_f32_16x16x64_bf16 v[48:51], v[4:7], a[136:143], v67\n"
    "v_smfmac_f32_16x16x64_bf16 v[52:55], v[4:7], a[200:207], v67\n"
    "s_nop 3\n"
    "v_pk_add_f32 v[48:49], v[48:49], v[50:51]\n"
    "s_nop 2\n"
    "v_pk_add_f32 v[52:53], v[52:53], v[54:55]\n"
    "v_cvt_pk_bf16_f32 v66, v48, v49\n"
    "v_cvt_pk_bf16_f32 v74, v52, v53\n"
    "s_add_u32 s62, s62, 0x20000\n"
    "s_addc_u32 s63, s63, 0\n"
    "s_cmp_lg_u32 s42, 0\n"
    "s_cbranch_scc1 L_vmc3%=\n"
    "s_mov_b32 exec_lo, 0\n"
    "global_store_dwordx4 v71, v[44:47], s[62:63] offset:1024 sc1\n"
    "s_mov_b64 exec, -1\n"
    "s_branch L_kvc3%=\n"
    "L_vmc3%=:\n"
    "v_mov_b32 v126, v66\n"
    "v_mov_b32 v127, v74\n"
    "global_store_dwordx2 v79, v[126:127], s[62:63] offset:1536 sc1\n"
    "L_kvc3%=:\n"
    "s_waitcnt vmcnt(0) lgkmcnt(0)\n"
    "s_barrier\n"
    "s_endpgm\n"
    :: "s"(ws), "s"(__builtin_amdgcn_readfirstlane(by * 4 + bx)), "s"(wave), "s"(ldsb), "s"(ws + OFF_EP), "s"(Wq), "s"(Wk), "s"(Wv), "s"(Wo) PADOP
    : "memory", "vcc", "v255", "a255", "s34", "s35", "s36", "s37", "s38", "s39", "s40", "s96", "s97", "s99", "s22", "s23", "s24", "s25", "s26", "s27", "s28", "s29", "scc");
  __builtin_unreachable();
}

extern "C" void kernel_launch(void* const* d_in, const int* in_sizes, int n_in, void* d_out, int out_size, void* d_ws, size_t ws_size,
                              hipStream_t stream) {
  const u16* x = (const u16*)d_in[0];
  const u16* mem0 = (const u16*)d_in[1];
  const u16* Wq = (const u16*)d_in[2];
  const u16* Wk = (const u16*)d_in[3];
  const u16* Wv = (const u16*)d_in[4];
  const u16* Wo = (const u16*)d_in[5];
  const u16* w_r = (const u16*)d_in[8];
  const u16* w_gate = (const u16*)d_in[9];
  const u16* w_up = (const u16*)d_in[10];
  const u16* w_down = (const u16*)d_in[11];
  u16* out = (u16*)d_out;
  char* ws = (char*)(((size_t)d_ws + 2 * MBY - 1) & ~(size_t)(2 * MBY - 1));
  rec_kernel<<<256, 256, 0, stream>>>(ws, x, mem0, Wq, Wk, Wv, Wo, w_r, w_gate, w_up, w_down, out);
}
```

```cpp
#define WIDE
#include <hip/hip_runtime.h>
#include <hip/hip_bf16.h>

typedef unsigned short u16;
typedef unsigned long long u64;
typedef __attribute__((ext_vector_type(8))) short bf16x8;
typedef __attribute__((ext_vector_type(4))) float f32x4;
typedef __attribute__((ext_vector_type(4))) unsigned u32x4;
#define GAS __attribute__((address_space(1)))
typedef GAS u64 gu64;
typedef GAS unsigned gu32;
#define RLX_AGENT __ATOMIC_RELAXED, __HIP_MEMORY_SCOPE_AGENT

constexpr int S_ = 2048, DM = 512, NH = 32, DH = 128, MM = 8, SEG = 32, NSEG = 64;
constexpr int HD = NH * DH;
constexpr int QKVN = 3 * HD;
constexpr int FF = 1792;

__device__ __forceinline__ float bf2f(u16 v) { return __builtin_bit_cast(float, (unsigned)v << 16); }
__device__ __forceinline__ float rbf(float f) { return (float)(__bf16)f; }

template <int CTRL> __device__ __forceinline__ float dpp_add(float v) {
  return v + __builtin_bit_cast(float, __builtin_amdgcn_update_dpp(0, __builtin_bit_cast(int, v), CTRL, 0xf, 0xf, false));
}
__device__ __forceinline__ float wave_sum(float v) {
  v = dpp_add<0x121>(v); v = dpp_add<0x122>(v); v = dpp_add<0x124>(v); v = dpp_add<0x128>(v);
  auto r = __builtin_amdgcn_permlane16_swap(__builtin_bit_cast(unsigned, v), __builtin_bit_cast(unsigned, v), false, false);
  const unsigned r0 = r[0], r1 = r[1];
  v = __builtin_bit_cast(float, r0) + __builtin_bit_cast(float, r1);
  auto q = __builtin_amdgcn_permlane32_swap(__builtin_bit_cast(unsigned, v), __builtin_bit_cast(unsigned, v), false, false);
  const unsigned q0 = q[0], q1 = q[1];
  return __builtin_bit_cast(float, q0) + __builtin_bit_cast(float, q1);
}
typedef __bf16 bf16x2_t __attribute__((ext_vector_type(2)));
typedef float f32x2_t __attribute__((ext_vector_type(2)));
__device__ __forceinline__ unsigned pk2(float a, float b) {
  f32x2_t v; v.x = a; v.y = b;
  return __builtin_bit_cast(unsigned, __builtin_convertvector(v, bf16x2_t));
}
__device__ __forceinline__ void rbf2(float& a, float& b) {
  const unsigned p = pk2(a, b);
  a = __builtin_bit_cast(float, p << 16); b = __builtin_bit_cast(float, p & 0xffff0000u);
}
__device__ __forceinline__ void ln_row(const float (&x)[8], float (&y)[8]) {
  float s = ((x[0] + x[1]) + (x[2] + x[3])) + ((x[4] + x[5]) + (x[6] + x[7]));
  s = wave_sum(s);
  const float mean = s * (1.0f / DM);
  float q = 0.f;
#pragma unroll
  for (int i = 0; i < 8; ++i) { float d = x[i] - mean; q += d * d; }
  q = wave_sum(q);
  float var = q * (1.0f / DM), mb = mean;
  rbf2(var, mb);
  const float sd = rbf(__builtin_amdgcn_sqrtf(rbf(var + 1e-5f)));
  const float inv = __builtin_amdgcn_rcpf(sd);
#pragma unroll
  for (int i = 0; i < 8; i += 2) {
    float a = x[i] - mb, b = x[i + 1] - mb;
    rbf2(a, b);
    a *= inv; b *= inv;
    rbf2(a, b);
    y[i] = a; y[i + 1] = b;
  }
}

constexpr size_t MBY = 1u << 20;
constexpr size_t OFF_G2 = 0, OFF_G3 = MBY, OFF_SYNC = 2 * MBY;
constexpr size_t OFF_EP = OFF_SYNC, OFF_ERR = OFF_SYNC + 512, OFF_RDY = OFF_SYNC + 0x1000, OFF_ACT = OFF_SYNC + 0x140000, OFF_RC2 = OFF_SYNC + 0x128000;
constexpr size_t OFF_CLM = OFF_SYNC + 0x10000, OFF_TAB = OFF_SYNC + 0xC000, OFF_XFL = OFF_SYNC + 0xE000;
constexpr size_t OFF_PIL = OFF_G3 + 0x80000, OFF_FLAGS = OFF_SYNC + 0x22000, OFF_OFL = OFF_SYNC + 0xA2000, OFF_HFL = OFF_SYNC + 0xE2000;
constexpr size_t OFF_RC = OFF_SYNC + 0x102000, OFF_PAR = OFF_SYNC + 0x112000;
constexpr unsigned SENT = 0xFFBFFFBFu;
#define BB 12
constexpr size_t BSZ = 32 * MBY;
__device__ __forceinline__ int blk(int s, int i, unsigned far) { return s * 524288 + i * 16384 + (int)(far ^ ((s & 4) << 11)); }
constexpr size_t OFF_XN = 4 * MBY, OFF_KF = BB * MBY, OFF_VF = OFF_KF + BSZ, OFF_QF = OFF_VF + BSZ, OFF_KM = OFF_QF + BSZ;
constexpr size_t OFF_O = OFF_KM + BSZ, OFF_HP = OFF_O + BSZ;

__device__ __forceinline__ unsigned tl_idx() { unsigned xcc; asm volatile("s_getreg_b32 %0, hwreg(HW_REG_XCC_ID)" : "=s"(xcc)); return (blockIdx.x & ~7u) | (xcc & 7u); }
#define GS(i) do {} while (0)
#define TS2(i) do {} while (0)
#define TSD() do {} while (0)
#define TL(i) do {} while (0)
#define TLP() do {} while (0)
#define TLX(i) do {} while (0)
#define PT(k, c) do {} while (0)
#define PTD do {} while (0)
#define PTE do {} while (0)
__device__ __forceinline__ u64 now_clk() { u64 t; asm volatile("s_memtime %0\n\ts_waitcnt lgkmcnt(0)" : "=s"(t) :: "memory"); return t; }

constexpr int NREC = 64;

__device__ __forceinline__ bool spin_bail(unsigned& spins, char* ws, u64 T) {
  ++spins;
  if ((spins & 255u) == 0u) {
    gu64* err = (gu64*)(ws + OFF_ERR);
    if (__hip_atomic_load(err, RLX_AGENT) == T) return true;
    if (spins > (1u << 20)) { __hip_atomic_store(err, T, RLX_AGENT); return true; }
  }
  return false;
}
__device__ __forceinline__ void set_flag(char* ws, size_t off, u64 T, unsigned v) { __hip_atomic_store((gu64*)(ws + off), T + v, RLX_AGENT); }
__device__ __forceinline__ bf16x8 mk8(unsigned a, unsigned b, unsigned c, unsigned d) {
  u32x4 v; v.x = a; v.y = b; v.z = c; v.w = d; return __builtin_bit_cast(bf16x8, v);
}
__device__ __forceinline__ void store16_sc1(const void* base, unsigned off, u32x4 v) {
  asm volatile("global_store_dwordx4 %0, %1, %2 sc1\n\ts_nop 1" :: "v"(off), "v"(v), "s"(base) : "memory");
}

#define NSPEC (58 * 32)
#define ASL 24
#define QSL 1
#define ADL 0
#define ATN 0
#define QDL 90
#define SLATE 56
#define NKP 6
#define WARMX 2
#define WDL 127
#define HFP 0
#define OFNT 2
#define NSIX 48
#define XROT 0
#define NPROBE 14
#define PSL 1
#define HSL 6
#define LAS __attribute__((address_space(3)))
__device__ __forceinline__ void unpack8(const u32x4 v, float (&f)[8]) {
  f[0] = bf2f(v.x & 0xffff); f[1] = bf2f(v.x >> 16); f[2] = bf2f(v.y & 0xffff); f[3] = bf2f(v.y >> 16);
  f[4] = bf2f(v.z & 0xffff); f[5] = bf2f(v.z >> 16); f[6] = bf2f(v.w & 0xffff); f[7] = bf2f(v.w >> 16);
}
template <int SL>
__device__ __forceinline__ void wait_flag(char* ws, unsigned off, u64 T, unsigned target) {
  gu64* p = (gu64*)(ws + off);
  unsigned spins = 0;
  while (!__all(__hip_atomic_load(p, RLX_AGENT) - T - target < 64u - target)) {
    __builtin_amdgcn_s_sleep(SL);
    if (spin_bail(spins, ws, T)) break;
  }
  __builtin_amdgcn_fence(__ATOMIC_ACQUIRE, "wavefront");
}
__device__ __forceinline__ bool marked(const u32x4 v) { return v.x == SENT || v.y == SENT || v.z == SENT || v.w == SENT; }
typedef __attribute__((ext_vector_type(4))) short s16x4;
__device__ __forceinline__ bf16x8 tr_frag(const char* p0, const char* p1) {
  const s16x4 a = __builtin_amdgcn_ds_read_tr16_b64_v4i16((LAS s16x4*)p0), b = __builtin_amdgcn_ds_read_tr16_b64_v4i16((LAS s16x4*)p1);
  return __builtin_shufflevector(a, b, 0, 1, 2, 3, 4, 5, 6, 7);
}

__device__ __forceinline__ float sum_g(float v) {
  auto r = __builtin_amdgcn_permlane16_swap(__builtin_bit_cast(unsigned, v), __builtin_bit_cast(unsigned, v), false, false);
  const unsigned r0 = r[0], r1 = r[1];
  v = __builtin_bit_cast(float, r0) + __builtin_bit_cast(float, r1);
  auto q = __builtin_amdgcn_permlane32_swap(__builtin_bit_cast(unsigned, v), __builtin_bit_cast(unsigned, v), false, false);
  const unsigned q0 = q[0], q1 = q[1];
  return __builtin_bit_cast(float, q0) + __builtin_bit_cast(float, q1);
}
__device__ __forceinline__ float max_g(float v) {
  auto r = __builtin_amdgcn_permlane16_swap(__builtin_bit_cast(unsigned, v), __builtin_bit_cast(unsigned, v), false, false);
  const unsigned r0 = r[0], r1 = r[1];
  v = fmaxf(__builtin_bit_cast(float, r0), __builtin_bit_cast(float, r1));
  auto q = __builtin_amdgcn_permlane32_swap(__builtin_bit_cast(unsigned, v), __builtin_bit_cast(unsigned, v), false, false);
  const unsigned q0 = q[0], q1 = q[1];
  return fmaxf(__builtin_bit_cast(float, q0), __builtin_bit_cast(float, q1));
}
__device__ __forceinline__ void ln_tile(u32x4 (&xr)[8]) {
  auto up = [](unsigned u) { f32x2_t v; v.x = __builtin_bit_cast(float, u << 16); v.y = __builtin_bit_cast(float, u & 0xffff0000u); return v; };
  f32x2_t f[8][4], sb[4] = {{0.f, 0.f}, {0.f, 0.f}, {0.f, 0.f}, {0.f, 0.f}};
#pragma unroll
  for (int k = 0; k < 8; ++k)
#pragma unroll
    for (int e = 0; e < 4; ++e) { const unsigned u = xr[k][e]; f[k][e] = up(u); sb[e] += f[k][e]; }
  const f32x2_t sa = (sb[0] + sb[1]) + (sb[2] + sb[3]);
  const float mean = sum_g(dpp_add<0x128>(sa.x + sa.y)) * (1.0f / DM);
  f32x2_t qb[4] = {{0.f, 0.f}, {0.f, 0.f}, {0.f, 0.f}, {0.f, 0.f}};
#pragma unroll
  for (int k = 0; k < 8; ++k)
#pragma unroll
    for (int e = 0; e < 4; ++e) { const f32x2_t d = f[k][e] - mean; qb[e] += d * d; }
  const f32x2_t qa = (qb[0] + qb[1]) + (qb[2] + qb[3]);
  float var = sum_g(dpp_add<0x128>(qa.x + qa.y)) * (1.0f / DM), mb = mean;
  rbf2(var, mb);
  const float inv = __builtin_amdgcn_rcpf(rbf(__builtin_amdgcn_sqrtf(rbf(var + 1e-5f))));
#pragma unroll
  for (int k = 0; k < 8; ++k)
#pragma unroll
    for (int e = 0; e < 4; ++e) {
      const f32x2_t d = f[k][e] - mb;
      const f32x2_t y = up(pk2(d.x, d.y)) * inv;
      xr[k][e] = pk2(y.x, y.y);
    }
}
__device__ __forceinline__ unsigned pgc(unsigned c) { return ((c & 1u) << 2) | (c >> 1); }
__device__ __forceinline__ unsigned probe(char* ws, int q, unsigned c, int lane) {
  const char* p = ws + OFF_G3 + 0xC0000 + (q & 7) * 32768 + q * 128;
  unsigned best = ~0u, near = 0, z = 0;
  asm volatile("global_atomic_or %0, %1, %0, %2 sc0 sc1\n\ts_waitcnt vmcnt(0)" : "+v"(z) : "v"((lane & 7) * 4096), "s"(p) : "memory");
#pragma unroll 1
  for (int i = 0; i < NPROBE; ++i) {
    if (i == 4) {
      unsigned m[2] = {~0u, ~0u};
#pragma unroll
      for (int k = 0; k < 8; ++k) m[(k >> 1) & 1] = min(m[(k >> 1) & 1], (unsigned)__builtin_amdgcn_readlane((int)best, k));
      near = m[1] < m[0] ? 2u : 0u;
    }
    const unsigned j = i + q, pg = i < 4 ? (pgc(j & 3u) | (((j ^ (j >> 1) ^ (q >> 2)) & 1u) << 1)) : (pgc(j & 3u) | (i < NPROBE - 2 ? near : near ^ 2u));
    u64 t0, t1;
    asm volatile("s_mov_b64 exec, 1\n\tv_mov_b32 %0, 0\n\ts_memtime %1\n\ts_waitcnt lgkmcnt(0)\n\tglobal_atomic_or %0, %0, %0, %3 sc0 sc1\n\ts_waitcnt vmcnt(0)\n\t"
                 "s_memtime %2\n\ts_waitcnt lgkmcnt(0)\n\ts_mov_b64 exec, -1" : "=&v"(z), "=&s"(t0), "=&s"(t1) : "s"(p + pg * 4096) : "memory");
    if (lane == pg) best = min(best, (unsigned)(t1 - t0));
  }
  const unsigned a = __builtin_amdgcn_readlane((int)best, near), b = __builtin_amdgcn_readlane((int)best, near | 1), d = __builtin_amdgcn_readlane((int)best, near | 4), e = __builtin_amdgcn_readlane((int)best, near | 5);
  unsigned o = ~0u;
#pragma unroll
  for (int k = 0; k < 4; ++k) o = min(o, (unsigned)__builtin_amdgcn_readlane((int)best, pgc(k) | (near ^ 2u)));
  if (o < min(min(a, b), min(d, e))) return 9u + (near ^ 2u);
  return 1u + ((near | (d + e < a + b ? 4u : 0u) | (b + e < a + d ? 1u : 0u)) ^ pgc(c) ^ 5u);
}
__device__ __forceinline__ u64 uni(u64 v) { return ((u64)(unsigned)__builtin_amdgcn_readfirstlane((unsigned)(v >> 32)) << 32) | (u64)(unsigned)__builtin_amdgcn_readfirstlane((unsigned)v); }
__device__ __forceinline__ u64 tag_of(u64 ep) { return (ep + 1) << 6; }
__device__ __forceinline__ void ln1_phase(char* ws, int q, u64 T, char* lds, int tid) {
  const int lane = tid & 63, wv = tid >> 6;
  u32x4 xr[8];
#pragma unroll
  for (int k = 0; k < 8; ++k) xr[k] = *(const u32x4*)(lds + (wv * 8 + (lane & 7)) * 1040 + (lane >> 3) * 128 + k * 16);
  ln_tile(xr);
#pragma unroll
  for (int k = 0; k < 8; ++k) store16_sc1(ws + OFF_XN, (unsigned)((q * 2 + (wv >> 1)) * 16384 + ((lane >> 3) * 8 + k) * 256 + ((wv & 1) * 8 + (lane & 7)) * 16), xr[k]);
  if (wv < 2) store16_sc1(ws, (unsigned)((wv == 0 ? OFF_RC : OFF_RC2) + q * 1024 + lane * 16), (u32x4){0u, 0u, 0u, 0u});
  store16_sc1(ws + OFF_ACT, (unsigned)(q * 4096 + tid * 16), (u32x4){SENT, SENT, SENT, SENT});
  asm volatile("s_waitcnt vmcnt(0)" ::: "memory");
  __syncthreads();
  if (tid == 0) set_flag(ws, OFF_XFL + q * 128, T, 1);
  TL(6);
}

__device__ __forceinline__ bool load_h(const __amdgpu_buffer_rsrc_t rs, const u32x4 xv, int row, unsigned far, int lane, float (&f)[8]) {
  f32x4 p[4][2];
#pragma unroll
  for (int k = 0; k < 4; ++k)
#pragma unroll
    for (int u = 0; u < 2; ++u)
      p[k][u] = __builtin_bit_cast(f32x4, __builtin_amdgcn_raw_buffer_load_b128(rs, (int)OFF_HP + blk(row >> 5, (k * 4 + (lane >> 4)) * 2 + ((row >> 4) & 1), far) + (row & 15) * 512 + (lane & 15) * 32 + u * 16, 0, 16));
  float xf[8];
  unpack8(xv, xf);
#pragma unroll
  for (int i = 0; i < 8; ++i) f[i] = rbf(xf[i] + rbf((p[0][i >> 2][i & 3] + p[1][i >> 2][i & 3]) + (p[2][i >> 2][i & 3] + p[3][i >> 2][i & 3])));
  bool mk = false;
  if (__any(__builtin_isunordered(f[0] + f[1], f[2] + f[3]) || __builtin_isunordered(f[4] + f[5], f[6] + f[7]))) {
#pragma unroll
    for (int k = 0; k < 8; ++k) mk |= marked(__builtin_bit_cast(u32x4, p[k >> 1][k & 1]));
  }
  return __any(mk);
}

__device__ __forceinline__ unsigned wait_par(char* ws, u64 T, int lane) {
  unsigned spins = 0; u64 p;
  for (;;) {
    p = __hip_atomic_load((gu64*)(ws + OFF_PAR + min(lane, 10) * 128), RLX_AGENT) - T - 1;
    if (__all(p < 16)) break;
    __builtin_amdgcn_s_sleep(8);
    if (spin_bail(spins, ws, T)) break;
  }
  unsigned psi = __builtin_popcount((unsigned)__ballot(p & 2) & 0x7ffu) >= 6 ? 2u : 0u;
  const unsigned use = (unsigned)__ballot((p & 10) == psi) & 0x7ffu;
#pragma unroll
  for (int k = 1; k < 8; k *= 4) psi |= 2 * __builtin_popcount((unsigned)__ballot(p & k) & use) > __builtin_popcount(use) ? k : 0;
  return psi;
}
#define GCLOB "memory", "vcc", "scc", "s40", "s41", "s42", "s43", "s44", "s45", "s46", "s47", "s48", "s49", "s50", "s51", "s52", "s53", "s54", "s55", "s56", "s57", "s58", "s59", "s60", "s61", "s62", "s63", "s64", "s65", "s66", "s67", "s68", "s69", "s70", "s71", "v109", "v110", "v111", "v112", "v113", "v114", "v115", "v116", "v117", "v118", "v119", "v120", "v121", "v122", "v123", "v124", "v125", "v126", "v127", "v128", "v129", "v130", "v131", "v132", "v133", "v134", "v135", "v136", "v137", "v138", "v139", "v140", "v141", "v142", "v143", "v144", "v145", "v146", "v147", "v148", "v149", "v150", "v151", "v152", "v153", "v154", "v155", "v156", "v157", "v158", "v159", "v160", "v161", "v162", "v163", "v164", "v165", "v166", "v167", "v168", "v169", "v170", "v171", "v172", "v173", "v174", "v175", "v176", "v177", "v178", "v179", "v180", "v181", "v182", "v183", "v184", "v185", "v186", "v187", "v188", "v189", "v190", "v191", "v192", "v193", "v194", "v195", "v196", "v197", "v198", "v199", "v200", "v201", "v202", "v203", "v204", "v205", "v206", "v207", "v208", "v209", "v210", "v211", "v212", "v213", "v214", "v215", "v216", "v217", "v218", "v219", "v220", "v221", "v222", "v223", "v224", "v225", "v226", "v227", "v228", "v229", "v230", "v231", "v232", "v233", "v234", "v235", "v236", "v237", "v238", "v239", "v240", "v241", "v242", "v243", "v244", "v245", "v246", "v247", "v248", "v249", "v250", "v251", "v252", "v253", "v254", "v255", "a0", "a1", "a2", "a3", "a4", "a5", "a6", "a7", "a8", "a9", "a10", "a11", "a12", "a13", "a14", "a15", "a16", "a17", "a18", "a19", "a20", "a21", "a22", "a23", "a24", "a25", "a26", "a27", "a28", "a29", "a30", "a31", "a32", "a33", "a34", "a35", "a36", "a37", "a38", "a39", "a40", "a41", "a42", "a43", "a44", "a45", "a46", "a47", "a48", "a49", "a50", "a51", "a52", "a53", "a54", "a55", "a56", "a57", "a58", "a59", "a60", "a61", "a62", "a63", "a64", "a65", "a66", "a67", "a68", "a69", "a70", "a71", "a72", "a73", "a74", "a75", "a76", "a77", "a78", "a79", "a80", "a81", "a82", "a83", "a84", "a85", "a86", "a87", "a88", "a89", "a90", "a91", "a92", "a93", "a94", "a95", "a96", "a97", "a98", "a99", "a100", "a101", "a102", "a103", "a104", "a105", "a106", "a107", "a108", "a109", "a110", "a111", "a112", "a113", "a114", "a115", "a116", "a117", "a118", "a119", "a120", "a121", "a122", "a123", "a124", "a125", "a126", "a127", "a128", "a129", "a130", "a131", "a132", "a133", "a134", "a135", "a136", "a137", "a138", "a139", "a140", "a141", "a142", "a143", "a144", "a145", "a146", "a147", "a148", "a149", "a150", "a151", "a152", "a153", "a154", "a155", "a156", "a157", "a158", "a159", "a160", "a161", "a162", "a163", "a164", "a165", "a166", "a167", "a168", "a169", "a170", "a171", "a172", "a173", "a174", "a175", "a176", "a177", "a178", "a179", "a180", "a181", "a182", "a183", "a184", "a185", "a186", "a187", "a188", "a189", "a190", "a191", "a192", "a193", "a194", "a195", "a196", "a197", "a198", "a199", "a200", "a201", "a202", "a203", "a204", "a205", "a206", "a207", "a208", "a209", "a210", "a211", "a212", "a213", "a214", "a215", "a216", "a217", "a218", "a219", "a220", "a221", "a222", "a223", "a224", "a225", "a226", "a227", "a228", "a229", "a230", "a231", "a232", "a233", "a234", "a235", "a236", "a237", "a238", "a239", "a240", "a241", "a242", "a243", "a244", "a245", "a246", "a247", "a248", "a249", "a250", "a251", "a252", "a253", "a254", "a255"
template <int MAT>
__device__ __forceinline__ u64 qkv_phase(char* ws, int h, u64 ep0, int tid) {
  const int lane = tid & 63, wv = __builtin_amdgcn_readfirstlane(tid >> 6), l15 = lane & 15, g = lane >> 4;
  const int wr = wv >> 1, wc = wv & 1;
  TLX(512 + (MAT * 32 + h) * 16 + 1);
  const u64 T = tag_of(ep0);
  TLX(512 + (MAT * 32 + h) * 16 + 2);
  const int qq = l15 >> 2, f = qq | ((g & 1) << 2);
  const unsigned rp = (g * 8 + qq) * 256 + (l15 & 3) * 8 + ((wc * 4) ^ f) * 32;
  char* op = ws + (MAT == 0 ? OFF_QF : (MAT == 1 ? OFF_KF : OFF_VF)) + blk(wr, h, 0) + wc * 4096;
  char* rdy = ws + OFF_RDY + (MAT * 32 + h) * 128;
  asm volatile("s_waitcnt vmcnt(0)\n\ts_barrier" ::: "memory");
#define GIN "s"(ws), "s"(uni(T)), "s"(op), "s"(rdy), "s"(wv), "v"(lane * 16), "v"(rp)
  if constexpr (MAT == 0) asm volatile(
    "s_mov_b64 s[40:41], %0\n"
    "s_mov_b64 s[42:43], %1\n"
    "s_mov_b64 s[44:45], %2\n"
    "s_mov_b64 s[54:55], %3\n"
    "s_mov_b32 s56, %4\n"
    "v_mov_b32 v121, %5\n"
    "s_add_u32 s58, s40, 0x20e000\n"
    "s_addc_u32 s59, s41, 0\n"
    "v_lshlrev_b32 v225, 3, v121\n"
    "v_and_b32 v224, 0x380, v225\n"
    "global_load_dwordx2 v[226:227], v224, s[58:59] sc1\n"
    "v_xor_b32 v112, 0, %6\n"
    "v_xor_b32 v113, 32, %6\n"
    "v_xor_b32 v114, 64, %6\n"
    "v_xor_b32 v115, 96, %6\n"
    "ds_read_b64_tr_b16 a[0:1], v112 offset:0\n"
    "ds_read_b64_tr_b16 a[2:3], v112 offset:1024\n"
    "ds_read_b64_tr_b16 a[4:5], v112 offset:8192\n"
    "ds_read_b64_tr_b16 a[6:7], v112 offset:9216\n"
    "ds_read_b64_tr_b16 a[8:9], v112 offset:16384\n"
    "ds_read_b64_tr_b16 a[10:11], v112 offset:17408\n"
    "ds_read_b64_tr_b16 a[12:13], v112 offset:24576\n"
    "ds_read_b64_tr_b16 a[14:15], v112 offset:25600\n"
    "s_waitcnt lgkmcnt(6)\n"
    "ds_read_b64_tr_b16 a[16:17], v112 offset:32768\n"
    "ds_read_b64_tr_b16 a[18:19], v112 offset:33792\n"
    "ds_read_b64_tr_b16 a[20:21], v112 offset:40960\n"
    "ds_read_b64_tr_b16 a[22:23], v112 offset:41984\n"
    "ds_read_b64_tr_b16 a[24:25], v112 offset:49152\n"
    "ds_read_b64_tr_b16 a[26:27], v112 offset:50176\n"
    "ds_read_b64_tr_b16 a[28:29], v112 offset:57344\n"
    "ds_read_b64_tr_b16 a[30:31], v112 offset:58368\n"
    "s_waitcnt lgkmcnt(6)\n"
    "ds_read_b64_tr_b16 a[64:65], v113 offset:0\n"
    "ds_read_b64_tr_b16 a[66:67], v113 offset:1024\n"
    "ds_read_b64_tr_b16 a[68:69], v113 offset:8192\n"
    "ds_read_b64_tr_b16 a[70:71], v113 offset:9216\n"
    "ds_read_b64_tr_b16 a[72:73], v113 offset:16384\n"
    "ds_read_b64_tr_b16 a[74:75], v113 offset:17408\n"
    "ds_read_b64_tr_b16 a[76:77], v113 offset:24576\n"
    "ds_read_b64_tr_b16 a[78:79], v113 offset:25600\n"
    "s_waitcnt lgkmcnt(6)\n"
    "ds_read_b64_tr_b16 a[80:81], v113 offset:32768\n"
    "ds_read_b64_tr_b16 a[82:83], v113 offset:33792\n"
    "ds_read_b64_tr_b16 a[84:85], v113 offset:40960\n"
    "ds_read_b64_tr_b16 a[86:87], v113 offset:41984\n"
    "ds_read_b64_tr_b16 a[88:89], v113 offset:49152\n"
    "ds_read_b64_tr_b16 a[90:91], v113 offset:50176\n"
    "ds_read_b64_tr_b16 a[92:93], v113 offset:57344\n"
    "ds_read_b64_tr_b16 a[94:95], v113 offset:58368\n"
    "s_waitcnt lgkmcnt(6)\n"
    "ds_read_b64_tr_b16 a[128:129], v114 offset:0\n"
    "ds_read_b64_tr_b16 a[130:131], v114 offset:1024\n"
    "ds_read_b64_tr_b16 a[132:133], v114 offset:8192\n"
    "ds_read_b64_tr_b16 a[134:135], v114 offset:9216\n"
    "ds_read_b64_tr_b16 a[136:137], v114 offset:16384\n"
    "ds_read_b64_tr_b16 a[138:139], v114 offset:17408\n"
    "ds_read_b64_tr_b16 a[140:141], v114 offset:24576\n"
    "ds_read_b64_tr_b16 a[142:143], v114 offset:25600\n"
    "s_waitcnt lgkmcnt(6)\n"
    "ds_read_b64_tr_b16 a[144:145], v114 offset:32768\n"
    "ds_read_b64_tr_b16 a[146:147], v114 offset:33792\n"
    "ds_read_b64_tr_b16 a[148:149], v114 offset:40960\n"
    "ds_read_b64_tr_b16 a[150:151], v114 offset:41984\n"
    "ds_read_b64_tr_b16 a[152:153], v114 offset:49152\n"
    "ds_read_b64_tr_b16 a[154:155], v114 offset:50176\n"
    "ds_read_b64_tr_b16 a[156:157], v114 offset:57344\n"
    "ds_read_b64_tr_b16 a[158:159], v114 offset:58368\n"
    "s_waitcnt lgkmcnt(6)\n"
    "ds_read_b64_tr_b16 a[192:193], v115 offset:0\n"
    "ds_read_b64_tr_b16 a[194:195], v115 offset:1024\n"
    "ds_read_b64_tr_b16 a[196:197], v115 offset:8192\n"
    "ds_read_b64_tr_b16 a[198:199], v115 offset:9216\n"
    "ds_read_b64_tr_b16 a[200:201], v115 offset:16384\n"
    "ds_read_b64_tr_b16 a[202:203], v115 offset:17408\n"
    "ds_read_b64_tr_b16 a[204:205], v115 offset:24576\n"
    "ds_read_b64_tr_b16 a[206:207], v115 offset:25600\n"
    "s_waitcnt lgkmcnt(6)\n"
    "ds_read_b64_tr_b16 a[208:209], v115 offset:32768\n"
    "ds_read_b64_tr_b16 a[210:211], v115 offset:33792\n"
    "ds_read_b64_tr_b16 a[212:213], v115 offset:40960\n"
    "ds_read_b64_tr_b16 a[214:215], v115 offset:41984\n"
    "ds_read_b64_tr_b16 a[216:217], v115 offset:49152\n"
    "ds_read_b64_tr_b16 a[218:219], v115 offset:50176\n"
    "ds_read_b64_tr_b16 a[220:221], v115 offset:57344\n"
    "ds_read_b64_tr_b16 a[222:223], v115 offset:58368\n"
    "s_waitcnt lgkmcnt(6)\n"
    "v_xor_b32 v112, 0, %6\n"
    "v_add_u32 v112, 0x10000, v112\n"
    "v_xor_b32 v113, 32, %6\n"
    "v_add_u32 v113, 0x10000, v113\n"
    "v_xor_b32 v114, 64, %6\n"
    "v_add_u32 v114, 0x10000, v114\n"
    "v_xor_b32 v115, 96, %6\n"
    "v_add_u32 v115, 0x10000, v115\n"
    "ds_read_b64_tr_b16 a[32:33], v112 offset:0\n"
    "ds_read_b64_tr_b16 a[34:35], v112 offset:1024\n"
    "ds_read_b64_tr_b16 a[36:37], v112 offset:8192\n"
    "ds_read_b64_tr_b16 a[38:39], v112 offset:9216\n"
    "ds_read_b64_tr_b16 a[40:41], v112 offset:16384\n"
    "ds_read_b64_tr_b16 a[42:43], v112 offset:17408\n"
    "ds_read_b64_tr_b16 a[44:45], v112 offset:24576\n"
    "ds_read_b64_tr_b16 a[46:47], v112 offset:25600\n"
    "s_waitcnt lgkmcnt(6)\n"
    "ds_read_b64_tr_b16 a[48:49], v112 offset:32768\n"
    "ds_read_b64_tr_b16 a[50:51], v112 offset:33792\n"
    "ds_read_b64_tr_b16 a[52:53], v112 offset:40960\n"
    "ds_read_b64_tr_b16 a[54:55], v112 offset:41984\n"
    "ds_read_b64_tr_b16 a[56:57], v112 offset:49152\n"
    "ds_read_b64_tr_b16 a[58:59], v112 offset:50176\n"
    "ds_read_b64_tr_b16 a[60:61], v112 offset:57344\n"
    "ds_read_b64_tr_b16 a[62:63], v112 offset:58368\n"
    "s_waitcnt lgkmcnt(6)\n"
    "ds_read_b64_tr_b16 a[96:97], v113 offset:0\n"
    "ds_read_b64_tr_b16 a[98:99], v113 offset:1024\n"
    "ds_read_b64_tr_b16 a[100:101], v113 offset:8192\n"
    "ds_read_b64_tr_b16 a[102:103], v113 offset:9216\n"
    "ds_read_b64_tr_b16 a[104:105], v113 offset:16384\n"
    "ds_read_b64_tr_b16 a[106:107], v113 offset:17408\n"
    "ds_read_b64_tr_b16 a[108:109], v113 offset:24576\n"
    "ds_read_b64_tr_b16 a[110:111], v113 offset:25600\n"
    "s_waitcnt lgkmcnt(6)\n"
    "ds_read_b64_tr_b16 a[112:113], v113 offset:32768\n"
    "ds_read_b64_tr_b16 a[114:115], v113 offset:33792\n"
    "ds_read_b64_tr_b16 a[116:117], v113 offset:40960\n"
    "ds_read_b64_tr_b16 a[118:119], v113 offset:41984\n"
    "ds_read_b64_tr_b16 a[120:121], v113 offset:49152\n"
    "ds_read_b64_tr_b16 a[122:123], v113 offset:50176\n"
    "ds_read_b64_tr_b16 a[124:125], v113 offset:57344\n"
    "ds_read_b64_tr_b16 a[126:127], v113 offset:58368\n"
    "s_waitcnt lgkmcnt(6)\n"
    "ds_read_b64_tr_b16 a[160:161], v114 offset:0\n"
    "ds_read_b64_tr_b16 a[162:163], v114 offset:1024\n"
    "ds_read_b64_tr_b16 a[164:165], v114 offset:8192\n"
    "ds_read_b64_tr_b16 a[166:167], v114 offset:9216\n"
    "ds_read_b64_tr_b16 a[168:169], v114 offset:16384\n"
    "ds_read_b64_tr_b16 a[170:171], v114 offset:17408\n"
    "ds_read_b64_tr_b16 a[172:173], v114 offset:24576\n"
    "ds_read_b64_tr_b16 a[174:175], v114 offset:25600\n"
    "s_waitcnt lgkmcnt(6)\n"
    "ds_read_b64_tr_b16 a[176:177], v114 offset:32768\n"
    "ds_read_b64_tr_b16 a[178:179], v114 offset:33792\n"
    "ds_read_b64_tr_b16 a[180:181], v114 offset:40960\n"
    "ds_read_b64_tr_b16 a[182:183], v114 offset:41984\n"
    "ds_read_b64_tr_b16 a[184:185], v114 offset:49152\n"
    "ds_read_b64_tr_b16 a[186:187], v114 offset:50176\n"
    "ds_read_b64_tr_b16 a[188:189], v114 offset:57344\n"
    "ds_read_b64_tr_b16 a[190:191], v114 offset:58368\n"
    "s_waitcnt lgkmcnt(6)\n"
    "ds_read_b64_tr_b16 a[224:225], v115 offset:0\n"
    "ds_read_b64_tr_b16 a[226:227], v115 offset:1024\n"
    "ds_read_b64_tr_b16 a[228:229], v115 offset:8192\n"
    "ds_read_b64_tr_b16 a[230:231], v115 offset:9216\n"
    "ds_read_b64_tr_b16 a[232:233], v115 offset:16384\n"
    "ds_read_b64_tr_b16 a[234:235], v115 offset:17408\n"
    "ds_read_b64_tr_b16 a[236:237], v115 offset:24576\n"
    "ds_read_b64_tr_b16 a[238:239], v115 offset:25600\n"
    "s_waitcnt lgkmcnt(6)\n"
    "ds_read_b64_tr_b16 a[240:241], v115 offset:32768\n"
    "ds_read_b64_tr_b16 a[242:243], v115 offset:33792\n"
    "ds_read_b64_tr_b16 a[244:245], v115 offset:40960\n"
    "ds_read_b64_tr_b16 a[246:247], v115 offset:41984\n"
    "ds_read_b64_tr_b16 a[248:249], v115 offset:49152\n"
    "ds_read_b64_tr_b16 a[250:251], v115 offset:50176\n"
    "ds_read_b64_tr_b16 a[252:253], v115 offset:57344\n"
    "ds_read_b64_tr_b16 a[254:255], v115 offset:58368\n"
    "s_waitcnt lgkmcnt(6)\n"
    "s_lshr_b32 s58, s56, 1\n"
    "s_lshl_b32 s58, s58, 15\n"
    "v_add_u32 v122, s58, v121\n"
    "v_add_u32 v123, 0x10000, v122\n"
    "s_lshl_b32 s50, s56, 14\n"
    "v_mov_b32 v118, 0\n"
    "v_mov_b32 v117, s43\n"
    "s_add_u32 s58, s50, 0x400000\n"
    "s_add_u32 s48, s40, s58\n"
    "s_addc_u32 s49, s41, 0\n"
    "s_mov_b32 s57, 0\n"
    "s_mov_b32 s51, 0\n"
    "s_waitcnt lgkmcnt(0)\n"
    "s_barrier\n"
    "s_add_u32 s58, s40, 0x20e000\n"
    "s_addc_u32 s59, s41, 0\n"
    "s_add_u32 s60, s40, 0x200200\n"
    "s_addc_u32 s61, s41, 0\n"
    "s_or_b32 s62, s42, 1\n"
    "s_mov_b32 s63, s43\n"
    "s_branch G_xc0%=\n"
    "G_xw0%=:\n"
    "global_load_dwordx2 v[226:227], v224, s[58:59] sc1\n"
    "G_xc0%=:\n"
    "s_waitcnt vmcnt(0)\n"
    "v_cmp_ne_u64 vcc, s[62:63], v[226:227]\n"
    "s_nop 0\n"
    "s_cbranch_vccz G_xok0%=\n"
    "s_sleep 4\n"
    "s_add_u32 s57, s57, 1\n"
    "s_and_b32 s68, s57, 0xff\n"
    "s_cbranch_scc1 G_xw0%=\n"
    "global_load_dwordx2 v[226:227], v118, s[60:61] sc1\n"
    "s_waitcnt vmcnt(0)\n"
    "v_cmp_eq_u64 vcc, s[42:43], v[226:227]\n"
    "s_nop 0\n"
    "s_cbranch_vccnz G_xok0%=\n"
    "s_cmp_lt_u32 s57, 0x40000\n"
    "s_cbranch_scc1 G_xw0%=\n"
    "v_mov_b32 v116, s42\n"
    "global_store_dwordx2 v118, v[116:117], s[60:61] sc1\n"
    "G_xok0%=:\n"
    "s_mov_b32 vcc_lo, 0xcccccccc\n"
    "s_mov_b32 vcc_hi, 0xcccccccc\n"
    "s_add_u32 m0, s50, 0\n"
    "s_add_u32 s64, s48, 0\n"
    "s_addc_u32 s65, s49, 0\n"
    "global_load_lds_dwordx4 v121, s[64:65] offset:0 sc1\n"
    "global_load_lds_dwordx4 v121, s[64:65] offset:1024 sc1\n"
    "global_load_lds_dwordx4 v121, s[64:65] offset:2048 sc1\n"
    "global_load_lds_dwordx4 v121, s[64:65] offset:3072 sc1\n"
    "s_add_u32 m0, s50, 4096\n"
    "s_add_u32 s64, s48, 4096\n"
    "s_addc_u32 s65, s49, 0\n"
    "global_load_lds_dwordx4 v121, s[64:65] offset:0 sc1\n"
    "global_load_lds_dwordx4 v121, s[64:65] offset:1024 sc1\n"
    "global_load_lds_dwordx4 v121, s[64:65] offset:2048 sc1\n"
    "global_load_lds_dwordx4 v121, s[64:65] offset:3072 sc1\n"
    "s_add_u32 m0, s50, 8192\n"
    "s_add_u32 s64, s48, 8192\n"
    "s_addc_u32 s65, s49, 0\n"
    "global_load_lds_dwordx4 v121, s[64:65] offset:0 sc1\n"
    "global_load_lds_dwordx4 v121, s[64:65] offset:1024 sc1\n"
    "global_load_lds_dwordx4 v121, s[64:65] offset:2048 sc1\n"
    "global_load_lds_dwordx4 v121, s[64:65] offset:3072 sc1\n"
    "s_add_u32 m0, s50, 12288\n"
    "s_add_u32 s64, s48, 12288\n"
    "s_addc_u32 s65, s49, 0\n"
    "s_add_u32 s48, s48, 0x10000\n"
    "s_addc_u32 s49, s49, 0\n"
    "global_load_lds_dwordx4 v121, s[64:65] offset:0 sc1\n"
    "global_load_lds_dwordx4 v121, s[64:65] offset:1024 sc1\n"
    "global_load_lds_dwordx4 v121, s[64:65] offset:2048 sc1\n"
    "global_load_lds_dwordx4 v121, s[64:65] offset:3072 sc1\n"
    "s_add_u32 m0, s50, 65536\n"
    "s_add_u32 s64, s48, 0\n"
    "s_addc_u32 s65, s49, 0\n"
    "global_load_lds_dwordx4 v121, s[64:65] offset:0 sc1\n"
    "global_load_lds_dwordx4 v121, s[64:65] offset:1024 sc1\n"
    "global_load_lds_dwordx4 v121, s[64:65] offset:2048 sc1\n"
    "global_load_lds_dwordx4 v121, s[64:65] offset:3072 sc1\n"
    "s_add_u32 m0, s50, 69632\n"
    "s_add_u32 s64, s48, 4096\n"
    "s_addc_u32 s65, s49, 0\n"
    "global_load_lds_dwordx4 v121, s[64:65] offset:0 sc1\n"
    "global_load_lds_dwordx4 v121, s[64:65] offset:1024 sc1\n"
    "global_load_lds_dwordx4 v121, s[64:65] offset:2048 sc1\n"
    "global_load_lds_dwordx4 v121, s[64:65] offset:3072 sc1\n"
    "s_add_u32 m0, s50, 73728\n"
    "s_add_u32 s64, s48, 8192\n"
    "s_addc_u32 s65, s49, 0\n"
    "global_load_lds_dwordx4 v121, s[64:65] offset:0 sc1\n"
    "global_load_lds_dwordx4 v121, s[64:65] offset:1024 sc1\n"
    "global_load_lds_dwordx4 v121, s[64:65] offset:2048 sc1\n"
    "s_mov_b64 s[66:67], s[44:45]\n"
    "s_sub_u32 s44, s44, 0x4400000\n"
    "s_subb_u32 s45, s45, 0\n"
    "s_waitcnt vmcnt(19) lgkmcnt(0)\n"
    "s_barrier\n"
    "v_lshlrev_b32 v112, 3, v121\n"
    "v_min_u32 v112, 0x500, v112\n"
    "s_add_u32 s58, s40, 0x312000\n"
    "s_addc_u32 s59, s41, 0\n"
    "global_load_dwordx2 v[114:115], v112, s[58:59] sc1\n"
    "ds_read_b128 v[192:195], v122 offset:0\n"
    "ds_read_b128 v[196:199], v122 offset:16384\n"
    "ds_read_b128 v[200:203], v122 offset:1024\n"
    "ds_read_b128 v[204:207], v122 offset:17408\n"
    "ds_read_b128 v[208:211], v122 offset:2048\n"
    "ds_read_b128 v[212:215], v122 offset:18432\n"
    "s_waitcnt lgkmcnt(4)\n"
    "s_branch G_even%=\n"
    ".p2align 6\n"
    "G_loop%=:\n"
    "v_mfma_f32_16x16x32_bf16 v[160:163], a[0:3], v[192:195], 0\n"
    "ds_read_b128 v[216:219], v123 offset:3072\n"
    "v_mfma_f32_16x16x32_bf16 v[164:167], a[64:67], v[192:195], 0\n"
    "ds_read_b128 v[220:223], v123 offset:19456\n"
    "v_mfma_f32_16x16x32_bf16 v[168:171], a[128:131], v[192:195], 0\n"
    "v_mfma_f32_16x16x32_bf16 v[172:175], a[192:195], v[192:195], 0\n"
    "v_cvt_pk_bf16_f32 v224, v128, v129\n"
    "v_cvt_pk_bf16_f32 v225, v130, v131\n"
    "v_mfma_f32_16x16x32_bf16 v[176:179], a[0:3], v[196:199], 0\n"
    "global_load_lds_dwordx4 v121, s[64:65] offset:3072 sc1\n"
    "v_mfma_f32_16x16x32_bf16 v[180:183], a[64:67], v[196:199], 0\n"
    "v_cvt_pk_bf16_f32 v226, v132, v133\n"
    "v_cvt_pk_bf16_f32 v227, v134, v135\n"
    "v_mfma_f32_16x16x32_bf16 v[184:187], a[128:131], v[196:199], 0\n"
    "v_cvt_pk_bf16_f32 v228, v144, v145\n"
    "v_cvt_pk_bf16_f32 v229, v146, v147\n"
    "v_mfma_f32_16x16x32_bf16 v[188:191], a[192:195], v[196:199], 0\n"
    "v_cvt_pk_bf16_f32 v230, v148, v149\n"
    "v_cvt_pk_bf16_f32 v231, v150, v151\n"
    "s_waitcnt lgkmcnt(4)\n"
    "v_mfma_f32_16x16x32_bf16 v[160:163], a[4:7], v[200:203], v[160:163]\n"
    "ds_read_b128 v[192:195], v123 offset:4096\n"
    "v_mfma_f32_16x16x32_bf16 v[164:167], a[68:71], v[200:203], v[164:167]\n"
    "ds_read_b128 v[196:199], v123 offset:20480\n"
    "v_mfma_f32_16x16x32_bf16 v[168:171], a[132:135], v[200:203], v[168:171]\n"
    "s_add_u32 m0, s50, 12288\n"
    "s_add_u32 s64, s48, 12288\n"
    "s_addc_u32 s65, s49, 0\n"
    "v_cvt_pk_bf16_f32 v232, v136, v137\n"
    "v_cvt_pk_bf16_f32 v233, v138, v139\n"
    "v_mfma_f32_16x16x32_bf16 v[172:175], a[196:199], v[200:203], v[172:175]\n"
    "s_add_u32 s48, s48, 0x10000\n"
    "s_addc_u32 s49, s49, 0\n"
    "v_cvt_pk_bf16_f32 v234, v140, v141\n"
    "v_cvt_pk_bf16_f32 v235, v142, v143\n"
    "v_mfma_f32_16x16x32_bf16 v[176:179], a[4:7], v[204:207], v[176:179]\n"
    "global_load_lds_dwordx4 v121, s[64:65] offset:0 sc1\n"
    "v_mfma_f32_16x16x32_bf16 v[180:183], a[68:71], v[204:207], v[180:183]\n"
    "v_cvt_pk_bf16_f32 v236, v152, v153\n"
    "v_cvt_pk_bf16_f32 v237, v154, v155\n"
    "v_mfma_f32_16x16x32_bf16 v[184:187], a[132:135], v[204:207], v[184:187]\n"
    "global_store_dwordx4 v121, v[224:227], s[44:45] offset:0 sc1\n"
    "v_mfma_f32_16x16x32_bf16 v[188:191], a[196:199], v[204:207], v[188:191]\n"
    "v_cvt_pk_bf16_f32 v238, v156, v157\n"
    "v_cvt_pk_bf16_f32 v239, v158, v159\n"
    "s_waitcnt lgkmcnt(4)\n"
    "v_mfma_f32_16x16x32_bf16 v[160:163], a[8:11], v[208:211], v[160:163]\n"
    "ds_read_b128 v[200:203], v123 offset:5120\n"
    "v_mfma_f32_16x16x32_bf16 v[164:167], a[72:75], v[208:211], v[164:167]\n"
    "ds_read_b128 v[204:207], v123 offset:21504\n"
    "v_mfma_f32_16x16x32_bf16 v[168:171], a[136:139], v[208:211], v[168:171]\n"
    "v_mfma_f32_16x16x32_bf16 v[172:175], a[200:203], v[208:211], v[172:175]\n"
    "v_mfma_f32_16x16x32_bf16 v[176:179], a[8:11], v[212:215], v[176:179]\n"
    "global_load_lds_dwordx4 v121, s[64:65] offset:1024 sc1\n"
    "v_mfma_f32_16x16x32_bf16 v[180:183], a[72:75], v[212:215], v[180:183]\n"
    "v_mfma_f32_16x16x32_bf16 v[184:187], a[136:139], v[212:215], v[184:187]\n"
    "global_store_dwordx4 v121, v[228:231], s[44:45] offset:1024 sc1\n"
    "v_mfma_f32_16x16x32_bf16 v[188:191], a[200:203], v[212:215], v[188:191]\n"
    "s_waitcnt lgkmcnt(4)\n"
    "v_mfma_f32_16x16x32_bf16 v[160:163], a[12:15], v[216:219], v[160:163]\n"
    "ds_read_b128 v[208:211], v123 offset:6144\n"
    "v_mfma_f32_16x16x32_bf16 v[164:167], a[76:79], v[216:219], v[164:167]\n"
    "ds_read_b128 v[212:215], v123 offset:22528\n"
    "v_mfma_f32_16x16x32_bf16 v[168:171], a[140:143], v[216:219], v[168:171]\n"
    "v_mfma_f32_16x16x32_bf16 v[172:175], a[204:207], v[216:219], v[172:175]\n"
    "v_mfma_f32_16x16x32_bf16 v[176:179], a[12:15], v[220:223], v[176:179]\n"
    "global_load_lds_dwordx4 v121, s[64:65] offset:2048 sc1\n"
    "v_mfma_f32_16x16x32_bf16 v[180:183], a[76:79], v[220:223], v[180:183]\n"
    "v_mfma_f32_16x16x32_bf16 v[184:187], a[140:143], v[220:223], v[184:187]\n"
    "global_store_dwordx4 v121, v[232:235], s[44:45] offset:2048 sc1\n"
    "v_mfma_f32_16x16x32_bf16 v[188:191], a[204:207], v[220:223], v[188:191]\n"
    "s_waitcnt lgkmcnt(4)\n"
    "v_mfma_f32_16x16x32_bf16 v[160:163], a[16:19], v[192:195], v[160:163]\n"
    "ds_read_b128 v[216:219], v123 offset:7168\n"
    "v_mfma_f32_16x16x32_bf16 v[164:167], a[80:83], v[192:195], v[164:167]\n"
    "ds_read_b128 v[220:223], v123 offset:23552\n"
    "v_mfma_f32_16x16x32_bf16 v[168:171], a[144:147], v[192:195], v[168:171]\n"
    "v_mfma_f32_16x16x32_bf16 v[172:175], a[208:211], v[192:195], v[172:175]\n"
    "v_mfma_f32_16x16x32_bf16 v[176:179], a[16:19], v[196:199], v[176:179]\n"
    "global_load_lds_dwordx4 v121, s[64:65] offset:3072 sc1\n"
    "v_mfma_f32_16x16x32_bf16 v[180:183], a[80:83], v[196:199], v[180:183]\n"
    "v_mfma_f32_16x16x32_bf16 v[184:187], a[144:147], v[196:199], v[184:187]\n"
    "global_store_dwordx4 v121, v[236:239], s[44:45] offset:3072 sc1\n"
    "v_mfma_f32_16x16x32_bf16 v[188:191], a[208:211], v[196:199], v[188:191]\n"
    "s_waitcnt lgkmcnt(4)\n"
    "v_mfma_f32_16x16x32_bf16 v[160:163], a[20:23], v[200:203], v[160:163]\n"
    "s_waitcnt vmcnt(12) lgkmcnt(0)\n"
    "s_barrier\n"
    "v_mfma_f32_16x16x32_bf16 v[164:167], a[84:87], v[200:203], v[164:167]\n"
    "ds_read_b128 v[192:195], v123 offset:8192\n"
    "v_mfma_f32_16x16x32_bf16 v[168:171], a[148:151], v[200:203], v[168:171]\n"
    "ds_read_b128 v[196:199], v123 offset:24576\n"
    "s_add_u32 m0, s50, 65536\n"
    "s_add_u32 s64, s48, 0\n"
    "s_addc_u32 s65, s49, 0\n"
    "v_mfma_f32_16x16x32_bf16 v[172:175], a[212:215], v[200:203], v[172:175]\n"
    "v_mfma_f32_16x16x32_bf16 v[176:179], a[20:23], v[204:207], v[176:179]\n"
    "global_load_lds_dwordx4 v121, s[64:65] offset:0 sc1\n"
    "v_mfma_f32_16x16x32_bf16 v[180:183], a[84:87], v[204:207], v[180:183]\n"
    "v_mfma_f32_16x16x32_bf16 v[184:187], a[148:151], v[204:207], v[184:187]\n"
    "v_mfma_f32_16x16x32_bf16 v[188:191], a[212:215], v[204:207], v[188:191]\n"
    "s_waitcnt lgkmcnt(4)\n"
    "v_mfma_f32_16x16x32_bf16 v[160:163], a[24:27], v[208:211], v[160:163]\n"
    "ds_read_b128 v[200:203], v123 offset:9216\n"
    "v_mfma_f32_16x16x32_bf16 v[164:167], a[88:91], v[208:211], v[164:167]\n"
    "ds_read_b128 v[204:207], v123 offset:25600\n"
    "v_mfma_f32_16x16x32_bf16 v[168:171], a[152:155], v[208:211], v[168:171]\n"
    "v_mfma_f32_16x16x32_bf16 v[172:175], a[216:219], v[208:211], v[172:175]\n"
    "v_mfma_f32_16x16x32_bf16 v[176:179], a[24:27], v[212:215], v[176:179]\n"
    "global_load_lds_dwordx4 v121, s[64:65] offset:1024 sc1\n"
    "v_mfma_f32_16x16x32_bf16 v[180:183], a[88:91], v[212:215], v[180:183]\n"
    "v_mfma_f32_16x16x32_bf16 v[184:187], a[152:155], v[212:215], v[184:187]\n"
    "v_mfma_f32_16x16x32_bf16 v[188:191], a[216:219], v[212:215], v[188:191]\n"
    "s_waitcnt lgkmcnt(4)\n"
    "v_mfma_f32_16x16x32_bf16 v[160:163], a[28:31], v[216:219], v[160:163]\n"
    "ds_read_b128 v[208:211], v123 offset:10240\n"
    "v_mfma_f32_16x16x32_bf16 v[164:167], a[92:95], v[216:219], v[164:167]\n"
    "ds_read_b128 v[212:215], v123 offset:26624\n"
    "v_mfma_f32_16x16x32_bf16 v[168:171], a[156:159], v[216:219], v[168:171]\n"
    "v_mfma_f32_16x16x32_bf16 v[172:175], a[220:223], v[216:219], v[172:175]\n"
    "v_mfma_f32_16x16x32_bf16 v[176:179], a[28:31], v[220:223], v[176:179]\n"
    "global_load_lds_dwordx4 v121, s[64:65] offset:2048 sc1\n"
    "v_mfma_f32_16x16x32_bf16 v[180:183], a[92:95], v[220:223], v[180:183]\n"
    "s_mov_b64 s[44:45], s[66:67]\n"
    "s_add_u32 s66, s66, 0x100000\n"
    "s_addc_u32 s67, s67, 0\n"
    "v_mfma_f32_16x16x32_bf16 v[184:187], a[156:159], v[220:223], v[184:187]\n"
    "s_xor_b32 s66, s66, 0x2000\n"
    "v_mfma_f32_16x16x32_bf16 v[188:191], a[220:223], v[220:223], v[188:191]\n"
    "s_waitcnt lgkmcnt(4)\n"
    "v_mfma_f32_16x16x32_bf16 v[160:163], a[32:35], v[192:195], v[160:163]\n"
    "ds_read_b128 v[216:219], v123 offset:11264\n"
    "v_mfma_f32_16x16x32_bf16 v[164:167], a[96:99], v[192:195], v[164:167]\n"
    "ds_read_b128 v[220:223], v123 offset:27648\n"
    "v_mfma_f32_16x16x32_bf16 v[168:171], a[160:163], v[192:195], v[168:171]\n"
    "v_mfma_f32_16x16x32_bf16 v[172:175], a[224:227], v[192:195], v[172:175]\n"
    "v_mfma_f32_16x16x32_bf16 v[176:179], a[32:35], v[196:199], v[176:179]\n"
    "global_load_lds_dwordx4 v121, s[64:65] offset:3072 sc1\n"
    "v_mfma_f32_16x16x32_bf16 v[180:183], a[96:99], v[196:199], v[180:183]\n"
    "v_mfma_f32_16x16x32_bf16 v[184:187], a[160:163], v[196:199], v[184:187]\n"
    "v_mfma_f32_16x16x32_bf16 v[188:191], a[224:227], v[196:199], v[188:191]\n"
    "s_waitcnt lgkmcnt(4)\n"
    "v_mfma_f32_16x16x32_bf16 v[160:163], a[36:39], v[200:203], v[160:163]\n"
    "ds_read_b128 v[192:195], v123 offset:12288\n"
    "v_mfma_f32_16x16x32_bf16 v[164:167], a[100:103], v[200:203], v[164:167]\n"
    "ds_read_b128 v[196:199], v123 offset:28672\n"
    "v_mfma_f32_16x16x32_bf16 v[168:171], a[164:167], v[200:203], v[168:171]\n"
    "s_add_u32 m0, s50, 69632\n"
    "s_add_u32 s64, s48, 4096\n"
    "s_addc_u32 s65, s49, 0\n"
    "v_mfma_f32_16x16x32_bf16 v[172:175], a[228:231], v[200:203], v[172:175]\n"
    "v_mfma_f32_16x16x32_bf16 v[176:179], a[36:39], v[204:207], v[176:179]\n"
    "global_load_lds_dwordx4 v121, s[64:65] offset:0 sc1\n"
    "v_mfma_f32_16x16x32_bf16 v[180:183], a[100:103], v[204:207], v[180:183]\n"
    "v_mfma_f32_16x16x32_bf16 v[184:187], a[164:167], v[204:207], v[184:187]\n"
    "v_mfma_f32_16x16x32_bf16 v[188:191], a[228:231], v[204:207], v[188:191]\n"
    "s_waitcnt lgkmcnt(4)\n"
    "v_mfma_f32_16x16x32_bf16 v[160:163], a[40:43], v[208:211], v[160:163]\n"
    "ds_read_b128 v[200:203], v123 offset:13312\n"
    "v_mfma_f32_16x16x32_bf16 v[164:167], a[104:107], v[208:211], v[164:167]\n"
    "ds_read_b128 v[204:207], v123 offset:29696\n"
    "v_mfma_f32_16x16x32_bf16 v[168:171], a[168:171], v[208:211], v[168:171]\n"
    "v_mfma_f32_16x16x32_bf16 v[172:175], a[232:235], v[208:211], v[172:175]\n"
    "v_mfma_f32_16x16x32_bf16 v[176:179], a[40:43], v[212:215], v[176:179]\n"
    "global_load_lds_dwordx4 v121, s[64:65] offset:1024 sc1\n"
    "v_mfma_f32_16x16x32_bf16 v[180:183], a[104:107], v[212:215], v[180:183]\n"
    "v_mfma_f32_16x16x32_bf16 v[184:187], a[168:171], v[212:215], v[184:187]\n"
    "v_mfma_f32_16x16x32_bf16 v[188:191], a[232:235], v[212:215], v[188:191]\n"
    "s_waitcnt lgkmcnt(4)\n"
    "v_mfma_f32_16x16x32_bf16 v[160:163], a[44:47], v[216:219], v[160:163]\n"
    "ds_read_b128 v[208:211], v123 offset:14336\n"
    "v_mfma_f32_16x16x32_bf16 v[164:167], a[108:111], v[216:219], v[164:167]\n"
    "ds_read_b128 v[212:215], v123 offset:30720\n"
    "v_mfma_f32_16x16x32_bf16 v[168:171], a[172:175], v[216:219], v[168:171]\n"
    "v_mfma_f32_16x16x32_bf16 v[172:175], a[236:239], v[216:219], v[172:175]\n"
    "v_mfma_f32_16x16x32_bf16 v[176:179], a[44:47], v[220:223], v[176:179]\n"
    "global_load_lds_dwordx4 v121, s[64:65] offset:2048 sc1\n"
    "v_mfma_f32_16x16x32_bf16 v[180:183], a[108:111], v[220:223], v[180:183]\n"
    "v_mfma_f32_16x16x32_bf16 v[184:187], a[172:175], v[220:223], v[184:187]\n"
    "v_mfma_f32_16x16x32_bf16 v[188:191], a[236:239], v[220:223], v[188:191]\n"
    "s_waitcnt lgkmcnt(4)\n"
    "v_mfma_f32_16x16x32_bf16 v[160:163], a[48:51], v[192:195], v[160:163]\n"
    "ds_read_b128 v[216:219], v123 offset:15360\n"
    "v_mfma_f32_16x16x32_bf16 v[164:167], a[112:115], v[192:195], v[164:167]\n"
    "ds_read_b128 v[220:223], v123 offset:31744\n"
    "v_mfma_f32_16x16x32_bf16 v[168:171], a[176:179], v[192:195], v[168:171]\n"
    "v_mfma_f32_16x16x32_bf16 v[172:175], a[240:243], v[192:195], v[172:175]\n"
    "v_mfma_f32_16x16x32_bf16 v[176:179], a[48:51], v[196:199], v[176:179]\n"
    "global_load_lds_dwordx4 v121, s[64:65] offset:3072 sc1\n"
    "v_mfma_f32_16x16x32_bf16 v[180:183], a[112:115], v[196:199], v[180:183]\n"
    "v_mfma_f32_16x16x32_bf16 v[184:187], a[176:179], v[196:199], v[184:187]\n"
    "v_mfma_f32_16x16x32_bf16 v[188:191], a[240:243], v[196:199], v[188:191]\n"
    "s_waitcnt lgkmcnt(4)\n"
    "v_mfma_f32_16x16x32_bf16 v[160:163], a[52:55], v[200:203], v[160:163]\n"
    "s_waitcnt vmcnt(8) lgkmcnt(0)\n"
    "s_barrier\n"
    "v_mfma_f32_16x16x32_bf16 v[164:167], a[116:119], v[200:203], v[164:167]\n"
    "ds_read_b128 v[192:195], v122 offset:0\n"
    "v_mfma_f32_16x16x32_bf16 v[168:171], a[180:183], v[200:203], v[168:171]\n"
    "ds_read_b128 v[196:199], v122 offset:16384\n"
    "s_add_u32 m0, s50, 73728\n"
    "s_add_u32 s64, s48, 8192\n"
    "s_addc_u32 s65, s49, 0\n"
    "v_mfma_f32_16x16x32_bf16 v[172:175], a[244:247], v[200:203], v[172:175]\n"
    "v_mfma_f32_16x16x32_bf16 v[176:179], a[52:55], v[204:207], v[176:179]\n"
    "global_load_lds_dwordx4 v121, s[64:65] offset:0 sc1\n"
    "v_mfma_f32_16x16x32_bf16 v[180:183], a[116:119], v[204:207], v[180:183]\n"
    "s_cmp_lg_u32 s56, 0\n"
    "s_cbranch_scc1 G_f1%=\n"
    "s_or_b32 s58, s42, s51\n"
    "s_mov_b64 exec, 1\n"
    "v_mov_b32 v116, s58\n"
    "global_store_dwordx2 v118, v[116:117], s[54:55] sc1\n"
    "s_mov_b64 exec, -1\n"
    "G_f1%=:\n"
    "v_mfma_f32_16x16x32_bf16 v[184:187], a[180:183], v[204:207], v[184:187]\n"
    "v_mfma_f32_16x16x32_bf16 v[188:191], a[244:247], v[204:207], v[188:191]\n"
    "s_waitcnt lgkmcnt(4)\n"
    "v_mfma_f32_16x16x32_bf16 v[160:163], a[56:59], v[208:211], v[160:163]\n"
    "ds_read_b128 v[200:203], v122 offset:1024\n"
    "v_mfma_f32_16x16x32_bf16 v[164:167], a[120:123], v[208:211], v[164:167]\n"
    "ds_read_b128 v[204:207], v122 offset:17408\n"
    "v_mfma_f32_16x16x32_bf16 v[168:171], a[184:187], v[208:211], v[168:171]\n"
    "v_mfma_f32_16x16x32_bf16 v[172:175], a[248:251], v[208:211], v[172:175]\n"
    "v_mfma_f32_16x16x32_bf16 v[176:179], a[56:59], v[212:215], v[176:179]\n"
    "global_load_lds_dwordx4 v121, s[64:65] offset:1024 sc1\n"
    "v_mfma_f32_16x16x32_bf16 v[180:183], a[120:123], v[212:215], v[180:183]\n"
    "v_mfma_f32_16x16x32_bf16 v[184:187], a[184:187], v[212:215], v[184:187]\n"
    "v_mfma_f32_16x16x32_bf16 v[188:191], a[248:251], v[212:215], v[188:191]\n"
    "s_add_u32 s51, s51, 1\n"
    "s_waitcnt lgkmcnt(4)\n"
    "v_mfma_f32_16x16x32_bf16 v[160:163], a[60:63], v[216:219], v[160:163]\n"
    "ds_read_b128 v[208:211], v122 offset:2048\n"
    "v_mfma_f32_16x16x32_bf16 v[164:167], a[124:127], v[216:219], v[164:167]\n"
    "ds_read_b128 v[212:215], v122 offset:18432\n"
    "v_mfma_f32_16x16x32_bf16 v[168:171], a[188:191], v[216:219], v[168:171]\n"
    "v_mfma_f32_16x16x32_bf16 v[172:175], a[252:255], v[216:219], v[172:175]\n"
    "v_mfma_f32_16x16x32_bf16 v[176:179], a[60:63], v[220:223], v[176:179]\n"
    "global_load_lds_dwordx4 v121, s[64:65] offset:2048 sc1\n"
    "v_mfma_f32_16x16x32_bf16 v[180:183], a[124:127], v[220:223], v[180:183]\n"
    "v_mfma_f32_16x16x32_bf16 v[184:187], a[188:191], v[220:223], v[184:187]\n"
    "v_mfma_f32_16x16x32_bf16 v[188:191], a[252:255], v[220:223], v[188:191]\n"
    "s_add_u32 s58, s51, -3\n"
    "s_cmp_gt_u32 s58, 28\n"
    "s_waitcnt lgkmcnt(4)\n"
    "s_cbranch_scc1 G_t1%=\n"
    "G_even%=:\n"
    "v_mfma_f32_16x16x32_bf16 v[128:131], a[0:3], v[192:195], 0\n"
    "ds_read_b128 v[216:219], v122 offset:3072\n"
    "v_mfma_f32_16x16x32_bf16 v[132:135], a[64:67], v[192:195], 0\n"
    "ds_read_b128 v[220:223], v122 offset:19456\n"
    "v_mfma_f32_16x16x32_bf16 v[136:139], a[128:131], v[192:195], 0\n"
    "v_mfma_f32_16x16x32_bf16 v[140:143], a[192:195], v[192:195], 0\n"
    "v_cvt_pk_bf16_f32 v224, v160, v161\n"
    "v_cvt_pk_bf16_f32 v225, v162, v163\n"
    "v_mfma_f32_16x16x32_bf16 v[144:147], a[0:3], v[196:199], 0\n"
    "global_load_lds_dwordx4 v121, s[64:65] offset:3072 sc1\n"
    "v_mfma_f32_16x16x32_bf16 v[148:151], a[64:67], v[196:199], 0\n"
    "v_cvt_pk_bf16_f32 v226, v164, v165\n"
    "v_cvt_pk_bf16_f32 v227, v166, v167\n"
    "v_mfma_f32_16x16x32_bf16 v[152:155], a[128:131], v[196:199], 0\n"
    "v_cvt_pk_bf16_f32 v228, v176, v177\n"
    "v_cvt_pk_bf16_f32 v229, v178, v179\n"
    "v_mfma_f32_16x16x32_bf16 v[156:159], a[192:195], v[196:199], 0\n"
    "v_cvt_pk_bf16_f32 v230, v180, v181\n"
    "v_cvt_pk_bf16_f32 v231, v182, v183\n"
    "s_waitcnt lgkmcnt(4)\n"
    "v_mfma_f32_16x16x32_bf16 v[128:131], a[4:7], v[200:203], v[128:131]\n"
    "ds_read_b128 v[192:195], v122 offset:4096\n"
    "v_mfma_f32_16x16x32_bf16 v[132:135], a[68:71], v[200:203], v[132:135]\n"
    "ds_read_b128 v[196:199], v122 offset:20480\n"
    "v_mfma_f32_16x16x32_bf16 v[136:139], a[132:135], v[200:203], v[136:139]\n"
    "s_add_u32 m0, s50, 77824\n"
    "s_add_u32 s64, s48, 12288\n"
    "s_addc_u32 s65, s49, 0\n"
    "v_cvt_pk_bf16_f32 v232, v168, v169\n"
    "v_cvt_pk_bf16_f32 v233, v170, v171\n"
    "v_mfma_f32_16x16x32_bf16 v[140:143], a[196:199], v[200:203], v[140:143]\n"
    "s_add_u32 s48, s48, 0x10000\n"
    "s_addc_u32 s49, s49, 0\n"
    "v_cvt_pk_bf16_f32 v234, v172, v173\n"
    "v_cvt_pk_bf16_f32 v235, v174, v175\n"
    "v_mfma_f32_16x16x32_bf16 v[144:147], a[4:7], v[204:207], v[144:147]\n"
    "global_load_lds_dwordx4 v121, s[64:65] offset:0 sc1\n"
    "v_mfma_f32_16x16x32_bf16 v[148:151], a[68:71], v[204:207], v[148:151]\n"
    "v_cvt_pk_bf16_f32 v236, v184, v185\n"
    "v_cvt_pk_bf16_f32 v237, v186, v187\n"
    "v_mfma_f32_16x16x32_bf16 v[152:155], a[132:135], v[204:207], v[152:155]\n"
    "global_store_dwordx4 v121, v[224:227], s[44:45] offset:0 sc1\n"
    "v_mfma_f32_16x16x32_bf16 v[156:159], a[196:199], v[204:207], v[156:159]\n"
    "v_cvt_pk_bf16_f32 v238, v188, v189\n"
    "v_cvt_pk_bf16_f32 v239, v190, v191\n"
    "s_waitcnt lgkmcnt(4)\n"
    "v_mfma_f32_16x16x32_bf16 v[128:131], a[8:11], v[208:211], v[128:131]\n"
    "ds_read_b128 v[200:203], v122 offset:5120\n"
    "v_mfma_f32_16x16x32_bf16 v[132:135], a[72:75], v[208:211], v[132:135]\n"
    "ds_read_b128 v[204:207], v122 offset:21504\n"
    "v_mfma_f32_16x16x32_bf16 v[136:139], a[136:139], v[208:211], v[136:139]\n"
    "v_mfma_f32_16x16x32_bf16 v[140:143], a[200:203], v[208:211], v[140:143]\n"
    "v_mfma_f32_16x16x32_bf16 v[144:147], a[8:11], v[212:215], v[144:147]\n"
    "global_load_lds_dwordx4 v121, s[64:65] offset:1024 sc1\n"
    "v_mfma_f32_16x16x32_bf16 v[148:151], a[72:75], v[212:215], v[148:151]\n"
    "v_mfma_f32_16x16x32_bf16 v[152:155], a[136:139], v[212:215], v[152:155]\n"
    "global_store_dwordx4 v121, v[228:231], s[44:45] offset:1024 sc1\n"
    "v_mfma_f32_16x16x32_bf16 v[156:159], a[200:203], v[212:215], v[156:159]\n"
    "s_waitcnt lgkmcnt(4)\n"
    "v_mfma_f32_16x16x32_bf16 v[128:131], a[12:15], v[216:219], v[128:131]\n"
    "ds_read_b128 v[208:211], v122 offset:6144\n"
    "v_mfma_f32_16x16x32_bf16 v[132:135], a[76:79], v[216:219], v[132:135]\n"
    "ds_read_b128 v[212:215], v122 offset:22528\n"
    "v_mfma_f32_16x16x32_bf16 v[136:139], a[140:143], v[216:219], v[136:139]\n"
    "v_mfma_f32_16x16x32_bf16 v[140:143], a[204:207], v[216:219], v[140:143]\n"
    "v_mfma_f32_16x16x32_bf16 v[144:147], a[12:15], v[220:223], v[144:147]\n"
    "global_load_lds_dwordx4 v121, s[64:65] offset:2048 sc1\n"
    "v_mfma_f32_16x16x32_bf16 v[148:151], a[76:79], v[220:223], v[148:151]\n"
    "v_mfma_f32_16x16x32_bf16 v[152:155], a[140:143], v[220:223], v[152:155]\n"
    "global_store_dwordx4 v121, v[232:235], s[44:45] offset:2048 sc1\n"
    "v_mfma_f32_16x16x32_bf16 v[156:159], a[204:207], v[220:223], v[156:159]\n"
    "s_waitcnt lgkmcnt(4)\n"
    "v_mfma_f32_16x16x32_bf16 v[128:131], a[16:19], v[192:195], v[128:131]\n"
    "ds_read_b128 v[216:219], v122 offset:7168\n"
    "v_mfma_f32_16x16x32_bf16 v[132:135], a[80:83], v[192:195], v[132:135]\n"
    "ds_read_b128 v[220:223], v122 offset:23552\n"
    "v_mfma_f32_16x16x32_bf16 v[136:139], a[144:147], v[192:195], v[136:139]\n"
    "v_mfma_f32_16x16x32_bf16 v[140:143], a[208:211], v[192:195], v[140:143]\n"
    "v_mfma_f32_16x16x32_bf16 v[144:147], a[16:19], v[196:199], v[144:147]\n"
    "global_load_lds_dwordx4 v121, s[64:65] offset:3072 sc1\n"
    "v_mfma_f32_16x16x32_bf16 v[148:151], a[80:83], v[196:199], v[148:151]\n"
    "v_mfma_f32_16x16x32_bf16 v[152:155], a[144:147], v[196:199], v[152:155]\n"
    "global_store_dwordx4 v121, v[236:239], s[44:45] offset:3072 sc1\n"
    "v_mfma_f32_16x16x32_bf16 v[156:159], a[208:211], v[196:199], v[156:159]\n"
    "s_waitcnt lgkmcnt(4)\n"
    "v_mfma_f32_16x16x32_bf16 v[128:131], a[20:23], v[200:203], v[128:131]\n"
    "s_waitcnt vmcnt(12) lgkmcnt(0)\n"
    "s_barrier\n"
    "v_mfma_f32_16x16x32_bf16 v[132:135], a[84:87], v[200:203], v[132:135]\n"
    "ds_read_b128 v[192:195], v122 offset:8192\n"
    "v_mfma_f32_16x16x32_bf16 v[136:139], a[148:151], v[200:203], v[136:139]\n"
    "ds_read_b128 v[196:199], v122 offset:24576\n"
    "s_add_u32 m0, s50, 0\n"
    "s_add_u32 s64, s48, 0\n"
    "s_addc_u32 s65, s49, 0\n"
    "v_mfma_f32_16x16x32_bf16 v[140:143], a[212:215], v[200:203], v[140:143]\n"
    "v_mfma_f32_16x16x32_bf16 v[144:147], a[20:23], v[204:207], v[144:147]\n"
    "global_load_lds_dwordx4 v121, s[64:65] offset:0 sc1\n"
    "v_mfma_f32_16x16x32_bf16 v[148:151], a[84:87], v[204:207], v[148:151]\n"
    "v_mfma_f32_16x16x32_bf16 v[152:155], a[148:151], v[204:207], v[152:155]\n"
    "v_mfma_f32_16x16x32_bf16 v[156:159], a[212:215], v[204:207], v[156:159]\n"
    "s_waitcnt lgkmcnt(4)\n"
    "v_mfma_f32_16x16x32_bf16 v[128:131], a[24:27], v[208:211], v[128:131]\n"
    "ds_read_b128 v[200:203], v122 offset:9216\n"
    "v_mfma_f32_16x16x32_bf16 v[132:135], a[88:91], v[208:211], v[132:135]\n"
    "ds_read_b128 v[204:207], v122 offset:25600\n"
    "v_mfma_f32_16x16x32_bf16 v[136:139], a[152:155], v[208:211], v[136:139]\n"
    "v_mfma_f32_16x16x32_bf16 v[140:143], a[216:219], v[208:211], v[140:143]\n"
    "v_mfma_f32_16x16x32_bf16 v[144:147], a[24:27], v[212:215], v[144:147]\n"
    "global_load_lds_dwordx4 v121, s[64:65] offset:1024 sc1\n"
    "v_mfma_f32_16x16x32_bf16 v[148:151], a[88:91], v[212:215], v[148:151]\n"
    "v_mfma_f32_16x16x32_bf16 v[152:155], a[152:155], v[212:215], v[152:155]\n"
    "v_mfma_f32_16x16x32_bf16 v[156:159], a[216:219], v[212:215], v[156:159]\n"
    "s_waitcnt lgkmcnt(4)\n"
    "v_mfma_f32_16x16x32_bf16 v[128:131], a[28:31], v[216:219], v[128:131]\n"
    "ds_read_b128 v[208:211], v122 offset:10240\n"
    "v_mfma_f32_16x16x32_bf16 v[132:135], a[92:95], v[216:219], v[132:135]\n"
    "ds_read_b128 v[212:215], v122 offset:26624\n"
    "v_mfma_f32_16x16x32_bf16 v[136:139], a[156:159], v[216:219], v[136:139]\n"
    "v_mfma_f32_16x16x32_bf16 v[140:143], a[220:223], v[216:219], v[140:143]\n"
    "v_mfma_f32_16x16x32_bf16 v[144:147], a[28:31], v[220:223], v[144:147]\n"
    "global_load_lds_dwordx4 v121, s[64:65] offset:2048 sc1\n"
    "v_mfma_f32_16x16x32_bf16 v[148:151], a[92:95], v[220:223], v[148:151]\n"
    "s_mov_b64 s[44:45], s[66:67]\n"
    "s_add_u32 s66, s66, 0x100000\n"
    "s_addc_u32 s67, s67, 0\n"
    "v_mfma_f32_16x16x32_bf16 v[152:155], a[156:159], v[220:223], v[152:155]\n"
    "v_mfma_f32_16x16x32_bf16 v[156:159], a[220:223], v[220:223], v[156:159]\n"
    "s_waitcnt lgkmcnt(4)\n"
    "v_mfma_f32_16x16x32_bf16 v[128:131], a[32:35], v[192:195], v[128:131]\n"
    "ds_read_b128 v[216:219], v122 offset:11264\n"
    "v_mfma_f32_16x16x32_bf16 v[132:135], a[96:99], v[192:195], v[132:135]\n"
    "ds_read_b128 v[220:223], v122 offset:27648\n"
    "v_mfma_f32_16x16x32_bf16 v[136:139], a[160:163], v[192:195], v[136:139]\n"
    "v_mfma_f32_16x16x32_bf16 v[140:143], a[224:227], v[192:195], v[140:143]\n"
    "v_mfma_f32_16x16x32_bf16 v[144:147], a[32:35], v[196:199], v[144:147]\n"
    "global_load_lds_dwordx4 v121, s[64:65] offset:3072 sc1\n"
    "v_mfma_f32_16x16x32_bf16 v[148:151], a[96:99], v[196:199], v[148:151]\n"
    "v_mfma_f32_16x16x32_bf16 v[152:155], a[160:163], v[196:199], v[152:155]\n"
    "v_mfma_f32_16x16x32_bf16 v[156:159], a[224:227], v[196:199], v[156:159]\n"
    "s_waitcnt lgkmcnt(4)\n"
    "v_mfma_f32_16x16x32_bf16 v[128:131], a[36:39], v[200:203], v[128:131]\n"
    "ds_read_b128 v[192:195], v122 offset:12288\n"
    "v_mfma_f32_16x16x32_bf16 v[132:135], a[100:103], v[200:203], v[132:135]\n"
    "ds_read_b128 v[196:199], v122 offset:28672\n"
    "v_mfma_f32_16x16x32_bf16 v[136:139], a[164:167], v[200:203], v[136:139]\n"
    "s_add_u32 m0, s50, 4096\n"
    "s_add_u32 s64, s48, 4096\n"
    "s_addc_u32 s65, s49, 0\n"
    "v_mfma_f32_16x16x32_bf16 v[140:143], a[228:231], v[200:203], v[140:143]\n"
    "v_mfma_f32_16x16x32_bf16 v[144:147], a[36:39], v[204:207], v[144:147]\n"
    "global_load_lds_dwordx4 v121, s[64:65] offset:0 sc1\n"
    "v_mfma_f32_16x16x32_bf16 v[148:151], a[100:103], v[204:207], v[148:151]\n"
    "v_mfma_f32_16x16x32_bf16 v[152:155], a[164:167], v[204:207], v[152:155]\n"
    "v_mfma_f32_16x16x32_bf16 v[156:159], a[228:231], v[204:207], v[156:159]\n"
    "s_waitcnt lgkmcnt(4)\n"
    "v_mfma_f32_16x16x32_bf16 v[128:131], a[40:43], v[208:211], v[128:131]\n"
    "ds_read_b128 v[200:203], v122 offset:13312\n"
    "v_mfma_f32_16x16x32_bf16 v[132:135], a[104:107], v[208:211], v[132:135]\n"
    "ds_read_b128 v[204:207], v122 offset:29696\n"
    "v_mfma_f32_16x16x32_bf16 v[136:139], a[168:171], v[208:211], v[136:139]\n"
    "v_mfma_f32_16x16x32_bf16 v[140:143], a[232:235], v[208:211], v[140:143]\n"
    "v_mfma_f32_16x16x32_bf16 v[144:147], a[40:43], v[212:215], v[144:147]\n"
    "global_load_lds_dwordx4 v121, s[64:65] offset:1024 sc1\n"
    "v_mfma_f32_16x16x32_bf16 v[148:151], a[104:107], v[212:215], v[148:151]\n"
    "v_mfma_f32_16x16x32_bf16 v[152:155], a[168:171], v[212:215], v[152:155]\n"
    "v_mfma_f32_16x16x32_bf16 v[156:159], a[232:235], v[212:215], v[156:159]\n"
    "s_waitcnt lgkmcnt(4)\n"
    "v_mfma_f32_16x16x32_bf16 v[128:131], a[44:47], v[216:219], v[128:131]\n"
    "ds_read_b128 v[208:211], v122 offset:14336\n"
    "v_mfma_f32_16x16x32_bf16 v[132:135], a[108:111], v[216:219], v[132:135]\n"
    "ds_read_b128 v[212:215], v122 offset:30720\n"
    "v_mfma_f32_16x16x32_bf16 v[136:139], a[172:175], v[216:219], v[136:139]\n"
    "v_mfma_f32_16x16x32_bf16 v[140:143], a[236:239], v[216:219], v[140:143]\n"
    "v_mfma_f32_16x16x32_bf16 v[144:147], a[44:47], v[220:223], v[144:147]\n"
    "global_load_lds_dwordx4 v121, s[64:65] offset:2048 sc1\n"
    "v_mfma_f32_16x16x32_bf16 v[148:151], a[108:111], v[220:223], v[148:151]\n"
    "v_mfma_f32_16x16x32_bf16 v[152:155], a[172:175], v[220:223], v[152:155]\n"
    "v_mfma_f32_16x16x32_bf16 v[156:159], a[236:239], v[220:223], v[156:159]\n"
    "s_waitcnt lgkmcnt(4)\n"
    "v_mfma_f32_16x16x32_bf16 v[128:131], a[48:51], v[192:195], v[128:131]\n"
    "ds_read_b128 v[216:219], v122 offset:15360\n"
    "v_mfma_f32_16x16x32_bf16 v[132:135], a[112:115], v[192:195], v[132:135]\n"
    "ds_read_b128 v[220:223], v122 offset:31744\n"
    "v_mfma_f32_16x16x32_bf16 v[136:139], a[176:179], v[192:195], v[136:139]\n"
    "v_mfma_f32_16x16x32_bf16 v[140:143], a[240:243], v[192:195], v[140:143]\n"
    "v_mfma_f32_16x16x32_bf16 v[144:147], a[48:51], v[196:199], v[144:147]\n"
    "global_load_lds_dwordx4 v121, s[64:65] offset:3072 sc1\n"
    "v_mfma_f32_16x16x32_bf16 v[148:151], a[112:115], v[196:199], v[148:151]\n"
    "v_mfma_f32_16x16x32_bf16 v[152:155], a[176:179], v[196:199], v[152:155]\n"
    "v_mfma_f32_16x16x32_bf16 v[156:159], a[240:243], v[196:199], v[156:159]\n"
    "s_waitcnt lgkmcnt(4)\n"
    "v_mfma_f32_16x16x32_bf16 v[128:131], a[52:55], v[200:203], v[128:131]\n"
    "s_waitcnt vmcnt(8) lgkmcnt(0)\n"
    "s_barrier\n"
    "v_mfma_f32_16x16x32_bf16 v[132:135], a[116:119], v[200:203], v[132:135]\n"
    "ds_read_b128 v[192:195], v123 offset:0\n"
    "v_mfma_f32_16x16x32_bf16 v[136:139], a[180:183], v[200:203], v[136:139]\n"
    "ds_read_b128 v[196:199], v123 offset:16384\n"
    "s_add_u32 m0, s50, 8192\n"
    "s_add_u32 s64, s48, 8192\n"
    "s_addc_u32 s65, s49, 0\n"
    "v_mfma_f32_16x16x32_bf16 v[140:143], a[244:247], v[200:203], v[140:143]\n"
    "v_mfma_f32_16x16x32_bf16 v[144:147], a[52:55], v[204:207], v[144:147]\n"
    "global_load_lds_dwordx4 v121, s[64:65] offset:0 sc1\n"
    "v_mfma_f32_16x16x32_bf16 v[148:151], a[116:119], v[204:207], v[148:151]\n"
    "s_cmp_lg_u32 s56, 0\n"
    "s_cbranch_scc1 G_f2%=\n"
    "s_or_b32 s58, s42, s51\n"
    "s_mov_b64 exec, 1\n"
    "v_mov_b32 v116, s58\n"
    "global_store_dwordx2 v118, v[116:117], s[54:55] sc1\n"
    "s_mov_b64 exec, -1\n"
    "G_f2%=:\n"
    "v_mfma_f32_16x16x32_bf16 v[152:155], a[180:183], v[204:207], v[152:155]\n"
    "v_mfma_f32_16x16x32_bf16 v[156:159], a[244:247], v[204:207], v[156:159]\n"
    "s_waitcnt lgkmcnt(4)\n"
    "v_mfma_f32_16x16x32_bf16 v[128:131], a[56:59], v[208:211], v[128:131]\n"
    "ds_read_b128 v[200:203], v123 offset:1024\n"
    "v_mfma_f32_16x16x32_bf16 v[132:135], a[120:123], v[208:211], v[132:135]\n"
    "ds_read_b128 v[204:207], v123 offset:17408\n"
    "v_mfma_f32_16x16x32_bf16 v[136:139], a[184:187], v[208:211], v[136:139]\n"
    "v_mfma_f32_16x16x32_bf16 v[140:143], a[248:251], v[208:211], v[140:143]\n"
    "v_mfma_f32_16x16x32_bf16 v[144:147], a[56:59], v[212:215], v[144:147]\n"
    "global_load_lds_dwordx4 v121, s[64:65] offset:1024 sc1\n"
    "v_mfma_f32_16x16x32_bf16 v[148:151], a[120:123], v[212:215], v[148:151]\n"
    "v_mfma_f32_16x16x32_bf16 v[152:155], a[184:187], v[212:215], v[152:155]\n"
    "v_mfma_f32_16x16x32_bf16 v[156:159], a[248:251], v[212:215], v[156:159]\n"
    "s_add_u32 s51, s51, 1\n"
    "s_waitcnt lgkmcnt(4)\n"
    "v_mfma_f32_16x16x32_bf16 v[128:131], a[60:63], v[216:219], v[128:131]\n"
    "ds_read_b128 v[208:211], v123 offset:2048\n"
    "v_mfma_f32_16x16x32_bf16 v[132:135], a[124:127], v[216:219], v[132:135]\n"
    "ds_read_b128 v[212:215], v123 offset:18432\n"
    "v_mfma_f32_16x16x32_bf16 v[136:139], a[188:191], v[216:219], v[136:139]\n"
    "v_mfma_f32_16x16x32_bf16 v[140:143], a[252:255], v[216:219], v[140:143]\n"
    "v_mfma_f32_16x16x32_bf16 v[144:147], a[60:63], v[220:223], v[144:147]\n"
    "global_load_lds_dwordx4 v121, s[64:65] offset:2048 sc1\n"
    "v_mfma_f32_16x16x32_bf16 v[148:151], a[124:127], v[220:223], v[148:151]\n"
    "v_mfma_f32_16x16x32_bf16 v[152:155], a[188:191], v[220:223], v[152:155]\n"
    "v_mfma_f32_16x16x32_bf16 v[156:159], a[252:255], v[220:223], v[156:159]\n"
    "s_add_u32 s58, s51, -3\n"
    "s_cmp_gt_u32 s58, 28\n"
    "s_waitcnt lgkmcnt(4)\n"
    "s_cbranch_scc0 G_loop%=\n"
    "s_branch G_pc%=\n"
    "G_pw%=:\n"
    "s_add_u32 s58, s40, 0x312000\n"
    "s_addc_u32 s59, s41, 0\n"
    "global_load_dwordx2 v[114:115], v112, s[58:59] sc1\n"
    "s_waitcnt vmcnt(0)\n"
    "G_pc%=:\n"
    "v_xor_b32 v114, s42, v114\n"
    "v_xor_b32 v115, s43, v115\n"
    "v_add_u32 v114, -1, v114\n"
    "v_cmp_le_u64 s[58:59], 16, v[114:115]\n"
    "v_and_b32 v114, 2, v114\n"
    "v_cmp_ne_u32 s[52:53], 0, v114\n"
    "s_cmp_eq_u64 s[58:59], 0\n"
    "s_cbranch_scc1 G_pok%=\n"
    "s_sleep 4\n"
    "s_add_u32 s57, s57, 1\n"
    "s_cmp_lt_u32 s57, 0x40000\n"
    "s_cbranch_scc1 G_pw%=\n"
    "G_pok%=:\n"
    "s_and_b32 s52, s52, 0x7ff\n"
    "s_bcnt1_i32_b32 s52, s52\n"
    "s_cmp_ge_u32 s52, 6\n"
    "s_cselect_b32 s52, 0, 0x2000\n"
    "s_xor_b32 s44, s44, s52\n"
    "s_xor_b32 s66, s66, s52\n"
    "s_nop 7\n"
    "v_cvt_pk_bf16_f32 v224, v128, v129\n"
    "v_cvt_pk_bf16_f32 v225, v130, v131\n"
    "v_cvt_pk_bf16_f32 v226, v132, v133\n"
    "v_cvt_pk_bf16_f32 v227, v134, v135\n"
    "v_cvt_pk_bf16_f32 v228, v144, v145\n"
    "v_cvt_pk_bf16_f32 v229, v146, v147\n"
    "v_cvt_pk_bf16_f32 v230, v148, v149\n"
    "v_cvt_pk_bf16_f32 v231, v150, v151\n"
    "v_cvt_pk_bf16_f32 v232, v136, v137\n"
    "v_cvt_pk_bf16_f32 v233, v138, v139\n"
    "v_cvt_pk_bf16_f32 v234, v140, v141\n"
    "v_cvt_pk_bf16_f32 v235, v142, v143\n"
    "v_cvt_pk_bf16_f32 v236, v152, v153\n"
    "v_cvt_pk_bf16_f32 v237, v154, v155\n"
    "v_cvt_pk_bf16_f32 v238, v156, v157\n"
    "v_cvt_pk_bf16_f32 v239, v158, v159\n"
    "global_store_dwordx4 v121, v[224:227], s[44:45] offset:0 sc1\n"
    "global_store_dwordx4 v121, v[228:231], s[44:45] offset:1024 sc1\n"
    "global_store_dwordx4 v121, v[232:235], s[44:45] offset:2048 sc1\n"
    "global_store_dwordx4 v121, v[236:239], s[44:45] offset:3072 sc1\n"
    "s_sub_u32 s44, s44, 0x4400000\n"
    "s_subb_u32 s45, s45, 0\n"
    "s_waitcnt vmcnt(0)\n"
    "s_barrier\n"
    "s_cmp_lg_u32 s56, 0\n"
    "s_cbranch_scc1 G_f3%=\n"
    "s_or_b32 s58, s42, s51\n"
    "s_mov_b64 exec, 1\n"
    "v_mov_b32 v116, s58\n"
    "global_store_dwordx2 v118, v[116:117], s[54:55] sc1\n"
    "s_mov_b64 exec, -1\n"
    "G_f3%=:\n"
    "s_branch G_loop%=\n"
    "G_t1%=:\n"
    "s_nop 7\n"
    "v_cvt_pk_bf16_f32 v224, v160, v161\n"
    "v_cvt_pk_bf16_f32 v225, v162, v163\n"
    "v_cvt_pk_bf16_f32 v226, v164, v165\n"
    "v_cvt_pk_bf16_f32 v227, v166, v167\n"
    "v_cvt_pk_bf16_f32 v228, v176, v177\n"
    "v_cvt_pk_bf16_f32 v229, v178, v179\n"
    "v_cvt_pk_bf16_f32 v230, v180, v181\n"
    "v_cvt_pk_bf16_f32 v231, v182, v183\n"
    "v_cvt_pk_bf16_f32 v232, v168, v169\n"
    "v_cvt_pk_bf16_f32 v233, v170, v171\n"
    "v_cvt_pk_bf16_f32 v234, v172, v173\n"
    "v_cvt_pk_bf16_f32 v235, v174, v175\n"
    "v_cvt_pk_bf16_f32 v236, v184, v185\n"
    "v_cvt_pk_bf16_f32 v237, v186, v187\n"
    "v_cvt_pk_bf16_f32 v238, v188, v189\n"
    "v_cvt_pk_bf16_f32 v239, v190, v191\n"
    "global_store_dwordx4 v121, v[224:227], s[44:45] offset:0 sc1\n"
    "global_store_dwordx4 v121, v[228:231], s[44:45] offset:1024 sc1\n"
    "global_store_dwordx4 v121, v[232:235], s[44:45] offset:2048 sc1\n"
    "global_store_dwordx4 v121, v[236:239], s[44:45] offset:3072 sc1\n"
    "s_sub_u32 s44, s44, 0x4400000\n"
    "s_subb_u32 s45, s45, 0\n"
    "s_waitcnt vmcnt(0)\n"
    "s_barrier\n"
    "s_cmp_lg_u32 s56, 0\n"
    "s_cbranch_scc1 G_f4%=\n"
    "s_or_b32 s58, s42, s51\n"
    "s_mov_b64 exec, 1\n"
    "v_mov_b32 v116, s58\n"
    "global_store_dwordx2 v118, v[116:117], s[54:55] sc1\n"
    "s_mov_b64 exec, -1\n"
    "G_f4%=:\n"
    "s_cmp_eq_u32 s51, 32\n"
    "s_cbranch_scc1 G_end%=\n"
    "s_add_u32 s58, s40, 0x20e000\n"
    "s_addc_u32 s59, s41, 0\n"
    "s_add_u32 s60, s40, 0x200200\n"
    "s_addc_u32 s61, s41, 0\n"
    "s_or_b32 s62, s42, 1\n"
    "s_mov_b32 s63, s43\n"
    "v_lshlrev_b32 v224, 3, v121\n"
    "G_xw1%=:\n"
    "global_load_dwordx2 v[226:227], v224, s[58:59] sc1\n"
    "G_xc1%=:\n"
    "s_waitcnt vmcnt(0)\n"
    "v_cmp_ne_u64 vcc, s[62:63], v[226:227]\n"
    "s_nop 0\n"
    "s_cbranch_vccz G_xok1%=\n"
    "s_sleep 4\n"
    "s_add_u32 s57, s57, 1\n"
    "s_and_b32 s68, s57, 0xff\n"
    "s_cbranch_scc1 G_xw1%=\n"
    "global_load_dwordx2 v[226:227], v118, s[60:61] sc1\n"
    "s_waitcnt vmcnt(0)\n"
    "v_cmp_eq_u64 vcc, s[42:43], v[226:227]\n"
    "s_nop 0\n"
    "s_cbranch_vccnz G_xok1%=\n"
    "s_cmp_lt_u32 s57, 0x40000\n"
    "s_cbranch_scc1 G_xw1%=\n"
    "v_mov_b32 v116, s42\n"
    "global_store_dwordx2 v118, v[116:117], s[60:61] sc1\n"
    "G_xok1%=:\n"
    "s_mov_b32 vcc_lo, 0xcccccccc\n"
    "s_mov_b32 vcc_hi, 0xcccccccc\n"
    "s_branch G_even%=\n"
    "G_end%=:\n"
    "s_waitcnt lgkmcnt(0)\n"
    :: GIN : GCLOB);
  else if constexpr (MAT == 1) asm volatile(
    "s_mov_b64 s[40:41], %0\n"
    "s_mov_b64 s[42:43], %1\n"
    "s_mov_b64 s[44:45], %2\n"
    "s_mov_b64 s[54:55], %3\n"
    "s_mov_b32 s56, %4\n"
    "v_mov_b32 v121, %5\n"
    "s_add_u32 s58, s40, 0x20e000\n"
    "s_addc_u32 s59, s41, 0\n"
    "v_lshlrev_b32 v225, 3, v121\n"
    "v_and_b32 v224, 0x380, v225\n"
    "global_load_dwordx2 v[226:227], v224, s[58:59] sc1\n"
    "v_xor_b32 v112, 0, %6\n"
    "v_xor_b32 v113, 32, %6\n"
    "v_xor_b32 v114, 64, %6\n"
    "v_xor_b32 v115, 96, %6\n"
    "ds_read_b64_tr_b16 a[0:1], v112 offset:0\n"
    "ds_read_b64_tr_b16 a[2:3], v112 offset:1024\n"
    "ds_read_b64_tr_b16 a[4:5], v112 offset:8192\n"
    "ds_read_b64_tr_b16 a[6:7], v112 offset:9216\n"
    "ds_read_b64_tr_b16 a[8:9], v112 offset:16384\n"
    "ds_read_b64_tr_b16 a[10:11], v112 offset:17408\n"
    "ds_read_b64_tr_b16 a[12:13], v112 offset:24576\n"
    "ds_read_b64_tr_b16 a[14:15], v112 offset:25600\n"
    "s_waitcnt lgkmcnt(6)\n"
    "ds_read_b64_tr_b16 a[16:17], v112 offset:32768\n"
    "ds_read_b64_tr_b16 a[18:19], v112 offset:33792\n"
    "ds_read_b64_tr_b16 a[20:21], v112 offset:40960\n"
    "ds_read_b64_tr_b16 a[22:23], v112 offset:41984\n"
    "ds_read_b64_tr_b16 a[24:25], v112 offset:49152\n"
    "ds_read_b64_tr_b16 a[26:27], v112 offset:50176\n"
    "ds_read_b64_tr_b16 a[28:29], v112 offset:57344\n"
    "ds_read_b64_tr_b16 a[30:31], v112 offset:58368\n"
    "s_waitcnt lgkmcnt(6)\n"
    "ds_read_b64_tr_b16 a[64:65], v113 offset:0\n"
    "ds_read_b64_tr_b16 a[66:67], v113 offset:1024\n"
    "ds_read_b64_tr_b16 a[68:69], v113 offset:8192\n"
    "ds_read_b64_tr_b16 a[70:71], v113 offset:9216\n"
    "ds_read_b64_tr_b16 a[72:73], v113 offset:16384\n"
    "ds_read_b64_tr_b16 a[74:75], v113 offset:17408\n"
    "ds_read_b64_tr_b16 a[76:77], v113 offset:24576\n"
    "ds_read_b64_tr_b16 a[78:79], v113 offset:25600\n"
    "s_waitcnt lgkmcnt(6)\n"
    "ds_read_b64_tr_b16 a[80:81], v113 offset:32768\n"
    "ds_read_b64_tr_b16 a[82:83], v113 offset:33792\n"
    "ds_read_b64_tr_b16 a[84:85], v113 offset:40960\n"
    "ds_read_b64_tr_b16 a[86:87], v113 offset:41984\n"
    "ds_read_b64_tr_b16 a[88:89], v113 offset:49152\n"
    "ds_read_b64_tr_b16 a[90:91], v113 offset:50176\n"
    "ds_read_b64_tr_b16 a[92:93], v113 offset:57344\n"
    "ds_read_b64_tr_b16 a[94:95], v113 offset:58368\n"
    "s_waitcnt lgkmcnt(6)\n"
    "ds_read_b64_tr_b16 a[128:129], v114 offset:0\n"
    "ds_read_b64_tr_b16 a[130:131], v114 offset:1024\n"
    "ds_read_b64_tr_b16 a[132:133], v114 offset:8192\n"
    "ds_read_b64_tr_b16 a[134:135], v114 offset:9216\n"
    "ds_read_b64_tr_b16 a[136:137], v114 offset:16384\n"
    "ds_read_b64_tr_b16 a[138:139], v114 offset:17408\n"
    "ds_read_b64_tr_b16 a[140:141], v114 offset:24576\n"
    "ds_read_b64_tr_b16 a[142:143], v114 offset:25600\n"
    "s_waitcnt lgkmcnt(6)\n"
    "ds_read_b64_tr_b16 a[144:145], v114 offset:32768\n"
    "ds_read_b64_tr_b16 a[146:147], v114 offset:33792\n"
    "ds_read_b64_tr_b16 a[148:149], v114 offset:40960\n"
    "ds_read_b64_tr_b16 a[150:151], v114 offset:41984\n"
    "ds_read_b64_tr_b16 a[152:153], v114 offset:49152\n"
    "ds_read_b64_tr_b16 a[154:155], v114 offset:50176\n"
    "ds_read_b64_tr_b16 a[156:157], v114 offset:57344\n"
    "ds_read_b64_tr_b16 a[158:159], v114 offset:58368\n"
    "s_waitcnt lgkmcnt(6)\n"
    "ds_read_b64_tr_b16 a[192:193], v115 offset:0\n"
    "ds_read_b64_tr_b16 a[194:195], v115 offset:1024\n"
    "ds_read_b64_tr_b16 a[196:197], v115 offset:8192\n"
    "ds_read_b64_tr_b16 a[198:199], v115 offset:9216\n"
    "ds_read_b64_tr_b16 a[200:201], v115 offset:16384\n"
    "ds_read_b64_tr_b16 a[202:203], v115 offset:17408\n"
    "ds_read_b64_tr_b16 a[204:205], v115 offset:24576\n"
    "ds_read_b64_tr_b16 a[206:207], v115 offset:25600\n"
    "s_waitcnt lgkmcnt(6)\n"
    "ds_read_b64_tr_b16 a[208:209], v115 offset:32768\n"
    "ds_read_b64_tr_b16 a[210:211], v115 offset:33792\n"
    "ds_read_b64_tr_b16 a[212:213], v115 offset:40960\n"
    "ds_read_b64_tr_b16 a[214:215], v115 offset:41984\n"
    "ds_read_b64_tr_b16 a[216:217], v115 offset:49152\n"
    "ds_read_b64_tr_b16 a[218:219], v115 offset:50176\n"
    "ds_read_b64_tr_b16 a[220:221], v115 offset:57344\n"
    "ds_read_b64_tr_b16 a[222:223], v115 offset:58368\n"
    "s_waitcnt lgkmcnt(6)\n"
    "v_xor_b32 v112, 0, %6\n"
    "v_add_u32 v112, 0x10000, v112\n"
    "v_xor_b32 v113, 32, %6\n"
    "v_add_u32 v113, 0x10000, v113\n"
    "v_xor_b32 v114, 64, %6\n"
    "v_add_u32 v114, 0x10000, v114\n"
    "v_xor_b32 v115, 96, %6\n"
    "v_add_u32 v115, 0x10000, v115\n"
    "ds_read_b64_tr_b16 a[32:33], v112 offset:0\n"
    "ds_read_b64_tr_b16 a[34:35], v112 offset:1024\n"
    "ds_read_b64_tr_b16 a[36:37], v112 offset:8192\n"
    "ds_read_b64_tr_b16 a[38:39], v112 offset:9216\n"
    "ds_read_b64_tr_b16 a[40:41], v112 offset:16384\n"
    "ds_read_b64_tr_b16 a[42:43], v112 offset:17408\n"
    "ds_read_b64_tr_b16 a[44:45], v112 offset:24576\n"
    "ds_read_b64_tr_b16 a[46:47], v112 offset:25600\n"
    "s_waitcnt lgkmcnt(6)\n"
    "ds_read_b64_tr_b16 a[48:49], v112 offset:32768\n"
    "ds_read_b64_tr_b16 a[50:51], v112 offset:33792\n"
    "ds_read_b64_tr_b16 a[52:53], v112 offset:40960\n"
    "ds_read_b64_tr_b16 a[54:55], v112 offset:41984\n"
    "ds_read_b64_tr_b16 a[56:57], v112 offset:49152\n"
    "ds_read_b64_tr_b16 a[58:59], v112 offset:50176\n"
    "ds_read_b64_tr_b16 a[60:61], v112 offset:57344\n"
    "ds_read_b64_tr_b16 a[62:63], v112 offset:58368\n"
    "s_waitcnt lgkmcnt(6)\n"
    "ds_read_b64_tr_b16 a[96:97], v113 offset:0\n"
    "ds_read_b64_tr_b16 a[98:99], v113 offset:1024\n"
    "ds_read_b64_tr_b16 a[100:101], v113 offset:8192\n"
    "ds_read_b64_tr_b16 a[102:103], v113 offset:9216\n"
    "ds_read_b64_tr_b16 a[104:105], v113 offset:16384\n"
    "ds_read_b64_tr_b16 a[106:107], v113 offset:17408\n"
    "ds_read_b64_tr_b16 a[108:109], v113 offset:24576\n"
    "ds_read_b64_tr_b16 a[110:111], v113 offset:25600\n"
    "s_waitcnt lgkmcnt(6)\n"
    "ds_read_b64_tr_b16 a[112:113], v113 offset:32768\n"
    "ds_read_b64_tr_b16 a[114:115], v113 offset:33792\n"
    "ds_read_b64_tr_b16 a[116:117], v113 offset:40960\n"
    "ds_read_b64_tr_b16 a[118:119], v113 offset:41984\n"
    "ds_read_b64_tr_b16 a[120:121], v113 offset:49152\n"
    "ds_read_b64_tr_b16 a[122:123], v113 offset:50176\n"
    "ds_read_b64_tr_b16 a[124:125], v113 offset:57344\n"
    "ds_read_b64_tr_b16 a[126:127], v113 offset:58368\n"
    "s_waitcnt lgkmcnt(6)\n"
    "ds_read_b64_tr_b16 a[160:161], v114 offset:0\n"
    "ds_read_b64_tr_b16 a[162:163], v114 offset:1024\n"
    "ds_read_b64_tr_b16 a[164:165], v114 offset:8192\n"
    "ds_read_b64_tr_b16 a[166:167], v114 offset:9216\n"
    "ds_read_b64_tr_b16 a[168:169], v114 offset:16384\n"
    "ds_read_b64_tr_b16 a[170:171], v114 offset:17408\n"
    "ds_read_b64_tr_b16 a[172:173], v114 offset:24576\n"
    "ds_read_b64_tr_b16 a[174:175], v114 offset:25600\n"
    "s_waitcnt lgkmcnt(6)\n"
    "ds_read_b64_tr_b16 a[176:177], v114 offset:32768\n"
    "ds_read_b64_tr_b16 a[178:179], v114 offset:33792\n"
    "ds_read_b64_tr_b16 a[180:181], v114 offset:40960\n"
    "ds_read_b64_tr_b16 a[182:183], v114 offset:41984\n"
    "ds_read_b64_tr_b16 a[184:185], v114 offset:49152\n"
    "ds_read_b64_tr_b16 a[186:187], v114 offset:50176\n"
    "ds_read_b64_tr_b16 a[188:189], v114 offset:57344\n"
    "ds_read_b64_tr_b16 a[190:191], v114 offset:58368\n"
    "s_waitcnt lgkmcnt(6)\n"
    "ds_read_b64_tr_b16 a[224:225], v115 offset:0\n"
    "ds_read_b64_tr_b16 a[226:227], v115 offset:1024\n"
    "ds_read_b64_tr_b16 a[228:229], v115 offset:8192\n"
    "ds_read_b64_tr_b16 a[230:231], v115 offset:9216\n"
    "ds_read_b64_tr_b16 a[232:233], v115 offset:16384\n"
    "ds_read_b64_tr_b16 a[234:235], v115 offset:17408\n"
    "ds_read_b64_tr_b16 a[236:237], v115 offset:24576\n"
    "ds_read_b64_tr_b16 a[238:239], v115 offset:25600\n"
    "s_waitcnt lgkmcnt(6)\n"
    "ds_read_b64_tr_b16 a[240:241], v115 offset:32768\n"
    "ds_read_b64_tr_b16 a[242:243], v115 offset:33792\n"
    "ds_read_b64_tr_b16 a[244:245], v115 offset:40960\n"
    "ds_read_b64_tr_b16 a[246:247], v115 offset:41984\n"
    "ds_read_b64_tr_b16 a[248:249], v115 offset:49152\n"
    "ds_read_b64_tr_b16 a[250:251], v115 offset:50176\n"
    "ds_read_b64_tr_b16 a[252:253], v115 offset:57344\n"
    "ds_read_b64_tr_b16 a[254:255], v115 offset:58368\n"
    "s_waitcnt lgkmcnt(6)\n"
    "s_lshr_b32 s58, s56, 1\n"
    "s_lshl_b32 s58, s58, 15\n"
    "v_add_u32 v122, s58, v121\n"
    "v_add_u32 v123, 0x10000, v122\n"
    "s_lshl_b32 s50, s56, 14\n"
    "v_mov_b32 v118, 0\n"
    "v_mov_b32 v117, s43\n"
    "s_add_u32 s58, s50, 0x400000\n"
    "s_add_u32 s48, s40, s58\n"
    "s_addc_u32 s49, s41, 0\n"
    "s_mov_b32 s57, 0\n"
    "s_mov_b32 s51, 0\n"
    "s_waitcnt lgkmcnt(0)\n"
    "s_barrier\n"
    "s_add_u32 s58, s40, 0x20e000\n"
    "s_addc_u32 s59, s41, 0\n"
    "s_add_u32 s60, s40, 0x200200\n"
    "s_addc_u32 s61, s41, 0\n"
    "s_or_b32 s62, s42, 1\n"
    "s_mov_b32 s63, s43\n"
    "s_branch G_xc0%=\n"
    "G_xw0%=:\n"
    "global_load_dwordx2 v[226:227], v224, s[58:59] sc1\n"
    "G_xc0%=:\n"
    "s_waitcnt vmcnt(0)\n"
    "v_cmp_ne_u64 vcc, s[62:63], v[226:227]\n"
    "s_nop 0\n"
    "s_cbranch_vccz G_xok0%=\n"
    "s_sleep 4\n"
    "s_add_u32 s57, s57, 1\n"
    "s_and_b32 s68, s57, 0xff\n"
    "s_cbranch_scc1 G_xw0%=\n"
    "global_load_dwordx2 v[226:227], v118, s[60:61] sc1\n"
    "s_waitcnt vmcnt(0)\n"
    "v_cmp_eq_u64 vcc, s[42:43], v[226:227]\n"
    "s_nop 0\n"
    "s_cbranch_vccnz G_xok0%=\n"
    "s_cmp_lt_u32 s57, 0x40000\n"
    "s_cbranch_scc1 G_xw0%=\n"
    "v_mov_b32 v116, s42\n"
    "global_store_dwordx2 v118, v[116:117], s[60:61] sc1\n"
    "G_xok0%=:\n"
    "s_mov_b32 vcc_lo, 0xcccccccc\n"
    "s_mov_b32 vcc_hi, 0xcccccccc\n"
    "s_add_u32 m0, s50, 0\n"
    "s_add_u32 s64, s48, 0\n"
    "s_addc_u32 s65, s49, 0\n"
    "global_load_lds_dwordx4 v121, s[64:65] offset:0 sc1\n"
    "global_load_lds_dwordx4 v121, s[64:65] offset:1024 sc1\n"
    "global_load_lds_dwordx4 v121, s[64:65] offset:2048 sc1\n"
    "global_load_lds_dwordx4 v121, s[64:65] offset:3072 sc1\n"
    "s_add_u32 m0, s50, 4096\n"
    "s_add_u32 s64, s48, 4096\n"
    "s_addc_u32 s65, s49, 0\n"
    "global_load_lds_dwordx4 v121, s[64:65] offset:0 sc1\n"
    "global_load_lds_dwordx4 v121, s[64:65] offset:1024 sc1\n"
    "global_load_lds_dwordx4 v121, s[64:65] offset:2048 sc1\n"
    "global_load_lds_dwordx4 v121, s[64:65] offset:3072 sc1\n"
    "s_add_u32 m0, s50, 8192\n"
    "s_add_u32 s64, s48, 8192\n"
    "s_addc_u32 s65, s49, 0\n"
    "global_load_lds_dwordx4 v121, s[64:65] offset:0 sc1\n"
    "global_load_lds_dwordx4 v121, s[64:65] offset:1024 sc1\n"
    "global_load_lds_dwordx4 v121, s[64:65] offset:2048 sc1\n"
    "global_load_lds_dwordx4 v121, s[64:65] offset:3072 sc1\n"
    "s_add_u32 m0, s50, 12288\n"
    "s_add_u32 s64, s48, 12288\n"
    "s_addc_u32 s65, s49, 0\n"
    "s_add_u32 s48, s48, 0x10000\n"
    "s_addc_u32 s49, s49, 0\n"
    "global_load_lds_dwordx4 v121, s[64:65] offset:0 sc1\n"
    "global_load_lds_dwordx4 v121, s[64:65] offset:1024 sc1\n"
    "global_load_lds_dwordx4 v121, s[64:65] offset:2048 sc1\n"
    "global_load_lds_dwordx4 v121, s[64:65] offset:3072 sc1\n"
    "s_add_u32 m0, s50, 65536\n"
    "s_add_u32 s64, s48, 0\n"
    "s_addc_u32 s65, s49, 0\n"
    "global_load_lds_dwordx4 v121, s[64:65] offset:0 sc1\n"
    "global_load_lds_dwordx4 v121, s[64:65] offset:1024 sc1\n"
    "global_load_lds_dwordx4 v121, s[64:65] offset:2048 sc1\n"
    "global_load_lds_dwordx4 v121, s[64:65] offset:3072 sc1\n"
    "s_add_u32 m0, s50, 69632\n"
    "s_add_u32 s64, s48, 4096\n"
    "s_addc_u32 s65, s49, 0\n"
    "global_load_lds_dwordx4 v121, s[64:65] offset:0 sc1\n"
    "global_load_lds_dwordx4 v121, s[64:65] offset:1024 sc1\n"
    "global_load_lds_dwordx4 v121, s[64:65] offset:2048 sc1\n"
    "global_load_lds_dwordx4 v121, s[64:65] offset:3072 sc1\n"
    "s_add_u32 m0, s50, 73728\n"
    "s_add_u32 s64, s48, 8192\n"
    "s_addc_u32 s65, s49, 0\n"
    "global_load_lds_dwordx4 v121, s[64:65] offset:0 sc1\n"
    "global_load_lds_dwordx4 v121, s[64:65] offset:1024 sc1\n"
    "global_load_lds_dwordx4 v121, s[64:65] offset:2048 sc1\n"
    "s_mov_b64 s[66:67], s[44:45]\n"
    "s_sub_u32 s44, s44, 0x300000\n"
    "s_subb_u32 s45, s45, 0\n"
    "s_waitcnt vmcnt(19) lgkmcnt(0)\n"
    "s_barrier\n"
    "v_lshlrev_b32 v112, 3, v121\n"
    "v_min_u32 v112, 0x500, v112\n"
    "s_add_u32 s58, s40, 0x312000\n"
    "s_addc_u32 s59, s41, 0\n"
    "global_load_dwordx2 v[114:115], v112, s[58:59] sc1\n"
    "ds_read_b128 v[192:195], v122 offset:0\n"
    "ds_read_b128 v[196:199], v122 offset:16384\n"
    "ds_read_b128 v[200:203], v122 offset:1024\n"
    "ds_read_b128 v[204:207], v122 offset:17408\n"
    "ds_read_b128 v[208:211], v122 offset:2048\n"
    "ds_read_b128 v[212:215], v122 offset:18432\n"
    "s_waitcnt lgkmcnt(4)\n"
    "s_branch G_even%=\n"
    ".p2align 6\n"
    "G_loop%=:\n"
    "v_mfma_f32_16x16x32_bf16 v[160:163], a[0:3], v[192:195], 0\n"
    "ds_read_b128 v[216:219], v123 offset:3072\n"
    "v_mfma_f32_16x16x32_bf16 v[164:167], a[64:67], v[192:195], 0\n"
    "ds_read_b128 v[220:223], v123 offset:19456\n"
    "v_mfma_f32_16x16x32_bf16 v[168:171], a[128:131], v[192:195], 0\n"
    "v_mfma_f32_16x16x32_bf16 v[172:175], a[192:195], v[192:195], 0\n"
    "v_cvt_pk_bf16_f32 v224, v128, v129\n"
    "v_cvt_pk_bf16_f32 v225, v130, v131\n"
    "v_mfma_f32_16x16x32_bf16 v[176:179], a[0:3], v[196:199], 0\n"
    "global_load_lds_dwordx4 v121, s[64:65] offset:3072 sc1\n"
    "v_mfma_f32_16x16x32_bf16 v[180:183], a[64:67], v[196:199], 0\n"
    "v_cvt_pk_bf16_f32 v226, v132, v133\n"
    "v_cvt_pk_bf16_f32 v227, v134, v135\n"
    "v_mfma_f32_16x16x32_bf16 v[184:187], a[128:131], v[196:199], 0\n"
    "v_cvt_pk_bf16_f32 v228, v144, v145\n"
    "v_cvt_pk_bf16_f32 v229, v146, v147\n"
    "v_mfma_f32_16x16x32_bf16 v[188:191], a[192:195], v[196:199], 0\n"
    "v_cvt_pk_bf16_f32 v230, v148, v149\n"
    "v_cvt_pk_bf16_f32 v231, v150, v151\n"
    "s_waitcnt lgkmcnt(4)\n"
    "v_mfma_f32_16x16x32_bf16 v[160:163], a[4:7], v[200:203], v[160:163]\n"
    "ds_read_b128 v[192:195], v123 offset:4096\n"
    "v_mfma_f32_16x16x32_bf16 v[164:167], a[68:71], v[200:203], v[164:167]\n"
    "ds_read_b128 v[196:199], v123 offset:20480\n"
    "v_mfma_f32_16x16x32_bf16 v[168:171], a[132:135], v[200:203], v[168:171]\n"
    "s_add_u32 m0, s50, 12288\n"
    "s_add_u32 s64, s48, 12288\n"
    "s_addc_u32 s65, s49, 0\n"
    "v_cvt_pk_bf16_f32 v232, v136, v137\n"
    "v_cvt_pk_bf16_f32 v233, v138, v139\n"
    "v_mfma_f32_16x16x32_bf16 v[172:175], a[196:199], v[200:203], v[172:175]\n"
    "s_add_u32 s48, s48, 0x10000\n"
    "s_addc_u32 s49, s49, 0\n"
    "v_cvt_pk_bf16_f32 v234, v140, v141\n"
    "v_cvt_pk_bf16_f32 v235, v142, v143\n"
    "v_mfma_f32_16x16x32_bf16 v[176:179], a[4:7], v[204:207], v[176:179]\n"
    "global_load_lds_dwordx4 v121, s[64:65] offset:0 sc1\n"
    "v_mfma_f32_16x16x32_bf16 v[180:183], a[68:71], v[204:207], v[180:183]\n"
    "v_cvt_pk_bf16_f32 v236, v152, v153\n"
    "v_cvt_pk_bf16_f32 v237, v154, v155\n"
    "v_mfma_f32_16x16x32_bf16 v[184:187], a[132:135], v[204:207], v[184:187]\n"
    "v_mfma_f32_16x16x32_bf16 v[188:191], a[196:199], v[204:207], v[188:191]\n"
    "v_cvt_pk_bf16_f32 v238, v156, v157\n"
    "v_cvt_pk_bf16_f32 v239, v158, v159\n"
    "s_waitcnt lgkmcnt(4)\n"
    "v_mfma_f32_16x16x32_bf16 v[160:163], a[8:11], v[208:211], v[160:163]\n"
    "ds_read_b128 v[200:203], v123 offset:5120\n"
    "v_mfma_f32_16x16x32_bf16 v[164:167], a[72:75], v[208:211], v[164:167]\n"
    "ds_read_b128 v[204:207], v123 offset:21504\n"
    "v_mfma_f32_16x16x32_bf16 v[168:171], a[136:139], v[208:211], v[168:171]\n"
    "s_not_b64 vcc, vcc\n"
    "v_cndmask_b32_dpp v240, v232, v224, vcc quad_perm:[0,1,0,1] row_mask:0xf bank_mask:0xf\n"
    "v_cndmask_b32_dpp v241, v233, v225, vcc quad_perm:[0,1,0,1] row_mask:0xf bank_mask:0xf\n"
    "v_mfma_f32_16x16x32_bf16 v[172:175], a[200:203], v[208:211], v[172:175]\n"
    "v_cndmask_b32_dpp v242, v234, v226, vcc quad_perm:[0,1,0,1] row_mask:0xf bank_mask:0xf\n"
    "v_cndmask_b32_dpp v243, v235, v227, vcc quad_perm:[0,1,0,1] row_mask:0xf bank_mask:0xf\n"
    "v_mfma_f32_16x16x32_bf16 v[176:179], a[8:11], v[212:215], v[176:179]\n"
    "global_load_lds_dwordx4 v121, s[64:65] offset:1024 sc1\n"
    "v_mfma_f32_16x16x32_bf16 v[180:183], a[72:75], v[212:215], v[180:183]\n"
    "v_cndmask_b32_dpp v248, v236, v228, vcc quad_perm:[0,1,0,1] row_mask:0xf bank_mask:0xf\n"
    "v_cndmask_b32_dpp v249, v237, v229, vcc quad_perm:[0,1,0,1] row_mask:0xf bank_mask:0xf\n"
    "v_mfma_f32_16x16x32_bf16 v[184:187], a[136:139], v[212:215], v[184:187]\n"
    "global_store_dwordx4 v121, v[240:243], s[44:45] offset:0 sc1\n"
    "v_mfma_f32_16x16x32_bf16 v[188:191], a[200:203], v[212:215], v[188:191]\n"
    "v_cndmask_b32_dpp v250, v238, v230, vcc quad_perm:[0,1,0,1] row_mask:0xf bank_mask:0xf\n"
    "v_cndmask_b32_dpp v251, v239, v231, vcc quad_perm:[0,1,0,1] row_mask:0xf bank_mask:0xf\n"
    "s_waitcnt lgkmcnt(4)\n"
    "v_mfma_f32_16x16x32_bf16 v[160:163], a[12:15], v[216:219], v[160:163]\n"
    "ds_read_b128 v[208:211], v123 offset:6144\n"
    "v_mfma_f32_16x16x32_bf16 v[164:167], a[76:79], v[216:219], v[164:167]\n"
    "ds_read_b128 v[212:215], v123 offset:22528\n"
    "v_mfma_f32_16x16x32_bf16 v[168:171], a[140:143], v[216:219], v[168:171]\n"
    "s_not_b64 vcc, vcc\n"
    "v_cndmask_b32_dpp v244, v224, v232, vcc quad_perm:[2,3,2,3] row_mask:0xf bank_mask:0xf\n"
    "v_cndmask_b32_dpp v245, v225, v233, vcc quad_perm:[2,3,2,3] row_mask:0xf bank_mask:0xf\n"
    "v_mfma_f32_16x16x32_bf16 v[172:175], a[204:207], v[216:219], v[172:175]\n"
    "v_cndmask_b32_dpp v246, v226, v234, vcc quad_perm:[2,3,2,3] row_mask:0xf bank_mask:0xf\n"
    "v_cndmask_b32_dpp v247, v227, v235, vcc quad_perm:[2,3,2,3] row_mask:0xf bank_mask:0xf\n"
    "v_mfma_f32_16x16x32_bf16 v[176:179], a[12:15], v[220:223], v[176:179]\n"
    "global_load_lds_dwordx4 v121, s[64:65] offset:2048 sc1\n"
    "v_mfma_f32_16x16x32_bf16 v[180:183], a[76:79], v[220:223], v[180:183]\n"
    "v_cndmask_b32_dpp v252, v228, v236, vcc quad_perm:[2,3,2,3] row_mask:0xf bank_mask:0xf\n"
    "v_cndmask_b32_dpp v253, v229, v237, vcc quad_perm:[2,3,2,3] row_mask:0xf bank_mask:0xf\n"
    "v_mfma_f32_16x16x32_bf16 v[184:187], a[140:143], v[220:223], v[184:187]\n"
    "global_store_dwordx4 v121, v[248:251], s[44:45] offset:2048 sc1\n"
    "v_mfma_f32_16x16x32_bf16 v[188:191], a[204:207], v[220:223], v[188:191]\n"
    "v_cndmask_b32_dpp v254, v230, v238, vcc quad_perm:[2,3,2,3] row_mask:0xf bank_mask:0xf\n"
    "v_cndmask_b32_dpp v255, v231, v239, vcc quad_perm:[2,3,2,3] row_mask:0xf bank_mask:0xf\n"
    "s_waitcnt lgkmcnt(4)\n"
    "v_mfma_f32_16x16x32_bf16 v[160:163], a[16:19], v[192:195], v[160:163]\n"
    "ds_read_b128 v[216:219], v123 offset:7168\n"
    "v_mfma_f32_16x16x32_bf16 v[164:167], a[80:83], v[192:195], v[164:167]\n"
    "ds_read_b128 v[220:223], v123 offset:23552\n"
    "v_mfma_f32_16x16x32_bf16 v[168:171], a[144:147], v[192:195], v[168:171]\n"
    "global_store_dwordx4 v121, v[244:247], s[44:45] offset:1024 sc1\n"
    "v_mfma_f32_16x16x32_bf16 v[172:175], a[208:211], v[192:195], v[172:175]\n"
    "v_mfma_f32_16x16x32_bf16 v[176:179], a[16:19], v[196:199], v[176:179]\n"
    "global_load_lds_dwordx4 v121, s[64:65] offset:3072 sc1\n"
    "v_mfma_f32_16x16x32_bf16 v[180:183], a[80:83], v[196:199], v[180:183]\n"
    "v_mfma_f32_16x16x32_bf16 v[184:187], a[144:147], v[196:199], v[184:187]\n"
    "global_store_dwordx4 v121, v[252:255], s[44:45] offset:3072 sc1\n"
    "v_mfma_f32_16x16x32_bf16 v[188:191], a[208:211], v[196:199], v[188:191]\n"
    "s_waitcnt lgkmcnt(4)\n"
    "v_mfma_f32_16x16x32_bf16 v[160:163], a[20:23], v[200:203], v[160:163]\n"
    "s_waitcnt vmcnt(12) lgkmcnt(0)\n"
    "s_barrier\n"
    "v_mfma_f32_16x16x32_bf16 v[164:167], a[84:87], v[200:203], v[164:167]\n"
    "ds_read_b128 v[192:195], v123 offset:8192\n"
    "v_mfma_f32_16x16x32_bf16 v[168:171], a[148:151], v[200:203], v[168:171]\n"
    "ds_read_b128 v[196:199], v123 offset:24576\n"
    "s_add_u32 m0, s50, 65536\n"
    "s_add_u32 s64, s48, 0\n"
    "s_addc_u32 s65, s49, 0\n"
    "v_mfma_f32_16x16x32_bf16 v[172:175], a[212:215], v[200:203], v[172:175]\n"
    "v_mfma_f32_16x16x32_bf16 v[176:179], a[20:23], v[204:207], v[176:179]\n"
    "global_load_lds_dwordx4 v121, s[64:65] offset:0 sc1\n"
    "v_mfma_f32_16x16x32_bf16 v[180:183], a[84:87], v[204:207], v[180:183]\n"
    "v_mfma_f32_16x16x32_bf16 v[184:187], a[148:151], v[204:207], v[184:187]\n"
    "v_mfma_f32_16x16x32_bf16 v[188:191], a[212:215], v[204:207], v[188:191]\n"
    "s_waitcnt lgkmcnt(4)\n"
    "v_mfma_f32_16x16x32_bf16 v[160:163], a[24:27], v[208:211], v[160:163]\n"
    "ds_read_b128 v[200:203], v123 offset:9216\n"
    "v_mfma_f32_16x16x32_bf16 v[164:167], a[88:91], v[208:211], v[164:167]\n"
    "ds_read_b128 v[204:207], v123 offset:25600\n"
    "v_mfma_f32_16x16x32_bf16 v[168:171], a[152:155], v[208:211], v[168:171]\n"
    "v_mfma_f32_16x16x32_bf16 v[172:175], a[216:219], v[208:211], v[172:175]\n"
    "v_mfma_f32_16x16x32_bf16 v[176:179], a[24:27], v[212:215], v[176:179]\n"
    "global_load_lds_dwordx4 v121, s[64:65] offset:1024 sc1\n"
    "v_mfma_f32_16x16x32_bf16 v[180:183], a[88:91], v[212:215], v[180:183]\n"
    "v_mfma_f32_16x16x32_bf16 v[184:187], a[152:155], v[212:215], v[184:187]\n"
    "v_mfma_f32_16x16x32_bf16 v[188:191], a[216:219], v[212:215], v[188:191]\n"
    "s_waitcnt lgkmcnt(4)\n"
    "v_mfma_f32_16x16x32_bf16 v[160:163], a[28:31], v[216:219], v[160:163]\n"
    "ds_read_b128 v[208:211], v123 offset:10240\n"
    "v_mfma_f32_16x16x32_bf16 v[164:167], a[92:95], v[216:219], v[164:167]\n"
    "ds_read_b128 v[212:215], v123 offset:26624\n"
    "v_mfma_f32_16x16x32_bf16 v[168:171], a[156:159], v[216:219], v[168:171]\n"
    "v_mfma_f32_16x16x32_bf16 v[172:175], a[220:223], v[216:219], v[172:175]\n"
    "v_mfma_f32_16x16x32_bf16 v[176:179], a[28:31], v[220:223], v[176:179]\n"
    "global_load_lds_dwordx4 v121, s[64:65] offset:2048 sc1\n"
    "v_mfma_f32_16x16x32_bf16 v[180:183], a[92:95], v[220:223], v[180:183]\n"
    "s_mov_b64 s[44:45], s[66:67]\n"
    "s_add_u32 s66, s66, 0x100000\n"
    "s_addc_u32 s67, s67, 0\n"
    "v_mfma_f32_16x16x32_bf16 v[184:187], a[156:159], v[220:223], v[184:187]\n"
    "s_xor_b32 s66, s66, 0x2000\n"
    "v_mfma_f32_16x16x32_bf16 v[188:191], a[220:223], v[220:223], v[188:191]\n"
    "s_waitcnt lgkmcnt(4)\n"
    "v_mfma_f32_16x16x32_bf16 v[160:163], a[32:35], v[192:195], v[160:163]\n"
    "ds_read_b128 v[216:219], v123 offset:11264\n"
    "v_mfma_f32_16x16x32_bf16 v[164:167], a[96:99], v[192:195], v[164:167]\n"
    "ds_read_b128 v[220:223], v123 offset:27648\n"
    "v_mfma_f32_16x16x32_bf16 v[168:171], a[160:163], v[192:195], v[168:171]\n"
    "v_mfma_f32_16x16x32_bf16 v[172:175], a[224:227], v[192:195], v[172:175]\n"
    "v_mfma_f32_16x16x32_bf16 v[176:179], a[32:35], v[196:199], v[176:179]\n"
    "global_load_lds_dwordx4 v121, s[64:65] offset:3072 sc1\n"
    "v_mfma_f32_16x16x32_bf16 v[180:183], a[96:99], v[196:199], v[180:183]\n"
    "v_mfma_f32_16x16x32_bf16 v[184:187], a[160:163], v[196:199], v[184:187]\n"
    "v_mfma_f32_16x16x32_bf16 v[188:191], a[224:227], v[196:199], v[188:191]\n"
    "s_waitcnt lgkmcnt(4)\n"
    "v_mfma_f32_16x16x32_bf16 v[160:163], a[36:39], v[200:203], v[160:163]\n"
    "ds_read_b128 v[192:195], v123 offset:12288\n"
    "v_mfma_f32_16x16x32_bf16 v[164:167], a[100:103], v[200:203], v[164:167]\n"
    "ds_read_b128 v[196:199], v123 offset:28672\n"
    "v_mfma_f32_16x16x32_bf16 v[168:171], a[164:167], v[200:203], v[168:171]\n"
    "s_add_u32 m0, s50, 69632\n"
    "s_add_u32 s64, s48, 4096\n"
    "s_addc_u32 s65, s49, 0\n"
    "v_mfma_f32_16x16x32_bf16 v[172:175], a[228:231], v[200:203], v[172:175]\n"
    "v_mfma_f32_16x16x32_bf16 v[176:179], a[36:39], v[204:207], v[176:179]\n"
    "global_load_lds_dwordx4 v121, s[64:65] offset:0 sc1\n"
    "v_mfma_f32_16x16x32_bf16 v[180:183], a[100:103], v[204:207], v[180:183]\n"
    "v_mfma_f32_16x16x32_bf16 v[184:187], a[164:167], v[204:207], v[184:187]\n"
    "v_mfma_f32_16x16x32_bf16 v[188:191], a[228:231], v[204:207], v[188:191]\n"
    "s_waitcnt lgkmcnt(4)\n"
    "v_mfma_f32_16x16x32_bf16 v[160:163], a[40:43], v[208:211], v[160:163]\n"
    "ds_read_b128 v[200:203], v123 offset:13312\n"
    "v_mfma_f32_16x16x32_bf16 v[164:167], a[104:107], v[208:211], v[164:167]\n"
    "ds_read_b128 v[204:207], v123 offset:29696\n"
    "v_mfma_f32_16x16x32_bf16 v[168:171], a[168:171], v[208:211], v[168:171]\n"
    "v_mfma_f32_16x16x32_bf16 v[172:175], a[232:235], v[208:211], v[172:175]\n"
    "v_mfma_f32_16x16x32_bf16 v[176:179], a[40:43], v[212:215], v[176:179]\n"
    "global_load_lds_dwordx4 v121, s[64:65] offset:1024 sc1\n"
    "v_mfma_f32_16x16x32_bf16 v[180:183], a[104:107], v[212:215], v[180:183]\n"
    "v_mfma_f32_16x16x32_bf16 v[184:187], a[168:171], v[212:215], v[184:187]\n"
    "v_mfma_f32_16x16x32_bf16 v[188:191], a[232:235], v[212:215], v[188:191]\n"
    "s_waitcnt lgkmcnt(4)\n"
    "v_mfma_f32_16x16x32_bf16 v[160:163], a[44:47], v[216:219], v[160:163]\n"
    "ds_read_b128 v[208:211], v123 offset:14336\n"
    "v_mfma_f32_16x16x32_bf16 v[164:167], a[108:111], v[216:219], v[164:167]\n"
    "ds_read_b128 v[212:215], v123 offset:30720\n"
    "v_mfma_f32_16x16x32_bf16 v[168:171], a[172:175], v[216:219], v[168:171]\n"
    "v_mfma_f32_16x16x32_bf16 v[172:175], a[236:239], v[216:219], v[172:175]\n"
    "v_mfma_f32_16x16x32_bf16 v[176:179], a[44:47], v[220:223], v[176:179]\n"
    "global_load_lds_dwordx4 v121, s[64:65] offset:2048 sc1\n"
    "v_mfma_f32_16x16x32_bf16 v[180:183], a[108:111], v[220:223], v[180:183]\n"
    "v_mfma_f32_16x16x32_bf16 v[184:187], a[172:175], v[220:223], v[184:187]\n"
    "v_mfma_f32_16x16x32_bf16 v[188:191], a[236:239], v[220:223], v[188:191]\n"
    "s_waitcnt lgkmcnt(4)\n"
    "v_mfma_f32_16x16x32_bf16 v[160:163], a[48:51], v[192:195], v[160:163]\n"
    "ds_read_b128 v[216:219], v123 offset:15360\n"
    "v_mfma_f32_16x16x32_bf16 v[164:167], a[112:115], v[192:195], v[164:167]\n"
    "ds_read_b128 v[220:223], v123 offset:31744\n"
    "v_mfma_f32_16x16x32_bf16 v[168:171], a[176:179], v[192:195], v[168:171]\n"
    "v_mfma_f32_16x16x32_bf16 v[172:175], a[240:243], v[192:195], v[172:175]\n"
    "v_mfma_f32_16x16x32_bf16 v[176:179], a[48:51], v[196:199], v[176:179]\n"
    "global_load_lds_dwordx4 v121, s[64:65] offset:3072 sc1\n"
    "v_mfma_f32_16x16x32_bf16 v[180:183], a[112:115], v[196:199], v[180:183]\n"
    "v_mfma_f32_16x16x32_bf16 v[184:187], a[176:179], v[196:199], v[184:187]\n"
    "v_mfma_f32_16x16x32_bf16 v[188:191], a[240:243], v[196:199], v[188:191]\n"
    "s_waitcnt lgkmcnt(4)\n"
    "v_mfma_f32_16x16x32_bf16 v[160:163], a[52:55], v[200:203], v[160:163]\n"
    "s_waitcnt vmcnt(8) lgkmcnt(0)\n"
    "s_barrier\n"
    "v_mfma_f32_16x16x32_bf16 v[164:167], a[116:119], v[200:203], v[164:167]\n"
    "ds_read_b128 v[192:195], v122 offset:0\n"
    "v_mfma_f32_16x16x32_bf16 v[168:171], a[180:183], v[200:203], v[168:171]\n"
    "ds_read_b128 v[196:199], v122 offset:16384\n"
    "s_add_u32 m0, s50, 73728\n"
    "s_add_u32 s64, s48, 8192\n"
    "s_addc_u32 s65, s49, 0\n"
    "v_mfma_f32_16x16x32_bf16 v[172:175], a[244:247], v[200:203], v[172:175]\n"
    "v_mfma_f32_16x16x32_bf16 v[176:179], a[52:55], v[204:207], v[176:179]\n"
    "global_load_lds_dwordx4 v121, s[64:65] offset:0 sc1\n"
    "v_mfma_f32_16x16x32_bf16 v[180:183], a[116:119], v[204:207], v[180:183]\n"
    "s_cmp_lg_u32 s56, 0\n"
    "s_cbranch_scc1 G_f1%=\n"
    "s_or_b32 s58, s42, s51\n"
    "s_mov_b64 exec, 1\n"
    "v_mov_b32 v116, s58\n"
    "global_store_dwordx2 v118, v[116:117], s[54:55] sc1\n"
    "s_mov_b64 exec, -1\n"
    "G_f1%=:\n"
    "v_mfma_f32_16x16x32_bf16 v[184:187], a[180:183], v[204:207], v[184:187]\n"
    "v_mfma_f32_16x16x32_bf16 v[188:191], a[244:247], v[204:207], v[188:191]\n"
    "s_waitcnt lgkmcnt(4)\n"
    "v_mfma_f32_16x16x32_bf16 v[160:163], a[56:59], v[208:211], v[160:163]\n"
    "ds_read_b128 v[200:203], v122 offset:1024\n"
    "v_mfma_f32_16x16x32_bf16 v[164:167], a[120:123], v[208:211], v[164:167]\n"
    "ds_read_b128 v[204:207], v122 offset:17408\n"
    "v_mfma_f32_16x16x32_bf16 v[168:171], a[184:187], v[208:211], v[168:171]\n"
    "v_mfma_f32_16x16x32_bf16 v[172:175], a[248:251], v[208:211], v[172:175]\n"
    "v_mfma_f32_16x16x32_bf16 v[176:179], a[56:59], v[212:215], v[176:179]\n"
    "global_load_lds_dwordx4 v121, s[64:65] offset:1024 sc1\n"
    "v_mfma_f32_16x16x32_bf16 v[180:183], a[120:123], v[212:215], v[180:183]\n"
    "v_mfma_f32_16x16x32_bf16 v[184:187], a[184:187], v[212:215], v[184:187]\n"
    "v_mfma_f32_16x16x32_bf16 v[188:191], a[248:251], v[212:215], v[188:191]\n"
    "s_add_u32 s51, s51, 1\n"
    "s_waitcnt lgkmcnt(4)\n"
    "v_mfma_f32_16x16x32_bf16 v[160:163], a[60:63], v[216:219], v[160:163]\n"
    "ds_read_b128 v[208:211], v122 offset:2048\n"
    "v_mfma_f32_16x16x32_bf16 v[164:167], a[124:127], v[216:219], v[164:167]\n"
    "ds_read_b128 v[212:215], v122 offset:18432\n"
    "v_mfma_f32_16x16x32_bf16 v[168:171], a[188:191], v[216:219], v[168:171]\n"
    "v_mfma_f32_16x16x32_bf16 v[172:175], a[252:255], v[216:219], v[172:175]\n"
    "v_mfma_f32_16x16x32_bf16 v[176:179], a[60:63], v[220:223], v[176:179]\n"
    "global_load_lds_dwordx4 v121, s[64:65] offset:2048 sc1\n"
    "v_mfma_f32_16x16x32_bf16 v[180:183], a[124:127], v[220:223], v[180:183]\n"
    "v_mfma_f32_16x16x32_bf16 v[184:187], a[188:191], v[220:223], v[184:187]\n"
    "v_mfma_f32_16x16x32_bf16 v[188:191], a[252:255], v[220:223], v[188:191]\n"
    "s_add_u32 s58, s51, -3\n"
    "s_cmp_gt_u32 s58, 28\n"
    "s_waitcnt lgkmcnt(4)\n"
    "s_cbranch_scc1 G_t1%=\n"
    "G_even%=:\n"
    "v_mfma_f32_16x16x32_bf16 v[128:131], a[0:3], v[192:195], 0\n"
    "ds_read_b128 v[216:219], v122 offset:3072\n"
    "v_mfma_f32_16x16x32_bf16 v[132:135], a[64:67], v[192:195], 0\n"
    "ds_read_b128 v[220:223], v122 offset:19456\n"
    "v_mfma_f32_16x16x32_bf16 v[136:139], a[128:131], v[192:195], 0\n"
    "v_mfma_f32_16x16x32_bf16 v[140:143], a[192:195], v[192:195], 0\n"
    "v_cvt_pk_bf16_f32 v224, v160, v161\n"
    "v_cvt_pk_bf16_f32 v225, v162, v163\n"
    "v_mfma_f32_16x16x32_bf16 v[144:147], a[0:3], v[196:199], 0\n"
    "global_load_lds_dwordx4 v121, s[64:65] offset:3072 sc1\n"
    "v_mfma_f32_16x16x32_bf16 v[148:151], a[64:67], v[196:199], 0\n"
    "v_cvt_pk_bf16_f32 v226, v164, v165\n"
    "v_cvt_pk_bf16_f32 v227, v166, v167\n"
    "v_mfma_f32_16x16x32_bf16 v[152:155], a[128:131], v[196:199], 0\n"
    "v_cvt_pk_bf16_f32 v228, v176, v177\n"
    "v_cvt_pk_bf16_f32 v229, v178, v179\n"
    "v_mfma_f32_16x16x32_bf16 v[156:159], a[192:195], v[196:199], 0\n"
    "v_cvt_pk_bf16_f32 v230, v180, v181\n"
    "v_cvt_pk_bf16_f32 v231, v182, v183\n"
    "s_waitcnt lgkmcnt(4)\n"
    "v_mfma_f32_16x16x32_bf16 v[128:131], a[4:7], v[200:203], v[128:131]\n"
    "ds_read_b128 v[192:195], v122 offset:4096\n"
    "v_mfma_f32_16x16x32_bf16 v[132:135], a[68:71], v[200:203], v[132:135]\n"
    "ds_read_b128 v[196:199], v122 offset:20480\n"
    "v_mfma_f32_16x16x32_bf16 v[136:139], a[132:135], v[200:203], v[136:139]\n"
    "s_add_u32 m0, s50, 77824\n"
    "s_add_u32 s64, s48, 12288\n"
    "s_addc_u32 s65, s49, 0\n"
    "v_cvt_pk_bf16_f32 v232, v168, v169\n"
    "v_cvt_pk_bf16_f32 v233, v170, v171\n"
    "v_mfma_f32_16x16x32_bf16 v[140:143], a[196:199], v[200:203], v[140:143]\n"
    "s_add_u32 s48, s48, 0x10000\n"
    "s_addc_u32 s49, s49, 0\n"
    "v_cvt_pk_bf16_f32 v234, v172, v173\n"
    "v_cvt_pk_bf16_f32 v235, v174, v175\n"
    "v_mfma_f32_16x16x32_bf16 v[144:147], a[4:7], v[204:207], v[144:147]\n"
    "global_load_lds_dwordx4 v121, s[64:65] offset:0 sc1\n"
    "v_mfma_f32_16x16x32_bf16 v[148:151], a[68:71], v[204:207], v[148:151]\n"
    "v_cvt_pk_bf16_f32 v236, v184, v185\n"
    "v_cvt_pk_bf16_f32 v237, v186, v187\n"
    "v_mfma_f32_16x16x32_bf16 v[152:155], a[132:135], v[204:207], v[152:155]\n"
    "v_mfma_f32_16x16x32_bf16 v[156:159], a[196:199], v[204:207], v[156:159]\n"
    "v_cvt_pk_bf16_f32 v238, v188, v189\n"
    "v_cvt_pk_bf16_f32 v239, v190, v191\n"
    "s_waitcnt lgkmcnt(4)\n"
    "v_mfma_f32_16x16x32_bf16 v[128:131], a[8:11], v[208:211], v[128:131]\n"
    "ds_read_b128 v[200:203], v122 offset:5120\n"
    "v_mfma_f32_16x16x32_bf16 v[132:135], a[72:75], v[208:211], v[132:135]\n"
    "ds_read_b128 v[204:207], v122 offset:21504\n"
    "v_mfma_f32_16x16x32_bf16 v[136:139], a[136:139], v[208:211], v[136:139]\n"
    "s_not_b64 vcc, vcc\n"
    "v_cndmask_b32_dpp v240, v232, v224, vcc quad_perm:[0,1,0,1] row_mask:0xf bank_mask:0xf\n"
    "v_cndmask_b32_dpp v241, v233, v225, vcc quad_perm:[0,1,0,1] row_mask:0xf bank_mask:0xf\n"
    "v_mfma_f32_16x16x32_bf16 v[140:143], a[200:203], v[208:211], v[140:143]\n"
    "v_cndmask_b32_dpp v242, v234, v226, vcc quad_perm:[0,1,0,1] row_mask:0xf bank_mask:0xf\n"
    "v_cndmask_b32_dpp v243, v235, v227, vcc quad_perm:[0,1,0,1] row_mask:0xf bank_mask:0xf\n"
    "v_mfma_f32_16x16x32_bf16 v[144:147], a[8:11], v[212:215], v[144:147]\n"
    "global_load_lds_dwordx4 v121, s[64:65] offset:1024 sc1\n"
    "v_mfma_f32_16x16x32_bf16 v[148:151], a[72:75], v[212:215], v[148:151]\n"
    "v_cndmask_b32_dpp v248, v236, v228, vcc quad_perm:[0,1,0,1] row_mask:0xf bank_mask:0xf\n"
    "v_cndmask_b32_dpp v249, v237, v229, vcc quad_perm:[0,1,0,1] row_mask:0xf bank_mask:0xf\n"
    "v_mfma_f32_16x16x32_bf16 v[152:155], a[136:139], v[212:215], v[152:155]\n"
    "global_store_dwordx4 v121, v[240:243], s[44:45] offset:0 sc1\n"
    "v_mfma_f32_16x16x32_bf16 v[156:159], a[200:203], v[212:215], v[156:159]\n"
    "v_cndmask_b32_dpp v250, v238, v230, vcc quad_perm:[0,1,0,1] row_mask:0xf bank_mask:0xf\n"
    "v_cndmask_b32_dpp v251, v239, v231, vcc quad_perm:[0,1,0,1] row_mask:0xf bank_mask:0xf\n"
    "s_waitcnt lgkmcnt(4)\n"
    "v_mfma_f32_16x16x32_bf16 v[128:131], a[12:15], v[216:219], v[128:131]\n"
    "ds_read_b128 v[208:211], v122 offset:6144\n"
    "v_mfma_f32_16x16x32_bf16 v[132:135], a[76:79], v[216:219], v[132:135]\n"
    "ds_read_b128 v[212:215], v122 offset:22528\n"
    "v_mfma_f32_16x16x32_bf16 v[136:139], a[140:143], v[216:219], v[136:139]\n"
    "s_not_b64 vcc, vcc\n"
    "v_cndmask_b32_dpp v244, v224, v232, vcc quad_perm:[2,3,2,3] row_mask:0xf bank_mask:0xf\n"
    "v_cndmask_b32_dpp v245, v225, v233, vcc quad_perm:[2,3,2,3] row_mask:0xf bank_mask:0xf\n"
    "v_mfma_f32_16x16x32_bf16 v[140:143], a[204:207], v[216:219], v[140:143]\n"
    "v_cndmask_b32_dpp v246, v226, v234, vcc quad_perm:[2,3,2,3] row_mask:0xf bank_mask:0xf\n"
    "v_cndmask_b32_dpp v247, v227, v235, vcc quad_perm:[2,3,2,3] row_mask:0xf bank_mask:0xf\n"
    "v_mfma_f32_16x16x32_bf16 v[144:147], a[12:15], v[220:223], v[144:147]\n"
    "global_load_lds_dwordx4 v121, s[64:65] offset:2048 sc1\n"
    "v_mfma_f32_16x16x32_bf16 v[148:151], a[76:79], v[220:223], v[148:151]\n"
    "v_cndmask_b32_dpp v252, v228, v236, vcc quad_perm:[2,3,2,3] row_mask:0xf bank_mask:0xf\n"
    "v_cndmask_b32_dpp v253, v229, v237, vcc quad_perm:[2,3,2,3] row_mask:0xf bank_mask:0xf\n"
    "v_mfma_f32_16x16x32_bf16 v[152:155], a[140:143], v[220:223], v[152:155]\n"
    "global_store_dwordx4 v121, v[248:251], s[44:45] offset:2048 sc1\n"
    "v_mfma_f32_16x16x32_bf16 v[156:159], a[204:207], v[220:223], v[156:159]\n"
    "v_cndmask_b32_dpp v254, v230, v238, vcc quad_perm:[2,3,2,3] row_mask:0xf bank_mask:0xf\n"
    "v_cndmask_b32_dpp v255, v231, v239, vcc quad_perm:[2,3,2,3] row_mask:0xf bank_mask:0xf\n"
    "s_waitcnt lgkmcnt(4)\n"
    "v_mfma_f32_16x16x32_bf16 v[128:131], a[16:19], v[192:195], v[128:131]\n"
    "ds_read_b128 v[216:219], v122 offset:7168\n"
    "v_mfma_f32_16x16x32_bf16 v[132:135], a[80:83], v[192:195], v[132:135]\n"
    "ds_read_b128 v[220:223], v122 offset:23552\n"
    "v_mfma_f32_16x16x32_bf16 v[136:139], a[144:147], v[192:195], v[136:139]\n"
    "global_store_dwordx4 v121, v[244:247], s[44:45] offset:1024 sc1\n"
    "v_mfma_f32_16x16x32_bf16 v[140:143], a[208:211], v[192:195], v[140:143]\n"
    "v_mfma_f32_16x16x32_bf16 v[144:147], a[16:19], v[196:199], v[144:147]\n"
    "global_load_lds_dwordx4 v121, s[64:65] offset:3072 sc1\n"
    "v_mfma_f32_16x16x32_bf16 v[148:151], a[80:83], v[196:199], v[148:151]\n"
    "v_mfma_f32_16x16x32_bf16 v[152:155], a[144:147], v[196:199], v[152:155]\n"
    "global_store_dwordx4 v121, v[252:255], s[44:45] offset:3072 sc1\n"
    "v_mfma_f32_16x16x32_bf16 v[156:159], a[208:211], v[196:199], v[156:159]\n"
    "s_waitcnt lgkmcnt(4)\n"
    "v_mfma_f32_16x16x32_bf16 v[128:131], a[20:23], v[200:203], v[128:131]\n"
    "s_waitcnt vmcnt(12) lgkmcnt(0)\n"
    "s_barrier\n"
    "v_mfma_f32_16x16x32_bf16 v[132:135], a[84:87], v[200:203], v[132:135]\n"
    "ds_read_b128 v[192:195], v122 offset:8192\n"
    "v_mfma_f32_16x16x32_bf16 v[136:139], a[148:151], v[200:203], v[136:139]\n"
    "ds_read_b128 v[196:199], v122 offset:24576\n"
    "s_add_u32 m0, s50, 0\n"
    "s_add_u32 s64, s48, 0\n"
    "s_addc_u32 s65, s49, 0\n"
    "v_mfma_f32_16x16x32_bf16 v[140:143], a[212:215], v[200:203], v[140:143]\n"
    "v_mfma_f32_16x16x32_bf16 v[144:147], a[20:23], v[204:207], v[144:147]\n"
    "global_load_lds_dwordx4 v121, s[64:65] offset:0 sc1\n"
    "v_mfma_f32_16x16x32_bf16 v[148:151], a[84:87], v[204:207], v[148:151]\n"
    "v_mfma_f32_16x16x32_bf16 v[152:155], a[148:151], v[204:207], v[152:155]\n"
    "v_mfma_f32_16x16x32_bf16 v[156:159], a[212:215], v[204:207], v[156:159]\n"
    "s_waitcnt lgkmcnt(4)\n"
    "v_mfma_f32_16x16x32_bf16 v[128:131], a[24:27], v[208:211], v[128:131]\n"
    "ds_read_b128 v[200:203], v122 offset:9216\n"
    "v_mfma_f32_16x16x32_bf16 v[132:135], a[88:91], v[208:211], v[132:135]\n"
    "ds_read_b128 v[204:207], v122 offset:25600\n"
    "v_mfma_f32_16x16x32_bf16 v[136:139], a[152:155], v[208:211], v[136:139]\n"
    "v_mfma_f32_16x16x32_bf16 v[140:143], a[216:219], v[208:211], v[140:143]\n"
    "v_mfma_f32_16x16x32_bf16 v[144:147], a[24:27], v[212:215], v[144:147]\n"
    "global_load_lds_dwordx4 v121, s[64:65] offset:1024 sc1\n"
    "v_mfma_f32_16x16x32_bf16 v[148:151], a[88:91], v[212:215], v[148:151]\n"
    "v_mfma_f32_16x16x32_bf16 v[152:155], a[152:155], v[212:215], v[152:155]\n"
    "v_mfma_f32_16x16x32_bf16 v[156:159], a[216:219], v[212:215], v[156:159]\n"
    "s_waitcnt lgkmcnt(4)\n"
    "v_mfma_f32_16x16x32_bf16 v[128:131], a[28:31], v[216:219], v[128:131]\n"
    "ds_read_b128 v[208:211], v122 offset:10240\n"
    "v_mfma_f32_16x16x32_bf16 v[132:135], a[92:95], v[216:219], v[132:135]\n"
    "ds_read_b128 v[212:215], v122 offset:26624\n"
    "v_mfma_f32_16x16x32_bf16 v[136:139], a[156:159], v[216:219], v[136:139]\n"
    "v_mfma_f32_16x16x32_bf16 v[140:143], a[220:223], v[216:219], v[140:143]\n"
    "v_mfma_f32_16x16x32_bf16 v[144:147], a[28:31], v[220:223], v[144:147]\n"
    "global_load_lds_dwordx4 v121, s[64:65] offset:2048 sc1\n"
    "v_mfma_f32_16x16x32_bf16 v[148:151], a[92:95], v[220:223], v[148:151]\n"
    "s_mov_b64 s[44:45], s[66:67]\n"
    "s_add_u32 s66, s66, 0x100000\n"
    "s_addc_u32 s67, s67, 0\n"
    "v_mfma_f32_16x16x32_bf16 v[152:155], a[156:159], v[220:223], v[152:155]\n"
    "v_mfma_f32_16x16x32_bf16 v[156:159], a[220:223], v[220:223], v[156:159]\n"
    "s_waitcnt lgkmcnt(4)\n"
    "v_mfma_f32_16x16x32_bf16 v[128:131], a[32:35], v[192:195], v[128:131]\n"
    "ds_read_b128 v[216:219], v122 offset:11264\n"
    "v_mfma_f32_16x16x32_bf16 v[132:135], a[96:99], v[192:195], v[132:135]\n"
    "ds_read_b128 v[220:223], v122 offset:27648\n"
    "v_mfma_f32_16x16x32_bf16 v[136:139], a[160:163], v[192:195], v[136:139]\n"
    "v_mfma_f32_16x16x32_bf16 v[140:143], a[224:227], v[192:195], v[140:143]\n"
    "v_mfma_f32_16x16x32_bf16 v[144:147], a[32:35], v[196:199], v[144:147]\n"
    "global_load_lds_dwordx4 v121, s[64:65] offset:3072 sc1\n"
    "v_mfma_f32_16x16x32_bf16 v[148:151], a[96:99], v[196:199], v[148:151]\n"
    "v_mfma_f32_16x16x32_bf16 v[152:155], a[160:163], v[196:199], v[152:155]\n"
    "v_mfma_f32_16x16x32_bf16 v[156:159], a[224:227], v[196:199], v[156:159]\n"
    "s_waitcnt lgkmcnt(4)\n"
    "v_mfma_f32_16x16x32_bf16 v[128:131], a[36:39], v[200:203], v[128:131]\n"
    "ds_read_b128 v[192:195], v122 offset:12288\n"
    "v_mfma_f32_16x16x32_bf16 v[132:135], a[100:103], v[200:203], v[132:135]\n"
    "ds_read_b128 v[196:199], v122 offset:28672\n"
    "v_mfma_f32_16x16x32_bf16 v[136:139], a[164:167], v[200:203], v[136:139]\n"
    "s_add_u32 m0, s50, 4096\n"
    "s_add_u32 s64, s48, 4096\n"
    "s_addc_u32 s65, s49, 0\n"
    "v_mfma_f32_16x16x32_bf16 v[140:143], a[228:231], v[200:203], v[140:143]\n"
    "v_mfma_f32_16x16x32_bf16 v[144:147], a[36:39], v[204:207], v[144:147]\n"
    "global_load_lds_dwordx4 v121, s[64:65] offset:0 sc1\n"
    "v_mfma_f32_16x16x32_bf16 v[148:151], a[100:103], v[204:207], v[148:151]\n"
    "v_mfma_f32_16x16x32_bf16 v[152:155], a[164:167], v[204:207], v[152:155]\n"
    "v_mfma_f32_16x16x32_bf16 v[156:159], a[228:231], v[204:207], v[156:159]\n"
    "s_waitcnt lgkmcnt(4)\n"
    "v_mfma_f32_16x16x32_bf16 v[128:131], a[40:43], v[208:211], v[128:131]\n"
    "ds_read_b128 v[200:203], v122 offset:13312\n"
    "v_mfma_f32_16x16x32_bf16 v[132:135], a[104:107], v[208:211], v[132:135]\n"
    "ds_read_b128 v[204:207], v122 offset:29696\n"
    "v_mfma_f32_16x16x32_bf16 v[136:139], a[168:171], v[208:211], v[136:139]\n"
    "v_mfma_f32_16x16x32_bf16 v[140:143], a[232:235], v[208:211], v[140:143]\n"
    "v_mfma_f32_16x16x32_bf16 v[144:147], a[40:43], v[212:215], v[144:147]\n"
    "global_load_lds_dwordx4 v121, s[64:65] offset:1024 sc1\n"
    "v_mfma_f32_16x16x32_bf16 v[148:151], a[104:107], v[212:215], v[148:151]\n"
    "v_mfma_f32_16x16x32_bf16 v[152:155], a[168:171], v[212:215], v[152:155]\n"
    "v_mfma_f32_16x16x32_bf16 v[156:159], a[232:235], v[212:215], v[156:159]\n"
    "s_waitcnt lgkmcnt(4)\n"
    "v_mfma_f32_16x16x32_bf16 v[128:131], a[44:47], v[216:219], v[128:131]\n"
    "ds_read_b128 v[208:211], v122 offset:14336\n"
    "v_mfma_f32_16x16x32_bf16 v[132:135], a[108:111], v[216:219], v[132:135]\n"
    "ds_read_b128 v[212:215], v122 offset:30720\n"
    "v_mfma_f32_16x16x32_bf16 v[136:139], a[172:175], v[216:219], v[136:139]\n"
    "v_mfma_f32_16x16x32_bf16 v[140:143], a[236:239], v[216:219], v[140:143]\n"
    "v_mfma_f32_16x16x32_bf16 v[144:147], a[44:47], v[220:223], v[144:147]\n"
    "global_load_lds_dwordx4 v121, s[64:65] offset:2048 sc1\n"
    "v_mfma_f32_16x16x32_bf16 v[148:151], a[108:111], v[220:223], v[148:151]\n"
    "v_mfma_f32_16x16x32_bf16 v[152:155], a[172:175], v[220:223], v[152:155]\n"
    "v_mfma_f32_16x16x32_bf16 v[156:159], a[236:239], v[220:223], v[156:159]\n"
    "s_waitcnt lgkmcnt(4)\n"
    "v_mfma_f32_16x16x32_bf16 v[128:131], a[48:51], v[192:195], v[128:131]\n"
    "ds_read_b128 v[216:219], v122 offset:15360\n"
    "v_mfma_f32_16x16x32_bf16 v[132:135], a[112:115], v[192:195], v[132:135]\n"
    "ds_read_b128 v[220:223], v122 offset:31744\n"
    "v_mfma_f32_16x16x32_bf16 v[136:139], a[176:179], v[192:195], v[136:139]\n"
    "v_mfma_f32_16x16x32_bf16 v[140:143], a[240:243], v[192:195], v[140:143]\n"
    "v_mfma_f32_16x16x32_bf16 v[144:147], a[48:51], v[196:199], v[144:147]\n"
    "global_load_lds_dwordx4 v121, s[64:65] offset:3072 sc1\n"
    "v_mfma_f32_16x16x32_bf16 v[148:151], a[112:115], v[196:199], v[148:151]\n"
    "v_mfma_f32_16x16x32_bf16 v[152:155], a[176:179], v[196:199], v[152:155]\n"
    "v_mfma_f32_16x16x32_bf16 v[156:159], a[240:243], v[196:199], v[156:159]\n"
    "s_waitcnt lgkmcnt(4)\n"
    "v_mfma_f32_16x16x32_bf16 v[128:131], a[52:55], v[200:203], v[128:131]\n"
    "s_waitcnt vmcnt(8) lgkmcnt(0)\n"
    "s_barrier\n"
    "v_mfma_f32_16x16x32_bf16 v[132:135], a[116:119], v[200:203], v[132:135]\n"
    "ds_read_b128 v[192:195], v123 offset:0\n"
    "v_mfma_f32_16x16x32_bf16 v[136:139], a[180:183], v[200:203], v[136:139]\n"
    "ds_read_b128 v[196:199], v123 offset:16384\n"
    "s_add_u32 m0, s50, 8192\n"
    "s_add_u32 s64, s48, 8192\n"
    "s_addc_u32 s65, s49, 0\n"
    "v_mfma_f32_16x16x32_bf16 v[140:143], a[244:247], v[200:203], v[140:143]\n"
    "v_mfma_f32_16x16x32_bf16 v[144:147], a[52:55], v[204:207], v[144:147]\n"
    "global_load_lds_dwordx4 v121, s[64:65] offset:0 sc1\n"
    "v_mfma_f32_16x16x32_bf16 v[148:151], a[116:119], v[204:207], v[148:151]\n"
    "s_cmp_lg_u32 s56, 0\n"
    "s_cbranch_scc1 G_f2%=\n"
    "s_or_b32 s58, s42, s51\n"
    "s_mov_b64 exec, 1\n"
    "v_mov_b32 v116, s58\n"
    "global_store_dwordx2 v118, v[116:117], s[54:55] sc1\n"
    "s_mov_b64 exec, -1\n"
    "G_f2%=:\n"
    "v_mfma_f32_16x16x32_bf16 v[152:155], a[180:183], v[204:207], v[152:155]\n"
    "v_mfma_f32_16x16x32_bf16 v[156:159], a[244:247], v[204:207], v[156:159]\n"
    "s_waitcnt lgkmcnt(4)\n"
    "v_mfma_f32_16x16x32_bf16 v[128:131], a[56:59], v[208:211], v[128:131]\n"
    "ds_read_b128 v[200:203], v123 offset:1024\n"
    "v_mfma_f32_16x16x32_bf16 v[132:135], a[120:123], v[208:211], v[132:135]\n"
    "ds_read_b128 v[204:207], v123 offset:17408\n"
    "v_mfma_f32_16x16x32_bf16 v[136:139], a[184:187], v[208:211], v[136:139]\n"
    "v_mfma_f32_16x16x32_bf16 v[140:143], a[248:251], v[208:211], v[140:143]\n"
    "v_mfma_f32_16x16x32_bf16 v[144:147], a[56:59], v[212:215], v[144:147]\n"
    "global_load_lds_dwordx4 v121, s[64:65] offset:1024 sc1\n"
    "v_mfma_f32_16x16x32_bf16 v[148:151], a[120:123], v[212:215], v[148:151]\n"
    "v_mfma_f32_16x16x32_bf16 v[152:155], a[184:187], v[212:215], v[152:155]\n"
    "v_mfma_f32_16x16x32_bf16 v[156:159], a[248:251], v[212:215], v[156:159]\n"
    "s_add_u32 s51, s51, 1\n"
    "s_waitcnt lgkmcnt(4)\n"
    "v_mfma_f32_16x16x32_bf16 v[128:131], a[60:63], v[216:219], v[128:131]\n"
    "ds_read_b128 v[208:211], v123 offset:2048\n"
    "v_mfma_f32_16x16x32_bf16 v[132:135], a[124:127], v[216:219], v[132:135]\n"
    "ds_read_b128 v[212:215], v123 offset:18432\n"
    "v_mfma_f32_16x16x32_bf16 v[136:139], a[188:191], v[216:219], v[136:139]\n"
    "v_mfma_f32_16x16x32_bf16 v[140:143], a[252:255], v[216:219], v[140:143]\n"
    "v_mfma_f32_16x16x32_bf16 v[144:147], a[60:63], v[220:223], v[144:147]\n"
    "global_load_lds_dwordx4 v121, s[64:65] offset:2048 sc1\n"
    "v_mfma_f32_16x16x32_bf16 v[148:151], a[124:127], v[220:223], v[148:151]\n"
    "v_mfma_f32_16x16x32_bf16 v[152:155], a[188:191], v[220:223], v[152:155]\n"
    "v_mfma_f32_16x16x32_bf16 v[156:159], a[252:255], v[220:223], v[156:159]\n"
    "s_add_u32 s58, s51, -3\n"
    "s_cmp_gt_u32 s58, 28\n"
    "s_waitcnt lgkmcnt(4)\n"
    "s_cbranch_scc0 G_loop%=\n"
    "s_branch G_pc%=\n"
    "G_pw%=:\n"
    "s_add_u32 s58, s40, 0x312000\n"
    "s_addc_u32 s59, s41, 0\n"
    "global_load_dwordx2 v[114:115], v112, s[58:59] sc1\n"
    "s_waitcnt vmcnt(0)\n"
    "G_pc%=:\n"
    "v_xor_b32 v114, s42, v114\n"
    "v_xor_b32 v115, s43, v115\n"
    "v_add_u32 v114, -1, v114\n"
    "v_cmp_le_u64 s[58:59], 16, v[114:115]\n"
    "v_and_b32 v114, 2, v114\n"
    "v_cmp_ne_u32 s[52:53], 0, v114\n"
    "s_cmp_eq_u64 s[58:59], 0\n"
    "s_cbranch_scc1 G_pok%=\n"
    "s_sleep 4\n"
    "s_add_u32 s57, s57, 1\n"
    "s_cmp_lt_u32 s57, 0x40000\n"
    "s_cbranch_scc1 G_pw%=\n"
    "G_pok%=:\n"
    "s_and_b32 s52, s52, 0x7ff\n"
    "s_bcnt1_i32_b32 s52, s52\n"
    "s_cmp_ge_u32 s52, 6\n"
    "s_cselect_b32 s52, 0, 0x2000\n"
    "s_xor_b32 s44, s44, s52\n"
    "s_xor_b32 s66, s66, s52\n"
    "s_nop 7\n"
    "v_cvt_pk_bf16_f32 v224, v128, v129\n"
    "v_cvt_pk_bf16_f32 v225, v130, v131\n"
    "v_cvt_pk_bf16_f32 v226, v132, v133\n"
    "v_cvt_pk_bf16_f32 v227, v134, v135\n"
    "v_cvt_pk_bf16_f32 v228, v144, v145\n"
    "v_cvt_pk_bf16_f32 v229, v146, v147\n"
    "v_cvt_pk_bf16_f32 v230, v148, v149\n"
    "v_cvt_pk_bf16_f32 v231, v150, v151\n"
    "v_cvt_pk_bf16_f32 v232, v136, v137\n"
    "v_cvt_pk_bf16_f32 v233, v138, v139\n"
    "v_cvt_pk_bf16_f32 v234, v140, v141\n"
    "v_cvt_pk_bf16_f32 v235, v142, v143\n"
    "v_cvt_pk_bf16_f32 v236, v152, v153\n"
    "v_cvt_pk_bf16_f32 v237, v154, v155\n"
    "v_cvt_pk_bf16_f32 v238, v156, v157\n"
    "v_cvt_pk_bf16_f32 v239, v158, v159\n"
    "s_not_b64 vcc, vcc\n"
    "v_cndmask_b32_dpp v240, v232, v224, vcc quad_perm:[0,1,0,1] row_mask:0xf bank_mask:0xf\n"
    "v_cndmask_b32_dpp v241, v233, v225, vcc quad_perm:[0,1,0,1] row_mask:0xf bank_mask:0xf\n"
    "v_cndmask_b32_dpp v242, v234, v226, vcc quad_perm:[0,1,0,1] row_mask:0xf bank_mask:0xf\n"
    "v_cndmask_b32_dpp v243, v235, v227, vcc quad_perm:[0,1,0,1] row_mask:0xf bank_mask:0xf\n"
    "v_cndmask_b32_dpp v248, v236, v228, vcc quad_perm:[0,1,0,1] row_mask:0xf bank_mask:0xf\n"
    "v_cndmask_b32_dpp v249, v237, v229, vcc quad_perm:[0,1,0,1] row_mask:0xf bank_mask:0xf\n"
    "v_cndmask_b32_dpp v250, v238, v230, vcc quad_perm:[0,1,0,1] row_mask:0xf bank_mask:0xf\n"
    "v_cndmask_b32_dpp v251, v239, v231, vcc quad_perm:[0,1,0,1] row_mask:0xf bank_mask:0xf\n"
    "s_not_b64 vcc, vcc\n"
    "v_cndmask_b32_dpp v244, v224, v232, vcc quad_perm:[2,3,2,3] row_mask:0xf bank_mask:0xf\n"
    "v_cndmask_b32_dpp v245, v225, v233, vcc quad_perm:[2,3,2,3] row_mask:0xf bank_mask:0xf\n"
    "v_cndmask_b32_dpp v246, v226, v234, vcc quad_perm:[2,3,2,3] row_mask:0xf bank_mask:0xf\n"
    "v_cndmask_b32_dpp v247, v227, v235, vcc quad_perm:[2,3,2,3] row_mask:0xf bank_mask:0xf\n"
    "v_cndmask_b32_dpp v252, v228, v236, vcc quad_perm:[2,3,2,3] row_mask:0xf bank_mask:0xf\n"
    "v_cndmask_b32_dpp v253, v229, v237, vcc quad_perm:[2,3,2,3] row_mask:0xf bank_mask:0xf\n"
    "v_cndmask_b32_dpp v254, v230, v238, vcc quad_perm:[2,3,2,3] row_mask:0xf bank_mask:0xf\n"
    "v_cndmask_b32_dpp v255, v231, v239, vcc quad_perm:[2,3,2,3] row_mask:0xf bank_mask:0xf\n"
    "global_store_dwordx4 v121, v[240:243], s[44:45] offset:0 sc1\n"
    "global_store_dwordx4 v121, v[248:251], s[44:45] offset:2048 sc1\n"
    "global_store_dwordx4 v121, v[244:247], s[44:45] offset:1024 sc1\n"
    "global_store_dwordx4 v121, v[252:255], s[44:45] offset:3072 sc1\n"
    "s_sub_u32 s44, s44, 0x300000\n"
    "s_subb_u32 s45, s45, 0\n"
    "s_waitcnt vmcnt(0)\n"
    "s_barrier\n"
    "s_cmp_lg_u32 s56, 0\n"
    "s_cbranch_scc1 G_f3%=\n"
    "s_or_b32 s58, s42, s51\n"
    "s_mov_b64 exec, 1\n"
    "v_mov_b32 v116, s58\n"
    "global_store_dwordx2 v118, v[116:117], s[54:55] sc1\n"
    "s_mov_b64 exec, -1\n"
    "G_f3%=:\n"
    "s_branch G_loop%=\n"
    "G_t1%=:\n"
    "s_nop 7\n"
    "v_cvt_pk_bf16_f32 v224, v160, v161\n"
    "v_cvt_pk_bf16_f32 v225, v162, v163\n"
    "v_cvt_pk_bf16_f32 v226, v164, v165\n"
    "v_cvt_pk_bf16_f32 v227, v166, v167\n"
    "v_cvt_pk_bf16_f32 v228, v176, v177\n"
    "v_cvt_pk_bf16_f32 v229, v178, v179\n"
    "v_cvt_pk_bf16_f32 v230, v180, v181\n"
    "v_cvt_pk_bf16_f32 v231, v182, v183\n"
    "v_cvt_pk_bf16_f32 v232, v168, v169\n"
    "v_cvt_pk_bf16_f32 v233, v170, v171\n"
    "v_cvt_pk_bf16_f32 v234, v172, v173\n"
    "v_cvt_pk_bf16_f32 v235, v174, v175\n"
    "v_cvt_pk_bf16_f32 v236, v184, v185\n"
    "v_cvt_pk_bf16_f32 v237, v186, v187\n"
    "v_cvt_pk_bf16_f32 v238, v188, v189\n"
    "v_cvt_pk_bf16_f32 v239, v190, v191\n"
    "s_not_b64 vcc, vcc\n"
    "v_cndmask_b32_dpp v240, v232, v224, vcc quad_perm:[0,1,0,1] row_mask:0xf bank_mask:0xf\n"
    "v_cndmask_b32_dpp v241, v233, v225, vcc quad_perm:[0,1,0,1] row_mask:0xf bank_mask:0xf\n"
    "v_cndmask_b32_dpp v242, v234, v226, vcc quad_perm:[0,1,0,1] row_mask:0xf bank_mask:0xf\n"
    "v_cndmask_b32_dpp v243, v235, v227, vcc quad_perm:[0,1,0,1] row_mask:0xf bank_mask:0xf\n"
    "v_cndmask_b32_dpp v248, v236, v228, vcc quad_perm:[0,1,0,1] row_mask:0xf bank_mask:0xf\n"
    "v_cndmask_b32_dpp v249, v237, v229, vcc quad_perm:[0,1,0,1] row_mask:0xf bank_mask:0xf\n"
    "v_cndmask_b32_dpp v250, v238, v230, vcc quad_perm:[0,1,0,1] row_mask:0xf bank_mask:0xf\n"
    "v_cndmask_b32_dpp v251, v239, v231, vcc quad_perm:[0,1,0,1] row_mask:0xf bank_mask:0xf\n"
    "s_not_b64 vcc, vcc\n"
    "v_cndmask_b32_dpp v244, v224, v232, vcc quad_perm:[2,3,2,3] row_mask:0xf bank_mask:0xf\n"
    "v_cndmask_b32_dpp v245, v225, v233, vcc quad_perm:[2,3,2,3] row_mask:0xf bank_mask:0xf\n"
    "v_cndmask_b32_dpp v246, v226, v234, vcc quad_perm:[2,3,2,3] row_mask:0xf bank_mask:0xf\n"
    "v_cndmask_b32_dpp v247, v227, v235, vcc quad_perm:[2,3,2,3] row_mask:0xf bank_mask:0xf\n"
    "v_cndmask_b32_dpp v252, v228, v236, vcc quad_perm:[2,3,2,3] row_mask:0xf bank_mask:0xf\n"
    "v_cndmask_b32_dpp v253, v229, v237, vcc quad_perm:[2,3,2,3] row_mask:0xf bank_mask:0xf\n"
    "v_cndmask_b32_dpp v254, v230, v238, vcc quad_perm:[2,3,2,3] row_mask:0xf bank_mask:0xf\n"
    "v_cndmask_b32_dpp v255, v231, v239, vcc quad_perm:[2,3,2,3] row_mask:0xf bank_mask:0xf\n"
    "global_store_dwordx4 v121, v[240:243], s[44:45] offset:0 sc1\n"
    "global_store_dwordx4 v121, v[248:251], s[44:45] offset:2048 sc1\n"
    "global_store_dwordx4 v121, v[244:247], s[44:45] offset:1024 sc1\n"
    "global_store_dwordx4 v121, v[252:255], s[44:45] offset:3072 sc1\n"
    "s_sub_u32 s44, s44, 0x300000\n"
    "s_subb_u32 s45, s45, 0\n"
    "s_waitcnt vmcnt(0)\n"
    "s_barrier\n"
    "s_cmp_lg_u32 s56, 0\n"
    "s_cbranch_scc1 G_f4%=\n"
    "s_or_b32 s58, s42, s51\n"
    "s_mov_b64 exec, 1\n"
    "v_mov_b32 v116, s58\n"
    "global_store_dwordx2 v118, v[116:117], s[54:55] sc1\n"
    "s_mov_b64 exec, -1\n"
    "G_f4%=:\n"
    "s_cmp_eq_u32 s51, 32\n"
    "s_cbranch_scc1 G_end%=\n"
    "s_add_u32 s58, s40, 0x20e000\n"
    "s_addc_u32 s59, s41, 0\n"
    "s_add_u32 s60, s40, 0x200200\n"
    "s_addc_u32 s61, s41, 0\n"
    "s_or_b32 s62, s42, 1\n"
    "s_mov_b32 s63, s43\n"
    "v_lshlrev_b32 v224, 3, v121\n"
    "G_xw1%=:\n"
    "global_load_dwordx2 v[226:227], v224, s[58:59] sc1\n"
    "G_xc1%=:\n"
    "s_waitcnt vmcnt(0)\n"
    "v_cmp_ne_u64 vcc, s[62:63], v[226:227]\n"
    "s_nop 0\n"
    "s_cbranch_vccz G_xok1%=\n"
    "s_sleep 4\n"
    "s_add_u32 s57, s57, 1\n"
    "s_and_b32 s68, s57, 0xff\n"
    "s_cbranch_scc1 G_xw1%=\n"
    "global_load_dwordx2 v[226:227], v118, s[60:61] sc1\n"
    "s_waitcnt vmcnt(0)\n"
    "v_cmp_eq_u64 vcc, s[42:43], v[226:227]\n"
    "s_nop 0\n"
    "s_cbranch_vccnz G_xok1%=\n"
    "s_cmp_lt_u32 s57, 0x40000\n"
    "s_cbranch_scc1 G_xw1%=\n"
    "v_mov_b32 v116, s42\n"
    "global_store_dwordx2 v118, v[116:117], s[60:61] sc1\n"
    "G_xok1%=:\n"
    "s_mov_b32 vcc_lo, 0xcccccccc\n"
    "s_mov_b32 vcc_hi, 0xcccccccc\n"
    "s_branch G_even%=\n"
    "G_end%=:\n"
    "s_waitcnt lgkmcnt(0)\n"
    :: GIN : GCLOB);
  else asm volatile(
    "s_mov_b64 s[40:41], %0\n"
    "s_mov_b64 s[42:43], %1\n"
    "s_mov_b64 s[44:45], %2\n"
    "s_mov_b64 s[54:55], %3\n"
    "s_mov_b32 s56, %4\n"
    "v_mov_b32 v121, %5\n"
    "s_add_u32 s58, s40, 0x20e000\n"
    "s_addc_u32 s59, s41, 0\n"
    "v_lshlrev_b32 v225, 3, v121\n"
    "v_and_b32 v224, 0x380, v225\n"
    "global_load_dwordx2 v[226:227], v224, s[58:59] sc1\n"
    "v_xor_b32 v112, 0, %6\n"
    "v_xor_b32 v113, 32, %6\n"
    "v_xor_b32 v114, 64, %6\n"
    "v_xor_b32 v115, 96, %6\n"
    "ds_read_b64_tr_b16 a[0:1], v112 offset:0\n"
    "ds_read_b64_tr_b16 a[2:3], v112 offset:1024\n"
    "ds_read_b64_tr_b16 a[4:5], v112 offset:8192\n"
    "ds_read_b64_tr_b16 a[6:7], v112 offset:9216\n"
    "ds_read_b64_tr_b16 a[8:9], v112 offset:16384\n"
    "ds_read_b64_tr_b16 a[10:11], v112 offset:17408\n"
    "ds_read_b64_tr_b16 a[12:13], v112 offset:24576\n"
    "ds_read_b64_tr_b16 a[14:15], v112 offset:25600\n"
    "s_waitcnt lgkmcnt(6)\n"
    "ds_read_b64_tr_b16 a[16:17], v112 offset:32768\n"
    "ds_read_b64_tr_b16 a[18:19], v112 offset:33792\n"
    "ds_read_b64_tr_b16 a[20:21], v112 offset:40960\n"
    "ds_read_b64_tr_b16 a[22:23], v112 offset:41984\n"
    "ds_read_b64_tr_b16 a[24:25], v112 offset:49152\n"
    "ds_read_b64_tr_b16 a[26:27], v112 offset:50176\n"
    "ds_read_b64_tr_b16 a[28:29], v112 offset:57344\n"
    "ds_read_b64_tr_b16 a[30:31], v112 offset:58368\n"
    "s_waitcnt lgkmcnt(6)\n"
    "ds_read_b64_tr_b16 a[64:65], v113 offset:0\n"
    "ds_read_b64_tr_b16 a[66:67], v113 offset:1024\n"
    "ds_read_b64_tr_b16 a[68:69], v113 offset:8192\n"
    "ds_read_b64_tr_b16 a[70:71], v113 offset:9216\n"
    "ds_read_b64_tr_b16 a[72:73], v113 offset:16384\n"
    "ds_read_b64_tr_b16 a[74:75], v113 offset:17408\n"
    "ds_read_b64_tr_b16 a[76:77], v113 offset:24576\n"
    "ds_read_b64_tr_b16 a[78:79], v113 offset:25600\n"
    "s_waitcnt lgkmcnt(6)\n"
    "ds_read_b64_tr_b16 a[80:81], v113 offset:32768\n"
    "ds_read_b64_tr_b16 a[82:83], v113 offset:33792\n"
    "ds_read_b64_tr_b16 a[84:85], v113 offset:40960\n"
    "ds_read_b64_tr_b16 a[86:87], v113 offset:41984\n"
    "ds_read_b64_tr_b16 a[88:89], v113 offset:49152\n"
    "ds_read_b64_tr_b16 a[90:91], v113 offset:50176\n"
    "ds_read_b64_tr_b16 a[92:93], v113 offset:57344\n"
    "ds_read_b64_tr_b16 a[94:95], v113 offset:58368\n"
    "s_waitcnt lgkmcnt(6)\n"
    "ds_read_b64_tr_b16 a[128:129], v114 offset:0\n"
    "ds_read_b64_tr_b16 a[130:131], v114 offset:1024\n"
    "ds_read_b64_tr_b16 a[132:133], v114 offset:8192\n"
    "ds_read_b64_tr_b16 a[134:135], v114 offset:9216\n"
    "ds_read_b64_tr_b16 a[136:137], v114 offset:16384\n"
    "ds_read_b64_tr_b16 a[138:139], v114 offset:17408\n"
    "ds_read_b64_tr_b16 a[140:141], v114 offset:24576\n"
    "ds_read_b64_tr_b16 a[142:143], v114 offset:25600\n"
    "s_waitcnt lgkmcnt(6)\n"
    "ds_read_b64_tr_b16 a[144:145], v114 offset:32768\n"
    "ds_read_b64_tr_b16 a[146:147], v114 offset:33792\n"
    "ds_read_b64_tr_b16 a[148:149], v114 offset:40960\n"
    "ds_read_b64_tr_b16 a[150:151], v114 offset:41984\n"
    "ds_read_b64_tr_b16 a[152:153], v114 offset:49152\n"
    "ds_read_b64_tr_b16 a[154:155], v114 offset:50176\n"
    "ds_read_b64_tr_b16 a[156:157], v114 offset:57344\n"
    "ds_read_b64_tr_b16 a[158:159], v114 offset:58368\n"
    "s_waitcnt lgkmcnt(6)\n"
    "ds_read_b64_tr_b16 a[192:193], v115 offset:0\n"
    "ds_read_b64_tr_b16 a[194:195], v115 offset:1024\n"
    "ds_read_b64_tr_b16 a[196:197], v115 offset:8192\n"
    "ds_read_b64_tr_b16 a[198:199], v115 offset:9216\n"
    "ds_read_b64_tr_b16 a[200:201], v115 offset:16384\n"
    "ds_read_b64_tr_b16 a[202:203], v115 offset:17408\n"
    "ds_read_b64_tr_b16 a[204:205], v115 offset:24576\n"
    "ds_read_b64_tr_b16 a[206:207], v115 offset:25600\n"
    "s_waitcnt lgkmcnt(6)\n"
    "ds_read_b64_tr_b16 a[208:209], v115 offset:32768\n"
    "ds_read_b64_tr_b16 a[210:211], v115 offset:33792\n"
    "ds_read_b64_tr_b16 a[212:213], v115 offset:40960\n"
    "ds_read_b64_tr_b16 a[214:215], v115 offset:41984\n"
    "ds_read_b64_tr_b16 a[216:217], v115 offset:49152\n"
    "ds_read_b64_tr_b16 a[218:219], v115 offset:50176\n"
    "ds_read_b64_tr_b16 a[220:221], v115 offset:57344\n"
    "ds_read_b64_tr_b16 a[222:223], v115 offset:58368\n"
    "s_waitcnt lgkmcnt(6)\n"
    "v_xor_b32 v112, 0, %6\n"
    "v_add_u32 v112, 0x10000, v112\n"
    "v_xor_b32 v113, 32, %6\n"
    "v_add_u32 v113, 0x10000, v113\n"
    "v_xor_b32 v114, 64, %6\n"
    "v_add_u32 v114, 0x10000, v114\n"
    "v_xor_b32 v115, 96, %6\n"
    "v_add_u32 v115, 0x10000, v115\n"
    "ds_read_b64_tr_b16 a[32:33], v112 offset:0\n"
    "ds_read_b64_tr_b16 a[34:35], v112 offset:1024\n"
    "ds_read_b64_tr_b16 a[36:37], v112 offset:8192\n"
    "ds_read_b64_tr_b16 a[38:39], v112 offset:9216\n"
    "ds_read_b64_tr_b16 a[40:41], v112 offset:16384\n"
    "ds_read_b64_tr_b16 a[42:43], v112 offset:17408\n"
    "ds_read_b64_tr_b16 a[44:45], v112 offset:24576\n"
    "ds_read_b64_tr_b16 a[46:47], v112 offset:25600\n"
    "s_waitcnt lgkmcnt(6)\n"
    "ds_read_b64_tr_b16 a[48:49], v112 offset:32768\n"
    "ds_read_b64_tr_b16 a[50:51], v112 offset:33792\n"
    "ds_read_b64_tr_b16 a[52:53], v112 offset:40960\n"
    "ds_read_b64_tr_b16 a[54:55], v112 offset:41984\n"
    "ds_read_b64_tr_b16 a[56:57], v112 offset:49152\n"
    "ds_read_b64_tr_b16 a[58:59], v112 offset:50176\n"
    "ds_read_b64_tr_b16 a[60:61], v112 offset:57344\n"
    "ds_read_b64_tr_b16 a[62:63], v112 offset:58368\n"
    "s_waitcnt lgkmcnt(6)\n"
    "ds_read_b64_tr_b16 a[96:97], v113 offset:0\n"
    "ds_read_b64_tr_b16 a[98:99], v113 offset:1024\n"
    "ds_read_b64_tr_b16 a[100:101], v113 offset:8192\n"
    "ds_read_b64_tr_b16 a[102:103], v113 offset:9216\n"
    "ds_read_b64_tr_b16 a[104:105], v113 offset:16384\n"
    "ds_read_b64_tr_b16 a[106:107], v113 offset:17408\n"
    "ds_read_b64_tr_b16 a[108:109], v113 offset:24576\n"
    "ds_read_b64_tr_b16 a[110:111], v113 offset:25600\n"
    "s_waitcnt lgkmcnt(6)\n"
    "ds_read_b64_tr_b16 a[112:113], v113 offset:32768\n"
    "ds_read_b64_tr_b16 a[114:115], v113 offset:33792\n"
    "ds_read_b64_tr_b16 a[116:117], v113 offset:40960\n"
    "ds_read_b64_tr_b16 a[118:119], v113 offset:41984\n"
    "ds_read_b64_tr_b16 a[120:121], v113 offset:49152\n"
    "ds_read_b64_tr_b16 a[122:123], v113 offset:50176\n"
    "ds_read_b64_tr_b16 a[124:125], v113 offset:57344\n"
    "ds_read_b64_tr_b16 a[126:127], v113 offset:58368\n"
    "s_waitcnt lgkmcnt(6)\n"
    "ds_read_b64_tr_b16 a[160:161], v114 offset:0\n"
    "ds_read_b64_tr_b16 a[162:163], v114 offset:1024\n"
    "ds_read_b64_tr_b16 a[164:165], v114 offset:8192\n"
    "ds_read_b64_tr_b16 a[166:167], v114 offset:9216\n"
    "ds_read_b64_tr_b16 a[168:169], v114 offset:16384\n"
    "ds_read_b64_tr_b16 a[170:171], v114 offset:17408\n"
    "ds_read_b64_tr_b16 a[172:173], v114 offset:24576\n"
    "ds_read_b64_tr_b16 a[174:175], v114 offset:25600\n"
    "s_waitcnt lgkmcnt(6)\n"
    "ds_read_b64_tr_b16 a[176:177], v114 offset:32768\n"
    "ds_read_b64_tr_b16 a[178:179], v114 offset:33792\n"
    "ds_read_b64_tr_b16 a[180:181], v114 offset:40960\n"
    "ds_read_b64_tr_b16 a[182:183], v114 offset:41984\n"
    "ds_read_b64_tr_b16 a[184:185], v114 offset:49152\n"
    "ds_read_b64_tr_b16 a[186:187], v114 offset:50176\n"
    "ds_read_b64_tr_b16 a[188:189], v114 offset:57344\n"
    "ds_read_b64_tr_b16 a[190:191], v114 offset:58368\n"
    "s_waitcnt lgkmcnt(6)\n"
    "ds_read_b64_tr_b16 a[224:225], v115 offset:0\n"
    "ds_read_b64_tr_b16 a[226:227], v115 offset:1024\n"
    "ds_read_b64_tr_b16 a[228:229], v115 offset:8192\n"
    "ds_read_b64_tr_b16 a[230:231], v115 offset:9216\n"
    "ds_read_b64_tr_b16 a[232:233], v115 offset:16384\n"
    "ds_read_b64_tr_b16 a[234:235], v115 offset:17408\n"
    "ds_read_b64_tr_b16 a[236:237], v115 offset:24576\n"
    "ds_read_b64_tr_b16 a[238:239], v115 offset:25600\n"
    "s_waitcnt lgkmcnt(6)\n"
    "ds_read_b64_tr_b16 a[240:241], v115 offset:32768\n"
    "ds_read_b64_tr_b16 a[242:243], v115 offset:33792\n"
    "ds_read_b64_tr_b16 a[244:245], v115 offset:40960\n"
    "ds_read_b64_tr_b16 a[246:247], v115 offset:41984\n"
    "ds_read_b64_tr_b16 a[248:249], v115 offset:49152\n"
    "ds_read_b64_tr_b16 a[250:251], v115 offset:50176\n"
    "ds_read_b64_tr_b16 a[252:253], v115 offset:57344\n"
    "ds_read_b64_tr_b16 a[254:255], v115 offset:58368\n"
    "s_waitcnt lgkmcnt(6)\n"
    "s_lshr_b32 s58, s56, 1\n"
    "s_lshl_b32 s58, s58, 15\n"
    "v_add_u32 v122, s58, v121\n"
    "v_add_u32 v123, 0x10000, v122\n"
    "s_lshl_b32 s50, s56, 14\n"
    "v_mov_b32 v118, 0\n"
    "v_mov_b32 v117, s43\n"
    "s_add_u32 s58, s50, 0x400000\n"
    "s_add_u32 s48, s40, s58\n"
    "s_addc_u32 s49, s41, 0\n"
    "s_mov_b32 s57, 0\n"
    "s_mov_b32 s51, 0\n"
    "s_waitcnt lgkmcnt(0)\n"
    "s_barrier\n"
    "s_add_u32 s58, s40, 0x20e000\n"
    "s_addc_u32 s59, s41, 0\n"
    "s_add_u32 s60, s40, 0x200200\n"
    "s_addc_u32 s61, s41, 0\n"
    "s_or_b32 s62, s42, 1\n"
    "s_mov_b32 s63, s43\n"
    "s_branch G_xc0%=\n"
    "G_xw0%=:\n"
    "global_load_dwordx2 v[226:227], v224, s[58:59] sc1\n"
    "G_xc0%=:\n"
    "s_waitcnt vmcnt(0)\n"
    "v_cmp_ne_u64 vcc, s[62:63], v[226:227]\n"
    "s_nop 0\n"
    "s_cbranch_vccz G_xok0%=\n"
    "s_sleep 4\n"
    "s_add_u32 s57, s57, 1\n"
    "s_and_b32 s68, s57, 0xff\n"
    "s_cbranch_scc1 G_xw0%=\n"
    "global_load_dwordx2 v[226:227], v118, s[60:61] sc1\n"
    "s_waitcnt vmcnt(0)\n"
    "v_cmp_eq_u64 vcc, s[42:43], v[226:227]\n"
    "s_nop 0\n"
    "s_cbranch_vccnz G_xok0%=\n"
    "s_cmp_lt_u32 s57, 0x40000\n"
    "s_cbranch_scc1 G_xw0%=\n"
    "v_mov_b32 v116, s42\n"
    "global_store_dwordx2 v118, v[116:117], s[60:61] sc1\n"
    "G_xok0%=:\n"
    "s_mov_b32 vcc_lo, 0xcccccccc\n"
    "s_mov_b32 vcc_hi, 0xcccccccc\n"
    "s_add_u32 m0, s50, 0\n"
    "s_add_u32 s64, s48, 0\n"
    "s_addc_u32 s65, s49, 0\n"
    "global_load_lds_dwordx4 v121, s[64:65] offset:0 sc1\n"
    "global_load_lds_dwordx4 v121, s[64:65] offset:1024 sc1\n"
    "global_load_lds_dwordx4 v121, s[64:65] offset:2048 sc1\n"
    "global_load_lds_dwordx4 v121, s[64:65] offset:3072 sc1\n"
    "s_add_u32 m0, s50, 4096\n"
    "s_add_u32 s64, s48, 4096\n"
    "s_addc_u32 s65, s49, 0\n"
    "global_load_lds_dwordx4 v121, s[64:65] offset:0 sc1\n"
    "global_load_lds_dwordx4 v121, s[64:65] offset:1024 sc1\n"
    "global_load_lds_dwordx4 v121, s[64:65] offset:2048 sc1\n"
    "global_load_lds_dwordx4 v121, s[64:65] offset:3072 sc1\n"
    "s_add_u32 m0, s50, 8192\n"
    "s_add_u32 s64, s48, 8192\n"
    "s_addc_u32 s65, s49, 0\n"
    "global_load_lds_dwordx4 v121, s[64:65] offset:0 sc1\n"
    "global_load_lds_dwordx4 v121, s[64:65] offset:1024 sc1\n"
    "global_load_lds_dwordx4 v121, s[64:65] offset:2048 sc1\n"
    "global_load_lds_dwordx4 v121, s[64:65] offset:3072 sc1\n"
    "s_add_u32 m0, s50, 12288\n"
    "s_add_u32 s64, s48, 12288\n"
    "s_addc_u32 s65, s49, 0\n"
    "s_add_u32 s48, s48, 0x10000\n"
    "s_addc_u32 s49, s49, 0\n"
    "global_load_lds_dwordx4 v121, s[64:65] offset:0 sc1\n"
    "global_load_lds_dwordx4 v121, s[64:65] offset:1024 sc1\n"
    "global_load_lds_dwordx4 v121, s[64:65] offset:2048 sc1\n"
    "global_load_lds_dwordx4 v121, s[64:65] offset:3072 sc1\n"
    "s_add_u32 m0, s50, 65536\n"
    "s_add_u32 s64, s48, 0\n"
    "s_addc_u32 s65, s49, 0\n"
    "global_load_lds_dwordx4 v121, s[64:65] offset:0 sc1\n"
    "global_load_lds_dwordx4 v121, s[64:65] offset:1024 sc1\n"
    "global_load_lds_dwordx4 v121, s[64:65] offset:2048 sc1\n"
    "global_load_lds_dwordx4 v121, s[64:65] offset:3072 sc1\n"
    "s_add_u32 m0, s50, 69632\n"
    "s_add_u32 s64, s48, 4096\n"
    "s_addc_u32 s65, s49, 0\n"
    "global_load_lds_dwordx4 v121, s[64:65] offset:0 sc1\n"
    "global_load_lds_dwordx4 v121, s[64:65] offset:1024 sc1\n"
    "global_load_lds_dwordx4 v121, s[64:65] offset:2048 sc1\n"
    "global_load_lds_dwordx4 v121, s[64:65] offset:3072 sc1\n"
    "s_add_u32 m0, s50, 73728\n"
    "s_add_u32 s64, s48, 8192\n"
    "s_addc_u32 s65, s49, 0\n"
    "global_load_lds_dwordx4 v121, s[64:65] offset:0 sc1\n"
    "global_load_lds_dwordx4 v121, s[64:65] offset:1024 sc1\n"
    "global_load_lds_dwordx4 v121, s[64:65] offset:2048 sc1\n"
    "s_mov_b64 s[66:67], s[44:45]\n"
    "s_sub_u32 s44, s44, 0x2200000\n"
    "s_subb_u32 s45, s45, 0\n"
    "s_waitcnt vmcnt(19) lgkmcnt(0)\n"
    "s_barrier\n"
    "v_lshlrev_b32 v112, 3, v121\n"
    "v_min_u32 v112, 0x500, v112\n"
    "s_add_u32 s58, s40, 0x312000\n"
    "s_addc_u32 s59, s41, 0\n"
    "global_load_dwordx2 v[114:115], v112, s[58:59] sc1\n"
    "ds_read_b128 v[192:195], v122 offset:0\n"
    "ds_read_b128 v[196:199], v122 offset:16384\n"
    "ds_read_b128 v[200:203], v122 offset:1024\n"
    "ds_read_b128 v[204:207], v122 offset:17408\n"
    "ds_read_b128 v[208:211], v122 offset:2048\n"
    "ds_read_b128 v[212:215], v122 offset:18432\n"
    "s_waitcnt lgkmcnt(4)\n"
    "s_branch G_even%=\n"
    ".p2align 6\n"
    "G_loop%=:\n"
    "v_mfma_f32_16x16x32_bf16 v[160:163], v[192:195], a[0:3], 0\n"
    "ds_read_b128 v[216:219], v123 offset:3072\n"
    "v_mfma_f32_16x16x32_bf16 v[164:167], v[192:195], a[64:67], 0\n"
    "ds_read_b128 v[220:223], v123 offset:19456\n"
    "v_mfma_f32_16x16x32_bf16 v[168:171], v[192:195], a[128:131], 0\n"
    "v_mfma_f32_16x16x32_bf16 v[172:175], v[192:195], a[192:195], 0\n"
    "v_cvt_pk_bf16_f32 v224, v128, v129\n"
    "v_cvt_pk_bf16_f32 v225, v130, v131\n"
    "v_mfma_f32_16x16x32_bf16 v[176:179], v[196:199], a[0:3], 0\n"
    "global_load_lds_dwordx4 v121, s[64:65] offset:3072 sc1\n"
    "v_mfma_f32_16x16x32_bf16 v[180:183], v[196:199], a[64:67], 0\n"
    "v_cvt_pk_bf16_f32 v226, v144, v145\n"
    "v_cvt_pk_bf16_f32 v227, v146, v147\n"
    "v_mfma_f32_16x16x32_bf16 v[184:187], v[196:199], a[128:131], 0\n"
    "v_cvt_pk_bf16_f32 v228, v132, v133\n"
    "v_cvt_pk_bf16_f32 v229, v134, v135\n"
    "v_mfma_f32_16x16x32_bf16 v[188:191], v[196:199], a[192:195], 0\n"
    "v_cvt_pk_bf16_f32 v230, v148, v149\n"
    "v_cvt_pk_bf16_f32 v231, v150, v151\n"
    "s_waitcnt lgkmcnt(4)\n"
    "v_mfma_f32_16x16x32_bf16 v[160:163], v[200:203], a[4:7], v[160:163]\n"
    "ds_read_b128 v[192:195], v123 offset:4096\n"
    "v_mfma_f32_16x16x32_bf16 v[164:167], v[200:203], a[68:71], v[164:167]\n"
    "ds_read_b128 v[196:199], v123 offset:20480\n"
    "v_mfma_f32_16x16x32_bf16 v[168:171], v[200:203], a[132:135], v[168:171]\n"
    "s_add_u32 m0, s50, 12288\n"
    "s_add_u32 s64, s48, 12288\n"
    "s_addc_u32 s65, s49, 0\n"
    "v_cvt_pk_bf16_f32 v232, v136, v137\n"
    "v_cvt_pk_bf16_f32 v233, v138, v139\n"
    "v_mfma_f32_16x16x32_bf16 v[172:175], v[200:203], a[196:199], v[172:175]\n"
    "s_add_u32 s48, s48, 0x10000\n"
    "s_addc_u32 s49, s49, 0\n"
    "v_cvt_pk_bf16_f32 v234, v152, v153\n"
    "v_cvt_pk_bf16_f32 v235, v154, v155\n"
    "v_mfma_f32_16x16x32_bf16 v[176:179], v[204:207], a[4:7], v[176:179]\n"
    "global_load_lds_dwordx4 v121, s[64:65] offset:0 sc1\n"
    "v_mfma_f32_16x16x32_bf16 v[180:183], v[204:207], a[68:71], v[180:183]\n"
    "v_cvt_pk_bf16_f32 v236, v140, v141\n"
    "v_cvt_pk_bf16_f32 v237, v142, v143\n"
    "v_mfma_f32_16x16x32_bf16 v[184:187], v[204:207], a[132:135], v[184:187]\n"
    "global_store_dwordx4 v121, v[224:227], s[44:45] offset:0 sc1\n"
    "v_mfma_f32_16x16x32_bf16 v[188:191], v[204:207], a[196:199], v[188:191]\n"
    "v_cvt_pk_bf16_f32 v238, v156, v157\n"
    "v_cvt_pk_bf16_f32 v239, v158, v159\n"
    "s_waitcnt lgkmcnt(4)\n"
    "v_mfma_f32_16x16x32_bf16 v[160:163], v[208:211], a[8:11], v[160:163]\n"
    "ds_read_b128 v[200:203], v123 offset:5120\n"
    "v_mfma_f32_16x16x32_bf16 v[164:167], v[208:211], a[72:75], v[164:167]\n"
    "ds_read_b128 v[204:207], v123 offset:21504\n"
    "v_mfma_f32_16x16x32_bf16 v[168:171], v[208:211], a[136:139], v[168:171]\n"
    "v_mfma_f32_16x16x32_bf16 v[172:175], v[208:211], a[200:203], v[172:175]\n"
    "v_mfma_f32_16x16x32_bf16 v[176:179], v[212:215], a[8:11], v[176:179]\n"
    "global_load_lds_dwordx4 v121, s[64:65] offset:1024 sc1\n"
    "v_mfma_f32_16x16x32_bf16 v[180:183], v[212:215], a[72:75], v[180:183]\n"
    "v_mfma_f32_16x16x32_bf16 v[184:187], v[212:215], a[136:139], v[184:187]\n"
    "global_store_dwordx4 v121, v[228:231], s[44:45] offset:1024 sc1\n"
    "v_mfma_f32_16x16x32_bf16 v[188:191], v[212:215], a[200:203], v[188:191]\n"
    "s_waitcnt lgkmcnt(4)\n"
    "v_mfma_f32_16x16x32_bf16 v[160:163], v[216:219], a[12:15], v[160:163]\n"
    "ds_read_b128 v[208:211], v123 offset:6144\n"
    "v_mfma_f32_16x16x32_bf16 v[164:167], v[216:219], a[76:79], v[164:167]\n"
    "ds_read_b128 v[212:215], v123 offset:22528\n"
    "v_mfma_f32_16x16x32_bf16 v[168:171], v[216:219], a[140:143], v[168:171]\n"
    "v_mfma_f32_16x16x32_bf16 v[172:175], v[216:219], a[204:207], v[172:175]\n"
    "v_mfma_f32_16x16x32_bf16 v[176:179], v[220:223], a[12:15], v[176:179]\n"
    "global_load_lds_dwordx4 v121, s[64:65] offset:2048 sc1\n"
    "v_mfma_f32_16x16x32_bf16 v[180:183], v[220:223], a[76:79], v[180:183]\n"
    "v_mfma_f32_16x16x32_bf16 v[184:187], v[220:223], a[140:143], v[184:187]\n"
    "global_store_dwordx4 v121, v[232:235], s[44:45] offset:2048 sc1\n"
    "v_mfma_f32_16x16x32_bf16 v[188:191], v[220:223], a[204:207], v[188:191]\n"
    "s_waitcnt lgkmcnt(4)\n"
    "v_mfma_f32_16x16x32_bf16 v[160:163], v[192:195], a[16:19], v[160:163]\n"
    "ds_read_b128 v[216:219], v123 offset:7168\n"
    "v_mfma_f32_16x16x32_bf16 v[164:167], v[192:195], a[80:83], v[164:167]\n"
    "ds_read_b128 v[220:223], v123 offset:23552\n"
    "v_mfma_f32_16x16x32_bf16 v[168:171], v[192:195], a[144:147], v[168:171]\n"
    "v_mfma_f32_16x16x32_bf16 v[172:175], v[192:195], a[208:211], v[172:175]\n"
    "v_mfma_f32_16x16x32_bf16 v[176:179], v[196:199], a[16:19], v[176:179]\n"
    "global_load_lds_dwordx4 v121, s[64:65] offset:3072 sc1\n"
    "v_mfma_f32_16x16x32_bf16 v[180:183], v[196:199], a[80:83], v[180:183]\n"
    "v_mfma_f32_16x16x32_bf16 v[184:187], v[196:199], a[144:147], v[184:187]\n"
    "global_store_dwordx4 v121, v[236:239], s[44:45] offset:3072 sc1\n"
    "v_mfma_f32_16x16x32_bf16 v[188:191], v[196:199], a[208:211], v[188:191]\n"
    "s_waitcnt lgkmcnt(4)\n"
    "v_mfma_f32_16x16x32_bf16 v[160:163], v[200:203], a[20:23], v[160:163]\n"
    "s_waitcnt vmcnt(12) lgkmcnt(0)\n"
    "s_barrier\n"
    "v_mfma_f32_16x16x32_bf16 v[164:167], v[200:203], a[84:87], v[164:167]\n"
    "ds_read_b128 v[192:195], v123 offset:8192\n"
    "v_mfma_f32_16x16x32_bf16 v[168:171], v[200:203], a[148:151], v[168:171]\n"
    "ds_read_b128 v[196:199], v123 offset:24576\n"
    "s_add_u32 m0, s50, 65536\n"
    "s_add_u32 s64, s48, 0\n"
    "s_addc_u32 s65, s49, 0\n"
    "v_mfma_f32_16x16x32_bf16 v[172:175], v[200:203], a[212:215], v[172:175]\n"
    "v_mfma_f32_16x16x32_bf16 v[176:179], v[204:207], a[20:23], v[176:179]\n"
    "global_load_lds_dwordx4 v121, s[64:65] offset:0 sc1\n"
    "v_mfma_f32_16x16x32_bf16 v[180:183], v[204:207], a[84:87], v[180:183]\n"
    "v_mfma_f32_16x16x32_bf16 v[184:187], v[204:207], a[148:151], v[184:187]\n"
    "v_mfma_f32_16x16x32_bf16 v[188:191], v[204:207], a[212:215], v[188:191]\n"
    "s_waitcnt lgkmcnt(4)\n"
    "v_mfma_f32_16x16x32_bf16 v[160:163], v[208:211], a[24:27], v[160:163]\n"
    "ds_read_b128 v[200:203], v123 offset:9216\n"
    "v_mfma_f32_16x16x32_bf16 v[164:167], v[208:211], a[88:91], v[164:167]\n"
    "ds_read_b128 v[204:207], v123 offset:25600\n"
    "v_mfma_f32_16x16x32_bf16 v[168:171], v[208:211], a[152:155], v[168:171]\n"
    "v_mfma_f32_16x16x32_bf16 v[172:175], v[208:211], a[216:219], v[172:175]\n"
    "v_mfma_f32_16x16x32_bf16 v[176:179], v[212:215], a[24:27], v[176:179]\n"
    "global_load_lds_dwordx4 v121, s[64:65] offset:1024 sc1\n"
    "v_mfma_f32_16x16x32_bf16 v[180:183], v[212:215], a[88:91], v[180:183]\n"
    "v_mfma_f32_16x16x32_bf16 v[184:187], v[212:215], a[152:155], v[184:187]\n"
    "v_mfma_f32_16x16x32_bf16 v[188:191], v[212:215], a[216:219], v[188:191]\n"
    "s_waitcnt lgkmcnt(4)\n"
    "v_mfma_f32_16x16x32_bf16 v[160:163], v[216:219], a[28:31], v[160:163]\n"
    "ds_read_b128 v[208:211], v123 offset:10240\n"
    "v_mfma_f32_16x16x32_bf16 v[164:167], v[216:219], a[92:95], v[164:167]\n"
    "ds_read_b128 v[212:215], v123 offset:26624\n"
    "v_mfma_f32_16x16x32_bf16 v[168:171], v[216:219], a[156:159], v[168:171]\n"
    "v_mfma_f32_16x16x32_bf16 v[172:175], v[216:219], a[220:223], v[172:175]\n"
    "v_mfma_f32_16x16x32_bf16 v[176:179], v[220:223], a[28:31], v[176:179]\n"
    "global_load_lds_dwordx4 v121, s[64:65] offset:2048 sc1\n"
    "v_mfma_f32_16x16x32_bf16 v[180:183], v[220:223], a[92:95], v[180:183]\n"
    "s_mov_b64 s[44:45], s[66:67]\n"
    "s_add_u32 s66, s66, 0x100000\n"
    "s_addc_u32 s67, s67, 0\n"
    "v_mfma_f32_16x16x32_bf16 v[184:187], v[220:223], a[156:159], v[184:187]\n"
    "s_xor_b32 s66, s66, 0x2000\n"
    "v_mfma_f32_16x16x32_bf16 v[188:191], v[220:223], a[220:223], v[188:191]\n"
    "s_waitcnt lgkmcnt(4)\n"
    "v_mfma_f32_16x16x32_bf16 v[160:163], v[192:195], a[32:35], v[160:163]\n"
    "ds_read_b128 v[216:219], v123 offset:11264\n"
    "v_mfma_f32_16x16x32_bf16 v[164:167], v[192:195], a[96:99], v[164:167]\n"
    "ds_read_b128 v[220:223], v123 offset:27648\n"
    "v_mfma_f32_16x16x32_bf16 v[168:171], v[192:195], a[160:163], v[168:171]\n"
    "v_mfma_f32_16x16x32_bf16 v[172:175], v[192:195], a[224:227], v[172:175]\n"
    "v_mfma_f32_16x16x32_bf16 v[176:179], v[196:199], a[32:35], v[176:179]\n"
    "global_load_lds_dwordx4 v121, s[64:65] offset:3072 sc1\n"
    "v_mfma_f32_16x16x32_bf16 v[180:183], v[196:199], a[96:99], v[180:183]\n"
    "v_mfma_f32_16x16x32_bf16 v[184:187], v[196:199], a[160:163], v[184:187]\n"
    "v_mfma_f32_16x16x32_bf16 v[188:191], v[196:199], a[224:227], v[188:191]\n"
    "s_waitcnt lgkmcnt(4)\n"
    "v_mfma_f32_16x16x32_bf16 v[160:163], v[200:203], a[36:39], v[160:163]\n"
    "ds_read_b128 v[192:195], v123 offset:12288\n"
    "v_mfma_f32_16x16x32_bf16 v[164:167], v[200:203], a[100:103], v[164:167]\n"
    "ds_read_b128 v[196:199], v123 offset:28672\n"
    "v_mfma_f32_16x16x32_bf16 v[168:171], v[200:203], a[164:167], v[168:171]\n"
    "s_add_u32 m0, s50, 69632\n"
    "s_add_u32 s64, s48, 4096\n"
    "s_addc_u32 s65, s49, 0\n"
    "v_mfma_f32_16x16x32_bf16 v[172:175], v[200:203], a[228:231], v[172:175]\n"
    "v_mfma_f32_16x16x32_bf16 v[176:179], v[204:207], a[36:39], v[176:179]\n"
    "global_load_lds_dwordx4 v121, s[64:65] offset:0 sc1\n"
    "v_mfma_f32_16x16x32_bf16 v[180:183], v[204:207], a[100:103], v[180:183]\n"
    "v_mfma_f32_16x16x32_bf16 v[184:187], v[204:207], a[164:167], v[184:187]\n"
    "v_mfma_f32_16x16x32_bf16 v[188:191], v[204:207], a[228:231], v[188:191]\n"
    "s_waitcnt lgkmcnt(4)\n"
    "v_mfma_f32_16x16x32_bf16 v[160:163], v[208:211], a[40:43], v[160:163]\n"
    "ds_read_b128 v[200:203], v123 offset:13312\n"
    "v_mfma_f32_16x16x32_bf16 v[164:167], v[208:211], a[104:107], v[164:167]\n"
    "ds_read_b128 v[204:207], v123 offset:29696\n"
    "v_mfma_f32_16x16x32_bf16 v[168:171], v[208:211], a[168:171], v[168:171]\n"
    "v_mfma_f32_16x16x32_bf16 v[172:175], v[208:211], a[232:235], v[172:175]\n"
    "v_mfma_f32_16x16x32_bf16 v[176:179], v[212:215], a[40:43], v[176:179]\n"
    "global_load_lds_dwordx4 v121, s[64:65] offset:1024 sc1\n"
    "v_mfma_f32_16x16x32_bf16 v[180:183], v[212:215], a[104:107], v[180:183]\n"
    "v_mfma_f32_16x16x32_bf16 v[184:187], v[212:215], a[168:171], v[184:187]\n"
    "v_mfma_f32_16x16x32_bf16 v[188:191], v[212:215], a[232:235], v[188:191]\n"
    "s_waitcnt lgkmcnt(4)\n"
    "v_mfma_f32_16x16x32_bf16 v[160:163], v[216:219], a[44:47], v[160:163]\n"
    "ds_read_b128 v[208:211], v123 offset:14336\n"
    "v_mfma_f32_16x16x32_bf16 v[164:167], v[216:219], a[108:111], v[164:167]\n"
    "ds_read_b128 v[212:215], v123 offset:30720\n"
    "v_mfma_f32_16x16x32_bf16 v[168:171], v[216:219], a[172:175], v[168:171]\n"
    "v_mfma_f32_16x16x32_bf16 v[172:175], v[216:219], a[236:239], v[172:175]\n"
    "v_mfma_f32_16x16x32_bf16 v[176:179], v[220:223], a[44:47], v[176:179]\n"
    "global_load_lds_dwordx4 v121, s[64:65] offset:2048 sc1\n"
    "v_mfma_f32_16x16x32_bf16 v[180:183], v[220:223], a[108:111], v[180:183]\n"
    "v_mfma_f32_16x16x32_bf16 v[184:187], v[220:223], a[172:175], v[184:187]\n"
    "v_mfma_f32_16x16x32_bf16 v[188:191], v[220:223], a[236:239], v[188:191]\n"
    "s_waitcnt lgkmcnt(4)\n"
    "v_mfma_f32_16x16x32_bf16 v[160:163], v[192:195], a[48:51], v[160:163]\n"
    "ds_read_b128 v[216:219], v123 offset:15360\n"
    "v_mfma_f32_16x16x32_bf16 v[164:167], v[192:195], a[112:115], v[164:167]\n"
    "ds_read_b128 v[220:223], v123 offset:31744\n"
    "v_mfma_f32_16x16x32_bf16 v[168:171], v[192:195], a[176:179], v[168:171]\n"
    "v_mfma_f32_16x16x32_bf16 v[172:175], v[192:195], a[240:243], v[172:175]\n"
    "v_mfma_f32_16x16x32_bf16 v[176:179], v[196:199], a[48:51], v[176:179]\n"
    "global_load_lds_dwordx4 v121, s[64:65] offset:3072 sc1\n"
    "v_mfma_f32_16x16x32_bf16 v[180:183], v[196:199], a[112:115], v[180:183]\n"
    "v_mfma_f32_16x16x32_bf16 v[184:187], v[196:199], a[176:179], v[184:187]\n"
    "v_mfma_f32_16x16x32_bf16 v[188:191], v[196:199], a[240:243], v[188:191]\n"
    "s_waitcnt lgkmcnt(4)\n"
    "v_mfma_f32_16x16x32_bf16 v[160:163], v[200:203], a[52:55], v[160:163]\n"
    "s_waitcnt vmcnt(8) lgkmcnt(0)\n"
    "s_barrier\n"
    "v_mfma_f32_16x16x32_bf16 v[164:167], v[200:203], a[116:119], v[164:167]\n"
    "ds_read_b128 v[192:195], v122 offset:0\n"
    "v_mfma_f32_16x16x32_bf16 v[168:171], v[200:203], a[180:183], v[168:171]\n"
    "ds_read_b128 v[196:199], v122 offset:16384\n"
    "s_add_u32 m0, s50, 73728\n"
    "s_add_u32 s64, s48, 8192\n"
    "s_addc_u32 s65, s49, 0\n"
    "v_mfma_f32_16x16x32_bf16 v[172:175], v[200:203], a[244:247], v[172:175]\n"
    "v_mfma_f32_16x16x32_bf16 v[176:179], v[204:207], a[52:55], v[176:179]\n"
    "global_load_lds_dwordx4 v121, s[64:65] offset:0 sc1\n"
    "v_mfma_f32_16x16x32_bf16 v[180:183], v[204:207], a[116:119], v[180:183]\n"
    "s_cmp_lg_u32 s56, 0\n"
    "s_cbranch_scc1 G_f1%=\n"
    "s_or_b32 s58, s42, s51\n"
    "s_mov_b64 exec, 1\n"
    "v_mov_b32 v116, s58\n"
    "global_store_dwordx2 v118, v[116:117], s[54:55] sc1\n"
    "s_mov_b64 exec, -1\n"
    "G_f1%=:\n"
    "v_mfma_f32_16x16x32_bf16 v[184:187], v[204:207], a[180:183], v[184:187]\n"
    "v_mfma_f32_16x16x32_bf16 v[188:191], v[204:207], a[244:247], v[188:191]\n"
    "s_waitcnt lgkmcnt(4)\n"
    "v_mfma_f32_16x16x32_bf16 v[160:163], v[208:211], a[56:59], v[160:163]\n"
    "ds_read_b128 v[200:203], v122 offset:1024\n"
    "v_mfma_f32_16x16x32_bf16 v[164:167], v[208:211], a[120:123], v[164:167]\n"
    "ds_read_b128 v[204:207], v122 offset:17408\n"
    "v_mfma_f32_16x16x32_bf16 v[168:171], v[208:211], a[184:187], v[168:171]\n"
    "v_mfma_f32_16x16x32_bf16 v[172:175], v[208:211], a[248:251], v[172:175]\n"
    "v_mfma_f32_16x16x32_bf16 v[176:179], v[212:215], a[56:59], v[176:179]\n"
    "global_load_lds_dwordx4 v121, s[64:65] offset:1024 sc1\n"
    "v_mfma_f32_16x16x32_bf16 v[180:183], v[212:215], a[120:123], v[180:183]\n"
    "v_mfma_f32_16x16x32_bf16 v[184:187], v[212:215], a[184:187], v[184:187]\n"
    "v_mfma_f32_16x16x32_bf16 v[188:191], v[212:215], a[248:251], v[188:191]\n"
    "s_add_u32 s51, s51, 1\n"
    "s_waitcnt lgkmcnt(4)\n"
    "v_mfma_f32_16x16x32_bf16 v[160:163], v[216:219], a[60:63], v[160:163]\n"
    "ds_read_b128 v[208:211], v122 offset:2048\n"
    "v_mfma_f32_16x16x32_bf16 v[164:167], v[216:219], a[124:127], v[164:167]\n"
    "ds_read_b128 v[212:215], v122 offset:18432\n"
    "v_mfma_f32_16x16x32_bf16 v[168:171], v[216:219], a[188:191], v[168:171]\n"
    "v_mfma_f32_16x16x32_bf16 v[172:175], v[216:219], a[252:255], v[172:175]\n"
    "v_mfma_f32_16x16x32_bf16 v[176:179], v[220:223], a[60:63], v[176:179]\n"
    "global_load_lds_dwordx4 v121, s[64:65] offset:2048 sc1\n"
    "v_mfma_f32_16x16x32_bf16 v[180:183], v[220:223], a[124:127], v[180:183]\n"
    "v_mfma_f32_16x16x32_bf16 v[184:187], v[220:223], a[188:191], v[184:187]\n"
    "v_mfma_f32_16x16x32_bf16 v[188:191], v[220:223], a[252:255], v[188:191]\n"
    "s_add_u32 s58, s51, -3\n"
    "s_cmp_gt_u32 s58, 28\n"
    "s_waitcnt lgkmcnt(4)\n"
    "s_cbranch_scc1 G_t1%=\n"
    "G_even%=:\n"
    "v_mfma_f32_16x16x32_bf16 v[128:131], v[192:195], a[0:3], 0\n"
    "ds_read_b128 v[216:219], v122 offset:3072\n"
    "v_mfma_f32_16x16x32_bf16 v[132:135], v[192:195], a[64:67], 0\n"
    "ds_read_b128 v[220:223], v122 offset:19456\n"
    "v_mfma_f32_16x16x32_bf16 v[136:139], v[192:195], a[128:131], 0\n"
    "v_mfma_f32_16x16x32_bf16 v[140:143], v[192:195], a[192:195], 0\n"
    "v_cvt_pk_bf16_f32 v224, v160, v161\n"
    "v_cvt_pk_bf16_f32 v225, v162, v163\n"
    "v_mfma_f32_16x16x32_bf16 v[144:147], v[196:199], a[0:3], 0\n"
    "global_load_lds_dwordx4 v121, s[64:65] offset:3072 sc1\n"
    "v_mfma_f32_16x16x32_bf16 v[148:151], v[196:199], a[64:67], 0\n"
    "v_cvt_pk_bf16_f32 v226, v176, v177\n"
    "v_cvt_pk_bf16_f32 v227, v178, v179\n"
    "v_mfma_f32_16x16x32_bf16 v[152:155], v[196:199], a[128:131], 0\n"
    "v_cvt_pk_bf16_f32 v228, v164, v165\n"
    "v_cvt_pk_bf16_f32 v229, v166, v167\n"
    "v_mfma_f32_16x16x32_bf16 v[156:159], v[196:199], a[192:195], 0\n"
    "v_cvt_pk_bf16_f32 v230, v180, v181\n"
    "v_cvt_pk_bf16_f32 v231, v182, v183\n"
    "s_waitcnt lgkmcnt(4)\n"
    "v_mfma_f32_16x16x32_bf16 v[128:131], v[200:203], a[4:7], v[128:131]\n"
    "ds_read_b128 v[192:195], v122 offset:4096\n"
    "v_mfma_f32_16x16x32_bf16 v[132:135], v[200:203], a[68:71], v[132:135]\n"
    "ds_read_b128 v[196:199], v122 offset:20480\n"
    "v_mfma_f32_16x16x32_bf16 v[136:139], v[200:203], a[132:135], v[136:139]\n"
    "s_add_u32 m0, s50, 77824\n"
    "s_add_u32 s64, s48, 12288\n"
    "s_addc_u32 s65, s49, 0\n"
    "v_cvt_pk_bf16_f32 v232, v168, v169\n"
    "v_cvt_pk_bf16_f32 v233, v170, v171\n"
    "v_mfma_f32_16x16x32_bf16 v[140:143], v[200:203], a[196:199], v[140:143]\n"
    "s_add_u32 s48, s48, 0x10000\n"
    "s_addc_u32 s49, s49, 0\n"
    "v_cvt_pk_bf16_f32 v234, v184, v185\n"
    "v_cvt_pk_bf16_f32 v235, v186, v187\n"
    "v_mfma_f32_16x16x32_bf16 v[144:147], v[204:207], a[4:7], v[144:147]\n"
    "global_load_lds_dwordx4 v121, s[64:65] offset:0 sc1\n"
    "v_mfma_f32_16x16x32_bf16 v[148:151], v[204:207], a[68:71], v[148:151]\n"
    "v_cvt_pk_bf16_f32 v236, v172, v173\n"
    "v_cvt_pk_bf16_f32 v237, v174, v175\n"
    "v_mfma_f32_16x16x32_bf16 v[152:155], v[204:207], a[132:135], v[152:155]\n"
    "global_store_dwordx4 v121, v[224:227], s[44:45] offset:0 sc1\n"
    "v_mfma_f32_16x16x32_bf16 v[156:159], v[204:207], a[196:199], v[156:159]\n"
    "v_cvt_pk_bf16_f32 v238, v188, v189\n"
    "v_cvt_pk_bf16_f32 v239, v190, v191\n"
    "s_waitcnt lgkmcnt(4)\n"
    "v_mfma_f32_16x16x32_bf16 v[128:131], v[208:211], a[8:11], v[128:131]\n"
    "ds_read_b128 v[200:203], v122 offset:5120\n"
    "v_mfma_f32_16x16x32_bf16 v[132:135], v[208:211], a[72:75], v[132:135]\n"
    "ds_read_b128 v[204:207], v122 offset:21504\n"
    "v_mfma_f32_16x16x32_bf16 v[136:139], v[208:211], a[136:139], v[136:139]\n"
    "v_mfma_f32_16x16x32_bf16 v[140:143], v[208:211], a[200:203], v[140:143]\n"
    "v_mfma_f32_16x16x32_bf16 v[144:147], v[212:215], a[8:11], v[144:147]\n"
    "global_load_lds_dwordx4 v121, s[64:65] offset:1024 sc1\n"
    "v_mfma_f32_16x16x32_bf16 v[148:151], v[212:215], a[72:75], v[148:151]\n"
    "v_mfma_f32_16x16x32_bf16 v[152:155], v[212:215], a[136:139], v[152:155]\n"
    "global_store_dwordx4 v121, v[228:231], s[44:45] offset:1024 sc1\n"
    "v_mfma_f32_16x16x32_bf16 v[156:159], v[212:215], a[200:203], v[156:159]\n"
    "s_waitcnt lgkmcnt(4)\n"
    "v_mfma_f32_16x16x32_bf16 v[128:131], v[216:219], a[12:15], v[128:131]\n"
    "ds_read_b128 v[208:211], v122 offset:6144\n"
    "v_mfma_f32_16x16x32_bf16 v[132:135], v[216:219], a[76:79], v[132:135]\n"
    "ds_read_b128 v[212:215], v122 offset:22528\n"
    "v_mfma_f32_16x16x32_bf16 v[136:139], v[216:219], a[140:143], v[136:139]\n"
    "v_mfma_f32_16x16x32_bf16 v[140:143], v[216:219], a[204:207], v[140:143]\n"
    "v_mfma_f32_16x16x32_bf16 v[144:147], v[220:223], a[12:15], v[144:147]\n"
    "global_load_lds_dwordx4 v121, s[64:65] offset:2048 sc1\n"
    "v_mfma_f32_16x16x32_bf16 v[148:151], v[220:223], a[76:79], v[148:151]\n"
    "v_mfma_f32_16x16x32_bf16 v[152:155], v[220:223], a[140:143], v[152:155]\n"
    "global_store_dwordx4 v121, v[232:235], s[44:45] offset:2048 sc1\n"
    "v_mfma_f32_16x16x32_bf16 v[156:159], v[220:223], a[204:207], v[156:159]\n"
    "s_waitcnt lgkmcnt(4)\n"
    "v_mfma_f32_16x16x32_bf16 v[128:131], v[192:195], a[16:19], v[128:131]\n"
    "ds_read_b128 v[216:219], v122 offset:7168\n"
    "v_mfma_f32_16x16x32_bf16 v[132:135], v[192:195], a[80:83], v[132:135]\n"
    "ds_read_b128 v[220:223], v122 offset:23552\n"
    "v_mfma_f32_16x16x32_bf16 v[136:139], v[192:195], a[144:147], v[136:139]\n"
    "v_mfma_f32_16x16x32_bf16 v[140:143], v[192:195], a[208:211], v[140:143]\n"
    "v_mfma_f32_16x16x32_bf16 v[144:147], v[196:199], a[16:19], v[144:147]\n"
    "global_load_lds_dwordx4 v121, s[64:65] offset:3072 sc1\n"
    "v_mfma_f32_16x16x32_bf16 v[148:151], v[196:199], a[80:83], v[148:151]\n"
    "v_mfma_f32_16x16x32_bf16 v[152:155], v[196:199], a[144:147], v[152:155]\n"
    "global_store_dwordx4 v121, v[236:239], s[44:45] offset:3072 sc1\n"
    "v_mfma_f32_16x16x32_bf16 v[156:159], v[196:199], a[208:211], v[156:159]\n"
    "s_waitcnt lgkmcnt(4)\n"
    "v_mfma_f32_16x16x32_bf16 v[128:131], v[200:203], a[20:23], v[128:131]\n"
    "s_waitcnt vmcnt(12) lgkmcnt(0)\n"
    "s_barrier\n"
    "v_mfma_f32_16x16x32_bf16 v[132:135], v[200:203], a[84:87], v[132:135]\n"
    "ds_read_b128 v[192:195], v122 offset:8192\n"
    "v_mfma_f32_16x16x32_bf16 v[136:139], v[200:203], a[148:151], v[136:139]\n"
    "ds_read_b128 v[196:199], v122 offset:24576\n"
    "s_add_u32 m0, s50, 0\n"
    "s_add_u32 s64, s48, 0\n"
    "s_addc_u32 s65, s49, 0\n"
    "v_mfma_f32_16x16x32_bf16 v[140:143], v[200:203], a[212:215], v[140:143]\n"
    "v_mfma_f32_16x16x32_bf16 v[144:147], v[204:207], a[20:23], v[144:147]\n"
    "global_load_lds_dwordx4 v121, s[64:65] offset:0 sc1\n"
    "v_mfma_f32_16x16x32_bf16 v[148:151], v[204:207], a[84:87], v[148:151]\n"
    "v_mfma_f32_16x16x32_bf16 v[152:155], v[204:207], a[148:151], v[152:155]\n"
    "v_mfma_f32_16x16x32_bf16 v[156:159], v[204:207], a[212:215], v[156:159]\n"
    "s_waitcnt lgkmcnt(4)\n"
    "v_mfma_f32_16x16x32_bf16 v[128:131], v[208:211], a[24:27], v[128:131]\n"
    "ds_read_b128 v[200:203], v122 offset:9216\n"
    "v_mfma_f32_16x16x32_bf16 v[132:135], v[208:211], a[88:91], v[132:135]\n"
    "ds_read_b128 v[204:207], v122 offset:25600\n"
    "v_mfma_f32_16x16x32_bf16 v[136:139], v[208:211], a[152:155], v[136:139]\n"
    "v_mfma_f32_16x16x32_bf16 v[140:143], v[208:211], a[216:219], v[140:143]\n"
    "v_mfma_f32_16x16x32_bf16 v[144:147], v[212:215], a[24:27], v[144:147]\n"
    "global_load_lds_dwordx4 v121, s[64:65] offset:1024 sc1\n"
    "v_mfma_f32_16x16x32_bf16 v[148:151], v[212:215], a[88:91], v[148:151]\n"
    "v_mfma_f32_16x16x32_bf16 v[152:155], v[212:215], a[152:155], v[152:155]\n"
    "v_mfma_f32_16x16x32_bf16 v[156:159], v[212:215], a[216:219], v[156:159]\n"
    "s_waitcnt lgkmcnt(4)\n"
    "v_mfma_f32_16x16x32_bf16 v[128:131], v[216:219], a[28:31], v[128:131]\n"
    "ds_read_b128 v[208:211], v122 offset:10240\n"
    "v_mfma_f32_16x16x32_bf16 v[132:135], v[216:219], a[92:95], v[132:135]\n"
    "ds_read_b128 v[212:215], v122 offset:26624\n"
    "v_mfma_f32_16x16x32_bf16 v[136:139], v[216:219], a[156:159], v[136:139]\n"
    "v_mfma_f32_16x16x32_bf16 v[140:143], v[216:219], a[220:223], v[140:143]\n"
    "v_mfma_f32_16x16x32_bf16 v[144:147], v[220:223], a[28:31], v[144:147]\n"
    "global_load_lds_dwordx4 v121, s[64:65] offset:2048 sc1\n"
    "v_mfma_f32_16x16x32_bf16 v[148:151], v[220:223], a[92:95], v[148:151]\n"
    "s_mov_b64 s[44:45], s[66:67]\n"
    "s_add_u32 s66, s66, 0x100000\n"
    "s_addc_u32 s67, s67, 0\n"
    "v_mfma_f32_16x16x32_bf16 v[152:155], v[220:223], a[156:159], v[152:155]\n"
    "v_mfma_f32_16x16x32_bf16 v[156:159], v[220:223], a[220:223], v[156:159]\n"
    "s_waitcnt lgkmcnt(4)\n"
    "v_mfma_f32_16x16x32_bf16 v[128:131], v[192:195], a[32:35], v[128:131]\n"
    "ds_read_b128 v[216:219], v122 offset:11264\n"
    "v_mfma_f32_16x16x32_bf16 v[132:135], v[192:195], a[96:99], v[132:135]\n"
    "ds_read_b128 v[220:223], v122 offset:27648\n"
    "v_mfma_f32_16x16x32_bf16 v[136:139], v[192:195], a[160:163], v[136:139]\n"
    "v_mfma_f32_16x16x32_bf16 v[140:143], v[192:195], a[224:227], v[140:143]\n"
    "v_mfma_f32_16x16x32_bf16 v[144:147], v[196:199], a[32:35], v[144:147]\n"
    "global_load_lds_dwordx4 v121, s[64:65] offset:3072 sc1\n"
    "v_mfma_f32_16x16x32_bf16 v[148:151], v[196:199], a[96:99], v[148:151]\n"
    "v_mfma_f32_16x16x32_bf16 v[152:155], v[196:199], a[160:163], v[152:155]\n"
    "v_mfma_f32_16x16x32_bf16 v[156:159], v[196:199], a[224:227], v[156:159]\n"
    "s_waitcnt lgkmcnt(4)\n"
    "v_mfma_f32_16x16x32_bf16 v[128:131], v[200:203], a[36:39], v[128:131]\n"
    "ds_read_b128 v[192:195], v122 offset:12288\n"
    "v_mfma_f32_16x16x32_bf16 v[132:135], v[200:203], a[100:103], v[132:135]\n"
    "ds_read_b128 v[196:199], v122 offset:28672\n"
    "v_mfma_f32_16x16x32_bf16 v[136:139], v[200:203], a[164:167], v[136:139]\n"
    "s_add_u32 m0, s50, 4096\n"
    "s_add_u32 s64, s48, 4096\n"
    "s_addc_u32 s65, s49, 0\n"
    "v_mfma_f32_16x16x32_bf16 v[140:143], v[200:203], a[228:231], v[140:143]\n"
    "v_mfma_f32_16x16x32_bf16 v[144:147], v[204:207], a[36:39], v[144:147]\n"
    "global_load_lds_dwordx4 v121, s[64:65] offset:0 sc1\n"
    "v_mfma_f32_16x16x32_bf16 v[148:151], v[204:207], a[100:103], v[148:151]\n"
    "v_mfma_f32_16x16x32_bf16 v[152:155], v[204:207], a[164:167], v[152:155]\n"
    "v_mfma_f32_16x16x32_bf16 v[156:159], v[204:207], a[228:231], v[156:159]\n"
    "s_waitcnt lgkmcnt(4)\n"
    "v_mfma_f32_16x16x32_bf16 v[128:131], v[208:211], a[40:43], v[128:131]\n"
    "ds_read_b128 v[200:203], v122 offset:13312\n"
    "v_mfma_f32_16x16x32_bf16 v[132:135], v[208:211], a[104:107], v[132:135]\n"
    "ds_read_b128 v[204:207], v122 offset:29696\n"
    "v_mfma_f32_16x16x32_bf16 v[136:139], v[208:211], a[168:171], v[136:139]\n"
    "v_mfma_f32_16x16x32_bf16 v[140:143], v[208:211], a[232:235], v[140:143]\n"
    "v_mfma_f32_16x16x32_bf16 v[144:147], v[212:215], a[40:43], v[144:147]\n"
    "global_load_lds_dwordx4 v121, s[64:65] offset:1024 sc1\n"
    "v_mfma_f32_16x16x32_bf16 v[148:151], v[212:215], a[104:107], v[148:151]\n"
    "v_mfma_f32_16x16x32_bf16 v[152:155], v[212:215], a[168:171], v[152:155]\n"
    "v_mfma_f32_16x16x32_bf16 v[156:159], v[212:215], a[232:235], v[156:159]\n"
    "s_waitcnt lgkmcnt(4)\n"
    "v_mfma_f32_16x16x32_bf16 v[128:131], v[216:219], a[44:47], v[128:131]\n"
    "ds_read_b128 v[208:211], v122 offset:14336\n"
    "v_mfma_f32_16x16x32_bf16 v[132:135], v[216:219], a[108:111], v[132:135]\n"
    "ds_read_b128 v[212:215], v122 offset:30720\n"
    "v_mfma_f32_16x16x32_bf16 v[136:139], v[216:219], a[172:175], v[136:139]\n"
    "v_mfma_f32_16x16x32_bf16 v[140:143], v[216:219], a[236:239], v[140:143]\n"
    "v_mfma_f32_16x16x32_bf16 v[144:147], v[220:223], a[44:47], v[144:147]\n"
    "global_load_lds_dwordx4 v121, s[64:65] offset:2048 sc1\n"
    "v_mfma_f32_16x16x32_bf16 v[148:151], v[220:223], a[108:111], v[148:151]\n"
    "v_mfma_f32_16x16x32_bf16 v[152:155], v[220:223], a[172:175], v[152:155]\n"
    "v_mfma_f32_16x16x32_bf16 v[156:159], v[220:223], a[236:239], v[156:159]\n"
    "s_waitcnt lgkmcnt(4)\n"
    "v_mfma_f32_16x16x32_bf16 v[128:131], v[192:195], a[48:51], v[128:131]\n"
    "ds_read_b128 v[216:219], v122 offset:15360\n"
    "v_mfma_f32_16x16x32_bf16 v[132:135], v[192:195], a[112:115], v[132:135]\n"
    "ds_read_b128 v[220:223], v122 offset:31744\n"
    "v_mfma_f32_16x16x32_bf16 v[136:139], v[192:195], a[176:179], v[136:139]\n"
    "v_mfma_f32_16x16x32_bf16 v[140:143], v[192:195], a[240:243], v[140:143]\n"
    "v_mfma_f32_16x16x32_bf16 v[144:147], v[196:199], a[48:51], v[144:147]\n"
    "global_load_lds_dwordx4 v121, s[64:65] offset:3072 sc1\n"
    "v_mfma_f32_16x16x32_bf16 v[148:151], v[196:199], a[112:115], v[148:151]\n"
    "v_mfma_f32_16x16x32_bf16 v[152:155], v[196:199], a[176:179], v[152:155]\n"
    "v_mfma_f32_16x16x32_bf16 v[156:159], v[196:199], a[240:243], v[156:159]\n"
    "s_waitcnt lgkmcnt(4)\n"
    "v_mfma_f32_16x16x32_bf16 v[128:131], v[200:203], a[52:55], v[128:131]\n"
    "s_waitcnt vmcnt(8) lgkmcnt(0)\n"
    "s_barrier\n"
    "v_mfma_f32_16x16x32_bf16 v[132:135], v[200:203], a[116:119], v[132:135]\n"
    "ds_read_b128 v[192:195], v123 offset:0\n"
    "v_mfma_f32_16x16x32_bf16 v[136:139], v[200:203], a[180:183], v[136:139]\n"
    "ds_read_b128 v[196:199], v123 offset:16384\n"
    "s_add_u32 m0, s50, 8192\n"
    "s_add_u32 s64, s48, 8192\n"
    "s_addc_u32 s65, s49, 0\n"
    "v_mfma_f32_16x16x32_bf16 v[140:143], v[200:203], a[244:247], v[140:143]\n"
    "v_mfma_f32_16x16x32_bf16 v[144:147], v[204:207], a[52:55], v[144:147]\n"
    "global_load_lds_dwordx4 v121, s[64:65] offset:0 sc1\n"
    "v_mfma_f32_16x16x32_bf16 v[148:151], v[204:207], a[116:119], v[148:151]\n"
    "s_cmp_lg_u32 s56, 0\n"
    "s_cbranch_scc1 G_f2%=\n"
    "s_or_b32 s58, s42, s51\n"
    "s_mov_b64 exec, 1\n"
    "v_mov_b32 v116, s58\n"
    "global_store_dwordx2 v118, v[116:117], s[54:55] sc1\n"
    "s_mov_b64 exec, -1\n"
    "G_f2%=:\n"
    "v_mfma_f32_16x16x32_bf16 v[152:155], v[204:207], a[180:183], v[152:155]\n"
    "v_mfma_f32_16x16x32_bf16 v[156:159], v[204:207], a[244:247], v[156:159]\n"
    "s_waitcnt lgkmcnt(4)\n"
    "v_mfma_f32_16x16x32_bf16 v[128:131], v[208:211], a[56:59], v[128:131]\n"
    "ds_read_b128 v[200:203], v123 offset:1024\n"
    "v_mfma_f32_16x16x32_bf16 v[132:135], v[208:211], a[120:123], v[132:135]\n"
    "ds_read_b128 v[204:207], v123 offset:17408\n"
    "v_mfma_f32_16x16x32_bf16 v[136:139], v[208:211], a[184:187], v[136:139]\n"
    "v_mfma_f32_16x16x32_bf16 v[140:143], v[208:211], a[248:251], v[140:143]\n"
    "v_mfma_f32_16x16x32_bf16 v[144:147], v[212:215], a[56:59], v[144:147]\n"
    "global_load_lds_dwordx4 v121, s[64:65] offset:1024 sc1\n"
    "v_mfma_f32_16x16x32_bf16 v[148:151], v[212:215], a[120:123], v[148:151]\n"
    "v_mfma_f32_16x16x32_bf16 v[152:155], v[212:215], a[184:187], v[152:155]\n"
    "v_mfma_f32_16x16x32_bf16 v[156:159], v[212:215], a[248:251], v[156:159]\n"
    "s_add_u32 s51, s51, 1\n"
    "s_waitcnt lgkmcnt(4)\n"
    "v_mfma_f32_16x16x32_bf16 v[128:131], v[216:219], a[60:63], v[128:131]\n"
    "ds_read_b128 v[208:211], v123 offset:2048\n"
    "v_mfma_f32_16x16x32_bf16 v[132:135], v[216:219], a[124:127], v[132:135]\n"
    "ds_read_b128 v[212:215], v123 offset:18432\n"
    "v_mfma_f32_16x16x32_bf16 v[136:139], v[216:219], a[188:191], v[136:139]\n"
    "v_mfma_f32_16x16x32_bf16 v[140:143], v[216:219], a[252:255], v[140:143]\n"
    "v_mfma_f32_16x16x32_bf16 v[144:147], v[220:223], a[60:63], v[144:147]\n"
    "global_load_lds_dwordx4 v121, s[64:65] offset:2048 sc1\n"
    "v_mfma_f32_16x16x32_bf16 v[148:151], v[220:223], a[124:127], v[148:151]\n"
    "v_mfma_f32_16x16x32_bf16 v[152:155], v[220:223], a[188:191], v[152:155]\n"
    "v_mfma_f32_16x16x32_bf16 v[156:159], v[220:223], a[252:255], v[156:159]\n"
    "s_add_u32 s58, s51, -3\n"
    "s_cmp_gt_u32 s58, 28\n"
    "s_waitcnt lgkmcnt(4)\n"
    "s_cbranch_scc0 G_loop%=\n"
    "s_branch G_pc%=\n"
    "G_pw%=:\n"
    "s_add_u32 s58, s40, 0x312000\n"
    "s_addc_u32 s59, s41, 0\n"
    "global_load_dwordx2 v[114:115], v112, s[58:59] sc1\n"
    "s_waitcnt vmcnt(0)\n"
    "G_pc%=:\n"
    "v_xor_b32 v114, s42, v114\n"
    "v_xor_b32 v115, s43, v115\n"
    "v_add_u32 v114, -1, v114\n"
    "v_cmp_le_u64 s[58:59], 16, v[114:115]\n"
    "v_and_b32 v114, 2, v114\n"
    "v_cmp_ne_u32 s[52:53], 0, v114\n"
    "s_cmp_eq_u64 s[58:59], 0\n"
    "s_cbranch_scc1 G_pok%=\n"
    "s_sleep 4\n"
    "s_add_u32 s57, s57, 1\n"
    "s_cmp_lt_u32 s57, 0x40000\n"
    "s_cbranch_scc1 G_pw%=\n"
    "G_pok%=:\n"
    "s_and_b32 s52, s52, 0x7ff\n"
    "s_bcnt1_i32_b32 s52, s52\n"
    "s_cmp_ge_u32 s52, 6\n"
    "s_cselect_b32 s52, 0, 0x2000\n"
    "s_xor_b32 s44, s44, s52\n"
    "s_xor_b32 s66, s66, s52\n"
    "s_nop 7\n"
    "v_cvt_pk_bf16_f32 v224, v128, v129\n"
    "v_cvt_pk_bf16_f32 v225, v130, v131\n"
    "v_cvt_pk_bf16_f32 v226, v144, v145\n"
    "v_cvt_pk_bf16_f32 v227, v146, v147\n"
    "v_cvt_pk_bf16_f32 v228, v132, v133\n"
    "v_cvt_pk_bf16_f32 v229, v134, v135\n"
    "v_cvt_pk_bf16_f32 v230, v148, v149\n"
    "v_cvt_pk_bf16_f32 v231, v150, v151\n"
    "v_cvt_pk_bf16_f32 v232, v136, v137\n"
    "v_cvt_pk_bf16_f32 v233, v138, v139\n"
    "v_cvt_pk_bf16_f32 v234, v152, v153\n"
    "v_cvt_pk_bf16_f32 v235, v154, v155\n"
    "v_cvt_pk_bf16_f32 v236, v140, v141\n"
    "v_cvt_pk_bf16_f32 v237, v142, v143\n"
    "v_cvt_pk_bf16_f32 v238, v156, v157\n"
    "v_cvt_pk_bf16_f32 v239, v158, v159\n"
    "global_store_dwordx4 v121, v[224:227], s[44:45] offset:0 sc1\n"
    "global_store_dwordx4 v121, v[228:231], s[44:45] offset:1024 sc1\n"
    "global_store_dwordx4 v121, v[232:235], s[44:45] offset:2048 sc1\n"
    "global_store_dwordx4 v121, v[236:239], s[44:45] offset:3072 sc1\n"
    "s_sub_u32 s44, s44, 0x2200000\n"
    "s_subb_u32 s45, s45, 0\n"
    "s_waitcnt vmcnt(0)\n"
    "s_barrier\n"
    "s_cmp_lg_u32 s56, 0\n"
    "s_cbranch_scc1 G_f3%=\n"
    "s_or_b32 s58, s42, s51\n"
    "s_mov_b64 exec, 1\n"
    "v_mov_b32 v116, s58\n"
    "global_store_dwordx2 v118, v[116:117], s[54:55] sc1\n"
    "s_mov_b64 exec, -1\n"
    "G_f3%=:\n"
    "s_branch G_loop%=\n"
    "G_t1%=:\n"
    "s_nop 7\n"
    "v_cvt_pk_bf16_f32 v224, v160, v161\n"
    "v_cvt_pk_bf16_f32 v225, v162, v163\n"
    "v_cvt_pk_bf16_f32 v226, v176, v177\n"
    "v_cvt_pk_bf16_f32 v227, v178, v179\n"
    "v_cvt_pk_bf16_f32 v228, v164, v165\n"
    "v_cvt_pk_bf16_f32 v229, v166, v167\n"
    "v_cvt_pk_bf16_f32 v230, v180, v181\n"
    "v_cvt_pk_bf16_f32 v231, v182, v183\n"
    "v_cvt_pk_bf16_f32 v232, v168, v169\n"
    "v_cvt_pk_bf16_f32 v233, v170, v171\n"
    "v_cvt_pk_bf16_f32 v234, v184, v185\n"
    "v_cvt_pk_bf16_f32 v235, v186, v187\n"
    "v_cvt_pk_bf16_f32 v236, v172, v173\n"
    "v_cvt_pk_bf16_f32 v237, v174, v175\n"
    "v_cvt_pk_bf16_f32 v238, v188, v189\n"
    "v_cvt_pk_bf16_f32 v239, v190, v191\n"
    "global_store_dwordx4 v121, v[224:227], s[44:45] offset:0 sc1\n"
    "global_store_dwordx4 v121, v[228:231], s[44:45] offset:1024 sc1\n"
    "global_store_dwordx4 v121, v[232:235], s[44:45] offset:2048 sc1\n"
    "global_store_dwordx4 v121, v[236:239], s[44:45] offset:3072 sc1\n"
    "s_sub_u32 s44, s44, 0x2200000\n"
    "s_subb_u32 s45, s45, 0\n"
    "s_waitcnt vmcnt(0)\n"
    "s_barrier\n"
    "s_cmp_lg_u32 s56, 0\n"
    "s_cbranch_scc1 G_f4%=\n"
    "s_or_b32 s58, s42, s51\n"
    "s_mov_b64 exec, 1\n"
    "v_mov_b32 v116, s58\n"
    "global_store_dwordx2 v118, v[116:117], s[54:55] sc1\n"
    "s_mov_b64 exec, -1\n"
    "G_f4%=:\n"
    "s_cmp_eq_u32 s51, 32\n"
    "s_cbranch_scc1 G_end%=\n"
    "s_add_u32 s58, s40, 0x20e000\n"
    "s_addc_u32 s59, s41, 0\n"
    "s_add_u32 s60, s40, 0x200200\n"
    "s_addc_u32 s61, s41, 0\n"
    "s_or_b32 s62, s42, 1\n"
    "s_mov_b32 s63, s43\n"
    "v_lshlrev_b32 v224, 3, v121\n"
    "G_xw1%=:\n"
    "global_load_dwordx2 v[226:227], v224, s[58:59] sc1\n"
    "G_xc1%=:\n"
    "s_waitcnt vmcnt(0)\n"
    "v_cmp_ne_u64 vcc, s[62:63], v[226:227]\n"
    "s_nop 0\n"
    "s_cbranch_vccz G_xok1%=\n"
    "s_sleep 4\n"
    "s_add_u32 s57, s57, 1\n"
    "s_and_b32 s68, s57, 0xff\n"
    "s_cbranch_scc1 G_xw1%=\n"
    "global_load_dwordx2 v[226:227], v118, s[60:61] sc1\n"
    "s_waitcnt vmcnt(0)\n"
    "v_cmp_eq_u64 vcc, s[42:43], v[226:227]\n"
    "s_nop 0\n"
    "s_cbranch_vccnz G_xok1%=\n"
    "s_cmp_lt_u32 s57, 0x40000\n"
    "s_cbranch_scc1 G_xw1%=\n"
    "v_mov_b32 v116, s42\n"
    "global_store_dwordx2 v118, v[116:117], s[60:61] sc1\n"
    "G_xok1%=:\n"
    "s_mov_b32 vcc_lo, 0xcccccccc\n"
    "s_mov_b32 vcc_hi, 0xcccccccc\n"
    "s_branch G_even%=\n"
    "G_end%=:\n"
    "s_waitcnt lgkmcnt(0)\n"
    :: GIN : GCLOB);
  return T;
}

__device__ __forceinline__ void xa_phase(char* ws, int grp, int h, u64 T, unsigned far, int lane) {
  const int l15 = lane & 15, g = lane >> 4;
  const float scale = 0.08838834764831845f;
  const __amdgpu_buffer_rsrc_t rs = __builtin_amdgcn_make_buffer_rsrc(ws, 0, (int)(250 * MBY), 0x00020000);
  auto ld16 = [&](int off) { return __builtin_bit_cast(bf16x8, __builtin_amdgcn_raw_buffer_load_b128(rs, off, 0, 16)); };
  PTD;
#pragma unroll
  for (int i = 0; i < (NSEG - SLATE) * 2; ++i) store16_sc1(ws + OFF_O, (unsigned)(blk(SLATE + (i >> 3) * 4 + grp, h, far) + (i & 7) * 1024 + lane * 16), (u32x4){SENT, SENT, SENT, SENT});
#pragma unroll 1
  for (int s = grp; s < NSEG; s += 4) {
    if (s < 48) wait_flag<8>(ws, (unsigned)OFF_RDY + ((lane < 3 ? lane : 0) * 32 + h) * 128, T, s < 44 ? (unsigned)(s >> 1) + 1u : 32u);
    const int bo = blk(s, h, far);
    const int qp = (int)OFF_QF + bo + lane * 16;
    const int kp = (int)OFF_KF + bo + ((l15 >> 1) & 1) * 1024 + (g * 16 + (l15 & 13)) * 16;
    const int vp = (int)OFF_VF + bo + lane * 16;
    const int km = (int)OFF_KM + bo;
    const int vm = km + 4096;
    bf16x8 qf[4][2], vf[8];
    f32x4 sc[2][3];
#pragma unroll
    for (int nt = 0; nt < 2; ++nt)
#pragma unroll
      for (int t = 0; t < 3; ++t) sc[nt][t] = (f32x4){0.f, 0.f, 0.f, 0.f};
#pragma unroll
    for (int ks = 0; ks < 4; ++ks) {
      bf16x8 kf[2];
#pragma unroll
      for (int nt = 0; nt < 2; ++nt) qf[ks][nt] = ld16(qp + ks * 2048 + nt * 1024);
#pragma unroll
      for (int t = 0; t < 2; ++t) kf[t] = ld16(kp + (ks >> 1) * 4096 + (ks & 1) * 32 + t * 2048);
#pragma unroll
      for (int nt = 0; nt < 2; ++nt)
#pragma unroll
        for (int t = 0; t < 2; ++t) sc[nt][t] = __builtin_amdgcn_mfma_f32_16x16x32_bf16(kf[t], qf[ks][nt], sc[nt][t], 0, 0, 0);
    }
#pragma unroll
    for (int e = 0; e < 8; ++e) vf[e] = ld16(vp + e * 1024);
    float mt[2], lt[2];
    f32x4 acc[2][8];
#pragma unroll
    for (int nt = 0; nt < 2; ++nt) {
      const int qi = nt * 16 + l15;
      float v[2][4], m = -3.0e38f;
#pragma unroll
      for (int t = 0; t < 2; ++t)
#pragma unroll
        for (int r = 0; r < 4; ++r) {
          v[t][r] = (t * 16 + g * 4 + r <= qi) ? rbf(rbf(sc[nt][t][r]) * scale) : -3.0e38f;
          m = fmaxf(m, v[t][r]);
        }
      m = max_g(m);
#pragma unroll
      for (int t = 0; t < 2; ++t)
#pragma unroll
        for (int r = 0; r < 4; r += 2) {
          v[t][r] = (t * 16 + g * 4 + r <= qi) ? __expf(v[t][r] - m) : 0.f;
          v[t][r + 1] = (t * 16 + g * 4 + r + 1 <= qi) ? __expf(v[t][r + 1] - m) : 0.f;
          rbf2(v[t][r], v[t][r + 1]);
        }
      mt[nt] = m;
      lt[nt] = sum_g(((v[0][0] + v[0][1]) + (v[0][2] + v[0][3])) + ((v[1][0] + v[1][1]) + (v[1][2] + v[1][3])));
      const bf16x8 pf = mk8(pk2(v[0][0], v[0][1]), pk2(v[0][2], v[0][3]), pk2(v[1][0], v[1][1]), pk2(v[1][2], v[1][3]));
#pragma unroll
      for (int e = 0; e < 8; ++e) acc[nt][e] = __builtin_amdgcn_mfma_f32_16x16x32_bf16(vf[e], pf, (f32x4){0.f, 0.f, 0.f, 0.f}, 0, 0, 0);
    }
#pragma unroll
    for (int e = 0; e < 8; ++e) asm volatile("" : "+v"(acc[0][e]), "+v"(acc[1][e]));
    asm volatile("" : "+v"(mt[0]), "+v"(mt[1]), "+v"(lt[0]), "+v"(lt[1]));
    if (h == 5) TLX(s * 8);
    wait_flag<PSL>(ws, (unsigned)OFF_FLAGS + (s * 64 + h + (lane & 1) * 32) * 128, T, 1u);
    if (s == 63) TL(4);
    PT(1, s == 63);
    if (h == 5) TLX(s * 8 + 1);
    bf16x8 k2[4];
    unsigned mv[4][2];
    for (unsigned spins = 0;;) {
#pragma unroll
    for (int ks = 0; ks < 4; ++ks) k2[ks] = ld16(km + (ks >> 1) * 1024 + (g * 16 + ((l15 >> 3) | ((l15 >> 1) & 2)) * 4 + (ks & 1) * 2 + (l15 & 1)) * 16);
#pragma unroll
    for (int w = 0; w < 4; ++w) { const auto t = __builtin_amdgcn_raw_buffer_load_b64(rs, vm + w * 512 + lane * 8, 0, 16); mv[w][0] = t[0]; mv[w][1] = t[1]; }
      bool mk = false;
#pragma unroll
      for (int ks = 0; ks < 4; ++ks) mk |= marked(__builtin_bit_cast(u32x4, k2[ks])) || mv[ks][0] == SENT || mv[ks][1] == SENT;
      if (!__any(mk) || spin_bail(spins, ws, T)) break;
      asm volatile("" ::: "memory");
    }
#pragma unroll
    for (int ks = 0; ks < 4; ++ks)
#pragma unroll
      for (int nt = 0; nt < 2; ++nt) sc[nt][2] = __builtin_amdgcn_mfma_f32_16x16x32_bf16(k2[ks], qf[ks][nt], sc[nt][2], 0, 0, 0);
#pragma unroll
    for (int nt = 0; nt < 2; ++nt) {
      float a = sc[nt][2][0], b = sc[nt][2][1];
      rbf2(a, b); a *= scale; b *= scale; rbf2(a, b);
      const float m = fmaxf(mt[nt], max_g(fmaxf(a, b)));
      const float al = __expf(mt[nt] - m);
      a = __expf(a - m); b = __expf(b - m);
      const float inv = __builtin_amdgcn_rcpf(al * lt[nt] + sum_g(a + b));
      const float sa = al * inv;
      if (nt == OFNT && s >= SLATE && lane == 0) set_flag(ws, OFF_OFL + (s * 32 + h) * 128, T, 1);
      const bf16x8 pf1 = mk8(pk2(a * inv, b * inv), 0u, 0u, 0u);
#pragma unroll
      for (int e = 0; e < 8; ++e) acc[nt][e] = __builtin_amdgcn_mfma_f32_16x16x32_bf16(mk8(mv[e >> 1][e & 1], 0u, 0u, 0u), pf1, acc[nt][e] * sa, 0, 0, 0);
#pragma unroll
      for (int w = 0; w < 4; ++w) {
        const f32x4 a0 = acc[nt][2 * w], a1 = acc[nt][2 * w + 1];
        u32x4 o; o.x = pk2(a0[0], a0[1]); o.y = pk2(a1[0], a1[1]); o.z = pk2(a0[2], a0[3]); o.w = pk2(a1[2], a1[3]);
        if (OFNT == 3 && nt == 1 && w == 0 && s >= SLATE && lane == 0) set_flag(ws, OFF_OFL + (s * 32 + h) * 128, T, 1);
        store16_sc1(ws + OFF_O, (unsigned)(bo + (nt * 4 + w) * 1024 + lane * 16), o);
      }
    }
    if (s < SLATE) {
      asm volatile("s_waitcnt vmcnt(0)" ::: "memory");
      if (lane == 0) set_flag(ws, OFF_OFL + (s * 32 + h) * 128, T, 1);
    } else if (OFNT == 2 && lane == 0) set_flag(ws, OFF_OFL + (s * 32 + h) * 128, T, 1);
    if (h == 5) TLX(s * 8 + 2);
    PT(2, s >= 60);
  }
  PTE;
}

static_assert(SLATE % 4 == 0 && SLATE >= NSIX, "late segments belong to the four last groups");
__device__ __forceinline__ void op_phase(char* ws, const u16* __restrict__ x, const u16* __restrict__ w_r, u16* out, const u16* __restrict__ Wo, int grp, int ks4, int cs, u64 T, unsigned far, char* lds, int tid) {
  const int lane = tid & 63, wv = __builtin_amdgcn_readfirstlane(tid >> 6), l15 = lane & 15, g = lane >> 4;
  const __amdgpu_buffer_rsrc_t rs = __builtin_amdgcn_make_buffer_rsrc(ws, 0, (int)(250 * MBY), 0x00020000);
  const int h0 = ks4 * 8 + wv * 2;
  PTD;
  PT(5, grp < 4);
  bf16x8 wo[2][32];
#pragma unroll
  for (int hh = 0; hh < 2; ++hh) {
#pragma unroll
    for (int i = 0; i < 32; ++i) {
      const int r = i * 4 + g, f = (r & 1) | ((r >> 1) & 2) | ((r >> 2) & 4);
      __builtin_amdgcn_global_load_lds((const GAS void*)(Wo + (size_t)((h0 + hh) * DH + r) * DM + cs * 128 + ((l15 ^ (f << 1)) * 8)),
                                       (LAS void*)(lds + wv * 32768 + i * 1024), 16, 0, 0);
    }
    asm volatile("s_waitcnt vmcnt(0)" ::: "memory");
    const int qq = l15 >> 2, f = (qq & 1) | ((g & 1) << 1) | ((qq >> 1) << 2);
    const char* rp = lds + wv * 32768 + (g * 4 + (qq & 1) + (qq >> 1) * 16) * 256 + (l15 & 3) * 8;
#pragma unroll
    for (int j = 0; j < 8; ++j)
#pragma unroll
      for (int ks = 0; ks < 4; ++ks) {
        const char* p = rp + ks * 8192 + ((j ^ f) * 32);
        wo[hh][j * 4 + ks] = tr_frag(p, p + 512);
      }
#pragma unroll
    for (int f2 = 0; f2 < 32; ++f2) asm volatile("" : "+a"(wo[hh][f2]));
    asm volatile("s_waitcnt lgkmcnt(0)" ::: "memory");
  }
  __syncthreads();
  constexpr int RS = 528, RW = 32 * RS;
  auto ln2 = [&](int s, bool fresh) {
      const int row = s * 32 + (ks4 * 4 + cs) * 2 + (wv & 1);
      const u32x4 xv = *(const u32x4*)(x + (size_t)row * DM + lane * 8);
      float wr[8];
      unpack8(*(const u32x4*)(w_r + lane * 8), wr);
      if (fresh) __builtin_amdgcn_s_sleep(HSL);
      wait_flag<PSL>(ws, (unsigned)OFF_HFL + s * 2048 + l15 * 128, T, 1u);
      if (s == 63) TL(7);
      float f[8], y[8];
      for (unsigned spins = 0; load_h(rs, xv, row, far, lane, f) && !spin_bail(spins, ws, T);) asm volatile("" ::: "memory");
      ln_row(f, y);
      float r = 0.f;
#pragma unroll
      for (int i = 0; i < 8; ++i) r += y[i] * wr[i];
      r = wave_sum(r);
      u32x4 z;
      z.x = pk2(f[0] + y[0], f[1] + y[1]); z.y = pk2(f[2] + y[2], f[3] + y[3]); z.z = pk2(f[4] + y[4], f[5] + y[5]); z.w = pk2(f[6] + y[6], f[7] + y[7]);
      const unsigned rw = (pk2(r, r) & 0xffff0000u) | 1u;
      if (lane < 8 && row >= NSPEC) __hip_atomic_store((gu32*)(ws + OFF_RC2 + (lane * 64 + ((row - NSPEC) >> 2)) * 128 + (row & 3) * 4), rw, RLX_AGENT);
      store16_sc1(out, (unsigned)(row * 1024 + lane * 16), z);
      asm volatile("s_waitcnt vmcnt(0)" ::: "memory");
      if (lane == 0) __hip_atomic_store((gu32*)(ws + OFF_RC + (row >> 2) * 128 + (row & 3) * 4), rw, RLX_AGENT);
      if ((row & 31) == 0) TLX(s * 8 + 6);
  };
  if (grp < 4) {
#pragma unroll
    for (int i = 0; i < NSEG - SLATE; ++i)
      store16_sc1(ws + OFF_HP, (unsigned)(blk(SLATE + (i & ~3) + ((NSIX + grp) & 3), (ks4 * 4 + cs) * 2 + ((i >> 1) & 1), far) + (i & 1) * 4096 + tid * 16), (u32x4){SENT, SENT, SENT, SENT});
  }
  int pend = -1, turn = 0;
  unsigned spins = 0;
  bool have = false;
  bf16x8 of[16];
  auto load_o = [&](int s) {
#pragma unroll
    for (int f = 0; f < 16; ++f) of[f] = __builtin_bit_cast(bf16x8, __builtin_amdgcn_raw_buffer_load_b128(rs, (int)OFF_O + blk(s, h0 + (f >> 3), far) + lane * 16 + (f & 7) * 1024, 0, 16));
  };
#pragma unroll 1
  for (int s = grp, sn; s < NSEG; s = sn) {
    sn = s + 6 < NSIX ? s + 6 : (s < NSIX ? (grp < 4 ? NSIX + grp : NSEG) : s + 4);
    f32x4 acc[2][8];
#pragma unroll
    for (int i = 0; i < 2; ++i)
#pragma unroll
      for (int j = 0; j < 8; ++j) { acc[i][j] = (f32x4){0.f, 0.f, 0.f, 0.f}; asm volatile("" : "+v"(acc[i][j])); }
    if (!have) {
      if (pend >= 0) { ln2(pend, pend < SLATE); pend = -1; }
      wait_flag<PSL>(ws, (unsigned)OFF_OFL + (s * 32 + h0 + (lane & 1)) * 128, T, 1u);
      if (s == 63) TL(1);
      load_o(s);
    }
    if (ks4 == 0 && cs == 0 && tid == 0) TLX(s * 8 + 3);
    const u64 nf = __hip_atomic_load((gu64*)(ws + OFF_OFL + (min(sn, NSEG - 1) * 32 + h0 + (lane & 1)) * 128), RLX_AGENT);
    __builtin_amdgcn_sched_barrier(0);
#pragma unroll
    for (int f = 0; f < 16; ++f)
#pragma unroll
      for (int j = 0; j < 8; ++j)
        asm volatile("v_mfma_f32_16x16x32_bf16 %0, %1, %2, %0" : "+v"(acc[(f >> 2) & 1][j]) : "a"(wo[f >> 3][j * 4 + (f & 3)]), "v"(of[f]));
    __builtin_amdgcn_sched_barrier(0);
    asm volatile("s_nop 7\n\ts_nop 3" : "+v"(acc[1][4]), "+v"(acc[1][5]), "+v"(acc[1][6]), "+v"(acc[1][7]), "+v"(acc[1][0]), "+v"(acc[1][1]), "+v"(acc[1][2]), "+v"(acc[1][3]));
    __builtin_amdgcn_sched_barrier(0);
    if (s >= SLATE && __any(__builtin_isunordered(acc[0][0][0], acc[1][0][0]))) {
      bool mk = false;
#pragma unroll
      for (int f = 0; f < 16; ++f) mk |= marked(__builtin_bit_cast(u32x4, of[f]));
      if (__any(mk) && !spin_bail(spins, ws, T)) { have = false; sn = s; continue; }
    }
    have = sn < NSEG && __all(nf == T + 1);
    if (have) load_o(sn);
    if (s == 63) TL(3);
    if (ks4 == 0 && cs == 0 && tid == 0) TLX(s * 8 + 4);
#pragma unroll
    for (int tt = 0; tt < 2; ++tt)
#pragma unroll
      for (int j = 0; j < 8; ++j) *(f32x4*)(lds + wv * RW + (tt * 16 + l15) * RS + j * 64 + g * 16) = acc[tt][j];
    asm volatile("s_waitcnt lgkmcnt(0)" ::: "memory");
    __builtin_amdgcn_s_barrier();
    asm volatile("" ::: "memory");
    if (HFP == 0 && tid == 0 && s >= SLATE) set_flag(ws, OFF_HFL + (s * 16 + ks4 * 4 + cs) * 128, T, 1);
#pragma unroll
    for (int i = 0; i < 4; ++i) {
      const int row = i * 8 + (tid >> 5);
      const char* p = lds + row * RS + (tid & 31) * 16;
      const f32x4 a = (*(const f32x4*)p + *(const f32x4*)(p + RW)) + (*(const f32x4*)(p + 2 * RW) + *(const f32x4*)(p + 3 * RW));
      store16_sc1(ws + OFF_HP, (unsigned)(blk(s, (ks4 * 4 + cs) * 2 + (i >> 1), far) + (i & 1) * 4096 + tid * 16), __builtin_bit_cast(u32x4, a));
    }
    if (s < SLATE) {
      asm volatile("s_waitcnt vmcnt(0)" ::: "memory");
      __syncthreads();
      if (tid == 0) set_flag(ws, OFF_HFL + (s * 16 + ks4 * 4 + cs) * 128, T, 1);
    } else {
      if (HFP == 1 && tid == 0) set_flag(ws, OFF_HFL + (s * 16 + ks4 * 4 + cs) * 128, T, 1);
      __builtin_amdgcn_s_barrier();
    }
    if (s == 63) TL(6);
    PT(3, s >= 60); PT(6, s == 48);
    if (ks4 == 0 && cs == 0 && tid == 0) TLX(s * 8 + 5);
    if (turn == 0 && pend >= 0) { ln2(pend, false); pend = -1; }
    if ((wv >> 1) == turn) pend = s;
    turn ^= 1;
  }
  if (pend >= 0) ln2(pend, pend < SLATE);
  PT(4, grp < 4);
  PTE;
}

__device__ __forceinline__ unsigned rkey(unsigned w, int idx) { return ((w ^ (((int)w >> 31) | 0x80000000u)) & 0xffff0000u) | (unsigned)(0xffff - idx); }
__device__ __forceinline__ int key_row(unsigned k) { return (0xffff - (int)(k & 0xffffu)) & 2047; }
__device__ __forceinline__ float key_val(unsigned k) { k &= 0xffff0000u; return __builtin_bit_cast(float, (k & 0x80000000u) ? k ^ 0x80000000u : (~k) & 0xffff0000u); }
__device__ __forceinline__ unsigned wave_umax(unsigned v) {
  v = max(v, (unsigned)__builtin_amdgcn_update_dpp(0, (int)v, 0x121, 0xf, 0xf, false)); v = max(v, (unsigned)__builtin_amdgcn_update_dpp(0, (int)v, 0x122, 0xf, 0xf, false));
  v = max(v, (unsigned)__builtin_amdgcn_update_dpp(0, (int)v, 0x124, 0xf, 0xf, false)); v = max(v, (unsigned)__builtin_amdgcn_update_dpp(0, (int)v, 0x128, 0xf, 0xf, false));
  return max(max((unsigned)__builtin_amdgcn_readlane((int)v, 0), (unsigned)__builtin_amdgcn_readlane((int)v, 16)),
             max((unsigned)__builtin_amdgcn_readlane((int)v, 32), (unsigned)__builtin_amdgcn_readlane((int)v, 48)));
}
__device__ __forceinline__ void push2(unsigned& a, unsigned& b, unsigned k) { b = max(b, min(a, k)); a = max(a, k); }
__device__ __forceinline__ void wave_top2(unsigned& a, unsigned& b) {
  const unsigned m = wave_umax(a);
  b = wave_umax(a == m ? b : a); a = m;
}
__device__ __forceinline__ void top2(char* ws, u64 T, int nrows, int tid, unsigned& k0, unsigned& k1, unsigned* sk) {
  const __amdgpu_buffer_rsrc_t rs = __builtin_amdgcn_make_buffer_rsrc(ws, 0, (int)(250 * MBY), 0x00020000);
  const int lane = tid & 63, wv = tid >> 6;
  const bool use0 = tid * 4 < nrows, use1 = (256 + tid) * 4 < nrows;
  u32x4 c[2];
  unsigned spins = 0;
  if (wv == 0) { wait_flag<12>(ws, (unsigned)OFF_HFL + ((nrows >> 5) - 1) * 2048, T, 1u); __builtin_amdgcn_s_sleep(48); }
  __syncthreads();
  for (;;) {
    c[0] = __builtin_bit_cast(u32x4, __builtin_amdgcn_raw_buffer_load_b128(rs, (int)OFF_RC + (use0 ? tid : 0) * 128, 0, 16));
    c[1] = __builtin_bit_cast(u32x4, __builtin_amdgcn_raw_buffer_load_b128(rs, (int)OFF_RC + (use1 ? 256 + tid : 0) * 128, 0, 16));
    if (__all(c[0].x & c[0].y & c[0].z & c[0].w & c[1].x & c[1].y & c[1].z & c[1].w & 1u)) break;
    __builtin_amdgcn_s_sleep(8);
    if (spin_bail(spins, ws, T)) break;
    asm volatile("" ::: "memory");
  }
  unsigned a = 0, b = 0;
#pragma unroll
  for (int k = 0; k < 2; ++k)
#pragma unroll
    for (int e = 0; e < 4; ++e) { const unsigned w = c[k][e]; push2(a, b, (k == 0 ? use0 : use1) ? rkey(w, (k * 256 + tid) * 4 + e) : 0u); }
  wave_top2(a, b);
  if (lane == 0) { sk[wv * 2] = a; sk[wv * 2 + 1] = b; }
  __syncthreads();
  a = 0; b = 0;
#pragma unroll
  for (int i = 0; i < 8; ++i) push2(a, b, sk[i]);
  k0 = a; k1 = b;
}
__device__ __forceinline__ bool spec_holds(char* ws, u64 T, unsigned xcc, unsigned& k0, unsigned& k1, unsigned* sk, int tid) {
  static_assert(S_ - NSPEC <= 256, "one line per lane");
  if (tid < 64) {
    const __amdgpu_buffer_rsrc_t rs = __builtin_amdgcn_make_buffer_rsrc(ws, 0, (int)(250 * MBY), 0x00020000);
    const bool use = tid < (S_ - NSPEC) / 4;
    u32x4 c;
    unsigned spins = 0;
    for (;;) {
      c = __builtin_bit_cast(u32x4, __builtin_amdgcn_raw_buffer_load_b128(rs, (int)OFF_RC2 + (xcc * 64 + (use ? tid : 0)) * 128, 0, 16));
      if (__all(c.x & c.y & c.z & c.w & 1u)) break;
      __builtin_amdgcn_s_sleep(2);
      if (spin_bail(spins, ws, T)) break;
      asm volatile("" ::: "memory");
    }
    unsigned a = 0, b = 0;
#pragma unroll
    for (int e = 0; e < 4; ++e) { const unsigned w = c[e]; push2(a, b, use ? rkey(w, NSPEC + tid * 4 + e) : 0u); }
    wave_top2(a, b);
    push2(a, b, k0); push2(a, b, k1);
    if (tid == 0) { sk[8] = a; sk[9] = b; }
  }
  __syncthreads();
  const bool same = sk[8] == k0 && sk[9] == k1;
  k0 = sk[8]; k1 = sk[9];
  return same;
}
__device__ __forceinline__ void sel_rows(char* ws, const u16* __restrict__ x, u64 T, unsigned far, int tid, int i0, int i1, float* hs, float* ys, char* yb) {
  const __amdgpu_buffer_rsrc_t rs = __builtin_amdgcn_make_buffer_rsrc(ws, 0, (int)(250 * MBY), 0x00020000);
  const int lane = tid & 63, wv = tid >> 6;
  if (wv < 2) {
    const int row = wv ? i1 : i0;
    for (unsigned spins = 0; __hip_atomic_load((gu32*)(ws + OFF_RC + (row >> 2) * 128 + (row & 3) * 4), RLX_AGENT) == 0u;)
      if (spin_bail(spins, ws, T)) break;
    float f[8], y[8];
    load_h(rs, *(const u32x4*)(x + (size_t)row * DM + lane * 8), row, far, lane, f);
    ln_row(f, y);
#pragma unroll
    for (int i = 0; i < 8; ++i) { hs[wv * DM + lane * 8 + i] = f[i]; ys[wv * DM + lane * 8 + i] = y[i]; }
    u32x4 o; o.x = pk2(y[0], y[1]); o.y = pk2(y[2], y[3]); o.z = pk2(y[4], y[5]); o.w = pk2(y[6], y[7]);
    *(u32x4*)(yb + wv * 1040 + lane * 16) = o;
  }
  __syncthreads();
}
template <int NF>
__device__ __forceinline__ void load_frags(bf16x8 (&w)[NF], const u16* __restrict__ src, int pitch, char* st, int lane) {
  const int l15 = lane & 15, g = lane >> 4, qq = l15 >> 2;
#pragma unroll
  for (int b = 0; b < NF / 2; b += 16) {
    constexpr int NK = 16;
#pragma unroll
    for (int i = 0; i < NK * 2; ++i) {
      if (b * 2 + i < NF) {
        const int r = i * 16 + (lane >> 2);
        __builtin_amdgcn_global_load_lds((const GAS void*)(src + (size_t)(b * 32 + r) * pitch + (((lane & 3) ^ ((r >> 2) & 2)) * 8)), (LAS void*)(st + i * 1024), 16, 0, 0);
      }
    }
    asm volatile("s_waitcnt vmcnt(0)" ::: "memory");
#pragma unroll
    for (int ks = 0; ks < NK; ++ks)
#pragma unroll
      for (int j = 0; j < 2; ++j)
        if ((b + ks) * 2 < NF) {
          const char* p = st + (ks * 32 + g * 8 + qq) * 64 + ((j ^ (g & 1)) * 32) + (l15 & 3) * 8;
          w[(b + ks) * 2 + j] = tr_frag(p, p + 256);
        }
#pragma unroll
    for (int f = 0; f < NK * 2; ++f) if (b * 2 + f < NF) asm volatile("" : "+a"(w[b * 2 + f]));
    asm volatile("s_waitcnt lgkmcnt(0)" ::: "memory");
  }
}

__device__ __forceinline__ void mlp1_phase(char* ws, const u16* __restrict__ x, const u16* __restrict__ wg, const u16* __restrict__ wu, int m, u64 T, unsigned far, char* lds, int tid) {
  unsigned* sk = (unsigned*)lds; float* hs = (float*)(lds + 128); float* xs = hs + 2 * DM; char* yb = (char*)(xs + 2 * DM);
  const int lane = tid & 63, wv = __builtin_amdgcn_readfirstlane(tid >> 6), l15 = lane & 15, g = lane >> 4;
  bf16x8 a[32], b[32];
  load_frags<32>(a, wg + m * 128 + wv * 32, FF, lds + 16384 + wv * 32768, lane);
  load_frags<32>(b, wu + m * 128 + wv * 32, FF, lds + 16384 + wv * 32768, lane);
  unsigned xcc, k0, k1;
  asm volatile("s_getreg_b32 %0, hwreg(HW_REG_XCC_ID)" : "=s"(xcc));
  xcc &= 7u;
#pragma unroll 1
  for (int round = 1; round < 3; ++round) {
    if (round < 2) top2(ws, T, NSPEC, tid, k0, k1, sk);
    const int i0 = key_row(k0), i1 = key_row(k1);
    TL(1);
    sel_rows(ws, x, T, far, tid, i0, i1, hs, xs, yb);
    TL(3);
    bf16x8 xf[16];
#pragma unroll
    for (int ks = 0; ks < 16; ++ks) xf[ks] = *(const bf16x8*)(yb + (l15 & 1) * 1040 + ks * 64 + g * 16);
    f32x4 acc[4];
#pragma unroll
    for (int j = 0; j < 4; ++j) { acc[j] = (f32x4){0.f, 0.f, 0.f, 0.f}; asm volatile("" : "+v"(acc[j])); }
    asm volatile("s_nop 1" ::: "memory");
#pragma unroll
    for (int ks = 0; ks < 16; ++ks)
#pragma unroll
      for (int j = 0; j < 2; ++j) {
        asm volatile("v_mfma_f32_16x16x32_bf16 %0, %1, %2, %0" : "+v"(acc[j]) : "v"(xf[ks]), "a"(a[ks * 2 + j]));
        asm volatile("v_mfma_f32_16x16x32_bf16 %0, %1, %2, %0" : "+v"(acc[2 + j]) : "v"(xf[ks]), "a"(b[ks * 2 + j]));
      }
    asm volatile("s_nop 7\n\ts_nop 3" : "+v"(acc[0]), "+v"(acc[1]), "+v"(acc[2]), "+v"(acc[3]));
    {
      const int t = g >> 1, j = g & 1;
      const float g0 = j ? acc[1][0] : acc[0][0], g1 = j ? acc[1][1] : acc[0][1], u0 = j ? acc[3][0] : acc[2][0], u1 = j ? acc[3][1] : acc[2][1];
      const float gt = rbf(t ? g1 : g0), u = rbf(t ? u1 : u0);
      const float sg = rbf(gt / (1.0f + __expf(-gt)));
      const float av = sg * u;
      u16* ab = (u16*)(lds + 12288) + wv * 64;
      ab[lane] = (u16)pk2(av, av);
      __builtin_amdgcn_fence(__ATOMIC_RELEASE, "wavefront"); __builtin_amdgcn_wave_barrier(); __builtin_amdgcn_fence(__ATOMIC_ACQUIRE, "wavefront");
      store16_sc1(ws + OFF_ACT, (unsigned)(round * 65536 + (lane >> 3) * 8192 + (m * 4 + wv) * 128 + (lane & 7) * 16), *(const u32x4*)(ab + (lane & 7) * 8));
    }
    TL(6);
    if (round == 1 && spec_holds(ws, T, xcc, k0, k1, sk, tid)) break;
  }
}

__device__ __forceinline__ void mlp2_phase(char* ws, const u16* __restrict__ x, const u16* __restrict__ wd, u16* out, int m, u64 T, unsigned far, char* lds, int tid) {
  unsigned* sk = (unsigned*)lds; float* hs = (float*)(lds + 128); float* xs = hs + 2 * DM; char* yb = (char*)(xs + 2 * DM);
  float* part = (float*)(lds + 12288);
  const __amdgpu_buffer_rsrc_t rs = __builtin_amdgcn_make_buffer_rsrc(ws, 0, (int)(250 * MBY), 0x00020000);
  const int lane = tid & 63, wv = __builtin_amdgcn_readfirstlane(tid >> 6), l15 = lane & 15, g = lane >> 4;
  bf16x8 a[28];
  load_frags<28>(a, wd + (size_t)wv * 448 * DM + m * 32, DM, lds + 16384 + wv * 32768, lane);
  unsigned xcc, k0, k1;
  asm volatile("s_getreg_b32 %0, hwreg(HW_REG_XCC_ID)" : "=s"(xcc));
  xcc &= 7u;
#pragma unroll 1
  for (int round = 1; round < 3; ++round) {
    if (round < 2) top2(ws, T, NSPEC, tid, k0, k1, sk);
    const int i0 = key_row(k0), i1 = key_row(k1);
    TL(1);
    sel_rows(ws, x, T, far, tid, i0, i1, hs, xs, yb);
    TL(3);
    u32x4 xf[14];
    unsigned spins = 0;
    __builtin_amdgcn_s_sleep(ASL);
    for (;;) {
      bool ok = true;
#pragma unroll
      for (int ks = 0; ks < 14; ++ks) {
        xf[ks] = __builtin_bit_cast(u32x4, __builtin_amdgcn_raw_buffer_load_b128(rs, (int)OFF_ACT + round * 65536 + xcc * 8192 + (wv * 14 + ks) * 128 + (l15 & 1) * 64 + g * 16, 0, 16));
        ok = ok && xf[ks].x != SENT && xf[ks].y != SENT && xf[ks].z != SENT && xf[ks].w != SENT;
      }
      if (__all(ok)) break;
      __builtin_amdgcn_s_sleep(2);
      if (spin_bail(spins, ws, T)) break;
      asm volatile("" ::: "memory");
    }
    TL(7);
    f32x4 acc[2];
#pragma unroll
    for (int j = 0; j < 2; ++j) { acc[j] = (f32x4){0.f, 0.f, 0.f, 0.f}; asm volatile("" : "+v"(acc[j])); }
    asm volatile("s_nop 1" ::: "memory");
#pragma unroll
    for (int ks = 0; ks < 14; ++ks)
#pragma unroll
      for (int j = 0; j < 2; ++j) asm volatile("v_mfma_f32_16x16x32_bf16 %0, %1, %2, %0" : "+v"(acc[j]) : "v"(xf[ks]), "a"(a[ks * 2 + j]));
    asm volatile("s_nop 7\n\ts_nop 3" : "+v"(acc[0]), "+v"(acc[1]));
    {
      const float e0 = (g & 1) ? acc[1][0] : acc[0][0], e1 = (g & 1) ? acc[1][1] : acc[0][1];
      part[tid] = (g >> 1) ? e1 : e0;
    }
    __syncthreads();
    const int t = (tid >> 4) & 1, cc = (tid & 15) * 2, idx = t ? i1 : i0, col = m * 32 + cc;
    const float r = key_val(t ? k1 : k0);
    const float sg = rbf(1.0f / (1.0f + __expf(-r)));
    float o[2];
#pragma unroll
    for (int e = 0; e < 2; ++e) {
      const int q = t * 32 + cc + e;
      const float y = (part[q] + part[64 + q]) + (part[128 + q] + part[192 + q]);
      const float upd = rbf(rbf(y) * sg);
      o[e] = hs[t * DM + col + e] + rbf(xs[t * DM + col + e] + upd);
    }
    TL(4);
    gu32* op = (gu32*)(out + (size_t)idx * DM + col);
    if (tid < 32) __hip_atomic_store(op, pk2(o[0], o[1]), RLX_AGENT);
    if (round == 2 || spec_holds(ws, T, xcc, k0, k1, sk, tid)) break;
    if (tid < 32) __hip_atomic_store(op, pk2(hs[t * DM + col] + xs[t * DM + col], hs[t * DM + col + 1] + xs[t * DM + col + 1]), RLX_AGENT);
    asm volatile("s_waitcnt vmcnt(0)" ::: "memory");
    __syncthreads();
  }
}

__device__ __forceinline__ void red_phase(char* ws, int b, u64 T, unsigned psi, int lane) {
  unsigned xcc; asm volatile("s_getreg_b32 %0, hwreg(HW_REG_XCC_ID)" : "=s"(xcc));
  const int g = lane >> 4;
  const unsigned own = (psi ^ pgc(xcc & 3u) ^ 5u) * 4096u;
  const unsigned so = (psi ^ pgc(((lane >> 3) & 1) + (lane >> 5) * 2) ^ 1u) * 4096 + (b >> 5) * 32768 + (b & 31) * 128 + ((g & 1) + (lane & 1) * 2 + (lane & 4)) * 16 + (lane & 2) * 4;
  const unsigned io = (unsigned)OFF_G2 + (b >> 2) * 65536 + (psi ^ pgc(b & 3) ^ ((b >> 5) << 2)) * 4096 + lane * 16;
  const unsigned one = (lane & 4) ? 0x3f800000u : 0x3f80u;
  asm volatile(
    "s_mov_b64 s[36:37], %0\n"
    "s_mov_b64 s[38:39], %1\n"
    "v_mov_b32 v31, %2\n"
    "v_mov_b32 v32, %3\n"
    "v_mov_b32 v33, %4\n"
    "v_mov_b32 v34, %5\n"
    "v_mov_b32 v35, %6\n"
    "v_mov_b32 v36, %7\n"
    "v_mov_b32 v37, %8\n"
    "v_mov_b32 v39, %9\n"
    "v_add_u32 v38, 0x10000, v37\n"
    "v_mov_b32 v40, 0xffbfffbf\n"
    "v_mov_b32 v41, 0xffbfffbf\n"
    "v_mov_b32 v42, 0xffbfffbf\n"
    "v_mov_b32 v43, 0xffbfffbf\n"
    "s_or_b32 s38, s38, 1\n"
    "s_mov_b32 s41, 0\n"
    "s_mov_b32 s47, 0\n"
    "s_add_u32 s64, s36, 0x200200\n"
    "s_addc_u32 s65, s37, 0\n"
    "s_getpc_b64 s[60:61]\n"
    "R_pc%=:\n"
    "s_add_u32 s60, s60, R_go%=-R_pc%=\n"
    "s_addc_u32 s61, s61, 0\n"
    "R_step%=:\n"
    "s_and_b32 s70, s41, 3\n"
    "s_lshl_b32 s71, s70, 16\n"
    "s_add_u32 s71, s71, 0x180000\n"
    "s_add_u32 s48, s36, s71\n"
    "s_addc_u32 s49, s37, 0\n"
    "s_add_u32 s71, s70, 1\n"
    "s_and_b32 s71, s71, 3\n"
    "s_lshl_b32 s71, s71, 17\n"
    "s_add_u32 s71, s71, 0x100000\n"
    "s_add_u32 s52, s36, s71\n"
    "s_addc_u32 s53, s37, 0\n"
    "s_xor_b32 s71, s71, 0x40000\n"
    "s_add_u32 s54, s36, s71\n"
    "s_addc_u32 s55, s37, 0\n"
    "s_and_b32 s70, s41, 1\n"
    "s_lshl_b32 s70, s70, 15\n"
    "s_add_u32 s50, s36, s70\n"
    "s_addc_u32 s51, s37, 0\n"
    "s_mov_b32 s45, 0\n"
    "s_mov_b32 s46, 0\n"
    "s_add_u32 s42, s38, s41\n"
    "s_cmp_eq_u32 s41, 0\n"
    "s_cbranch_scc1 R_look%=\n"
    "s_add_u32 s44, s44, 2950\n"
    "R_lookat%=:\n"
    "R_w1%=:\n"
    "s_memtime s[56:57]\n"
    "s_waitcnt lgkmcnt(0)\n"
    "s_sub_u32 s70, s44, s56\n"
    "s_sub_u32 s71, s70, 800\n"
    "s_cmp_lt_u32 s71, 0x2000\n"
    "s_cbranch_scc1 R_w1%=\n"
    "R_look%=:\n"
    "global_load_dwordx4 v[64:67], v39, s[48:49] sc1\n"
    "s_sleep 3\n"
    "s_memtime s[56:57]\n"
    "s_memrealtime s[70:71]\n"
    "s_nop 3\n"
    "s_memrealtime s[72:73]\n"
    "s_nop 3\n"
    "s_memrealtime s[74:75]\n"
    "s_nop 3\n"
    "s_memrealtime s[76:77]\n"
    "s_nop 3\n"
    "s_memrealtime s[78:79]\n"
    "s_nop 3\n"
    "s_memrealtime s[80:81]\n"
    "s_mov_b32 s59, 0\n"
    "s_waitcnt lgkmcnt(0)\n"
    "s_mul_i32 s58, s70, 24\n"
    "s_cmp_eq_u32 s70, s72\n"
    "s_addc_u32 s59, s59, 0\n"
    "s_cmp_eq_u32 s70, s74\n"
    "s_addc_u32 s59, s59, 0\n"
    "s_cmp_eq_u32 s70, s76\n"
    "s_addc_u32 s59, s59, 0\n"
    "s_cmp_eq_u32 s70, s78\n"
    "s_addc_u32 s59, s59, 0\n"
    "s_cmp_eq_u32 s70, s80\n"
    "s_addc_u32 s59, s59, 0\n"
    "s_mul_i32 s59, s59, 20\n"
    "s_sub_u32 s58, s58, s59\n"
    "s_sub_u32 s58, s58, 190\n"
    "s_waitcnt vmcnt(0)\n"
    "v_subrev_u32 v67, s58, v67\n"
    "v_cmp_ne_u64 s[74:75], s[38:39], v[64:65]\n"
    "v_cmp_ne_u32 vcc, s42, v66\n"
    "v_max_i32_dpp v67, v67, v67 row_ror:1 row_mask:0xf bank_mask:0xf\n"
    "s_nop 1\n"
    "v_max_i32_dpp v67, v67, v67 row_ror:2 row_mask:0xf bank_mask:0xf\n"
    "s_nop 1\n"
    "v_max_i32_dpp v67, v67, v67 row_ror:4 row_mask:0xf bank_mask:0xf\n"
    "s_nop 1\n"
    "v_max_i32_dpp v67, v67, v67 row_ror:8 row_mask:0xf bank_mask:0xf\n"
    "s_nop 0\n"
    "v_readlane_b32 s70, v67, 0\n"
    "v_readlane_b32 s71, v67, 16\n"
    "v_readlane_b32 s72, v67, 32\n"
    "v_readlane_b32 s73, v67, 48\n"
    "s_or_b64 vcc, vcc, s[74:75]\n"
    "s_cbranch_scc1 R_notyet%=\n"
    "s_max_i32 s70, s70, s71\n"
    "s_max_i32 s72, s72, s73\n"
    "s_max_i32 s70, s70, s72\n"
    "s_min_i32 s70, s70, 8000\n"
    "s_add_u32 s44, s56, s70\n"
    "s_cmp_gt_i32 s70, 1210\n"
    "s_cbranch_scc1 R_far%=\n"
    "R_timed%=:\n"
    "R_w2%=:\n"
    "s_memtime s[56:57]\n"
    "s_waitcnt lgkmcnt(0)\n"
    "s_sub_u32 s70, s44, s56\n"
    "s_sub_u32 s71, s70, 200\n"
    "s_cmp_lt_u32 s71, 0x2000\n"
    "s_cbranch_scc1 R_w2%=\n"
    "s_sub_i32 s70, s70, 76\n"
    "s_max_i32 s70, s70, 0\n"
    "s_min_i32 s70, s70, 124\n"
    "s_andn2_b32 s70, s70, 3\n"
    "s_sub_u32 s72, s60, s70\n"
    "s_subb_u32 s73, s61, 0\n"
    "s_setpc_b64 s[72:73]\n"
    ".p2align 6\n"
    "s_nop 0\n"
    "s_nop 0\n"
    "s_nop 0\n"
    "s_nop 0\n"
    "s_nop 0\n"
    "s_nop 0\n"
    "s_nop 0\n"
    "s_nop 0\n"
    "s_nop 0\n"
    "s_nop 0\n"
    "s_nop 0\n"
    "s_nop 0\n"
    "s_nop 0\n"
    "s_nop 0\n"
    "s_nop 0\n"
    "s_nop 0\n"
    "s_nop 0\n"
    "s_nop 0\n"
    "s_nop 0\n"
    "s_nop 0\n"
    "s_nop 0\n"
    "s_nop 0\n"
    "s_nop 0\n"
    "s_nop 0\n"
    "s_nop 0\n"
    "s_nop 0\n"
    "s_nop 0\n"
    "s_nop 0\n"
    "s_nop 0\n"
    "s_nop 0\n"
    "s_nop 0\n"
    "s_nop 0\n"
    "R_go%=:\n"
    "global_load_dwordx4 v[48:51], v36, s[50:51] offset:0 sc1\n"
    "global_load_dwordx4 v[52:55], v36, s[50:51] offset:1024 sc1\n"
    "global_load_dwordx4 v[56:59], v36, s[50:51] offset:2048 sc1\n"
    "global_load_dwordx4 v[60:63], v36, s[50:51] offset:3072 sc1\n"
    "s_waitcnt vmcnt(3)\n"
    "v_mfma_f32_16x16x32_bf16 v[44:47], v[32:35], v[48:51], 0\n"
    "s_waitcnt vmcnt(2)\n"
    "v_mfma_f32_16x16x32_bf16 v[44:47], v[32:35], v[52:55], v[44:47]\n"
    "s_waitcnt vmcnt(1)\n"
    "v_mfma_f32_16x16x32_bf16 v[44:47], v[32:35], v[56:59], v[44:47]\n"
    "s_waitcnt vmcnt(0)\n"
    "v_mfma_f32_16x16x32_bf16 v[44:47], v[32:35], v[60:63], v[44:47]\n"
    "s_nop 7\n"
    "v_add_f32_dpp v44, v44, v44 row_ror:8 row_mask:0xf bank_mask:0xf\n"
    "v_add_f32_dpp v45, v45, v45 row_ror:8 row_mask:0xf bank_mask:0xf\n"
    "v_add_f32_dpp v46, v46, v46 row_ror:8 row_mask:0xf bank_mask:0xf\n"
    "v_add_f32_dpp v47, v47, v47 row_ror:8 row_mask:0xf bank_mask:0xf\n"
    "v_cmpx_o_f32 vcc, v44, v45\n"
    "R_pub%=:\n"
    "v_cvt_pk_bf16_f32 v64, v44, v45\n"
    "v_cvt_pk_bf16_f32 v65, v46, v47\n"
    "global_store_dwordx2 v37, v[64:65], s[52:53] sc1\n"
    "global_store_dwordx2 v38, v[64:65], s[52:53] sc1\n"
    "s_memtime s[58:59]\n"
    "s_cmp_eq_u64 exec, -1\n"
    "s_mov_b64 exec, -1\n"
    "s_cbranch_scc0 R_miss%=\n"
    "s_waitcnt lgkmcnt(0)\n"
    "s_cmp_eq_u32 s45, 0\n"
    "s_cbranch_scc1 R_nomiss%=\n"
    "s_add_u32 s48, s48, 0x10000\n"
    "s_andn2_b32 s48, s48, 0x40000\n"
    "s_add_u32 s70, s41, 1\n"
    "s_add_u32 s70, s70, s38\n"
    "v_mov_b32 v66, s70\n"
    "s_mov_b64 exec, 15\n"
    "global_store_dword v31, v66, s[48:49] offset:16 sc1\n"
    "s_mov_b64 exec, -1\n"
    "R_nomiss%=:\n"
    "s_add_u32 s58, s58, 600\n"
    "R_w3%=:\n"
    "s_memtime s[56:57]\n"
    "s_waitcnt lgkmcnt(0)\n"
    "s_sub_u32 s70, s58, s56\n"
    "s_sub_u32 s71, s70, 0\n"
    "s_cmp_lt_u32 s71, 0x2000\n"
    "s_cbranch_scc1 R_w3%=\n"
    "global_store_dwordx2 v37, v[40:41], s[54:55] sc1\n"
    "global_store_dwordx2 v38, v[40:41], s[54:55] sc1\n"
    "global_store_dwordx4 v36, v[40:43], s[50:51] offset:0 sc1\n"
    "global_store_dwordx4 v36, v[40:43], s[50:51] offset:1024 sc1\n"
    "global_store_dwordx4 v36, v[40:43], s[50:51] offset:2048 sc1\n"
    "global_store_dwordx4 v36, v[40:43], s[50:51] offset:3072 sc1\n"
    "s_add_u32 s41, s41, 1\n"
    "s_cmp_lt_u32 s41, 63\n"
    "s_cbranch_scc1 R_step%=\n"
    "s_endpgm\n"
    "R_far%=:\n"
    "s_add_u32 s46, s46, 1\n"
    "s_cmp_lt_u32 s46, 16\n"
    "s_cbranch_scc1 R_lookat%=\n"
    "s_branch R_timed%=\n"
    "R_notyet%=:\n"
    "s_add_u32 s47, s47, 1\n"
    "s_and_b32 s70, s47, 0xff\n"
    "s_cbranch_scc1 R_look%=\n"
    "v_mov_b32 v64, 0\n"
    "global_load_dwordx2 v[66:67], v64, s[64:65] sc1\n"
    "s_sub_u32 s70, s38, 1\n"
    "s_mov_b32 s71, s39\n"
    "s_waitcnt vmcnt(0)\n"
    "v_cmp_eq_u64 vcc, s[70:71], v[66:67]\n"
    "s_nop 1\n"
    "s_cbranch_vccnz R_go%=\n"
    "s_cmp_lt_u32 s47, 0x40000\n"
    "s_cbranch_scc1 R_look%=\n"
    "v_mov_b32 v66, s70\n"
    "v_mov_b32 v67, s71\n"
    "global_store_dwordx2 v64, v[66:67], s[64:65] sc1\n"
    "s_branch R_go%=\n"
    "R_miss%=:\n"
    "s_add_u32 s45, s45, 1\n"
    "s_cmp_eq_u32 s45, 1\n"
    "s_cbranch_scc1 R_go%=\n"
    "s_mov_b64 s[72:73], 0\n"
    "v_cmp_eq_u32 vcc, v40, v48\n"
    "s_nop 0\n"
    "s_or_b64 s[72:73], s[72:73], vcc\n"
    "v_cmp_eq_u32 vcc, v40, v49\n"
    "s_nop 0\n"
    "s_or_b64 s[72:73], s[72:73], vcc\n"
    "v_cmp_eq_u32 vcc, v40, v50\n"
    "s_nop 0\n"
    "s_or_b64 s[72:73], s[72:73], vcc\n"
    "v_cmp_eq_u32 vcc, v40, v51\n"
    "s_nop 0\n"
    "s_or_b64 s[72:73], s[72:73], vcc\n"
    "v_cmp_eq_u32 vcc, v40, v52\n"
    "s_nop 0\n"
    "s_or_b64 s[72:73], s[72:73], vcc\n"
    "v_cmp_eq_u32 vcc, v40, v53\n"
    "s_nop 0\n"
    "s_or_b64 s[72:73], s[72:73], vcc\n"
    "v_cmp_eq_u32 vcc, v40, v54\n"
    "s_nop 0\n"
    "s_or_b64 s[72:73], s[72:73], vcc\n"
    "v_cmp_eq_u32 vcc, v40, v55\n"
    "s_nop 0\n"
    "s_or_b64 s[72:73], s[72:73], vcc\n"
    "v_cmp_eq_u32 vcc, v40, v56\n"
    "s_nop 0\n"
    "s_or_b64 s[72:73], s[72:73], vcc\n"
    "v_cmp_eq_u32 vcc, v40, v57\n"
    "s_nop 0\n"
    "s_or_b64 s[72:73], s[72:73], vcc\n"
    "v_cmp_eq_u32 vcc, v40, v58\n"
    "s_nop 0\n"
    "s_or_b64 s[72:73], s[72:73], vcc\n"
    "v_cmp_eq_u32 vcc, v40, v59\n"
    "s_nop 0\n"
    "s_or_b64 s[72:73], s[72:73], vcc\n"
    "v_cmp_eq_u32 vcc, v40, v60\n"
    "s_nop 0\n"
    "s_or_b64 s[72:73], s[72:73], vcc\n"
    "v_cmp_eq_u32 vcc, v40, v61\n"
    "s_nop 0\n"
    "s_or_b64 s[72:73], s[72:73], vcc\n"
    "v_cmp_eq_u32 vcc, v40, v62\n"
    "s_nop 0\n"
    "s_or_b64 s[72:73], s[72:73], vcc\n"
    "v_cmp_eq_u32 vcc, v40, v63\n"
    "s_nop 0\n"
    "s_or_b64 s[72:73], s[72:73], vcc\n"
    "s_cbranch_scc0 R_pub%=\n"
    "s_add_u32 s47, s47, 1\n"
    "s_and_b32 s70, s47, 0xff\n"
    "s_cbranch_scc1 R_look%=\n"
    "v_mov_b32 v64, 0\n"
    "global_load_dwordx2 v[66:67], v64, s[64:65] sc1\n"
    "s_sub_u32 s70, s38, 1\n"
    "s_mov_b32 s71, s39\n"
    "s_waitcnt vmcnt(0)\n"
    "v_cmp_eq_u64 vcc, s[70:71], v[66:67]\n"
    "s_nop 1\n"
    "s_cbranch_vccnz R_pub%=\n"
    "s_cmp_lt_u32 s47, 0x40000\n"
    "s_cbranch_scc1 R_look%=\n"
    "v_mov_b32 v66, s70\n"
    "v_mov_b32 v67, s71\n"
    "global_store_dwordx2 v64, v[66:67], s[64:65] sc1\n"
    "s_branch R_pub%=\n"
    "mk_code_end:\n"
    :: "s"(ws), "s"(uni(T)), "v"((psi ^ pgc(lane & 3) ^ 5u) * 4096 + b * 64), "v"((lane & 3) == 0 ? one : 0u), "v"((lane & 3) == 1 ? one : 0u), "v"((lane & 3) == 2 ? one : 0u), "v"((lane & 3) == 3 ? one : 0u),
       "v"(io), "v"(so), "v"(own + lane * 64), "s"(ws + 224 * MBY + b * 2048)
    : "memory", "vcc", "scc", "s36", "s37", "s38", "s39", "s40", "s62", "s63", "s99", "v31", "v32", "v33", "v34", "v35", "v36", "v37", "v38", "v39", "v67");
  __builtin_unreachable();
}

__global__ void __launch_bounds__(256, 1) rec_kernel(char* ws, const u16* __restrict__ x, const u16* __restrict__ mem0, const u16* __restrict__ Wq,
                                                     const u16* __restrict__ Wk, const u16* __restrict__ Wv, const u16* __restrict__ Wo,
                                                     const u16* __restrict__ w_r, const u16* __restrict__ wg, const u16* __restrict__ wu,
                                                     const u16* __restrict__ wd, u16* out) {
  __shared__ __attribute__((aligned(1024))) char lds[158720];
  const int tid = threadIdx.x, lane = tid & 63, wv = tid >> 6;
#define PADOP
  TL(0);
  u64 ep0;
  int role;
  {
    unsigned xcc; asm volatile("s_getreg_b32 %0, hwreg(HW_REG_XCC_ID)" : "=s"(xcc));
    role = (int)((blockIdx.x & ~7u) | ((xcc + XROT) & 7u));
  }
  if ((role >> 5) == ((role & 4) ? 0 : 6)) {
    unsigned tmp;
    asm volatile("s_getpc_b64 s[40:41]\n"
                 "mk_pc:\n\t"
                 "s_mov_b32 m0, 0x24000\n\t"
                 "v_min_u32 %0, mk_code_end-mk_pc-4, %1\n\t"
                 "global_load_lds_dword %0, s[40:41]" : "=&v"(tmp) : "v"((((blockIdx.x >> 3) & 3) * 256 + tid) * 128) : "s40", "s41", "memory");
  }
  const int wave = __builtin_amdgcn_readfirstlane(wv);
  unsigned near = 0;
#pragma unroll 1
  for (int pass = 0;; ++pass) {
    role = __builtin_amdgcn_readfirstlane(role);
    const int bx = role & 7, by = role >> 3;
    const bool red = bx < 4 && by >= 16, att = bx >= 4 && by < 8;
    const char* src = red ? (const char*)(x + (size_t)((by - 16) * 4 + bx) * 32 * DM) : att ? (const char*)(Wo + (size_t)((bx & 3) * 8 + by) * DH * DM)
                    : (const char*)((bx < 4 ? (by < 8 ? Wk : Wq) : (by < 16 ? Wk : (by < 24 ? Wv : Wq))) + ((bx & 3) * 8 + (by & 7)) * DH);
    const unsigned sstep = red || att ? 16384u : 64u * HD * 2u, lstep = red ? 1040u : 1024u;
    unsigned vo[4];
#pragma unroll
    for (int i = 0; i < 4; ++i) {
      const int r = wave * 16 + i * 4 + (lane >> 4), f = (r & 3) | ((bx < 4 ? r : r >> 1) & 4);
      vo[i] = red || att ? (wave * 4 + i) * 1024 + lane * 16 : r * (HD * 2) + (((lane & 15) ^ (f << 1)) * 16);
    }
    if (att) __builtin_amdgcn_s_sleep(ADL);
    if (bx >= 4 && by >= 24) __builtin_amdgcn_s_sleep(QDL);
    if (red && (by - 16) * 4 + bx >= 53 && wave == 3) near = probe(ws, (by - 16) * 4 + bx - 53, bx, lane);
    unsigned olo, ohi;
#define FD4 "global_load_lds_dwordx4 %3, s[42:43]\n\ts_add_u32 m0, m0, %10\n\ts_nop 0\n\tglobal_load_lds_dwordx4 %4, s[42:43]\n\ts_add_u32 m0, m0, %10\n\ts_nop 0\n\t" \
            "global_load_lds_dwordx4 %5, s[42:43]\n\ts_add_u32 m0, m0, %10\n\ts_nop 0\n\tglobal_load_lds_dwordx4 %6, s[42:43]\n\t" \
            "s_add_u32 m0, m0, s44\n\ts_add_u32 s42, s42, %9\n\ts_addc_u32 s43, s43, 0\n\t"
    asm volatile("s_load_dwordx2 %0, %11, 0x0\n\ts_mov_b64 s[42:43], %7\n\ts_mov_b32 m0, %8\n\ts_mul_i32 s44, %10, 13\n\ts_mov_b32 s45, %13\n\t"
                 FD4 FD4
                 "s_waitcnt lgkmcnt(0)\n\ts_mov_b64 s[46:47], %0\n\ts_add_u32 s46, s46, 1\n\ts_addc_u32 s47, s47, 0\n\ts_lshl_b64 s[46:47], s[46:47], 6\n\t"
                 "v_mov_b32 v40, s46\n\tv_mov_b32 v41, s47\n\tv_mov_b32 v42, 0\n\t"
                 "s_cmp_eq_u32 %14, 0\n\ts_cbranch_scc0 2f\n\t"
                 "s_mov_b64 exec, 1\n\tglobal_atomic_swap_x2 v[40:41], v42, v[40:41], %12 sc0\n\ts_mov_b64 exec, -1\n"
                 "2:\n\ts_cmp_eq_u32 s45, 0\n\ts_cbranch_scc0 1f\n\ts_waitcnt vmcnt(0)\n\ts_branch 3f\n"
                 "1:\n\t" FD4 "s_sub_u32 s45, s45, 1\n\ts_cmp_lg_u32 s45, 0\n\ts_cbranch_scc1 1b\n\ts_waitcnt vmcnt(24)\n"
                 "3:\n\tv_mov_b32 %1, v40\n\tv_mov_b32 %2, v41"
                 : "=&s"(ep0), "=v"(olo), "=v"(ohi)
                 : "v"(vo[0]), "v"(vo[1]), "v"(vo[2]), "v"(vo[3]), "s"(uni((u64)src)), "s"(wave * 4 * lstep), "s"(sstep), "s"(lstep), "s"(ws + OFF_EP), "s"(ws + OFF_CLM + role * 128),
                   "s"(red ? 0 : (att ? ATN : 6)), "s"(wave + pass)
                 : "memory", "scc", "s42", "s43", "s44", "s45", "s46", "s47", "v40", "v41", "v42");
    ep0 = uni(ep0);
    u64 old = ((u64)ohi << 32) | olo;
    int got = role;
    if (tid == 0 && pass == 0) {
      const u64 T0 = tag_of(ep0);
      while (old == T0) { got = (got + 1) & 255; old = __hip_atomic_exchange((gu64*)(ws + OFF_CLM + got * 128), T0, RLX_AGENT); }
      *(int*)(lds + 158704) = got;
    }
    asm volatile("s_waitcnt lgkmcnt(0)\n\ts_barrier" ::: "memory");
    got = __builtin_amdgcn_readfirstlane(*(int*)(lds + 158704));
    if (got == role) break;
    asm volatile("s_waitcnt vmcnt(0)\n\ts_barrier" ::: "memory");
    role = got;
  }
  const int bx = role & 7, by = role >> 3;
  if (bx >= 4 || by >= 16) {
    const int xc = bx & 3;
    if (bx < 4) {
      const int i = (by - 16) * 4 + bx;
      if (i >= 53 && tid == 192) set_flag(ws, OFF_PAR + (i - 53) * 128, tag_of(ep0), near);
      const u64 T = tag_of(ep0);
      if (i < 53) ln1_phase(ws, i, T, lds, tid);
      const int p = lane >> 4, q = lane & 3;
      unsigned lo = SENT, hi = SENT;
      if (wv == 0) {
        u16 el[4];
#pragma unroll
        for (int k = 0; k < 4; ++k) {
          u16 t = mem0[((i & 1) * 4 + p) * DM + (i >> 1) * 16 + k * 4 + q];
          if ((t & 0x7f80) == 0x7f80 && (t & 0x7f)) t |= 0x40;
          el[k] = t;
        }
        lo = el[0] | ((unsigned)el[1] << 16); hi = el[2] | ((unsigned)el[3] << 16);
      }
      const unsigned par = wait_par(ws, T, lane);
      gu64* gp = (gu64*)(ws + OFF_G3 + wv * 131072 + (par ^ pgc((lane >> 2) & 3) ^ 1u) * 4096 + (i >> 5) * 32768 + (i & 31) * 128 + ((p & 1) + (q & 1) * 2 + (p & 2) * 2) * 16 + (q & 2) * 4);
      __hip_atomic_store(gp, ((u64)hi << 32) | lo, RLX_AGENT);
      __hip_atomic_store(gp + 8192, ((u64)hi << 32) | lo, RLX_AGENT);
#pragma unroll
      for (int k = 0; k < 2; ++k)
        store16_sc1(ws + OFF_G2, (unsigned)(k * 32768 + (i >> 2) * 65536 + (par ^ pgc(i & 3) ^ ((i >> 5) << 2)) * 4096 + tid * 16), (u32x4){SENT, SENT, SENT, SENT});
      asm volatile("s_waitcnt vmcnt(0)" ::: "memory");
      __syncthreads();
      if (tid == 0) set_flag(ws, OFF_TAB + i * 128, T, 1u + par);
#pragma unroll
      for (int k = 0; k < NKP; ++k)
        store16_sc1(ws + OFF_KM, (unsigned)(blk(52 + 2 * k + (i >> 5), i & 31, (~par & 2u) << 12) + (tid >> 7) * 4096 + (tid & 127) * 16), (u32x4){SENT, SENT, SENT, SENT});
      if (i >= 53) ln1_phase(ws, i, T, lds, tid);
      asm volatile("s_waitcnt vmcnt(0)" ::: "memory");
      __syncthreads();
      if (wv == 0) red_phase(ws, i, T, par, lane);
    } else if (by < 8) {
      {
        const unsigned o = (((xc * 8 + by) * 4 + wv) * 64 + lane) * 128;
        __builtin_amdgcn_s_sleep(WDL);
        asm volatile("s_mov_b32 m0, 0x24000\n\ts_nop 0\n\tglobal_load_lds_dword %0, %2\n\t"
                     "global_load_lds_dword %0, %4\n\tglobal_load_lds_dword %7, %4\n\tglobal_load_lds_dword %0, %5\n\tglobal_load_lds_dword %7, %5\n\t"
                     "global_load_lds_dword %0, %6\n\tglobal_load_lds_dword %7, %6\n\t"
                     :: "v"(o), "v"(o + 1048576), "s"(ws + OFF_FLAGS), "s"(out), "s"(wg), "s"(wu), "s"(wd), "v"(min(o + 1048576, (unsigned)(DM * FF * 2 - 128))) : "memory");
      }
      xa_phase(ws, by & 3, (xc * 2 + (by >> 2)) * 4 + wv, tag_of(ep0), (~wait_par(ws, tag_of(ep0), lane) & 2u) << 12, lane);
    } else {
      const int jj = by - 8, h = xc * 8 + (jj & 7), m = (jj & 7) * 4 + xc;
      u64 T;
      if (jj < 8) T = qkv_phase<1>(ws, h, ep0, tid);
      else if (jj < 16) T = qkv_phase<2>(ws, h, ep0, tid);
      else T = qkv_phase<0>(ws, h, ep0, tid);
      TL(2);
      const unsigned far = (~wait_par(ws, T, lane) & 2u) << 12;
      op_phase(ws, x, w_r, out, Wo, jj >> 2, xc, jj & 3, T, far, lds, tid);
      if (jj >= 16) __syncthreads();
      if (jj < 16) {}
      else if (m < 14) mlp1_phase(ws, x, wg, wu, m, T, far, lds, tid);
      else if (m >= 16) {
        mlp2_phase(ws, x, wd, out, m - 16, T, far, lds, tid);
        if (m == 31 && tid == 0) __hip_atomic_store((gu64*)(ws + OFF_EP), T >> 6, RLX_AGENT);
      }
    }
    TL(5);
    return;
  }
  const unsigned ldsb = (unsigned)(size_t)(LAS char*)lds;
  asm volatile(
    "s_mov_b64 s[36:37], %0\n"
    "s_mov_b32 s38, %1\n"
    "s_mov_b32 s39, %2\n"
    "s_mov_b32 s40, %3\n"
    "s_load_dwordx2 s[96:97], %4, 0x0\n"
    "s_mov_b64 s[22:23], %5\n"
    "s_mov_b64 s[24:25], %6\n"
    "s_mov_b64 s[26:27], %7\n"
    "s_mov_b64 s[28:29], %8\n"
    "s_mov_b64 exec, -1\n"
    "s_mov_b32 s90, 0\n"
    "s_getreg_b32 s10, hwreg(HW_REG_XCC_ID)\n"
    "s_and_b32 s41, s38, 3\n"
    "s_lshl_b32 s41, s41, 3\n"
    "s_bfe_u32 s42, s38, 0x30002\n"
    "s_add_u32 s41, s41, s42\n"
    "s_lshr_b32 s42, s38, 5\n"
    "v_mbcnt_lo_u32_b32 v0, -1, 0\n"
    "v_mbcnt_hi_u32_b32 v0, -1, v0\n"
    "v_and_b32 v1, 15, v0\n"
    "v_lshrrev_b32 v2, 4, v0\n"
    "v_lshlrev_b32 v71, 4, v0\n"
    "s_and_b32 s11, s10, 7\n"
    "s_add_u32 s68, s36, 0x200200\n"
    "s_addc_u32 s69, s37, 0\n"
    "s_lshl_b32 s78, s39, 4\n"
    "v_add_u32 v8, s78, v2\n"
    "v_lshlrev_b32 v12, 1, v2\n"
    "v_xor_b32 v12, v12, v1\n"
    "v_and_b32 v9, 1, v2\n"
    "v_lshlrev_b32 v9, 1, v9\n"
    "v_xor_b32 v9, v9, v1\n"
    "s_and_b32 s79, s39, 1\n"
    "s_lshl_b32 s79, s79, 3\n"
    "v_add_u32 v10, 0, v8\n"
    "v_xor_b32 v11, 0, v12\n"
    "v_lshlrev_b32 v11, 4, v11\n"
    "v_lshl_add_u32 v84, v10, 13, v11\n"
    "v_xor_b32 v11, 0, v9\n"
    "v_xor_b32 v11, s79, v11\n"
    "v_lshlrev_b32 v11, 4, v11\n"
    "v_lshl_add_u32 v88, v10, 10, v11\n"
    "v_add_u32 v10, 4, v8\n"
    "v_xor_b32 v11, 8, v12\n"
    "v_lshlrev_b32 v11, 4, v11\n"
    "v_lshl_add_u32 v85, v10, 13, v11\n"
    "v_xor_b32 v11, 4, v9\n"
    "v_xor_b32 v11, s79, v11\n"
    "v_lshlrev_b32 v11, 4, v11\n"
    "v_lshl_add_u32 v89, v10, 10, v11\n"
    "v_add_u32 v10, 8, v8\n"
    "v_xor_b32 v11, 0, v12\n"
    "v_lshlrev_b32 v11, 4, v11\n"
    "v_lshl_add_u32 v86, v10, 13, v11\n"
    "v_xor_b32 v11, 0, v9\n"
    "v_xor_b32 v11, s79, v11\n"
    "v_lshlrev_b32 v11, 4, v11\n"
    "v_lshl_add_u32 v90, v10, 10, v11\n"
    "v_add_u32 v10, 12, v8\n"
    "v_xor_b32 v11, 8, v12\n"
    "v_lshlrev_b32 v11, 4, v11\n"
    "v_lshl_add_u32 v87, v10, 13, v11\n"
    "v_xor_b32 v11, 4, v9\n"
    "v_xor_b32 v11, s79, v11\n"
    "v_lshlrev_b32 v11, 4, v11\n"
    "v_lshl_add_u32 v91, v10, 10, v11\n"
    "s_lshl_b32 s78, s41, 8\n"
    "s_add_u32 s22, s22, s78\n"
    "s_addc_u32 s23, s23, 0\n"
    "s_add_u32 s24, s24, s78\n"
    "s_addc_u32 s25, s25, 0\n"
    "s_add_u32 s26, s26, s78\n"
    "s_addc_u32 s27, s27, 0\n"
    "s_lshl_b32 s78, s41, 17\n"
    "s_lshl_b32 s79, s42, 9\n"
    "s_add_u32 s78, s78, s79\n"
    "s_add_u32 s28, s28, s78\n"
    "s_addc_u32 s29, s29, 0\n"
    "s_lshl_b32 s30, s39, 12\n"
    "s_add_u32 s30, s30, s40\n"
    "v_lshrrev_b32 v8, 2, v1\n"
    "v_and_b32 v9, 1, v8\n"
    "v_lshrrev_b32 v10, 1, v8\n"
    "v_and_b32 v11, 1, v2\n"
    "v_and_b32 v12, 3, v1\n"
    "v_lshl_add_u32 v14, v11, 1, v10\n"
    "v_lshl_add_u32 v14, v9, 2, v14\n"
    "v_lshrrev_b32 v13, 1, v2\n"
    "v_lshl_add_u32 v13, v13, 4, v14\n"
    "v_lshlrev_b32 v13, 8, v13\n"
    "v_add_u32 v13, s40, v13\n"
    "v_lshlrev_b32 v14, 5, v14\n"
    "s_lshl_b32 s78, s39, 6\n"
    "v_lshl_add_u32 v15, v12, 3, s78\n"
    "v_xor_b32 v15, v15, v14\n"
    "v_add_u32 v96, v15, v13\n"
    "v_xor_b32 v15, 32, v15\n"
    "v_add_u32 v97, v15, v13\n"
    "s_lshr_b32 s78, s39, 1\n"
    "s_lshl_b32 s78, s78, 7\n"
    "s_and_b32 s79, s39, 1\n"
    "s_lshl_b32 s79, s79, 4\n"
    "s_add_u32 s78, s78, s79\n"
    "v_and_b32 v15, 1, v12\n"
    "v_lshl_add_u32 v15, v15, 5, s78\n"
    "v_lshrrev_b32 v16, 1, v12\n"
    "v_lshl_add_u32 v15, v16, 3, v15\n"
    "v_xor_b32 v15, v15, v14\n"
    "v_add_u32 v92, v15, v13\n"
    "v_xor_b32 v15, 64, v15\n"
    "v_add_u32 v93, v15, v13\n"
    "v_add_u32 v98, 0x10000, v96\n"
    "v_add_u32 v99, 0x10000, v97\n"
    "v_add_u32 v94, 0x10000, v92\n"
    "v_add_u32 v95, 0x10000, v93\n"
    "v_lshl_add_u32 v13, v2, 2, v9\n"
    "v_lshl_add_u32 v13, v10, 4, v13\n"
    "v_lshl_add_u32 v14, v11, 1, v9\n"
    "v_lshl_add_u32 v14, v10, 2, v14\n"
    "v_xor_b32 v14, v14, v12\n"
    "v_lshlrev_b32 v14, 1, v14\n"
    "s_and_b32 s78, s39, 1\n"
    "s_lshl_b32 s78, s78, 3\n"
    "v_xor_b32 v14, s78, v14\n"
    "v_lshlrev_b32 v14, 4, v14\n"
    "v_lshl_add_u32 v14, v13, 8, v14\n"
    "s_lshr_b32 s78, s39, 1\n"
    "s_lshl_b32 s78, s78, 14\n"
    "s_add_u32 s78, s78, 0x10000\n"
    "v_add_u32 v104, s78, v14\n"
    "s_mul_i32 s78, s39, 34816\n"
    "s_add_u32 s78, s78, 18688\n"
    "v_add_u32 v116, s78, v71\n"
    "s_cmp_lg_u32 s42, 0\n"
    "s_cbranch_scc1 L_rb0%=\n"
    "ds_read_b64_tr_b16 a[0:1], v92 offset:0\n"
    "ds_read_b64_tr_b16 a[2:3], v92 offset:2048\n"
    "ds_read_b64_tr_b16 a[4:5], v92 offset:8192\n"
    "ds_read_b64_tr_b16 a[6:7], v92 offset:10240\n"
    "ds_read_b64_tr_b16 a[64:65], v93 offset:0\n"
    "ds_read_b64_tr_b16 a[66:67], v93 offset:2048\n"
    "ds_read_b64_tr_b16 a[68:69], v93 offset:8192\n"
    "ds_read_b64_tr_b16 a[70:71], v93 offset:10240\n"
    "s_waitcnt vmcnt(24) lgkmcnt(0)\n"
    "s_barrier\n"
    "s_add_u32 s32, s26, 0x0\n"
    "s_addc_u32 s33, s27, 0\n"
    "s_add_u32 m0, s30, 0\n"
    "s_nop 0\n"
    "global_load_lds_dwordx4 v84, s[32:33]\n"
    "s_add_u32 m0, s30, 1024\n"
    "s_nop 0\n"
    "global_load_lds_dwordx4 v85, s[32:33]\n"
    "s_add_u32 m0, s30, 2048\n"
    "s_nop 0\n"
    "global_load_lds_dwordx4 v86, s[32:33]\n"
    "s_add_u32 m0, s30, 3072\n"
    "s_nop 0\n"
    "global_load_lds_dwordx4 v87, s[32:33]\n"
    "ds_read_b64_tr_b16 a[8:9], v92 offset:16384\n"
    "ds_read_b64_tr_b16 a[10:11], v92 offset:18432\n"
    "ds_read_b64_tr_b16 a[12:13], v92 offset:24576\n"
    "ds_read_b64_tr_b16 a[14:15], v92 offset:26624\n"
    "ds_read_b64_tr_b16 a[72:73], v93 offset:16384\n"
    "ds_read_b64_tr_b16 a[74:75], v93 offset:18432\n"
    "ds_read_b64_tr_b16 a[76:77], v93 offset:24576\n"
    "ds_read_b64_tr_b16 a[78:79], v93 offset:26624\n"
    "s_waitcnt vmcnt(24) lgkmcnt(0)\n"
    "s_barrier\n"
    "s_add_u32 s32, s26, 0x80000\n"
    "s_addc_u32 s33, s27, 0\n"
    "s_add_u32 m0, s30, 16384\n"
    "s_nop 0\n"
    "global_load_lds_dwordx4 v84, s[32:33]\n"
    "s_add_u32 m0, s30, 17408\n"
    "s_nop 0\n"
    "global_load_lds_dwordx4 v85, s[32:33]\n"
    "s_add_u32 m0, s30, 18432\n"
    "s_nop 0\n"
    "global_load_lds_dwordx4 v86, s[32:33]\n"
    "s_add_u32 m0, s30, 19456\n"
    "s_nop 0\n"
    "global_load_lds_dwordx4 v87, s[32:33]\n"
    "ds_read_b64_tr_b16 a[16:17], v92 offset:32768\n"
    "ds_read_b64_tr_b16 a[18:19], v92 offset:34816\n"
    "ds_read_b64_tr_b16 a[20:21], v92 offset:40960\n"
    "ds_read_b64_tr_b16 a[22:23], v92 offset:43008\n"
    "ds_read_b64_tr_b16 a[80:81], v93 offset:32768\n"
    "ds_read_b64_tr_b16 a[82:83], v93 offset:34816\n"
    "ds_read_b64_tr_b16 a[84:85], v93 offset:40960\n"
    "ds_read_b64_tr_b16 a[86:87], v93 offset:43008\n"
    "s_waitcnt vmcnt(24) lgkmcnt(0)\n"
    "s_barrier\n"
    "s_add_u32 s32, s26, 0x100000\n"
    "s_addc_u32 s33, s27, 0\n"
    "s_add_u32 m0, s30, 32768\n"
    "s_nop 0\n"
    "global_load_lds_dwordx4 v84, s[32:33]\n"
    "s_add_u32 m0, s30, 33792\n"
    "s_nop 0\n"
    "global_load_lds_dwordx4 v85, s[32:33]\n"
    "s_add_u32 m0, s30, 34816\n"
    "s_nop 0\n"
    "global_load_lds_dwordx4 v86, s[32:33]\n"
    "s_add_u32 m0, s30, 35840\n"
    "s_nop 0\n"
    "global_load_lds_dwordx4 v87, s[32:33]\n"
    "ds_read_b64_tr_b16 a[24:25], v92 offset:49152\n"
    "ds_read_b64_tr_b16 a[26:27], v92 offset:51200\n"
    "ds_read_b64_tr_b16 a[28:29], v92 offset:57344\n"
    "ds_read_b64_tr_b16 a[30:31], v92 offset:59392\n"
    "ds_read_b64_tr_b16 a[88:89], v93 offset:49152\n"
    "ds_read_b64_tr_b16 a[90:91], v93 offset:51200\n"
    "ds_read_b64_tr_b16 a[92:93], v93 offset:57344\n"
    "ds_read_b64_tr_b16 a[94:95], v93 offset:59392\n"
    "s_waitcnt vmcnt(24) lgkmcnt(0)\n"
    "s_barrier\n"
    "s_add_u32 s32, s26, 0x180000\n"
    "s_addc_u32 s33, s27, 0\n"
    "s_add_u32 m0, s30, 49152\n"
    "s_nop 0\n"
    "global_load_lds_dwordx4 v84, s[32:33]\n"
    "s_add_u32 m0, s30, 50176\n"
    "s_nop 0\n"
    "global_load_lds_dwordx4 v85, s[32:33]\n"
    "s_add_u32 m0, s30, 51200\n"
    "s_nop 0\n"
    "global_load_lds_dwordx4 v86, s[32:33]\n"
    "s_add_u32 m0, s30, 52224\n"
    "s_nop 0\n"
    "global_load_lds_dwordx4 v87, s[32:33]\n"
    "ds_read_b64_tr_b16 a[32:33], v94 offset:0\n"
    "ds_read_b64_tr_b16 a[34:35], v94 offset:2048\n"
    "ds_read_b64_tr_b16 a[36:37], v94 offset:8192\n"
    "ds_read_b64_tr_b16 a[38:39], v94 offset:10240\n"
    "ds_read_b64_tr_b16 a[96:97], v95 offset:0\n"
    "ds_read_b64_tr_b16 a[98:99], v95 offset:2048\n"
    "ds_read_b64_tr_b16 a[100:101], v95 offset:8192\n"
    "ds_read_b64_tr_b16 a[102:103], v95 offset:10240\n"
    "s_waitcnt vmcnt(24) lgkmcnt(0)\n"
    "s_barrier\n"
    "s_add_u32 s32, s28, 0x0\n"
    "s_addc_u32 s33, s29, 0\n"
    "s_add_u32 m0, s30, 65536\n"
    "s_nop 0\n"
    "global_load_lds_dwordx4 v88, s[32:33]\n"
    "s_add_u32 m0, s30, 66560\n"
    "s_nop 0\n"
    "global_load_lds_dwordx4 v89, s[32:33]\n"
    "s_add_u32 m0, s30, 67584\n"
    "s_nop 0\n"
    "global_load_lds_dwordx4 v90, s[32:33]\n"
    "s_add_u32 m0, s30, 68608\n"
    "s_nop 0\n"
    "global_load_lds_dwordx4 v91, s[32:33]\n"
    "ds_read_b64_tr_b16 a[40:41], v94 offset:16384\n"
    "ds_read_b64_tr_b16 a[42:43], v94 offset:18432\n"
    "ds_read_b64_tr_b16 a[44:45], v94 offset:24576\n"
    "ds_read_b64_tr_b16 a[46:47], v94 offset:26624\n"
    "ds_read_b64_tr_b16 a[104:105], v95 offset:16384\n"
    "ds_read_b64_tr_b16 a[106:107], v95 offset:18432\n"
    "ds_read_b64_tr_b16 a[108:109], v95 offset:24576\n"
    "ds_read_b64_tr_b16 a[110:111], v95 offset:26624\n"
    "s_waitcnt vmcnt(24) lgkmcnt(0)\n"
    "s_barrier\n"
    "s_add_u32 s32, s28, 0x100\n"
    "s_addc_u32 s33, s29, 0\n"
    "s_add_u32 m0, s30, 81920\n"
    "s_nop 0\n"
    "global_load_lds_dwordx4 v88, s[32:33]\n"
    "s_add_u32 m0, s30, 82944\n"
    "s_nop 0\n"
    "global_load_lds_dwordx4 v89, s[32:33]\n"
    "s_add_u32 m0, s30, 83968\n"
    "s_nop 0\n"
    "global_load_lds_dwordx4 v90, s[32:33]\n"
    "s_add_u32 m0, s30, 84992\n"
    "s_nop 0\n"
    "global_load_lds_dwordx4 v91, s[32:33]\n"
    "ds_read_b64_tr_b16 a[48:49], v94 offset:32768\n"
    "ds_read_b64_tr_b16 a[50:51], v94 offset:34816\n"
    "ds_read_b64_tr_b16 a[52:53], v94 offset:40960\n"
    "ds_read_b64_tr_b16 a[54:55], v94 offset:43008\n"
    "ds_read_b64_tr_b16 a[112:113], v95 offset:32768\n"
    "ds_read_b64_tr_b16 a[114:115], v95 offset:34816\n"
    "ds_read_b64_tr_b16 a[116:117], v95 offset:40960\n"
    "ds_read_b64_tr_b16 a[118:119], v95 offset:43008\n"
    "s_waitcnt vmcnt(24) lgkmcnt(0)\n"
    "s_barrier\n"
    "s_add_u32 s32, s28, 0x10000\n"
    "s_addc_u32 s33, s29, 0\n"
    "s_add_u32 m0, s30, 98304\n"
    "s_nop 0\n"
    "global_load_lds_dwordx4 v88, s[32:33]\n"
    "s_add_u32 m0, s30, 99328\n"
    "s_nop 0\n"
    "global_load_lds_dwordx4 v89, s[32:33]\n"
    "s_add_u32 m0, s30, 100352\n"
    "s_nop 0\n"
    "global_load_lds_dwordx4 v90, s[32:33]\n"
    "s_add_u32 m0, s30, 101376\n"
    "s_nop 0\n"
    "global_load_lds_dwordx4 v91, s[32:33]\n"
    "ds_read_b64_tr_b16 a[56:57], v94 offset:49152\n"
    "ds_read_b64_tr_b16 a[58:59], v94 offset:51200\n"
    "ds_read_b64_tr_b16 a[60:61], v94 offset:57344\n"
    "ds_read_b64_tr_b16 a[62:63], v94 offset:59392\n"
    "ds_read_b64_tr_b16 a[120:121], v95 offset:49152\n"
    "ds_read_b64_tr_b16 a[122:123], v95 offset:51200\n"
    "ds_read_b64_tr_b16 a[124:125], v95 offset:57344\n"
    "ds_read_b64_tr_b16 a[126:127], v95 offset:59392\n"
    "s_branch L_rc0%=\n"
    "L_rb0%=:\n"
    "ds_read_b64_tr_b16 v[128:129], v92 offset:0\n"
    "ds_read_b64_tr_b16 v[130:131], v92 offset:2048\n"
    "ds_read_b64_tr_b16 v[132:133], v92 offset:8192\n"
    "ds_read_b64_tr_b16 v[134:135], v92 offset:10240\n"
    "ds_read_b64_tr_b16 v[136:137], v93 offset:0\n"
    "ds_read_b64_tr_b16 v[138:139], v93 offset:2048\n"
    "ds_read_b64_tr_b16 v[140:141], v93 offset:8192\n"
    "ds_read_b64_tr_b16 v[142:143], v93 offset:10240\n"
    "s_waitcnt vmcnt(24) lgkmcnt(0)\n"
    "s_barrier\n"
    "s_add_u32 s32, s26, 0x200000\n"
    "s_addc_u32 s33, s27, 0\n"
    "s_add_u32 m0, s30, 0\n"
    "s_nop 0\n"
    "global_load_lds_dwordx4 v84, s[32:33]\n"
    "s_add_u32 m0, s30, 1024\n"
    "s_nop 0\n"
    "global_load_lds_dwordx4 v85, s[32:33]\n"
    "s_add_u32 m0, s30, 2048\n"
    "s_nop 0\n"
    "global_load_lds_dwordx4 v86, s[32:33]\n"
    "s_add_u32 m0, s30, 3072\n"
    "s_nop 0\n"
    "global_load_lds_dwordx4 v87, s[32:33]\n"
    "ds_read_b64_tr_b16 v[144:145], v92 offset:16384\n"
    "ds_read_b64_tr_b16 v[146:147], v92 offset:18432\n"
    "ds_read_b64_tr_b16 v[148:149], v92 offset:24576\n"
    "ds_read_b64_tr_b16 v[150:151], v92 offset:26624\n"
    "ds_read_b64_tr_b16 v[152:153], v93 offset:16384\n"
    "ds_read_b64_tr_b16 v[154:155], v93 offset:18432\n"
    "ds_read_b64_tr_b16 v[156:157], v93 offset:24576\n"
    "ds_read_b64_tr_b16 v[158:159], v93 offset:26624\n"
    "s_waitcnt vmcnt(24) lgkmcnt(0)\n"
    "s_barrier\n"
    "s_add_u32 s32, s26, 0x280000\n"
    "s_addc_u32 s33, s27, 0\n"
    "s_add_u32 m0, s30, 16384\n"
    "s_nop 0\n"
    "global_load_lds_dwordx4 v84, s[32:33]\n"
    "s_add_u32 m0, s30, 17408\n"
    "s_nop 0\n"
    "global_load_lds_dwordx4 v85, s[32:33]\n"
    "s_add_u32 m0, s30, 18432\n"
    "s_nop 0\n"
    "global_load_lds_dwordx4 v86, s[32:33]\n"
    "s_add_u32 m0, s30, 19456\n"
    "s_nop 0\n"
    "global_load_lds_dwordx4 v87, s[32:33]\n"
    "ds_read_b64_tr_b16 v[160:161], v92 offset:32768\n"
    "ds_read_b64_tr_b16 v[162:163], v92 offset:34816\n"
    "ds_read_b64_tr_b16 v[164:165], v92 offset:40960\n"
    "ds_read_b64_tr_b16 v[166:167], v92 offset:43008\n"
    "ds_read_b64_tr_b16 v[168:169], v93 offset:32768\n"
    "ds_read_b64_tr_b16 v[170:171], v93 offset:34816\n"
    "ds_read_b64_tr_b16 v[172:173], v93 offset:40960\n"
    "ds_read_b64_tr_b16 v[174:175], v93 offset:43008\n"
    "s_waitcnt vmcnt(24) lgkmcnt(0)\n"
    "s_barrier\n"
    "s_add_u32 s32, s26, 0x300000\n"
    "s_addc_u32 s33, s27, 0\n"
    "s_add_u32 m0, s30, 32768\n"
    "s_nop 0\n"
    "global_load_lds_dwordx4 v84, s[32:33]\n"
    "s_add_u32 m0, s30, 33792\n"
    "s_nop 0\n"
    "global_load_lds_dwordx4 v85, s[32:33]\n"
    "s_add_u32 m0, s30, 34816\n"
    "s_nop 0\n"
    "global_load_lds_dwordx4 v86, s[32:33]\n"
    "s_add_u32 m0, s30, 35840\n"
    "s_nop 0\n"
    "global_load_lds_dwordx4 v87, s[32:33]\n"
    "ds_read_b64_tr_b16 v[176:177], v92 offset:49152\n"
    "ds_read_b64_tr_b16 v[178:179], v92 offset:51200\n"
    "ds_read_b64_tr_b16 v[180:181], v92 offset:57344\n"
    "ds_read_b64_tr_b16 v[182:183], v92 offset:59392\n"
    "ds_read_b64_tr_b16 v[184:185], v93 offset:49152\n"
    "ds_read_b64_tr_b16 v[186:187], v93 offset:51200\n"
    "ds_read_b64_tr_b16 v[188:189], v93 offset:57344\n"
    "ds_read_b64_tr_b16 v[190:191], v93 offset:59392\n"
    "s_waitcnt vmcnt(24) lgkmcnt(0)\n"
    "s_barrier\n"
    "s_add_u32 s32, s26, 0x380000\n"
    "s_addc_u32 s33, s27, 0\n"
    "s_add_u32 m0, s30, 49152\n"
    "s_nop 0\n"
    "global_load_lds_dwordx4 v84, s[32:33]\n"
    "s_add_u32 m0, s30, 50176\n"
    "s_nop 0\n"
    "global_load_lds_dwordx4 v85, s[32:33]\n"
    "s_add_u32 m0, s30, 51200\n"
    "s_nop 0\n"
    "global_load_lds_dwordx4 v86, s[32:33]\n"
    "s_add_u32 m0, s30, 52224\n"
    "s_nop 0\n"
    "global_load_lds_dwordx4 v87, s[32:33]\n"
    "ds_read_b64_tr_b16 v[192:193], v94 offset:0\n"
    "ds_read_b64_tr_b16 v[194:195], v94 offset:2048\n"
    "ds_read_b64_tr_b16 v[196:197], v94 offset:8192\n"
    "ds_read_b64_tr_b16 v[198:199], v94 offset:10240\n"
    "ds_read_b64_tr_b16 v[200:201], v95 offset:0\n"
    "ds_read_b64_tr_b16 v[202:203], v95 offset:2048\n"
    "ds_read_b64_tr_b16 v[204:205], v95 offset:8192\n"
    "ds_read_b64_tr_b16 v[206:207], v95 offset:10240\n"
    "s_waitcnt vmcnt(24) lgkmcnt(0)\n"
    "s_barrier\n"
    "s_add_u32 s32, s28, 0x0\n"
    "s_addc_u32 s33, s29, 0\n"
    "s_add_u32 m0, s30, 65536\n"
    "s_nop 0\n"
    "global_load_lds_dwordx4 v88, s[32:33]\n"
    "s_add_u32 m0, s30, 66560\n"
    "s_nop 0\n"
    "global_load_lds_dwordx4 v89, s[32:33]\n"
    "s_add_u32 m0, s30, 67584\n"
    "s_nop 0\n"
    "global_load_lds_dwordx4 v90, s[32:33]\n"
    "s_add_u32 m0, s30, 68608\n"
    "s_nop 0\n"
    "global_load_lds_dwordx4 v91, s[32:33]\n"
    "ds_read_b64_tr_b16 v[208:209], v94 offset:16384\n"
    "ds_read_b64_tr_b16 v[210:211], v94 offset:18432\n"
    "ds_read_b64_tr_b16 v[212:213], v94 offset:24576\n"
    "ds_read_b64_tr_b16 v[214:215], v94 offset:26624\n"
    "ds_read_b64_tr_b16 v[216:217], v95 offset:16384\n"
    "ds_read_b64_tr_b16 v[218:219], v95 offset:18432\n"
    "ds_read_b64_tr_b16 v[220:221], v95 offset:24576\n"
    "ds_read_b64_tr_b16 v[222:223], v95 offset:26624\n"
    "s_waitcnt vmcnt(24) lgkmcnt(0)\n"
    "s_barrier\n"
    "s_add_u32 s32, s28, 0x100\n"
    "s_addc_u32 s33, s29, 0\n"
    "s_add_u32 m0, s30, 81920\n"
    "s_nop 0\n"
    "global_load_lds_dwordx4 v88, s[32:33]\n"
    "s_add_u32 m0, s30, 82944\n"
    "s_nop 0\n"
    "global_load_lds_dwordx4 v89, s[32:33]\n"
    "s_add_u32 m0, s30, 83968\n"
    "s_nop 0\n"
    "global_load_lds_dwordx4 v90, s[32:33]\n"
    "s_add_u32 m0, s30, 84992\n"
    "s_nop 0\n"
    "global_load_lds_dwordx4 v91, s[32:33]\n"
    "ds_read_b64_tr_b16 v[224:225], v94 offset:32768\n"
    "ds_read_b64_tr_b16 v[226:227], v94 offset:34816\n"
    "ds_read_b64_tr_b16 v[228:229], v94 offset:40960\n"
    "ds_read_b64_tr_b16 v[230:231], v94 offset:43008\n"
    "ds_read_b64_tr_b16 v[232:233], v95 offset:32768\n"
    "ds_read_b64_tr_b16 v[234:235], v95 offset:34816\n"
    "ds_read_b64_tr_b16 v[236:237], v95 offset:40960\n"
    "ds_read_b64_tr_b16 v[238:239], v95 offset:43008\n"
    "s_waitcnt vmcnt(24) lgkmcnt(0)\n"
    "s_barrier\n"
    "s_add_u32 s32, s28, 0x10000\n"
    "s_addc_u32 s33, s29, 0\n"
    "s_add_u32 m0, s30, 98304\n"
    "s_nop 0\n"
    "global_load_lds_dwordx4 v88, s[32:33]\n"
    "s_add_u32 m0, s30, 99328\n"
    "s_nop 0\n"
    "global_load_lds_dwordx4 v89, s[32:33]\n"
    "s_add_u32 m0, s30, 100352\n"
    "s_nop 0\n"
    "global_load_lds_dwordx4 v90, s[32:33]\n"
    "s_add_u32 m0, s30, 101376\n"
    "s_nop 0\n"
    "global_load_lds_dwordx4 v91, s[32:33]\n"
    "ds_read_b64_tr_b16 v[240:241], v94 offset:49152\n"
    "ds_read_b64_tr_b16 v[242:243], v94 offset:51200\n"
    "ds_read_b64_tr_b16 v[244:245], v94 offset:57344\n"
    "ds_read_b64_tr_b16 v[246:247], v94 offset:59392\n"
    "ds_read_b64_tr_b16 v[248:249], v95 offset:49152\n"
    "ds_read_b64_tr_b16 v[250:251], v95 offset:51200\n"
    "ds_read_b64_tr_b16 v[252:253], v95 offset:57344\n"
    "ds_read_b64_tr_b16 v[254:255], v95 offset:59392\n"
    "L_rc0%=:\n"
    "s_mov_b32 s70, 0xcccccccc\n"
    "s_mov_b32 s71, 0xcccccccc\n"
    "s_mov_b32 s50, 0xffbfffbf\n"
    "s_mov_b32 s72, 0x33333333\n"
    "s_mov_b32 s73, 0x0\n"
    "s_mov_b32 s74, 0x0\n"
    "s_mov_b32 s75, 0x33333333\n"
    "v_mov_b32 v9, 0x4444\n"
    "v_mov_b32 v10, 0xeeee\n"
    "v_cndmask_b32 v67, v9, v10, s[70:71]\n"
    "s_mov_b32 s76, 0x3e0293ee\n"
    "s_mov_b32 s77, 0x3e0293ee\n"
    "v_mov_b32 v80, 0x3f803f80\n"
    "v_mov_b32 v81, 0x3f803f80\n"
    "v_mov_b32 v82, 0x3f803f80\n"
    "v_mov_b32 v83, 0x3f803f80\n"
    "v_lshrrev_b32 v8, 2, v1\n"
    "v_lshlrev_b32 v8, 3, v8\n"
    "v_lshl_add_u32 v8, v2, 8, v8\n"
    "v_and_b32 v9, 1, v2\n"
    "v_lshlrev_b32 v10, 1, v9\n"
    "v_and_b32 v11, 3, v1\n"
    "v_xor_b32 v10, v10, v11\n"
    "v_lshl_add_u32 v13, v10, 5, v8\n"
    "v_lshl_add_u32 v112, v9, 7, v13\n"
    "v_xor_b32 v10, 1, v10\n"
    "v_xor_b32 v9, 1, v9\n"
    "v_lshl_add_u32 v13, v10, 5, v8\n"
    "v_lshl_add_u32 v113, v9, 7, v13\n"
    "v_and_b32 v8, 13, v1\n"
    "v_lshlrev_b32 v8, 4, v8\n"
    "v_lshrrev_b32 v9, 1, v2\n"
    "v_lshl_add_u32 v8, v9, 8, v8\n"
    "v_and_b32 v9, 1, v2\n"
    "v_lshl_add_u32 v8, v9, 3, v8\n"
    "v_add_u32 v9, 1056, v8\n"
    "v_cndmask_b32 v75, v8, v9, s[70:71]\n"
    "v_lshrrev_b32 v8, 2, v1\n"
    "v_lshrrev_b32 v9, 3, v1\n"
    "v_xor_b32 v9, v9, v8\n"
    "v_lshlrev_b32 v9, 7, v9\n"
    "v_and_b32 v10, 3, v1\n"
    "v_lshl_add_u32 v9, v10, 3, v9\n"
    "v_xor_b32 v10, v8, v2\n"
    "v_lshl_add_u32 v114, v10, 5, v9\n"
    "v_and_b32 v10, 1, v2\n"
    "v_lshrrev_b32 v11, 1, v2\n"
    "v_lshl_add_u32 v10, v10, 1, v11\n"
    "v_xor_b32 v10, v8, v10\n"
    "v_lshl_add_u32 v115, v10, 5, v9\n"
    "v_lshrrev_b32 v79, 1, v71\n"
    "s_add_u32 s62, s36, 0x6b82000\n"
    "s_addc_u32 s63, s37, 0\n"
    "s_lshl_b32 s78, s42, 12\n"
    "s_add_u32 s62, s62, s78\n"
    "s_addc_u32 s63, s63, 0\n"
    "s_lshl_b32 s78, s41, 14\n"
    "s_add_u32 s62, s62, s78\n"
    "s_addc_u32 s63, s63, 0\n"
    "s_lshl_b32 s79, s39, 10\n"
    "s_add_u32 s78, s78, s79\n"
    "s_add_u32 s58, s36, 0xc00800\n"
    "s_addc_u32 s59, s37, 0\n"
    "s_add_u32 s58, s58, s78\n"
    "s_addc_u32 s59, s59, 0\n"
    "s_add_u32 s78, s78, s79\n"
    "s_add_u32 s60, s36, 0x2c00000\n"
    "s_addc_u32 s61, s37, 0\n"
    "s_add_u32 s60, s60, s78\n"
    "s_addc_u32 s61, s61, 0\n"
    "s_add_u32 s12, s36, 0x180000\n"
    "s_addc_u32 s13, s37, 0\n"
    "s_add_u32 s66, s36, 0x202000\n"
    "s_addc_u32 s67, s37, 0\n"
    "s_lshl_b32 s78, s41, 7\n"
    "s_add_u32 s66, s66, s78\n"
    "s_addc_u32 s67, s67, 0\n"
    "s_add_u32 s44, s36, 0x0\n"
    "s_addc_u32 s45, s37, 0\n"
    "s_waitcnt lgkmcnt(0)\n"
    "s_add_u32 s96, s96, 1\n"
    "s_addc_u32 s97, s97, 0\n"
    "s_lshl_b64 s[96:97], s[96:97], 6\n"
    "s_cmp_lg_u32 s42, 0\n"
    "s_cbranch_scc1 L_rb1%=\n"
    "s_waitcnt vmcnt(24) lgkmcnt(0)\n"
    "s_barrier\n"
    "s_add_u32 s32, s28, 0x10100\n"
    "s_addc_u32 s33, s29, 0\n"
    "s_add_u32 m0, s30, 114688\n"
    "s_nop 0\n"
    "global_load_lds_dwordx4 v88, s[32:33]\n"
    "s_add_u32 m0, s30, 115712\n"
    "s_nop 0\n"
    "global_load_lds_dwordx4 v89, s[32:33]\n"
    "s_add_u32 m0, s30, 116736\n"
    "s_nop 0\n"
    "global_load_lds_dwordx4 v90, s[32:33]\n"
    "s_add_u32 m0, s30, 117760\n"
    "s_nop 0\n"
    "global_load_lds_dwordx4 v91, s[32:33]\n"
    "ds_read_b64_tr_b16 a[128:129], v96 offset:0\n"
    "ds_read_b64_tr_b16 a[130:131], v96 offset:2048\n"
    "ds_read_b64_tr_b16 a[132:133], v96 offset:8192\n"
    "ds_read_b64_tr_b16 a[134:135], v96 offset:10240\n"
    "ds_read_b64_tr_b16 a[192:193], v97 offset:0\n"
    "ds_read_b64_tr_b16 a[194:195], v97 offset:2048\n"
    "ds_read_b64_tr_b16 a[196:197], v97 offset:8192\n"
    "ds_read_b64_tr_b16 a[198:199], v97 offset:10240\n"
    "s_waitcnt vmcnt(24) lgkmcnt(0)\n"
    "s_barrier\n"
    "s_add_u32 s32, s26, 0x200000\n"
    "s_addc_u32 s33, s27, 0\n"
    "s_add_u32 m0, s30, 0\n"
    "s_nop 0\n"
    "global_load_lds_dwordx4 v84, s[32:33]\n"
    "s_add_u32 m0, s30, 1024\n"
    "s_nop 0\n"
    "global_load_lds_dwordx4 v85, s[32:33]\n"
    "s_add_u32 m0, s30, 2048\n"
    "s_nop 0\n"
    "global_load_lds_dwordx4 v86, s[32:33]\n"
    "s_add_u32 m0, s30, 3072\n"
    "s_nop 0\n"
    "global_load_lds_dwordx4 v87, s[32:33]\n"
    "ds_read_b64_tr_b16 a[136:137], v96 offset:16384\n"
    "ds_read_b64_tr_b16 a[138:139], v96 offset:18432\n"
    "ds_read_b64_tr_b16 a[140:141], v96 offset:24576\n"
    "ds_read_b64_tr_b16 a[142:143], v96 offset:26624\n"
    "ds_read_b64_tr_b16 a[200:201], v97 offset:16384\n"
    "ds_read_b64_tr_b16 a[202:203], v97 offset:18432\n"
    "ds_read_b64_tr_b16 a[204:205], v97 offset:24576\n"
    "ds_read_b64_tr_b16 a[206:207], v97 offset:26624\n"
    "s_waitcnt vmcnt(24) lgkmcnt(0)\n"
    "s_barrier\n"
    "s_add_u32 s32, s26, 0x280000\n"
    "s_addc_u32 s33, s27, 0\n"
    "s_add_u32 m0, s30, 16384\n"
    "s_nop 0\n"
    "global_load_lds_dwordx4 v84, s[32:33]\n"
    "s_add_u32 m0, s30, 17408\n"
    "s_nop 0\n"
    "global_load_lds_dwordx4 v85, s[32:33]\n"
    "s_add_u32 m0, s30, 18432\n"
    "s_nop 0\n"
    "global_load_lds_dwordx4 v86, s[32:33]\n"
    "s_add_u32 m0, s30, 19456\n"
    "s_nop 0\n"
    "global_load_lds_dwordx4 v87, s[32:33]\n"
    "ds_read_b64_tr_b16 a[144:145], v96 offset:32768\n"
    "ds_read_b64_tr_b16 a[146:147], v96 offset:34816\n"
    "ds_read_b64_tr_b16 a[148:149], v96 offset:40960\n"
    "ds_read_b64_tr_b16 a[150:151], v96 offset:43008\n"
    "ds_read_b64_tr_b16 a[208:209], v97 offset:32768\n"
    "ds_read_b64_tr_b16 a[210:211], v97 offset:34816\n"
    "ds_read_b64_tr_b16 a[212:213], v97 offset:40960\n"
    "ds_read_b64_tr_b16 a[214:215], v97 offset:43008\n"
    "s_waitcnt vmcnt(24) lgkmcnt(0)\n"
    "s_barrier\n"
    "s_add_u32 s32, s26, 0x300000\n"
    "s_addc_u32 s33, s27, 0\n"
    "s_add_u32 m0, s30, 32768\n"
    "s_nop 0\n"
    "global_load_lds_dwordx4 v84, s[32:33]\n"
    "s_add_u32 m0, s30, 33792\n"
    "s_nop 0\n"
    "global_load_lds_dwordx4 v85, s[32:33]\n"
    "s_add_u32 m0, s30, 34816\n"
    "s_nop 0\n"
    "global_load_lds_dwordx4 v86, s[32:33]\n"
    "s_add_u32 m0, s30, 35840\n"
    "s_nop 0\n"
    "global_load_lds_dwordx4 v87, s[32:33]\n"
    "ds_read_b64_tr_b16 a[152:153], v96 offset:49152\n"
    "ds_read_b64_tr_b16 a[154:155], v96 offset:51200\n"
    "ds_read_b64_tr_b16 a[156:157], v96 offset:57344\n"
    "ds_read_b64_tr_b16 a[158:159], v96 offset:59392\n"
    "ds_read_b64_tr_b16 a[216:217], v97 offset:49152\n"
    "ds_read_b64_tr_b16 a[218:219], v97 offset:51200\n"
    "ds_read_b64_tr_b16 a[220:221], v97 offset:57344\n"
    "ds_read_b64_tr_b16 a[222:223], v97 offset:59392\n"
    "s_waitcnt vmcnt(24) lgkmcnt(0)\n"
    "s_barrier\n"
    "s_add_u32 s32, s26, 0x380000\n"
    "s_addc_u32 s33, s27, 0\n"
    "s_add_u32 m0, s30, 49152\n"
    "s_nop 0\n"
    "global_load_lds_dwordx4 v84, s[32:33]\n"
    "s_add_u32 m0, s30, 50176\n"
    "s_nop 0\n"
    "global_load_lds_dwordx4 v85, s[32:33]\n"
    "s_add_u32 m0, s30, 51200\n"
    "s_nop 0\n"
    "global_load_lds_dwordx4 v86, s[32:33]\n"
    "s_add_u32 m0, s30, 52224\n"
    "s_nop 0\n"
    "global_load_lds_dwordx4 v87, s[32:33]\n"
    "s_waitcnt vmcnt(24) lgkmcnt(0)\n"
    "s_barrier\n"
    "ds_read_b64_tr_b16 v[0:1], v104 offset:0\n"
    "ds_read_b64_tr_b16 v[2:3], v104 offset:512\n"
    "ds_read_b64_tr_b16 v[4:5], v104 offset:8192\n"
    "ds_read_b64_tr_b16 v[6:7], v104 offset:8704\n"
    "ds_read_b64_tr_b16 v[16:17], v104 offset:8\n"
    "ds_read_b64_tr_b16 v[18:19], v104 offset:520\n"
    "ds_read_b64_tr_b16 v[20:21], v104 offset:8200\n"
    "ds_read_b64_tr_b16 v[22:23], v104 offset:8712\n"
    "s_waitcnt lgkmcnt(4)\n"
    "ds_read_b64_tr_b16 v[32:33], v104 offset:16\n"
    "ds_read_b64_tr_b16 v[34:35], v104 offset:528\n"
    "ds_read_b64_tr_b16 v[36:37], v104 offset:8208\n"
    "ds_read_b64_tr_b16 v[38:39], v104 offset:8720\n"
    "ds_read_b64_tr_b16 v[48:49], v104 offset:24\n"
    "ds_read_b64_tr_b16 v[50:51], v104 offset:536\n"
    "ds_read_b64_tr_b16 v[52:53], v104 offset:8216\n"
    "ds_read_b64_tr_b16 v[54:55], v104 offset:8728\n"
    "s_waitcnt vmcnt(20) lgkmcnt(0)\n"
    "s_barrier\n"
    "s_add_u32 s32, s22, 0x0\n"
    "s_addc_u32 s33, s23, 0\n"
    "s_add_u32 m0, s30, 65536\n"
    "s_nop 0\n"
    "global_load_lds_dwordx4 v84, s[32:33]\n"
    "s_add_u32 m0, s30, 66560\n"
    "s_nop 0\n"
    "global_load_lds_dwordx4 v85, s[32:33]\n"
    "s_add_u32 m0, s30, 67584\n"
    "s_nop 0\n"
    "global_load_lds_dwordx4 v86, s[32:33]\n"
    "s_add_u32 m0, s30, 68608\n"
    "s_nop 0\n"
    "global_load_lds_dwordx4 v87, s[32:33]\n"
    "s_add_u32 s32, s22, 0x80000\n"
    "s_addc_u32 s33, s23, 0\n"
    "s_add_u32 m0, s30, 81920\n"
    "s_nop 0\n"
    "global_load_lds_dwordx4 v84, s[32:33]\n"
    "s_add_u32 m0, s30, 82944\n"
    "s_nop 0\n"
    "global_load_lds_dwordx4 v85, s[32:33]\n"
    "s_add_u32 m0, s30, 83968\n"
    "s_nop 0\n"
    "global_load_lds_dwordx4 v86, s[32:33]\n"
    "s_add_u32 m0, s30, 84992\n"
    "s_nop 0\n"
    "global_load_lds_dwordx4 v87, s[32:33]\n"
    "s_waitcnt vmcnt(24) lgkmcnt(0)\n"
    "s_barrier\n"
    "ds_read_b64_tr_b16 v[8:9], v104 offset:32768\n"
    "ds_read_b64_tr_b16 v[10:11], v104 offset:33280\n"
    "ds_read_b64_tr_b16 v[12:13], v104 offset:40960\n"
    "ds_read_b64_tr_b16 v[14:15], v104 offset:41472\n"
    "ds_read_b64_tr_b16 v[24:25], v104 offset:32776\n"
    "ds_read_b64_tr_b16 v[26:27], v104 offset:33288\n"
    "ds_read_b64_tr_b16 v[28:29], v104 offset:40968\n"
    "ds_read_b64_tr_b16 v[30:31], v104 offset:41480\n"
    "s_waitcnt lgkmcnt(4)\n"
    "ds_read_b64_tr_b16 v[40:41], v104 offset:32784\n"
    "ds_read_b64_tr_b16 v[42:43], v104 offset:33296\n"
    "ds_read_b64_tr_b16 v[44:45], v104 offset:40976\n"
    "ds_read_b64_tr_b16 v[46:47], v104 offset:41488\n"
    "ds_read_b64_tr_b16 v[56:57], v104 offset:32792\n"
    "ds_read_b64_tr_b16 v[58:59], v104 offset:33304\n"
    "ds_read_b64_tr_b16 v[60:61], v104 offset:40984\n"
    "ds_read_b64_tr_b16 v[62:63], v104 offset:41496\n"
    "s_waitcnt vmcnt(20) lgkmcnt(0)\n"
    "s_barrier\n"
    "s_add_u32 s32, s22, 0x100000\n"
    "s_addc_u32 s33, s23, 0\n"
    "s_add_u32 m0, s30, 98304\n"
    "s_nop 0\n"
    "global_load_lds_dwordx4 v84, s[32:33]\n"
    "s_add_u32 m0, s30, 99328\n"
    "s_nop 0\n"
    "global_load_lds_dwordx4 v85, s[32:33]\n"
    "s_add_u32 m0, s30, 100352\n"
    "s_nop 0\n"
    "global_load_lds_dwordx4 v86, s[32:33]\n"
    "s_add_u32 m0, s30, 101376\n"
    "s_nop 0\n"
    "global_load_lds_dwordx4 v87, s[32:33]\n"
    "s_add_u32 s32, s22, 0x180000\n"
    "s_addc_u32 s33, s23, 0\n"
    "s_add_u32 m0, s30, 114688\n"
    "s_nop 0\n"
    "global_load_lds_dwordx4 v84, s[32:33]\n"
    "s_add_u32 m0, s30, 115712\n"
    "s_nop 0\n"
    "global_load_lds_dwordx4 v85, s[32:33]\n"
    "s_add_u32 m0, s30, 116736\n"
    "s_nop 0\n"
    "global_load_lds_dwordx4 v86, s[32:33]\n"
    "s_add_u32 m0, s30, 117760\n"
    "s_nop 0\n"
    "global_load_lds_dwordx4 v87, s[32:33]\n"
    "ds_read_b64_tr_b16 a[160:161], v96 offset:0\n"
    "ds_read_b64_tr_b16 a[162:163], v96 offset:2048\n"
    "ds_read_b64_tr_b16 a[164:165], v96 offset:8192\n"
    "ds_read_b64_tr_b16 a[166:167], v96 offset:10240\n"
    "ds_read_b64_tr_b16 a[224:225], v97 offset:0\n"
    "ds_read_b64_tr_b16 a[226:227], v97 offset:2048\n"
    "ds_read_b64_tr_b16 a[228:229], v97 offset:8192\n"
    "ds_read_b64_tr_b16 a[230:231], v97 offset:10240\n"
    "s_waitcnt vmcnt(24) lgkmcnt(0)\n"
    "s_barrier\n"
    "s_add_u32 s32, s22, 0x200000\n"
    "s_addc_u32 s33, s23, 0\n"
    "s_add_u32 m0, s30, 0\n"
    "s_nop 0\n"
    "global_load_lds_dwordx4 v84, s[32:33]\n"
    "s_add_u32 m0, s30, 1024\n"
    "s_nop 0\n"
    "global_load_lds_dwordx4 v85, s[32:33]\n"
    "s_add_u32 m0, s30, 2048\n"
    "s_nop 0\n"
    "global_load_lds_dwordx4 v86, s[32:33]\n"
    "s_add_u32 m0, s30, 3072\n"
    "s_nop 0\n"
    "global_load_lds_dwordx4 v87, s[32:33]\n"
    "ds_read_b64_tr_b16 a[168:169], v96 offset:16384\n"
    "ds_read_b64_tr_b16 a[170:171], v96 offset:18432\n"
    "ds_read_b64_tr_b16 a[172:173], v96 offset:24576\n"
    "ds_read_b64_tr_b16 a[174:175], v96 offset:26624\n"
    "ds_read_b64_tr_b16 a[232:233], v97 offset:16384\n"
    "ds_read_b64_tr_b16 a[234:235], v97 offset:18432\n"
    "ds_read_b64_tr_b16 a[236:237], v97 offset:24576\n"
    "ds_read_b64_tr_b16 a[238:239], v97 offset:26624\n"
    "s_waitcnt vmcnt(24) lgkmcnt(0)\n"
    "s_barrier\n"
    "s_add_u32 s32, s22, 0x280000\n"
    "s_addc_u32 s33, s23, 0\n"
    "s_add_u32 m0, s30, 16384\n"
    "s_nop 0\n"
    "global_load_lds_dwordx4 v84, s[32:33]\n"
    "s_add_u32 m0, s30, 17408\n"
    "s_nop 0\n"
    "global_load_lds_dwordx4 v85, s[32:33]\n"
    "s_add_u32 m0, s30, 18432\n"
    "s_nop 0\n"
    "global_load_lds_dwordx4 v86, s[32:33]\n"
    "s_add_u32 m0, s30, 19456\n"
    "s_nop 0\n"
    "global_load_lds_dwordx4 v87, s[32:33]\n"
    "ds_read_b64_tr_b16 a[176:177], v96 offset:32768\n"
    "ds_read_b64_tr_b16 a[178:179], v96 offset:34816\n"
    "ds_read_b64_tr_b16 a[180:181], v96 offset:40960\n"
    "ds_read_b64_tr_b16 a[182:183], v96 offset:43008\n"
    "ds_read_b64_tr_b16 a[240:241], v97 offset:32768\n"
    "ds_read_b64_tr_b16 a[242:243], v97 offset:34816\n"
    "ds_read_b64_tr_b16 a[244:245], v97 offset:40960\n"
    "ds_read_b64_tr_b16 a[246:247], v97 offset:43008\n"
    "s_waitcnt vmcnt(24) lgkmcnt(0)\n"
    "s_barrier\n"
    "s_add_u32 s32, s22, 0x300000\n"
    "s_addc_u32 s33, s23, 0\n"
    "s_add_u32 m0, s30, 32768\n"
    "s_nop 0\n"
    "global_load_lds_dwordx4 v84, s[32:33]\n"
    "s_add_u32 m0, s30, 33792\n"
    "s_nop 0\n"
    "global_load_lds_dwordx4 v85, s[32:33]\n"
    "s_add_u32 m0, s30, 34816\n"
    "s_nop 0\n"
    "global_load_lds_dwordx4 v86, s[32:33]\n"
    "s_add_u32 m0, s30, 35840\n"
    "s_nop 0\n"
    "global_load_lds_dwordx4 v87, s[32:33]\n"
    "ds_read_b64_tr_b16 a[184:185], v96 offset:49152\n"
    "ds_read_b64_tr_b16 a[186:187], v96 offset:51200\n"
    "ds_read_b64_tr_b16 a[188:189], v96 offset:57344\n"
    "ds_read_b64_tr_b16 a[190:191], v96 offset:59392\n"
    "ds_read_b64_tr_b16 a[248:249], v97 offset:49152\n"
    "ds_read_b64_tr_b16 a[250:251], v97 offset:51200\n"
    "ds_read_b64_tr_b16 a[252:253], v97 offset:57344\n"
    "ds_read_b64_tr_b16 a[254:255], v97 offset:59392\n"
    "s_waitcnt vmcnt(24) lgkmcnt(0)\n"
    "s_barrier\n"
    "s_add_u32 s32, s22, 0x380000\n"
    "s_addc_u32 s33, s23, 0\n"
    "s_add_u32 m0, s30, 49152\n"
    "s_nop 0\n"
    "global_load_lds_dwordx4 v84, s[32:33]\n"
    "s_add_u32 m0, s30, 50176\n"
    "s_nop 0\n"
    "global_load_lds_dwordx4 v85, s[32:33]\n"
    "s_add_u32 m0, s30, 51200\n"
    "s_nop 0\n"
    "global_load_lds_dwordx4 v86, s[32:33]\n"
    "s_add_u32 m0, s30, 52224\n"
    "s_nop 0\n"
    "global_load_lds_dwordx4 v87, s[32:33]\n"
    "ds_read_b64_tr_b16 v[128:129], v94 offset:0\n"
    "ds_read_b64_tr_b16 v[130:131], v94 offset:2048\n"
    "ds_read_b64_tr_b16 v[132:133], v94 offset:8192\n"
    "ds_read_b64_tr_b16 v[134:135], v94 offset:10240\n"
    "ds_read_b64_tr_b16 v[136:137], v95 offset:0\n"
    "ds_read_b64_tr_b16 v[138:139], v95 offset:2048\n"
    "ds_read_b64_tr_b16 v[140:141], v95 offset:8192\n"
    "ds_read_b64_tr_b16 v[142:143], v95 offset:10240\n"
    "s_waitcnt vmcnt(24) lgkmcnt(0)\n"
    "s_barrier\n"
    "ds_read_b64_tr_b16 v[144:145], v94 offset:16384\n"
    "ds_read_b64_tr_b16 v[146:147], v94 offset:18432\n"
    "ds_read_b64_tr_b16 v[148:149], v94 offset:24576\n"
    "ds_read_b64_tr_b16 v[150:151], v94 offset:26624\n"
    "ds_read_b64_tr_b16 v[152:153], v95 offset:16384\n"
    "ds_read_b64_tr_b16 v[154:155], v95 offset:18432\n"
    "ds_read_b64_tr_b16 v[156:157], v95 offset:24576\n"
    "ds_read_b64_tr_b16 v[158:159], v95 offset:26624\n"
    "s_waitcnt vmcnt(20) lgkmcnt(0)\n"
    "s_barrier\n"
    "ds_read_b64_tr_b16 v[160:161], v94 offset:32768\n"
    "ds_read_b64_tr_b16 v[162:163], v94 offset:34816\n"
    "ds_read_b64_tr_b16 v[164:165], v94 offset:40960\n"
    "ds_read_b64_tr_b16 v[166:167], v94 offset:43008\n"
    "ds_read_b64_tr_b16 v[168:169], v95 offset:32768\n"
    "ds_read_b64_tr_b16 v[170:171], v95 offset:34816\n"
    "ds_read_b64_tr_b16 v[172:173], v95 offset:40960\n"
    "ds_read_b64_tr_b16 v[174:175], v95 offset:43008\n"
    "s_waitcnt vmcnt(16) lgkmcnt(0)\n"
    "s_barrier\n"
    "ds_read_b64_tr_b16 v[176:177], v94 offset:49152\n"
    "ds_read_b64_tr_b16 v[178:179], v94 offset:51200\n"
    "ds_read_b64_tr_b16 v[180:181], v94 offset:57344\n"
    "ds_read_b64_tr_b16 v[182:183], v94 offset:59392\n"
    "ds_read_b64_tr_b16 v[184:185], v95 offset:49152\n"
    "ds_read_b64_tr_b16 v[186:187], v95 offset:51200\n"
    "ds_read_b64_tr_b16 v[188:189], v95 offset:57344\n"
    "ds_read_b64_tr_b16 v[190:191], v95 offset:59392\n"
    "s_waitcnt vmcnt(12) lgkmcnt(0)\n"
    "s_barrier\n"
    "ds_read_b64_tr_b16 v[192:193], v92 offset:0\n"
    "ds_read_b64_tr_b16 v[194:195], v92 offset:2048\n"
    "ds_read_b64_tr_b16 v[196:197], v92 offset:8192\n"
    "ds_read_b64_tr_b16 v[198:199], v92 offset:10240\n"
    "ds_read_b64_tr_b16 v[200:201], v93 offset:0\n"
    "ds_read_b64_tr_b16 v[202:203], v93 offset:2048\n"
    "ds_read_b64_tr_b16 v[204:205], v93 offset:8192\n"
    "ds_read_b64_tr_b16 v[206:207], v93 offset:10240\n"
    "s_waitcnt vmcnt(8) lgkmcnt(0)\n"
    "s_barrier\n"
    "v_lshlrev_b32 v100, 3, v71\n"
    "s_add_u32 s82, s36, 0x20c000\n"
    "s_addc_u32 s83, s37, 0\n"
    "v_and_b32 v126, 16, v71\n"
    "v_lshlrev_b32 v126, 8, v126\n"
    "global_load_dwordx2 v[84:85], v100, s[82:83] sc1\n"
    "global_load_dwordx2 v[124:125], v126, s[66:67] sc1\n"
    "ds_read_b64_tr_b16 v[208:209], v92 offset:16384\n"
    "ds_read_b64_tr_b16 v[210:211], v92 offset:18432\n"
    "ds_read_b64_tr_b16 v[212:213], v92 offset:24576\n"
    "ds_read_b64_tr_b16 v[214:215], v92 offset:26624\n"
    "ds_read_b64_tr_b16 v[216:217], v93 offset:16384\n"
    "ds_read_b64_tr_b16 v[218:219], v93 offset:18432\n"
    "ds_read_b64_tr_b16 v[220:221], v93 offset:24576\n"
    "ds_read_b64_tr_b16 v[222:223], v93 offset:26624\n"
    "s_waitcnt vmcnt(6) lgkmcnt(0)\n"
    "s_barrier\n"
    "ds_read_b64_tr_b16 v[224:225], v92 offset:32768\n"
    "ds_read_b64_tr_b16 v[226:227], v92 offset:34816\n"
    "ds_read_b64_tr_b16 v[228:229], v92 offset:40960\n"
    "ds_read_b64_tr_b16 v[230:231], v92 offset:43008\n"
    "ds_read_b64_tr_b16 v[232:233], v93 offset:32768\n"
    "ds_read_b64_tr_b16 v[234:235], v93 offset:34816\n"
    "ds_read_b64_tr_b16 v[236:237], v93 offset:40960\n"
    "ds_read_b64_tr_b16 v[238:239], v93 offset:43008\n"
    "s_waitcnt vmcnt(2) lgkmcnt(0)\n"
    "s_barrier\n"
    "ds_read_b64_tr_b16 v[240:241], v92 offset:49152\n"
    "ds_read_b64_tr_b16 v[242:243], v92 offset:51200\n"
    "ds_read_b64_tr_b16 v[244:245], v92 offset:57344\n"
    "ds_read_b64_tr_b16 v[246:247], v92 offset:59392\n"
    "ds_read_b64_tr_b16 v[248:249], v93 offset:49152\n"
    "ds_read_b64_tr_b16 v[250:251], v93 offset:51200\n"
    "ds_read_b64_tr_b16 v[252:253], v93 offset:57344\n"
    "ds_read_b64_tr_b16 v[254:255], v93 offset:59392\n"
    "s_branch L_rc1%=\n"
    "L_rb1%=:\n"
    "s_waitcnt vmcnt(24) lgkmcnt(0)\n"
    "s_barrier\n"
    "s_add_u32 s32, s28, 0x10100\n"
    "s_addc_u32 s33, s29, 0\n"
    "s_add_u32 m0, s30, 114688\n"
    "s_nop 0\n"
    "global_load_lds_dwordx4 v88, s[32:33]\n"
    "s_add_u32 m0, s30, 115712\n"
    "s_nop 0\n"
    "global_load_lds_dwordx4 v89, s[32:33]\n"
    "s_add_u32 m0, s30, 116736\n"
    "s_nop 0\n"
    "global_load_lds_dwordx4 v90, s[32:33]\n"
    "s_add_u32 m0, s30, 117760\n"
    "s_nop 0\n"
    "global_load_lds_dwordx4 v91, s[32:33]\n"
    "ds_read_b64_tr_b16 a[160:161], v96 offset:0\n"
    "ds_read_b64_tr_b16 a[162:163], v96 offset:2048\n"
    "ds_read_b64_tr_b16 a[164:165], v96 offset:8192\n"
    "ds_read_b64_tr_b16 a[166:167], v96 offset:10240\n"
    "ds_read_b64_tr_b16 a[224:225], v97 offset:0\n"
    "ds_read_b64_tr_b16 a[226:227], v97 offset:2048\n"
    "ds_read_b64_tr_b16 a[228:229], v97 offset:8192\n"
    "ds_read_b64_tr_b16 a[230:231], v97 offset:10240\n"
    "s_waitcnt vmcnt(24) lgkmcnt(0)\n"
    "s_barrier\n"
    "s_add_u32 s32, s26, 0x0\n"
    "s_addc_u32 s33, s27, 0\n"
    "s_add_u32 m0, s30, 0\n"
    "s_nop 0\n"
    "global_load_lds_dwordx4 v84, s[32:33]\n"
    "s_add_u32 m0, s30, 1024\n"
    "s_nop 0\n"
    "global_load_lds_dwordx4 v85, s[32:33]\n"
    "s_add_u32 m0, s30, 2048\n"
    "s_nop 0\n"
    "global_load_lds_dwordx4 v86, s[32:33]\n"
    "s_add_u32 m0, s30, 3072\n"
    "s_nop 0\n"
    "global_load_lds_dwordx4 v87, s[32:33]\n"
    "ds_read_b64_tr_b16 a[168:169], v96 offset:16384\n"
    "ds_read_b64_tr_b16 a[170:171], v96 offset:18432\n"
    "ds_read_b64_tr_b16 a[172:173], v96 offset:24576\n"
    "ds_read_b64_tr_b16 a[174:175], v96 offset:26624\n"
    "ds_read_b64_tr_b16 a[232:233], v97 offset:16384\n"
    "ds_read_b64_tr_b16 a[234:235], v97 offset:18432\n"
    "ds_read_b64_tr_b16 a[236:237], v97 offset:24576\n"
    "ds_read_b64_tr_b16 a[238:239], v97 offset:26624\n"
    "s_waitcnt vmcnt(24) lgkmcnt(0)\n"
    "s_barrier\n"
    "s_add_u32 s32, s26, 0x80000\n"
    "s_addc_u32 s33, s27, 0\n"
    "s_add_u32 m0, s30, 16384\n"
    "s_nop 0\n"
    "global_load_lds_dwordx4 v84, s[32:33]\n"
    "s_add_u32 m0, s30, 17408\n"
    "s_nop 0\n"
    "global_load_lds_dwordx4 v85, s[32:33]\n"
    "s_add_u32 m0, s30, 18432\n"
    "s_nop 0\n"
    "global_load_lds_dwordx4 v86, s[32:33]\n"
    "s_add_u32 m0, s30, 19456\n"
    "s_nop 0\n"
    "global_load_lds_dwordx4 v87, s[32:33]\n"
    "ds_read_b64_tr_b16 a[176:177], v96 offset:32768\n"
    "ds_read_b64_tr_b16 a[178:179], v96 offset:34816\n"
    "ds_read_b64_tr_b16 a[180:181], v96 offset:40960\n"
    "ds_read_b64_tr_b16 a[182:183], v96 offset:43008\n"
    "ds_read_b64_tr_b16 a[240:241], v97 offset:32768\n"
    "ds_read_b64_tr_b16 a[242:243], v97 offset:34816\n"
    "ds_read_b64_tr_b16 a[244:245], v97 offset:40960\n"
    "ds_read_b64_tr_b16 a[246:247], v97 offset:43008\n"
    "s_waitcnt vmcnt(24) lgkmcnt(0)\n"
    "s_barrier\n"
    "s_add_u32 s32, s26, 0x100000\n"
    "s_addc_u32 s33, s27, 0\n"
    "s_add_u32 m0, s30, 32768\n"
    "s_nop 0\n"
    "global_load_lds_dwordx4 v84, s[32:33]\n"
    "s_add_u32 m0, s30, 33792\n"
    "s_nop 0\n"
    "global_load_lds_dwordx4 v85, s[32:33]\n"
    "s_add_u32 m0, s30, 34816\n"
    "s_nop 0\n"
    "global_load_lds_dwordx4 v86, s[32:33]\n"
    "s_add_u32 m0, s30, 35840\n"
    "s_nop 0\n"
    "global_load_lds_dwordx4 v87, s[32:33]\n"
    "ds_read_b64_tr_b16 a[184:185], v96 offset:49152\n"
    "ds_read_b64_tr_b16 a[186:187], v96 offset:51200\n"
    "ds_read_b64_tr_b16 a[188:189], v96 offset:57344\n"
    "ds_read_b64_tr_b16 a[190:191], v96 offset:59392\n"
    "ds_read_b64_tr_b16 a[248:249], v97 offset:49152\n"
    "ds_read_b64_tr_b16 a[250:251], v97 offset:51200\n"
    "ds_read_b64_tr_b16 a[252:253], v97 offset:57344\n"
    "ds_read_b64_tr_b16 a[254:255], v97 offset:59392\n"
    "s_waitcnt vmcnt(24) lgkmcnt(0)\n"
    "s_barrier\n"
    "s_add_u32 s32, s26, 0x180000\n"
    "s_addc_u32 s33, s27, 0\n"
    "s_add_u32 m0, s30, 49152\n"
    "s_nop 0\n"
    "global_load_lds_dwordx4 v84, s[32:33]\n"
    "s_add_u32 m0, s30, 50176\n"
    "s_nop 0\n"
    "global_load_lds_dwordx4 v85, s[32:33]\n"
    "s_add_u32 m0, s30, 51200\n"
    "s_nop 0\n"
    "global_load_lds_dwordx4 v86, s[32:33]\n"
    "s_add_u32 m0, s30, 52224\n"
    "s_nop 0\n"
    "global_load_lds_dwordx4 v87, s[32:33]\n"
    "s_waitcnt vmcnt(24) lgkmcnt(0)\n"
    "s_barrier\n"
    "ds_read_b64_tr_b16 v[0:1], v104 offset:0\n"
    "ds_read_b64_tr_b16 v[2:3], v104 offset:512\n"
    "ds_read_b64_tr_b16 v[4:5], v104 offset:8192\n"
    "ds_read_b64_tr_b16 v[6:7], v104 offset:8704\n"
    "ds_read_b64_tr_b16 v[16:17], v104 offset:8\n"
    "ds_read_b64_tr_b16 v[18:19], v104 offset:520\n"
    "ds_read_b64_tr_b16 v[20:21], v104 offset:8200\n"
    "ds_read_b64_tr_b16 v[22:23], v104 offset:8712\n"
    "s_waitcnt lgkmcnt(4)\n"
    "ds_read_b64_tr_b16 v[32:33], v104 offset:16\n"
    "ds_read_b64_tr_b16 v[34:35], v104 offset:528\n"
    "ds_read_b64_tr_b16 v[36:37], v104 offset:8208\n"
    "ds_read_b64_tr_b16 v[38:39], v104 offset:8720\n"
    "ds_read_b64_tr_b16 v[48:49], v104 offset:24\n"
    "ds_read_b64_tr_b16 v[50:51], v104 offset:536\n"
    "ds_read_b64_tr_b16 v[52:53], v104 offset:8216\n"
    "ds_read_b64_tr_b16 v[54:55], v104 offset:8728\n"
    "s_waitcnt vmcnt(20) lgkmcnt(0)\n"
    "s_barrier\n"
    "s_add_u32 s32, s24, 0x0\n"
    "s_addc_u32 s33, s25, 0\n"
    "s_add_u32 m0, s30, 65536\n"
    "s_nop 0\n"
    "global_load_lds_dwordx4 v84, s[32:33]\n"
    "s_add_u32 m0, s30, 66560\n"
    "s_nop 0\n"
    "global_load_lds_dwordx4 v85, s[32:33]\n"
    "s_add_u32 m0, s30, 67584\n"
    "s_nop 0\n"
    "global_load_lds_dwordx4 v86, s[32:33]\n"
    "s_add_u32 m0, s30, 68608\n"
    "s_nop 0\n"
    "global_load_lds_dwordx4 v87, s[32:33]\n"
    "s_add_u32 s32, s24, 0x80000\n"
    "s_addc_u32 s33, s25, 0\n"
    "s_add_u32 m0, s30, 81920\n"
    "s_nop 0\n"
    "global_load_lds_dwordx4 v84, s[32:33]\n"
    "s_add_u32 m0, s30, 82944\n"
    "s_nop 0\n"
    "global_load_lds_dwordx4 v85, s[32:33]\n"
    "s_add_u32 m0, s30, 83968\n"
    "s_nop 0\n"
    "global_load_lds_dwordx4 v86, s[32:33]\n"
    "s_add_u32 m0, s30, 84992\n"
    "s_nop 0\n"
    "global_load_lds_dwordx4 v87, s[32:33]\n"
    "s_waitcnt vmcnt(24) lgkmcnt(0)\n"
    "s_barrier\n"
    "ds_read_b64_tr_b16 v[8:9], v104 offset:32768\n"
    "ds_read_b64_tr_b16 v[10:11], v104 offset:33280\n"
    "ds_read_b64_tr_b16 v[12:13], v104 offset:40960\n"
    "ds_read_b64_tr_b16 v[14:15], v104 offset:41472\n"
    "ds_read_b64_tr_b16 v[24:25], v104 offset:32776\n"
    "ds_read_b64_tr_b16 v[26:27], v104 offset:33288\n"
    "ds_read_b64_tr_b16 v[28:29], v104 offset:40968\n"
    "ds_read_b64_tr_b16 v[30:31], v104 offset:41480\n"
    "s_waitcnt lgkmcnt(4)\n"
    "ds_read_b64_tr_b16 v[40:41], v104 offset:32784\n"
    "ds_read_b64_tr_b16 v[42:43], v104 offset:33296\n"
    "ds_read_b64_tr_b16 v[44:45], v104 offset:40976\n"
    "ds_read_b64_tr_b16 v[46:47], v104 offset:41488\n"
    "ds_read_b64_tr_b16 v[56:57], v104 offset:32792\n"
    "ds_read_b64_tr_b16 v[58:59], v104 offset:33304\n"
    "ds_read_b64_tr_b16 v[60:61], v104 offset:40984\n"
    "ds_read_b64_tr_b16 v[62:63], v104 offset:41496\n"
    "s_waitcnt vmcnt(20) lgkmcnt(0)\n"
    "s_barrier\n"
    "s_add_u32 s32, s24, 0x100000\n"
    "s_addc_u32 s33, s25, 0\n"
    "s_add_u32 m0, s30, 98304\n"
    "s_nop 0\n"
    "global_load_lds_dwordx4 v84, s[32:33]\n"
    "s_add_u32 m0, s30, 99328\n"
    "s_nop 0\n"
    "global_load_lds_dwordx4 v85, s[32:33]\n"
    "s_add_u32 m0, s30, 100352\n"
    "s_nop 0\n"
    "global_load_lds_dwordx4 v86, s[32:33]\n"
    "s_add_u32 m0, s30, 101376\n"
    "s_nop 0\n"
    "global_load_lds_dwordx4 v87, s[32:33]\n"
    "s_add_u32 s32, s24, 0x180000\n"
    "s_addc_u32 s33, s25, 0\n"
    "s_add_u32 m0, s30, 114688\n"
    "s_nop 0\n"
    "global_load_lds_dwordx4 v84, s[32:33]\n"
    "s_add_u32 m0, s30, 115712\n"
    "s_nop 0\n"
    "global_load_lds_dwordx4 v85, s[32:33]\n"
    "s_add_u32 m0, s30, 116736\n"
    "s_nop 0\n"
    "global_load_lds_dwordx4 v86, s[32:33]\n"
    "s_add_u32 m0, s30, 117760\n"
    "s_nop 0\n"
    "global_load_lds_dwordx4 v87, s[32:33]\n"
    "ds_read_b64_tr_b16 a[128:129], v96 offset:0\n"
    "ds_read_b64_tr_b16 a[130:131], v96 offset:2048\n"
    "ds_read_b64_tr_b16 a[132:133], v96 offset:8192\n"
    "ds_read_b64_tr_b16 a[134:135], v96 offset:10240\n"
    "ds_read_b64_tr_b16 a[192:193], v97 offset:0\n"
    "ds_read_b64_tr_b16 a[194:195], v97 offset:2048\n"
    "ds_read_b64_tr_b16 a[196:197], v97 offset:8192\n"
    "ds_read_b64_tr_b16 a[198:199], v97 offset:10240\n"
    "s_waitcnt vmcnt(24) lgkmcnt(0)\n"
    "s_barrier\n"
    "s_add_u32 s32, s24, 0x200000\n"
    "s_addc_u32 s33, s25, 0\n"
    "s_add_u32 m0, s30, 0\n"
    "s_nop 0\n"
    "global_load_lds_dwordx4 v84, s[32:33]\n"
    "s_add_u32 m0, s30, 1024\n"
    "s_nop 0\n"
    "global_load_lds_dwordx4 v85, s[32:33]\n"
    "s_add_u32 m0, s30, 2048\n"
    "s_nop 0\n"
    "global_load_lds_dwordx4 v86, s[32:33]\n"
    "s_add_u32 m0, s30, 3072\n"
    "s_nop 0\n"
    "global_load_lds_dwordx4 v87, s[32:33]\n"
    "ds_read_b64_tr_b16 a[136:137], v96 offset:16384\n"
    "ds_read_b64_tr_b16 a[138:139], v96 offset:18432\n"
    "ds_read_b64_tr_b16 a[140:141], v96 offset:24576\n"
    "ds_read_b64_tr_b16 a[142:143], v96 offset:26624\n"
    "ds_read_b64_tr_b16 a[200:201], v97 offset:16384\n"
    "ds_read_b64_tr_b16 a[202:203], v97 offset:18432\n"
    "ds_read_b64_tr_b16 a[204:205], v97 offset:24576\n"
    "ds_read_b64_tr_b16 a[206:207], v97 offset:26624\n"
    "s_waitcnt vmcnt(24) lgkmcnt(0)\n"
    "s_barrier\n"
    "s_add_u32 s32, s24, 0x280000\n"
    "s_addc_u32 s33, s25, 0\n"
    "s_add_u32 m0, s30, 16384\n"
    "s_nop 0\n"
    "global_load_lds_dwordx4 v84, s[32:33]\n"
    "s_add_u32 m0, s30, 17408\n"
    "s_nop 0\n"
    "global_load_lds_dwordx4 v85, s[32:33]\n"
    "s_add_u32 m0, s30, 18432\n"
    "s_nop 0\n"
    "global_load_lds_dwordx4 v86, s[32:33]\n"
    "s_add_u32 m0, s30, 19456\n"
    "s_nop 0\n"
    "global_load_lds_dwordx4 v87, s[32:33]\n"
    "ds_read_b64_tr_b16 a[144:145], v96 offset:32768\n"
    "ds_read_b64_tr_b16 a[146:147], v96 offset:34816\n"
    "ds_read_b64_tr_b16 a[148:149], v96 offset:40960\n"
    "ds_read_b64_tr_b16 a[150:151], v96 offset:43008\n"
    "ds_read_b64_tr_b16 a[208:209], v97 offset:32768\n"
    "ds_read_b64_tr_b16 a[210:211], v97 offset:34816\n"
    "ds_read_b64_tr_b16 a[212:213], v97 offset:40960\n"
    "ds_read_b64_tr_b16 a[214:215], v97 offset:43008\n"
    "s_waitcnt vmcnt(24) lgkmcnt(0)\n"
    "s_barrier\n"
    "s_add_u32 s32, s24, 0x300000\n"
    "s_addc_u32 s33, s25, 0\n"
    "s_add_u32 m0, s30, 32768\n"
    "s_nop 0\n"
    "global_load_lds_dwordx4 v84, s[32:33]\n"
    "s_add_u32 m0, s30, 33792\n"
    "s_nop 0\n"
    "global_load_lds_dwordx4 v85, s[32:33]\n"
    "s_add_u32 m0, s30, 34816\n"
    "s_nop 0\n"
    "global_load_lds_dwordx4 v86, s[32:33]\n"
    "s_add_u32 m0, s30, 35840\n"
    "s_nop 0\n"
    "global_load_lds_dwordx4 v87, s[32:33]\n"
    "ds_read_b64_tr_b16 a[152:153], v96 offset:49152\n"
    "ds_read_b64_tr_b16 a[154:155], v96 offset:51200\n"
    "ds_read_b64_tr_b16 a[156:157], v96 offset:57344\n"
    "ds_read_b64_tr_b16 a[158:159], v96 offset:59392\n"
    "ds_read_b64_tr_b16 a[216:217], v97 offset:49152\n"
    "ds_read_b64_tr_b16 a[218:219], v97 offset:51200\n"
    "ds_read_b64_tr_b16 a[220:221], v97 offset:57344\n"
    "ds_read_b64_tr_b16 a[222:223], v97 offset:59392\n"
    "s_waitcnt vmcnt(24) lgkmcnt(0)\n"
    "s_barrier\n"
    "s_add_u32 s32, s24, 0x380000\n"
    "s_addc_u32 s33, s25, 0\n"
    "s_add_u32 m0, s30, 49152\n"
    "s_nop 0\n"
    "global_load_lds_dwordx4 v84, s[32:33]\n"
    "s_add_u32 m0, s30, 50176\n"
    "s_nop 0\n"
    "global_load_lds_dwordx4 v85, s[32:33]\n"
    "s_add_u32 m0, s30, 51200\n"
    "s_nop 0\n"
    "global_load_lds_dwordx4 v86, s[32:33]\n"
    "s_add_u32 m0, s30, 52224\n"
    "s_nop 0\n"
    "global_load_lds_dwordx4 v87, s[32:33]\n"
    "ds_read_b64_tr_b16 a[0:1], v94 offset:0\n"
    "ds_read_b64_tr_b16 a[2:3], v94 offset:2048\n"
    "ds_read_b64_tr_b16 a[4:5], v94 offset:8192\n"
    "ds_read_b64_tr_b16 a[6:7], v94 offset:10240\n"
    "ds_read_b64_tr_b16 a[64:65], v95 offset:0\n"
    "ds_read_b64_tr_b16 a[66:67], v95 offset:2048\n"
    "ds_read_b64_tr_b16 a[68:69], v95 offset:8192\n"
    "ds_read_b64_tr_b16 a[70:71], v95 offset:10240\n"
    "s_waitcnt vmcnt(24) lgkmcnt(0)\n"
    "s_barrier\n"
    "ds_read_b64_tr_b16 a[8:9], v94 offset:16384\n"
    "ds_read_b64_tr_b16 a[10:11], v94 offset:18432\n"
    "ds_read_b64_tr_b16 a[12:13], v94 offset:24576\n"
    "ds_read_b64_tr_b16 a[14:15], v94 offset:26624\n"
    "ds_read_b64_tr_b16 a[72:73], v95 offset:16384\n"
    "ds_read_b64_tr_b16 a[74:75], v95 offset:18432\n"
    "ds_read_b64_tr_b16 a[76:77], v95 offset:24576\n"
    "ds_read_b64_tr_b16 a[78:79], v95 offset:26624\n"
    "s_waitcnt vmcnt(20) lgkmcnt(0)\n"
    "s_barrier\n"
    "ds_read_b64_tr_b16 a[16:17], v94 offset:32768\n"
    "ds_read_b64_tr_b16 a[18:19], v94 offset:34816\n"
    "ds_read_b64_tr_b16 a[20:21], v94 offset:40960\n"
    "ds_read_b64_tr_b16 a[22:23], v94 offset:43008\n"
    "ds_read_b64_tr_b16 a[80:81], v95 offset:32768\n"
    "ds_read_b64_tr_b16 a[82:83], v95 offset:34816\n"
    "ds_read_b64_tr_b16 a[84:85], v95 offset:40960\n"
    "ds_read_b64_tr_b16 a[86:87], v95 offset:43008\n"
    "s_waitcnt vmcnt(16) lgkmcnt(0)\n"
    "s_barrier\n"
    "ds_read_b64_tr_b16 a[24:25], v94 offset:49152\n"
    "ds_read_b64_tr_b16 a[26:27], v94 offset:51200\n"
    "ds_read_b64_tr_b16 a[28:29], v94 offset:57344\n"
    "ds_read_b64_tr_b16 a[30:31], v94 offset:59392\n"
    "ds_read_b64_tr_b16 a[88:89], v95 offset:49152\n"
    "ds_read_b64_tr_b16 a[90:91], v95 offset:51200\n"
    "ds_read_b64_tr_b16 a[92:93], v95 offset:57344\n"
    "ds_read_b64_tr_b16 a[94:95], v95 offset:59392\n"
    "s_waitcnt vmcnt(12) lgkmcnt(0)\n"
    "s_barrier\n"
    "ds_read_b64_tr_b16 a[32:33], v92 offset:0\n"
    "ds_read_b64_tr_b16 a[34:35], v92 offset:2048\n"
    "ds_read_b64_tr_b16 a[36:37], v92 offset:8192\n"
    "ds_read_b64_tr_b16 a[38:39], v92 offset:10240\n"
    "ds_read_b64_tr_b16 a[96:97], v93 offset:0\n"
    "ds_read_b64_tr_b16 a[98:99], v93 offset:2048\n"
    "ds_read_b64_tr_b16 a[100:101], v93 offset:8192\n"
    "ds_read_b64_tr_b16 a[102:103], v93 offset:10240\n"
    "s_waitcnt vmcnt(8) lgkmcnt(0)\n"
    "s_barrier\n"
    "v_lshlrev_b32 v100, 3, v71\n"
    "s_add_u32 s82, s36, 0x20c000\n"
    "s_addc_u32 s83, s37, 0\n"
    "v_and_b32 v126, 16, v71\n"
    "v_lshlrev_b32 v126, 8, v126\n"
    "global_load_dwordx2 v[84:85], v100, s[82:83] sc1\n"
    "global_load_dwordx2 v[124:125], v126, s[66:67] sc1\n"
    "ds_read_b64_tr_b16 a[40:41], v92 offset:16384\n"
    "ds_read_b64_tr_b16 a[42:43], v92 offset:18432\n"
    "ds_read_b64_tr_b16 a[44:45], v92 offset:24576\n"
    "ds_read_b64_tr_b16 a[46:47], v92 offset:26624\n"
    "ds_read_b64_tr_b16 a[104:105], v93 offset:16384\n"
    "ds_read_b64_tr_b16 a[106:107], v93 offset:18432\n"
    "ds_read_b64_tr_b16 a[108:109], v93 offset:24576\n"
    "ds_read_b64_tr_b16 a[110:111], v93 offset:26624\n"
    "s_waitcnt vmcnt(6) lgkmcnt(0)\n"
    "s_barrier\n"
    "ds_read_b64_tr_b16 a[48:49], v92 offset:32768\n"
    "ds_read_b64_tr_b16 a[50:51], v92 offset:34816\n"
    "ds_read_b64_tr_b16 a[52:53], v92 offset:40960\n"
    "ds_read_b64_tr_b16 a[54:55], v92 offset:43008\n"
    "ds_read_b64_tr_b16 a[112:113], v93 offset:32768\n"
    "ds_read_b64_tr_b16 a[114:115], v93 offset:34816\n"
    "ds_read_b64_tr_b16 a[116:117], v93 offset:40960\n"
    "ds_read_b64_tr_b16 a[118:119], v93 offset:43008\n"
    "s_waitcnt vmcnt(2) lgkmcnt(0)\n"
    "s_barrier\n"
    "ds_read_b64_tr_b16 a[56:57], v92 offset:49152\n"
    "ds_read_b64_tr_b16 a[58:59], v92 offset:51200\n"
    "ds_read_b64_tr_b16 a[60:61], v92 offset:57344\n"
    "ds_read_b64_tr_b16 a[62:63], v92 offset:59392\n"
    "ds_read_b64_tr_b16 a[120:121], v93 offset:49152\n"
    "ds_read_b64_tr_b16 a[122:123], v93 offset:51200\n"
    "ds_read_b64_tr_b16 a[124:125], v93 offset:57344\n"
    "ds_read_b64_tr_b16 a[126:127], v93 offset:59392\n"
    "L_rc1%=:\n"
    "s_waitcnt vmcnt(0) lgkmcnt(0)\n"
    "s_barrier\n"
    "v_xor_b32 v124, s96, v124\n"
    "v_xor_b32 v125, s97, v125\n"
    "v_cmp_gt_u64 vcc, 64, v[124:125]\n"
    "s_nop 1\n"
    "v_cndmask_b32 v124, 0, v124, vcc\n"
    "s_nop 0\n"
    "v_readlane_b32 s80, v124, 0\n"
    "v_readlane_b32 s81, v124, 1\n"
    "ds_write_b128 v116, v[60:63] offset:15360\n"
    "ds_write_b128 v116, v[56:59] offset:14336\n"
    "ds_write_b128 v116, v[52:55] offset:13312\n"
    "ds_write_b128 v116, v[48:51] offset:12288\n"
    "ds_write_b128 v116, v[44:47] offset:11264\n"
    "ds_write_b128 v116, v[40:43] offset:10240\n"
    "ds_write_b128 v116, v[36:39] offset:9216\n"
    "ds_write_b128 v116, v[32:35] offset:8192\n"
    "s_waitcnt lgkmcnt(4)\n"
    "ds_write_b128 v116, v[28:31] offset:7168\n"
    "ds_write_b128 v116, v[24:27] offset:6144\n"
    "ds_write_b128 v116, v[20:23] offset:5120\n"
    "ds_write_b128 v116, v[16:19] offset:4096\n"
    "ds_write_b128 v116, v[12:15] offset:3072\n"
    "ds_write_b128 v116, v[8:11] offset:2048\n"
    "ds_write_b128 v116, v[4:7] offset:1024\n"
    "ds_write_b128 v116, v[0:3] offset:0\n"
    "s_waitcnt lgkmcnt(4)\n"
    "s_min_u32 s92, s80, s81\n"
    "s_branch L_tabc%=\n"
    "L_tab%=:\n"
    "global_load_dwordx2 v[84:85], v100, s[82:83] sc1\n"
    "s_waitcnt vmcnt(0)\n"
    "L_tabc%=:\n"
    "v_xor_b32 v84, s96, v84\n"
    "v_xor_b32 v85, s97, v85\n"
    "v_cmp_gt_u64 vcc, 64, v[84:85]\n"
    "s_nop 1\n"
    "v_cndmask_b32 v84, 0, v84, vcc\n"
    "v_cmp_eq_u32 vcc, 0, v84\n"
    "s_nop 0\n"
    "s_cbranch_vccz L_tabok%=\n"
    "s_sleep 2\n"
    "s_add_u32 s90, s90, 1\n"
    "s_and_b32 s78, s90, 0xff\n"
    "s_cmp_lg_u32 s78, 0\n"
    "s_cbranch_scc1 L_tab%=\n"
    "v_mov_b32 v126, 0\n"
    "global_load_dwordx2 v[126:127], v126, s[68:69] sc1\n"
    "s_waitcnt vmcnt(0)\n"
    "v_cmp_eq_u64 vcc, s[96:97], v[126:127]\n"
    "s_nop 1\n"
    "s_cbranch_vccnz L_bail_tab%=\n"
    "s_cmp_lt_u32 s90, 0x40000\n"
    "s_cbranch_scc1 L_tab%=\n"
    "v_mov_b32 v126, s96\n"
    "v_mov_b32 v127, s97\n"
    "s_mov_b64 exec, 1\n"
    "global_store_dwordx2 v71, v[126:127], s[68:69] sc1\n"
    "s_mov_b64 exec, -1\n"
    "L_bail_tab%=:\n"
    "L_tabok%=:\n"
    "v_readfirstlane_b32 s93, v84\n"
    "s_nop 0\n"
    "s_sub_u32 s93, s93, 1\n"
    "s_and_b32 s93, s93, 7\n"
    "s_andn2_b32 s78, 2, s93\n"
    "s_lshl_b32 s78, s78, 12\n"
    "s_xor_b32 s58, s58, s78\n"
    "s_xor_b32 s60, s60, s78\n"
    "s_xor_b32 s62, s62, s78\n"
    "s_mov_b32 s43, 0\n"
    "s_mov_b32 s19, 0\n"
    "s_cmp_eq_u32 s92, 0\n"
    "s_cbranch_scc1 L_nokv%=\n"
    "global_load_dwordx4 v[56:59], v71, s[58:59] offset:-2048 sc1\n"
    "global_load_dwordx4 v[60:63], v71, s[58:59] offset:2048 sc1\n"
    "global_load_dwordx2 v[64:65], v71, s[60:61] offset:0 sc1\n"
    "global_load_dwordx2 v[68:69], v71, s[60:61] offset:8 sc1\n"
    "global_load_dwordx2 v[72:73], v71, s[60:61] offset:1024 sc1\n"
    "global_load_dwordx2 v[76:77], v71, s[60:61] offset:1032 sc1\n"
    "s_mov_b32 s19, 1\n"
    "L_nokv%=:\n"
    "s_cmp_lg_u32 s39, 0\n"
    "s_cbranch_scc1 L_sv1%=\n"
    "ds_write_b128 v116, a[0:3] offset:16384\n"
    "ds_write_b128 v116, a[4:7] offset:17408\n"
    "ds_write_b128 v116, a[64:67] offset:18432\n"
    "ds_write_b128 v116, a[68:71] offset:19456\n"
    "ds_write_b128 v116, a[8:11] offset:20480\n"
    "ds_write_b128 v116, a[12:15] offset:21504\n"
    "ds_write_b128 v116, a[72:75] offset:22528\n"
    "ds_write_b128 v116, a[76:79] offset:23552\n"
    "s_waitcnt lgkmcnt(4)\n"
    "ds_write_b128 v116, a[16:19] offset:24576\n"
    "ds_write_b128 v116, a[20:23] offset:25600\n"
    "ds_write_b128 v116, a[80:83] offset:26624\n"
    "ds_write_b128 v116, a[84:87] offset:27648\n"
    "ds_write_b128 v116, a[32:35] offset:28672\n"
    "ds_write_b128 v116, a[36:39] offset:29696\n"
    "ds_write_b128 v116, a[96:99] offset:30720\n"
    "ds_write_b128 v116, a[100:103] offset:31744\n"
    "s_waitcnt lgkmcnt(4)\n"
    "s_branch L_sv4%=\n"
    "L_sv1%=:\n"
    "s_cmp_lg_u32 s39, 1\n"
    "s_cbranch_scc1 L_sv2%=\n"
    "ds_write_b128 v116, a[16:19] offset:16384\n"
    "ds_write_b128 v116, a[20:23] offset:17408\n"
    "ds_write_b128 v116, a[80:83] offset:18432\n"
    "ds_write_b128 v116, a[84:87] offset:19456\n"
    "ds_write_b128 v116, a[24:27] offset:20480\n"
    "ds_write_b128 v116, a[28:31] offset:21504\n"
    "ds_write_b128 v116, a[88:91] offset:22528\n"
    "ds_write_b128 v116, a[92:95] offset:23552\n"
    "s_waitcnt lgkmcnt(4)\n"
    "ds_write_b128 v116, a[0:3] offset:24576\n"
    "ds_write_b128 v116, a[4:7] offset:25600\n"
    "ds_write_b128 v116, a[64:67] offset:26624\n"
    "ds_write_b128 v116, a[68:71] offset:27648\n"
    "ds_write_b128 v116, a[48:51] offset:28672\n"
    "ds_write_b128 v116, a[52:55] offset:29696\n"
    "ds_write_b128 v116, a[112:115] offset:30720\n"
    "ds_write_b128 v116, a[116:119] offset:31744\n"
    "s_waitcnt lgkmcnt(4)\n"
    "s_branch L_sv4%=\n"
    "L_sv2%=:\n"
    "s_cmp_lg_u32 s39, 2\n"
    "s_cbranch_scc1 L_sv3%=\n"
    "ds_write_b128 v116, a[32:35] offset:16384\n"
    "ds_write_b128 v116, a[36:39] offset:17408\n"
    "ds_write_b128 v116, a[96:99] offset:18432\n"
    "ds_write_b128 v116, a[100:103] offset:19456\n"
    "ds_write_b128 v116, a[40:43] offset:20480\n"
    "ds_write_b128 v116, a[44:47] offset:21504\n"
    "ds_write_b128 v116, a[104:107] offset:22528\n"
    "ds_write_b128 v116, a[108:111] offset:23552\n"
    "s_waitcnt lgkmcnt(4)\n"
    "ds_write_b128 v116, a[48:51] offset:24576\n"
    "ds_write_b128 v116, a[52:55] offset:25600\n"
    "ds_write_b128 v116, a[112:115] offset:26624\n"
    "ds_write_b128 v116, a[116:119] offset:27648\n"
    "ds_write_b128 v116, a[0:3] offset:28672\n"
    "ds_write_b128 v116, a[4:7] offset:29696\n"
    "ds_write_b128 v116, a[64:67] offset:30720\n"
    "ds_write_b128 v116, a[68:71] offset:31744\n"
    "s_waitcnt lgkmcnt(4)\n"
    "s_branch L_sv4%=\n"
    "L_sv3%=:\n"
    "ds_write_b128 v116, a[48:51] offset:16384\n"
    "ds_write_b128 v116, a[52:55] offset:17408\n"
    "ds_write_b128 v116, a[112:115] offset:18432\n"
    "ds_write_b128 v116, a[116:119] offset:19456\n"
    "ds_write_b128 v116, a[56:59] offset:20480\n"
    "ds_write_b128 v116, a[60:63] offset:21504\n"
    "ds_write_b128 v116, a[120:123] offset:22528\n"
    "ds_write_b128 v116, a[124:127] offset:23552\n"
    "s_waitcnt lgkmcnt(4)\n"
    "ds_write_b128 v116, a[32:35] offset:24576\n"
    "ds_write_b128 v116, a[36:39] offset:25600\n"
    "ds_write_b128 v116, a[96:99] offset:26624\n"
    "ds_write_b128 v116, a[100:103] offset:27648\n"
    "ds_write_b128 v116, a[16:19] offset:28672\n"
    "ds_write_b128 v116, a[20:23] offset:29696\n"
    "ds_write_b128 v116, a[80:83] offset:30720\n"
    "ds_write_b128 v116, a[84:87] offset:31744\n"
    "s_waitcnt lgkmcnt(4)\n"
    "L_sv4%=:\n"
    "v_lshrrev_b32 v0, 4, v71\n"
    "v_and_b32 v1, 15, v0\n"
    "v_lshrrev_b32 v2, 4, v0\n"
    "s_lshl_b32 s78, s42, 4\n"
    "s_lshl_b32 s79, s39, 2\n"
    "s_add_u32 s78, s78, s79\n"
    "v_lshrrev_b32 v8, 2, v1\n"
    "v_add_u32 v8, s78, v8\n"
    "v_lshrrev_b32 v10, 1, v8\n"
    "v_lshlrev_b32 v10, 16, v10\n"
    "v_and_b32 v9, 1, v8\n"
    "s_lshr_b32 s78, s39, 1\n"
    "s_lshl_b32 s78, s78, 4\n"
    "s_xor_b32 s78, s78, s41\n"
    "v_xor_b32 v8, s78, v8\n"
    "v_lshl_add_u32 v10, v8, 7, v10\n"
    "v_lshrrev_b32 v8, 1, v2\n"
    "v_lshl_add_u32 v9, v8, 2, v9\n"
    "s_lshl_b32 s78, s42, 2\n"
    "s_xor_b32 s78, s78, s93\n"
    "v_xor_b32 v9, s78, v9\n"
    "v_lshl_add_u32 v10, v9, 12, v10\n"
    "v_and_b32 v8, 1, v2\n"
    "v_lshl_add_u32 v10, v8, 6, v10\n"
    "v_and_b32 v8, 3, v1\n"
    "v_lshl_add_u32 v117, v8, 4, v10\n"
    "s_lshl_b32 s78, s42, 5\n"
    "s_add_u32 s78, s78, s41\n"
    "s_lshl_b32 s78, s78, 7\n"
    "s_add_u32 s64, s36, 0x220000\n"
    "s_addc_u32 s65, s37, 0\n"
    "s_add_u32 s64, s64, s78\n"
    "s_addc_u32 s65, s65, 0\n"
    "s_and_b32 s78, s11, 1\n"
    "s_lshl_b32 s78, s78, 2\n"
    "s_bfe_u32 s79, s11, 0x10001\n"
    "s_or_b32 s78, s78, s79\n"
    "s_xor_b32 s78, s78, s93\n"
    "s_xor_b32 s78, s78, 1\n"
    "s_lshl_b32 s78, s78, 12\n"
    "s_lshl_b32 s79, s42, 16\n"
    "s_add_u32 s79, s79, s78\n"
    "s_lshr_b32 s80, s39, 1\n"
    "s_lshl_b32 s80, s80, 15\n"
    "s_add_u32 s79, s79, s80\n"
    "s_and_b32 s80, s39, 1\n"
    "s_lshl_b32 s80, s80, 11\n"
    "s_add_u32 s79, s79, s80\n"
    "s_add_u32 s54, s36, 0x100000\n"
    "s_addc_u32 s55, s37, 0\n"
    "s_add_u32 s54, s54, s79\n"
    "s_addc_u32 s55, s55, 0\n"
    "s_mov_b64 s[48:49], s[54:55]\n"
    "s_lshl_b32 s79, s38, 6\n"
    "s_add_u32 s56, s12, s79\n"
    "s_addc_u32 s57, s13, 0\n"
    "s_xor_b32 s78, s78, 0x4000\n"
    "s_add_u32 s12, s12, s78\n"
    "s_addc_u32 s13, s13, 0\n"
    "v_and_b32 v8, 1, v0\n"
    "v_bfe_u32 v9, v0, 1, 1\n"
    "v_lshl_add_u32 v8, v8, 2, v9\n"
    "s_xor_b32 s78, s93, 5\n"
    "v_xor_b32 v8, s78, v8\n"
    "v_lshlrev_b32 v118, 12, v8\n"
    "v_lshlrev_b32 v119, 2, v71\n"
    "s_mov_b32 s43, 0\n"
    "v_mov_b64 v[104:105], 0\n"
    "v_mov_b64 v[106:107], 0\n"
    "v_mov_b64 v[108:109], 0\n"
    "v_mov_b64 v[110:111], 0\n"
    "v_mov_b64 v[32:33], 0\n"
    "v_mov_b64 v[34:35], 0\n"
    "v_mov_b64 v[36:37], 0\n"
    "v_mov_b64 v[38:39], 0\n"
    "v_mov_b64 v[40:41], 0\n"
    "v_mov_b64 v[42:43], 0\n"
    "v_mov_b64 v[44:45], 0\n"
    "v_mov_b64 v[46:47], 0\n"
    "v_mov_b64 v[48:49], 0\n"
    "v_mov_b64 v[50:51], 0\n"
    "v_mov_b64 v[52:53], 0\n"
    "v_mov_b64 v[54:55], 0\n"
    "s_cmp_lg_u32 s19, 0\n"
    "s_cbranch_scc1 L_kvdone%=\n"
    "s_add_u32 s79, s43, 1\n"
    "s_min_u32 s79, s79, 63\n"
    "s_lshr_b32 s79, s79, 1\n"
    "s_cmp_lt_u32 s79, s92\n"
    "s_cbranch_scc1 L_cntokp%=\n"
    "L_cntp%=:\n"
    "v_and_b32 v126, 16, v71\n"
    "v_lshlrev_b32 v126, 8, v126\n"
    "global_load_dwordx2 v[126:127], v126, s[66:67] sc1\n"
    "s_waitcnt vmcnt(0)\n"
    "v_xor_b32 v126, s96, v126\n"
    "v_xor_b32 v127, s97, v127\n"
    "v_cmp_gt_u64 vcc, 64, v[126:127]\n"
    "s_nop 1\n"
    "v_cndmask_b32 v126, 0, v126, vcc\n"
    "s_nop 0\n"
    "v_readlane_b32 s80, v126, 0\n"
    "v_readlane_b32 s81, v126, 1\n"
    "s_nop 1\n"
    "s_min_u32 s92, s80, s81\n"
    "s_cmp_lt_u32 s79, s92\n"
    "s_cbranch_scc1 L_cntokp%=\n"
    "s_sleep 2\n"
    "s_add_u32 s90, s90, 1\n"
    "s_and_b32 s78, s90, 0xff\n"
    "s_cmp_lg_u32 s78, 0\n"
    "s_cbranch_scc1 L_cntp%=\n"
    "v_mov_b32 v126, 0\n"
    "global_load_dwordx2 v[126:127], v126, s[68:69] sc1\n"
    "s_waitcnt vmcnt(0)\n"
    "v_cmp_eq_u64 vcc, s[96:97], v[126:127]\n"
    "s_nop 1\n"
    "s_cbranch_vccnz L_bail_cntp%=\n"
    "s_cmp_lt_u32 s90, 0x40000\n"
    "s_cbranch_scc1 L_cntp%=\n"
    "v_mov_b32 v126, s96\n"
    "v_mov_b32 v127, s97\n"
    "s_mov_b64 exec, 1\n"
    "global_store_dwordx2 v71, v[126:127], s[68:69] sc1\n"
    "s_mov_b64 exec, -1\n"
    "L_bail_cntp%=:\n"
    "L_cntokp%=:\n"
    "global_load_dwordx4 v[56:59], v71, s[58:59] offset:-2048 sc1\n"
    "global_load_dwordx4 v[60:63], v71, s[58:59] offset:2048 sc1\n"
    "global_load_dwordx2 v[64:65], v71, s[60:61] offset:0 sc1\n"
    "global_load_dwordx2 v[68:69], v71, s[60:61] offset:8 sc1\n"
    "global_load_dwordx2 v[72:73], v71, s[60:61] offset:1024 sc1\n"
    "global_load_dwordx2 v[76:77], v71, s[60:61] offset:1032 sc1\n"
    "L_kvdone%=:\n"
    "s_add_u32 s11, s43, 1\n"
    "s_and_b32 s11, s11, 3\n"
    "s_cselect_b32 s11, 0, 0x2000\n"
    "s_add_u32 s58, s58, 0x80000\n"
    "s_addc_u32 s59, s59, 0\n"
    "s_xor_b32 s58, s58, s11\n"
    "s_add_u32 s60, s60, 0x80000\n"
    "s_addc_u32 s61, s61, 0\n"
    "s_xor_b32 s60, s60, s11\n"
    "s_waitcnt lgkmcnt(0)\n"
    "s_memtime s[6:7]\n"
    "s_memrealtime s[78:79]\n"
    "s_nop 3\n"
    "s_memrealtime s[80:81]\n"
    "s_nop 3\n"
    "s_memrealtime s[82:83]\n"
    "s_nop 3\n"
    "s_memrealtime s[84:85]\n"
    "s_nop 3\n"
    "s_memrealtime s[86:87]\n"
    "s_nop 3\n"
    "s_memrealtime s[88:89]\n"
    "s_mov_b32 s10, 0\n"
    "s_waitcnt lgkmcnt(0)\n"
    "s_mul_i32 s91, s78, 24\n"
    "s_cmp_eq_u32 s78, s80\n"
    "s_addc_u32 s10, s10, 0\n"
    "s_cmp_eq_u32 s78, s82\n"
    "s_addc_u32 s10, s10, 0\n"
    "s_cmp_eq_u32 s78, s84\n"
    "s_addc_u32 s10, s10, 0\n"
    "s_cmp_eq_u32 s78, s86\n"
    "s_addc_u32 s10, s10, 0\n"
    "s_cmp_eq_u32 s78, s88\n"
    "s_addc_u32 s10, s10, 0\n"
    "s_mul_i32 s10, s10, 20\n"
    "s_sub_u32 s91, s91, s6\n"
    "s_sub_u32 s91, s91, s10\n"
    "s_mov_b32 s94, 100\n"
    "s_mov_b64 s[0:1], 0\n"
    "s_mov_b64 s[40:41], 0\n"
    "s_mov_b32 s51, 2120\n"
    "s_mov_b32 s15, 740\n"
    "s_mov_b32 s3, 1140\n"
    "s_add_u32 s46, s91, 2400\n"
    "s_getpc_b64 s[4:5]\n"
    "L_pc%=:\n"
    "s_add_u32 s4, s4, L_sled%=-L_pc%=\n"
    "s_addc_u32 s5, s5, 0\n"
    "v_or_b32 v84, 1, s96\n"
    "v_mov_b32 v85, s97\n"
    "v_add_u32 v86, s43, v84\n"
    "s_cmp_eq_u32 s39, 1\n"
    "s_cbranch_scc1 L_poll1%=\n"
    "s_cmp_eq_u32 s39, 2\n"
    "s_cbranch_scc1 L_poll2%=\n"
    "s_cmp_eq_u32 s39, 3\n"
    "s_cbranch_scc1 L_poll3%=\n"
    "s_branch L_poll0%=\n"
    ".p2align 6\n"
    "L_poll0%=:\n"
    "global_load_dwordx4 v[0:3], v71, s[54:55] offset:0 sc1\n"
    "global_load_dwordx4 v[4:7], v71, s[54:55] offset:1024 sc1\n"
    "s_mov_b32 m0, 51456\n"
    "s_waitcnt vmcnt(0)\n"
    "s_branch L_top0%=\n"
    ".p2align 6\n"
    "L_top0%=:\n"
    "s_waitcnt vmcnt(3)\n"
    "v_smfmac_f32_16x16x64_bf16 v[40:43], v[0:3], a[0:7], v67\n"
    "ds_write_b128 v71, v[0:3] offset:0\n"
    "v_smfmac_f32_16x16x64_bf16 v[44:47], v[0:3], a[64:71], v67\n"
    "v_smfmac_f32_16x16x64_bf16 v[32:35], v[0:3], v[128:135], v67\n"
    "v_smfmac_f32_16x16x64_bf16 v[36:39], v[0:3], v[136:143], v67\n"
    "s_waitcnt vmcnt(2)\n"
    "v_smfmac_f32_16x16x64_bf16 v[40:43], v[4:7], a[8:15], v67\n"
    "ds_write_b128 v71, v[4:7] offset:1024\n"
    "s_waitcnt lgkmcnt(1)\n"
    "s_barrier\n"
    "ds_read_b128 v[8:11], v71 offset:2048\n"
    "v_smfmac_f32_16x16x64_bf16 v[44:47], v[4:7], a[72:79], v67\n"
    "ds_read_b128 v[16:19], v71 offset:4096\n"
    "v_smfmac_f32_16x16x64_bf16 v[32:35], v[4:7], v[144:151], v67\n"
    "ds_read_b128 v[24:27], v71 offset:6144\n"
    "v_smfmac_f32_16x16x64_bf16 v[36:39], v[4:7], v[152:159], v67\n"
    "s_waitcnt lgkmcnt(3)\n"
    "s_barrier\n"
    "v_smfmac_f32_16x16x64_bf16 v[48:51], v[0:3], a[128:135], v67\n"
    "s_waitcnt lgkmcnt(2)\n"
    "ds_read_b128 v[12:15], v71 offset:3072\n"
    "v_smfmac_f32_16x16x64_bf16 v[32:35], v[8:11], v[160:167], v67\n"
    "ds_read_b128 v[20:23], v71 offset:5120\n"
    "v_smfmac_f32_16x16x64_bf16 v[36:39], v[8:11], v[168:175], v67\n"
    "s_waitcnt lgkmcnt(2)\n"
    "ds_read_b128 v[28:31], v71 offset:7168\n"
    "v_smfmac_f32_16x16x64_bf16 v[32:35], v[16:19], v[192:199], v67\n"
    "s_mov_b32 s78, 0x12345\n"
    "ds_read_b128 a[0:3], v116 offset:0\n"
    "v_smfmac_f32_16x16x64_bf16 v[36:39], v[16:19], v[200:207], v67\n"
    "s_mov_b32 s78, 0x12345\n"
    "ds_read_b128 a[4:7], v116 offset:1024\n"
    "v_smfmac_f32_16x16x64_bf16 v[32:35], v[24:27], v[224:231], v67\n"
    "s_mov_b32 s78, 0x12345\n"
    "ds_read_b128 a[64:67], v116 offset:4096\n"
    "v_smfmac_f32_16x16x64_bf16 v[36:39], v[24:27], v[232:239], v67\n"
    "s_waitcnt lgkmcnt(5)\n"
    "ds_read_b128 a[68:71], v116 offset:5120\n"
    "v_smfmac_f32_16x16x64_bf16 v[32:35], v[12:15], v[176:183], v67\n"
    "s_sub_i32 s2, s6, s16\n"
    "ds_read_b128 a[8:11], v116 offset:2048\n"
    "v_smfmac_f32_16x16x64_bf16 v[36:39], v[12:15], v[184:191], v67\n"
    "s_waitcnt lgkmcnt(5)\n"
    "ds_read_b128 a[12:15], v116 offset:3072\n"
    "v_smfmac_f32_16x16x64_bf16 v[32:35], v[20:23], v[208:215], v67\n"
    "s_cmp_gt_i32 s2, s3\n"
    "ds_read_b128 a[72:75], v116 offset:6144\n"
    "v_smfmac_f32_16x16x64_bf16 v[36:39], v[20:23], v[216:223], v67\n"
    "ds_read_b128 a[76:79], v116 offset:7168\n"
    "v_smfmac_f32_16x16x64_bf16 v[32:35], v[28:31], v[240:247], v67\n"
    "s_nop 0\n"
    "v_smfmac_f32_16x16x64_bf16 v[36:39], v[28:31], v[248:255], v67\n"
    "v_smfmac_f32_16x16x64_bf16 v[40:43], v[8:11], a[16:23], v67\n"
    "v_add_f32 v32, v32, v34\n"
    "v_add_f32_e64 v33, v33, v35\n"
    "v_smfmac_f32_16x16x64_bf16 v[44:47], v[8:11], a[80:87], v67\n"
    "ds_read_b128 a[16:19], v116 offset:8192\n"
    "v_cvt_pk_bf16_f32 v126, v32, v33\n"
    "v_add_f32 v36, v36, v38\n"
    "v_add_f32 v37, v37, v39\n"
    "v_cvt_pk_bf16_f32 v127, v36, v37\n"
    "v_smfmac_f32_16x16x64_bf16 v[40:43], v[16:19], a[32:39], v67\n"
    "ds_write_b64 v114, v[126:127] offset:8192\n"
    "v_smfmac_f32_16x16x64_bf16 v[44:47], v[16:19], a[96:103], v67\n"
    "ds_read_b128 a[20:23], v116 offset:9216\n"
    "v_cmp_u_f32_e64 vcc, v32, v33\n"
    "v_smfmac_f32_16x16x64_bf16 v[40:43], v[24:27], a[48:55], v67\n"
    "ds_read_b128 a[80:83], v116 offset:12288\n"
    "v_mov_b32_e64 v87, s76\n"
    "v_smfmac_f32_16x16x64_bf16 v[44:47], v[24:27], a[112:119], v67\n"
    "s_cbranch_vccnz L_slow0%=\n"
    "L_go0%=:\n"
    "v_smfmac_f32_16x16x64_bf16 v[40:43], v[12:15], a[24:31], v67\n"
    "s_waitcnt lgkmcnt(2)\n"
    "s_barrier\n"
    "ds_read_b64_tr_b16 v[32:33], v112 offset:8192\n"
    "v_smfmac_f32_16x16x64_bf16 v[44:47], v[12:15], a[88:95], v67\n"
    "ds_read_b64_tr_b16 v[34:35], v113 offset:8192\n"
    "ds_read_b64_tr_b16 v[36:37], v112 offset:9216\n"
    "v_smfmac_f32_16x16x64_bf16 v[40:43], v[20:23], a[40:47], v67\n"
    "ds_read_b64_tr_b16 v[38:39], v113 offset:9216\n"
    "v_smfmac_f32_16x16x64_bf16 v[44:47], v[20:23], a[104:111], v67\n"
    "global_load_dwordx4 v[96:99], v71, s[58:59] offset:-2048 sc1\n"
    "v_smfmac_f32_16x16x64_bf16 v[40:43], v[28:31], a[56:63], v67\n"
    "ds_read_b128 a[84:87], v116 offset:13312\n"
    "v_smfmac_f32_16x16x64_bf16 v[44:47], v[28:31], a[120:127], v67\n"
    "s_waitcnt lgkmcnt(1)\n"
    "ds_read_b128 a[32:35], v116 offset:10240\n"
    "v_mfma_f32_16x16x32_bf16 v[88:91], v[56:59], v[32:35], 0\n"
    "v_add_f32 v40, v40, v42\n"
    "v_add_f32 v41, v41, v43\n"
    "v_mfma_f32_16x16x32_bf16 v[88:91], v[60:63], v[36:39], v[88:91]\n"
    "v_cvt_pk_bf16_f32 v126, v40, v41\n"
    "v_add_f32 v44, v44, v46\n"
    "v_add_f32 v45, v45, v47\n"
    "v_cvt_pk_bf16_f32 v127, v44, v45\n"
    "v_smfmac_f32_16x16x64_bf16 v[52:55], v[0:3], a[192:199], v67\n"
    "ds_write_b64 v115, v[126:127] offset:10240\n"
    "v_add_f32_dpp v88, v90, v88 quad_perm:[2,3,2,3] row_mask:0xf bank_mask:0xf\n"
    "v_add_f32_dpp v89, v91, v89 quad_perm:[2,3,2,3] row_mask:0xf bank_mask:0xf\n"
    "v_smfmac_f32_16x16x64_bf16 v[48:51], v[4:7], a[136:143], v67\n"
    "ds_write_b64 v75, v[88:89] offset:14336\n"
    "v_smfmac_f32_16x16x64_bf16 v[52:55], v[4:7], a[200:207], v67\n"
    "global_load_dwordx4 v[100:103], v71, s[58:59] offset:2048 sc1\n"
    "v_smfmac_f32_16x16x64_bf16 v[48:51], v[8:11], a[144:151], v67\n"
    "global_load_lds_dwordx4 v71, s[60:61] offset:0 sc1\n"
    "v_smfmac_f32_16x16x64_bf16 v[52:55], v[8:11], a[208:215], v67\n"
    "global_load_lds_dwordx4 v71, s[60:61] offset:1024 sc1\n"
    "v_smfmac_f32_16x16x64_bf16 v[48:51], v[16:19], a[160:167], v67\n"
    "s_waitcnt lgkmcnt(0)\n"
    "s_barrier\n"
    "ds_read_b64_tr_b16 v[40:41], v112 offset:10240\n"
    "v_smfmac_f32_16x16x64_bf16 v[52:55], v[16:19], a[224:231], v67\n"
    "ds_read_b64_tr_b16 v[42:43], v113 offset:10240\n"
    "ds_read_b64_tr_b16 v[44:45], v112 offset:11264\n"
    "v_smfmac_f32_16x16x64_bf16 v[48:51], v[24:27], a[176:183], v67\n"
    "ds_read_b64_tr_b16 v[46:47], v113 offset:11264\n"
    "ds_read_b128 v[88:91], v71 offset:14336\n"
    "v_smfmac_f32_16x16x64_bf16 v[52:55], v[24:27], a[240:247], v67\n"
    "s_cmp_eq_u32 s43, 63\n"
    "ds_read_b128 a[36:39], v116 offset:11264\n"
    "v_smfmac_f32_16x16x64_bf16 v[48:51], v[12:15], a[152:159], v67\n"
    "s_waitcnt lgkmcnt(4)\n"
    "v_mfma_f32_16x16x32_bf16 v[92:95], v[40:43], v[32:35], 0\n"
    "s_waitcnt lgkmcnt(2)\n"
    "ds_read_b128 a[96:99], v116 offset:14336\n"
    "v_mfma_f32_16x16x32_bf16 v[92:95], v[44:47], v[36:39], v[92:95]\n"
    "s_cbranch_scc1 L_last0%=\n"
    "s_waitcnt lgkmcnt(2)\n"
    "v_max3_f32 v122, v88, v89, v90\n"
    "v_max_f32 v122, v122, v91\n"
    "v_smfmac_f32_16x16x64_bf16 v[52:55], v[12:15], a[216:223], v67\n"
    "ds_read_b128 a[100:103], v116 offset:15360\n"
    "v_max_f32_dpp v123, v122, v122 quad_perm:[2,3,0,1] row_mask:0xf bank_mask:0xf\n"
    "v_add_f32_dpp v92, v94, v92 quad_perm:[2,3,2,3] row_mask:0xf bank_mask:0xf\n"
    "v_add_f32_dpp v93, v95, v93 quad_perm:[2,3,2,3] row_mask:0xf bank_mask:0xf\n"
    "v_max3_f32 v122, v123, v92, v93\n"
    "v_smfmac_f32_16x16x64_bf16 v[48:51], v[20:23], a[168:175], v67\n"
    "v_mul_f32_dpp v121, v122, v87 quad_perm:[0,1,0,1] row_mask:0xf bank_mask:0xf\n"
    "v_mul_f32_dpp v123, v122, v87 quad_perm:[0,1,0,1] row_mask:0xf bank_mask:0xf\n"
    "v_smfmac_f32_16x16x64_bf16 v[52:55], v[20:23], a[232:239], v67\n"
    "v_permlane16_swap_b32 v123, v121\n"
    "v_max_f32 v122, v123, v121\n"
    "v_max_f32_e64 v123, v123, v121\n"
    "v_smfmac_f32_16x16x64_bf16 v[48:51], v[28:31], a[184:191], v67\n"
    "v_smfmac_f32_16x16x64_bf16 v[52:55], v[28:31], a[248:255], v67\n"
    "v_permlane32_swap_b32 v122, v123\n"
    "v_max_f32 v122, v122, v123\n"
    "v_pk_fma_f32 v[92:93], v[92:93], s[76:77], v[122:123] op_sel_hi:[1,1,0] neg_lo:[0,0,1] neg_hi:[0,0,1]\n"
    "v_pk_fma_f32 v[88:89], v[88:89], s[76:77], v[122:123] op_sel_hi:[1,1,0] neg_lo:[0,0,1] neg_hi:[0,0,1]\n"
    "v_pk_fma_f32 v[90:91], v[90:91], s[76:77], v[122:123] op_sel_hi:[1,1,0] neg_lo:[0,0,1] neg_hi:[0,0,1]\n"
    "v_exp_f32 v92, v92\n"
    "v_exp_f32 v93, v93\n"
    "v_exp_f32 v88, v88\n"
    "v_exp_f32 v89, v89\n"
    "v_exp_f32 v90, v90\n"
    "v_exp_f32 v91, v91\n"
    "v_pk_add_f32 v[48:49], v[48:49], v[50:51]\n"
    "v_pk_add_f32 v[52:53], v[52:53], v[54:55]\n"
    "v_cvt_pk_bf16_f32 v126, v92, v93\n"
    "v_cvt_pk_bf16_f32 v88, v88, v89\n"
    "v_cvt_pk_bf16_f32 v90, v90, v91\n"
    "v_cndmask_b32 v89, 0, v126, s[72:73]\n"
    "v_cndmask_b32 v91, 0, v126, s[74:75]\n"
    "v_cvt_pk_bf16_f32 v66, v48, v49\n"
    "v_cvt_pk_bf16_f32 v70, v48, v49\n"
    "v_cvt_pk_bf16_f32 v74, v52, v53\n"
    "v_cvt_pk_bf16_f32 v78, v52, v53\n"
    "v_smfmac_f32_16x16x64_bf16 v[104:107], v[88:91], v[64:71], v67\n"
    "v_mov_b64_e64 v[0:1], 0\n"
    "v_smfmac_f32_16x16x64_bf16 v[108:111], v[88:91], v[72:79], v67\n"
    "v_mov_b64_e64 v[2:3], 0\n"
    "v_mfma_f32_16x16x32_bf16 v[84:87], v[88:91], v[80:83], 0\n"
    "v_add_f32 v104, v104, v106\n"
    "v_add_f32 v105, v105, v107\n"
    "v_cvt_pk_bf16_f32 v104, v104, v105\n"
    "v_add_f32 v108, v108, v110\n"
    "v_add_f32 v109, v109, v111\n"
    "v_cvt_pk_bf16_f32 v105, v108, v109\n"
    "ds_write_b64 v114, v[104:105] offset:12288\n"
    "v_pk_add_f32 v[124:125], v[84:85], v[86:87]\n"
    "v_rcp_f32 v124, v124\n"
    "v_rcp_f32 v125, v125\n"
    "v_mov_b64 v[4:5], 0\n"
    "v_mov_b64 v[6:7], 0\n"
    "v_mov_b64 v[8:9], 0\n"
    "v_mov_b64 v[10:11], 0\n"
    "v_mov_b64 v[12:13], 0\n"
    "v_mov_b64 v[14:15], 0\n"
    "v_mfma_f32_16x16x32_bf16 v[92:95], v[80:83], v[80:83], 0\n"
    "s_waitcnt lgkmcnt(0)\n"
    "s_barrier\n"
    "ds_read_b64_tr_b16 v[32:33], v112 offset:12288\n"
    "v_mfma_f32_16x16x32_bf16 v[92:95], v[80:83], v[80:83], 0\n"
    "ds_read_b64_tr_b16 v[34:35], v113 offset:12288\n"
    "ds_read_b64_tr_b16 v[36:37], v112 offset:13312\n"
    "v_mfma_f32_16x16x32_bf16 v[92:95], v[80:83], v[80:83], 0\n"
    "ds_read_b64_tr_b16 v[38:39], v113 offset:13312\n"
    "v_mfma_f32_16x16x32_bf16 v[92:95], v[80:83], v[80:83], 0\n"
    "s_waitcnt lgkmcnt(2)\n"
    "v_smfmac_f32_16x16x64_bf16 v[0:3], v[32:35], a[0:7], v67\n"
    "v_smfmac_f32_16x16x64_bf16 v[4:7], v[32:35], a[64:71], v67\n"
    "s_waitcnt lgkmcnt(0)\n"
    "v_smfmac_f32_16x16x64_bf16 v[0:3], v[36:39], a[8:15], v67\n"
    "v_smfmac_f32_16x16x64_bf16 v[4:7], v[36:39], a[72:79], v67\n"
    "v_smfmac_f32_16x16x64_bf16 v[8:11], v[32:35], a[16:23], v67\n"
    "v_add_f32_e64 v0, v0, v2\n"
    "v_smfmac_f32_16x16x64_bf16 v[12:15], v[32:35], a[80:87], v67\n"
    "v_add_f32_e64 v1, v1, v3\n"
    "v_smfmac_f32_16x16x64_bf16 v[8:11], v[36:39], a[32:39], v67\n"
    "v_add_f32_e64 v4, v4, v6\n"
    "v_smfmac_f32_16x16x64_bf16 v[12:15], v[36:39], a[96:103], v67\n"
    "v_add_f32 v5, v5, v7\n"
    "v_mul_f32 v0, v0, v124\n"
    "v_mul_f32_e64 v1, v1, v125\n"
    "v_cvt_pk_bf16_f32 v88, v0, v1\n"
    "v_pk_mul_f32 v[4:5], v[4:5], v[124:125]\n"
    "v_pk_add_f32 v[8:9], v[8:9], v[10:11]\n"
    "v_pk_add_f32 v[12:13], v[12:13], v[14:15]\n"
    "v_cvt_pk_bf16_f32 v89, v4, v5\n"
    "v_pk_mul_f32 v[8:9], v[8:9], v[124:125]\n"
    "v_pk_mul_f32 v[12:13], v[12:13], v[124:125]\n"
    "v_cvt_pk_bf16_f32 v90, v8, v9\n"
    "v_cvt_pk_bf16_f32 v91, v12, v13\n"
    "global_store_dwordx4 v117, v[88:91], s[44:45] sc1\n"
    "s_add_u32 s43, s43, 1\n"
    "s_sleep 3\n"
    "ds_read_b128 a[0:3], v116 offset:16384\n"
    "ds_read_b128 a[4:7], v116 offset:17408\n"
    "ds_read_b128 a[64:67], v116 offset:18432\n"
    "ds_read_b128 a[68:71], v116 offset:19456\n"
    "ds_read_b128 a[8:11], v116 offset:20480\n"
    "ds_read_b128 a[12:15], v116 offset:21504\n"
    "ds_read_b128 a[72:75], v116 offset:22528\n"
    "ds_read_b128 a[76:79], v116 offset:23552\n"
    "ds_read_b128 a[16:19], v116 offset:24576\n"
    "ds_read_b128 a[20:23], v116 offset:25600\n"
    "ds_read_b128 a[80:83], v116 offset:26624\n"
    "ds_read_b128 a[84:87], v116 offset:27648\n"
    "ds_read_b128 a[32:35], v116 offset:28672\n"
    "s_waitcnt lgkmcnt(8)\n"
    "ds_read_b128 a[36:39], v116 offset:29696\n"
    "ds_read_b128 a[96:99], v116 offset:30720\n"
    "ds_read_b128 a[100:103], v116 offset:31744\n"
    "s_and_b32 s11, s43, 3\n"
    "s_lshl_b32 s11, s11, 17\n"
    "s_add_u32 s54, s48, s11\n"
    "s_addc_u32 s55, s49, 0\n"
    "s_add_u32 s11, s43, -1\n"
    "s_and_b32 s11, s11, 3\n"
    "s_cselect_b32 s11, 0, 0x2000\n"
    "s_add_u32 s62, s62, 0x80000\n"
    "s_addc_u32 s63, s63, 0\n"
    "s_xor_b32 s62, s62, s11\n"
    "s_add_u32 s11, s43, 1\n"
    "s_and_b32 s11, s11, 3\n"
    "s_cselect_b32 s11, 0, 0x2000\n"
    "s_add_u32 s58, s58, 0x80000\n"
    "s_addc_u32 s59, s59, 0\n"
    "s_xor_b32 s58, s58, s11\n"
    "s_add_u32 s60, s60, 0x80000\n"
    "s_addc_u32 s61, s61, 0\n"
    "s_xor_b32 s60, s60, s11\n"
    "s_xor_b32 s44, s44, 0x8000\n"
    "v_mov_b64 v[32:33], 0\n"
    "v_mov_b64 v[34:35], 0\n"
    "v_mov_b64 v[36:37], 0\n"
    "v_mov_b64 v[38:39], 0\n"
    "v_mov_b64 v[48:49], 0\n"
    "v_mov_b64 v[50:51], 0\n"
    "v_mov_b64 v[52:53], 0\n"
    "v_mov_b64 v[54:55], 0\n"
    "v_mov_b64 v[104:105], 0\n"
    "v_mov_b64 v[106:107], 0\n"
    "v_mov_b64 v[108:109], 0\n"
    "v_mov_b64 v[110:111], 0\n"
    "v_or_b32 v84, 1, s96\n"
    "v_mov_b32 v85, s97\n"
    "v_add_u32 v86, s43, v84\n"
    "s_cmp_lg_u32 s43, 54\n"
    "s_cbranch_scc1 L_fl%=\n"
    "s_mov_b64 exec, 1\n"
    "global_store_dwordx2 v71, v[84:85], s[64:65] sc1\n"
    "s_mov_b64 exec, -1\n"
    "s_add_u32 s64, s64, 8192\n"
    "s_addc_u32 s65, s65, 0\n"
    "L_fl%=:\n"
    "s_waitcnt vmcnt(1)\n"
    "ds_read_b64 v[64:65], v116 offset:32768\n"
    "ds_read_b64 v[68:69], v116 offset:32776\n"
    "ds_read_b64 v[72:73], v116 offset:33792\n"
    "ds_read_b64 v[76:77], v116 offset:33800\n"
    "v_mov_b64 v[56:57], v[96:97]\n"
    "v_mov_b64 v[58:59], v[98:99]\n"
    "v_mov_b64 v[60:61], v[100:101]\n"
    "v_mov_b64 v[62:63], v[102:103]\n"
    "s_waitcnt lgkmcnt(0)\n"
    "s_barrier\n"
    "global_load_dwordx4 v[0:3], v71, s[54:55] offset:0 sc1\n"
    "s_nop 3\n"
    "global_load_dwordx4 v[4:7], v71, s[54:55] offset:1024 sc1\n"
    "s_nop 15\n"
    "s_cmp_lg_u32 s42, 0\n"
    "s_cbranch_scc1 L_vmb0%=\n"
    "s_mov_b32 exec_hi, 0\n"
    "global_store_dwordx4 v71, v[40:43], s[62:63] offset:0 sc1\n"
    "s_mov_b64 exec, -1\n"
    "s_branch L_kvb0%=\n"
    "L_vmb0%=:\n"
    "v_mov_b32 v126, v66\n"
    "v_mov_b32 v127, v74\n"
    "global_store_dwordx2 v79, v[126:127], s[62:63] offset:0 sc1\n"
    "L_kvb0%=:\n"
    "s_mov_b64 exec, 1\n"
    "global_store_dwordx2 v71, v[84:85], s[64:65] sc1\n"
    "s_add_u32 s64, s64, 8192\n"
    "s_addc_u32 s65, s65, 0\n"
    "s_mov_b64 exec, -1\n"
    "s_nop 1\n"
    "v_mov_b64 v[40:41], 0\n"
    "v_mov_b64 v[42:43], 0\n"
    "v_mov_b64 v[44:45], 0\n"
    "v_mov_b64 v[46:47], 0\n"
    "s_branch L_top0%=\n"
    "L_slow0%=:\n"
    "v_cmp_eq_u32 s[78:79], s50, v0\n"
    "v_cmp_eq_u32 s[80:81], s50, v1\n"
    "v_cmp_eq_u32 s[82:83], s50, v2\n"
    "v_cmp_eq_u32 s[84:85], s50, v3\n"
    "v_cmp_eq_u32 s[86:87], s50, v4\n"
    "v_cmp_eq_u32 s[88:89], s50, v5\n"
    "v_cmp_eq_u32 s[52:53], s50, v6\n"
    "v_cmp_eq_u32 vcc, s50, v7\n"
    "global_load_dwordx4 v[0:3], v71, s[54:55] offset:0 sc1\n"
    "global_load_dwordx4 v[4:7], v71, s[54:55] offset:1024 sc1\n"
    "s_waitcnt lgkmcnt(0)\n"
    "s_barrier\n"
    "s_or_b64 vcc, vcc, s[78:79]\n"
    "s_or_b64 vcc, vcc, s[80:81]\n"
    "s_or_b64 vcc, vcc, s[82:83]\n"
    "s_or_b64 vcc, vcc, s[84:85]\n"
    "s_or_b64 vcc, vcc, s[86:87]\n"
    "s_or_b64 vcc, vcc, s[88:89]\n"
    "s_or_b64 vcc, vcc, s[52:53]\n"
    "s_cbranch_scc1 L_redo0%=\n"
    "v_cmp_eq_u32 s[78:79], s50, v8\n"
    "v_cmp_eq_u32 s[80:81], s50, v9\n"
    "v_cmp_eq_u32 s[82:83], s50, v10\n"
    "v_cmp_eq_u32 s[84:85], s50, v11\n"
    "v_cmp_eq_u32 s[86:87], s50, v16\n"
    "v_cmp_eq_u32 s[88:89], s50, v17\n"
    "v_cmp_eq_u32 s[52:53], s50, v18\n"
    "v_cmp_eq_u32 vcc, s50, v19\n"
    "s_nop 0\n"
    "s_or_b64 vcc, vcc, s[78:79]\n"
    "s_or_b64 vcc, vcc, s[80:81]\n"
    "s_or_b64 vcc, vcc, s[82:83]\n"
    "s_or_b64 vcc, vcc, s[84:85]\n"
    "s_or_b64 vcc, vcc, s[86:87]\n"
    "s_or_b64 vcc, vcc, s[88:89]\n"
    "s_or_b64 vcc, vcc, s[52:53]\n"
    "s_cbranch_scc1 L_redo0%=\n"
    "v_cmp_eq_u32 s[78:79], s50, v24\n"
    "v_cmp_eq_u32 s[80:81], s50, v25\n"
    "v_cmp_eq_u32 s[82:83], s50, v26\n"
    "v_cmp_eq_u32 s[84:85], s50, v27\n"
    "v_cmp_eq_u32 s[86:87], s50, v12\n"
    "v_cmp_eq_u32 s[88:89], s50, v13\n"
    "v_cmp_eq_u32 s[52:53], s50, v14\n"
    "v_cmp_eq_u32 vcc, s50, v15\n"
    "s_nop 0\n"
    "s_or_b64 vcc, vcc, s[78:79]\n"
    "s_or_b64 vcc, vcc, s[80:81]\n"
    "s_or_b64 vcc, vcc, s[82:83]\n"
    "s_or_b64 vcc, vcc, s[84:85]\n"
    "s_or_b64 vcc, vcc, s[86:87]\n"
    "s_or_b64 vcc, vcc, s[88:89]\n"
    "s_or_b64 vcc, vcc, s[52:53]\n"
    "s_cbranch_scc1 L_redo0%=\n"
    "v_cmp_eq_u32 s[78:79], s50, v20\n"
    "v_cmp_eq_u32 s[80:81], s50, v21\n"
    "v_cmp_eq_u32 s[82:83], s50, v22\n"
    "v_cmp_eq_u32 s[84:85], s50, v23\n"
    "v_cmp_eq_u32 s[86:87], s50, v28\n"
    "v_cmp_eq_u32 s[88:89], s50, v29\n"
    "v_cmp_eq_u32 s[52:53], s50, v30\n"
    "v_cmp_eq_u32 vcc, s50, v31\n"
    "s_nop 0\n"
    "s_or_b64 vcc, vcc, s[78:79]\n"
    "s_or_b64 vcc, vcc, s[80:81]\n"
    "s_or_b64 vcc, vcc, s[82:83]\n"
    "s_or_b64 vcc, vcc, s[84:85]\n"
    "s_or_b64 vcc, vcc, s[86:87]\n"
    "s_or_b64 vcc, vcc, s[88:89]\n"
    "s_or_b64 vcc, vcc, s[52:53]\n"
    "s_cbranch_scc1 L_redo0%=\n"
    "s_waitcnt vmcnt(0)\n"
    "s_branch L_go0%=\n"
    "L_redo0%=:\n"
    "ds_read_b128 a[0:3], v116 offset:16384\n"
    "ds_read_b128 a[4:7], v116 offset:17408\n"
    "ds_read_b128 a[64:67], v116 offset:18432\n"
    "ds_read_b128 a[68:71], v116 offset:19456\n"
    "ds_read_b128 a[8:11], v116 offset:20480\n"
    "ds_read_b128 a[12:15], v116 offset:21504\n"
    "ds_read_b128 a[72:75], v116 offset:22528\n"
    "ds_read_b128 a[76:79], v116 offset:23552\n"
    "ds_read_b128 a[16:19], v116 offset:24576\n"
    "ds_read_b128 a[20:23], v116 offset:25600\n"
    "ds_read_b128 a[80:83], v116 offset:26624\n"
    "ds_read_b128 a[84:87], v116 offset:27648\n"
    "ds_read_b128 a[32:35], v116 offset:28672\n"
    "ds_read_b128 a[36:39], v116 offset:29696\n"
    "s_waitcnt lgkmcnt(8)\n"
    "ds_read_b128 a[96:99], v116 offset:30720\n"
    "ds_read_b128 a[100:103], v116 offset:31744\n"
    "v_mov_b64 v[32:33], 0\n"
    "v_mov_b64 v[34:35], 0\n"
    "v_mov_b64 v[36:37], 0\n"
    "v_mov_b64 v[38:39], 0\n"
    "v_mov_b64 v[40:41], 0\n"
    "v_mov_b64 v[42:43], 0\n"
    "v_mov_b64 v[44:45], 0\n"
    "v_mov_b64 v[46:47], 0\n"
    "v_mov_b64 v[48:49], 0\n"
    "v_mov_b64 v[50:51], 0\n"
    "v_mov_b64 v[52:53], 0\n"
    "v_mov_b64 v[54:55], 0\n"
    "s_add_u32 s90, s90, 1\n"
    "s_and_b32 s78, s90, 0xff\n"
    "s_cmp_lg_u32 s78, 0\n"
    "s_cbranch_scc1 L_re0%=\n"
    "v_mov_b32 v126, 0\n"
    "global_load_dwordx2 v[126:127], v126, s[68:69] sc1\n"
    "s_waitcnt vmcnt(0)\n"
    "v_cmp_eq_u64 vcc, s[96:97], v[126:127]\n"
    "s_nop 1\n"
    "s_cbranch_vccnz L_bail_g0%=\n"
    "s_cmp_lt_u32 s90, 0x40000\n"
    "s_cbranch_scc1 L_re0%=\n"
    "v_mov_b32 v126, s96\n"
    "v_mov_b32 v127, s97\n"
    "s_mov_b64 exec, 1\n"
    "global_store_dwordx2 v71, v[126:127], s[68:69] sc1\n"
    "s_mov_b64 exec, -1\n"
    "L_bail_g0%=:\n"
    "s_branch L_go0%=\n"
    "L_re0%=:\n"
    "s_waitcnt vmcnt(0) lgkmcnt(0)\n"
    "s_branch L_top0%=\n"
    "L_last0%=:\n"
    "s_mov_b64 exec, 1\n"
    "global_store_dwordx2 v71, v[84:85], s[64:65] sc1\n"
    "s_mov_b64 exec, -1\n"
    "v_smfmac_f32_16x16x64_bf16 v[52:55], v[12:15], a[216:223], v67\n"
    "v_smfmac_f32_16x16x64_bf16 v[48:51], v[20:23], a[168:175], v67\n"
    "v_smfmac_f32_16x16x64_bf16 v[52:55], v[20:23], a[232:239], v67\n"
    "v_smfmac_f32_16x16x64_bf16 v[48:51], v[28:31], a[184:191], v67\n"
    "v_smfmac_f32_16x16x64_bf16 v[52:55], v[28:31], a[248:255], v67\n"
    "s_add_u32 s11, s43, 0\n"
    "s_and_b32 s11, s11, 3\n"
    "s_cselect_b32 s11, 0, 0x2000\n"
    "s_add_u32 s62, s62, 0x80000\n"
    "s_addc_u32 s63, s63, 0\n"
    "s_xor_b32 s62, s62, s11\n"
    "v_pk_add_f32 v[48:49], v[48:49], v[50:51]\n"
    "s_nop 0\n"
    "v_pk_add_f32 v[52:53], v[52:53], v[54:55]\n"
    "v_cvt_pk_bf16_f32 v66, v48, v49\n"
    "v_cvt_pk_bf16_f32 v74, v52, v53\n"
    "s_cmp_lg_u32 s42, 0\n"
    "s_cbranch_scc1 L_vmc0%=\n"
    "s_mov_b32 exec_hi, 0\n"
    "global_store_dwordx4 v71, v[40:43], s[62:63] offset:0 sc1\n"
    "s_mov_b64 exec, -1\n"
    "s_branch L_kvc0%=\n"
    "L_vmc0%=:\n"
    "v_mov_b32 v126, v66\n"
    "v_mov_b32 v127, v74\n"
    "global_store_dwordx2 v79, v[126:127], s[62:63] offset:0 sc1\n"
    "L_kvc0%=:\n"
    "s_waitcnt vmcnt(0) lgkmcnt(0)\n"
    "s_endpgm\n"
    ".p2align 6\n"
    "L_poll1%=:\n"
    "global_load_dwordx4 v[8:11], v71, s[54:55] offset:0 sc1\n"
    "global_load_dwordx4 v[12:15], v71, s[54:55] offset:1024 sc1\n"
    "s_memtime s[16:17]\n"
    "s_mov_b32 s38, 0\n"
    "s_waitcnt lgkmcnt(0)\n"
    "s_add_u32 s14, s16, s46\n"
    "s_andn2_b32 s14, s14, 1\n"
    "s_or_b32 s14, s14, s38\n"
    "s_mov_b64 exec, 15\n"
    "v_mov_b32 v87, s14\n"
    "global_store_dwordx4 v118, v[84:87], s[56:57] sc1\n"
    "s_mov_b64 exec, -1\n"
    "s_mov_b32 m0, 86272\n"
    "s_waitcnt vmcnt(0)\n"
    "s_branch L_top1%=\n"
    ".p2align 6\n"
    "L_top1%=:\n"
    "s_waitcnt vmcnt(3)\n"
    "v_smfmac_f32_16x16x64_bf16 v[40:43], v[8:11], a[16:23], v67\n"
    "ds_write_b128 v71, v[8:11] offset:2048\n"
    "v_smfmac_f32_16x16x64_bf16 v[44:47], v[8:11], a[80:87], v67\n"
    "v_smfmac_f32_16x16x64_bf16 v[32:35], v[8:11], v[160:167], v67\n"
    "v_smfmac_f32_16x16x64_bf16 v[36:39], v[8:11], v[168:175], v67\n"
    "s_waitcnt vmcnt(2)\n"
    "v_smfmac_f32_16x16x64_bf16 v[40:43], v[12:15], a[24:31], v67\n"
    "ds_write_b128 v71, v[12:15] offset:3072\n"
    "s_waitcnt lgkmcnt(1)\n"
    "s_barrier\n"
    "ds_read_b128 v[0:3], v71 offset:0\n"
    "v_smfmac_f32_16x16x64_bf16 v[44:47], v[12:15], a[88:95], v67\n"
    "ds_read_b128 v[24:27], v71 offset:6144\n"
    "v_smfmac_f32_16x16x64_bf16 v[32:35], v[12:15], v[176:183], v67\n"
    "ds_read_b128 v[16:19], v71 offset:4096\n"
    "v_smfmac_f32_16x16x64_bf16 v[36:39], v[12:15], v[184:191], v67\n"
    "s_waitcnt lgkmcnt(3)\n"
    "s_barrier\n"
    "v_smfmac_f32_16x16x64_bf16 v[48:51], v[8:11], a[144:151], v67\n"
    "s_waitcnt lgkmcnt(2)\n"
    "ds_read_b128 v[4:7], v71 offset:1024\n"
    "v_smfmac_f32_16x16x64_bf16 v[32:35], v[0:3], v[128:135], v67\n"
    "ds_read_b128 v[28:31], v71 offset:7168\n"
    "v_smfmac_f32_16x16x64_bf16 v[36:39], v[0:3], v[136:143], v67\n"
    "s_waitcnt lgkmcnt(2)\n"
    "ds_read_b128 v[20:23], v71 offset:5120\n"
    "v_smfmac_f32_16x16x64_bf16 v[32:35], v[24:27], v[224:231], v67\n"
    "s_memtime s[6:7]\n"
    "ds_read_b128 a[16:19], v116 offset:0\n"
    "v_smfmac_f32_16x16x64_bf16 v[36:39], v[24:27], v[232:239], v67\n"
    "s_mov_b32 s78, 0x12345\n"
    "ds_read_b128 a[20:23], v116 offset:1024\n"
    "v_smfmac_f32_16x16x64_bf16 v[32:35], v[16:19], v[192:199], v67\n"
    "s_mov_b32 s78, 0x12345\n"
    "ds_read_b128 a[80:83], v116 offset:4096\n"
    "v_smfmac_f32_16x16x64_bf16 v[36:39], v[16:19], v[200:207], v67\n"
    "s_waitcnt lgkmcnt(5)\n"
    "ds_read_b128 a[84:87], v116 offset:5120\n"
    "v_smfmac_f32_16x16x64_bf16 v[32:35], v[4:7], v[144:151], v67\n"
    "s_sub_i32 s2, s6, s16\n"
    "ds_read_b128 a[24:27], v116 offset:2048\n"
    "v_smfmac_f32_16x16x64_bf16 v[36:39], v[4:7], v[152:159], v67\n"
    "s_waitcnt lgkmcnt(5)\n"
    "ds_read_b128 a[28:31], v116 offset:3072\n"
    "v_smfmac_f32_16x16x64_bf16 v[32:35], v[28:31], v[240:247], v67\n"
    "s_cmp_gt_i32 s2, s3\n"
    "ds_read_b128 a[88:91], v116 offset:6144\n"
    "v_smfmac_f32_16x16x64_bf16 v[36:39], v[28:31], v[248:255], v67\n"
    "ds_read_b128 a[92:95], v116 offset:7168\n"
    "v_smfmac_f32_16x16x64_bf16 v[32:35], v[20:23], v[208:215], v67\n"
    "s_cbranch_scc1 L_fix%=\n"
    "L_fixed%=:\n"
    "v_smfmac_f32_16x16x64_bf16 v[36:39], v[20:23], v[216:223], v67\n"
    "v_smfmac_f32_16x16x64_bf16 v[40:43], v[0:3], a[0:7], v67\n"
    "v_add_f32 v32, v32, v34\n"
    "v_add_f32_e64 v33, v33, v35\n"
    "v_smfmac_f32_16x16x64_bf16 v[44:47], v[0:3], a[64:71], v67\n"
    "ds_read_b128 a[0:3], v116 offset:8192\n"
    "v_cvt_pk_bf16_f32 v126, v32, v33\n"
    "v_add_f32 v36, v36, v38\n"
    "v_add_f32 v37, v37, v39\n"
    "v_cvt_pk_bf16_f32 v127, v36, v37\n"
    "v_smfmac_f32_16x16x64_bf16 v[40:43], v[24:27], a[48:55], v67\n"
    "ds_write_b64 v114, v[126:127] offset:8704\n"
    "v_smfmac_f32_16x16x64_bf16 v[44:47], v[24:27], a[112:119], v67\n"
    "ds_read_b128 a[4:7], v116 offset:9216\n"
    "v_cmp_u_f32_e64 vcc, v32, v33\n"
    "v_smfmac_f32_16x16x64_bf16 v[40:43], v[16:19], a[32:39], v67\n"
    "ds_read_b128 a[64:67], v116 offset:12288\n"
    "v_mov_b32_e64 v87, s76\n"
    "v_smfmac_f32_16x16x64_bf16 v[44:47], v[16:19], a[96:103], v67\n"
    "s_cbranch_vccnz L_slow1%=\n"
    "L_go1%=:\n"
    "v_smfmac_f32_16x16x64_bf16 v[40:43], v[4:7], a[8:15], v67\n"
    "s_waitcnt lgkmcnt(2)\n"
    "s_barrier\n"
    "ds_read_b64_tr_b16 v[32:33], v112 offset:8192\n"
    "v_smfmac_f32_16x16x64_bf16 v[44:47], v[4:7], a[72:79], v67\n"
    "ds_read_b64_tr_b16 v[34:35], v113 offset:8192\n"
    "ds_read_b64_tr_b16 v[36:37], v112 offset:9216\n"
    "v_smfmac_f32_16x16x64_bf16 v[40:43], v[28:31], a[56:63], v67\n"
    "ds_read_b64_tr_b16 v[38:39], v113 offset:9216\n"
    "v_smfmac_f32_16x16x64_bf16 v[44:47], v[28:31], a[120:127], v67\n"
    "global_load_dwordx4 v[96:99], v71, s[58:59] offset:-2048 sc1\n"
    "v_smfmac_f32_16x16x64_bf16 v[40:43], v[20:23], a[40:47], v67\n"
    "ds_read_b128 a[68:71], v116 offset:13312\n"
    "v_smfmac_f32_16x16x64_bf16 v[44:47], v[20:23], a[104:111], v67\n"
    "s_waitcnt lgkmcnt(1)\n"
    "ds_read_b128 a[48:51], v116 offset:10240\n"
    "v_mfma_f32_16x16x32_bf16 v[88:91], v[56:59], v[32:35], 0\n"
    "v_add_f32 v40, v40, v42\n"
    "v_add_f32 v41, v41, v43\n"
    "v_mfma_f32_16x16x32_bf16 v[88:91], v[60:63], v[36:39], v[88:91]\n"
    "v_cvt_pk_bf16_f32 v126, v40, v41\n"
    "v_add_f32 v44, v44, v46\n"
    "v_add_f32 v45, v45, v47\n"
    "v_cvt_pk_bf16_f32 v127, v44, v45\n"
    "v_smfmac_f32_16x16x64_bf16 v[52:55], v[8:11], a[208:215], v67\n"
    "ds_write_b64 v115, v[126:127] offset:10752\n"
    "v_add_f32_dpp v88, v90, v88 quad_perm:[2,3,2,3] row_mask:0xf bank_mask:0xf\n"
    "v_add_f32_dpp v89, v91, v89 quad_perm:[2,3,2,3] row_mask:0xf bank_mask:0xf\n"
    "v_smfmac_f32_16x16x64_bf16 v[48:51], v[12:15], a[152:159], v67\n"
    "ds_write_b64 v75, v[88:89] offset:14368\n"
    "v_smfmac_f32_16x16x64_bf16 v[52:55], v[12:15], a[216:223], v67\n"
    "global_load_dwordx4 v[100:103], v71, s[58:59] offset:2048 sc1\n"
    "v_smfmac_f32_16x16x64_bf16 v[48:51], v[0:3], a[128:135], v67\n"
    "global_load_lds_dwordx4 v71, s[60:61] offset:0 sc1\n"
    "v_smfmac_f32_16x16x64_bf16 v[52:55], v[0:3], a[192:199], v67\n"
    "global_load_lds_dwordx4 v71, s[60:61] offset:1024 sc1\n"
    "v_smfmac_f32_16x16x64_bf16 v[48:51], v[24:27], a[176:183], v67\n"
    "s_waitcnt lgkmcnt(0)\n"
    "s_barrier\n"
    "ds_read_b64_tr_b16 v[40:41], v112 offset:10240\n"
    "v_smfmac_f32_16x16x64_bf16 v[52:55], v[24:27], a[240:247], v67\n"
    "ds_read_b64_tr_b16 v[42:43], v113 offset:10240\n"
    "ds_read_b64_tr_b16 v[44:45], v112 offset:11264\n"
    "v_smfmac_f32_16x16x64_bf16 v[48:51], v[16:19], a[160:167], v67\n"
    "ds_read_b64_tr_b16 v[46:47], v113 offset:11264\n"
    "ds_read_b128 v[88:91], v71 offset:14336\n"
    "v_smfmac_f32_16x16x64_bf16 v[52:55], v[16:19], a[224:231], v67\n"
    "s_cmp_eq_u32 s43, 63\n"
    "ds_read_b128 a[52:55], v116 offset:11264\n"
    "v_smfmac_f32_16x16x64_bf16 v[48:51], v[4:7], a[136:143], v67\n"
    "s_waitcnt lgkmcnt(4)\n"
    "v_mfma_f32_16x16x32_bf16 v[92:95], v[40:43], v[32:35], 0\n"
    "s_waitcnt lgkmcnt(2)\n"
    "ds_read_b128 a[112:115], v116 offset:14336\n"
    "v_mfma_f32_16x16x32_bf16 v[92:95], v[44:47], v[36:39], v[92:95]\n"
    "s_cbranch_scc1 L_last1%=\n"
    "s_waitcnt lgkmcnt(2)\n"
    "v_max3_f32 v122, v88, v89, v90\n"
    "v_max_f32 v122, v122, v91\n"
    "v_smfmac_f32_16x16x64_bf16 v[52:55], v[4:7], a[200:207], v67\n"
    "ds_read_b128 a[116:119], v116 offset:15360\n"
    "v_max_f32_dpp v123, v122, v122 quad_perm:[2,3,0,1] row_mask:0xf bank_mask:0xf\n"
    "v_add_f32_dpp v92, v94, v92 quad_perm:[2,3,2,3] row_mask:0xf bank_mask:0xf\n"
    "v_add_f32_dpp v93, v95, v93 quad_perm:[2,3,2,3] row_mask:0xf bank_mask:0xf\n"
    "v_max3_f32 v122, v123, v92, v93\n"
    "v_smfmac_f32_16x16x64_bf16 v[48:51], v[28:31], a[184:191], v67\n"
    "v_mul_f32_dpp v121, v122, v87 quad_perm:[0,1,0,1] row_mask:0xf bank_mask:0xf\n"
    "v_mul_f32_dpp v123, v122, v87 quad_perm:[0,1,0,1] row_mask:0xf bank_mask:0xf\n"
    "v_smfmac_f32_16x16x64_bf16 v[52:55], v[28:31], a[248:255], v67\n"
    "v_permlane16_swap_b32 v123, v121\n"
    "v_max_f32 v122, v123, v121\n"
    "v_max_f32_e64 v123, v123, v121\n"
    "v_smfmac_f32_16x16x64_bf16 v[48:51], v[20:23], a[168:175], v67\n"
    "v_smfmac_f32_16x16x64_bf16 v[52:55], v[20:23], a[232:239], v67\n"
    "v_permlane32_swap_b32 v122, v123\n"
    "v_max_f32 v122, v122, v123\n"
    "v_pk_fma_f32 v[92:93], v[92:93], s[76:77], v[122:123] op_sel_hi:[1,1,0] neg_lo:[0,0,1] neg_hi:[0,0,1]\n"
    "v_pk_fma_f32 v[88:89], v[88:89], s[76:77], v[122:123] op_sel_hi:[1,1,0] neg_lo:[0,0,1] neg_hi:[0,0,1]\n"
    "v_pk_fma_f32 v[90:91], v[90:91], s[76:77], v[122:123] op_sel_hi:[1,1,0] neg_lo:[0,0,1] neg_hi:[0,0,1]\n"
    "v_exp_f32 v92, v92\n"
    "v_exp_f32 v93, v93\n"
    "v_exp_f32 v88, v88\n"
    "v_exp_f32 v89, v89\n"
    "v_exp_f32 v90, v90\n"
    "v_exp_f32 v91, v91\n"
    "v_pk_add_f32 v[48:49], v[48:49], v[50:51]\n"
    "v_pk_add_f32 v[52:53], v[52:53], v[54:55]\n"
    "v_cvt_pk_bf16_f32 v126, v92, v93\n"
    "v_cvt_pk_bf16_f32 v88, v88, v89\n"
    "v_cvt_pk_bf16_f32 v90, v90, v91\n"
    "v_cndmask_b32 v89, 0, v126, s[72:73]\n"
    "v_cndmask_b32 v91, 0, v126, s[74:75]\n"
    "v_cvt_pk_bf16_f32 v66, v48, v49\n"
    "v_cvt_pk_bf16_f32 v70, v48, v49\n"
    "v_cvt_pk_bf16_f32 v74, v52, v53\n"
    "v_cvt_pk_bf16_f32 v78, v52, v53\n"
    "v_smfmac_f32_16x16x64_bf16 v[104:107], v[88:91], v[64:71], v67\n"
    "v_mov_b64_e64 v[0:1], 0\n"
    "v_smfmac_f32_16x16x64_bf16 v[108:111], v[88:91], v[72:79], v67\n"
    "v_mov_b64_e64 v[2:3], 0\n"
    "v_mfma_f32_16x16x32_bf16 v[84:87], v[88:91], v[80:83], 0\n"
    "v_add_f32 v104, v104, v106\n"
    "v_add_f32 v105, v105, v107\n"
    "v_cvt_pk_bf16_f32 v104, v104, v105\n"
    "v_add_f32 v108, v108, v110\n"
    "v_add_f32 v109, v109, v111\n"
    "v_cvt_pk_bf16_f32 v105, v108, v109\n"
    "ds_write_b64 v114, v[104:105] offset:12800\n"
    "v_pk_add_f32 v[124:125], v[84:85], v[86:87]\n"
    "v_rcp_f32 v124, v124\n"
    "v_rcp_f32 v125, v125\n"
    "v_mov_b64 v[4:5], 0\n"
    "v_mov_b64 v[6:7], 0\n"
    "v_mov_b64 v[8:9], 0\n"
    "v_mov_b64 v[10:11], 0\n"
    "v_mov_b64 v[12:13], 0\n"
    "v_mov_b64 v[14:15], 0\n"
    "v_mfma_f32_16x16x32_bf16 v[92:95], v[80:83], v[80:83], 0\n"
    "s_waitcnt lgkmcnt(0)\n"
    "s_barrier\n"
    "ds_read_b64_tr_b16 v[32:33], v112 offset:12288\n"
    "v_mfma_f32_16x16x32_bf16 v[92:95], v[80:83], v[80:83], 0\n"
    "ds_read_b64_tr_b16 v[34:35], v113 offset:12288\n"
    "ds_read_b64_tr_b16 v[36:37], v112 offset:13312\n"
    "v_mfma_f32_16x16x32_bf16 v[92:95], v[80:83], v[80:83], 0\n"
    "ds_read_b64_tr_b16 v[38:39], v113 offset:13312\n"
    "v_mfma_f32_16x16x32_bf16 v[92:95], v[80:83], v[80:83], 0\n"
    "global_load_dwordx3 v[120:122], v119, s[12:13] offset:8 sc1\n"
    "s_waitcnt lgkmcnt(2)\n"
    "v_smfmac_f32_16x16x64_bf16 v[0:3], v[32:35], a[16:23], v67\n"
    "v_smfmac_f32_16x16x64_bf16 v[4:7], v[32:35], a[80:87], v67\n"
    "s_waitcnt lgkmcnt(0)\n"
    "v_smfmac_f32_16x16x64_bf16 v[0:3], v[36:39], a[24:31], v67\n"
    "v_smfmac_f32_16x16x64_bf16 v[4:7], v[36:39], a[88:95], v67\n"
    "v_smfmac_f32_16x16x64_bf16 v[8:11], v[32:35], a[0:7], v67\n"
    "v_add_f32_e64 v0, v0, v2\n"
    "v_smfmac_f32_16x16x64_bf16 v[12:15], v[32:35], a[64:71], v67\n"
    "v_add_f32_e64 v1, v1, v3\n"
    "v_smfmac_f32_16x16x64_bf16 v[8:11], v[36:39], a[48:55], v67\n"
    "v_add_f32_e64 v4, v4, v6\n"
    "v_smfmac_f32_16x16x64_bf16 v[12:15], v[36:39], a[112:119], v67\n"
    "v_add_f32 v5, v5, v7\n"
    "v_mul_f32 v0, v0, v124\n"
    "v_mul_f32_e64 v1, v1, v125\n"
    "v_cvt_pk_bf16_f32 v88, v0, v1\n"
    "v_pk_mul_f32 v[4:5], v[4:5], v[124:125]\n"
    "v_pk_add_f32 v[8:9], v[8:9], v[10:11]\n"
    "v_pk_add_f32 v[12:13], v[12:13], v[14:15]\n"
    "v_cvt_pk_bf16_f32 v89, v4, v5\n"
    "v_pk_mul_f32 v[8:9], v[8:9], v[124:125]\n"
    "v_pk_mul_f32 v[12:13], v[12:13], v[124:125]\n"
    "v_cvt_pk_bf16_f32 v90, v8, v9\n"
    "v_cvt_pk_bf16_f32 v91, v12, v13\n"
    "global_store_dwordx4 v117, v[88:91], s[44:45] sc1\n"
    "s_memtime s[8:9]\n"
    "s_add_u32 s43, s43, 1\n"
    "ds_read_b128 a[16:19], v116 offset:16384\n"
    "ds_read_b128 a[20:23], v116 offset:17408\n"
    "ds_read_b128 a[80:83], v116 offset:18432\n"
    "ds_read_b128 a[84:87], v116 offset:19456\n"
    "ds_read_b128 a[24:27], v116 offset:20480\n"
    "ds_read_b128 a[28:31], v116 offset:21504\n"
    "ds_read_b128 a[88:91], v116 offset:22528\n"
    "ds_read_b128 a[92:95], v116 offset:23552\n"
    "ds_read_b128 a[0:3], v116 offset:24576\n"
    "ds_read_b128 a[4:7], v116 offset:25600\n"
    "ds_read_b128 a[64:67], v116 offset:26624\n"
    "ds_read_b128 a[68:71], v116 offset:27648\n"
    "ds_read_b128 a[48:51], v116 offset:28672\n"
    "s_waitcnt lgkmcnt(8)\n"
    "ds_read_b128 a[52:55], v116 offset:29696\n"
    "ds_read_b128 a[112:115], v116 offset:30720\n"
    "ds_read_b128 a[116:119], v116 offset:31744\n"
    "s_and_b32 s11, s43, 3\n"
    "s_lshl_b32 s11, s11, 17\n"
    "s_add_u32 s54, s48, s11\n"
    "s_addc_u32 s55, s49, 0\n"
    "s_sub_u32 s19, s8, s16\n"
    "s_sub_i32 s2, s2, s15\n"
    "s_max_i32 s2, s2, -64\n"
    "s_min_i32 s2, s2, 64\n"
    "s_ashr_i32 s2, s2, 1\n"
    "s_add_i32 s15, s15, s2\n"
    "s_cmp_lt_u32 s43, 27\n"
    "s_cselect_b32 s46, 0, 0\n"
    "s_mov_b32 s47, s91\n"
    "s_waitcnt vmcnt(1)\n"
    "ds_read_b64 v[64:65], v116 offset:32768\n"
    "ds_read_b64 v[68:69], v116 offset:32776\n"
    "ds_read_b64 v[72:73], v116 offset:33792\n"
    "ds_read_b64 v[76:77], v116 offset:33800\n"
    "v_mov_b64 v[56:57], v[96:97]\n"
    "v_mov_b64 v[58:59], v[98:99]\n"
    "v_mov_b64 v[60:61], v[100:101]\n"
    "v_mov_b64 v[62:63], v[102:103]\n"
    "s_add_u32 s39, s96, s43\n"
    "v_cmp_eq_u32 s[52:53], s39, v122\n"
    "s_mov_b32 s11, 0\n"
    "L_lk%=:\n"
    "v_cmp_eq_u32 vcc, s39, v120\n"
    "v_and_b32 v122, 1, v121\n"
    "v_subrev_u32 v120, s14, v121\n"
    "v_cmp_eq_u32 s[100:101], 1, v122\n"
    "v_max_i32 v120, 0, v120\n"
    "v_cndmask_b32 v120, 0, v120, vcc\n"
    "s_and_b64 s[100:101], s[100:101], vcc\n"
    "s_not_b64 s[82:83], vcc\n"
    "v_max_u32_dpp v120, v120, v120 row_ror:1 row_mask:0xf bank_mask:0xf\n"
    "s_nop 1\n"
    "v_max_u32_dpp v120, v120, v120 row_ror:2 row_mask:0xf bank_mask:0xf\n"
    "s_nop 1\n"
    "v_max_u32_dpp v120, v120, v120 row_ror:4 row_mask:0xf bank_mask:0xf\n"
    "s_nop 1\n"
    "v_max_u32_dpp v120, v120, v120 row_ror:8 row_mask:0xf bank_mask:0xf\n"
    "s_memtime s[6:7]\n"
    "v_readlane_b32 s78, v120, 0\n"
    "v_readlane_b32 s79, v120, 16\n"
    "v_readlane_b32 s80, v120, 32\n"
    "v_readlane_b32 s81, v120, 48\n"
    "s_add_u32 s9, s14, 1030\n"
    "s_max_u32 s78, s78, s79\n"
    "s_max_u32 s80, s80, s81\n"
    "s_max_u32 s78, s78, s80\n"
    "s_min_u32 s78, s78, 0x4000\n"
    "s_add_u32 s9, s9, s78\n"
    "s_min_u32 s3, s78, 100\n"
    "s_add_u32 s3, s3, 30\n"
    "s_add_u32 s3, s3, s15\n"
    "s_sub_u32 s8, s9, s91\n"
    "s_sub_u32 s8, s8, s94\n"
    "s_waitcnt lgkmcnt(0)\n"
    "s_sub_i32 s78, s8, s6\n"
    "s_cmp_gt_i32 s78, 1300\n"
    "s_cbranch_scc1 L_far%=\n"
    "s_cmp_lg_u64 s[82:83], 0\n"
    "s_cbranch_scc1 L_miss%=\n"
    "L_near%=:\n"
    "s_cmp_lt_i32 s78, 640\n"
    "s_cbranch_scc1 L_sy%=\n"
    "L_wa%=:\n"
    "s_memtime s[6:7]\n"
    "s_waitcnt lgkmcnt(0)\n"
    "s_sub_i32 s78, s8, s6\n"
    "s_sub_u32 s79, s78, 540\n"
    "s_cmp_lt_u32 s79, 0x5000\n"
    "s_cbranch_scc1 L_wa%=\n"
    "L_sy%=:\n"
    "s_memtime s[6:7]\n"
    "s_memrealtime s[78:79]\n"
    "s_cmp_lg_u64 s[52:53], 0\n"
    "s_cselect_b64 s[100:101], 0, s[100:101]\n"
    "s_lshl_b32 s11, s0, 1\n"
    "s_sub_u32 s11, s11, s40\n"
    "s_memrealtime s[80:81]\n"
    "s_add_u32 s11, s11, 24\n"
    "s_min_u32 s11, s11, 700\n"
    "s_add_u32 s10, s0, 8\n"
    "s_cmp_eq_u32 s0, 0\n"
    "s_memrealtime s[82:83]\n"
    "s_cselect_b32 s10, 0, s10\n"
    "s_sub_i32 s0, s0, 4\n"
    "s_max_i32 s0, s0, s40\n"
    "s_cmp_lg_u64 s[52:53], 0\n"
    "s_memrealtime s[84:85]\n"
    "s_cselect_b32 s0, s11, s0\n"
    "s_cselect_b32 s40, s10, s40\n"
    "s_lshl_b32 s11, s1, 1\n"
    "s_sub_u32 s11, s11, s41\n"
    "s_memrealtime s[86:87]\n"
    "s_add_u32 s11, s11, 24\n"
    "s_min_u32 s11, s11, 700\n"
    "s_add_u32 s10, s1, 8\n"
    "s_cmp_eq_u32 s1, 0\n"
    "s_memrealtime s[88:89]\n"
    "s_cselect_b32 s10, 0, s10\n"
    "s_sub_i32 s1, s1, 4\n"
    "s_max_i32 s1, s1, s41\n"
    "s_cmp_lg_u64 s[100:101], 0\n"
    "s_cselect_b32 s1, s11, s1\n"
    "s_cselect_b32 s41, s10, s41\n"
    "s_mov_b32 s38, 0\n"
    "s_add_u32 s9, s9, s1\n"
    "s_add_u32 s46, s46, s0\n"
    "s_mov_b32 s10, 0\n"
    "s_waitcnt lgkmcnt(0)\n"
    "s_mul_i32 s91, s78, 24\n"
    "s_cmp_eq_u32 s78, s80\n"
    "s_addc_u32 s10, s10, 0\n"
    "s_cmp_eq_u32 s78, s82\n"
    "s_addc_u32 s10, s10, 0\n"
    "s_cmp_eq_u32 s78, s84\n"
    "s_addc_u32 s10, s10, 0\n"
    "s_cmp_eq_u32 s78, s86\n"
    "s_addc_u32 s10, s10, 0\n"
    "s_cmp_eq_u32 s78, s88\n"
    "s_addc_u32 s10, s10, 0\n"
    "s_mul_i32 s10, s10, 20\n"
    "s_sub_u32 s91, s91, s6\n"
    "s_sub_u32 s91, s91, s10\n"
    "s_sub_u32 s8, s9, s91\n"
    "s_sub_i32 s94, s91, s47\n"
    "s_add_i32 s19, s19, s94\n"
    "s_sub_i32 s19, s19, s51\n"
    "s_max_i32 s19, s19, -24\n"
    "s_min_i32 s19, s19, 100\n"
    "s_ashr_i32 s19, s19, 1\n"
    "s_add_i32 s51, s51, s19\n"
    "s_add_u32 s46, s46, s51\n"
    "s_add_u32 s46, s46, s91\n"
    "L_wb%=:\n"
    "s_memtime s[6:7]\n"
    "s_waitcnt lgkmcnt(0)\n"
    "s_sub_i32 s78, s8, s6\n"
    "s_sub_u32 s79, s78, 200\n"
    "s_cmp_lt_u32 s79, 0x5000\n"
    "s_cbranch_scc1 L_wb%=\n"
    "s_sub_i32 s78, s78, 84\n"
    "s_max_i32 s78, s78, 0\n"
    "s_min_i32 s78, s78, 124\n"
    "s_andn2_b32 s78, s78, 3\n"
    "s_sub_u32 s80, s4, s78\n"
    "s_subb_u32 s81, s5, 0\n"
    "s_setpc_b64 s[80:81]\n"
    "L_far%=:\n"
    "L_wc%=:\n"
    "s_memtime s[6:7]\n"
    "s_waitcnt lgkmcnt(0)\n"
    "s_sub_i32 s78, s8, s6\n"
    "s_sub_u32 s79, s78, 1300\n"
    "s_cmp_lt_u32 s79, 0x5000\n"
    "s_cbranch_scc1 L_wc%=\n"
    "L_miss%=:\n"
    "global_load_dwordx2 v[120:121], v119, s[12:13] offset:8 sc1\n"
    "s_add_u32 s11, s11, 1\n"
    "s_cmp_gt_u32 s11, 40\n"
    "s_waitcnt vmcnt(0)\n"
    "s_cbranch_scc0 L_lk%=\n"
    "s_branch L_near%=\n"
    ".p2align 6\n"
    "s_nop 0\n"
    "s_nop 0\n"
    "s_nop 0\n"
    "s_nop 0\n"
    "s_nop 0\n"
    "s_nop 0\n"
    "s_nop 0\n"
    "s_nop 0\n"
    "s_nop 0\n"
    "s_nop 0\n"
    "s_nop 0\n"
    "s_nop 0\n"
    "s_nop 0\n"
    "s_nop 0\n"
    "s_nop 0\n"
    "s_nop 0\n"
    "s_nop 0\n"
    "s_nop 0\n"
    "s_nop 0\n"
    "s_nop 0\n"
    "s_nop 0\n"
    "s_nop 0\n"
    "s_nop 0\n"
    "s_nop 0\n"
    "s_nop 0\n"
    "s_nop 0\n"
    "s_nop 0\n"
    "s_nop 0\n"
    "s_nop 0\n"
    "s_nop 0\n"
    "s_nop 0\n"
    "s_nop 0\n"
    "L_sled%=:\n"
    "s_barrier\n"
    "global_load_dwordx4 v[8:11], v71, s[54:55] offset:0 sc1\n"
    "s_nop 3\n"
    "global_load_dwordx4 v[12:15], v71, s[54:55] offset:1024 sc1\n"
    "s_memtime s[16:17]\n"
    "s_add_u32 s11, s43, -1\n"
    "s_and_b32 s11, s11, 3\n"
    "s_cselect_b32 s11, 0, 0x2000\n"
    "s_add_u32 s62, s62, 0x80000\n"
    "s_addc_u32 s63, s63, 0\n"
    "s_xor_b32 s62, s62, s11\n"
    "s_add_u32 s11, s43, 1\n"
    "s_and_b32 s11, s11, 3\n"
    "s_cselect_b32 s11, 0, 0x2000\n"
    "s_add_u32 s58, s58, 0x80000\n"
    "s_addc_u32 s59, s59, 0\n"
    "s_xor_b32 s58, s58, s11\n"
    "s_add_u32 s60, s60, 0x80000\n"
    "s_addc_u32 s61, s61, 0\n"
    "s_xor_b32 s60, s60, s11\n"
    "s_xor_b32 s44, s44, 0x8000\n"
    "s_cmp_lg_u32 s42, 0\n"
    "s_cbranch_scc1 L_vmb1%=\n"
    "s_mov_b32 exec_lo, 0\n"
    "global_store_dwordx4 v71, v[40:43], s[62:63] offset:0 sc1\n"
    "s_mov_b64 exec, -1\n"
    "s_branch L_kvb1%=\n"
    "L_vmb1%=:\n"
    "v_mov_b32 v126, v66\n"
    "v_mov_b32 v127, v74\n"
    "global_store_dwordx2 v79, v[126:127], s[62:63] offset:512 sc1\n"
    "L_kvb1%=:\n"
    "s_nop 1\n"
    "v_mov_b64 v[40:41], 0\n"
    "v_mov_b64 v[42:43], 0\n"
    "v_mov_b64 v[44:45], 0\n"
    "v_mov_b64 v[46:47], 0\n"
    "s_add_u32 s12, s12, 0x10000\n"
    "s_andn2_b32 s12, s12, 0x40000\n"
    "s_add_u32 s56, s56, 0x10000\n"
    "s_andn2_b32 s56, s56, 0x40000\n"
    "s_waitcnt lgkmcnt(0)\n"
    "v_mov_b64 v[32:33], 0\n"
    "v_mov_b64 v[34:35], 0\n"
    "v_mov_b64 v[36:37], 0\n"
    "v_mov_b64 v[38:39], 0\n"
    "v_mov_b64 v[48:49], 0\n"
    "v_mov_b64 v[50:51], 0\n"
    "v_mov_b64 v[52:53], 0\n"
    "v_mov_b64 v[54:55], 0\n"
    "v_mov_b64 v[104:105], 0\n"
    "v_mov_b64 v[106:107], 0\n"
    "v_mov_b64 v[108:109], 0\n"
    "v_mov_b64 v[110:111], 0\n"
    "v_or_b32 v84, 1, s96\n"
    "v_mov_b32 v85, s97\n"
    "v_add_u32 v86, s43, v84\n"
    "s_add_u32 s14, s16, s46\n"
    "s_andn2_b32 s14, s14, 1\n"
    "s_or_b32 s14, s14, s38\n"
    "s_mov_b64 exec, 15\n"
    "v_mov_b32 v87, s14\n"
    "global_store_dwordx4 v118, v[84:87], s[56:57] sc1\n"
    "s_mov_b64 exec, -1\n"
    "s_branch L_top1%=\n"
    "L_fix%=:\n"
    "s_sub_u32 s16, s6, s15\n"
    "s_add_u32 s16, s16, 60\n"
    "s_add_u32 s14, s16, s46\n"
    "s_andn2_b32 s14, s14, 1\n"
    "s_or_b32 s14, s14, s38\n"
    "s_mov_b64 exec, 15\n"
    "v_mov_b32 v87, s14\n"
    "global_store_dwordx4 v118, v[84:87], s[56:57] sc1\n"
    "s_mov_b64 exec, -1\n"
    "s_branch L_fixed%=\n"
    "L_slow1%=:\n"
    "v_cmp_eq_u32 s[78:79], s50, v8\n"
    "v_cmp_eq_u32 s[80:81], s50, v9\n"
    "v_cmp_eq_u32 s[82:83], s50, v10\n"
    "v_cmp_eq_u32 s[84:85], s50, v11\n"
    "v_cmp_eq_u32 s[86:87], s50, v12\n"
    "v_cmp_eq_u32 s[88:89], s50, v13\n"
    "v_cmp_eq_u32 s[52:53], s50, v14\n"
    "v_cmp_eq_u32 vcc, s50, v15\n"
    "global_load_dwordx4 v[8:11], v71, s[54:55] offset:0 sc1\n"
    "global_load_dwordx4 v[12:15], v71, s[54:55] offset:1024 sc1\n"
    "s_memtime s[16:17]\n"
    "s_waitcnt lgkmcnt(0)\n"
    "s_barrier\n"
    "s_add_u32 s16, s16, 400\n"
    "s_mov_b32 s38, 1\n"
    "s_add_u32 s14, s16, s46\n"
    "s_andn2_b32 s14, s14, 1\n"
    "s_or_b32 s14, s14, s38\n"
    "s_mov_b64 exec, 15\n"
    "v_mov_b32 v87, s14\n"
    "global_store_dwordx4 v118, v[84:87], s[56:57] sc1\n"
    "s_mov_b64 exec, -1\n"
    "s_or_b64 vcc, vcc, s[78:79]\n"
    "s_or_b64 vcc, vcc, s[80:81]\n"
    "s_or_b64 vcc, vcc, s[82:83]\n"
    "s_or_b64 vcc, vcc, s[84:85]\n"
    "s_or_b64 vcc, vcc, s[86:87]\n"
    "s_or_b64 vcc, vcc, s[88:89]\n"
    "s_or_b64 vcc, vcc, s[52:53]\n"
    "s_cbranch_scc1 L_redo1%=\n"
    "v_cmp_eq_u32 s[78:79], s50, v0\n"
    "v_cmp_eq_u32 s[80:81], s50, v1\n"
    "v_cmp_eq_u32 s[82:83], s50, v2\n"
    "v_cmp_eq_u32 s[84:85], s50, v3\n"
    "v_cmp_eq_u32 s[86:87], s50, v24\n"
    "v_cmp_eq_u32 s[88:89], s50, v25\n"
    "v_cmp_eq_u32 s[52:53], s50, v26\n"
    "v_cmp_eq_u32 vcc, s50, v27\n"
    "s_nop 0\n"
    "s_or_b64 vcc, vcc, s[78:79]\n"
    "s_or_b64 vcc, vcc, s[80:81]\n"
    "s_or_b64 vcc, vcc, s[82:83]\n"
    "s_or_b64 vcc, vcc, s[84:85]\n"
    "s_or_b64 vcc, vcc, s[86:87]\n"
    "s_or_b64 vcc, vcc, s[88:89]\n"
    "s_or_b64 vcc, vcc, s[52:53]\n"
    "s_cbranch_scc1 L_redo1%=\n"
    "v_cmp_eq_u32 s[78:79], s50, v16\n"
    "v_cmp_eq_u32 s[80:81], s50, v17\n"
    "v_cmp_eq_u32 s[82:83], s50, v18\n"
    "v_cmp_eq_u32 s[84:85], s50, v19\n"
    "v_cmp_eq_u32 s[86:87], s50, v4\n"
    "v_cmp_eq_u32 s[88:89], s50, v5\n"
    "v_cmp_eq_u32 s[52:53], s50, v6\n"
    "v_cmp_eq_u32 vcc, s50, v7\n"
    "s_nop 0\n"
    "s_or_b64 vcc, vcc, s[78:79]\n"
    "s_or_b64 vcc, vcc, s[80:81]\n"
    "s_or_b64 vcc, vcc, s[82:83]\n"
    "s_or_b64 vcc, vcc, s[84:85]\n"
    "s_or_b64 vcc, vcc, s[86:87]\n"
    "s_or_b64 vcc, vcc, s[88:89]\n"
    "s_or_b64 vcc, vcc, s[52:53]\n"
    "s_cbranch_scc1 L_redo1%=\n"
    "v_cmp_eq_u32 s[78:79], s50, v28\n"
    "v_cmp_eq_u32 s[80:81], s50, v29\n"
    "v_cmp_eq_u32 s[82:83], s50, v30\n"
    "v_cmp_eq_u32 s[84:85], s50, v31\n"
    "v_cmp_eq_u32 s[86:87], s50, v20\n"
    "v_cmp_eq_u32 s[88:89], s50, v21\n"
    "v_cmp_eq_u32 s[52:53], s50, v22\n"
    "v_cmp_eq_u32 vcc, s50, v23\n"
    "s_nop 0\n"
    "s_or_b64 vcc, vcc, s[78:79]\n"
    "s_or_b64 vcc, vcc, s[80:81]\n"
    "s_or_b64 vcc, vcc, s[82:83]\n"
    "s_or_b64 vcc, vcc, s[84:85]\n"
    "s_or_b64 vcc, vcc, s[86:87]\n"
    "s_or_b64 vcc, vcc, s[88:89]\n"
    "s_or_b64 vcc, vcc, s[52:53]\n"
    "s_cbranch_scc1 L_redo1%=\n"
    "s_waitcnt vmcnt(0)\n"
    "s_branch L_go1%=\n"
    "L_redo1%=:\n"
    "ds_read_b128 a[16:19], v116 offset:16384\n"
    "ds_read_b128 a[20:23], v116 offset:17408\n"
    "ds_read_b128 a[80:83], v116 offset:18432\n"
    "ds_read_b128 a[84:87], v116 offset:19456\n"
    "ds_read_b128 a[24:27], v116 offset:20480\n"
    "ds_read_b128 a[28:31], v116 offset:21504\n"
    "ds_read_b128 a[88:91], v116 offset:22528\n"
    "ds_read_b128 a[92:95], v116 offset:23552\n"
    "ds_read_b128 a[0:3], v116 offset:24576\n"
    "ds_read_b128 a[4:7], v116 offset:25600\n"
    "ds_read_b128 a[64:67], v116 offset:26624\n"
    "ds_read_b128 a[68:71], v116 offset:27648\n"
    "ds_read_b128 a[48:51], v116 offset:28672\n"
    "ds_read_b128 a[52:55], v116 offset:29696\n"
    "s_waitcnt lgkmcnt(8)\n"
    "ds_read_b128 a[112:115], v116 offset:30720\n"
    "ds_read_b128 a[116:119], v116 offset:31744\n"
    "v_mov_b64 v[32:33], 0\n"
    "v_mov_b64 v[34:35], 0\n"
    "v_mov_b64 v[36:37], 0\n"
    "v_mov_b64 v[38:39], 0\n"
    "v_mov_b64 v[40:41], 0\n"
    "v_mov_b64 v[42:43], 0\n"
    "v_mov_b64 v[44:45], 0\n"
    "v_mov_b64 v[46:47], 0\n"
    "v_mov_b64 v[48:49], 0\n"
    "v_mov_b64 v[50:51], 0\n"
    "v_mov_b64 v[52:53], 0\n"
    "v_mov_b64 v[54:55], 0\n"
    "s_add_u32 s90, s90, 1\n"
    "s_and_b32 s78, s90, 0xff\n"
    "s_cmp_lg_u32 s78, 0\n"
    "s_cbranch_scc1 L_re1%=\n"
    "v_mov_b32 v126, 0\n"
    "global_load_dwordx2 v[126:127], v126, s[68:69] sc1\n"
    "s_waitcnt vmcnt(0)\n"
    "v_cmp_eq_u64 vcc, s[96:97], v[126:127]\n"
    "s_nop 1\n"
    "s_cbranch_vccnz L_bail_g1%=\n"
    "s_cmp_lt_u32 s90, 0x40000\n"
    "s_cbranch_scc1 L_re1%=\n"
    "v_mov_b32 v126, s96\n"
    "v_mov_b32 v127, s97\n"
    "s_mov_b64 exec, 1\n"
    "global_store_dwordx2 v71, v[126:127], s[68:69] sc1\n"
    "s_mov_b64 exec, -1\n"
    "L_bail_g1%=:\n"
    "s_branch L_go1%=\n"
    "L_re1%=:\n"
    "s_waitcnt vmcnt(1) lgkmcnt(0)\n"
    "s_branch L_top1%=\n"
    "L_last1%=:\n"
    "v_smfmac_f32_16x16x64_bf16 v[52:55], v[4:7], a[200:207], v67\n"
    "v_smfmac_f32_16x16x64_bf16 v[48:51], v[28:31], a[184:191], v67\n"
    "v_smfmac_f32_16x16x64_bf16 v[52:55], v[28:31], a[248:255], v67\n"
    "v_smfmac_f32_16x16x64_bf16 v[48:51], v[20:23], a[168:175], v67\n"
    "v_smfmac_f32_16x16x64_bf16 v[52:55], v[20:23], a[232:239], v67\n"
    "s_add_u32 s11, s43, 0\n"
    "s_and_b32 s11, s11, 3\n"
    "s_cselect_b32 s11, 0, 0x2000\n"
    "s_add_u32 s62, s62, 0x80000\n"
    "s_addc_u32 s63, s63, 0\n"
    "s_xor_b32 s62, s62, s11\n"
    "v_pk_add_f32 v[48:49], v[48:49], v[50:51]\n"
    "s_nop 0\n"
    "v_pk_add_f32 v[52:53], v[52:53], v[54:55]\n"
    "v_cvt_pk_bf16_f32 v66, v48, v49\n"
    "v_cvt_pk_bf16_f32 v74, v52, v53\n"
    "s_cmp_lg_u32 s42, 0\n"
    "s_cbranch_scc1 L_vmc1%=\n"
    "s_mov_b32 exec_lo, 0\n"
    "global_store_dwordx4 v71, v[40:43], s[62:63] offset:0 sc1\n"
    "s_mov_b64 exec, -1\n"
    "s_branch L_kvc1%=\n"
    "L_vmc1%=:\n"
    "v_mov_b32 v126, v66\n"
    "v_mov_b32 v127, v74\n"
    "global_store_dwordx2 v79, v[126:127], s[62:63] offset:512 sc1\n"
    "L_kvc1%=:\n"
    "s_waitcnt vmcnt(0) lgkmcnt(0)\n"
    "s_endpgm\n"
    ".p2align 6\n"
    "L_poll2%=:\n"
    "global_load_dwordx4 v[16:19], v71, s[54:55] offset:0 sc1\n"
    "global_load_dwordx4 v[20:23], v71, s[54:55] offset:1024 sc1\n"
    "s_mov_b32 m0, 121088\n"
    "s_waitcnt vmcnt(0)\n"
    "s_branch L_top2%=\n"
    ".p2align 6\n"
    "L_top2%=:\n"
    "s_waitcnt vmcnt(2)\n"
    "v_smfmac_f32_16x16x64_bf16 v[40:43], v[16:19], a[32:39], v67\n"
    "ds_write_b128 v71, v[16:19] offset:4096\n"
    "v_smfmac_f32_16x16x64_bf16 v[44:47], v[16:19], a[96:103], v67\n"
    "v_smfmac_f32_16x16x64_bf16 v[32:35], v[16:19], v[192:199], v67\n"
    "v_smfmac_f32_16x16x64_bf16 v[36:39], v[16:19], v[200:207], v67\n"
    "s_waitcnt vmcnt(1)\n"
    "v_smfmac_f32_16x16x64_bf16 v[40:43], v[20:23], a[40:47], v67\n"
    "ds_write_b128 v71, v[20:23] offset:5120\n"
    "s_waitcnt lgkmcnt(1)\n"
    "s_barrier\n"
    "ds_read_b128 v[24:27], v71 offset:6144\n"
    "v_smfmac_f32_16x16x64_bf16 v[44:47], v[20:23], a[104:111], v67\n"
    "ds_read_b128 v[0:3], v71 offset:0\n"
    "v_smfmac_f32_16x16x64_bf16 v[32:35], v[20:23], v[208:215], v67\n"
    "ds_read_b128 v[8:11], v71 offset:2048\n"
    "v_smfmac_f32_16x16x64_bf16 v[36:39], v[20:23], v[216:223], v67\n"
    "s_waitcnt lgkmcnt(3)\n"
    "s_barrier\n"
    "v_smfmac_f32_16x16x64_bf16 v[48:51], v[16:19], a[160:167], v67\n"
    "s_waitcnt lgkmcnt(2)\n"
    "ds_read_b128 v[28:31], v71 offset:7168\n"
    "v_smfmac_f32_16x16x64_bf16 v[32:35], v[24:27], v[224:231], v67\n"
    "ds_read_b128 v[4:7], v71 offset:1024\n"
    "v_smfmac_f32_16x16x64_bf16 v[36:39], v[24:27], v[232:239], v67\n"
    "s_waitcnt lgkmcnt(2)\n"
    "ds_read_b128 v[12:15], v71 offset:3072\n"
    "v_smfmac_f32_16x16x64_bf16 v[32:35], v[0:3], v[128:135], v67\n"
    "s_mov_b32 s78, 0x12345\n"
    "ds_read_b128 a[32:35], v116 offset:0\n"
    "v_smfmac_f32_16x16x64_bf16 v[36:39], v[0:3], v[136:143], v67\n"
    "s_mov_b32 s78, 0x12345\n"
    "ds_read_b128 a[36:39], v116 offset:1024\n"
    "v_smfmac_f32_16x16x64_bf16 v[32:35], v[8:11], v[160:167], v67\n"
    "s_mov_b32 s78, 0x12345\n"
    "ds_read_b128 a[96:99], v116 offset:4096\n"
    "v_smfmac_f32_16x16x64_bf16 v[36:39], v[8:11], v[168:175], v67\n"
    "s_waitcnt lgkmcnt(5)\n"
    "ds_read_b128 a[100:103], v116 offset:5120\n"
    "v_smfmac_f32_16x16x64_bf16 v[32:35], v[28:31], v[240:247], v67\n"
    "s_sub_i32 s2, s6, s16\n"
    "ds_read_b128 a[40:43], v116 offset:2048\n"
    "v_smfmac_f32_16x16x64_bf16 v[36:39], v[28:31], v[248:255], v67\n"
    "s_waitcnt lgkmcnt(5)\n"
    "ds_read_b128 a[44:47], v116 offset:3072\n"
    "v_smfmac_f32_16x16x64_bf16 v[32:35], v[4:7], v[144:151], v67\n"
    "s_cmp_gt_i32 s2, s3\n"
    "ds_read_b128 a[104:107], v116 offset:6144\n"
    "v_smfmac_f32_16x16x64_bf16 v[36:39], v[4:7], v[152:159], v67\n"
    "ds_read_b128 a[108:111], v116 offset:7168\n"
    "v_smfmac_f32_16x16x64_bf16 v[32:35], v[12:15], v[176:183], v67\n"
    "s_nop 0\n"
    "v_smfmac_f32_16x16x64_bf16 v[36:39], v[12:15], v[184:191], v67\n"
    "v_smfmac_f32_16x16x64_bf16 v[40:43], v[24:27], a[48:55], v67\n"
    "v_add_f32 v32, v32, v34\n"
    "v_add_f32_e64 v33, v33, v35\n"
    "v_smfmac_f32_16x16x64_bf16 v[44:47], v[24:27], a[112:119], v67\n"
    "ds_read_b128 a[48:51], v116 offset:8192\n"
    "v_cvt_pk_bf16_f32 v126, v32, v33\n"
    "v_add_f32 v36, v36, v38\n"
    "v_add_f32 v37, v37, v39\n"
    "v_cvt_pk_bf16_f32 v127, v36, v37\n"
    "v_smfmac_f32_16x16x64_bf16 v[40:43], v[0:3], a[0:7], v67\n"
    "ds_write_b64 v114, v[126:127] offset:9216\n"
    "v_smfmac_f32_16x16x64_bf16 v[44:47], v[0:3], a[64:71], v67\n"
    "ds_read_b128 a[52:55], v116 offset:9216\n"
    "v_cmp_u_f32_e64 vcc, v32, v33\n"
    "v_smfmac_f32_16x16x64_bf16 v[40:43], v[8:11], a[16:23], v67\n"
    "ds_read_b128 a[112:115], v116 offset:12288\n"
    "v_mov_b32_e64 v87, s76\n"
    "v_smfmac_f32_16x16x64_bf16 v[44:47], v[8:11], a[80:87], v67\n"
    "s_cbranch_vccnz L_slow2%=\n"
    "L_go2%=:\n"
    "v_smfmac_f32_16x16x64_bf16 v[40:43], v[28:31], a[56:63], v67\n"
    "s_waitcnt lgkmcnt(2)\n"
    "s_barrier\n"
    "ds_read_b64_tr_b16 v[32:33], v112 offset:8192\n"
    "v_smfmac_f32_16x16x64_bf16 v[44:47], v[28:31], a[120:127], v67\n"
    "ds_read_b64_tr_b16 v[34:35], v113 offset:8192\n"
    "ds_read_b64_tr_b16 v[36:37], v112 offset:9216\n"
    "v_smfmac_f32_16x16x64_bf16 v[40:43], v[4:7], a[8:15], v67\n"
    "ds_read_b64_tr_b16 v[38:39], v113 offset:9216\n"
    "v_smfmac_f32_16x16x64_bf16 v[44:47], v[4:7], a[72:79], v67\n"
    "global_load_dwordx4 v[96:99], v71, s[58:59] offset:-2048 sc1\n"
    "v_smfmac_f32_16x16x64_bf16 v[40:43], v[12:15], a[24:31], v67\n"
    "ds_read_b128 a[116:119], v116 offset:13312\n"
    "v_smfmac_f32_16x16x64_bf16 v[44:47], v[12:15], a[88:95], v67\n"
    "s_waitcnt lgkmcnt(1)\n"
    "ds_read_b128 a[0:3], v116 offset:10240\n"
    "v_mfma_f32_16x16x32_bf16 v[88:91], v[56:59], v[32:35], 0\n"
    "v_add_f32 v40, v40, v42\n"
    "v_add_f32 v41, v41, v43\n"
    "v_mfma_f32_16x16x32_bf16 v[88:91], v[60:63], v[36:39], v[88:91]\n"
    "v_cvt_pk_bf16_f32 v126, v40, v41\n"
    "v_add_f32 v44, v44, v46\n"
    "v_add_f32 v45, v45, v47\n"
    "v_cvt_pk_bf16_f32 v127, v44, v45\n"
    "v_smfmac_f32_16x16x64_bf16 v[52:55], v[16:19], a[224:231], v67\n"
    "ds_write_b64 v115, v[126:127] offset:11264\n"
    "v_add_f32_dpp v88, v90, v88 quad_perm:[2,3,2,3] row_mask:0xf bank_mask:0xf\n"
    "v_add_f32_dpp v89, v91, v89 quad_perm:[2,3,2,3] row_mask:0xf bank_mask:0xf\n"
    "v_smfmac_f32_16x16x64_bf16 v[48:51], v[20:23], a[168:175], v67\n"
    "ds_write_b64 v75, v[88:89] offset:14848\n"
    "v_smfmac_f32_16x16x64_bf16 v[52:55], v[20:23], a[232:239], v67\n"
    "global_load_dwordx4 v[100:103], v71, s[58:59] offset:2048 sc1\n"
    "v_smfmac_f32_16x16x64_bf16 v[48:51], v[24:27], a[176:183], v67\n"
    "global_load_lds_dwordx4 v71, s[60:61] offset:0 sc1\n"
    "v_smfmac_f32_16x16x64_bf16 v[52:55], v[24:27], a[240:247], v67\n"
    "global_load_lds_dwordx4 v71, s[60:61] offset:1024 sc1\n"
    "v_smfmac_f32_16x16x64_bf16 v[48:51], v[0:3], a[128:135], v67\n"
    "s_waitcnt lgkmcnt(0)\n"
    "s_barrier\n"
    "ds_read_b64_tr_b16 v[40:41], v112 offset:10240\n"
    "v_smfmac_f32_16x16x64_bf16 v[52:55], v[0:3], a[192:199], v67\n"
    "ds_read_b64_tr_b16 v[42:43], v113 offset:10240\n"
    "ds_read_b64_tr_b16 v[44:45], v112 offset:11264\n"
    "v_smfmac_f32_16x16x64_bf16 v[48:51], v[8:11], a[144:151], v67\n"
    "ds_read_b64_tr_b16 v[46:47], v113 offset:11264\n"
    "ds_read_b128 v[88:91], v71 offset:14336\n"
    "v_smfmac_f32_16x16x64_bf16 v[52:55], v[8:11], a[208:215], v67\n"
    "s_cmp_eq_u32 s43, 63\n"
    "ds_read_b128 a[4:7], v116 offset:11264\n"
    "v_smfmac_f32_16x16x64_bf16 v[48:51], v[28:31], a[184:191], v67\n"
    "s_waitcnt lgkmcnt(4)\n"
    "v_mfma_f32_16x16x32_bf16 v[92:95], v[40:43], v[32:35], 0\n"
    "s_waitcnt lgkmcnt(2)\n"
    "ds_read_b128 a[64:67], v116 offset:14336\n"
    "v_mfma_f32_16x16x32_bf16 v[92:95], v[44:47], v[36:39], v[92:95]\n"
    "s_cbranch_scc1 L_last2%=\n"
    "s_waitcnt lgkmcnt(2)\n"
    "v_max3_f32 v122, v88, v89, v90\n"
    "v_max_f32 v122, v122, v91\n"
    "v_smfmac_f32_16x16x64_bf16 v[52:55], v[28:31], a[248:255], v67\n"
    "ds_read_b128 a[68:71], v116 offset:15360\n"
    "v_max_f32_dpp v123, v122, v122 quad_perm:[2,3,0,1] row_mask:0xf bank_mask:0xf\n"
    "v_add_f32_dpp v92, v94, v92 quad_perm:[2,3,2,3] row_mask:0xf bank_mask:0xf\n"
    "v_add_f32_dpp v93, v95, v93 quad_perm:[2,3,2,3] row_mask:0xf bank_mask:0xf\n"
    "v_max3_f32 v122, v123, v92, v93\n"
    "v_smfmac_f32_16x16x64_bf16 v[48:51], v[4:7], a[136:143], v67\n"
    "v_mul_f32_dpp v121, v122, v87 quad_perm:[0,1,0,1] row_mask:0xf bank_mask:0xf\n"
    "v_mul_f32_dpp v123, v122, v87 quad_perm:[0,1,0,1] row_mask:0xf bank_mask:0xf\n"
    "v_smfmac_f32_16x16x64_bf16 v[52:55], v[4:7], a[200:207], v67\n"
    "v_permlane16_swap_b32 v123, v121\n"
    "v_max_f32 v122, v123, v121\n"
    "v_max_f32_e64 v123, v123, v121\n"
    "v_smfmac_f32_16x16x64_bf16 v[48:51], v[12:15], a[152:159], v67\n"
    "v_smfmac_f32_16x16x64_bf16 v[52:55], v[12:15], a[216:223], v67\n"
    "v_permlane32_swap_b32 v122, v123\n"
    "v_max_f32 v122, v122, v123\n"
    "v_pk_fma_f32 v[92:93], v[92:93], s[76:77], v[122:123] op_sel_hi:[1,1,0] neg_lo:[0,0,1] neg_hi:[0,0,1]\n"
    "v_pk_fma_f32 v[88:89], v[88:89], s[76:77], v[122:123] op_sel_hi:[1,1,0] neg_lo:[0,0,1] neg_hi:[0,0,1]\n"
    "v_pk_fma_f32 v[90:91], v[90:91], s[76:77], v[122:123] op_sel_hi:[1,1,0] neg_lo:[0,0,1] neg_hi:[0,0,1]\n"
    "v_exp_f32 v92, v92\n"
    "v_exp_f32 v93, v93\n"
    "v_exp_f32 v88, v88\n"
    "v_exp_f32 v89, v89\n"
    "v_exp_f32 v90, v90\n"
    "v_exp_f32 v91, v91\n"
    "v_pk_add_f32 v[48:49], v[48:49], v[50:51]\n"
    "v_pk_add_f32 v[52:53], v[52:53], v[54:55]\n"
    "v_cvt_pk_bf16_f32 v126, v92, v93\n"
    "v_cvt_pk_bf16_f32 v88, v88, v89\n"
    "v_cvt_pk_bf16_f32 v90, v90, v91\n"
    "v_cndmask_b32 v89, 0, v126, s[72:73]\n"
    "v_cndmask_b32 v91, 0, v126, s[74:75]\n"
    "v_cvt_pk_bf16_f32 v66, v48, v49\n"
    "v_cvt_pk_bf16_f32 v70, v48, v49\n"
    "v_cvt_pk_bf16_f32 v74, v52, v53\n"
    "v_cvt_pk_bf16_f32 v78, v52, v53\n"
    "v_smfmac_f32_16x16x64_bf16 v[104:107], v[88:91], v[64:71], v67\n"
    "v_mov_b64_e64 v[0:1], 0\n"
    "v_smfmac_f32_16x16x64_bf16 v[108:111], v[88:91], v[72:79], v67\n"
    "v_mov_b64_e64 v[2:3], 0\n"
    "v_mfma_f32_16x16x32_bf16 v[84:87], v[88:91], v[80:83], 0\n"
    "v_add_f32 v104, v104, v106\n"
    "v_add_f32 v105, v105, v107\n"
    "v_cvt_pk_bf16_f32 v104, v104, v105\n"
    "v_add_f32 v108, v108, v110\n"
    "v_add_f32 v109, v109, v111\n"
    "v_cvt_pk_bf16_f32 v105, v108, v109\n"
    "ds_write_b64 v114, v[104:105] offset:13312\n"
    "v_pk_add_f32 v[124:125], v[84:85], v[86:87]\n"
    "v_rcp_f32 v124, v124\n"
    "v_rcp_f32 v125, v125\n"
    "v_mov_b64 v[4:5], 0\n"
    "v_mov_b64 v[6:7], 0\n"
    "v_mov_b64 v[8:9], 0\n"
    "v_mov_b64 v[10:11], 0\n"
    "v_mov_b64 v[12:13], 0\n"
    "v_mov_b64 v[14:15], 0\n"
    "v_mfma_f32_16x16x32_bf16 v[92:95], v[80:83], v[80:83], 0\n"
    "s_waitcnt lgkmcnt(0)\n"
    "s_barrier\n"
    "ds_read_b64_tr_b16 v[32:33], v112 offset:12288\n"
    "v_mfma_f32_16x16x32_bf16 v[92:95], v[80:83], v[80:83], 0\n"
    "ds_read_b64_tr_b16 v[34:35], v113 offset:12288\n"
    "ds_read_b64_tr_b16 v[36:37], v112 offset:13312\n"
    "v_mfma_f32_16x16x32_bf16 v[92:95], v[80:83], v[80:83], 0\n"
    "ds_read_b64_tr_b16 v[38:39], v113 offset:13312\n"
    "v_mfma_f32_16x16x32_bf16 v[92:95], v[80:83], v[80:83], 0\n"
    "s_waitcnt lgkmcnt(2)\n"
    "v_smfmac_f32_16x16x64_bf16 v[0:3], v[32:35], a[32:39], v67\n"
    "v_smfmac_f32_16x16x64_bf16 v[4:7], v[32:35], a[96:103], v67\n"
    "s_waitcnt lgkmcnt(0)\n"
    "v_smfmac_f32_16x16x64_bf16 v[0:3], v[36:39], a[40:47], v67\n"
    "v_smfmac_f32_16x16x64_bf16 v[4:7], v[36:39], a[104:111], v67\n"
    "v_smfmac_f32_16x16x64_bf16 v[8:11], v[32:35], a[48:55], v67\n"
    "v_add_f32_e64 v0, v0, v2\n"
    "v_smfmac_f32_16x16x64_bf16 v[12:15], v[32:35], a[112:119], v67\n"
    "v_add_f32_e64 v1, v1, v3\n"
    "v_smfmac_f32_16x16x64_bf16 v[8:11], v[36:39], a[0:7], v67\n"
    "v_add_f32_e64 v4, v4, v6\n"
    "v_smfmac_f32_16x16x64_bf16 v[12:15], v[36:39], a[64:71], v67\n"
    "v_add_f32 v5, v5, v7\n"
    "v_mul_f32 v0, v0, v124\n"
    "v_mul_f32_e64 v1, v1, v125\n"
    "v_cvt_pk_bf16_f32 v88, v0, v1\n"
    "v_pk_mul_f32 v[4:5], v[4:5], v[124:125]\n"
    "v_pk_add_f32 v[8:9], v[8:9], v[10:11]\n"
    "v_pk_add_f32 v[12:13], v[12:13], v[14:15]\n"
    "v_cvt_pk_bf16_f32 v89, v4, v5\n"
    "v_pk_mul_f32 v[8:9], v[8:9], v[124:125]\n"
    "v_pk_mul_f32 v[12:13], v[12:13], v[124:125]\n"
    "v_cvt_pk_bf16_f32 v90, v8, v9\n"
    "v_cvt_pk_bf16_f32 v91, v12, v13\n"
    "global_store_dwordx4 v117, v[88:91], s[44:45] sc1\n"
    "s_add_u32 s43, s43, 1\n"
    "s_sleep 3\n"
    "s_add_u32 s79, s43, 1\n"
    "s_min_u32 s79, s79, 63\n"
    "s_lshr_b32 s79, s79, 1\n"
    "s_cmp_lt_u32 s79, s92\n"
    "s_cbranch_scc1 L_cntokl%=\n"
    "L_cntl%=:\n"
    "v_and_b32 v126, 16, v71\n"
    "v_lshlrev_b32 v126, 8, v126\n"
    "global_load_dwordx2 v[126:127], v126, s[66:67] sc1\n"
    "s_waitcnt vmcnt(0)\n"
    "v_xor_b32 v126, s96, v126\n"
    "v_xor_b32 v127, s97, v127\n"
    "v_cmp_gt_u64 vcc, 64, v[126:127]\n"
    "s_nop 1\n"
    "v_cndmask_b32 v126, 0, v126, vcc\n"
    "s_nop 0\n"
    "v_readlane_b32 s80, v126, 0\n"
    "v_readlane_b32 s81, v126, 1\n"
    "s_nop 1\n"
    "s_min_u32 s92, s80, s81\n"
    "s_cmp_lt_u32 s79, s92\n"
    "s_cbranch_scc1 L_cntokl%=\n"
    "s_sleep 2\n"
    "s_add_u32 s90, s90, 1\n"
    "s_and_b32 s78, s90, 0xff\n"
    "s_cmp_lg_u32 s78, 0\n"
    "s_cbranch_scc1 L_cntl%=\n"
    "v_mov_b32 v126, 0\n"
    "global_load_dwordx2 v[126:127], v126, s[68:69] sc1\n"
    "s_waitcnt vmcnt(0)\n"
    "v_cmp_eq_u64 vcc, s[96:97], v[126:127]\n"
    "s_nop 1\n"
    "s_cbranch_vccnz L_bail_cntl%=\n"
    "s_cmp_lt_u32 s90, 0x40000\n"
    "s_cbranch_scc1 L_cntl%=\n"
    "v_mov_b32 v126, s96\n"
    "v_mov_b32 v127, s97\n"
    "s_mov_b64 exec, 1\n"
    "global_store_dwordx2 v71, v[126:127], s[68:69] sc1\n"
    "s_mov_b64 exec, -1\n"
    "L_bail_cntl%=:\n"
    "L_cntokl%=:\n"
    "ds_read_b128 a[32:35], v116 offset:16384\n"
    "ds_read_b128 a[36:39], v116 offset:17408\n"
    "ds_read_b128 a[96:99], v116 offset:18432\n"
    "ds_read_b128 a[100:103], v116 offset:19456\n"
    "ds_read_b128 a[40:43], v116 offset:20480\n"
    "ds_read_b128 a[44:47], v116 offset:21504\n"
    "ds_read_b128 a[104:107], v116 offset:22528\n"
    "ds_read_b128 a[108:111], v116 offset:23552\n"
    "ds_read_b128 a[48:51], v116 offset:24576\n"
    "ds_read_b128 a[52:55], v116 offset:25600\n"
    "ds_read_b128 a[112:115], v116 offset:26624\n"
    "ds_read_b128 a[116:119], v116 offset:27648\n"
    "ds_read_b128 a[0:3], v116 offset:28672\n"
    "s_waitcnt lgkmcnt(8)\n"
    "ds_read_b128 a[4:7], v116 offset:29696\n"
    "ds_read_b128 a[64:67], v116 offset:30720\n"
    "ds_read_b128 a[68:71], v116 offset:31744\n"
    "s_and_b32 s11, s43, 3\n"
    "s_lshl_b32 s11, s11, 17\n"
    "s_add_u32 s54, s48, s11\n"
    "s_addc_u32 s55, s49, 0\n"
    "s_add_u32 s11, s43, -1\n"
    "s_and_b32 s11, s11, 3\n"
    "s_cselect_b32 s11, 0, 0x2000\n"
    "s_add_u32 s62, s62, 0x80000\n"
    "s_addc_u32 s63, s63, 0\n"
    "s_xor_b32 s62, s62, s11\n"
    "s_add_u32 s11, s43, 1\n"
    "s_and_b32 s11, s11, 3\n"
    "s_cselect_b32 s11, 0, 0x2000\n"
    "s_add_u32 s58, s58, 0x80000\n"
    "s_addc_u32 s59, s59, 0\n"
    "s_xor_b32 s58, s58, s11\n"
    "s_add_u32 s60, s60, 0x80000\n"
    "s_addc_u32 s61, s61, 0\n"
    "s_xor_b32 s60, s60, s11\n"
    "s_xor_b32 s44, s44, 0x8000\n"
    "v_mov_b64 v[32:33], 0\n"
    "v_mov_b64 v[34:35], 0\n"
    "v_mov_b64 v[36:37], 0\n"
    "v_mov_b64 v[38:39], 0\n"
    "v_mov_b64 v[48:49], 0\n"
    "v_mov_b64 v[50:51], 0\n"
    "v_mov_b64 v[52:53], 0\n"
    "v_mov_b64 v[54:55], 0\n"
    "v_mov_b64 v[104:105], 0\n"
    "v_mov_b64 v[106:107], 0\n"
    "v_mov_b64 v[108:109], 0\n"
    "v_mov_b64 v[110:111], 0\n"
    "s_waitcnt vmcnt(1)\n"
    "ds_read_b64 v[64:65], v116 offset:32768\n"
    "ds_read_b64 v[68:69], v116 offset:32776\n"
    "ds_read_b64 v[72:73], v116 offset:33792\n"
    "ds_read_b64 v[76:77], v116 offset:33800\n"
    "v_mov_b64 v[56:57], v[96:97]\n"
    "v_mov_b64 v[58:59], v[98:99]\n"
    "v_mov_b64 v[60:61], v[100:101]\n"
    "v_mov_b64 v[62:63], v[102:103]\n"
    "s_waitcnt lgkmcnt(0)\n"
    "s_barrier\n"
    "global_load_dwordx4 v[16:19], v71, s[54:55] offset:0 sc1\n"
    "s_nop 3\n"
    "global_load_dwordx4 v[20:23], v71, s[54:55] offset:1024 sc1\n"
    "s_nop 15\n"
    "s_cmp_lg_u32 s42, 0\n"
    "s_cbranch_scc1 L_vmb2%=\n"
    "s_mov_b32 exec_hi, 0\n"
    "global_store_dwordx4 v71, v[44:47], s[62:63] offset:1024 sc1\n"
    "s_mov_b64 exec, -1\n"
    "s_branch L_kvb2%=\n"
    "L_vmb2%=:\n"
    "v_mov_b32 v126, v66\n"
    "v_mov_b32 v127, v74\n"
    "global_store_dwordx2 v79, v[126:127], s[62:63] offset:1024 sc1\n"
    "L_kvb2%=:\n"
    "s_nop 1\n"
    "v_mov_b64 v[40:41], 0\n"
    "v_mov_b64 v[42:43], 0\n"
    "v_mov_b64 v[44:45], 0\n"
    "v_mov_b64 v[46:47], 0\n"
    "s_branch L_top2%=\n"
    "L_slow2%=:\n"
    "v_cmp_eq_u32 s[78:79], s50, v16\n"
    "v_cmp_eq_u32 s[80:81], s50, v17\n"
    "v_cmp_eq_u32 s[82:83], s50, v18\n"
    "v_cmp_eq_u32 s[84:85], s50, v19\n"
    "v_cmp_eq_u32 s[86:87], s50, v20\n"
    "v_cmp_eq_u32 s[88:89], s50, v21\n"
    "v_cmp_eq_u32 s[52:53], s50, v22\n"
    "v_cmp_eq_u32 vcc, s50, v23\n"
    "global_load_dwordx4 v[16:19], v71, s[54:55] offset:0 sc1\n"
    "global_load_dwordx4 v[20:23], v71, s[54:55] offset:1024 sc1\n"
    "s_waitcnt lgkmcnt(0)\n"
    "s_barrier\n"
    "s_or_b64 vcc, vcc, s[78:79]\n"
    "s_or_b64 vcc, vcc, s[80:81]\n"
    "s_or_b64 vcc, vcc, s[82:83]\n"
    "s_or_b64 vcc, vcc, s[84:85]\n"
    "s_or_b64 vcc, vcc, s[86:87]\n"
    "s_or_b64 vcc, vcc, s[88:89]\n"
    "s_or_b64 vcc, vcc, s[52:53]\n"
    "s_cbranch_scc1 L_redo2%=\n"
    "v_cmp_eq_u32 s[78:79], s50, v24\n"
    "v_cmp_eq_u32 s[80:81], s50, v25\n"
    "v_cmp_eq_u32 s[82:83], s50, v26\n"
    "v_cmp_eq_u32 s[84:85], s50, v27\n"
    "v_cmp_eq_u32 s[86:87], s50, v0\n"
    "v_cmp_eq_u32 s[88:89], s50, v1\n"
    "v_cmp_eq_u32 s[52:53], s50, v2\n"
    "v_cmp_eq_u32 vcc, s50, v3\n"
    "s_nop 0\n"
    "s_or_b64 vcc, vcc, s[78:79]\n"
    "s_or_b64 vcc, vcc, s[80:81]\n"
    "s_or_b64 vcc, vcc, s[82:83]\n"
    "s_or_b64 vcc, vcc, s[84:85]\n"
    "s_or_b64 vcc, vcc, s[86:87]\n"
    "s_or_b64 vcc, vcc, s[88:89]\n"
    "s_or_b64 vcc, vcc, s[52:53]\n"
    "s_cbranch_scc1 L_redo2%=\n"
    "v_cmp_eq_u32 s[78:79], s50, v8\n"
    "v_cmp_eq_u32 s[80:81], s50, v9\n"
    "v_cmp_eq_u32 s[82:83], s50, v10\n"
    "v_cmp_eq_u32 s[84:85], s50, v11\n"
    "v_cmp_eq_u32 s[86:87], s50, v28\n"
    "v_cmp_eq_u32 s[88:89], s50, v29\n"
    "v_cmp_eq_u32 s[52:53], s50, v30\n"
    "v_cmp_eq_u32 vcc, s50, v31\n"
    "s_nop 0\n"
    "s_or_b64 vcc, vcc, s[78:79]\n"
    "s_or_b64 vcc, vcc, s[80:81]\n"
    "s_or_b64 vcc, vcc, s[82:83]\n"
    "s_or_b64 vcc, vcc, s[84:85]\n"
    "s_or_b64 vcc, vcc, s[86:87]\n"
    "s_or_b64 vcc, vcc, s[88:89]\n"
    "s_or_b64 vcc, vcc, s[52:53]\n"
    "s_cbranch_scc1 L_redo2%=\n"
    "v_cmp_eq_u32 s[78:79], s50, v4\n"
    "v_cmp_eq_u32 s[80:81], s50, v5\n"
    "v_cmp_eq_u32 s[82:83], s50, v6\n"
    "v_cmp_eq_u32 s[84:85], s50, v7\n"
    "v_cmp_eq_u32 s[86:87], s50, v12\n"
    "v_cmp_eq_u32 s[88:89], s50, v13\n"
    "v_cmp_eq_u32 s[52:53], s50, v14\n"
    "v_cmp_eq_u32 vcc, s50, v15\n"
    "s_nop 0\n"
    "s_or_b64 vcc, vcc, s[78:79]\n"
    "s_or_b64 vcc, vcc, s[80:81]\n"
    "s_or_b64 vcc, vcc, s[82:83]\n"
    "s_or_b64 vcc, vcc, s[84:85]\n"
    "s_or_b64 vcc, vcc, s[86:87]\n"
    "s_or_b64 vcc, vcc, s[88:89]\n"
    "s_or_b64 vcc, vcc, s[52:53]\n"
    "s_cbranch_scc1 L_redo2%=\n"
    "s_waitcnt vmcnt(0)\n"
    "s_branch L_go2%=\n"
    "L_redo2%=:\n"
    "ds_read_b128 a[32:35], v116 offset:16384\n"
    "ds_read_b128 a[36:39], v116 offset:17408\n"
    "ds_read_b128 a[96:99], v116 offset:18432\n"
    "ds_read_b128 a[100:103], v116 offset:19456\n"
    "ds_read_b128 a[40:43], v116 offset:20480\n"
    "ds_read_b128 a[44:47], v116 offset:21504\n"
    "ds_read_b128 a[104:107], v116 offset:22528\n"
    "ds_read_b128 a[108:111], v116 offset:23552\n"
    "ds_read_b128 a[48:51], v116 offset:24576\n"
    "ds_read_b128 a[52:55], v116 offset:25600\n"
    "ds_read_b128 a[112:115], v116 offset:26624\n"
    "ds_read_b128 a[116:119], v116 offset:27648\n"
    "ds_read_b128 a[0:3], v116 offset:28672\n"
    "ds_read_b128 a[4:7], v116 offset:29696\n"
    "s_waitcnt lgkmcnt(8)\n"
    "ds_read_b128 a[64:67], v116 offset:30720\n"
    "ds_read_b128 a[68:71], v116 offset:31744\n"
    "v_mov_b64 v[32:33], 0\n"
    "v_mov_b64 v[34:35], 0\n"
    "v_mov_b64 v[36:37], 0\n"
    "v_mov_b64 v[38:39], 0\n"
    "v_mov_b64 v[40:41], 0\n"
    "v_mov_b64 v[42:43], 0\n"
    "v_mov_b64 v[44:45], 0\n"
    "v_mov_b64 v[46:47], 0\n"
    "v_mov_b64 v[48:49], 0\n"
    "v_mov_b64 v[50:51], 0\n"
    "v_mov_b64 v[52:53], 0\n"
    "v_mov_b64 v[54:55], 0\n"
    "s_add_u32 s90, s90, 1\n"
    "s_and_b32 s78, s90, 0xff\n"
    "s_cmp_lg_u32 s78, 0\n"
    "s_cbranch_scc1 L_re2%=\n"
    "v_mov_b32 v126, 0\n"
    "global_load_dwordx2 v[126:127], v126, s[68:69] sc1\n"
    "s_waitcnt vmcnt(0)\n"
    "v_cmp_eq_u64 vcc, s[96:97], v[126:127]\n"
    "s_nop 1\n"
    "s_cbranch_vccnz L_bail_g2%=\n"
    "s_cmp_lt_u32 s90, 0x40000\n"
    "s_cbranch_scc1 L_re2%=\n"
    "v_mov_b32 v126, s96\n"
    "v_mov_b32 v127, s97\n"
    "s_mov_b64 exec, 1\n"
    "global_store_dwordx2 v71, v[126:127], s[68:69] sc1\n"
    "s_mov_b64 exec, -1\n"
    "L_bail_g2%=:\n"
    "s_branch L_go2%=\n"
    "L_re2%=:\n"
    "s_waitcnt vmcnt(0) lgkmcnt(0)\n"
    "s_branch L_top2%=\n"
    "L_last2%=:\n"
    "v_smfmac_f32_16x16x64_bf16 v[52:55], v[28:31], a[248:255], v67\n"
    "v_smfmac_f32_16x16x64_bf16 v[48:51], v[4:7], a[136:143], v67\n"
    "v_smfmac_f32_16x16x64_bf16 v[52:55], v[4:7], a[200:207], v67\n"
    "v_smfmac_f32_16x16x64_bf16 v[48:51], v[12:15], a[152:159], v67\n"
    "v_smfmac_f32_16x16x64_bf16 v[52:55], v[12:15], a[216:223], v67\n"
    "s_add_u32 s11, s43, 0\n"
    "s_and_b32 s11, s11, 3\n"
    "s_cselect_b32 s11, 0, 0x2000\n"
    "s_add_u32 s62, s62, 0x80000\n"
    "s_addc_u32 s63, s63, 0\n"
    "s_xor_b32 s62, s62, s11\n"
    "v_pk_add_f32 v[48:49], v[48:49], v[50:51]\n"
    "s_nop 0\n"
    "v_pk_add_f32 v[52:53], v[52:53], v[54:55]\n"
    "v_cvt_pk_bf16_f32 v66, v48, v49\n"
    "v_cvt_pk_bf16_f32 v74, v52, v53\n"
    "s_cmp_lg_u32 s42, 0\n"
    "s_cbranch_scc1 L_vmc2%=\n"
    "s_mov_b32 exec_hi, 0\n"
    "global_store_dwordx4 v71, v[44:47], s[62:63] offset:1024 sc1\n"
    "s_mov_b64 exec, -1\n"
    "s_branch L_kvc2%=\n"
    "L_vmc2%=:\n"
    "v_mov_b32 v126, v66\n"
    "v_mov_b32 v127, v74\n"
    "global_store_dwordx2 v79, v[126:127], s[62:63] offset:1024 sc1\n"
    "L_kvc2%=:\n"
    "s_waitcnt vmcnt(0) lgkmcnt(0)\n"
    "s_endpgm\n"
    ".p2align 6\n"
    "L_poll3%=:\n"
    "global_load_dwordx4 v[24:27], v71, s[54:55] offset:0 sc1\n"
    "global_load_dwordx4 v[28:31], v71, s[54:55] offset:1024 sc1\n"
    "s_mov_b32 m0, 155904\n"
    "s_waitcnt vmcnt(0)\n"
    "s_branch L_top3%=\n"
    ".p2align 6\n"
    "L_top3%=:\n"
    "s_waitcnt vmcnt(2)\n"
    "v_smfmac_f32_16x16x64_bf16 v[40:43], v[24:27], a[48:55], v67\n"
    "ds_write_b128 v71, v[24:27] offset:6144\n"
    "v_smfmac_f32_16x16x64_bf16 v[44:47], v[24:27], a[112:119], v67\n"
    "v_smfmac_f32_16x16x64_bf16 v[32:35], v[24:27], v[224:231], v67\n"
    "v_smfmac_f32_16x16x64_bf16 v[36:39], v[24:27], v[232:239], v67\n"
    "s_waitcnt vmcnt(1)\n"
    "v_smfmac_f32_16x16x64_bf16 v[40:43], v[28:31], a[56:63], v67\n"
    "ds_write_b128 v71, v[28:31] offset:7168\n"
    "s_waitcnt lgkmcnt(1)\n"
    "s_barrier\n"
    "ds_read_b128 v[16:19], v71 offset:4096\n"
    "v_smfmac_f32_16x16x64_bf16 v[44:47], v[28:31], a[120:127], v67\n"
    "ds_read_b128 v[8:11], v71 offset:2048\n"
    "v_smfmac_f32_16x16x64_bf16 v[32:35], v[28:31], v[240:247], v67\n"
    "ds_read_b128 v[0:3], v71 offset:0\n"
    "v_smfmac_f32_16x16x64_bf16 v[36:39], v[28:31], v[248:255], v67\n"
    "s_waitcnt lgkmcnt(3)\n"
    "s_barrier\n"
    "v_smfmac_f32_16x16x64_bf16 v[48:51], v[24:27], a[176:183], v67\n"
    "s_waitcnt lgkmcnt(2)\n"
    "ds_read_b128 v[20:23], v71 offset:5120\n"
    "v_smfmac_f32_16x16x64_bf16 v[32:35], v[16:19], v[192:199], v67\n"
    "ds_read_b128 v[12:15], v71 offset:3072\n"
    "v_smfmac_f32_16x16x64_bf16 v[36:39], v[16:19], v[200:207], v67\n"
    "s_waitcnt lgkmcnt(2)\n"
    "ds_read_b128 v[4:7], v71 offset:1024\n"
    "v_smfmac_f32_16x16x64_bf16 v[32:35], v[8:11], v[160:167], v67\n"
    "s_mov_b32 s78, 0x12345\n"
    "ds_read_b128 a[48:51], v116 offset:0\n"
    "v_smfmac_f32_16x16x64_bf16 v[36:39], v[8:11], v[168:175], v67\n"
    "s_mov_b32 s78, 0x12345\n"
    "ds_read_b128 a[52:55], v116 offset:1024\n"
    "v_smfmac_f32_16x16x64_bf16 v[32:35], v[0:3], v[128:135], v67\n"
    "s_mov_b32 s78, 0x12345\n"
    "ds_read_b128 a[112:115], v116 offset:4096\n"
    "v_smfmac_f32_16x16x64_bf16 v[36:39], v[0:3], v[136:143], v67\n"
    "s_waitcnt lgkmcnt(5)\n"
    "ds_read_b128 a[116:119], v116 offset:5120\n"
    "v_smfmac_f32_16x16x64_bf16 v[32:35], v[20:23], v[208:215], v67\n"
    "s_sub_i32 s2, s6, s16\n"
    "ds_read_b128 a[56:59], v116 offset:2048\n"
    "v_smfmac_f32_16x16x64_bf16 v[36:39], v[20:23], v[216:223], v67\n"
    "s_waitcnt lgkmcnt(5)\n"
    "ds_read_b128 a[60:63], v116 offset:3072\n"
    "v_smfmac_f32_16x16x64_bf16 v[32:35], v[12:15], v[176:183], v67\n"
    "s_cmp_gt_i32 s2, s3\n"
    "ds_read_b128 a[120:123], v116 offset:6144\n"
    "v_smfmac_f32_16x16x64_bf16 v[36:39], v[12:15], v[184:191], v67\n"
    "ds_read_b128 a[124:127], v116 offset:7168\n"
    "v_smfmac_f32_16x16x64_bf16 v[32:35], v[4:7], v[144:151], v67\n"
    "s_nop 0\n"
    "v_smfmac_f32_16x16x64_bf16 v[36:39], v[4:7], v[152:159], v67\n"
    "v_smfmac_f32_16x16x64_bf16 v[40:43], v[16:19], a[32:39], v67\n"
    "v_add_f32 v32, v32, v34\n"
    "v_add_f32_e64 v33, v33, v35\n"
    "v_smfmac_f32_16x16x64_bf16 v[44:47], v[16:19], a[96:103], v67\n"
    "ds_read_b128 a[32:35], v116 offset:8192\n"
    "v_cvt_pk_bf16_f32 v126, v32, v33\n"
    "v_add_f32 v36, v36, v38\n"
    "v_add_f32 v37, v37, v39\n"
    "v_cvt_pk_bf16_f32 v127, v36, v37\n"
    "v_smfmac_f32_16x16x64_bf16 v[40:43], v[8:11], a[16:23], v67\n"
    "ds_write_b64 v114, v[126:127] offset:9728\n"
    "v_smfmac_f32_16x16x64_bf16 v[44:47], v[8:11], a[80:87], v67\n"
    "ds_read_b128 a[36:39], v116 offset:9216\n"
    "v_cmp_u_f32_e64 vcc, v32, v33\n"
    "v_smfmac_f32_16x16x64_bf16 v[40:43], v[0:3], a[0:7], v67\n"
    "ds_read_b128 a[96:99], v116 offset:12288\n"
    "v_mov_b32_e64 v87, s76\n"
    "v_smfmac_f32_16x16x64_bf16 v[44:47], v[0:3], a[64:71], v67\n"
    "s_cbranch_vccnz L_slow3%=\n"
    "L_go3%=:\n"
    "v_smfmac_f32_16x16x64_bf16 v[40:43], v[20:23], a[40:47], v67\n"
    "s_waitcnt lgkmcnt(2)\n"
    "s_barrier\n"
    "ds_read_b64_tr_b16 v[32:33], v112 offset:8192\n"
    "v_smfmac_f32_16x16x64_bf16 v[44:47], v[20:23], a[104:111], v67\n"
    "ds_read_b64_tr_b16 v[34:35], v113 offset:8192\n"
    "ds_read_b64_tr_b16 v[36:37], v112 offset:9216\n"
    "v_smfmac_f32_16x16x64_bf16 v[40:43], v[12:15], a[24:31], v67\n"
    "ds_read_b64_tr_b16 v[38:39], v113 offset:9216\n"
    "v_smfmac_f32_16x16x64_bf16 v[44:47], v[12:15], a[88:95], v67\n"
    "global_load_dwordx4 v[96:99], v71, s[58:59] offset:-2048 sc1\n"
    "v_smfmac_f32_16x16x64_bf16 v[40:43], v[4:7], a[8:15], v67\n"
    "ds_read_b128 a[100:103], v116 offset:13312\n"
    "v_smfmac_f32_16x16x64_bf16 v[44:47], v[4:7], a[72:79], v67\n"
    "s_waitcnt lgkmcnt(1)\n"
    "ds_read_b128 a[16:19], v116 offset:10240\n"
    "v_mfma_f32_16x16x32_bf16 v[88:91], v[56:59], v[32:35], 0\n"
    "v_add_f32 v40, v40, v42\n"
    "v_add_f32 v41, v41, v43\n"
    "v_mfma_f32_16x16x32_bf16 v[88:91], v[60:63], v[36:39], v[88:91]\n"
    "v_cvt_pk_bf16_f32 v126, v40, v41\n"
    "v_add_f32 v44, v44, v46\n"
    "v_add_f32 v45, v45, v47\n"
    "v_cvt_pk_bf16_f32 v127, v44, v45\n"
    "v_smfmac_f32_16x16x64_bf16 v[52:55], v[24:27], a[240:247], v67\n"
    "ds_write_b64 v115, v[126:127] offset:11776\n"
    "v_add_f32_dpp v88, v90, v88 quad_perm:[2,3,2,3] row_mask:0xf bank_mask:0xf\n"
    "v_add_f32_dpp v89, v91, v89 quad_perm:[2,3,2,3] row_mask:0xf bank_mask:0xf\n"
    "v_smfmac_f32_16x16x64_bf16 v[48:51], v[28:31], a[184:191], v67\n"
    "ds_write_b64 v75, v[88:89] offset:14880\n"
    "v_smfmac_f32_16x16x64_bf16 v[52:55], v[28:31], a[248:255], v67\n"
    "global_load_dwordx4 v[100:103], v71, s[58:59] offset:2048 sc1\n"
    "v_smfmac_f32_16x16x64_bf16 v[48:51], v[16:19], a[160:167], v67\n"
    "global_load_lds_dwordx4 v71, s[60:61] offset:0 sc1\n"
    "v_smfmac_f32_16x16x64_bf16 v[52:55], v[16:19], a[224:231], v67\n"
    "global_load_lds_dwordx4 v71, s[60:61] offset:1024 sc1\n"
    "v_smfmac_f32_16x16x64_bf16 v[48:51], v[8:11], a[144:151], v67\n"
    "s_waitcnt lgkmcnt(0)\n"
    "s_barrier\n"
    "ds_read_b64_tr_b16 v[40:41], v112 offset:10240\n"
    "v_smfmac_f32_16x16x64_bf16 v[52:55], v[8:11], a[208:215], v67\n"
    "ds_read_b64_tr_b16 v[42:43], v113 offset:10240\n"
    "ds_read_b64_tr_b16 v[44:45], v112 offset:11264\n"
    "v_smfmac_f32_16x16x64_bf16 v[48:51], v[0:3], a[128:135], v67\n"
    "ds_read_b64_tr_b16 v[46:47], v113 offset:11264\n"
    "ds_read_b128 v[88:91], v71 offset:14336\n"
    "v_smfmac_f32_16x16x64_bf16 v[52:55], v[0:3], a[192:199], v67\n"
    "s_cmp_eq_u32 s43, 63\n"
    "ds_read_b128 a[20:23], v116 offset:11264\n"
    "v_smfmac_f32_16x16x64_bf16 v[48:51], v[20:23], a[168:175], v67\n"
    "s_waitcnt lgkmcnt(4)\n"
    "v_mfma_f32_16x16x32_bf16 v[92:95], v[40:43], v[32:35], 0\n"
    "s_waitcnt lgkmcnt(2)\n"
    "ds_read_b128 a[80:83], v116 offset:14336\n"
    "v_mfma_f32_16x16x32_bf16 v[92:95], v[44:47], v[36:39], v[92:95]\n"
    "s_cbranch_scc1 L_last3%=\n"
    "s_waitcnt lgkmcnt(2)\n"
    "v_max3_f32 v122, v88, v89, v90\n"
    "v_max_f32 v122, v122, v91\n"
    "v_smfmac_f32_16x16x64_bf16 v[52:55], v[20:23], a[232:239], v67\n"
    "ds_read_b128 a[84:87], v116 offset:15360\n"
    "v_max_f32_dpp v123, v122, v122 quad_perm:[2,3,0,1] row_mask:0xf bank_mask:0xf\n"
    "v_add_f32_dpp v92, v94, v92 quad_perm:[2,3,2,3] row_mask:0xf bank_mask:0xf\n"
    "v_add_f32_dpp v93, v95, v93 quad_perm:[2,3,2,3] row_mask:0xf bank_mask:0xf\n"
    "v_max3_f32 v122, v123, v92, v93\n"
    "v_smfmac_f32_16x16x64_bf16 v[48:51], v[12:15], a[152:159], v67\n"
    "v_mul_f32_dpp v121, v122, v87 quad_perm:[0,1,0,1] row_mask:0xf bank_mask:0xf\n"
    "v_mul_f32_dpp v123, v122, v87 quad_perm:[0,1,0,1] row_mask:0xf bank_mask:0xf\n"
    "v_smfmac_f32_16x16x64_bf16 v[52:55], v[12:15], a[216:223], v67\n"
    "v_permlane16_swap_b32 v123, v121\n"
    "v_max_f32 v122, v123, v121\n"
    "v_max_f32_e64 v123, v123, v121\n"
    "v_smfmac_f32_16x16x64_bf16 v[48:51], v[4:7], a[136:143], v67\n"
    "v_smfmac_f32_16x16x64_bf16 v[52:55], v[4:7], a[200:207], v67\n"
    "v_permlane32_swap_b32 v122, v123\n"
    "v_max_f32 v122, v122, v123\n"
    "v_pk_fma_f32 v[92:93], v[92:93], s[76:77], v[122:123] op_sel_hi:[1,1,0] neg_lo:[0,0,1] neg_hi:[0,0,1]\n"
    "v_pk_fma_f32 v[88:89], v[88:89], s[76:77], v[122:123] op_sel_hi:[1,1,0] neg_lo:[0,0,1] neg_hi:[0,0,1]\n"
    "v_pk_fma_f32 v[90:91], v[90:91], s[76:77], v[122:123] op_sel_hi:[1,1,0] neg_lo:[0,0,1] neg_hi:[0,0,1]\n"
    "v_exp_f32 v92, v92\n"
    "v_exp_f32 v93, v93\n"
    "v_exp_f32 v88, v88\n"
    "v_exp_f32 v89, v89\n"
    "v_exp_f32 v90, v90\n"
    "v_exp_f32 v91, v91\n"
    "v_pk_add_f32 v[48:49], v[48:49], v[50:51]\n"
    "v_pk_add_f32 v[52:53], v[52:53], v[54:55]\n"
    "v_cvt_pk_bf16_f32 v126, v92, v93\n"
    "v_cvt_pk_bf16_f32 v88, v88, v89\n"
    "v_cvt_pk_bf16_f32 v90, v90, v91\n"
    "v_cndmask_b32 v89, 0, v126, s[72:73]\n"
    "v_cndmask_b32 v91, 0, v126, s[74:75]\n"
    "v_cvt_pk_bf16_f32 v66, v48, v49\n"
    "v_cvt_pk_bf16_f32 v70, v48, v49\n"
    "v_cvt_pk_bf16_f32 v74, v52, v53\n"
    "v_cvt_pk_bf16_f32 v78, v52, v53\n"
    "v_smfmac_f32_16x16x64_bf16 v[104:107], v[88:91], v[64:71], v67\n"
    "v_mov_b64_e64 v[0:1], 0\n"
    "v_smfmac_f32_16x16x64_bf16 v[108:111], v[88:91], v[72:79], v67\n"
    "v_mov_b64_e64 v[2:3], 0\n"
    "v_mfma_f32_16x16x32_bf16 v[84:87], v[88:91], v[80:83], 0\n"
    "v_add_f32 v104, v104, v106\n"
    "v_add_f32 v105, v105, v107\n"
    "v_cvt_pk_bf16_f32 v104, v104, v105\n"
    "v_add_f32 v108, v108, v110\n"
    "v_add_f32 v109, v109, v111\n"
    "v_cvt_pk_bf16_f32 v105, v108, v109\n"
    "ds_write_b64 v114, v[104:105] offset:13824\n"
    "v_pk_add_f32 v[124:125], v[84:85], v[86:87]\n"
    "v_rcp_f32 v124, v124\n"
    "v_rcp_f32 v125, v125\n"
    "v_mov_b64 v[4:5], 0\n"
    "v_mov_b64 v[6:7], 0\n"
    "v_mov_b64 v[8:9], 0\n"
    "v_mov_b64 v[10:11], 0\n"
    "v_mov_b64 v[12:13], 0\n"
    "v_mov_b64 v[14:15], 0\n"
    "v_mfma_f32_16x16x32_bf16 v[92:95], v[80:83], v[80:83], 0\n"
    "s_waitcnt lgkmcnt(0)\n"
    "s_barrier\n"
    "ds_read_b64_tr_b16 v[32:33], v112 offset:12288\n"
    "v_mfma_f32_16x16x32_bf16 v[92:95], v[80:83], v[80:83], 0\n"
    "ds_read_b64_tr_b16 v[34:35], v113 offset:12288\n"
    "ds_read_b64_tr_b16 v[36:37], v112 offset:13312\n"
    "v_mfma_f32_16x16x32_bf16 v[92:95], v[80:83], v[80:83], 0\n"
    "ds_read_b64_tr_b16 v[38:39], v113 offset:13312\n"
    "v_mfma_f32_16x16x32_bf16 v[92:95], v[80:83], v[80:83], 0\n"
    "s_waitcnt lgkmcnt(2)\n"
    "v_smfmac_f32_16x16x64_bf16 v[0:3], v[32:35], a[48:55], v67\n"
    "v_smfmac_f32_16x16x64_bf16 v[4:7], v[32:35], a[112:119], v67\n"
    "s_waitcnt lgkmcnt(0)\n"
    "v_smfmac_f32_16x16x64_bf16 v[0:3], v[36:39], a[56:63], v67\n"
    "v_smfmac_f32_16x16x64_bf16 v[4:7], v[36:39], a[120:127], v67\n"
    "v_smfmac_f32_16x16x64_bf16 v[8:11], v[32:35], a[32:39], v67\n"
    "v_add_f32_e64 v0, v0, v2\n"
    "v_smfmac_f32_16x16x64_bf16 v[12:15], v[32:35], a[96:103], v67\n"
    "v_add_f32_e64 v1, v1, v3\n"
    "v_smfmac_f32_16x16x64_bf16 v[8:11], v[36:39], a[16:23], v67\n"
    "v_add_f32_e64 v4, v4, v6\n"
    "v_smfmac_f32_16x16x64_bf16 v[12:15], v[36:39], a[80:87], v67\n"
    "v_add_f32 v5, v5, v7\n"
    "v_mul_f32 v0, v0, v124\n"
    "v_mul_f32_e64 v1, v1, v125\n"
    "v_cvt_pk_bf16_f32 v88, v0, v1\n"
    "v_pk_mul_f32 v[4:5], v[4:5], v[124:125]\n"
    "v_pk_add_f32 v[8:9], v[8:9], v[10:11]\n"
    "v_pk_add_f32 v[12:13], v[12:13], v[14:15]\n"
    "v_cvt_pk_bf16_f32 v89, v4, v5\n"
    "v_pk_mul_f32 v[8:9], v[8:9], v[124:125]\n"
    "v_pk_mul_f32 v[12:13], v[12:13], v[124:125]\n"
    "v_cvt_pk_bf16_f32 v90, v8, v9\n"
    "v_cvt_pk_bf16_f32 v91, v12, v13\n"
    "global_store_dwordx4 v117, v[88:91], s[44:45] sc1\n"
    "s_add_u32 s43, s43, 1\n"
    "s_sleep 3\n"
    "ds_read_b128 a[48:51], v116 offset:16384\n"
    "ds_read_b128 a[52:55], v116 offset:17408\n"
    "ds_read_b128 a[112:115], v116 offset:18432\n"
    "ds_read_b128 a[116:119], v116 offset:19456\n"
    "ds_read_b128 a[56:59], v116 offset:20480\n"
    "ds_read_b128 a[60:63], v116 offset:21504\n"
    "ds_read_b128 a[120:123], v116 offset:22528\n"
    "ds_read_b128 a[124:127], v116 offset:23552\n"
    "ds_read_b128 a[32:35], v116 offset:24576\n"
    "ds_read_b128 a[36:39], v116 offset:25600\n"
    "ds_read_b128 a[96:99], v116 offset:26624\n"
    "ds_read_b128 a[100:103], v116 offset:27648\n"
    "ds_read_b128 a[16:19], v116 offset:28672\n"
    "s_waitcnt lgkmcnt(8)\n"
    "ds_read_b128 a[20:23], v116 offset:29696\n"
    "ds_read_b128 a[80:83], v116 offset:30720\n"
    "ds_read_b128 a[84:87], v116 offset:31744\n"
    "s_and_b32 s11, s43, 3\n"
    "s_lshl_b32 s11, s11, 17\n"
    "s_add_u32 s54, s48, s11\n"
    "s_addc_u32 s55, s49, 0\n"
    "s_add_u32 s11, s43, -1\n"
    "s_and_b32 s11, s11, 3\n"
    "s_cselect_b32 s11, 0, 0x2000\n"
    "s_add_u32 s62, s62, 0x80000\n"
    "s_addc_u32 s63, s63, 0\n"
    "s_xor_b32 s62, s62, s11\n"
    "s_add_u32 s11, s43, 1\n"
    "s_and_b32 s11, s11, 3\n"
    "s_cselect_b32 s11, 0, 0x2000\n"
    "s_add_u32 s58, s58, 0x80000\n"
    "s_addc_u32 s59, s59, 0\n"
    "s_xor_b32 s58, s58, s11\n"
    "s_add_u32 s60, s60, 0x80000\n"
    "s_addc_u32 s61, s61, 0\n"
    "s_xor_b32 s60, s60, s11\n"
    "s_xor_b32 s44, s44, 0x8000\n"
    "v_mov_b64 v[32:33], 0\n"
    "v_mov_b64 v[34:35], 0\n"
    "v_mov_b64 v[36:37], 0\n"
    "v_mov_b64 v[38:39], 0\n"
    "v_mov_b64 v[48:49], 0\n"
    "v_mov_b64 v[50:51], 0\n"
    "v_mov_b64 v[52:53], 0\n"
    "v_mov_b64 v[54:55], 0\n"
    "v_mov_b64 v[104:105], 0\n"
    "v_mov_b64 v[106:107], 0\n"
    "v_mov_b64 v[108:109], 0\n"
    "v_mov_b64 v[110:111], 0\n"
    "s_waitcnt vmcnt(1)\n"
    "ds_read_b64 v[64:65], v116 offset:32768\n"
    "ds_read_b64 v[68:69], v116 offset:32776\n"
    "ds_read_b64 v[72:73], v116 offset:33792\n"
    "ds_read_b64 v[76:77], v116 offset:33800\n"
    "v_mov_b64 v[56:57], v[96:97]\n"
    "v_mov_b64 v[58:59], v[98:99]\n"
    "v_mov_b64 v[60:61], v[100:101]\n"
    "v_mov_b64 v[62:63], v[102:103]\n"
    "s_waitcnt lgkmcnt(0)\n"
    "s_barrier\n"
    "global_load_dwordx4 v[24:27], v71, s[54:55] offset:0 sc1\n"
    "s_nop 3\n"
    "global_load_dwordx4 v[28:31], v71, s[54:55] offset:1024 sc1\n"
    "s_nop 15\n"
    "s_cmp_lg_u32 s42, 0\n"
    "s_cbranch_scc1 L_vmb3%=\n"
    "s_mov_b32 exec_lo, 0\n"
    "global_store_dwordx4 v71, v[44:47], s[62:63] offset:1024 sc1\n"
    "s_mov_b64 exec, -1\n"
    "s_branch L_kvb3%=\n"
    "L_vmb3%=:\n"
    "v_mov_b32 v126, v66\n"
    "v_mov_b32 v127, v74\n"
    "global_store_dwordx2 v79, v[126:127], s[62:63] offset:1536 sc1\n"
    "L_kvb3%=:\n"
    "s_nop 1\n"
    "v_mov_b64 v[40:41], 0\n"
    "v_mov_b64 v[42:43], 0\n"
    "v_mov_b64 v[44:45], 0\n"
    "v_mov_b64 v[46:47], 0\n"
    "s_branch L_top3%=\n"
    "L_slow3%=:\n"
    "v_cmp_eq_u32 s[78:79], s50, v24\n"
    "v_cmp_eq_u32 s[80:81], s50, v25\n"
    "v_cmp_eq_u32 s[82:83], s50, v26\n"
    "v_cmp_eq_u32 s[84:85], s50, v27\n"
    "v_cmp_eq_u32 s[86:87], s50, v28\n"
    "v_cmp_eq_u32 s[88:89], s50, v29\n"
    "v_cmp_eq_u32 s[52:53], s50, v30\n"
    "v_cmp_eq_u32 vcc, s50, v31\n"
    "global_load_dwordx4 v[24:27], v71, s[54:55] offset:0 sc1\n"
    "global_load_dwordx4 v[28:31], v71, s[54:55] offset:1024 sc1\n"
    "s_waitcnt lgkmcnt(0)\n"
    "s_barrier\n"
    "s_or_b64 vcc, vcc, s[78:79]\n"
    "s_or_b64 vcc, vcc, s[80:81]\n"
    "s_or_b64 vcc, vcc, s[82:83]\n"
    "s_or_b64 vcc, vcc, s[84:85]\n"
    "s_or_b64 vcc, vcc, s[86:87]\n"
    "s_or_b64 vcc, vcc, s[88:89]\n"
    "s_or_b64 vcc, vcc, s[52:53]\n"
    "s_cbranch_scc1 L_redo3%=\n"
    "v_cmp_eq_u32 s[78:79], s50, v16\n"
    "v_cmp_eq_u32 s[80:81], s50, v17\n"
    "v_cmp_eq_u32 s[82:83], s50, v18\n"
    "v_cmp_eq_u32 s[84:85], s50, v19\n"
    "v_cmp_eq_u32 s[86:87], s50, v8\n"
    "v_cmp_eq_u32 s[88:89], s50, v9\n"
    "v_cmp_eq_u32 s[52:53], s50, v10\n"
    "v_cmp_eq_u32 vcc, s50, v11\n"
    "s_nop 0\n"
    "s_or_b64 vcc, vcc, s[78:79]\n"
    "s_or_b64 vcc, vcc, s[80:81]\n"
    "s_or_b64 vcc, vcc, s[82:83]\n"
    "s_or_b64 vcc, vcc, s[84:85]\n"
    "s_or_b64 vcc, vcc, s[86:87]\n"
    "s_or_b64 vcc, vcc, s[88:89]\n"
    "s_or_b64 vcc, vcc, s[52:53]\n"
    "s_cbranch_scc1 L_redo3%=\n"
    "v_cmp_eq_u32 s[78:79], s50, v0\n"
    "v_cmp_eq_u32 s[80:81], s50, v1\n"
    "v_cmp_eq_u32 s[82:83], s50, v2\n"
    "v_cmp_eq_u32 s[84:85], s50, v3\n"
    "v_cmp_eq_u32 s[86:87], s50, v20\n"
    "v_cmp_eq_u32 s[88:89], s50, v21\n"
    "v_cmp_eq_u32 s[52:53], s50, v22\n"
    "v_cmp_eq_u32 vcc, s50, v23\n"
    "s_nop 0\n"
    "s_or_b64 vcc, vcc, s[78:79]\n"
    "s_or_b64 vcc, vcc, s[80:81]\n"
    "s_or_b64 vcc, vcc, s[82:83]\n"
    "s_or_b64 vcc, vcc, s[84:85]\n"
    "s_or_b64 vcc, vcc, s[86:87]\n"
    "s_or_b64 vcc, vcc, s[88:89]\n"
    "s_or_b64 vcc, vcc, s[52:53]\n"
    "s_cbranch_scc1 L_redo3%=\n"
    "v_cmp_eq_u32 s[78:79], s50, v12\n"
    "v_cmp_eq_u32 s[80:81], s50, v13\n"
    "v_cmp_eq_u32 s[82:83], s50, v14\n"
    "v_cmp_eq_u32 s[84:85], s50, v15\n"
    "v_cmp_eq_u32 s[86:87], s50, v4\n"
    "v_cmp_eq_u32 s[88:89], s50, v5\n"
    "v_cmp_eq_u32 s[52:53], s50, v6\n"
    "v_cmp_eq_u32 vcc, s50, v7\n"
    "s_nop 0\n"
    "s_or_b64 vcc, vcc, s[78:79]\n"
    "s_or_b64 vcc, vcc, s[80:81]\n"
    "s_or_b64 vcc, vcc, s[82:83]\n"
    "s_or_b64 vcc, vcc, s[84:85]\n"
    "s_or_b64 vcc, vcc, s[86:87]\n"
    "s_or_b64 vcc, vcc, s[88:89]\n"
    "s_or_b64 vcc, vcc, s[52:53]\n"
    "s_cbranch_scc1 L_redo3%=\n"
    "s_waitcnt vmcnt(0)\n"
    "s_branch L_go3%=\n"
    "L_redo3%=:\n"
    "ds_read_b128 a[48:51], v116 offset:16384\n"
    "ds_read_b128 a[52:55], v116 offset:17408\n"
    "ds_read_b128 a[112:115], v116 offset:18432\n"
    "ds_read_b128 a[116:119], v116 offset:19456\n"
    "ds_read_b128 a[56:59], v116 offset:20480\n"
    "ds_read_b128 a[60:63], v116 offset:21504\n"
    "ds_read_b128 a[120:123], v116 offset:22528\n"
    "ds_read_b128 a[124:127], v116 offset:23552\n"
    "ds_read_b128 a[32:35], v116 offset:24576\n"
    "ds_read_b128 a[36:39], v116 offset:25600\n"
    "ds_read_b128 a[96:99], v116 offset:26624\n"
    "ds_read_b128 a[100:103], v116 offset:27648\n"
    "ds_read_b128 a[16:19], v116 offset:28672\n"
    "ds_read_b128 a[20:23], v116 offset:29696\n"
    "s_waitcnt lgkmcnt(8)\n"
    "ds_read_b128 a[80:83], v116 offset:30720\n"
    "ds_read_b128 a[84:87], v116 offset:31744\n"
    "v_mov_b64 v[32:33], 0\n"
    "v_mov_b64 v[34:35], 0\n"
    "v_mov_b64 v[36:37], 0\n"
    "v_mov_b64 v[38:39], 0\n"
    "v_mov_b64 v[40:41], 0\n"
    "v_mov_b64 v[42:43], 0\n"
    "v_mov_b64 v[44:45], 0\n"
    "v_mov_b64 v[46:47], 0\n"
    "v_mov_b64 v[48:49], 0\n"
    "v_mov_b64 v[50:51], 0\n"
    "v_mov_b64 v[52:53], 0\n"
    "v_mov_b64 v[54:55], 0\n"
    "s_add_u32 s90, s90, 1\n"
    "s_and_b32 s78, s90, 0xff\n"
    "s_cmp_lg_u32 s78, 0\n"
    "s_cbranch_scc1 L_re3%=\n"
    "v_mov_b32 v126, 0\n"
    "global_load_dwordx2 v[126:127], v126, s[68:69] sc1\n"
    "s_waitcnt vmcnt(0)\n"
    "v_cmp_eq_u64 vcc, s[96:97], v[126:127]\n"
    "s_nop 1\n"
    "s_cbranch_vccnz L_bail_g3%=\n"
    "s_cmp_lt_u32 s90, 0x40000\n"
    "s_cbranch_scc1 L_re3%=\n"
    "v_mov_b32 v126, s96\n"
    "v_mov_b32 v127, s97\n"
    "s_mov_b64 exec, 1\n"
    "global_store_dwordx2 v71, v[126:127], s[68:69] sc1\n"
    "s_mov_b64 exec, -1\n"
    "L_bail_g3%=:\n"
    "s_branch L_go3%=\n"
    "L_re3%=:\n"
    "s_waitcnt vmcnt(0) lgkmcnt(0)\n"
    "s_branch L_top3%=\n"
    "L_last3%=:\n"
    "v_smfmac_f32_16x16x64_bf16 v[52:55], v[20:23], a[232:239], v67\n"
    "v_smfmac_f32_16x16x64_bf16 v[48:51], v[12:15], a[152:159], v67\n"
    "v_smfmac_f32_16x16x64_bf16 v[52:55], v[12:15], a[216:223], v67\n"
    "v_smfmac_f32_16x16x64_bf16 v[48:51], v[4:7], a[136:143], v67\n"
    "v_smfmac_f32_16x16x64_bf16 v[52:55], v[4:7], a[200:207], v67\n"
    "s_add_u32 s11, s43, 0\n"
    "s_and_b32 s11, s11, 3\n"
    "s_cselect_b32 s11, 0, 0x2000\n"
    "s_add_u32 s62, s62, 0x80000\n"
    "s_addc_u32 s63, s63, 0\n"
    "s_xor_b32 s62, s62, s11\n"
    "v_pk_add_f32 v[48:49], v[48:49], v[50:51]\n"
    "s_nop 0\n"
    "v_pk_add_f32 v[52:53], v[52:53], v[54:55]\n"
    "v_cvt_pk_bf16_f32 v66, v48, v49\n"
    "v_cvt_pk_bf16_f32 v74, v52, v53\n"
    "s_cmp_lg_u32 s42, 0\n"
    "s_cbranch_scc1 L_vmc3%=\n"
    "s_mov_b32 exec_lo, 0\n"
    "global_store_dwordx4 v71, v[44:47], s[62:63] offset:1024 sc1\n"
    "s_mov_b64 exec, -1\n"
    "s_branch L_kvc3%=\n"
    "L_vmc3%=:\n"
    "v_mov_b32 v126, v66\n"
    "v_mov_b32 v127, v74\n"
    "global_store_dwordx2 v79, v[126:127], s[62:63] offset:1536 sc1\n"
    "L_kvc3%=:\n"
    "s_waitcnt vmcnt(0) lgkmcnt(0)\n"
    "s_endpgm\n"
    :: "s"(ws), "s"(__builtin_amdgcn_readfirstlane(by * 4 + bx)), "s"(wave), "s"(ldsb), "s"(ws + OFF_EP), "s"(Wq), "s"(Wk), "s"(Wv), "s"(Wo) PADOP
    : "memory", "vcc", "v255", "a255", "s34", "s35", "s36", "s37", "s38", "s39", "s40", "s96", "s97", "s101", "s22", "s23", "s24", "s25", "s26", "s27", "s28", "s29", "scc");
  __builtin_unreachable();
}

extern "C" void kernel_launch(void* const* d_in, const int* in_sizes, int n_in, void* d_out, int out_size, void* d_ws, size_t ws_size,
                              hipStream_t stream) {
  const u16* x = (const u16*)d_in[0];
  const u16* mem0 = (const u16*)d_in[1];
  const u16* Wq = (const u16*)d_in[2];
  const u16* Wk = (const u16*)d_in[3];
  const u16* Wv = (const u16*)d_in[4];
  const u16* Wo = (const u16*)d_in[5];
  const u16* w_r = (const u16*)d_in[8];
  const u16* w_gate = (const u16*)d_in[9];
  const u16* w_up = (const u16*)d_in[10];
  const u16* w_down = (const u16*)d_in[11];
  u16* out = (u16*)d_out;
  char* ws = (char*)(((size_t)d_ws + 2 * MBY - 1) & ~(size_t)(2 * MBY - 1));
  rec_kernel<<<256, 256, 0, stream>>>(ws, x, mem0, Wq, Wk, Wv, Wo, w_r, w_gate, w_up, w_down, out);
}
```

```cpp
#define WIDE
#include <hip/hip_runtime.h>
#include <hip/hip_bf16.h>

typedef unsigned short u16;
typedef unsigned long long u64;
typedef __attribute__((ext_vector_type(8))) short bf16x8;
typedef __attribute__((ext_vector_type(4))) float f32x4;
typedef __attribute__((ext_vector_type(4))) unsigned u32x4;
#define GAS __attribute__((address_space(1)))
typedef GAS u64 gu64;
typedef GAS unsigned gu32;
#define RLX_AGENT __ATOMIC_RELAXED, __HIP_MEMORY_SCOPE_AGENT

constexpr int S_ = 2048, DM = 512, NH = 32, DH = 128, MM = 8, SEG = 32, NSEG = 64;
constexpr int HD = NH * DH;
constexpr int QKVN = 3 * HD;
constexpr int FF = 1792;

__device__ __forceinline__ float bf2f(u16 v) { return __builtin_bit_cast(float, (unsigned)v << 16); }
__device__ __forceinline__ float rbf(float f) { return (float)(__bf16)f; }

template <int CTRL> __device__ __forceinline__ float dpp_add(float v) {
  return v + __builtin_bit_cast(float, __builtin_amdgcn_update_dpp(0, __builtin_bit_cast(int, v), CTRL, 0xf, 0xf, false));
}
__device__ __forceinline__ float wave_sum(float v) {
  v = dpp_add<0x121>(v); v = dpp_add<0x122>(v); v = dpp_add<0x124>(v); v = dpp_add<0x128>(v);
  auto r = __builtin_amdgcn_permlane16_swap(__builtin_bit_cast(unsigned, v), __builtin_bit_cast(unsigned, v), false, false);
  const unsigned r0 = r[0], r1 = r[1];
  v = __builtin_bit_cast(float, r0) + __builtin_bit_cast(float, r1);
  auto q = __builtin_amdgcn_permlane32_swap(__builtin_bit_cast(unsigned, v), __builtin_bit_cast(unsigned, v), false, false);
  const unsigned q0 = q[0], q1 = q[1];
  return __builtin_bit_cast(float, q0) + __builtin_bit_cast(float, q1);
}
typedef __bf16 bf16x2_t __attribute__((ext_vector_type(2)));
typedef float f32x2_t __attribute__((ext_vector_type(2)));
__device__ __forceinline__ unsigned pk2(float a, float b) {
  f32x2_t v; v.x = a; v.y = b;
  return __builtin_bit_cast(unsigned, __builtin_convertvector(v, bf16x2_t));
}
__device__ __forceinline__ void rbf2(float& a, float& b) {
  const unsigned p = pk2(a, b);
  a = __builtin_bit_cast(float, p << 16); b = __builtin_bit_cast(float, p & 0xffff0000u);
}
__device__ __forceinline__ void ln_row(const float (&x)[8], float (&y)[8]) {
  float s = ((x[0] + x[1]) + (x[2] + x[3])) + ((x[4] + x[5]) + (x[6] + x[7]));
  s = wave_sum(s);
  const float mean = s * (1.0f / DM);
  float q = 0.f;
#pragma unroll
  for (int i = 0; i < 8; ++i) { float d = x[i] - mean; q += d * d; }
  q = wave_sum(q);
  float var = q * (1.0f / DM), mb = mean;
  rbf2(var, mb);
  const float sd = rbf(__builtin_amdgcn_sqrtf(rbf(var + 1e-5f)));
  const float inv = __builtin_amdgcn_rcpf(sd);
#pragma unroll
  for (int i = 0; i < 8; i += 2) {
    float a = x[i] - mb, b = x[i + 1] - mb;
    rbf2(a, b);
    a *= inv; b *= inv;
    rbf2(a, b);
    y[i] = a; y[i + 1] = b;
  }
}

constexpr size_t MBY = 1u << 20;
constexpr size_t OFF_G2 = 0, OFF_G3 = MBY, OFF_SYNC = 2 * MBY;
constexpr size_t OFF_EP = OFF_SYNC, OFF_ERR = OFF_SYNC + 512, OFF_RDY = OFF_SYNC + 0x1000, OFF_ACT = OFF_SYNC + 0x140000, OFF_RC2 = OFF_SYNC + 0x128000;
constexpr size_t OFF_CLM = OFF_SYNC + 0x10000, OFF_TAB = OFF_SYNC + 0xC000, OFF_XFL = OFF_SYNC + 0xE000;
constexpr size_t OFF_PIL = OFF_G3 + 0x80000, OFF_FLAGS = OFF_SYNC + 0x22000, OFF_OFL = OFF_SYNC + 0xA2000, OFF_HFL = OFF_SYNC + 0xE2000;
constexpr size_t OFF_RC = OFF_SYNC + 0x102000, OFF_PAR = OFF_SYNC + 0x112000, OFF_PRB = OFF_SYNC + 0x114000;
constexpr unsigned SENT = 0xFFBFFFBFu;
#define BB 12
constexpr size_t BSZ = 32 * MBY;
__device__ __forceinline__ int blk(int s, int i, unsigned far) { return s * 524288 + i * 16384 + (int)(far ^ ((s & 4) << 11)); }
constexpr size_t OFF_XN = 4 * MBY, OFF_KF = BB * MBY, OFF_VF = OFF_KF + BSZ, OFF_QF = OFF_VF + BSZ, OFF_KM = OFF_QF + BSZ;
constexpr size_t OFF_O = OFF_KM + BSZ, OFF_HP = OFF_O + BSZ;

__device__ __forceinline__ unsigned tl_idx() { unsigned xcc; asm volatile("s_getreg_b32 %0, hwreg(HW_REG_XCC_ID)" : "=s"(xcc)); return (blockIdx.x & ~7u) | (xcc & 7u); }
#define GS(i) do {} while (0)
#define TS2(i) do {} while (0)
#define TSD() do {} while (0)
#define TL(i) do {} while (0)
#define TLP() do {} while (0)
#define TLX(i) do {} while (0)
#define PT(k, c) do {} while (0)
#define PTD do {} while (0)
#define PTE do {} while (0)
__device__ __forceinline__ u64 now_clk() { u64 t; asm volatile("s_memtime %0\n\ts_waitcnt lgkmcnt(0)" : "=s"(t) :: "memory"); return t; }

constexpr int NREC = 64;

__device__ __forceinline__ bool spin_bail(unsigned& spins, char* ws, u64 T) {
  ++spins;
  if ((spins & 255u) == 0u) {
    gu64* err = (gu64*)(ws + OFF_ERR);
    if (__hip_atomic_load(err, RLX_AGENT) == T) return true;
    if (spins > (1u << 20)) { __hip_atomic_store(err, T, RLX_AGENT); return true; }
  }
  return false;
}
__device__ __forceinline__ void set_flag(char* ws, size_t off, u64 T, unsigned v) { __hip_atomic_store((gu64*)(ws + off), T + v, RLX_AGENT); }
__device__ __forceinline__ bf16x8 mk8(unsigned a, unsigned b, unsigned c, unsigned d) {
  u32x4 v; v.x = a; v.y = b; v.z = c; v.w = d; return __builtin_bit_cast(bf16x8, v);
}
__device__ __forceinline__ void store16_sc1(const void* base, unsigned off, u32x4 v) {
  asm volatile("global_store_dwordx4 %0, %1, %2 sc1\n\ts_nop 1" :: "v"(off), "v"(v), "s"(base) : "memory");
}

#define NSPEC (58 * 32)
#define ASL 24
#define QSL 1
#define ADL 0
#define ATN 0
#define QDL 90
#define SLATE 56
#define NKP 6
#define WARMX 2
#define WDL 127
#define DSL 50
#define HFP 0
#define OFNT 2
#define NSIX 48
#define XROT 0
#define NPROBE 14
#define PSL 1
#define HSL 6
#define LAS __attribute__((address_space(3)))
__device__ __forceinline__ void unpack8(const u32x4 v, float (&f)[8]) {
  f[0] = bf2f(v.x & 0xffff); f[1] = bf2f(v.x >> 16); f[2] = bf2f(v.y & 0xffff); f[3] = bf2f(v.y >> 16);
  f[4] = bf2f(v.z & 0xffff); f[5] = bf2f(v.z >> 16); f[6] = bf2f(v.w & 0xffff); f[7] = bf2f(v.w >> 16);
}
template <int SL>
__device__ __forceinline__ void wait_flag(char* ws, unsigned off, u64 T, unsigned target) {
  gu64* p = (gu64*)(ws + off);
  unsigned spins = 0;
  while (!__all(__hip_atomic_load(p, RLX_AGENT) - T - target < 64u - target)) {
    __builtin_amdgcn_s_sleep(SL);
    if (spin_bail(spins, ws, T)) break;
  }
  __builtin_amdgcn_fence(__ATOMIC_ACQUIRE, "wavefront");
}
__device__ __forceinline__ bool marked(const u32x4 v) { return v.x == SENT || v.y == SENT || v.z == SENT || v.w == SENT; }
typedef __attribute__((ext_vector_type(4))) short s16x4;
__device__ __forceinline__ bf16x8 tr_frag(const char* p0, const char* p1) {
  const s16x4 a = __builtin_amdgcn_ds_read_tr16_b64_v4i16((LAS s16x4*)p0), b = __builtin_amdgcn_ds_read_tr16_b64_v4i16((LAS s16x4*)p1);
  return __builtin_shufflevector(a, b, 0, 1, 2, 3, 4, 5, 6, 7);
}

__device__ __forceinline__ float sum_g(float v) {
  auto r = __builtin_amdgcn_permlane16_swap(__builtin_bit_cast(unsigned, v), __builtin_bit_cast(unsigned, v), false, false);
  const unsigned r0 = r[0], r1 = r[1];
  v = __builtin_bit_cast(float, r0) + __builtin_bit_cast(float, r1);
  auto q = __builtin_amdgcn_permlane32_swap(__builtin_bit_cast(unsigned, v), __builtin_bit_cast(unsigned, v), false, false);
  const unsigned q0 = q[0], q1 = q[1];
  return __builtin_bit_cast(float, q0) + __builtin_bit_cast(float, q1);
}
__device__ __forceinline__ float max_g(float v) {
  auto r = __builtin_amdgcn_permlane16_swap(__builtin_bit_cast(unsigned, v), __builtin_bit_cast(unsigned, v), false, false);
  const unsigned r0 = r[0], r1 = r[1];
  v = fmaxf(__builtin_bit_cast(float, r0), __builtin_bit_cast(float, r1));
  auto q = __builtin_amdgcn_permlane32_swap(__builtin_bit_cast(unsigned, v), __builtin_bit_cast(unsigned, v), false, false);
  const unsigned q0 = q[0], q1 = q[1];
  return fmaxf(__builtin_bit_cast(float, q0), __builtin_bit_cast(float, q1));
}
__device__ __forceinline__ void ln_tile(u32x4 (&xr)[8]) {
  auto up = [](unsigned u) { f32x2_t v; v.x = __builtin_bit_cast(float, u << 16); v.y = __builtin_bit_cast(float, u & 0xffff0000u); return v; };
  f32x2_t f[8][4], sb[4] = {{0.f, 0.f}, {0.f, 0.f}, {0.f, 0.f}, {0.f, 0.f}};
#pragma unroll
  for (int k = 0; k < 8; ++k)
#pragma unroll
    for (int e = 0; e < 4; ++e) { const unsigned u = xr[k][e]; f[k][e] = up(u); sb[e] += f[k][e]; }
  const f32x2_t sa = (sb[0] + sb[1]) + (sb[2] + sb[3]);
  const float mean = sum_g(dpp_add<0x128>(sa.x + sa.y)) * (1.0f / DM);
  f32x2_t qb[4] = {{0.f, 0.f}, {0.f, 0.f}, {0.f, 0.f}, {0.f, 0.f}};
#pragma unroll
  for (int k = 0; k < 8; ++k)
#pragma unroll
    for (int e = 0; e < 4; ++e) { const f32x2_t d = f[k][e] - mean; qb[e] += d * d; }
  const f32x2_t qa = (qb[0] + qb[1]) + (qb[2] + qb[3]);
  float var = sum_g(dpp_add<0x128>(qa.x + qa.y)) * (1.0f / DM), mb = mean;
  rbf2(var, mb);
  const float inv = __builtin_amdgcn_rcpf(rbf(__builtin_amdgcn_sqrtf(rbf(var + 1e-5f))));
#pragma unroll
  for (int k = 0; k < 8; ++k)
#pragma unroll
    for (int e = 0; e < 4; ++e) {
      const f32x2_t d = f[k][e] - mb;
      const f32x2_t y = up(pk2(d.x, d.y)) * inv;
      xr[k][e] = pk2(y.x, y.y);
    }
}
__device__ __forceinline__ u64 uni(u64 v) { return ((u64)(unsigned)__builtin_amdgcn_readfirstlane((unsigned)(v >> 32)) << 32) | (u64)(unsigned)__builtin_amdgcn_readfirstlane((unsigned)v); }
__device__ __forceinline__ unsigned pgc(unsigned c) { return ((c & 1u) << 2) | (c >> 1); }
__device__ __forceinline__ unsigned cand(int q, int i) { const unsigned j = (i + (q >> 3)) & 3u; return j * 2 + ((j ^ (j >> 1) ^ (q >> 2)) & 1u); }
__device__ __forceinline__ void probe(char* ws, int q, int lane) {
  const char* p = ws + OFF_G3 + 0xC0000 + (q & 7) * 32768 + (q >> 3) * 128;
  unsigned z = 0;
  u64 ep, r = 0;
  asm volatile("s_load_dwordx2 %1, %4, 0x0\n\tglobal_atomic_or %0, %2, %0, %3 sc0 sc1\n\ts_waitcnt vmcnt(0) lgkmcnt(0)" : "+v"(z), "=&s"(ep) : "v"((lane & 7) * 4096), "s"(p), "s"(ws + OFF_EP) : "memory");
  TLX(2240 + q);
#pragma unroll 1
  for (int i = 0; i < 4; ++i) {
    u64 t0, t1;
    asm volatile("s_mov_b64 exec, 1\n\tv_mov_b32 %0, 0\n\ts_memtime %1\n\ts_waitcnt lgkmcnt(0)\n\tglobal_atomic_or %0, %0, %0, %3 sc0 sc1\n\ts_waitcnt vmcnt(0)\n\t"
                 "s_memtime %2\n\ts_waitcnt lgkmcnt(0)\n\ts_mov_b64 exec, -1" : "=&v"(z), "=&s"(t0), "=&s"(t1) : "s"(p + (cand(q, i) ^ pgc(q & 3) ^ 5u) * 4096) : "memory");
    r |= (u64)min((unsigned)(t1 - t0), 65535u) << (16 * i);
  }
  const u64 T = (uni(ep) + 1) << 6;
  TLX(2304 + q);
  if (lane < 8) store16_sc1(ws + OFF_PRB, (unsigned)(q * 128 + lane * 16), (u32x4){(unsigned)T, (unsigned)(T >> 32), (unsigned)r, (unsigned)(r >> 32)});
}
__device__ __forceinline__ void decide(char* ws, u64 T, unsigned* m, int lane) {
  const __amdgpu_buffer_rsrc_t rs = __builtin_amdgcn_make_buffer_rsrc(ws, 0, (int)(250 * MBY), 0x00020000);
  const int q = max(lane, 16);
  u32x4 v;
  bool ok;
  m[lane] = 65535u;
  TL(2);
  __builtin_amdgcn_s_sleep(DSL);
  for (unsigned spins = 0;;) {
    v = __builtin_bit_cast(u32x4, __builtin_amdgcn_raw_buffer_load_b128(rs, (int)OFF_PRB + q * 128, 0, 16));
    ok = v.x == (unsigned)T && v.y == (unsigned)(T >> 32);
    if (__builtin_popcountll(__ballot(ok) >> 16) >= 40 || spin_bail(spins, ws, T)) break;
    __builtin_amdgcn_s_sleep(4);
  }
  TL(3);
  if (ok) {
#pragma unroll
    for (int i = 0; i < 4; ++i) __hip_atomic_fetch_min(m + cand(q, i), ((i & 2 ? v.w : v.z) >> ((i & 1) * 16)) & 65535u, __ATOMIC_RELAXED, __HIP_MEMORY_SCOPE_WORKGROUP);
  }
  asm volatile("s_waitcnt lgkmcnt(0)" ::: "memory");
  const bool n = (m[2] + m[3]) + (m[6] + m[7]) < (m[0] + m[1]) + (m[4] + m[5]);
  const unsigned a = n ? m[2] : m[0], b = n ? m[3] : m[1], d = n ? m[6] : m[4], e = n ? m[7] : m[5];
  unsigned psi = (n ? 2u : 0u) | (d + e < a + b ? 4u : 0u) | (b + e < a + d ? 1u : 0u);
  const u64 w = T + 1 + psi;
  store16_sc1(ws + OFF_PAR, (unsigned)(lane * 16), (u32x4){(unsigned)w, (unsigned)(w >> 32), (unsigned)w, (unsigned)(w >> 32)});
}
__device__ __forceinline__ u64 tag_of(u64 ep) { return (ep + 1) << 6; }
__device__ __forceinline__ void ln1_phase(char* ws, int q, u64 T, char* lds, int tid) {
  const int lane = tid & 63, wv = tid >> 6;
  u32x4 xr[8];
#pragma unroll
  for (int k = 0; k < 8; ++k) xr[k] = *(const u32x4*)(lds + (wv * 8 + (lane & 7)) * 1040 + (lane >> 3) * 128 + k * 16);
  ln_tile(xr);
#pragma unroll
  for (int k = 0; k < 8; ++k) store16_sc1(ws + OFF_XN, (unsigned)((q * 2 + (wv >> 1)) * 16384 + ((lane >> 3) * 8 + k) * 256 + ((wv & 1) * 8 + (lane & 7)) * 16), xr[k]);
  if (wv < 2) store16_sc1(ws, (unsigned)((wv == 0 ? OFF_RC : OFF_RC2) + q * 1024 + lane * 16), (u32x4){0u, 0u, 0u, 0u});
  store16_sc1(ws + OFF_ACT, (unsigned)(q * 4096 + tid * 16), (u32x4){SENT, SENT, SENT, SENT});
  asm volatile("s_waitcnt vmcnt(0)" ::: "memory");
  __syncthreads();
  if (tid == 0) set_flag(ws, OFF_XFL + q * 128, T, 1);
  TL(6);
}

__device__ __forceinline__ bool load_h(const __amdgpu_buffer_rsrc_t rs, const u32x4 xv, int row, unsigned far, int lane, float (&f)[8]) {
  f32x4 p[4][2];
#pragma unroll
  for (int k = 0; k < 4; ++k)
#pragma unroll
    for (int u = 0; u < 2; ++u)
      p[k][u] = __builtin_bit_cast(f32x4, __builtin_amdgcn_raw_buffer_load_b128(rs, (int)OFF_HP + blk(row >> 5, (k * 4 + (lane >> 4)) * 2 + ((row >> 4) & 1), far) + (row & 15) * 512 + (lane & 15) * 32 + u * 16, 0, 16));
  float xf[8];
  unpack8(xv, xf);
#pragma unroll
  for (int i = 0; i < 8; ++i) f[i] = rbf(xf[i] + rbf((p[0][i >> 2][i & 3] + p[1][i >> 2][i & 3]) + (p[2][i >> 2][i & 3] + p[3][i >> 2][i & 3])));
  bool mk = false;
  if (__any(__builtin_isunordered(f[0] + f[1], f[2] + f[3]) || __builtin_isunordered(f[4] + f[5], f[6] + f[7]))) {
#pragma unroll
    for (int k = 0; k < 8; ++k) mk |= marked(__builtin_bit_cast(u32x4, p[k >> 1][k & 1]));
  }
  return __any(mk);
}

__device__ __forceinline__ unsigned wait_par(char* ws, u64 T, int copy) {
  unsigned spins = 0; u64 p;
  for (;;) {
    p = uni(__hip_atomic_load((gu64*)(ws + OFF_PAR + copy * 128), RLX_AGENT)) - T - 1;
    if (p < 8) break;
    __builtin_amdgcn_s_sleep(6);
    if (spin_bail(spins, ws, T)) break;
  }
  return (unsigned)p & 7u;
}
#define GCLOB "memory", "vcc", "scc", "s40", "s41", "s42", "s43", "s44", "s45", "s46", "s47", "s48", "s49", "s50", "s51", "s52", "s53", "s54", "s55", "s56", "s57", "s58", "s59", "s60", "s61", "s62", "s63", "s64", "s65", "s66", "s67", "s68", "s69", "s70", "s71", "v109", "v110", "v111", "v112", "v113", "v114", "v115", "v116", "v117", "v118", "v119", "v120", "v121", "v122", "v123", "v124", "v125", "v126", "v127", "v128", "v129", "v130", "v131", "v132", "v133", "v134", "v135", "v136", "v137", "v138", "v139", "v140", "v141", "v142", "v143", "v144", "v145", "v146", "v147", "v148", "v149", "v150", "v151", "v152", "v153", "v154", "v155", "v156", "v157", "v158", "v159", "v160", "v161", "v162", "v163", "v164", "v165", "v166", "v167", "v168", "v169", "v170", "v171", "v172", "v173", "v174", "v175", "v176", "v177", "v178", "v179", "v180", "v181", "v182", "v183", "v184", "v185", "v186", "v187", "v188", "v189", "v190", "v191", "v192", "v193", "v194", "v195", "v196", "v197", "v198", "v199", "v200", "v201", "v202", "v203", "v204", "v205", "v206", "v207", "v208", "v209", "v210", "v211", "v212", "v213", "v214", "v215", "v216", "v217", "v218", "v219", "v220", "v221", "v222", "v223", "v224", "v225", "v226", "v227", "v228", "v229", "v230", "v231", "v232", "v233", "v234", "v235", "v236", "v237", "v238", "v239", "v240", "v241", "v242", "v243", "v244", "v245", "v246", "v247", "v248", "v249", "v250", "v251", "v252", "v253", "v254", "v255", "a0", "a1", "a2", "a3", "a4", "a5", "a6", "a7", "a8", "a9", "a10", "a11", "a12", "a13", "a14", "a15", "a16", "a17", "a18", "a19", "a20", "a21", "a22", "a23", "a24", "a25", "a26", "a27", "a28", "a29", "a30", "a31", "a32", "a33", "a34", "a35", "a36", "a37", "a38", "a39", "a40", "a41", "a42", "a43", "a44", "a45", "a46", "a47", "a48", "a49", "a50", "a51", "a52", "a53", "a54", "a55", "a56", "a57", "a58", "a59", "a60", "a61", "a62", "a63", "a64", "a65", "a66", "a67", "a68", "a69", "a70", "a71", "a72", "a73", "a74", "a75", "a76", "a77", "a78", "a79", "a80", "a81", "a82", "a83", "a84", "a85", "a86", "a87", "a88", "a89", "a90", "a91", "a92", "a93", "a94", "a95", "a96", "a97", "a98", "a99", "a100", "a101", "a102", "a103", "a104", "a105", "a106", "a107", "a108", "a109", "a110", "a111", "a112", "a113", "a114", "a115", "a116", "a117", "a118", "a119", "a120", "a121", "a122", "a123", "a124", "a125", "a126", "a127", "a128", "a129", "a130", "a131", "a132", "a133", "a134", "a135", "a136", "a137", "a138", "a139", "a140", "a141", "a142", "a143", "a144", "a145", "a146", "a147", "a148", "a149", "a150", "a151", "a152", "a153", "a154", "a155", "a156", "a157", "a158", "a159", "a160", "a161", "a162", "a163", "a164", "a165", "a166", "a167", "a168", "a169", "a170", "a171", "a172", "a173", "a174", "a175", "a176", "a177", "a178", "a179", "a180", "a181", "a182", "a183", "a184", "a185", "a186", "a187", "a188", "a189", "a190", "a191", "a192", "a193", "a194", "a195", "a196", "a197", "a198", "a199", "a200", "a201", "a202", "a203", "a204", "a205", "a206", "a207", "a208", "a209", "a210", "a211", "a212", "a213", "a214", "a215", "a216", "a217", "a218", "a219", "a220", "a221", "a222", "a223", "a224", "a225", "a226", "a227", "a228", "a229", "a230", "a231", "a232", "a233", "a234", "a235", "a236", "a237", "a238", "a239", "a240", "a241", "a242", "a243", "a244", "a245", "a246", "a247", "a248", "a249", "a250", "a251", "a252", "a253", "a254", "a255"
template <int MAT>
__device__ __forceinline__ u64 qkv_phase(char* ws, int h, u64 ep0, int tid) {
  const int lane = tid & 63, wv = __builtin_amdgcn_readfirstlane(tid >> 6), l15 = lane & 15, g = lane >> 4;
  const int wr = wv >> 1, wc = wv & 1;
  TLX(512 + (MAT * 32 + h) * 16 + 1);
  const u64 T = tag_of(ep0);
  TLX(512 + (MAT * 32 + h) * 16 + 2);
  const int qq = l15 >> 2, f = qq | ((g & 1) << 2);
  const unsigned rp = (g * 8 + qq) * 256 + (l15 & 3) * 8 + ((wc * 4) ^ f) * 32;
  char* op = ws + (MAT == 0 ? OFF_QF : (MAT == 1 ? OFF_KF : OFF_VF)) + blk(wr, h, 0) + wc * 4096;
  char* rdy = ws + OFF_RDY + (MAT * 32 + h) * 128;
  asm volatile("s_waitcnt vmcnt(0)\n\ts_barrier" ::: "memory");
#define GIN "s"(ws), "s"(uni(T)), "s"(op), "s"(rdy), "s"(wv), "v"(lane * 16), "v"(rp)
  if constexpr (MAT == 0) asm volatile(
    "s_mov_b64 s[40:41], %0\n"
    "s_mov_b64 s[42:43], %1\n"
    "s_mov_b64 s[44:45], %2\n"
    "s_mov_b64 s[54:55], %3\n"
    "s_mov_b32 s56, %4\n"
    "v_mov_b32 v121, %5\n"
    "s_add_u32 s58, s40, 0x20e000\n"
    "s_addc_u32 s59, s41, 0\n"
    "v_lshlrev_b32 v225, 3, v121\n"
    "v_and_b32 v224, 0x380, v225\n"
    "global_load_dwordx2 v[226:227], v224, s[58:59] sc1\n"
    "v_xor_b32 v112, 0, %6\n"
    "v_xor_b32 v113, 32, %6\n"
    "v_xor_b32 v114, 64, %6\n"
    "v_xor_b32 v115, 96, %6\n"
    "ds_read_b64_tr_b16 a[0:1], v112 offset:0\n"
    "ds_read_b64_tr_b16 a[2:3], v112 offset:1024\n"
    "ds_read_b64_tr_b16 a[4:5], v112 offset:8192\n"
    "ds_read_b64_tr_b16 a[6:7], v112 offset:9216\n"
    "ds_read_b64_tr_b16 a[8:9], v112 offset:16384\n"
    "ds_read_b64_tr_b16 a[10:11], v112 offset:17408\n"
    "ds_read_b64_tr_b16 a[12:13], v112 offset:24576\n"
    "ds_read_b64_tr_b16 a[14:15], v112 offset:25600\n"
    "s_waitcnt lgkmcnt(6)\n"
    "ds_read_b64_tr_b16 a[16:17], v112 offset:32768\n"
    "ds_read_b64_tr_b16 a[18:19], v112 offset:33792\n"
    "ds_read_b64_tr_b16 a[20:21], v112 offset:40960\n"
    "ds_read_b64_tr_b16 a[22:23], v112 offset:41984\n"
    "ds_read_b64_tr_b16 a[24:25], v112 offset:49152\n"
    "ds_read_b64_tr_b16 a[26:27], v112 offset:50176\n"
    "ds_read_b64_tr_b16 a[28:29], v112 offset:57344\n"
    "ds_read_b64_tr_b16 a[30:31], v112 offset:58368\n"
    "s_waitcnt lgkmcnt(6)\n"
    "ds_read_b64_tr_b16 a[64:65], v113 offset:0\n"
    "ds_read_b64_tr_b16 a[66:67], v113 offset:1024\n"
    "ds_read_b64_tr_b16 a[68:69], v113 offset:8192\n"
    "ds_read_b64_tr_b16 a[70:71], v113 offset:9216\n"
    "ds_read_b64_tr_b16 a[72:73], v113 offset:16384\n"
    "ds_read_b64_tr_b16 a[74:75], v113 offset:17408\n"
    "ds_read_b64_tr_b16 a[76:77], v113 offset:24576\n"
    "ds_read_b64_tr_b16 a[78:79], v113 offset:25600\n"
    "s_waitcnt lgkmcnt(6)\n"
    "ds_read_b64_tr_b16 a[80:81], v113 offset:32768\n"
    "ds_read_b64_tr_b16 a[82:83], v113 offset:33792\n"
    "ds_read_b64_tr_b16 a[84:85], v113 offset:40960\n"
    "ds_read_b64_tr_b16 a[86:87], v113 offset:41984\n"
    "ds_read_b64_tr_b16 a[88:89], v113 offset:49152\n"
    "ds_read_b64_tr_b16 a[90:91], v113 offset:50176\n"
    "ds_read_b64_tr_b16 a[92:93], v113 offset:57344\n"
    "ds_read_b64_tr_b16 a[94:95], v113 offset:58368\n"
    "s_waitcnt lgkmcnt(6)\n"
    "ds_read_b64_tr_b16 a[128:129], v114 offset:0\n"
    "ds_read_b64_tr_b16 a[130:131], v114 offset:1024\n"
    "ds_read_b64_tr_b16 a[132:133], v114 offset:8192\n"
    "ds_read_b64_tr_b16 a[134:135], v114 offset:9216\n"
    "ds_read_b64_tr_b16 a[136:137], v114 offset:16384\n"
    "ds_read_b64_tr_b16 a[138:139], v114 offset:17408\n"
    "ds_read_b64_tr_b16 a[140:141], v114 offset:24576\n"
    "ds_read_b64_tr_b16 a[142:143], v114 offset:25600\n"
    "s_waitcnt lgkmcnt(6)\n"
    "ds_read_b64_tr_b16 a[144:145], v114 offset:32768\n"
    "ds_read_b64_tr_b16 a[146:147], v114 offset:33792\n"
    "ds_read_b64_tr_b16 a[148:149], v114 offset:40960\n"
    "ds_read_b64_tr_b16 a[150:151], v114 offset:41984\n"
    "ds_read_b64_tr_b16 a[152:153], v114 offset:49152\n"
    "ds_read_b64_tr_b16 a[154:155], v114 offset:50176\n"
    "ds_read_b64_tr_b16 a[156:157], v114 offset:57344\n"
    "ds_read_b64_tr_b16 a[158:159], v114 offset:58368\n"
    "s_waitcnt lgkmcnt(6)\n"
    "ds_read_b64_tr_b16 a[192:193], v115 offset:0\n"
    "ds_read_b64_tr_b16 a[194:195], v115 offset:1024\n"
    "ds_read_b64_tr_b16 a[196:197], v115 offset:8192\n"
    "ds_read_b64_tr_b16 a[198:199], v115 offset:9216\n"
    "ds_read_b64_tr_b16 a[200:201], v115 offset:16384\n"
    "ds_read_b64_tr_b16 a[202:203], v115 offset:17408\n"
    "ds_read_b64_tr_b16 a[204:205], v115 offset:24576\n"
    "ds_read_b64_tr_b16 a[206:207], v115 offset:25600\n"
    "s_waitcnt lgkmcnt(6)\n"
    "ds_read_b64_tr_b16 a[208:209], v115 offset:32768\n"
    "ds_read_b64_tr_b16 a[210:211], v115 offset:33792\n"
    "ds_read_b64_tr_b16 a[212:213], v115 offset:40960\n"
    "ds_read_b64_tr_b16 a[214:215], v115 offset:41984\n"
    "ds_read_b64_tr_b16 a[216:217], v115 offset:49152\n"
    "ds_read_b64_tr_b16 a[218:219], v115 offset:50176\n"
    "ds_read_b64_tr_b16 a[220:221], v115 offset:57344\n"
    "ds_read_b64_tr_b16 a[222:223], v115 offset:58368\n"
    "s_waitcnt lgkmcnt(6)\n"
    "v_xor_b32 v112, 0, %6\n"
    "v_add_u32 v112, 0x10000, v112\n"
    "v_xor_b32 v113, 32, %6\n"
    "v_add_u32 v113, 0x10000, v113\n"
    "v_xor_b32 v114, 64, %6\n"
    "v_add_u32 v114, 0x10000, v114\n"
    "v_xor_b32 v115, 96, %6\n"
    "v_add_u32 v115, 0x10000, v115\n"
    "ds_read_b64_tr_b16 a[32:33], v112 offset:0\n"
    "ds_read_b64_tr_b16 a[34:35], v112 offset:1024\n"
    "ds_read_b64_tr_b16 a[36:37], v112 offset:8192\n"
    "ds_read_b64_tr_b16 a[38:39], v112 offset:9216\n"
    "ds_read_b64_tr_b16 a[40:41], v112 offset:16384\n"
    "ds_read_b64_tr_b16 a[42:43], v112 offset:17408\n"
    "ds_read_b64_tr_b16 a[44:45], v112 offset:24576\n"
    "ds_read_b64_tr_b16 a[46:47], v112 offset:25600\n"
    "s_waitcnt lgkmcnt(6)\n"
    "ds_read_b64_tr_b16 a[48:49], v112 offset:32768\n"
    "ds_read_b64_tr_b16 a[50:51], v112 offset:33792\n"
    "ds_read_b64_tr_b16 a[52:53], v112 offset:40960\n"
    "ds_read_b64_tr_b16 a[54:55], v112 offset:41984\n"
    "ds_read_b64_tr_b16 a[56:57], v112 offset:49152\n"
    "ds_read_b64_tr_b16 a[58:59], v112 offset:50176\n"
    "ds_read_b64_tr_b16 a[60:61], v112 offset:57344\n"
    "ds_read_b64_tr_b16 a[62:63], v112 offset:58368\n"
    "s_waitcnt lgkmcnt(6)\n"
    "ds_read_b64_tr_b16 a[96:97], v113 offset:0\n"
    "ds_read_b64_tr_b16 a[98:99], v113 offset:1024\n"
    "ds_read_b64_tr_b16 a[100:101], v113 offset:8192\n"
    "ds_read_b64_tr_b16 a[102:103], v113 offset:9216\n"
    "ds_read_b64_tr_b16 a[104:105], v113 offset:16384\n"
    "ds_read_b64_tr_b16 a[106:107], v113 offset:17408\n"
    "ds_read_b64_tr_b16 a[108:109], v113 offset:24576\n"
    "ds_read_b64_tr_b16 a[110:111], v113 offset:25600\n"
    "s_waitcnt lgkmcnt(6)\n"
    "ds_read_b64_tr_b16 a[112:113], v113 offset:32768\n"
    "ds_read_b64_tr_b16 a[114:115], v113 offset:33792\n"
    "ds_read_b64_tr_b16 a[116:117], v113 offset:40960\n"
    "ds_read_b64_tr_b16 a[118:119], v113 offset:41984\n"
    "ds_read_b64_tr_b16 a[120:121], v113 offset:49152\n"
    "ds_read_b64_tr_b16 a[122:123], v113 offset:50176\n"
    "ds_read_b64_tr_b16 a[124:125], v113 offset:57344\n"
    "ds_read_b64_tr_b16 a[126:127], v113 offset:58368\n"
    "s_waitcnt lgkmcnt(6)\n"
    "ds_read_b64_tr_b16 a[160:161], v114 offset:0\n"
    "ds_read_b64_tr_b16 a[162:163], v114 offset:1024\n"
    "ds_read_b64_tr_b16 a[164:165], v114 offset:8192\n"
    "ds_read_b64_tr_b16 a[166:167], v114 offset:9216\n"
    "ds_read_b64_tr_b16 a[168:169], v114 offset:16384\n"
    "ds_read_b64_tr_b16 a[170:171], v114 offset:17408\n"
    "ds_read_b64_tr_b16 a[172:173], v114 offset:24576\n"
    "ds_read_b64_tr_b16 a[174:175], v114 offset:25600\n"
    "s_waitcnt lgkmcnt(6)\n"
    "ds_read_b64_tr_b16 a[176:177], v114 offset:32768\n"
    "ds_read_b64_tr_b16 a[178:179], v114 offset:33792\n"
    "ds_read_b64_tr_b16 a[180:181], v114 offset:40960\n"
    "ds_read_b64_tr_b16 a[182:183], v114 offset:41984\n"
    "ds_read_b64_tr_b16 a[184:185], v114 offset:49152\n"
    "ds_read_b64_tr_b16 a[186:187], v114 offset:50176\n"
    "ds_read_b64_tr_b16 a[188:189], v114 offset:57344\n"
    "ds_read_b64_tr_b16 a[190:191], v114 offset:58368\n"
    "s_waitcnt lgkmcnt(6)\n"
    "ds_read_b64_tr_b16 a[224:225], v115 offset:0\n"
    "ds_read_b64_tr_b16 a[226:227], v115 offset:1024\n"
    "ds_read_b64_tr_b16 a[228:229], v115 offset:8192\n"
    "ds_read_b64_tr_b16 a[230:231], v115 offset:9216\n"
    "ds_read_b64_tr_b16 a[232:233], v115 offset:16384\n"
    "ds_read_b64_tr_b16 a[234:235], v115 offset:17408\n"
    "ds_read_b64_tr_b16 a[236:237], v115 offset:24576\n"
    "ds_read_b64_tr_b16 a[238:239], v115 offset:25600\n"
    "s_waitcnt lgkmcnt(6)\n"
    "ds_read_b64_tr_b16 a[240:241], v115 offset:32768\n"
    "ds_read_b64_tr_b16 a[242:243], v115 offset:33792\n"
    "ds_read_b64_tr_b16 a[244:245], v115 offset:40960\n"
    "ds_read_b64_tr_b16 a[246:247], v115 offset:41984\n"
    "ds_read_b64_tr_b16 a[248:249], v115 offset:49152\n"
    "ds_read_b64_tr_b16 a[250:251], v115 offset:50176\n"
    "ds_read_b64_tr_b16 a[252:253], v115 offset:57344\n"
    "ds_read_b64_tr_b16 a[254:255], v115 offset:58368\n"
    "s_waitcnt lgkmcnt(6)\n"
    "s_lshr_b32 s58, s56, 1\n"
    "s_lshl_b32 s58, s58, 15\n"
    "v_add_u32 v122, s58, v121\n"
    "v_add_u32 v123, 0x10000, v122\n"
    "s_lshl_b32 s50, s56, 14\n"
    "v_mov_b32 v118, 0\n"
    "v_mov_b32 v117, s43\n"
    "s_add_u32 s58, s50, 0x400000\n"
    "s_add_u32 s48, s40, s58\n"
    "s_addc_u32 s49, s41, 0\n"
    "s_mov_b32 s57, 0\n"
    "s_mov_b32 s51, 0\n"
    "s_waitcnt lgkmcnt(0)\n"
    "s_barrier\n"
    "s_add_u32 s58, s40, 0x20e000\n"
    "s_addc_u32 s59, s41, 0\n"
    "s_add_u32 s60, s40, 0x200200\n"
    "s_addc_u32 s61, s41, 0\n"
    "s_or_b32 s62, s42, 1\n"
    "s_mov_b32 s63, s43\n"
    "s_branch G_xc0%=\n"
    "G_xw0%=:\n"
    "global_load_dwordx2 v[226:227], v224, s[58:59] sc1\n"
    "G_xc0%=:\n"
    "s_waitcnt vmcnt(0)\n"
    "v_cmp_ne_u64 vcc, s[62:63], v[226:227]\n"
    "s_nop 0\n"
    "s_cbranch_vccz G_xok0%=\n"
    "s_sleep 4\n"
    "s_add_u32 s57, s57, 1\n"
    "s_and_b32 s68, s57, 0xff\n"
    "s_cbranch_scc1 G_xw0%=\n"
    "global_load_dwordx2 v[226:227], v118, s[60:61] sc1\n"
    "s_waitcnt vmcnt(0)\n"
    "v_cmp_eq_u64 vcc, s[42:43], v[226:227]\n"
    "s_nop 0\n"
    "s_cbranch_vccnz G_xok0%=\n"
    "s_cmp_lt_u32 s57, 0x40000\n"
    "s_cbranch_scc1 G_xw0%=\n"
    "v_mov_b32 v116, s42\n"
    "global_store_dwordx2 v118, v[116:117], s[60:61] sc1\n"
    "G_xok0%=:\n"
    "s_mov_b32 vcc_lo, 0xcccccccc\n"
    "s_mov_b32 vcc_hi, 0xcccccccc\n"
    "s_add_u32 m0, s50, 0\n"
    "s_add_u32 s64, s48, 0\n"
    "s_addc_u32 s65, s49, 0\n"
    "global_load_lds_dwordx4 v121, s[64:65] offset:0 sc1\n"
    "global_load_lds_dwordx4 v121, s[64:65] offset:1024 sc1\n"
    "global_load_lds_dwordx4 v121, s[64:65] offset:2048 sc1\n"
    "global_load_lds_dwordx4 v121, s[64:65] offset:3072 sc1\n"
    "s_add_u32 m0, s50, 4096\n"
    "s_add_u32 s64, s48, 4096\n"
    "s_addc_u32 s65, s49, 0\n"
    "global_load_lds_dwordx4 v121, s[64:65] offset:0 sc1\n"
    "global_load_lds_dwordx4 v121, s[64:65] offset:1024 sc1\n"
    "global_load_lds_dwordx4 v121, s[64:65] offset:2048 sc1\n"
    "global_load_lds_dwordx4 v121, s[64:65] offset:3072 sc1\n"
    "s_add_u32 m0, s50, 8192\n"
    "s_add_u32 s64, s48, 8192\n"
    "s_addc_u32 s65, s49, 0\n"
    "global_load_lds_dwordx4 v121, s[64:65] offset:0 sc1\n"
    "global_load_lds_dwordx4 v121, s[64:65] offset:1024 sc1\n"
    "global_load_lds_dwordx4 v121, s[64:65] offset:2048 sc1\n"
    "global_load_lds_dwordx4 v121, s[64:65] offset:3072 sc1\n"
    "s_add_u32 m0, s50, 12288\n"
    "s_add_u32 s64, s48, 12288\n"
    "s_addc_u32 s65, s49, 0\n"
    "s_add_u32 s48, s48, 0x10000\n"
    "s_addc_u32 s49, s49, 0\n"
    "global_load_lds_dwordx4 v121, s[64:65] offset:0 sc1\n"
    "global_load_lds_dwordx4 v121, s[64:65] offset:1024 sc1\n"
    "global_load_lds_dwordx4 v121, s[64:65] offset:2048 sc1\n"
    "global_load_lds_dwordx4 v121, s[64:65] offset:3072 sc1\n"
    "s_add_u32 m0, s50, 65536\n"
    "s_add_u32 s64, s48, 0\n"
    "s_addc_u32 s65, s49, 0\n"
    "global_load_lds_dwordx4 v121, s[64:65] offset:0 sc1\n"
    "global_load_lds_dwordx4 v121, s[64:65] offset:1024 sc1\n"
    "global_load_lds_dwordx4 v121, s[64:65] offset:2048 sc1\n"
    "global_load_lds_dwordx4 v121, s[64:65] offset:3072 sc1\n"
    "s_add_u32 m0, s50, 69632\n"
    "s_add_u32 s64, s48, 4096\n"
    "s_addc_u32 s65, s49, 0\n"
    "global_load_lds_dwordx4 v121, s[64:65] offset:0 sc1\n"
    "global_load_lds_dwordx4 v121, s[64:65] offset:1024 sc1\n"
    "global_load_lds_dwordx4 v121, s[64:65] offset:2048 sc1\n"
    "global_load_lds_dwordx4 v121, s[64:65] offset:3072 sc1\n"
    "s_add_u32 m0, s50, 73728\n"
    "s_add_u32 s64, s48, 8192\n"
    "s_addc_u32 s65, s49, 0\n"
    "global_load_lds_dwordx4 v121, s[64:65] offset:0 sc1\n"
    "global_load_lds_dwordx4 v121, s[64:65] offset:1024 sc1\n"
    "global_load_lds_dwordx4 v121, s[64:65] offset:2048 sc1\n"
    "s_mov_b64 s[66:67], s[44:45]\n"
    "s_sub_u32 s44, s44, 0x4400000\n"
    "s_subb_u32 s45, s45, 0\n"
    "s_waitcnt vmcnt(19) lgkmcnt(0)\n"
    "s_barrier\n"
    "s_bfe_u32 s58, s54, 0x2000a\n"
    "s_lshl_b32 s58, s58, 7\n"
    "s_add_u32 s58, s58, 512\n"
    "v_mov_b32 v112, s58\n"
    "s_add_u32 s58, s40, 0x312000\n"
    "s_addc_u32 s59, s41, 0\n"
    "global_load_dwordx2 v[114:115], v112, s[58:59] sc1\n"
    "ds_read_b128 v[192:195], v122 offset:0\n"
    "ds_read_b128 v[196:199], v122 offset:16384\n"
    "ds_read_b128 v[200:203], v122 offset:1024\n"
    "ds_read_b128 v[204:207], v122 offset:17408\n"
    "ds_read_b128 v[208:211], v122 offset:2048\n"
    "ds_read_b128 v[212:215], v122 offset:18432\n"
    "s_waitcnt lgkmcnt(4)\n"
    "s_branch G_even%=\n"
    ".p2align 6\n"
    "G_loop%=:\n"
    "v_mfma_f32_16x16x32_bf16 v[160:163], a[0:3], v[192:195], 0\n"
    "ds_read_b128 v[216:219], v123 offset:3072\n"
    "v_mfma_f32_16x16x32_bf16 v[164:167], a[64:67], v[192:195], 0\n"
    "ds_read_b128 v[220:223], v123 offset:19456\n"
    "v_mfma_f32_16x16x32_bf16 v[168:171], a[128:131], v[192:195], 0\n"
    "v_mfma_f32_16x16x32_bf16 v[172:175], a[192:195], v[192:195], 0\n"
    "v_cvt_pk_bf16_f32 v224, v128, v129\n"
    "v_cvt_pk_bf16_f32 v225, v130, v131\n"
    "v_mfma_f32_16x16x32_bf16 v[176:179], a[0:3], v[196:199], 0\n"
    "global_load_lds_dwordx4 v121, s[64:65] offset:3072 sc1\n"
    "v_mfma_f32_16x16x32_bf16 v[180:183], a[64:67], v[196:199], 0\n"
    "v_cvt_pk_bf16_f32 v226, v132, v133\n"
    "v_cvt_pk_bf16_f32 v227, v134, v135\n"
    "v_mfma_f32_16x16x32_bf16 v[184:187], a[128:131], v[196:199], 0\n"
    "v_cvt_pk_bf16_f32 v228, v144, v145\n"
    "v_cvt_pk_bf16_f32 v229, v146, v147\n"
    "v_mfma_f32_16x16x32_bf16 v[188:191], a[192:195], v[196:199], 0\n"
    "v_cvt_pk_bf16_f32 v230, v148, v149\n"
    "v_cvt_pk_bf16_f32 v231, v150, v151\n"
    "s_waitcnt lgkmcnt(4)\n"
    "v_mfma_f32_16x16x32_bf16 v[160:163], a[4:7], v[200:203], v[160:163]\n"
    "ds_read_b128 v[192:195], v123 offset:4096\n"
    "v_mfma_f32_16x16x32_bf16 v[164:167], a[68:71], v[200:203], v[164:167]\n"
    "ds_read_b128 v[196:199], v123 offset:20480\n"
    "v_mfma_f32_16x16x32_bf16 v[168:171], a[132:135], v[200:203], v[168:171]\n"
    "s_add_u32 m0, s50, 12288\n"
    "s_add_u32 s64, s48, 12288\n"
    "s_addc_u32 s65, s49, 0\n"
    "v_cvt_pk_bf16_f32 v232, v136, v137\n"
    "v_cvt_pk_bf16_f32 v233, v138, v139\n"
    "v_mfma_f32_16x16x32_bf16 v[172:175], a[196:199], v[200:203], v[172:175]\n"
    "s_add_u32 s48, s48, 0x10000\n"
    "s_addc_u32 s49, s49, 0\n"
    "v_cvt_pk_bf16_f32 v234, v140, v141\n"
    "v_cvt_pk_bf16_f32 v235, v142, v143\n"
    "v_mfma_f32_16x16x32_bf16 v[176:179], a[4:7], v[204:207], v[176:179]\n"
    "global_load_lds_dwordx4 v121, s[64:65] offset:0 sc1\n"
    "v_mfma_f32_16x16x32_bf16 v[180:183], a[68:71], v[204:207], v[180:183]\n"
    "v_cvt_pk_bf16_f32 v236, v152, v153\n"
    "v_cvt_pk_bf16_f32 v237, v154, v155\n"
    "v_mfma_f32_16x16x32_bf16 v[184:187], a[132:135], v[204:207], v[184:187]\n"
    "global_store_dwordx4 v121, v[224:227], s[44:45] offset:0 sc1\n"
    "v_mfma_f32_16x16x32_bf16 v[188:191], a[196:199], v[204:207], v[188:191]\n"
    "v_cvt_pk_bf16_f32 v238, v156, v157\n"
    "v_cvt_pk_bf16_f32 v239, v158, v159\n"
    "s_waitcnt lgkmcnt(4)\n"
    "v_mfma_f32_16x16x32_bf16 v[160:163], a[8:11], v[208:211], v[160:163]\n"
    "ds_read_b128 v[200:203], v123 offset:5120\n"
    "v_mfma_f32_16x16x32_bf16 v[164:167], a[72:75], v[208:211], v[164:167]\n"
    "ds_read_b128 v[204:207], v123 offset:21504\n"
    "v_mfma_f32_16x16x32_bf16 v[168:171], a[136:139], v[208:211], v[168:171]\n"
    "v_mfma_f32_16x16x32_bf16 v[172:175], a[200:203], v[208:211], v[172:175]\n"
    "v_mfma_f32_16x16x32_bf16 v[176:179], a[8:11], v[212:215], v[176:179]\n"
    "global_load_lds_dwordx4 v121, s[64:65] offset:1024 sc1\n"
    "v_mfma_f32_16x16x32_bf16 v[180:183], a[72:75], v[212:215], v[180:183]\n"
    "v_mfma_f32_16x16x32_bf16 v[184:187], a[136:139], v[212:215], v[184:187]\n"
    "global_store_dwordx4 v121, v[228:231], s[44:45] offset:1024 sc1\n"
    "v_mfma_f32_16x16x32_bf16 v[188:191], a[200:203], v[212:215], v[188:191]\n"
    "s_waitcnt lgkmcnt(4)\n"
    "v_mfma_f32_16x16x32_bf16 v[160:163], a[12:15], v[216:219], v[160:163]\n"
    "ds_read_b128 v[208:211], v123 offset:6144\n"
    "v_mfma_f32_16x16x32_bf16 v[164:167], a[76:79], v[216:219], v[164:167]\n"
    "ds_read_b128 v[212:215], v123 offset:22528\n"
    "v_mfma_f32_16x16x32_bf16 v[168:171], a[140:143], v[216:219], v[168:171]\n"
    "v_mfma_f32_16x16x32_bf16 v[172:175], a[204:207], v[216:219], v[172:175]\n"
    "v_mfma_f32_16x16x32_bf16 v[176:179], a[12:15], v[220:223], v[176:179]\n"
    "global_load_lds_dwordx4 v121, s[64:65] offset:2048 sc1\n"
    "v_mfma_f32_16x16x32_bf16 v[180:183], a[76:79], v[220:223], v[180:183]\n"
    "v_mfma_f32_16x16x32_bf16 v[184:187], a[140:143], v[220:223], v[184:187]\n"
    "global_store_dwordx4 v121, v[232:235], s[44:45] offset:2048 sc1\n"
    "v_mfma_f32_16x16x32_bf16 v[188:191], a[204:207], v[220:223], v[188:191]\n"
    "s_waitcnt lgkmcnt(4)\n"
    "v_mfma_f32_16x16x32_bf16 v[160:163], a[16:19], v[192:195], v[160:163]\n"
    "ds_read_b128 v[216:219], v123 offset:7168\n"
    "v_mfma_f32_16x16x32_bf16 v[164:167], a[80:83], v[192:195], v[164:167]\n"
    "ds_read_b128 v[220:223], v123 offset:23552\n"
    "v_mfma_f32_16x16x32_bf16 v[168:171], a[144:147], v[192:195], v[168:171]\n"
    "v_mfma_f32_16x16x32_bf16 v[172:175], a[208:211], v[192:195], v[172:175]\n"
    "v_mfma_f32_16x16x32_bf16 v[176:179], a[16:19], v[196:199], v[176:179]\n"
    "global_load_lds_dwordx4 v121, s[64:65] offset:3072 sc1\n"
    "v_mfma_f32_16x16x32_bf16 v[180:183], a[80:83], v[196:199], v[180:183]\n"
    "v_mfma_f32_16x16x32_bf16 v[184:187], a[144:147], v[196:199], v[184:187]\n"
    "global_store_dwordx4 v121, v[236:239], s[44:45] offset:3072 sc1\n"
    "v_mfma_f32_16x16x32_bf16 v[188:191], a[208:211], v[196:199], v[188:191]\n"
    "s_waitcnt lgkmcnt(4)\n"
    "v_mfma_f32_16x16x32_bf16 v[160:163], a[20:23], v[200:203], v[160:163]\n"
    "s_waitcnt vmcnt(12) lgkmcnt(0)\n"
    "s_barrier\n"
    "v_mfma_f32_16x16x32_bf16 v[164:167], a[84:87], v[200:203], v[164:167]\n"
    "ds_read_b128 v[192:195], v123 offset:8192\n"
    "v_mfma_f32_16x16x32_bf16 v[168:171], a[148:151], v[200:203], v[168:171]\n"
    "ds_read_b128 v[196:199], v123 offset:24576\n"
    "s_add_u32 m0, s50, 65536\n"
    "s_add_u32 s64, s48, 0\n"
    "s_addc_u32 s65, s49, 0\n"
    "v_mfma_f32_16x16x32_bf16 v[172:175], a[212:215], v[200:203], v[172:175]\n"
    "v_mfma_f32_16x16x32_bf16 v[176:179], a[20:23], v[204:207], v[176:179]\n"
    "global_load_lds_dwordx4 v121, s[64:65] offset:0 sc1\n"
    "v_mfma_f32_16x16x32_bf16 v[180:183], a[84:87], v[204:207], v[180:183]\n"
    "v_mfma_f32_16x16x32_bf16 v[184:187], a[148:151], v[204:207], v[184:187]\n"
    "v_mfma_f32_16x16x32_bf16 v[188:191], a[212:215], v[204:207], v[188:191]\n"
    "s_waitcnt lgkmcnt(4)\n"
    "v_mfma_f32_16x16x32_bf16 v[160:163], a[24:27], v[208:211], v[160:163]\n"
    "ds_read_b128 v[200:203], v123 offset:9216\n"
    "v_mfma_f32_16x16x32_bf16 v[164:167], a[88:91], v[208:211], v[164:167]\n"
    "ds_read_b128 v[204:207], v123 offset:25600\n"
    "v_mfma_f32_16x16x32_bf16 v[168:171], a[152:155], v[208:211], v[168:171]\n"
    "v_mfma_f32_16x16x32_bf16 v[172:175], a[216:219], v[208:211], v[172:175]\n"
    "v_mfma_f32_16x16x32_bf16 v[176:179], a[24:27], v[212:215], v[176:179]\n"
    "global_load_lds_dwordx4 v121, s[64:65] offset:1024 sc1\n"
    "v_mfma_f32_16x16x32_bf16 v[180:183], a[88:91], v[212:215], v[180:183]\n"
    "v_mfma_f32_16x16x32_bf16 v[184:187], a[152:155], v[212:215], v[184:187]\n"
    "v_mfma_f32_16x16x32_bf16 v[188:191], a[216:219], v[212:215], v[188:191]\n"
    "s_waitcnt lgkmcnt(4)\n"
    "v_mfma_f32_16x16x32_bf16 v[160:163], a[28:31], v[216:219], v[160:163]\n"
    "ds_read_b128 v[208:211], v123 offset:10240\n"
    "v_mfma_f32_16x16x32_bf16 v[164:167], a[92:95], v[216:219], v[164:167]\n"
    "ds_read_b128 v[212:215], v123 offset:26624\n"
    "v_mfma_f32_16x16x32_bf16 v[168:171], a[156:159], v[216:219], v[168:171]\n"
    "v_mfma_f32_16x16x32_bf16 v[172:175], a[220:223], v[216:219], v[172:175]\n"
    "v_mfma_f32_16x16x32_bf16 v[176:179], a[28:31], v[220:223], v[176:179]\n"
    "global_load_lds_dwordx4 v121, s[64:65] offset:2048 sc1\n"
    "v_mfma_f32_16x16x32_bf16 v[180:183], a[92:95], v[220:223], v[180:183]\n"
    "s_mov_b64 s[44:45], s[66:67]\n"
    "s_add_u32 s66, s66, 0x100000\n"
    "s_addc_u32 s67, s67, 0\n"
    "v_mfma_f32_16x16x32_bf16 v[184:187], a[156:159], v[220:223], v[184:187]\n"
    "s_xor_b32 s66, s66, 0x2000\n"
    "v_mfma_f32_16x16x32_bf16 v[188:191], a[220:223], v[220:223], v[188:191]\n"
    "s_waitcnt lgkmcnt(4)\n"
    "v_mfma_f32_16x16x32_bf16 v[160:163], a[32:35], v[192:195], v[160:163]\n"
    "ds_read_b128 v[216:219], v123 offset:11264\n"
    "v_mfma_f32_16x16x32_bf16 v[164:167], a[96:99], v[192:195], v[164:167]\n"
    "ds_read_b128 v[220:223], v123 offset:27648\n"
    "v_mfma_f32_16x16x32_bf16 v[168:171], a[160:163], v[192:195], v[168:171]\n"
    "v_mfma_f32_16x16x32_bf16 v[172:175], a[224:227], v[192:195], v[172:175]\n"
    "v_mfma_f32_16x16x32_bf16 v[176:179], a[32:35], v[196:199], v[176:179]\n"
    "global_load_lds_dwordx4 v121, s[64:65] offset:3072 sc1\n"
    "v_mfma_f32_16x16x32_bf16 v[180:183], a[96:99], v[196:199], v[180:183]\n"
    "v_mfma_f32_16x16x32_bf16 v[184:187], a[160:163], v[196:199], v[184:187]\n"
    "v_mfma_f32_16x16x32_bf16 v[188:191], a[224:227], v[196:199], v[188:191]\n"
    "s_waitcnt lgkmcnt(4)\n"
    "v_mfma_f32_16x16x32_bf16 v[160:163], a[36:39], v[200:203], v[160:163]\n"
    "ds_read_b128 v[192:195], v123 offset:12288\n"
    "v_mfma_f32_16x16x32_bf16 v[164:167], a[100:103], v[200:203], v[164:167]\n"
    "ds_read_b128 v[196:199], v123 offset:28672\n"
    "v_mfma_f32_16x16x32_bf16 v[168:171], a[164:167], v[200:203], v[168:171]\n"
    "s_add_u32 m0, s50, 69632\n"
    "s_add_u32 s64, s48, 4096\n"
    "s_addc_u32 s65, s49, 0\n"
    "v_mfma_f32_16x16x32_bf16 v[172:175], a[228:231], v[200:203], v[172:175]\n"
    "v_mfma_f32_16x16x32_bf16 v[176:179], a[36:39], v[204:207], v[176:179]\n"
    "global_load_lds_dwordx4 v121, s[64:65] offset:0 sc1\n"
    "v_mfma_f32_16x16x32_bf16 v[180:183], a[100:103], v[204:207], v[180:183]\n"
    "v_mfma_f32_16x16x32_bf16 v[184:187], a[164:167], v[204:207], v[184:187]\n"
    "v_mfma_f32_16x16x32_bf16 v[188:191], a[228:231], v[204:207], v[188:191]\n"
    "s_waitcnt lgkmcnt(4)\n"
    "v_mfma_f32_16x16x32_bf16 v[160:163], a[40:43], v[208:211], v[160:163]\n"
    "ds_read_b128 v[200:203], v123 offset:13312\n"
    "v_mfma_f32_16x16x32_bf16 v[164:167], a[104:107], v[208:211], v[164:167]\n"
    "ds_read_b128 v[204:207], v123 offset:29696\n"
    "v_mfma_f32_16x16x32_bf16 v[168:171], a[168:171], v[208:211], v[168:171]\n"
    "v_mfma_f32_16x16x32_bf16 v[172:175], a[232:235], v[208:211], v[172:175]\n"
    "v_mfma_f32_16x16x32_bf16 v[176:179], a[40:43], v[212:215], v[176:179]\n"
    "global_load_lds_dwordx4 v121, s[64:65] offset:1024 sc1\n"
    "v_mfma_f32_16x16x32_bf16 v[180:183], a[104:107], v[212:215], v[180:183]\n"
    "v_mfma_f32_16x16x32_bf16 v[184:187], a[168:171], v[212:215], v[184:187]\n"
    "v_mfma_f32_16x16x32_bf16 v[188:191], a[232:235], v[212:215], v[188:191]\n"
    "s_waitcnt lgkmcnt(4)\n"
    "v_mfma_f32_16x16x32_bf16 v[160:163], a[44:47], v[216:219], v[160:163]\n"
    "ds_read_b128 v[208:211], v123 offset:14336\n"
    "v_mfma_f32_16x16x32_bf16 v[164:167], a[108:111], v[216:219], v[164:167]\n"
    "ds_read_b128 v[212:215], v123 offset:30720\n"
    "v_mfma_f32_16x16x32_bf16 v[168:171], a[172:175], v[216:219], v[168:171]\n"
    "v_mfma_f32_16x16x32_bf16 v[172:175], a[236:239], v[216:219], v[172:175]\n"
    "v_mfma_f32_16x16x32_bf16 v[176:179], a[44:47], v[220:223], v[176:179]\n"
    "global_load_lds_dwordx4 v121, s[64:65] offset:2048 sc1\n"
    "v_mfma_f32_16x16x32_bf16 v[180:183], a[108:111], v[220:223], v[180:183]\n"
    "v_mfma_f32_16x16x32_bf16 v[184:187], a[172:175], v[220:223], v[184:187]\n"
    "v_mfma_f32_16x16x32_bf16 v[188:191], a[236:239], v[220:223], v[188:191]\n"
    "s_waitcnt lgkmcnt(4)\n"
    "v_mfma_f32_16x16x32_bf16 v[160:163], a[48:51], v[192:195], v[160:163]\n"
    "ds_read_b128 v[216:219], v123 offset:15360\n"
    "v_mfma_f32_16x16x32_bf16 v[164:167], a[112:115], v[192:195], v[164:167]\n"
    "ds_read_b128 v[220:223], v123 offset:31744\n"
    "v_mfma_f32_16x16x32_bf16 v[168:171], a[176:179], v[192:195], v[168:171]\n"
    "v_mfma_f32_16x16x32_bf16 v[172:175], a[240:243], v[192:195], v[172:175]\n"
    "v_mfma_f32_16x16x32_bf16 v[176:179], a[48:51], v[196:199], v[176:179]\n"
    "global_load_lds_dwordx4 v121, s[64:65] offset:3072 sc1\n"
    "v_mfma_f32_16x16x32_bf16 v[180:183], a[112:115], v[196:199], v[180:183]\n"
    "v_mfma_f32_16x16x32_bf16 v[184:187], a[176:179], v[196:199], v[184:187]\n"
    "v_mfma_f32_16x16x32_bf16 v[188:191], a[240:243], v[196:199], v[188:191]\n"
    "s_waitcnt lgkmcnt(4)\n"
    "v_mfma_f32_16x16x32_bf16 v[160:163], a[52:55], v[200:203], v[160:163]\n"
    "s_waitcnt vmcnt(8) lgkmcnt(0)\n"
    "s_barrier\n"
    "v_mfma_f32_16x16x32_bf16 v[164:167], a[116:119], v[200:203], v[164:167]\n"
    "ds_read_b128 v[192:195], v122 offset:0\n"
    "v_mfma_f32_16x16x32_bf16 v[168:171], a[180:183], v[200:203], v[168:171]\n"
    "ds_read_b128 v[196:199], v122 offset:16384\n"
    "s_add_u32 m0, s50, 73728\n"
    "s_add_u32 s64, s48, 8192\n"
    "s_addc_u32 s65, s49, 0\n"
    "v_mfma_f32_16x16x32_bf16 v[172:175], a[244:247], v[200:203], v[172:175]\n"
    "v_mfma_f32_16x16x32_bf16 v[176:179], a[52:55], v[204:207], v[176:179]\n"
    "global_load_lds_dwordx4 v121, s[64:65] offset:0 sc1\n"
    "v_mfma_f32_16x16x32_bf16 v[180:183], a[116:119], v[204:207], v[180:183]\n"
    "s_cmp_lg_u32 s56, 0\n"
    "s_cbranch_scc1 G_f1%=\n"
    "s_or_b32 s58, s42, s51\n"
    "s_mov_b64 exec, 1\n"
    "v_mov_b32 v116, s58\n"
    "global_store_dwordx2 v118, v[116:117], s[54:55] sc1\n"
    "s_mov_b64 exec, -1\n"
    "G_f1%=:\n"
    "v_mfma_f32_16x16x32_bf16 v[184:187], a[180:183], v[204:207], v[184:187]\n"
    "v_mfma_f32_16x16x32_bf16 v[188:191], a[244:247], v[204:207], v[188:191]\n"
    "s_waitcnt lgkmcnt(4)\n"
    "v_mfma_f32_16x16x32_bf16 v[160:163], a[56:59], v[208:211], v[160:163]\n"
    "ds_read_b128 v[200:203], v122 offset:1024\n"
    "v_mfma_f32_16x16x32_bf16 v[164:167], a[120:123], v[208:211], v[164:167]\n"
    "ds_read_b128 v[204:207], v122 offset:17408\n"
    "v_mfma_f32_16x16x32_bf16 v[168:171], a[184:187], v[208:211], v[168:171]\n"
    "v_mfma_f32_16x16x32_bf16 v[172:175], a[248:251], v[208:211], v[172:175]\n"
    "v_mfma_f32_16x16x32_bf16 v[176:179], a[56:59], v[212:215], v[176:179]\n"
    "global_load_lds_dwordx4 v121, s[64:65] offset:1024 sc1\n"
    "v_mfma_f32_16x16x32_bf16 v[180:183], a[120:123], v[212:215], v[180:183]\n"
    "v_mfma_f32_16x16x32_bf16 v[184:187], a[184:187], v[212:215], v[184:187]\n"
    "v_mfma_f32_16x16x32_bf16 v[188:191], a[248:251], v[212:215], v[188:191]\n"
    "s_add_u32 s51, s51, 1\n"
    "s_waitcnt lgkmcnt(4)\n"
    "v_mfma_f32_16x16x32_bf16 v[160:163], a[60:63], v[216:219], v[160:163]\n"
    "ds_read_b128 v[208:211], v122 offset:2048\n"
    "v_mfma_f32_16x16x32_bf16 v[164:167], a[124:127], v[216:219], v[164:167]\n"
    "ds_read_b128 v[212:215], v122 offset:18432\n"
    "v_mfma_f32_16x16x32_bf16 v[168:171], a[188:191], v[216:219], v[168:171]\n"
    "v_mfma_f32_16x16x32_bf16 v[172:175], a[252:255], v[216:219], v[172:175]\n"
    "v_mfma_f32_16x16x32_bf16 v[176:179], a[60:63], v[220:223], v[176:179]\n"
    "global_load_lds_dwordx4 v121, s[64:65] offset:2048 sc1\n"
    "v_mfma_f32_16x16x32_bf16 v[180:183], a[124:127], v[220:223], v[180:183]\n"
    "v_mfma_f32_16x16x32_bf16 v[184:187], a[188:191], v[220:223], v[184:187]\n"
    "v_mfma_f32_16x16x32_bf16 v[188:191], a[252:255], v[220:223], v[188:191]\n"
    "s_add_u32 s58, s51, -3\n"
    "s_cmp_gt_u32 s58, 28\n"
    "s_waitcnt lgkmcnt(4)\n"
    "s_cbranch_scc1 G_t1%=\n"
    "G_even%=:\n"
    "v_mfma_f32_16x16x32_bf16 v[128:131], a[0:3], v[192:195], 0\n"
    "ds_read_b128 v[216:219], v122 offset:3072\n"
    "v_mfma_f32_16x16x32_bf16 v[132:135], a[64:67], v[192:195], 0\n"
    "ds_read_b128 v[220:223], v122 offset:19456\n"
    "v_mfma_f32_16x16x32_bf16 v[136:139], a[128:131], v[192:195], 0\n"
    "v_mfma_f32_16x16x32_bf16 v[140:143], a[192:195], v[192:195], 0\n"
    "v_cvt_pk_bf16_f32 v224, v160, v161\n"
    "v_cvt_pk_bf16_f32 v225, v162, v163\n"
    "v_mfma_f32_16x16x32_bf16 v[144:147], a[0:3], v[196:199], 0\n"
    "global_load_lds_dwordx4 v121, s[64:65] offset:3072 sc1\n"
    "v_mfma_f32_16x16x32_bf16 v[148:151], a[64:67], v[196:199], 0\n"
    "v_cvt_pk_bf16_f32 v226, v164, v165\n"
    "v_cvt_pk_bf16_f32 v227, v166, v167\n"
    "v_mfma_f32_16x16x32_bf16 v[152:155], a[128:131], v[196:199], 0\n"
    "v_cvt_pk_bf16_f32 v228, v176, v177\n"
    "v_cvt_pk_bf16_f32 v229, v178, v179\n"
    "v_mfma_f32_16x16x32_bf16 v[156:159], a[192:195], v[196:199], 0\n"
    "v_cvt_pk_bf16_f32 v230, v180, v181\n"
    "v_cvt_pk_bf16_f32 v231, v182, v183\n"
    "s_waitcnt lgkmcnt(4)\n"
    "v_mfma_f32_16x16x32_bf16 v[128:131], a[4:7], v[200:203], v[128:131]\n"
    "ds_read_b128 v[192:195], v122 offset:4096\n"
    "v_mfma_f32_16x16x32_bf16 v[132:135], a[68:71], v[200:203], v[132:135]\n"
    "ds_read_b128 v[196:199], v122 offset:20480\n"
    "v_mfma_f32_16x16x32_bf16 v[136:139], a[132:135], v[200:203], v[136:139]\n"
    "s_add_u32 m0, s50, 77824\n"
    "s_add_u32 s64, s48, 12288\n"
    "s_addc_u32 s65, s49, 0\n"
    "v_cvt_pk_bf16_f32 v232, v168, v169\n"
    "v_cvt_pk_bf16_f32 v233, v170, v171\n"
    "v_mfma_f32_16x16x32_bf16 v[140:143], a[196:199], v[200:203], v[140:143]\n"
    "s_add_u32 s48, s48, 0x10000\n"
    "s_addc_u32 s49, s49, 0\n"
    "v_cvt_pk_bf16_f32 v234, v172, v173\n"
    "v_cvt_pk_bf16_f32 v235, v174, v175\n"
    "v_mfma_f32_16x16x32_bf16 v[144:147], a[4:7], v[204:207], v[144:147]\n"
    "global_load_lds_dwordx4 v121, s[64:65] offset:0 sc1\n"
    "v_mfma_f32_16x16x32_bf16 v[148:151], a[68:71], v[204:207], v[148:151]\n"
    "v_cvt_pk_bf16_f32 v236, v184, v185\n"
    "v_cvt_pk_bf16_f32 v237, v186, v187\n"
    "v_mfma_f32_16x16x32_bf16 v[152:155], a[132:135], v[204:207], v[152:155]\n"
    "global_store_dwordx4 v121, v[224:227], s[44:45] offset:0 sc1\n"
    "v_mfma_f32_16x16x32_bf16 v[156:159], a[196:199], v[204:207], v[156:159]\n"
    "v_cvt_pk_bf16_f32 v238, v188, v189\n"
    "v_cvt_pk_bf16_f32 v239, v190, v191\n"
    "s_waitcnt lgkmcnt(4)\n"
    "v_mfma_f32_16x16x32_bf16 v[128:131], a[8:11], v[208:211], v[128:131]\n"
    "ds_read_b128 v[200:203], v122 offset:5120\n"
    "v_mfma_f32_16x16x32_bf16 v[132:135], a[72:75], v[208:211], v[132:135]\n"
    "ds_read_b128 v[204:207], v122 offset:21504\n"
    "v_mfma_f32_16x16x32_bf16 v[136:139], a[136:139], v[208:211], v[136:139]\n"
    "v_mfma_f32_16x16x32_bf16 v[140:143], a[200:203], v[208:211], v[140:143]\n"
    "v_mfma_f32_16x16x32_bf16 v[144:147], a[8:11], v[212:215], v[144:147]\n"
    "global_load_lds_dwordx4 v121, s[64:65] offset:1024 sc1\n"
    "v_mfma_f32_16x16x32_bf16 v[148:151], a[72:75], v[212:215], v[148:151]\n"
    "v_mfma_f32_16x16x32_bf16 v[152:155], a[136:139], v[212:215], v[152:155]\n"
    "global_store_dwordx4 v121, v[228:231], s[44:45] offset:1024 sc1\n"
    "v_mfma_f32_16x16x32_bf16 v[156:159], a[200:203], v[212:215], v[156:159]\n"
    "s_waitcnt lgkmcnt(4)\n"
    "v_mfma_f32_16x16x32_bf16 v[128:131], a[12:15], v[216:219], v[128:131]\n"
    "ds_read_b128 v[208:211], v122 offset:6144\n"
    "v_mfma_f32_16x16x32_bf16 v[132:135], a[76:79], v[216:219], v[132:135]\n"
    "ds_read_b128 v[212:215], v122 offset:22528\n"
    "v_mfma_f32_16x16x32_bf16 v[136:139], a[140:143], v[216:219], v[136:139]\n"
    "v_mfma_f32_16x16x32_bf16 v[140:143], a[204:207], v[216:219], v[140:143]\n"
    "v_mfma_f32_16x16x32_bf16 v[144:147], a[12:15], v[220:223], v[144:147]\n"
    "global_load_lds_dwordx4 v121, s[64:65] offset:2048 sc1\n"
    "v_mfma_f32_16x16x32_bf16 v[148:151], a[76:79], v[220:223], v[148:151]\n"
    "v_mfma_f32_16x16x32_bf16 v[152:155], a[140:143], v[220:223], v[152:155]\n"
    "global_store_dwordx4 v121, v[232:235], s[44:45] offset:2048 sc1\n"
    "v_mfma_f32_16x16x32_bf16 v[156:159], a[204:207], v[220:223], v[156:159]\n"
    "s_waitcnt lgkmcnt(4)\n"
    "v_mfma_f32_16x16x32_bf16 v[128:131], a[16:19], v[192:195], v[128:131]\n"
    "ds_read_b128 v[216:219], v122 offset:7168\n"
    "v_mfma_f32_16x16x32_bf16 v[132:135], a[80:83], v[192:195], v[132:135]\n"
    "ds_read_b128 v[220:223], v122 offset:23552\n"
    "v_mfma_f32_16x16x32_bf16 v[136:139], a[144:147], v[192:195], v[136:139]\n"
    "v_mfma_f32_16x16x32_bf16 v[140:143], a[208:211], v[192:195], v[140:143]\n"
    "v_mfma_f32_16x16x32_bf16 v[144:147], a[16:19], v[196:199], v[144:147]\n"
    "global_load_lds_dwordx4 v121, s[64:65] offset:3072 sc1\n"
    "v_mfma_f32_16x16x32_bf16 v[148:151], a[80:83], v[196:199], v[148:151]\n"
    "v_mfma_f32_16x16x32_bf16 v[152:155], a[144:147], v[196:199], v[152:155]\n"
    "global_store_dwordx4 v121, v[236:239], s[44:45] offset:3072 sc1\n"
    "v_mfma_f32_16x16x32_bf16 v[156:159], a[208:211], v[196:199], v[156:159]\n"
    "s_waitcnt lgkmcnt(4)\n"
    "v_mfma_f32_16x16x32_bf16 v[128:131], a[20:23], v[200:203], v[128:131]\n"
    "s_waitcnt vmcnt(12) lgkmcnt(0)\n"
    "s_barrier\n"
    "v_mfma_f32_16x16x32_bf16 v[132:135], a[84:87], v[200:203], v[132:135]\n"
    "ds_read_b128 v[192:195], v122 offset:8192\n"
    "v_mfma_f32_16x16x32_bf16 v[136:139], a[148:151], v[200:203], v[136:139]\n"
    "ds_read_b128 v[196:199], v122 offset:24576\n"
    "s_add_u32 m0, s50, 0\n"
    "s_add_u32 s64, s48, 0\n"
    "s_addc_u32 s65, s49, 0\n"
    "v_mfma_f32_16x16x32_bf16 v[140:143], a[212:215], v[200:203], v[140:143]\n"
    "v_mfma_f32_16x16x32_bf16 v[144:147], a[20:23], v[204:207], v[144:147]\n"
    "global_load_lds_dwordx4 v121, s[64:65] offset:0 sc1\n"
    "v_mfma_f32_16x16x32_bf16 v[148:151], a[84:87], v[204:207], v[148:151]\n"
    "v_mfma_f32_16x16x32_bf16 v[152:155], a[148:151], v[204:207], v[152:155]\n"
    "v_mfma_f32_16x16x32_bf16 v[156:159], a[212:215], v[204:207], v[156:159]\n"
    "s_waitcnt lgkmcnt(4)\n"
    "v_mfma_f32_16x16x32_bf16 v[128:131], a[24:27], v[208:211], v[128:131]\n"
    "ds_read_b128 v[200:203], v122 offset:9216\n"
    "v_mfma_f32_16x16x32_bf16 v[132:135], a[88:91], v[208:211], v[132:135]\n"
    "ds_read_b128 v[204:207], v122 offset:25600\n"
    "v_mfma_f32_16x16x32_bf16 v[136:139], a[152:155], v[208:211], v[136:139]\n"
    "v_mfma_f32_16x16x32_bf16 v[140:143], a[216:219], v[208:211], v[140:143]\n"
    "v_mfma_f32_16x16x32_bf16 v[144:147], a[24:27], v[212:215], v[144:147]\n"
    "global_load_lds_dwordx4 v121, s[64:65] offset:1024 sc1\n"
    "v_mfma_f32_16x16x32_bf16 v[148:151], a[88:91], v[212:215], v[148:151]\n"
    "v_mfma_f32_16x16x32_bf16 v[152:155], a[152:155], v[212:215], v[152:155]\n"
    "v_mfma_f32_16x16x32_bf16 v[156:159], a[216:219], v[212:215], v[156:159]\n"
    "s_waitcnt lgkmcnt(4)\n"
    "v_mfma_f32_16x16x32_bf16 v[128:131], a[28:31], v[216:219], v[128:131]\n"
    "ds_read_b128 v[208:211], v122 offset:10240\n"
    "v_mfma_f32_16x16x32_bf16 v[132:135], a[92:95], v[216:219], v[132:135]\n"
    "ds_read_b128 v[212:215], v122 offset:26624\n"
    "v_mfma_f32_16x16x32_bf16 v[136:139], a[156:159], v[216:219], v[136:139]\n"
    "v_mfma_f32_16x16x32_bf16 v[140:143], a[220:223], v[216:219], v[140:143]\n"
    "v_mfma_f32_16x16x32_bf16 v[144:147], a[28:31], v[220:223], v[144:147]\n"
    "global_load_lds_dwordx4 v121, s[64:65] offset:2048 sc1\n"
    "v_mfma_f32_16x16x32_bf16 v[148:151], a[92:95], v[220:223], v[148:151]\n"
    "s_mov_b64 s[44:45], s[66:67]\n"
    "s_add_u32 s66, s66, 0x100000\n"
    "s_addc_u32 s67, s67, 0\n"
    "v_mfma_f32_16x16x32_bf16 v[152:155], a[156:159], v[220:223], v[152:155]\n"
    "v_mfma_f32_16x16x32_bf16 v[156:159], a[220:223], v[220:223], v[156:159]\n"
    "s_waitcnt lgkmcnt(4)\n"
    "v_mfma_f32_16x16x32_bf16 v[128:131], a[32:35], v[192:195], v[128:131]\n"
    "ds_read_b128 v[216:219], v122 offset:11264\n"
    "v_mfma_f32_16x16x32_bf16 v[132:135], a[96:99], v[192:195], v[132:135]\n"
    "ds_read_b128 v[220:223], v122 offset:27648\n"
    "v_mfma_f32_16x16x32_bf16 v[136:139], a[160:163], v[192:195], v[136:139]\n"
    "v_mfma_f32_16x16x32_bf16 v[140:143], a[224:227], v[192:195], v[140:143]\n"
    "v_mfma_f32_16x16x32_bf16 v[144:147], a[32:35], v[196:199], v[144:147]\n"
    "global_load_lds_dwordx4 v121, s[64:65] offset:3072 sc1\n"
    "v_mfma_f32_16x16x32_bf16 v[148:151], a[96:99], v[196:199], v[148:151]\n"
    "v_mfma_f32_16x16x32_bf16 v[152:155], a[160:163], v[196:199], v[152:155]\n"
    "v_mfma_f32_16x16x32_bf16 v[156:159], a[224:227], v[196:199], v[156:159]\n"
    "s_waitcnt lgkmcnt(4)\n"
    "v_mfma_f32_16x16x32_bf16 v[128:131], a[36:39], v[200:203], v[128:131]\n"
    "ds_read_b128 v[192:195], v122 offset:12288\n"
    "v_mfma_f32_16x16x32_bf16 v[132:135], a[100:103], v[200:203], v[132:135]\n"
    "ds_read_b128 v[196:199], v122 offset:28672\n"
    "v_mfma_f32_16x16x32_bf16 v[136:139], a[164:167], v[200:203], v[136:139]\n"
    "s_add_u32 m0, s50, 4096\n"
    "s_add_u32 s64, s48, 4096\n"
    "s_addc_u32 s65, s49, 0\n"
    "v_mfma_f32_16x16x32_bf16 v[140:143], a[228:231], v[200:203], v[140:143]\n"
    "v_mfma_f32_16x16x32_bf16 v[144:147], a[36:39], v[204:207], v[144:147]\n"
    "global_load_lds_dwordx4 v121, s[64:65] offset:0 sc1\n"
    "v_mfma_f32_16x16x32_bf16 v[148:151], a[100:103], v[204:207], v[148:151]\n"
    "v_mfma_f32_16x16x32_bf16 v[152:155], a[164:167], v[204:207], v[152:155]\n"
    "v_mfma_f32_16x16x32_bf16 v[156:159], a[228:231], v[204:207], v[156:159]\n"
    "s_waitcnt lgkmcnt(4)\n"
    "v_mfma_f32_16x16x32_bf16 v[128:131], a[40:43], v[208:211], v[128:131]\n"
    "ds_read_b128 v[200:203], v122 offset:13312\n"
    "v_mfma_f32_16x16x32_bf16 v[132:135], a[104:107], v[208:211], v[132:135]\n"
    "ds_read_b128 v[204:207], v122 offset:29696\n"
    "v_mfma_f32_16x16x32_bf16 v[136:139], a[168:171], v[208:211], v[136:139]\n"
    "v_mfma_f32_16x16x32_bf16 v[140:143], a[232:235], v[208:211], v[140:143]\n"
    "v_mfma_f32_16x16x32_bf16 v[144:147], a[40:43], v[212:215], v[144:147]\n"
    "global_load_lds_dwordx4 v121, s[64:65] offset:1024 sc1\n"
    "v_mfma_f32_16x16x32_bf16 v[148:151], a[104:107], v[212:215], v[148:151]\n"
    "v_mfma_f32_16x16x32_bf16 v[152:155], a[168:171], v[212:215], v[152:155]\n"
    "v_mfma_f32_16x16x32_bf16 v[156:159], a[232:235], v[212:215], v[156:159]\n"
    "s_waitcnt lgkmcnt(4)\n"
    "v_mfma_f32_16x16x32_bf16 v[128:131], a[44:47], v[216:219], v[128:131]\n"
    "ds_read_b128 v[208:211], v122 offset:14336\n"
    "v_mfma_f32_16x16x32_bf16 v[132:135], a[108:111], v[216:219], v[132:135]\n"
    "ds_read_b128 v[212:215], v122 offset:30720\n"
    "v_mfma_f32_16x16x32_bf16 v[136:139], a[172:175], v[216:219], v[136:139]\n"
    "v_mfma_f32_16x16x32_bf16 v[140:143], a[236:239], v[216:219], v[140:143]\n"
    "v_mfma_f32_16x16x32_bf16 v[144:147], a[44:47], v[220:223], v[144:147]\n"
    "global_load_lds_dwordx4 v121, s[64:65] offset:2048 sc1\n"
    "v_mfma_f32_16x16x32_bf16 v[148:151], a[108:111], v[220:223], v[148:151]\n"
    "v_mfma_f32_16x16x32_bf16 v[152:155], a[172:175], v[220:223], v[152:155]\n"
    "v_mfma_f32_16x16x32_bf16 v[156:159], a[236:239], v[220:223], v[156:159]\n"
    "s_waitcnt lgkmcnt(4)\n"
    "v_mfma_f32_16x16x32_bf16 v[128:131], a[48:51], v[192:195], v[128:131]\n"
    "ds_read_b128 v[216:219], v122 offset:15360\n"
    "v_mfma_f32_16x16x32_bf16 v[132:135], a[112:115], v[192:195], v[132:135]\n"
    "ds_read_b128 v[220:223], v122 offset:31744\n"
    "v_mfma_f32_16x16x32_bf16 v[136:139], a[176:179], v[192:195], v[136:139]\n"
    "v_mfma_f32_16x16x32_bf16 v[140:143], a[240:243], v[192:195], v[140:143]\n"
    "v_mfma_f32_16x16x32_bf16 v[144:147], a[48:51], v[196:199], v[144:147]\n"
    "global_load_lds_dwordx4 v121, s[64:65] offset:3072 sc1\n"
    "v_mfma_f32_16x16x32_bf16 v[148:151], a[112:115], v[196:199], v[148:151]\n"
    "v_mfma_f32_16x16x32_bf16 v[152:155], a[176:179], v[196:199], v[152:155]\n"
    "v_mfma_f32_16x16x32_bf16 v[156:159], a[240:243], v[196:199], v[156:159]\n"
    "s_waitcnt lgkmcnt(4)\n"
    "v_mfma_f32_16x16x32_bf16 v[128:131], a[52:55], v[200:203], v[128:131]\n"
    "s_waitcnt vmcnt(8) lgkmcnt(0)\n"
    "s_barrier\n"
    "v_mfma_f32_16x16x32_bf16 v[132:135], a[116:119], v[200:203], v[132:135]\n"
    "ds_read_b128 v[192:195], v123 offset:0\n"
    "v_mfma_f32_16x16x32_bf16 v[136:139], a[180:183], v[200:203], v[136:139]\n"
    "ds_read_b128 v[196:199], v123 offset:16384\n"
    "s_add_u32 m0, s50, 8192\n"
    "s_add_u32 s64, s48, 8192\n"
    "s_addc_u32 s65, s49, 0\n"
    "v_mfma_f32_16x16x32_bf16 v[140:143], a[244:247], v[200:203], v[140:143]\n"
    "v_mfma_f32_16x16x32_bf16 v[144:147], a[52:55], v[204:207], v[144:147]\n"
    "global_load_lds_dwordx4 v121, s[64:65] offset:0 sc1\n"
    "v_mfma_f32_16x16x32_bf16 v[148:151], a[116:119], v[204:207], v[148:151]\n"
    "s_cmp_lg_u32 s56, 0\n"
    "s_cbranch_scc1 G_f2%=\n"
    "s_or_b32 s58, s42, s51\n"
    "s_mov_b64 exec, 1\n"
    "v_mov_b32 v116, s58\n"
    "global_store_dwordx2 v118, v[116:117], s[54:55] sc1\n"
    "s_mov_b64 exec, -1\n"
    "G_f2%=:\n"
    "v_mfma_f32_16x16x32_bf16 v[152:155], a[180:183], v[204:207], v[152:155]\n"
    "v_mfma_f32_16x16x32_bf16 v[156:159], a[244:247], v[204:207], v[156:159]\n"
    "s_waitcnt lgkmcnt(4)\n"
    "v_mfma_f32_16x16x32_bf16 v[128:131], a[56:59], v[208:211], v[128:131]\n"
    "ds_read_b128 v[200:203], v123 offset:1024\n"
    "v_mfma_f32_16x16x32_bf16 v[132:135], a[120:123], v[208:211], v[132:135]\n"
    "ds_read_b128 v[204:207], v123 offset:17408\n"
    "v_mfma_f32_16x16x32_bf16 v[136:139], a[184:187], v[208:211], v[136:139]\n"
    "v_mfma_f32_16x16x32_bf16 v[140:143], a[248:251], v[208:211], v[140:143]\n"
    "v_mfma_f32_16x16x32_bf16 v[144:147], a[56:59], v[212:215], v[144:147]\n"
    "global_load_lds_dwordx4 v121, s[64:65] offset:1024 sc1\n"
    "v_mfma_f32_16x16x32_bf16 v[148:151], a[120:123], v[212:215], v[148:151]\n"
    "v_mfma_f32_16x16x32_bf16 v[152:155], a[184:187], v[212:215], v[152:155]\n"
    "v_mfma_f32_16x16x32_bf16 v[156:159], a[248:251], v[212:215], v[156:159]\n"
    "s_add_u32 s51, s51, 1\n"
    "s_waitcnt lgkmcnt(4)\n"
    "v_mfma_f32_16x16x32_bf16 v[128:131], a[60:63], v[216:219], v[128:131]\n"
    "ds_read_b128 v[208:211], v123 offset:2048\n"
    "v_mfma_f32_16x16x32_bf16 v[132:135], a[124:127], v[216:219], v[132:135]\n"
    "ds_read_b128 v[212:215], v123 offset:18432\n"
    "v_mfma_f32_16x16x32_bf16 v[136:139], a[188:191], v[216:219], v[136:139]\n"
    "v_mfma_f32_16x16x32_bf16 v[140:143], a[252:255], v[216:219], v[140:143]\n"
    "v_mfma_f32_16x16x32_bf16 v[144:147], a[60:63], v[220:223], v[144:147]\n"
    "global_load_lds_dwordx4 v121, s[64:65] offset:2048 sc1\n"
    "v_mfma_f32_16x16x32_bf16 v[148:151], a[124:127], v[220:223], v[148:151]\n"
    "v_mfma_f32_16x16x32_bf16 v[152:155], a[188:191], v[220:223], v[152:155]\n"
    "v_mfma_f32_16x16x32_bf16 v[156:159], a[252:255], v[220:223], v[156:159]\n"
    "s_add_u32 s58, s51, -3\n"
    "s_cmp_gt_u32 s58, 28\n"
    "s_waitcnt lgkmcnt(4)\n"
    "s_cbranch_scc0 G_loop%=\n"
    "s_branch G_pc%=\n"
    "G_pw%=:\n"
    "s_add_u32 s58, s40, 0x312000\n"
    "s_addc_u32 s59, s41, 0\n"
    "global_load_dwordx2 v[114:115], v112, s[58:59] sc1\n"
    "s_waitcnt vmcnt(0)\n"
    "G_pc%=:\n"
    "v_xor_b32 v114, s42, v114\n"
    "v_xor_b32 v115, s43, v115\n"
    "v_add_u32 v114, -1, v114\n"
    "v_cmp_le_u64 s[58:59], 8, v[114:115]\n"
    "v_and_b32 v114, 2, v114\n"
    "v_cmp_ne_u32 s[52:53], 0, v114\n"
    "s_cmp_eq_u64 s[58:59], 0\n"
    "s_cbranch_scc1 G_pok%=\n"
    "s_sleep 4\n"
    "s_add_u32 s57, s57, 1\n"
    "s_cmp_lt_u32 s57, 0x40000\n"
    "s_cbranch_scc1 G_pw%=\n"
    "G_pok%=:\n"
    "s_cmp_lg_u32 s52, 0\n"
    "s_cselect_b32 s52, 0, 0x2000\n"
    "s_xor_b32 s44, s44, s52\n"
    "s_xor_b32 s66, s66, s52\n"
    "s_nop 7\n"
    "v_cvt_pk_bf16_f32 v224, v128, v129\n"
    "v_cvt_pk_bf16_f32 v225, v130, v131\n"
    "v_cvt_pk_bf16_f32 v226, v132, v133\n"
    "v_cvt_pk_bf16_f32 v227, v134, v135\n"
    "v_cvt_pk_bf16_f32 v228, v144, v145\n"
    "v_cvt_pk_bf16_f32 v229, v146, v147\n"
    "v_cvt_pk_bf16_f32 v230, v148, v149\n"
    "v_cvt_pk_bf16_f32 v231, v150, v151\n"
    "v_cvt_pk_bf16_f32 v232, v136, v137\n"
    "v_cvt_pk_bf16_f32 v233, v138, v139\n"
    "v_cvt_pk_bf16_f32 v234, v140, v141\n"
    "v_cvt_pk_bf16_f32 v235, v142, v143\n"
    "v_cvt_pk_bf16_f32 v236, v152, v153\n"
    "v_cvt_pk_bf16_f32 v237, v154, v155\n"
    "v_cvt_pk_bf16_f32 v238, v156, v157\n"
    "v_cvt_pk_bf16_f32 v239, v158, v159\n"
    "global_store_dwordx4 v121, v[224:227], s[44:45] offset:0 sc1\n"
    "global_store_dwordx4 v121, v[228:231], s[44:45] offset:1024 sc1\n"
    "global_store_dwordx4 v121, v[232:235], s[44:45] offset:2048 sc1\n"
    "global_store_dwordx4 v121, v[236:239], s[44:45] offset:3072 sc1\n"
    "s_sub_u32 s44, s44, 0x4400000\n"
    "s_subb_u32 s45, s45, 0\n"
    "s_waitcnt vmcnt(0)\n"
    "s_barrier\n"
    "s_cmp_lg_u32 s56, 0\n"
    "s_cbranch_scc1 G_f3%=\n"
    "s_or_b32 s58, s42, s51\n"
    "s_mov_b64 exec, 1\n"
    "v_mov_b32 v116, s58\n"
    "global_store_dwordx2 v118, v[116:117], s[54:55] sc1\n"
    "s_mov_b64 exec, -1\n"
    "G_f3%=:\n"
    "s_branch G_loop%=\n"
    "G_t1%=:\n"
    "s_nop 7\n"
    "v_cvt_pk_bf16_f32 v224, v160, v161\n"
    "v_cvt_pk_bf16_f32 v225, v162, v163\n"
    "v_cvt_pk_bf16_f32 v226, v164, v165\n"
    "v_cvt_pk_bf16_f32 v227, v166, v167\n"
    "v_cvt_pk_bf16_f32 v228, v176, v177\n"
    "v_cvt_pk_bf16_f32 v229, v178, v179\n"
    "v_cvt_pk_bf16_f32 v230, v180, v181\n"
    "v_cvt_pk_bf16_f32 v231, v182, v183\n"
    "v_cvt_pk_bf16_f32 v232, v168, v169\n"
    "v_cvt_pk_bf16_f32 v233, v170, v171\n"
    "v_cvt_pk_bf16_f32 v234, v172, v173\n"
    "v_cvt_pk_bf16_f32 v235, v174, v175\n"
    "v_cvt_pk_bf16_f32 v236, v184, v185\n"
    "v_cvt_pk_bf16_f32 v237, v186, v187\n"
    "v_cvt_pk_bf16_f32 v238, v188, v189\n"
    "v_cvt_pk_bf16_f32 v239, v190, v191\n"
    "global_store_dwordx4 v121, v[224:227], s[44:45] offset:0 sc1\n"
    "global_store_dwordx4 v121, v[228:231], s[44:45] offset:1024 sc1\n"
    "global_store_dwordx4 v121, v[232:235], s[44:45] offset:2048 sc1\n"
    "global_store_dwordx4 v121, v[236:239], s[44:45] offset:3072 sc1\n"
    "s_sub_u32 s44, s44, 0x4400000\n"
    "s_subb_u32 s45, s45, 0\n"
    "s_waitcnt vmcnt(0)\n"
    "s_barrier\n"
    "s_cmp_lg_u32 s56, 0\n"
    "s_cbranch_scc1 G_f4%=\n"
    "s_or_b32 s58, s42, s51\n"
    "s_mov_b64 exec, 1\n"
    "v_mov_b32 v116, s58\n"
    "global_store_dwordx2 v118, v[116:117], s[54:55] sc1\n"
    "s_mov_b64 exec, -1\n"
    "G_f4%=:\n"
    "s_cmp_eq_u32 s51, 32\n"
    "s_cbranch_scc1 G_end%=\n"
    "s_add_u32 s58, s40, 0x20e000\n"
    "s_addc_u32 s59, s41, 0\n"
    "s_add_u32 s60, s40, 0x200200\n"
    "s_addc_u32 s61, s41, 0\n"
    "s_or_b32 s62, s42, 1\n"
    "s_mov_b32 s63, s43\n"
    "v_lshlrev_b32 v224, 3, v121\n"
    "G_xw1%=:\n"
    "global_load_dwordx2 v[226:227], v224, s[58:59] sc1\n"
    "G_xc1%=:\n"
    "s_waitcnt vmcnt(0)\n"
    "v_cmp_ne_u64 vcc, s[62:63], v[226:227]\n"
    "s_nop 0\n"
    "s_cbranch_vccz G_xok1%=\n"
    "s_sleep 4\n"
    "s_add_u32 s57, s57, 1\n"
    "s_and_b32 s68, s57, 0xff\n"
    "s_cbranch_scc1 G_xw1%=\n"
    "global_load_dwordx2 v[226:227], v118, s[60:61] sc1\n"
    "s_waitcnt vmcnt(0)\n"
    "v_cmp_eq_u64 vcc, s[42:43], v[226:227]\n"
    "s_nop 0\n"
    "s_cbranch_vccnz G_xok1%=\n"
    "s_cmp_lt_u32 s57, 0x40000\n"
    "s_cbranch_scc1 G_xw1%=\n"
    "v_mov_b32 v116, s42\n"
    "global_store_dwordx2 v118, v[116:117], s[60:61] sc1\n"
    "G_xok1%=:\n"
    "s_mov_b32 vcc_lo, 0xcccccccc\n"
    "s_mov_b32 vcc_hi, 0xcccccccc\n"
    "s_branch G_even%=\n"
    "G_end%=:\n"
    "s_waitcnt lgkmcnt(0)\n"
    :: GIN : GCLOB);
  else if constexpr (MAT == 1) asm volatile(
    "s_mov_b64 s[40:41], %0\n"
    "s_mov_b64 s[42:43], %1\n"
    "s_mov_b64 s[44:45], %2\n"
    "s_mov_b64 s[54:55], %3\n"
    "s_mov_b32 s56, %4\n"
    "v_mov_b32 v121, %5\n"
    "s_add_u32 s58, s40, 0x20e000\n"
    "s_addc_u32 s59, s41, 0\n"
    "v_lshlrev_b32 v225, 3, v121\n"
    "v_and_b32 v224, 0x380, v225\n"
    "global_load_dwordx2 v[226:227], v224, s[58:59] sc1\n"
    "v_xor_b32 v112, 0, %6\n"
    "v_xor_b32 v113, 32, %6\n"
    "v_xor_b32 v114, 64, %6\n"
    "v_xor_b32 v115, 96, %6\n"
    "ds_read_b64_tr_b16 a[0:1], v112 offset:0\n"
    "ds_read_b64_tr_b16 a[2:3], v112 offset:1024\n"
    "ds_read_b64_tr_b16 a[4:5], v112 offset:8192\n"
    "ds_read_b64_tr_b16 a[6:7], v112 offset:9216\n"
    "ds_read_b64_tr_b16 a[8:9], v112 offset:16384\n"
    "ds_read_b64_tr_b16 a[10:11], v112 offset:17408\n"
    "ds_read_b64_tr_b16 a[12:13], v112 offset:24576\n"
    "ds_read_b64_tr_b16 a[14:15], v112 offset:25600\n"
    "s_waitcnt lgkmcnt(6)\n"
    "ds_read_b64_tr_b16 a[16:17], v112 offset:32768\n"
    "ds_read_b64_tr_b16 a[18:19], v112 offset:33792\n"
    "ds_read_b64_tr_b16 a[20:21], v112 offset:40960\n"
    "ds_read_b64_tr_b16 a[22:23], v112 offset:41984\n"
    "ds_read_b64_tr_b16 a[24:25], v112 offset:49152\n"
    "ds_read_b64_tr_b16 a[26:27], v112 offset:50176\n"
    "ds_read_b64_tr_b16 a[28:29], v112 offset:57344\n"
    "ds_read_b64_tr_b16 a[30:31], v112 offset:58368\n"
    "s_waitcnt lgkmcnt(6)\n"
    "ds_read_b64_tr_b16 a[64:65], v113 offset:0\n"
    "ds_read_b64_tr_b16 a[66:67], v113 offset:1024\n"
    "ds_read_b64_tr_b16 a[68:69], v113 offset:8192\n"
    "ds_read_b64_tr_b16 a[70:71], v113 offset:9216\n"
    "ds_read_b64_tr_b16 a[72:73], v113 offset:16384\n"
    "ds_read_b64_tr_b16 a[74:75], v113 offset:17408\n"
    "ds_read_b64_tr_b16 a[76:77], v113 offset:24576\n"
    "ds_read_b64_tr_b16 a[78:79], v113 offset:25600\n"
    "s_waitcnt lgkmcnt(6)\n"
    "ds_read_b64_tr_b16 a[80:81], v113 offset:32768\n"
    "ds_read_b64_tr_b16 a[82:83], v113 offset:33792\n"
    "ds_read_b64_tr_b16 a[84:85], v113 offset:40960\n"
    "ds_read_b64_tr_b16 a[86:87], v113 offset:41984\n"
    "ds_read_b64_tr_b16 a[88:89], v113 offset:49152\n"
    "ds_read_b64_tr_b16 a[90:91], v113 offset:50176\n"
    "ds_read_b64_tr_b16 a[92:93], v113 offset:57344\n"
    "ds_read_b64_tr_b16 a[94:95], v113 offset:58368\n"
    "s_waitcnt lgkmcnt(6)\n"
    "ds_read_b64_tr_b16 a[128:129], v114 offset:0\n"
    "ds_read_b64_tr_b16 a[130:131], v114 offset:1024\n"
    "ds_read_b64_tr_b16 a[132:133], v114 offset:8192\n"
    "ds_read_b64_tr_b16 a[134:135], v114 offset:9216\n"
    "ds_read_b64_tr_b16 a[136:137], v114 offset:16384\n"
    "ds_read_b64_tr_b16 a[138:139], v114 offset:17408\n"
    "ds_read_b64_tr_b16 a[140:141], v114 offset:24576\n"
    "ds_read_b64_tr_b16 a[142:143], v114 offset:25600\n"
    "s_waitcnt lgkmcnt(6)\n"
    "ds_read_b64_tr_b16 a[144:145], v114 offset:32768\n"
    "ds_read_b64_tr_b16 a[146:147], v114 offset:33792\n"
    "ds_read_b64_tr_b16 a[148:149], v114 offset:40960\n"
    "ds_read_b64_tr_b16 a[150:151], v114 offset:41984\n"
    "ds_read_b64_tr_b16 a[152:153], v114 offset:49152\n"
    "ds_read_b64_tr_b16 a[154:155], v114 offset:50176\n"
    "ds_read_b64_tr_b16 a[156:157], v114 offset:57344\n"
    "ds_read_b64_tr_b16 a[158:159], v114 offset:58368\n"
    "s_waitcnt lgkmcnt(6)\n"
    "ds_read_b64_tr_b16 a[192:193], v115 offset:0\n"
    "ds_read_b64_tr_b16 a[194:195], v115 offset:1024\n"
    "ds_read_b64_tr_b16 a[196:197], v115 offset:8192\n"
    "ds_read_b64_tr_b16 a[198:199], v115 offset:9216\n"
    "ds_read_b64_tr_b16 a[200:201], v115 offset:16384\n"
    "ds_read_b64_tr_b16 a[202:203], v115 offset:17408\n"
    "ds_read_b64_tr_b16 a[204:205], v115 offset:24576\n"
    "ds_read_b64_tr_b16 a[206:207], v115 offset:25600\n"
    "s_waitcnt lgkmcnt(6)\n"
    "ds_read_b64_tr_b16 a[208:209], v115 offset:32768\n"
    "ds_read_b64_tr_b16 a[210:211], v115 offset:33792\n"
    "ds_read_b64_tr_b16 a[212:213], v115 offset:40960\n"
    "ds_read_b64_tr_b16 a[214:215], v115 offset:41984\n"
    "ds_read_b64_tr_b16 a[216:217], v115 offset:49152\n"
    "ds_read_b64_tr_b16 a[218:219], v115 offset:50176\n"
    "ds_read_b64_tr_b16 a[220:221], v115 offset:57344\n"
    "ds_read_b64_tr_b16 a[222:223], v115 offset:58368\n"
    "s_waitcnt lgkmcnt(6)\n"
    "v_xor_b32 v112, 0, %6\n"
    "v_add_u32 v112, 0x10000, v112\n"
    "v_xor_b32 v113, 32, %6\n"
    "v_add_u32 v113, 0x10000, v113\n"
    "v_xor_b32 v114, 64, %6\n"
    "v_add_u32 v114, 0x10000, v114\n"
    "v_xor_b32 v115, 96, %6\n"
    "v_add_u32 v115, 0x10000, v115\n"
    "ds_read_b64_tr_b16 a[32:33], v112 offset:0\n"
    "ds_read_b64_tr_b16 a[34:35], v112 offset:1024\n"
    "ds_read_b64_tr_b16 a[36:37], v112 offset:8192\n"
    "ds_read_b64_tr_b16 a[38:39], v112 offset:9216\n"
    "ds_read_b64_tr_b16 a[40:41], v112 offset:16384\n"
    "ds_read_b64_tr_b16 a[42:43], v112 offset:17408\n"
    "ds_read_b64_tr_b16 a[44:45], v112 offset:24576\n"
    "ds_read_b64_tr_b16 a[46:47], v112 offset:25600\n"
    "s_waitcnt lgkmcnt(6)\n"
    "ds_read_b64_tr_b16 a[48:49], v112 offset:32768\n"
    "ds_read_b64_tr_b16 a[50:51], v112 offset:33792\n"
    "ds_read_b64_tr_b16 a[52:53], v112 offset:40960\n"
    "ds_read_b64_tr_b16 a[54:55], v112 offset:41984\n"
    "ds_read_b64_tr_b16 a[56:57], v112 offset:49152\n"
    "ds_read_b64_tr_b16 a[58:59], v112 offset:50176\n"
    "ds_read_b64_tr_b16 a[60:61], v112 offset:57344\n"
    "ds_read_b64_tr_b16 a[62:63], v112 offset:58368\n"
    "s_waitcnt lgkmcnt(6)\n"
    "ds_read_b64_tr_b16 a[96:97], v113 offset:0\n"
    "ds_read_b64_tr_b16 a[98:99], v113 offset:1024\n"
    "ds_read_b64_tr_b16 a[100:101], v113 offset:8192\n"
    "ds_read_b64_tr_b16 a[102:103], v113 offset:9216\n"
    "ds_read_b64_tr_b16 a[104:105], v113 offset:16384\n"
    "ds_read_b64_tr_b16 a[106:107], v113 offset:17408\n"
    "ds_read_b64_tr_b16 a[108:109], v113 offset:24576\n"
    "ds_read_b64_tr_b16 a[110:111], v113 offset:25600\n"
    "s_waitcnt lgkmcnt(6)\n"
    "ds_read_b64_tr_b16 a[112:113], v113 offset:32768\n"
    "ds_read_b64_tr_b16 a[114:115], v113 offset:33792\n"
    "ds_read_b64_tr_b16 a[116:117], v113 offset:40960\n"
    "ds_read_b64_tr_b16 a[118:119], v113 offset:41984\n"
    "ds_read_b64_tr_b16 a[120:121], v113 offset:49152\n"
    "ds_read_b64_tr_b16 a[122:123], v113 offset:50176\n"
    "ds_read_b64_tr_b16 a[124:125], v113 offset:57344\n"
    "ds_read_b64_tr_b16 a[126:127], v113 offset:58368\n"
    "s_waitcnt lgkmcnt(6)\n"
    "ds_read_b64_tr_b16 a[160:161], v114 offset:0\n"
    "ds_read_b64_tr_b16 a[162:163], v114 offset:1024\n"
    "ds_read_b64_tr_b16 a[164:165], v114 offset:8192\n"
    "ds_read_b64_tr_b16 a[166:167], v114 offset:9216\n"
    "ds_read_b64_tr_b16 a[168:169], v114 offset:16384\n"
    "ds_read_b64_tr_b16 a[170:171], v114 offset:17408\n"
    "ds_read_b64_tr_b16 a[172:173], v114 offset:24576\n"
    "ds_read_b64_tr_b16 a[174:175], v114 offset:25600\n"
    "s_waitcnt lgkmcnt(6)\n"
    "ds_read_b64_tr_b16 a[176:177], v114 offset:32768\n"
    "ds_read_b64_tr_b16 a[178:179], v114 offset:33792\n"
    "ds_read_b64_tr_b16 a[180:181], v114 offset:40960\n"
    "ds_read_b64_tr_b16 a[182:183], v114 offset:41984\n"
    "ds_read_b64_tr_b16 a[184:185], v114 offset:49152\n"
    "ds_read_b64_tr_b16 a[186:187], v114 offset:50176\n"
    "ds_read_b64_tr_b16 a[188:189], v114 offset:57344\n"
    "ds_read_b64_tr_b16 a[190:191], v114 offset:58368\n"
    "s_waitcnt lgkmcnt(6)\n"
    "ds_read_b64_tr_b16 a[224:225], v115 offset:0\n"
    "ds_read_b64_tr_b16 a[226:227], v115 offset:1024\n"
    "ds_read_b64_tr_b16 a[228:229], v115 offset:8192\n"
    "ds_read_b64_tr_b16 a[230:231], v115 offset:9216\n"
    "ds_read_b64_tr_b16 a[232:233], v115 offset:16384\n"
    "ds_read_b64_tr_b16 a[234:235], v115 offset:17408\n"
    "ds_read_b64_tr_b16 a[236:237], v115 offset:24576\n"
    "ds_read_b64_tr_b16 a[238:239], v115 offset:25600\n"
    "s_waitcnt lgkmcnt(6)\n"
    "ds_read_b64_tr_b16 a[240:241], v115 offset:32768\n"
    "ds_read_b64_tr_b16 a[242:243], v115 offset:33792\n"
    "ds_read_b64_tr_b16 a[244:245], v115 offset:40960\n"
    "ds_read_b64_tr_b16 a[246:247], v115 offset:41984\n"
    "ds_read_b64_tr_b16 a[248:249], v115 offset:49152\n"
    "ds_read_b64_tr_b16 a[250:251], v115 offset:50176\n"
    "ds_read_b64_tr_b16 a[252:253], v115 offset:57344\n"
    "ds_read_b64_tr_b16 a[254:255], v115 offset:58368\n"
    "s_waitcnt lgkmcnt(6)\n"
    "s_lshr_b32 s58, s56, 1\n"
    "s_lshl_b32 s58, s58, 15\n"
    "v_add_u32 v122, s58, v121\n"
    "v_add_u32 v123, 0x10000, v122\n"
    "s_lshl_b32 s50, s56, 14\n"
    "v_mov_b32 v118, 0\n"
    "v_mov_b32 v117, s43\n"
    "s_add_u32 s58, s50, 0x400000\n"
    "s_add_u32 s48, s40, s58\n"
    "s_addc_u32 s49, s41, 0\n"
    "s_mov_b32 s57, 0\n"
    "s_mov_b32 s51, 0\n"
    "s_waitcnt lgkmcnt(0)\n"
    "s_barrier\n"
    "s_add_u32 s58, s40, 0x20e000\n"
    "s_addc_u32 s59, s41, 0\n"
    "s_add_u32 s60, s40, 0x200200\n"
    "s_addc_u32 s61, s41, 0\n"
    "s_or_b32 s62, s42, 1\n"
    "s_mov_b32 s63, s43\n"
    "s_branch G_xc0%=\n"
    "G_xw0%=:\n"
    "global_load_dwordx2 v[226:227], v224, s[58:59] sc1\n"
    "G_xc0%=:\n"
    "s_waitcnt vmcnt(0)\n"
    "v_cmp_ne_u64 vcc, s[62:63], v[226:227]\n"
    "s_nop 0\n"
    "s_cbranch_vccz G_xok0%=\n"
    "s_sleep 4\n"
    "s_add_u32 s57, s57, 1\n"
    "s_and_b32 s68, s57, 0xff\n"
    "s_cbranch_scc1 G_xw0%=\n"
    "global_load_dwordx2 v[226:227], v118, s[60:61] sc1\n"
    "s_waitcnt vmcnt(0)\n"
    "v_cmp_eq_u64 vcc, s[42:43], v[226:227]\n"
    "s_nop 0\n"
    "s_cbranch_vccnz G_xok0%=\n"
    "s_cmp_lt_u32 s57, 0x40000\n"
    "s_cbranch_scc1 G_xw0%=\n"
    "v_mov_b32 v116, s42\n"
    "global_store_dwordx2 v118, v[116:117], s[60:61] sc1\n"
    "G_xok0%=:\n"
    "s_mov_b32 vcc_lo, 0xcccccccc\n"
    "s_mov_b32 vcc_hi, 0xcccccccc\n"
    "s_add_u32 m0, s50, 0\n"
    "s_add_u32 s64, s48, 0\n"
    "s_addc_u32 s65, s49, 0\n"
    "global_load_lds_dwordx4 v121, s[64:65] offset:0 sc1\n"
    "global_load_lds_dwordx4 v121, s[64:65] offset:1024 sc1\n"
    "global_load_lds_dwordx4 v121, s[64:65] offset:2048 sc1\n"
    "global_load_lds_dwordx4 v121, s[64:65] offset:3072 sc1\n"
    "s_add_u32 m0, s50, 4096\n"
    "s_add_u32 s64, s48, 4096\n"
    "s_addc_u32 s65, s49, 0\n"
    "global_load_lds_dwordx4 v121, s[64:65] offset:0 sc1\n"
    "global_load_lds_dwordx4 v121, s[64:65] offset:1024 sc1\n"
    "global_load_lds_dwordx4 v121, s[64:65] offset:2048 sc1\n"
    "global_load_lds_dwordx4 v121, s[64:65] offset:3072 sc1\n"
    "s_add_u32 m0, s50, 8192\n"
    "s_add_u32 s64, s48, 8192\n"
    "s_addc_u32 s65, s49, 0\n"
    "global_load_lds_dwordx4 v121, s[64:65] offset:0 sc1\n"
    "global_load_lds_dwordx4 v121, s[64:65] offset:1024 sc1\n"
    "global_load_lds_dwordx4 v121, s[64:65] offset:2048 sc1\n"
    "global_load_lds_dwordx4 v121, s[64:65] offset:3072 sc1\n"
    "s_add_u32 m0, s50, 12288\n"
    "s_add_u32 s64, s48, 12288\n"
    "s_addc_u32 s65, s49, 0\n"
    "s_add_u32 s48, s48, 0x10000\n"
    "s_addc_u32 s49, s49, 0\n"
    "global_load_lds_dwordx4 v121, s[64:65] offset:0 sc1\n"
    "global_load_lds_dwordx4 v121, s[64:65] offset:1024 sc1\n"
    "global_load_lds_dwordx4 v121, s[64:65] offset:2048 sc1\n"
    "global_load_lds_dwordx4 v121, s[64:65] offset:3072 sc1\n"
    "s_add_u32 m0, s50, 65536\n"
    "s_add_u32 s64, s48, 0\n"
    "s_addc_u32 s65, s49, 0\n"
    "global_load_lds_dwordx4 v121, s[64:65] offset:0 sc1\n"
    "global_load_lds_dwordx4 v121, s[64:65] offset:1024 sc1\n"
    "global_load_lds_dwordx4 v121, s[64:65] offset:2048 sc1\n"
    "global_load_lds_dwordx4 v121, s[64:65] offset:3072 sc1\n"
    "s_add_u32 m0, s50, 69632\n"
    "s_add_u32 s64, s48, 4096\n"
    "s_addc_u32 s65, s49, 0\n"
    "global_load_lds_dwordx4 v121, s[64:65] offset:0 sc1\n"
    "global_load_lds_dwordx4 v121, s[64:65] offset:1024 sc1\n"
    "global_load_lds_dwordx4 v121, s[64:65] offset:2048 sc1\n"
    "global_load_lds_dwordx4 v121, s[64:65] offset:3072 sc1\n"
    "s_add_u32 m0, s50, 73728\n"
    "s_add_u32 s64, s48, 8192\n"
    "s_addc_u32 s65, s49, 0\n"
    "global_load_lds_dwordx4 v121, s[64:65] offset:0 sc1\n"
    "global_load_lds_dwordx4 v121, s[64:65] offset:1024 sc1\n"
    "global_load_lds_dwordx4 v121, s[64:65] offset:2048 sc1\n"
    "s_mov_b64 s[66:67], s[44:45]\n"
    "s_sub_u32 s44, s44, 0x300000\n"
    "s_subb_u32 s45, s45, 0\n"
    "s_waitcnt vmcnt(19) lgkmcnt(0)\n"
    "s_barrier\n"
    "s_bfe_u32 s58, s54, 0x2000a\n"
    "s_lshl_b32 s58, s58, 7\n"
    "s_add_u32 s58, s58, 512\n"
    "v_mov_b32 v112, s58\n"
    "s_add_u32 s58, s40, 0x312000\n"
    "s_addc_u32 s59, s41, 0\n"
    "global_load_dwordx2 v[114:115], v112, s[58:59] sc1\n"
    "ds_read_b128 v[192:195], v122 offset:0\n"
    "ds_read_b128 v[196:199], v122 offset:16384\n"
    "ds_read_b128 v[200:203], v122 offset:1024\n"
    "ds_read_b128 v[204:207], v122 offset:17408\n"
    "ds_read_b128 v[208:211], v122 offset:2048\n"
    "ds_read_b128 v[212:215], v122 offset:18432\n"
    "s_waitcnt lgkmcnt(4)\n"
    "s_branch G_even%=\n"
    ".p2align 6\n"
    "G_loop%=:\n"
    "v_mfma_f32_16x16x32_bf16 v[160:163], a[0:3], v[192:195], 0\n"
    "ds_read_b128 v[216:219], v123 offset:3072\n"
    "v_mfma_f32_16x16x32_bf16 v[164:167], a[64:67], v[192:195], 0\n"
    "ds_read_b128 v[220:223], v123 offset:19456\n"
    "v_mfma_f32_16x16x32_bf16 v[168:171], a[128:131], v[192:195], 0\n"
    "v_mfma_f32_16x16x32_bf16 v[172:175], a[192:195], v[192:195], 0\n"
    "v_cvt_pk_bf16_f32 v224, v128, v129\n"
    "v_cvt_pk_bf16_f32 v225, v130, v131\n"
    "v_mfma_f32_16x16x32_bf16 v[176:179], a[0:3], v[196:199], 0\n"
    "global_load_lds_dwordx4 v121, s[64:65] offset:3072 sc1\n"
    "v_mfma_f32_16x16x32_bf16 v[180:183], a[64:67], v[196:199], 0\n"
    "v_cvt_pk_bf16_f32 v226, v132, v133\n"
    "v_cvt_pk_bf16_f32 v227, v134, v135\n"
    "v_mfma_f32_16x16x32_bf16 v[184:187], a[128:131], v[196:199], 0\n"
    "v_cvt_pk_bf16_f32 v228, v144, v145\n"
    "v_cvt_pk_bf16_f32 v229, v146, v147\n"
    "v_mfma_f32_16x16x32_bf16 v[188:191], a[192:195], v[196:199], 0\n"
    "v_cvt_pk_bf16_f32 v230, v148, v149\n"
    "v_cvt_pk_bf16_f32 v231, v150, v151\n"
    "s_waitcnt lgkmcnt(4)\n"
    "v_mfma_f32_16x16x32_bf16 v[160:163], a[4:7], v[200:203], v[160:163]\n"
    "ds_read_b128 v[192:195], v123 offset:4096\n"
    "v_mfma_f32_16x16x32_bf16 v[164:167], a[68:71], v[200:203], v[164:167]\n"
    "ds_read_b128 v[196:199], v123 offset:20480\n"
    "v_mfma_f32_16x16x32_bf16 v[168:171], a[132:135], v[200:203], v[168:171]\n"
    "s_add_u32 m0, s50, 12288\n"
    "s_add_u32 s64, s48, 12288\n"
    "s_addc_u32 s65, s49, 0\n"
    "v_cvt_pk_bf16_f32 v232, v136, v137\n"
    "v_cvt_pk_bf16_f32 v233, v138, v139\n"
    "v_mfma_f32_16x16x32_bf16 v[172:175], a[196:199], v[200:203], v[172:175]\n"
    "s_add_u32 s48, s48, 0x10000\n"
    "s_addc_u32 s49, s49, 0\n"
    "v_cvt_pk_bf16_f32 v234, v140, v141\n"
    "v_cvt_pk_bf16_f32 v235, v142, v143\n"
    "v_mfma_f32_16x16x32_bf16 v[176:179], a[4:7], v[204:207], v[176:179]\n"
    "global_load_lds_dwordx4 v121, s[64:65] offset:0 sc1\n"
    "v_mfma_f32_16x16x32_bf16 v[180:183], a[68:71], v[204:207], v[180:183]\n"
    "v_cvt_pk_bf16_f32 v236, v152, v153\n"
    "v_cvt_pk_bf16_f32 v237, v154, v155\n"
    "v_mfma_f32_16x16x32_bf16 v[184:187], a[132:135], v[204:207], v[184:187]\n"
    "v_mfma_f32_16x16x32_bf16 v[188:191], a[196:199], v[204:207], v[188:191]\n"
    "v_cvt_pk_bf16_f32 v238, v156, v157\n"
    "v_cvt_pk_bf16_f32 v239, v158, v159\n"
    "s_waitcnt lgkmcnt(4)\n"
    "v_mfma_f32_16x16x32_bf16 v[160:163], a[8:11], v[208:211], v[160:163]\n"
    "ds_read_b128 v[200:203], v123 offset:5120\n"
    "v_mfma_f32_16x16x32_bf16 v[164:167], a[72:75], v[208:211], v[164:167]\n"
    "ds_read_b128 v[204:207], v123 offset:21504\n"
    "v_mfma_f32_16x16x32_bf16 v[168:171], a[136:139], v[208:211], v[168:171]\n"
    "s_not_b64 vcc, vcc\n"
    "v_cndmask_b32_dpp v240, v232, v224, vcc quad_perm:[0,1,0,1] row_mask:0xf bank_mask:0xf\n"
    "v_cndmask_b32_dpp v241, v233, v225, vcc quad_perm:[0,1,0,1] row_mask:0xf bank_mask:0xf\n"
    "v_mfma_f32_16x16x32_bf16 v[172:175], a[200:203], v[208:211], v[172:175]\n"
    "v_cndmask_b32_dpp v242, v234, v226, vcc quad_perm:[0,1,0,1] row_mask:0xf bank_mask:0xf\n"
    "v_cndmask_b32_dpp v243, v235, v227, vcc quad_perm:[0,1,0,1] row_mask:0xf bank_mask:0xf\n"
    "v_mfma_f32_16x16x32_bf16 v[176:179], a[8:11], v[212:215], v[176:179]\n"
    "global_load_lds_dwordx4 v121, s[64:65] offset:1024 sc1\n"
    "v_mfma_f32_16x16x32_bf16 v[180:183], a[72:75], v[212:215], v[180:183]\n"
    "v_cndmask_b32_dpp v248, v236, v228, vcc quad_perm:[0,1,0,1] row_mask:0xf bank_mask:0xf\n"
    "v_cndmask_b32_dpp v249, v237, v229, vcc quad_perm:[0,1,0,1] row_mask:0xf bank_mask:0xf\n"
    "v_mfma_f32_16x16x32_bf16 v[184:187], a[136:139], v[212:215], v[184:187]\n"
    "global_store_dwordx4 v121, v[240:243], s[44:45] offset:0 sc1\n"
    "v_mfma_f32_16x16x32_bf16 v[188:191], a[200:203], v[212:215], v[188:191]\n"
    "v_cndmask_b32_dpp v250, v238, v230, vcc quad_perm:[0,1,0,1] row_mask:0xf bank_mask:0xf\n"
    "v_cndmask_b32_dpp v251, v239, v231, vcc quad_perm:[0,1,0,1] row_mask:0xf bank_mask:0xf\n"
    "s_waitcnt lgkmcnt(4)\n"
    "v_mfma_f32_16x16x32_bf16 v[160:163], a[12:15], v[216:219], v[160:163]\n"
    "ds_read_b128 v[208:211], v123 offset:6144\n"
    "v_mfma_f32_16x16x32_bf16 v[164:167], a[76:79], v[216:219], v[164:167]\n"
    "ds_read_b128 v[212:215], v123 offset:22528\n"
    "v_mfma_f32_16x16x32_bf16 v[168:171], a[140:143], v[216:219], v[168:171]\n"
    "s_not_b64 vcc, vcc\n"
    "v_cndmask_b32_dpp v244, v224, v232, vcc quad_perm:[2,3,2,3] row_mask:0xf bank_mask:0xf\n"
    "v_cndmask_b32_dpp v245, v225, v233, vcc quad_perm:[2,3,2,3] row_mask:0xf bank_mask:0xf\n"
    "v_mfma_f32_16x16x32_bf16 v[172:175], a[204:207], v[216:219], v[172:175]\n"
    "v_cndmask_b32_dpp v246, v226, v234, vcc quad_perm:[2,3,2,3] row_mask:0xf bank_mask:0xf\n"
    "v_cndmask_b32_dpp v247, v227, v235, vcc quad_perm:[2,3,2,3] row_mask:0xf bank_mask:0xf\n"
    "v_mfma_f32_16x16x32_bf16 v[176:179], a[12:15], v[220:223], v[176:179]\n"
    "global_load_lds_dwordx4 v121, s[64:65] offset:2048 sc1\n"
    "v_mfma_f32_16x16x32_bf16 v[180:183], a[76:79], v[220:223], v[180:183]\n"
    "v_cndmask_b32_dpp v252, v228, v236, vcc quad_perm:[2,3,2,3] row_mask:0xf bank_mask:0xf\n"
    "v_cndmask_b32_dpp v253, v229, v237, vcc quad_perm:[2,3,2,3] row_mask:0xf bank_mask:0xf\n"
    "v_mfma_f32_16x16x32_bf16 v[184:187], a[140:143], v[220:223], v[184:187]\n"
    "global_store_dwordx4 v121, v[248:251], s[44:45] offset:2048 sc1\n"
    "v_mfma_f32_16x16x32_bf16 v[188:191], a[204:207], v[220:223], v[188:191]\n"
    "v_cndmask_b32_dpp v254, v230, v238, vcc quad_perm:[2,3,2,3] row_mask:0xf bank_mask:0xf\n"
    "v_cndmask_b32_dpp v255, v231, v239, vcc quad_perm:[2,3,2,3] row_mask:0xf bank_mask:0xf\n"
    "s_waitcnt lgkmcnt(4)\n"
    "v_mfma_f32_16x16x32_bf16 v[160:163], a[16:19], v[192:195], v[160:163]\n"
    "ds_read_b128 v[216:219], v123 offset:7168\n"
    "v_mfma_f32_16x16x32_bf16 v[164:167], a[80:83], v[192:195], v[164:167]\n"
    "ds_read_b128 v[220:223], v123 offset:23552\n"
    "v_mfma_f32_16x16x32_bf16 v[168:171], a[144:147], v[192:195], v[168:171]\n"
    "global_store_dwordx4 v121, v[244:247], s[44:45] offset:1024 sc1\n"
    "v_mfma_f32_16x16x32_bf16 v[172:175], a[208:211], v[192:195], v[172:175]\n"
    "v_mfma_f32_16x16x32_bf16 v[176:179], a[16:19], v[196:199], v[176:179]\n"
    "global_load_lds_dwordx4 v121, s[64:65] offset:3072 sc1\n"
    "v_mfma_f32_16x16x32_bf16 v[180:183], a[80:83], v[196:199], v[180:183]\n"
    "v_mfma_f32_16x16x32_bf16 v[184:187], a[144:147], v[196:199], v[184:187]\n"
    "global_store_dwordx4 v121, v[252:255], s[44:45] offset:3072 sc1\n"
    "v_mfma_f32_16x16x32_bf16 v[188:191], a[208:211], v[196:199], v[188:191]\n"
    "s_waitcnt lgkmcnt(4)\n"
    "v_mfma_f32_16x16x32_bf16 v[160:163], a[20:23], v[200:203], v[160:163]\n"
    "s_waitcnt vmcnt(12) lgkmcnt(0)\n"
    "s_barrier\n"
    "v_mfma_f32_16x16x32_bf16 v[164:167], a[84:87], v[200:203], v[164:167]\n"
    "ds_read_b128 v[192:195], v123 offset:8192\n"
    "v_mfma_f32_16x16x32_bf16 v[168:171], a[148:151], v[200:203], v[168:171]\n"
    "ds_read_b128 v[196:199], v123 offset:24576\n"
    "s_add_u32 m0, s50, 65536\n"
    "s_add_u32 s64, s48, 0\n"
    "s_addc_u32 s65, s49, 0\n"
    "v_mfma_f32_16x16x32_bf16 v[172:175], a[212:215], v[200:203], v[172:175]\n"
    "v_mfma_f32_16x16x32_bf16 v[176:179], a[20:23], v[204:207], v[176:179]\n"
    "global_load_lds_dwordx4 v121, s[64:65] offset:0 sc1\n"
    "v_mfma_f32_16x16x32_bf16 v[180:183], a[84:87], v[204:207], v[180:183]\n"
    "v_mfma_f32_16x16x32_bf16 v[184:187], a[148:151], v[204:207], v[184:187]\n"
    "v_mfma_f32_16x16x32_bf16 v[188:191], a[212:215], v[204:207], v[188:191]\n"
    "s_waitcnt lgkmcnt(4)\n"
    "v_mfma_f32_16x16x32_bf16 v[160:163], a[24:27], v[208:211], v[160:163]\n"
    "ds_read_b128 v[200:203], v123 offset:9216\n"
    "v_mfma_f32_16x16x32_bf16 v[164:167], a[88:91], v[208:211], v[164:167]\n"
    "ds_read_b128 v[204:207], v123 offset:25600\n"
    "v_mfma_f32_16x16x32_bf16 v[168:171], a[152:155], v[208:211], v[168:171]\n"
    "v_mfma_f32_16x16x32_bf16 v[172:175], a[216:219], v[208:211], v[172:175]\n"
    "v_mfma_f32_16x16x32_bf16 v[176:179], a[24:27], v[212:215], v[176:179]\n"
    "global_load_lds_dwordx4 v121, s[64:65] offset:1024 sc1\n"
    "v_mfma_f32_16x16x32_bf16 v[180:183], a[88:91], v[212:215], v[180:183]\n"
    "v_mfma_f32_16x16x32_bf16 v[184:187], a[152:155], v[212:215], v[184:187]\n"
    "v_mfma_f32_16x16x32_bf16 v[188:191], a[216:219], v[212:215], v[188:191]\n"
    "s_waitcnt lgkmcnt(4)\n"
    "v_mfma_f32_16x16x32_bf16 v[160:163], a[28:31], v[216:219], v[160:163]\n"
    "ds_read_b128 v[208:211], v123 offset:10240\n"
    "v_mfma_f32_16x16x32_bf16 v[164:167], a[92:95], v[216:219], v[164:167]\n"
    "ds_read_b128 v[212:215], v123 offset:26624\n"
    "v_mfma_f32_16x16x32_bf16 v[168:171], a[156:159], v[216:219], v[168:171]\n"
    "v_mfma_f32_16x16x32_bf16 v[172:175], a[220:223], v[216:219], v[172:175]\n"
    "v_mfma_f32_16x16x32_bf16 v[176:179], a[28:31], v[220:223], v[176:179]\n"
    "global_load_lds_dwordx4 v121, s[64:65] offset:2048 sc1\n"
    "v_mfma_f32_16x16x32_bf16 v[180:183], a[92:95], v[220:223], v[180:183]\n"
    "s_mov_b64 s[44:45], s[66:67]\n"
    "s_add_u32 s66, s66, 0x100000\n"
    "s_addc_u32 s67, s67, 0\n"
    "v_mfma_f32_16x16x32_bf16 v[184:187], a[156:159], v[220:223], v[184:187]\n"
    "s_xor_b32 s66, s66, 0x2000\n"
    "v_mfma_f32_16x16x32_bf16 v[188:191], a[220:223], v[220:223], v[188:191]\n"
    "s_waitcnt lgkmcnt(4)\n"
    "v_mfma_f32_16x16x32_bf16 v[160:163], a[32:35], v[192:195], v[160:163]\n"
    "ds_read_b128 v[216:219], v123 offset:11264\n"
    "v_mfma_f32_16x16x32_bf16 v[164:167], a[96:99], v[192:195], v[164:167]\n"
    "ds_read_b128 v[220:223], v123 offset:27648\n"
    "v_mfma_f32_16x16x32_bf16 v[168:171], a[160:163], v[192:195], v[168:171]\n"
    "v_mfma_f32_16x16x32_bf16 v[172:175], a[224:227], v[192:195], v[172:175]\n"
    "v_mfma_f32_16x16x32_bf16 v[176:179], a[32:35], v[196:199], v[176:179]\n"
    "global_load_lds_dwordx4 v121, s[64:65] offset:3072 sc1\n"
    "v_mfma_f32_16x16x32_bf16 v[180:183], a[96:99], v[196:199], v[180:183]\n"
    "v_mfma_f32_16x16x32_bf16 v[184:187], a[160:163], v[196:199], v[184:187]\n"
    "v_mfma_f32_16x16x32_bf16 v[188:191], a[224:227], v[196:199], v[188:191]\n"
    "s_waitcnt lgkmcnt(4)\n"
    "v_mfma_f32_16x16x32_bf16 v[160:163], a[36:39], v[200:203], v[160:163]\n"
    "ds_read_b128 v[192:195], v123 offset:12288\n"
    "v_mfma_f32_16x16x32_bf16 v[164:167], a[100:103], v[200:203], v[164:167]\n"
    "ds_read_b128 v[196:199], v123 offset:28672\n"
    "v_mfma_f32_16x16x32_bf16 v[168:171], a[164:167], v[200:203], v[168:171]\n"
    "s_add_u32 m0, s50, 69632\n"
    "s_add_u32 s64, s48, 4096\n"
    "s_addc_u32 s65, s49, 0\n"
    "v_mfma_f32_16x16x32_bf16 v[172:175], a[228:231], v[200:203], v[172:175]\n"
    "v_mfma_f32_16x16x32_bf16 v[176:179], a[36:39], v[204:207], v[176:179]\n"
    "global_load_lds_dwordx4 v121, s[64:65] offset:0 sc1\n"
    "v_mfma_f32_16x16x32_bf16 v[180:183], a[100:103], v[204:207], v[180:183]\n"
    "v_mfma_f32_16x16x32_bf16 v[184:187], a[164:167], v[204:207], v[184:187]\n"
    "v_mfma_f32_16x16x32_bf16 v[188:191], a[228:231], v[204:207], v[188:191]\n"
    "s_waitcnt lgkmcnt(4)\n"
    "v_mfma_f32_16x16x32_bf16 v[160:163], a[40:43], v[208:211], v[160:163]\n"
    "ds_read_b128 v[200:203], v123 offset:13312\n"
    "v_mfma_f32_16x16x32_bf16 v[164:167], a[104:107], v[208:211], v[164:167]\n"
    "ds_read_b128 v[204:207], v123 offset:29696\n"
    "v_mfma_f32_16x16x32_bf16 v[168:171], a[168:171], v[208:211], v[168:171]\n"
    "v_mfma_f32_16x16x32_bf16 v[172:175], a[232:235], v[208:211], v[172:175]\n"
    "v_mfma_f32_16x16x32_bf16 v[176:179], a[40:43], v[212:215], v[176:179]\n"
    "global_load_lds_dwordx4 v121, s[64:65] offset:1024 sc1\n"
    "v_mfma_f32_16x16x32_bf16 v[180:183], a[104:107], v[212:215], v[180:183]\n"
    "v_mfma_f32_16x16x32_bf16 v[184:187], a[168:171], v[212:215], v[184:187]\n"
    "v_mfma_f32_16x16x32_bf16 v[188:191], a[232:235], v[212:215], v[188:191]\n"
    "s_waitcnt lgkmcnt(4)\n"
    "v_mfma_f32_16x16x32_bf16 v[160:163], a[44:47], v[216:219], v[160:163]\n"
    "ds_read_b128 v[208:211], v123 offset:14336\n"
    "v_mfma_f32_16x16x32_bf16 v[164:167], a[108:111], v[216:219], v[164:167]\n"
    "ds_read_b128 v[212:215], v123 offset:30720\n"
    "v_mfma_f32_16x16x32_bf16 v[168:171], a[172:175], v[216:219], v[168:171]\n"
    "v_mfma_f32_16x16x32_bf16 v[172:175], a[236:239], v[216:219], v[172:175]\n"
    "v_mfma_f32_16x16x32_bf16 v[176:179], a[44:47], v[220:223], v[176:179]\n"
    "global_load_lds_dwordx4 v121, s[64:65] offset:2048 sc1\n"
    "v_mfma_f32_16x16x32_bf16 v[180:183], a[108:111], v[220:223], v[180:183]\n"
    "v_mfma_f32_16x16x32_bf16 v[184:187], a[172:175], v[220:223], v[184:187]\n"
    "v_mfma_f32_16x16x32_bf16 v[188:191], a[236:239], v[220:223], v[188:191]\n"
    "s_waitcnt lgkmcnt(4)\n"
    "v_mfma_f32_16x16x32_bf16 v[160:163], a[48:51], v[192:195], v[160:163]\n"
    "ds_read_b128 v[216:219], v123 offset:15360\n"
    "v_mfma_f32_16x16x32_bf16 v[164:167], a[112:115], v[192:195], v[164:167]\n"
    "ds_read_b128 v[220:223], v123 offset:31744\n"
    "v_mfma_f32_16x16x32_bf16 v[168:171], a[176:179], v[192:195], v[168:171]\n"
    "v_mfma_f32_16x16x32_bf16 v[172:175], a[240:243], v[192:195], v[172:175]\n"
    "v_mfma_f32_16x16x32_bf16 v[176:179], a[48:51], v[196:199], v[176:179]\n"
    "global_load_lds_dwordx4 v121, s[64:65] offset:3072 sc1\n"
    "v_mfma_f32_16x16x32_bf16 v[180:183], a[112:115], v[196:199], v[180:183]\n"
    "v_mfma_f32_16x16x32_bf16 v[184:187], a[176:179], v[196:199], v[184:187]\n"
    "v_mfma_f32_16x16x32_bf16 v[188:191], a[240:243], v[196:199], v[188:191]\n"
    "s_waitcnt lgkmcnt(4)\n"
    "v_mfma_f32_16x16x32_bf16 v[160:163], a[52:55], v[200:203], v[160:163]\n"
    "s_waitcnt vmcnt(8) lgkmcnt(0)\n"
    "s_barrier\n"
    "v_mfma_f32_16x16x32_bf16 v[164:167], a[116:119], v[200:203], v[164:167]\n"
    "ds_read_b128 v[192:195], v122 offset:0\n"
    "v_mfma_f32_16x16x32_bf16 v[168:171], a[180:183], v[200:203], v[168:171]\n"
    "ds_read_b128 v[196:199], v122 offset:16384\n"
    "s_add_u32 m0, s50, 73728\n"
    "s_add_u32 s64, s48, 8192\n"
    "s_addc_u32 s65, s49, 0\n"
    "v_mfma_f32_16x16x32_bf16 v[172:175], a[244:247], v[200:203], v[172:175]\n"
    "v_mfma_f32_16x16x32_bf16 v[176:179], a[52:55], v[204:207], v[176:179]\n"
    "global_load_lds_dwordx4 v121, s[64:65] offset:0 sc1\n"
    "v_mfma_f32_16x16x32_bf16 v[180:183], a[116:119], v[204:207], v[180:183]\n"
    "s_cmp_lg_u32 s56, 0\n"
    "s_cbranch_scc1 G_f1%=\n"
    "s_or_b32 s58, s42, s51\n"
    "s_mov_b64 exec, 1\n"
    "v_mov_b32 v116, s58\n"
    "global_store_dwordx2 v118, v[116:117], s[54:55] sc1\n"
    "s_mov_b64 exec, -1\n"
    "G_f1%=:\n"
    "v_mfma_f32_16x16x32_bf16 v[184:187], a[180:183], v[204:207], v[184:187]\n"
    "v_mfma_f32_16x16x32_bf16 v[188:191], a[244:247], v[204:207], v[188:191]\n"
    "s_waitcnt lgkmcnt(4)\n"
    "v_mfma_f32_16x16x32_bf16 v[160:163], a[56:59], v[208:211], v[160:163]\n"
    "ds_read_b128 v[200:203], v122 offset:1024\n"
    "v_mfma_f32_16x16x32_bf16 v[164:167], a[120:123], v[208:211], v[164:167]\n"
    "ds_read_b128 v[204:207], v122 offset:17408\n"
    "v_mfma_f32_16x16x32_bf16 v[168:171], a[184:187], v[208:211], v[168:171]\n"
    "v_mfma_f32_16x16x32_bf16 v[172:175], a[248:251], v[208:211], v[172:175]\n"
    "v_mfma_f32_16x16x32_bf16 v[176:179], a[56:59], v[212:215], v[176:179]\n"
    "global_load_lds_dwordx4 v121, s[64:65] offset:1024 sc1\n"
    "v_mfma_f32_16x16x32_bf16 v[180:183], a[120:123], v[212:215], v[180:183]\n"
    "v_mfma_f32_16x16x32_bf16 v[184:187], a[184:187], v[212:215], v[184:187]\n"
    "v_mfma_f32_16x16x32_bf16 v[188:191], a[248:251], v[212:215], v[188:191]\n"
    "s_add_u32 s51, s51, 1\n"
    "s_waitcnt lgkmcnt(4)\n"
    "v_mfma_f32_16x16x32_bf16 v[160:163], a[60:63], v[216:219], v[160:163]\n"
    "ds_read_b128 v[208:211], v122 offset:2048\n"
    "v_mfma_f32_16x16x32_bf16 v[164:167], a[124:127], v[216:219], v[164:167]\n"
    "ds_read_b128 v[212:215], v122 offset:18432\n"
    "v_mfma_f32_16x16x32_bf16 v[168:171], a[188:191], v[216:219], v[168:171]\n"
    "v_mfma_f32_16x16x32_bf16 v[172:175], a[252:255], v[216:219], v[172:175]\n"
    "v_mfma_f32_16x16x32_bf16 v[176:179], a[60:63], v[220:223], v[176:179]\n"
    "global_load_lds_dwordx4 v121, s[64:65] offset:2048 sc1\n"
    "v_mfma_f32_16x16x32_bf16 v[180:183], a[124:127], v[220:223], v[180:183]\n"
    "v_mfma_f32_16x16x32_bf16 v[184:187], a[188:191], v[220:223], v[184:187]\n"
    "v_mfma_f32_16x16x32_bf16 v[188:191], a[252:255], v[220:223], v[188:191]\n"
    "s_add_u32 s58, s51, -3\n"
    "s_cmp_gt_u32 s58, 28\n"
    "s_waitcnt lgkmcnt(4)\n"
    "s_cbranch_scc1 G_t1%=\n"
    "G_even%=:\n"
    "v_mfma_f32_16x16x32_bf16 v[128:131], a[0:3], v[192:195], 0\n"
    "ds_read_b128 v[216:219], v122 offset:3072\n"
    "v_mfma_f32_16x16x32_bf16 v[132:135], a[64:67], v[192:195], 0\n"
    "ds_read_b128 v[220:223], v122 offset:19456\n"
    "v_mfma_f32_16x16x32_bf16 v[136:139], a[128:131], v[192:195], 0\n"
    "v_mfma_f32_16x16x32_bf16 v[140:143], a[192:195], v[192:195], 0\n"
    "v_cvt_pk_bf16_f32 v224, v160, v161\n"
    "v_cvt_pk_bf16_f32 v225, v162, v163\n"
    "v_mfma_f32_16x16x32_bf16 v[144:147], a[0:3], v[196:199], 0\n"
    "global_load_lds_dwordx4 v121, s[64:65] offset:3072 sc1\n"
    "v_mfma_f32_16x16x32_bf16 v[148:151], a[64:67], v[196:199], 0\n"
    "v_cvt_pk_bf16_f32 v226, v164, v165\n"
    "v_cvt_pk_bf16_f32 v227, v166, v167\n"
    "v_mfma_f32_16x16x32_bf16 v[152:155], a[128:131], v[196:199], 0\n"
    "v_cvt_pk_bf16_f32 v228, v176, v177\n"
    "v_cvt_pk_bf16_f32 v229, v178, v179\n"
    "v_mfma_f32_16x16x32_bf16 v[156:159], a[192:195], v[196:199], 0\n"
    "v_cvt_pk_bf16_f32 v230, v180, v181\n"
    "v_cvt_pk_bf16_f32 v231, v182, v183\n"
    "s_waitcnt lgkmcnt(4)\n"
    "v_mfma_f32_16x16x32_bf16 v[128:131], a[4:7], v[200:203], v[128:131]\n"
    "ds_read_b128 v[192:195], v122 offset:4096\n"
    "v_mfma_f32_16x16x32_bf16 v[132:135], a[68:71], v[200:203], v[132:135]\n"
    "ds_read_b128 v[196:199], v122 offset:20480\n"
    "v_mfma_f32_16x16x32_bf16 v[136:139], a[132:135], v[200:203], v[136:139]\n"
    "s_add_u32 m0, s50, 77824\n"
    "s_add_u32 s64, s48, 12288\n"
    "s_addc_u32 s65, s49, 0\n"
    "v_cvt_pk_bf16_f32 v232, v168, v169\n"
    "v_cvt_pk_bf16_f32 v233, v170, v171\n"
    "v_mfma_f32_16x16x32_bf16 v[140:143], a[196:199], v[200:203], v[140:143]\n"
    "s_add_u32 s48, s48, 0x10000\n"
    "s_addc_u32 s49, s49, 0\n"
    "v_cvt_pk_bf16_f32 v234, v172, v173\n"
    "v_cvt_pk_bf16_f32 v235, v174, v175\n"
    "v_mfma_f32_16x16x32_bf16 v[144:147], a[4:7], v[204:207], v[144:147]\n"
    "global_load_lds_dwordx4 v121, s[64:65] offset:0 sc1\n"
    "v_mfma_f32_16x16x32_bf16 v[148:151], a[68:71], v[204:207], v[148:151]\n"
    "v_cvt_pk_bf16_f32 v236, v184, v185\n"
    "v_cvt_pk_bf16_f32 v237, v186, v187\n"
    "v_mfma_f32_16x16x32_bf16 v[152:155], a[132:135], v[204:207], v[152:155]\n"
    "v_mfma_f32_16x16x32_bf16 v[156:159], a[196:199], v[204:207], v[156:159]\n"
    "v_cvt_pk_bf16_f32 v238, v188, v189\n"
    "v_cvt_pk_bf16_f32 v239, v190, v191\n"
    "s_waitcnt lgkmcnt(4)\n"
    "v_mfma_f32_16x16x32_bf16 v[128:131], a[8:11], v[208:211], v[128:131]\n"
    "ds_read_b128 v[200:203], v122 offset:5120\n"
    "v_mfma_f32_16x16x32_bf16 v[132:135], a[72:75], v[208:211], v[132:135]\n"
    "ds_read_b128 v[204:207], v122 offset:21504\n"
    "v_mfma_f32_16x16x32_bf16 v[136:139], a[136:139], v[208:211], v[136:139]\n"
    "s_not_b64 vcc, vcc\n"
    "v_cndmask_b32_dpp v240, v232, v224, vcc quad_perm:[0,1,0,1] row_mask:0xf bank_mask:0xf\n"
    "v_cndmask_b32_dpp v241, v233, v225, vcc quad_perm:[0,1,0,1] row_mask:0xf bank_mask:0xf\n"
    "v_mfma_f32_16x16x32_bf16 v[140:143], a[200:203], v[208:211], v[140:143]\n"
    "v_cndmask_b32_dpp v242, v234, v226, vcc quad_perm:[0,1,0,1] row_mask:0xf bank_mask:0xf\n"
    "v_cndmask_b32_dpp v243, v235, v227, vcc quad_perm:[0,1,0,1] row_mask:0xf bank_mask:0xf\n"
    "v_mfma_f32_16x16x32_bf16 v[144:147], a[8:11], v[212:215], v[144:147]\n"
    "global_load_lds_dwordx4 v121, s[64:65] offset:1024 sc1\n"
    "v_mfma_f32_16x16x32_bf16 v[148:151], a[72:75], v[212:215], v[148:151]\n"
    "v_cndmask_b32_dpp v248, v236, v228, vcc quad_perm:[0,1,0,1] row_mask:0xf bank_mask:0xf\n"
    "v_cndmask_b32_dpp v249, v237, v229, vcc quad_perm:[0,1,0,1] row_mask:0xf bank_mask:0xf\n"
    "v_mfma_f32_16x16x32_bf16 v[152:155], a[136:139], v[212:215], v[152:155]\n"
    "global_store_dwordx4 v121, v[240:243], s[44:45] offset:0 sc1\n"
    "v_mfma_f32_16x16x32_bf16 v[156:159], a[200:203], v[212:215], v[156:159]\n"
    "v_cndmask_b32_dpp v250, v238, v230, vcc quad_perm:[0,1,0,1] row_mask:0xf bank_mask:0xf\n"
    "v_cndmask_b32_dpp v251, v239, v231, vcc quad_perm:[0,1,0,1] row_mask:0xf bank_mask:0xf\n"
    "s_waitcnt lgkmcnt(4)\n"
    "v_mfma_f32_16x16x32_bf16 v[128:131], a[12:15], v[216:219], v[128:131]\n"
    "ds_read_b128 v[208:211], v122 offset:6144\n"
    "v_mfma_f32_16x16x32_bf16 v[132:135], a[76:79], v[216:219], v[132:135]\n"
    "ds_read_b128 v[212:215], v122 offset:22528\n"
    "v_mfma_f32_16x16x32_bf16 v[136:139], a[140:143], v[216:219], v[136:139]\n"
    "s_not_b64 vcc, vcc\n"
    "v_cndmask_b32_dpp v244, v224, v232, vcc quad_perm:[2,3,2,3] row_mask:0xf bank_mask:0xf\n"
    "v_cndmask_b32_dpp v245, v225, v233, vcc quad_perm:[2,3,2,3] row_mask:0xf bank_mask:0xf\n"
    "v_mfma_f32_16x16x32_bf16 v[140:143], a[204:207], v[216:219], v[140:143]\n"
    "v_cndmask_b32_dpp v246, v226, v234, vcc quad_perm:[2,3,2,3] row_mask:0xf bank_mask:0xf\n"
    "v_cndmask_b32_dpp v247, v227, v235, vcc quad_perm:[2,3,2,3] row_mask:0xf bank_mask:0xf\n"
    "v_mfma_f32_16x16x32_bf16 v[144:147], a[12:15], v[220:223], v[144:147]\n"
    "global_load_lds_dwordx4 v121, s[64:65] offset:2048 sc1\n"
    "v_mfma_f32_16x16x32_bf16 v[148:151], a[76:79], v[220:223], v[148:151]\n"
    "v_cndmask_b32_dpp v252, v228, v236, vcc quad_perm:[2,3,2,3] row_mask:0xf bank_mask:0xf\n"
    "v_cndmask_b32_dpp v253, v229, v237, vcc quad_perm:[2,3,2,3] row_mask:0xf bank_mask:0xf\n"
    "v_mfma_f32_16x16x32_bf16 v[152:155], a[140:143], v[220:223], v[152:155]\n"
    "global_store_dwordx4 v121, v[248:251], s[44:45] offset:2048 sc1\n"
    "v_mfma_f32_16x16x32_bf16 v[156:159], a[204:207], v[220:223], v[156:159]\n"
    "v_cndmask_b32_dpp v254, v230, v238, vcc quad_perm:[2,3,2,3] row_mask:0xf bank_mask:0xf\n"
    "v_cndmask_b32_dpp v255, v231, v239, vcc quad_perm:[2,3,2,3] row_mask:0xf bank_mask:0xf\n"
    "s_waitcnt lgkmcnt(4)\n"
    "v_mfma_f32_16x16x32_bf16 v[128:131], a[16:19], v[192:195], v[128:131]\n"
    "ds_read_b128 v[216:219], v122 offset:7168\n"
    "v_mfma_f32_16x16x32_bf16 v[132:135], a[80:83], v[192:195], v[132:135]\n"
    "ds_read_b128 v[220:223], v122 offset:23552\n"
    "v_mfma_f32_16x16x32_bf16 v[136:139], a[144:147], v[192:195], v[136:139]\n"
    "global_store_dwordx4 v121, v[244:247], s[44:45] offset:1024 sc1\n"
    "v_mfma_f32_16x16x32_bf16 v[140:143], a[208:211], v[192:195], v[140:143]\n"
    "v_mfma_f32_16x16x32_bf16 v[144:147], a[16:19], v[196:199], v[144:147]\n"
    "global_load_lds_dwordx4 v121, s[64:65] offset:3072 sc1\n"
    "v_mfma_f32_16x16x32_bf16 v[148:151], a[80:83], v[196:199], v[148:151]\n"
    "v_mfma_f32_16x16x32_bf16 v[152:155], a[144:147], v[196:199], v[152:155]\n"
    "global_store_dwordx4 v121, v[252:255], s[44:45] offset:3072 sc1\n"
    "v_mfma_f32_16x16x32_bf16 v[156:159], a[208:211], v[196:199], v[156:159]\n"
    "s_waitcnt lgkmcnt(4)\n"
    "v_mfma_f32_16x16x32_bf16 v[128:131], a[20:23], v[200:203], v[128:131]\n"
    "s_waitcnt vmcnt(12) lgkmcnt(0)\n"
    "s_barrier\n"
    "v_mfma_f32_16x16x32_bf16 v[132:135], a[84:87], v[200:203], v[132:135]\n"
    "ds_read_b128 v[192:195], v122 offset:8192\n"
    "v_mfma_f32_16x16x32_bf16 v[136:139], a[148:151], v[200:203], v[136:139]\n"
    "ds_read_b128 v[196:199], v122 offset:24576\n"
    "s_add_u32 m0, s50, 0\n"
    "s_add_u32 s64, s48, 0\n"
    "s_addc_u32 s65, s49, 0\n"
    "v_mfma_f32_16x16x32_bf16 v[140:143], a[212:215], v[200:203], v[140:143]\n"
    "v_mfma_f32_16x16x32_bf16 v[144:147], a[20:23], v[204:207], v[144:147]\n"
    "global_load_lds_dwordx4 v121, s[64:65] offset:0 sc1\n"
    "v_mfma_f32_16x16x32_bf16 v[148:151], a[84:87], v[204:207], v[148:151]\n"
    "v_mfma_f32_16x16x32_bf16 v[152:155], a[148:151], v[204:207], v[152:155]\n"
    "v_mfma_f32_16x16x32_bf16 v[156:159], a[212:215], v[204:207], v[156:159]\n"
    "s_waitcnt lgkmcnt(4)\n"
    "v_mfma_f32_16x16x32_bf16 v[128:131], a[24:27], v[208:211], v[128:131]\n"
    "ds_read_b128 v[200:203], v122 offset:9216\n"
    "v_mfma_f32_16x16x32_bf16 v[132:135], a[88:91], v[208:211], v[132:135]\n"
    "ds_read_b128 v[204:207], v122 offset:25600\n"
    "v_mfma_f32_16x16x32_bf16 v[136:139], a[152:155], v[208:211], v[136:139]\n"
    "v_mfma_f32_16x16x32_bf16 v[140:143], a[216:219], v[208:211], v[140:143]\n"
    "v_mfma_f32_16x16x32_bf16 v[144:147], a[24:27], v[212:215], v[144:147]\n"
    "global_load_lds_dwordx4 v121, s[64:65] offset:1024 sc1\n"
    "v_mfma_f32_16x16x32_bf16 v[148:151], a[88:91], v[212:215], v[148:151]\n"
    "v_mfma_f32_16x16x32_bf16 v[152:155], a[152:155], v[212:215], v[152:155]\n"
    "v_mfma_f32_16x16x32_bf16 v[156:159], a[216:219], v[212:215], v[156:159]\n"
    "s_waitcnt lgkmcnt(4)\n"
    "v_mfma_f32_16x16x32_bf16 v[128:131], a[28:31], v[216:219], v[128:131]\n"
    "ds_read_b128 v[208:211], v122 offset:10240\n"
    "v_mfma_f32_16x16x32_bf16 v[132:135], a[92:95], v[216:219], v[132:135]\n"
    "ds_read_b128 v[212:215], v122 offset:26624\n"
    "v_mfma_f32_16x16x32_bf16 v[136:139], a[156:159], v[216:219], v[136:139]\n"
    "v_mfma_f32_16x16x32_bf16 v[140:143], a[220:223], v[216:219], v[140:143]\n"
    "v_mfma_f32_16x16x32_bf16 v[144:147], a[28:31], v[220:223], v[144:147]\n"
    "global_load_lds_dwordx4 v121, s[64:65] offset:2048 sc1\n"
    "v_mfma_f32_16x16x32_bf16 v[148:151], a[92:95], v[220:223], v[148:151]\n"
    "s_mov_b64 s[44:45], s[66:67]\n"
    "s_add_u32 s66, s66, 0x100000\n"
    "s_addc_u32 s67, s67, 0\n"
    "v_mfma_f32_16x16x32_bf16 v[152:155], a[156:159], v[220:223], v[152:155]\n"
    "v_mfma_f32_16x16x32_bf16 v[156:159], a[220:223], v[220:223], v[156:159]\n"
    "s_waitcnt lgkmcnt(4)\n"
    "v_mfma_f32_16x16x32_bf16 v[128:131], a[32:35], v[192:195], v[128:131]\n"
    "ds_read_b128 v[216:219], v122 offset:11264\n"
    "v_mfma_f32_16x16x32_bf16 v[132:135], a[96:99], v[192:195], v[132:135]\n"
    "ds_read_b128 v[220:223], v122 offset:27648\n"
    "v_mfma_f32_16x16x32_bf16 v[136:139], a[160:163], v[192:195], v[136:139]\n"
    "v_mfma_f32_16x16x32_bf16 v[140:143], a[224:227], v[192:195], v[140:143]\n"
    "v_mfma_f32_16x16x32_bf16 v[144:147], a[32:35], v[196:199], v[144:147]\n"
    "global_load_lds_dwordx4 v121, s[64:65] offset:3072 sc1\n"
    "v_mfma_f32_16x16x32_bf16 v[148:151], a[96:99], v[196:199], v[148:151]\n"
    "v_mfma_f32_16x16x32_bf16 v[152:155], a[160:163], v[196:199], v[152:155]\n"
    "v_mfma_f32_16x16x32_bf16 v[156:159], a[224:227], v[196:199], v[156:159]\n"
    "s_waitcnt lgkmcnt(4)\n"
    "v_mfma_f32_16x16x32_bf16 v[128:131], a[36:39], v[200:203], v[128:131]\n"
    "ds_read_b128 v[192:195], v122 offset:12288\n"
    "v_mfma_f32_16x16x32_bf16 v[132:135], a[100:103], v[200:203], v[132:135]\n"
    "ds_read_b128 v[196:199], v122 offset:28672\n"
    "v_mfma_f32_16x16x32_bf16 v[136:139], a[164:167], v[200:203], v[136:139]\n"
    "s_add_u32 m0, s50, 4096\n"
    "s_add_u32 s64, s48, 4096\n"
    "s_addc_u32 s65, s49, 0\n"
    "v_mfma_f32_16x16x32_bf16 v[140:143], a[228:231], v[200:203], v[140:143]\n"
    "v_mfma_f32_16x16x32_bf16 v[144:147], a[36:39], v[204:207], v[144:147]\n"
    "global_load_lds_dwordx4 v121, s[64:65] offset:0 sc1\n"
    "v_mfma_f32_16x16x32_bf16 v[148:151], a[100:103], v[204:207], v[148:151]\n"
    "v_mfma_f32_16x16x32_bf16 v[152:155], a[164:167], v[204:207], v[152:155]\n"
    "v_mfma_f32_16x16x32_bf16 v[156:159], a[228:231], v[204:207], v[156:159]\n"
    "s_waitcnt lgkmcnt(4)\n"
    "v_mfma_f32_16x16x32_bf16 v[128:131], a[40:43], v[208:211], v[128:131]\n"
    "ds_read_b128 v[200:203], v122 offset:13312\n"
    "v_mfma_f32_16x16x32_bf16 v[132:135], a[104:107], v[208:211], v[132:135]\n"
    "ds_read_b128 v[204:207], v122 offset:29696\n"
    "v_mfma_f32_16x16x32_bf16 v[136:139], a[168:171], v[208:211], v[136:139]\n"
    "v_mfma_f32_16x16x32_bf16 v[140:143], a[232:235], v[208:211], v[140:143]\n"
    "v_mfma_f32_16x16x32_bf16 v[144:147], a[40:43], v[212:215], v[144:147]\n"
    "global_load_lds_dwordx4 v121, s[64:65] offset:1024 sc1\n"
    "v_mfma_f32_16x16x32_bf16 v[148:151], a[104:107], v[212:215], v[148:151]\n"
    "v_mfma_f32_16x16x32_bf16 v[152:155], a[168:171], v[212:215], v[152:155]\n"
    "v_mfma_f32_16x16x32_bf16 v[156:159], a[232:235], v[212:215], v[156:159]\n"
    "s_waitcnt lgkmcnt(4)\n"
    "v_mfma_f32_16x16x32_bf16 v[128:131], a[44:47], v[216:219], v[128:131]\n"
    "ds_read_b128 v[208:211], v122 offset:14336\n"
    "v_mfma_f32_16x16x32_bf16 v[132:135], a[108:111], v[216:219], v[132:135]\n"
    "ds_read_b128 v[212:215], v122 offset:30720\n"
    "v_mfma_f32_16x16x32_bf16 v[136:139], a[172:175], v[216:219], v[136:139]\n"
    "v_mfma_f32_16x16x32_bf16 v[140:143], a[236:239], v[216:219], v[140:143]\n"
    "v_mfma_f32_16x16x32_bf16 v[144:147], a[44:47], v[220:223], v[144:147]\n"
    "global_load_lds_dwordx4 v121, s[64:65] offset:2048 sc1\n"
    "v_mfma_f32_16x16x32_bf16 v[148:151], a[108:111], v[220:223], v[148:151]\n"
    "v_mfma_f32_16x16x32_bf16 v[152:155], a[172:175], v[220:223], v[152:155]\n"
    "v_mfma_f32_16x16x32_bf16 v[156:159], a[236:239], v[220:223], v[156:159]\n"
    "s_waitcnt lgkmcnt(4)\n"
    "v_mfma_f32_16x16x32_bf16 v[128:131], a[48:51], v[192:195], v[128:131]\n"
    "ds_read_b128 v[216:219], v122 offset:15360\n"
    "v_mfma_f32_16x16x32_bf16 v[132:135], a[112:115], v[192:195], v[132:135]\n"
    "ds_read_b128 v[220:223], v122 offset:31744\n"
    "v_mfma_f32_16x16x32_bf16 v[136:139], a[176:179], v[192:195], v[136:139]\n"
    "v_mfma_f32_16x16x32_bf16 v[140:143], a[240:243], v[192:195], v[140:143]\n"
    "v_mfma_f32_16x16x32_bf16 v[144:147], a[48:51], v[196:199], v[144:147]\n"
    "global_load_lds_dwordx4 v121, s[64:65] offset:3072 sc1\n"
    "v_mfma_f32_16x16x32_bf16 v[148:151], a[112:115], v[196:199], v[148:151]\n"
    "v_mfma_f32_16x16x32_bf16 v[152:155], a[176:179], v[196:199], v[152:155]\n"
    "v_mfma_f32_16x16x32_bf16 v[156:159], a[240:243], v[196:199], v[156:159]\n"
    "s_waitcnt lgkmcnt(4)\n"
    "v_mfma_f32_16x16x32_bf16 v[128:131], a[52:55], v[200:203], v[128:131]\n"
    "s_waitcnt vmcnt(8) lgkmcnt(0)\n"
    "s_barrier\n"
    "v_mfma_f32_16x16x32_bf16 v[132:135], a[116:119], v[200:203], v[132:135]\n"
    "ds_read_b128 v[192:195], v123 offset:0\n"
    "v_mfma_f32_16x16x32_bf16 v[136:139], a[180:183], v[200:203], v[136:139]\n"
    "ds_read_b128 v[196:199], v123 offset:16384\n"
    "s_add_u32 m0, s50, 8192\n"
    "s_add_u32 s64, s48, 8192\n"
    "s_addc_u32 s65, s49, 0\n"
    "v_mfma_f32_16x16x32_bf16 v[140:143], a[244:247], v[200:203], v[140:143]\n"
    "v_mfma_f32_16x16x32_bf16 v[144:147], a[52:55], v[204:207], v[144:147]\n"
    "global_load_lds_dwordx4 v121, s[64:65] offset:0 sc1\n"
    "v_mfma_f32_16x16x32_bf16 v[148:151], a[116:119], v[204:207], v[148:151]\n"
    "s_cmp_lg_u32 s56, 0\n"
    "s_cbranch_scc1 G_f2%=\n"
    "s_or_b32 s58, s42, s51\n"
    "s_mov_b64 exec, 1\n"
    "v_mov_b32 v116, s58\n"
    "global_store_dwordx2 v118, v[116:117], s[54:55] sc1\n"
    "s_mov_b64 exec, -1\n"
    "G_f2%=:\n"
    "v_mfma_f32_16x16x32_bf16 v[152:155], a[180:183], v[204:207], v[152:155]\n"
    "v_mfma_f32_16x16x32_bf16 v[156:159], a[244:247], v[204:207], v[156:159]\n"
    "s_waitcnt lgkmcnt(4)\n"
    "v_mfma_f32_16x16x32_bf16 v[128:131], a[56:59], v[208:211], v[128:131]\n"
    "ds_read_b128 v[200:203], v123 offset:1024\n"
    "v_mfma_f32_16x16x32_bf16 v[132:135], a[120:123], v[208:211], v[132:135]\n"
    "ds_read_b128 v[204:207], v123 offset:17408\n"
    "v_mfma_f32_16x16x32_bf16 v[136:139], a[184:187], v[208:211], v[136:139]\n"
    "v_mfma_f32_16x16x32_bf16 v[140:143], a[248:251], v[208:211], v[140:143]\n"
    "v_mfma_f32_16x16x32_bf16 v[144:147], a[56:59], v[212:215], v[144:147]\n"
    "global_load_lds_dwordx4 v121, s[64:65] offset:1024 sc1\n"
    "v_mfma_f32_16x16x32_bf16 v[148:151], a[120:123], v[212:215], v[148:151]\n"
    "v_mfma_f32_16x16x32_bf16 v[152:155], a[184:187], v[212:215], v[152:155]\n"
    "v_mfma_f32_16x16x32_bf16 v[156:159], a[248:251], v[212:215], v[156:159]\n"
    "s_add_u32 s51, s51, 1\n"
    "s_waitcnt lgkmcnt(4)\n"
    "v_mfma_f32_16x16x32_bf16 v[128:131], a[60:63], v[216:219], v[128:131]\n"
    "ds_read_b128 v[208:211], v123 offset:2048\n"
    "v_mfma_f32_16x16x32_bf16 v[132:135], a[124:127], v[216:219], v[132:135]\n"
    "ds_read_b128 v[212:215], v123 offset:18432\n"
    "v_mfma_f32_16x16x32_bf16 v[136:139], a[188:191], v[216:219], v[136:139]\n"
    "v_mfma_f32_16x16x32_bf16 v[140:143], a[252:255], v[216:219], v[140:143]\n"
    "v_mfma_f32_16x16x32_bf16 v[144:147], a[60:63], v[220:223], v[144:147]\n"
    "global_load_lds_dwordx4 v121, s[64:65] offset:2048 sc1\n"
    "v_mfma_f32_16x16x32_bf16 v[148:151], a[124:127], v[220:223], v[148:151]\n"
    "v_mfma_f32_16x16x32_bf16 v[152:155], a[188:191], v[220:223], v[152:155]\n"
    "v_mfma_f32_16x16x32_bf16 v[156:159], a[252:255], v[220:223], v[156:159]\n"
    "s_add_u32 s58, s51, -3\n"
    "s_cmp_gt_u32 s58, 28\n"
    "s_waitcnt lgkmcnt(4)\n"
    "s_cbranch_scc0 G_loop%=\n"
    "s_branch G_pc%=\n"
    "G_pw%=:\n"
    "s_add_u32 s58, s40, 0x312000\n"
    "s_addc_u32 s59, s41, 0\n"
    "global_load_dwordx2 v[114:115], v112, s[58:59] sc1\n"
    "s_waitcnt vmcnt(0)\n"
    "G_pc%=:\n"
    "v_xor_b32 v114, s42, v114\n"
    "v_xor_b32 v115, s43, v115\n"
    "v_add_u32 v114, -1, v114\n"
    "v_cmp_le_u64 s[58:59], 8, v[114:115]\n"
    "v_and_b32 v114, 2, v114\n"
    "v_cmp_ne_u32 s[52:53], 0, v114\n"
    "s_cmp_eq_u64 s[58:59], 0\n"
    "s_cbranch_scc1 G_pok%=\n"
    "s_sleep 4\n"
    "s_add_u32 s57, s57, 1\n"
    "s_cmp_lt_u32 s57, 0x40000\n"
    "s_cbranch_scc1 G_pw%=\n"
    "G_pok%=:\n"
    "s_cmp_lg_u32 s52, 0\n"
    "s_cselect_b32 s52, 0, 0x2000\n"
    "s_xor_b32 s44, s44, s52\n"
    "s_xor_b32 s66, s66, s52\n"
    "s_nop 7\n"
    "v_cvt_pk_bf16_f32 v224, v128, v129\n"
    "v_cvt_pk_bf16_f32 v225, v130, v131\n"
    "v_cvt_pk_bf16_f32 v226, v132, v133\n"
    "v_cvt_pk_bf16_f32 v227, v134, v135\n"
    "v_cvt_pk_bf16_f32 v228, v144, v145\n"
    "v_cvt_pk_bf16_f32 v229, v146, v147\n"
    "v_cvt_pk_bf16_f32 v230, v148, v149\n"
    "v_cvt_pk_bf16_f32 v231, v150, v151\n"
    "v_cvt_pk_bf16_f32 v232, v136, v137\n"
    "v_cvt_pk_bf16_f32 v233, v138, v139\n"
    "v_cvt_pk_bf16_f32 v234, v140, v141\n"
    "v_cvt_pk_bf16_f32 v235, v142, v143\n"
    "v_cvt_pk_bf16_f32 v236, v152, v153\n"
    "v_cvt_pk_bf16_f32 v237, v154, v155\n"
    "v_cvt_pk_bf16_f32 v238, v156, v157\n"
    "v_cvt_pk_bf16_f32 v239, v158, v159\n"
    "s_not_b64 vcc, vcc\n"
    "v_cndmask_b32_dpp v240, v232, v224, vcc quad_perm:[0,1,0,1] row_mask:0xf bank_mask:0xf\n"
    "v_cndmask_b32_dpp v241, v233, v225, vcc quad_perm:[0,1,0,1] row_mask:0xf bank_mask:0xf\n"
    "v_cndmask_b32_dpp v242, v234, v226, vcc quad_perm:[0,1,0,1] row_mask:0xf bank_mask:0xf\n"
    "v_cndmask_b32_dpp v243, v235, v227, vcc quad_perm:[0,1,0,1] row_mask:0xf bank_mask:0xf\n"
    "v_cndmask_b32_dpp v248, v236, v228, vcc quad_perm:[0,1,0,1] row_mask:0xf bank_mask:0xf\n"
    "v_cndmask_b32_dpp v249, v237, v229, vcc quad_perm:[0,1,0,1] row_mask:0xf bank_mask:0xf\n"
    "v_cndmask_b32_dpp v250, v238, v230, vcc quad_perm:[0,1,0,1] row_mask:0xf bank_mask:0xf\n"
    "v_cndmask_b32_dpp v251, v239, v231, vcc quad_perm:[0,1,0,1] row_mask:0xf bank_mask:0xf\n"
    "s_not_b64 vcc, vcc\n"
    "v_cndmask_b32_dpp v244, v224, v232, vcc quad_perm:[2,3,2,3] row_mask:0xf bank_mask:0xf\n"
    "v_cndmask_b32_dpp v245, v225, v233, vcc quad_perm:[2,3,2,3] row_mask:0xf bank_mask:0xf\n"
    "v_cndmask_b32_dpp v246, v226, v234, vcc quad_perm:[2,3,2,3] row_mask:0xf bank_mask:0xf\n"
    "v_cndmask_b32_dpp v247, v227, v235, vcc quad_perm:[2,3,2,3] row_mask:0xf bank_mask:0xf\n"
    "v_cndmask_b32_dpp v252, v228, v236, vcc quad_perm:[2,3,2,3] row_mask:0xf bank_mask:0xf\n"
    "v_cndmask_b32_dpp v253, v229, v237, vcc quad_perm:[2,3,2,3] row_mask:0xf bank_mask:0xf\n"
    "v_cndmask_b32_dpp v254, v230, v238, vcc quad_perm:[2,3,2,3] row_mask:0xf bank_mask:0xf\n"
    "v_cndmask_b32_dpp v255, v231, v239, vcc quad_perm:[2,3,2,3] row_mask:0xf bank_mask:0xf\n"
    "global_store_dwordx4 v121, v[240:243], s[44:45] offset:0 sc1\n"
    "global_store_dwordx4 v121, v[248:251], s[44:45] offset:2048 sc1\n"
    "global_store_dwordx4 v121, v[244:247], s[44:45] offset:1024 sc1\n"
    "global_store_dwordx4 v121, v[252:255], s[44:45] offset:3072 sc1\n"
    "s_sub_u32 s44, s44, 0x300000\n"
    "s_subb_u32 s45, s45, 0\n"
    "s_waitcnt vmcnt(0)\n"
    "s_barrier\n"
    "s_cmp_lg_u32 s56, 0\n"
    "s_cbranch_scc1 G_f3%=\n"
    "s_or_b32 s58, s42, s51\n"
    "s_mov_b64 exec, 1\n"
    "v_mov_b32 v116, s58\n"
    "global_store_dwordx2 v118, v[116:117], s[54:55] sc1\n"
    "s_mov_b64 exec, -1\n"
    "G_f3%=:\n"
    "s_branch G_loop%=\n"
    "G_t1%=:\n"
    "s_nop 7\n"
    "v_cvt_pk_bf16_f32 v224, v160, v161\n"
    "v_cvt_pk_bf16_f32 v225, v162, v163\n"
    "v_cvt_pk_bf16_f32 v226, v164, v165\n"
    "v_cvt_pk_bf16_f32 v227, v166, v167\n"
    "v_cvt_pk_bf16_f32 v228, v176, v177\n"
    "v_cvt_pk_bf16_f32 v229, v178, v179\n"
    "v_cvt_pk_bf16_f32 v230, v180, v181\n"
    "v_cvt_pk_bf16_f32 v231, v182, v183\n"
    "v_cvt_pk_bf16_f32 v232, v168, v169\n"
    "v_cvt_pk_bf16_f32 v233, v170, v171\n"
    "v_cvt_pk_bf16_f32 v234, v172, v173\n"
    "v_cvt_pk_bf16_f32 v235, v174, v175\n"
    "v_cvt_pk_bf16_f32 v236, v184, v185\n"
    "v_cvt_pk_bf16_f32 v237, v186, v187\n"
    "v_cvt_pk_bf16_f32 v238, v188, v189\n"
    "v_cvt_pk_bf16_f32 v239, v190, v191\n"
    "s_not_b64 vcc, vcc\n"
    "v_cndmask_b32_dpp v240, v232, v224, vcc quad_perm:[0,1,0,1] row_mask:0xf bank_mask:0xf\n"
    "v_cndmask_b32_dpp v241, v233, v225, vcc quad_perm:[0,1,0,1] row_mask:0xf bank_mask:0xf\n"
    "v_cndmask_b32_dpp v242, v234, v226, vcc quad_perm:[0,1,0,1] row_mask:0xf bank_mask:0xf\n"
    "v_cndmask_b32_dpp v243, v235, v227, vcc quad_perm:[0,1,0,1] row_mask:0xf bank_mask:0xf\n"
    "v_cndmask_b32_dpp v248, v236, v228, vcc quad_perm:[0,1,0,1] row_mask:0xf bank_mask:0xf\n"
    "v_cndmask_b32_dpp v249, v237, v229, vcc quad_perm:[0,1,0,1] row_mask:0xf bank_mask:0xf\n"
    "v_cndmask_b32_dpp v250, v238, v230, vcc quad_perm:[0,1,0,1] row_mask:0xf bank_mask:0xf\n"
    "v_cndmask_b32_dpp v251, v239, v231, vcc quad_perm:[0,1,0,1] row_mask:0xf bank_mask:0xf\n"
    "s_not_b64 vcc, vcc\n"
    "v_cndmask_b32_dpp v244, v224, v232, vcc quad_perm:[2,3,2,3] row_mask:0xf bank_mask:0xf\n"
    "v_cndmask_b32_dpp v245, v225, v233, vcc quad_perm:[2,3,2,3] row_mask:0xf bank_mask:0xf\n"
    "v_cndmask_b32_dpp v246, v226, v234, vcc quad_perm:[2,3,2,3] row_mask:0xf bank_mask:0xf\n"
    "v_cndmask_b32_dpp v247, v227, v235, vcc quad_perm:[2,3,2,3] row_mask:0xf bank_mask:0xf\n"
    "v_cndmask_b32_dpp v252, v228, v236, vcc quad_perm:[2,3,2,3] row_mask:0xf bank_mask:0xf\n"
    "v_cndmask_b32_dpp v253, v229, v237, vcc quad_perm:[2,3,2,3] row_mask:0xf bank_mask:0xf\n"
    "v_cndmask_b32_dpp v254, v230, v238, vcc quad_perm:[2,3,2,3] row_mask:0xf bank_mask:0xf\n"
    "v_cndmask_b32_dpp v255, v231, v239, vcc quad_perm:[2,3,2,3] row_mask:0xf bank_mask:0xf\n"
    "global_store_dwordx4 v121, v[240:243], s[44:45] offset:0 sc1\n"
    "global_store_dwordx4 v121, v[248:251], s[44:45] offset:2048 sc1\n"
    "global_store_dwordx4 v121, v[244:247], s[44:45] offset:1024 sc1\n"
    "global_store_dwordx4 v121, v[252:255], s[44:45] offset:3072 sc1\n"
    "s_sub_u32 s44, s44, 0x300000\n"
    "s_subb_u32 s45, s45, 0\n"
    "s_waitcnt vmcnt(0)\n"
    "s_barrier\n"
    "s_cmp_lg_u32 s56, 0\n"
    "s_cbranch_scc1 G_f4%=\n"
    "s_or_b32 s58, s42, s51\n"
    "s_mov_b64 exec, 1\n"
    "v_mov_b32 v116, s58\n"
    "global_store_dwordx2 v118, v[116:117], s[54:55] sc1\n"
    "s_mov_b64 exec, -1\n"
    "G_f4%=:\n"
    "s_cmp_eq_u32 s51, 32\n"
    "s_cbranch_scc1 G_end%=\n"
    "s_add_u32 s58, s40, 0x20e000\n"
    "s_addc_u32 s59, s41, 0\n"
    "s_add_u32 s60, s40, 0x200200\n"
    "s_addc_u32 s61, s41, 0\n"
    "s_or_b32 s62, s42, 1\n"
    "s_mov_b32 s63, s43\n"
    "v_lshlrev_b32 v224, 3, v121\n"
    "G_xw1%=:\n"
    "global_load_dwordx2 v[226:227], v224, s[58:59] sc1\n"
    "G_xc1%=:\n"
    "s_waitcnt vmcnt(0)\n"
    "v_cmp_ne_u64 vcc, s[62:63], v[226:227]\n"
    "s_nop 0\n"
    "s_cbranch_vccz G_xok1%=\n"
    "s_sleep 4\n"
    "s_add_u32 s57, s57, 1\n"
    "s_and_b32 s68, s57, 0xff\n"
    "s_cbranch_scc1 G_xw1%=\n"
    "global_load_dwordx2 v[226:227], v118, s[60:61] sc1\n"
    "s_waitcnt vmcnt(0)\n"
    "v_cmp_eq_u64 vcc, s[42:43], v[226:227]\n"
    "s_nop 0\n"
    "s_cbranch_vccnz G_xok1%=\n"
    "s_cmp_lt_u32 s57, 0x40000\n"
    "s_cbranch_scc1 G_xw1%=\n"
    "v_mov_b32 v116, s42\n"
    "global_store_dwordx2 v118, v[116:117], s[60:61] sc1\n"
    "G_xok1%=:\n"
    "s_mov_b32 vcc_lo, 0xcccccccc\n"
    "s_mov_b32 vcc_hi, 0xcccccccc\n"
    "s_branch G_even%=\n"
    "G_end%=:\n"
    "s_waitcnt lgkmcnt(0)\n"
    :: GIN : GCLOB);
  else asm volatile(
    "s_mov_b64 s[40:41], %0\n"
    "s_mov_b64 s[42:43], %1\n"
    "s_mov_b64 s[44:45], %2\n"
    "s_mov_b64 s[54:55], %3\n"
    "s_mov_b32 s56, %4\n"
    "v_mov_b32 v121, %5\n"
    "s_add_u32 s58, s40, 0x20e000\n"
    "s_addc_u32 s59, s41, 0\n"
    "v_lshlrev_b32 v225, 3, v121\n"
    "v_and_b32 v224, 0x380, v225\n"
    "global_load_dwordx2 v[226:227], v224, s[58:59] sc1\n"
    "v_xor_b32 v112, 0, %6\n"
    "v_xor_b32 v113, 32, %6\n"
    "v_xor_b32 v114, 64, %6\n"
    "v_xor_b32 v115, 96, %6\n"
    "ds_read_b64_tr_b16 a[0:1], v112 offset:0\n"
    "ds_read_b64_tr_b16 a[2:3], v112 offset:1024\n"
    "ds_read_b64_tr_b16 a[4:5], v112 offset:8192\n"
    "ds_read_b64_tr_b16 a[6:7], v112 offset:9216\n"
    "ds_read_b64_tr_b16 a[8:9], v112 offset:16384\n"
    "ds_read_b64_tr_b16 a[10:11], v112 offset:17408\n"
    "ds_read_b64_tr_b16 a[12:13], v112 offset:24576\n"
    "ds_read_b64_tr_b16 a[14:15], v112 offset:25600\n"
    "s_waitcnt lgkmcnt(6)\n"
    "ds_read_b64_tr_b16 a[16:17], v112 offset:32768\n"
    "ds_read_b64_tr_b16 a[18:19], v112 offset:33792\n"
    "ds_read_b64_tr_b16 a[20:21], v112 offset:40960\n"
    "ds_read_b64_tr_b16 a[22:23], v112 offset:41984\n"
    "ds_read_b64_tr_b16 a[24:25], v112 offset:49152\n"
    "ds_read_b64_tr_b16 a[26:27], v112 offset:50176\n"
    "ds_read_b64_tr_b16 a[28:29], v112 offset:57344\n"
    "ds_read_b64_tr_b16 a[30:31], v112 offset:58368\n"
    "s_waitcnt lgkmcnt(6)\n"
    "ds_read_b64_tr_b16 a[64:65], v113 offset:0\n"
    "ds_read_b64_tr_b16 a[66:67], v113 offset:1024\n"
    "ds_read_b64_tr_b16 a[68:69], v113 offset:8192\n"
    "ds_read_b64_tr_b16 a[70:71], v113 offset:9216\n"
    "ds_read_b64_tr_b16 a[72:73], v113 offset:16384\n"
    "ds_read_b64_tr_b16 a[74:75], v113 offset:17408\n"
    "ds_read_b64_tr_b16 a[76:77], v113 offset:24576\n"
    "ds_read_b64_tr_b16 a[78:79], v113 offset:25600\n"
    "s_waitcnt lgkmcnt(6)\n"
    "ds_read_b64_tr_b16 a[80:81], v113 offset:32768\n"
    "ds_read_b64_tr_b16 a[82:83], v113 offset:33792\n"
    "ds_read_b64_tr_b16 a[84:85], v113 offset:40960\n"
    "ds_read_b64_tr_b16 a[86:87], v113 offset:41984\n"
    "ds_read_b64_tr_b16 a[88:89], v113 offset:49152\n"
    "ds_read_b64_tr_b16 a[90:91], v113 offset:50176\n"
    "ds_read_b64_tr_b16 a[92:93], v113 offset:57344\n"
    "ds_read_b64_tr_b16 a[94:95], v113 offset:58368\n"
    "s_waitcnt lgkmcnt(6)\n"
    "ds_read_b64_tr_b16 a[128:129], v114 offset:0\n"
    "ds_read_b64_tr_b16 a[130:131], v114 offset:1024\n"
    "ds_read_b64_tr_b16 a[132:133], v114 offset:8192\n"
    "ds_read_b64_tr_b16 a[134:135], v114 offset:9216\n"
    "ds_read_b64_tr_b16 a[136:137], v114 offset:16384\n"
    "ds_read_b64_tr_b16 a[138:139], v114 offset:17408\n"
    "ds_read_b64_tr_b16 a[140:141], v114 offset:24576\n"
    "ds_read_b64_tr_b16 a[142:143], v114 offset:25600\n"
    "s_waitcnt lgkmcnt(6)\n"
    "ds_read_b64_tr_b16 a[144:145], v114 offset:32768\n"
    "ds_read_b64_tr_b16 a[146:147], v114 offset:33792\n"
    "ds_read_b64_tr_b16 a[148:149], v114 offset:40960\n"
    "ds_read_b64_tr_b16 a[150:151], v114 offset:41984\n"
    "ds_read_b64_tr_b16 a[152:153], v114 offset:49152\n"
    "ds_read_b64_tr_b16 a[154:155], v114 offset:50176\n"
    "ds_read_b64_tr_b16 a[156:157], v114 offset:57344\n"
    "ds_read_b64_tr_b16 a[158:159], v114 offset:58368\n"
    "s_waitcnt lgkmcnt(6)\n"
    "ds_read_b64_tr_b16 a[192:193], v115 offset:0\n"
    "ds_read_b64_tr_b16 a[194:195], v115 offset:1024\n"
    "ds_read_b64_tr_b16 a[196:197], v115 offset:8192\n"
    "ds_read_b64_tr_b16 a[198:199], v115 offset:9216\n"
    "ds_read_b64_tr_b16 a[200:201], v115 offset:16384\n"
    "ds_read_b64_tr_b16 a[202:203], v115 offset:17408\n"
    "ds_read_b64_tr_b16 a[204:205], v115 offset:24576\n"
    "ds_read_b64_tr_b16 a[206:207], v115 offset:25600\n"
    "s_waitcnt lgkmcnt(6)\n"
    "ds_read_b64_tr_b16 a[208:209], v115 offset:32768\n"
    "ds_read_b64_tr_b16 a[210:211], v115 offset:33792\n"
    "ds_read_b64_tr_b16 a[212:213], v115 offset:40960\n"
    "ds_read_b64_tr_b16 a[214:215], v115 offset:41984\n"
    "ds_read_b64_tr_b16 a[216:217], v115 offset:49152\n"
    "ds_read_b64_tr_b16 a[218:219], v115 offset:50176\n"
    "ds_read_b64_tr_b16 a[220:221], v115 offset:57344\n"
    "ds_read_b64_tr_b16 a[222:223], v115 offset:58368\n"
    "s_waitcnt lgkmcnt(6)\n"
    "v_xor_b32 v112, 0, %6\n"
    "v_add_u32 v112, 0x10000, v112\n"
    "v_xor_b32 v113, 32, %6\n"
    "v_add_u32 v113, 0x10000, v113\n"
    "v_xor_b32 v114, 64, %6\n"
    "v_add_u32 v114, 0x10000, v114\n"
    "v_xor_b32 v115, 96, %6\n"
    "v_add_u32 v115, 0x10000, v115\n"
    "ds_read_b64_tr_b16 a[32:33], v112 offset:0\n"
    "ds_read_b64_tr_b16 a[34:35], v112 offset:1024\n"
    "ds_read_b64_tr_b16 a[36:37], v112 offset:8192\n"
    "ds_read_b64_tr_b16 a[38:39], v112 offset:9216\n"
    "ds_read_b64_tr_b16 a[40:41], v112 offset:16384\n"
    "ds_read_b64_tr_b16 a[42:43], v112 offset:17408\n"
    "ds_read_b64_tr_b16 a[44:45], v112 offset:24576\n"
    "ds_read_b64_tr_b16 a[46:47], v112 offset:25600\n"
    "s_waitcnt lgkmcnt(6)\n"
    "ds_read_b64_tr_b16 a[48:49], v112 offset:32768\n"
    "ds_read_b64_tr_b16 a[50:51], v112 offset:33792\n"
    "ds_read_b64_tr_b16 a[52:53], v112 offset:40960\n"
    "ds_read_b64_tr_b16 a[54:55], v112 offset:41984\n"
    "ds_read_b64_tr_b16 a[56:57], v112 offset:49152\n"
    "ds_read_b64_tr_b16 a[58:59], v112 offset:50176\n"
    "ds_read_b64_tr_b16 a[60:61], v112 offset:57344\n"
    "ds_read_b64_tr_b16 a[62:63], v112 offset:58368\n"
    "s_waitcnt lgkmcnt(6)\n"
    "ds_read_b64_tr_b16 a[96:97], v113 offset:0\n"
    "ds_read_b64_tr_b16 a[98:99], v113 offset:1024\n"
    "ds_read_b64_tr_b16 a[100:101], v113 offset:8192\n"
    "ds_read_b64_tr_b16 a[102:103], v113 offset:9216\n"
    "ds_read_b64_tr_b16 a[104:105], v113 offset:16384\n"
    "ds_read_b64_tr_b16 a[106:107], v113 offset:17408\n"
    "ds_read_b64_tr_b16 a[108:109], v113 offset:24576\n"
    "ds_read_b64_tr_b16 a[110:111], v113 offset:25600\n"
    "s_waitcnt lgkmcnt(6)\n"
    "ds_read_b64_tr_b16 a[112:113], v113 offset:32768\n"
    "ds_read_b64_tr_b16 a[114:115], v113 offset:33792\n"
    "ds_read_b64_tr_b16 a[116:117], v113 offset:40960\n"
    "ds_read_b64_tr_b16 a[118:119], v113 offset:41984\n"
    "ds_read_b64_tr_b16 a[120:121], v113 offset:49152\n"
    "ds_read_b64_tr_b16 a[122:123], v113 offset:50176\n"
    "ds_read_b64_tr_b16 a[124:125], v113 offset:57344\n"
    "ds_read_b64_tr_b16 a[126:127], v113 offset:58368\n"
    "s_waitcnt lgkmcnt(6)\n"
    "ds_read_b64_tr_b16 a[160:161], v114 offset:0\n"
    "ds_read_b64_tr_b16 a[162:163], v114 offset:1024\n"
    "ds_read_b64_tr_b16 a[164:165], v114 offset:8192\n"
    "ds_read_b64_tr_b16 a[166:167], v114 offset:9216\n"
    "ds_read_b64_tr_b16 a[168:169], v114 offset:16384\n"
    "ds_read_b64_tr_b16 a[170:171], v114 offset:17408\n"
    "ds_read_b64_tr_b16 a[172:173], v114 offset:24576\n"
    "ds_read_b64_tr_b16 a[174:175], v114 offset:25600\n"
    "s_waitcnt lgkmcnt(6)\n"
    "ds_read_b64_tr_b16 a[176:177], v114 offset:32768\n"
    "ds_read_b64_tr_b16 a[178:179], v114 offset:33792\n"
    "ds_read_b64_tr_b16 a[180:181], v114 offset:40960\n"
    "ds_read_b64_tr_b16 a[182:183], v114 offset:41984\n"
    "ds_read_b64_tr_b16 a[184:185], v114 offset:49152\n"
    "ds_read_b64_tr_b16 a[186:187], v114 offset:50176\n"
    "ds_read_b64_tr_b16 a[188:189], v114 offset:57344\n"
    "ds_read_b64_tr_b16 a[190:191], v114 offset:58368\n"
    "s_waitcnt lgkmcnt(6)\n"
    "ds_read_b64_tr_b16 a[224:225], v115 offset:0\n"
    "ds_read_b64_tr_b16 a[226:227], v115 offset:1024\n"
    "ds_read_b64_tr_b16 a[228:229], v115 offset:8192\n"
    "ds_read_b64_tr_b16 a[230:231], v115 offset:9216\n"
    "ds_read_b64_tr_b16 a[232:233], v115 offset:16384\n"
    "ds_read_b64_tr_b16 a[234:235], v115 offset:17408\n"
    "ds_read_b64_tr_b16 a[236:237], v115 offset:24576\n"
    "ds_read_b64_tr_b16 a[238:239], v115 offset:25600\n"
    "s_waitcnt lgkmcnt(6)\n"
    "ds_read_b64_tr_b16 a[240:241], v115 offset:32768\n"
    "ds_read_b64_tr_b16 a[242:243], v115 offset:33792\n"
    "ds_read_b64_tr_b16 a[244:245], v115 offset:40960\n"
    "ds_read_b64_tr_b16 a[246:247], v115 offset:41984\n"
    "ds_read_b64_tr_b16 a[248:249], v115 offset:49152\n"
    "ds_read_b64_tr_b16 a[250:251], v115 offset:50176\n"
    "ds_read_b64_tr_b16 a[252:253], v115 offset:57344\n"
    "ds_read_b64_tr_b16 a[254:255], v115 offset:58368\n"
    "s_waitcnt lgkmcnt(6)\n"
    "s_lshr_b32 s58, s56, 1\n"
    "s_lshl_b32 s58, s58, 15\n"
    "v_add_u32 v122, s58, v121\n"
    "v_add_u32 v123, 0x10000, v122\n"
    "s_lshl_b32 s50, s56, 14\n"
    "v_mov_b32 v118, 0\n"
    "v_mov_b32 v117, s43\n"
    "s_add_u32 s58, s50, 0x400000\n"
    "s_add_u32 s48, s40, s58\n"
    "s_addc_u32 s49, s41, 0\n"
    "s_mov_b32 s57, 0\n"
    "s_mov_b32 s51, 0\n"
    "s_waitcnt lgkmcnt(0)\n"
    "s_barrier\n"
    "s_add_u32 s58, s40, 0x20e000\n"
    "s_addc_u32 s59, s41, 0\n"
    "s_add_u32 s60, s40, 0x200200\n"
    "s_addc_u32 s61, s41, 0\n"
    "s_or_b32 s62, s42, 1\n"
    "s_mov_b32 s63, s43\n"
    "s_branch G_xc0%=\n"
    "G_xw0%=:\n"
    "global_load_dwordx2 v[226:227], v224, s[58:59] sc1\n"
    "G_xc0%=:\n"
    "s_waitcnt vmcnt(0)\n"
    "v_cmp_ne_u64 vcc, s[62:63], v[226:227]\n"
    "s_nop 0\n"
    "s_cbranch_vccz G_xok0%=\n"
    "s_sleep 4\n"
    "s_add_u32 s57, s57, 1\n"
    "s_and_b32 s68, s57, 0xff\n"
    "s_cbranch_scc1 G_xw0%=\n"
    "global_load_dwordx2 v[226:227], v118, s[60:61] sc1\n"
    "s_waitcnt vmcnt(0)\n"
    "v_cmp_eq_u64 vcc, s[42:43], v[226:227]\n"
    "s_nop 0\n"
    "s_cbranch_vccnz G_xok0%=\n"
    "s_cmp_lt_u32 s57, 0x40000\n"
    "s_cbranch_scc1 G_xw0%=\n"
    "v_mov_b32 v116, s42\n"
    "global_store_dwordx2 v118, v[116:117], s[60:61] sc1\n"
    "G_xok0%=:\n"
    "s_mov_b32 vcc_lo, 0xcccccccc\n"
    "s_mov_b32 vcc_hi, 0xcccccccc\n"
    "s_add_u32 m0, s50, 0\n"
    "s_add_u32 s64, s48, 0\n"
    "s_addc_u32 s65, s49, 0\n"
    "global_load_lds_dwordx4 v121, s[64:65] offset:0 sc1\n"
    "global_load_lds_dwordx4 v121, s[64:65] offset:1024 sc1\n"
    "global_load_lds_dwordx4 v121, s[64:65] offset:2048 sc1\n"
    "global_load_lds_dwordx4 v121, s[64:65] offset:3072 sc1\n"
    "s_add_u32 m0, s50, 4096\n"
    "s_add_u32 s64, s48, 4096\n"
    "s_addc_u32 s65, s49, 0\n"
    "global_load_lds_dwordx4 v121, s[64:65] offset:0 sc1\n"
    "global_load_lds_dwordx4 v121, s[64:65] offset:1024 sc1\n"
    "global_load_lds_dwordx4 v121, s[64:65] offset:2048 sc1\n"
    "global_load_lds_dwordx4 v121, s[64:65] offset:3072 sc1\n"
    "s_add_u32 m0, s50, 8192\n"
    "s_add_u32 s64, s48, 8192\n"
    "s_addc_u32 s65, s49, 0\n"
    "global_load_lds_dwordx4 v121, s[64:65] offset:0 sc1\n"
    "global_load_lds_dwordx4 v121, s[64:65] offset:1024 sc1\n"
    "global_load_lds_dwordx4 v121, s[64:65] offset:2048 sc1\n"
    "global_load_lds_dwordx4 v121, s[64:65] offset:3072 sc1\n"
    "s_add_u32 m0, s50, 12288\n"
    "s_add_u32 s64, s48, 12288\n"
    "s_addc_u32 s65, s49, 0\n"
    "s_add_u32 s48, s48, 0x10000\n"
    "s_addc_u32 s49, s49, 0\n"
    "global_load_lds_dwordx4 v121, s[64:65] offset:0 sc1\n"
    "global_load_lds_dwordx4 v121, s[64:65] offset:1024 sc1\n"
    "global_load_lds_dwordx4 v121, s[64:65] offset:2048 sc1\n"
    "global_load_lds_dwordx4 v121, s[64:65] offset:3072 sc1\n"
    "s_add_u32 m0, s50, 65536\n"
    "s_add_u32 s64, s48, 0\n"
    "s_addc_u32 s65, s49, 0\n"
    "global_load_lds_dwordx4 v121, s[64:65] offset:0 sc1\n"
    "global_load_lds_dwordx4 v121, s[64:65] offset:1024 sc1\n"
    "global_load_lds_dwordx4 v121, s[64:65] offset:2048 sc1\n"
    "global_load_lds_dwordx4 v121, s[64:65] offset:3072 sc1\n"
    "s_add_u32 m0, s50, 69632\n"
    "s_add_u32 s64, s48, 4096\n"
    "s_addc_u32 s65, s49, 0\n"
    "global_load_lds_dwordx4 v121, s[64:65] offset:0 sc1\n"
    "global_load_lds_dwordx4 v121, s[64:65] offset:1024 sc1\n"
    "global_load_lds_dwordx4 v121, s[64:65] offset:2048 sc1\n"
    "global_load_lds_dwordx4 v121, s[64:65] offset:3072 sc1\n"
    "s_add_u32 m0, s50, 73728\n"
    "s_add_u32 s64, s48, 8192\n"
    "s_addc_u32 s65, s49, 0\n"
    "global_load_lds_dwordx4 v121, s[64:65] offset:0 sc1\n"
    "global_load_lds_dwordx4 v121, s[64:65] offset:1024 sc1\n"
    "global_load_lds_dwordx4 v121, s[64:65] offset:2048 sc1\n"
    "s_mov_b64 s[66:67], s[44:45]\n"
    "s_sub_u32 s44, s44, 0x2200000\n"
    "s_subb_u32 s45, s45, 0\n"
    "s_waitcnt vmcnt(19) lgkmcnt(0)\n"
    "s_barrier\n"
    "s_bfe_u32 s58, s54, 0x2000a\n"
    "s_lshl_b32 s58, s58, 7\n"
    "s_add_u32 s58, s58, 512\n"
    "v_mov_b32 v112, s58\n"
    "s_add_u32 s58, s40, 0x312000\n"
    "s_addc_u32 s59, s41, 0\n"
    "global_load_dwordx2 v[114:115], v112, s[58:59] sc1\n"
    "ds_read_b128 v[192:195], v122 offset:0\n"
    "ds_read_b128 v[196:199], v122 offset:16384\n"
    "ds_read_b128 v[200:203], v122 offset:1024\n"
    "ds_read_b128 v[204:207], v122 offset:17408\n"
    "ds_read_b128 v[208:211], v122 offset:2048\n"
    "ds_read_b128 v[212:215], v122 offset:18432\n"
    "s_waitcnt lgkmcnt(4)\n"
    "s_branch G_even%=\n"
    ".p2align 6\n"
    "G_loop%=:\n"
    "v_mfma_f32_16x16x32_bf16 v[160:163], v[192:195], a[0:3], 0\n"
    "ds_read_b128 v[216:219], v123 offset:3072\n"
    "v_mfma_f32_16x16x32_bf16 v[164:167], v[192:195], a[64:67], 0\n"
    "ds_read_b128 v[220:223], v123 offset:19456\n"
    "v_mfma_f32_16x16x32_bf16 v[168:171], v[192:195], a[128:131], 0\n"
    "v_mfma_f32_16x16x32_bf16 v[172:175], v[192:195], a[192:195], 0\n"
    "v_cvt_pk_bf16_f32 v224, v128, v129\n"
    "v_cvt_pk_bf16_f32 v225, v130, v131\n"
    "v_mfma_f32_16x16x32_bf16 v[176:179], v[196:199], a[0:3], 0\n"
    "global_load_lds_dwordx4 v121, s[64:65] offset:3072 sc1\n"
    "v_mfma_f32_16x16x32_bf16 v[180:183], v[196:199], a[64:67], 0\n"
    "v_cvt_pk_bf16_f32 v226, v144, v145\n"
    "v_cvt_pk_bf16_f32 v227, v146, v147\n"
    "v_mfma_f32_16x16x32_bf16 v[184:187], v[196:199], a[128:131], 0\n"
    "v_cvt_pk_bf16_f32 v228, v132, v133\n"
    "v_cvt_pk_bf16_f32 v229, v134, v135\n"
    "v_mfma_f32_16x16x32_bf16 v[188:191], v[196:199], a[192:195], 0\n"
    "v_cvt_pk_bf16_f32 v230, v148, v149\n"
    "v_cvt_pk_bf16_f32 v231, v150, v151\n"
    "s_waitcnt lgkmcnt(4)\n"
    "v_mfma_f32_16x16x32_bf16 v[160:163], v[200:203], a[4:7], v[160:163]\n"
    "ds_read_b128 v[192:195], v123 offset:4096\n"
    "v_mfma_f32_16x16x32_bf16 v[164:167], v[200:203], a[68:71], v[164:167]\n"
    "ds_read_b128 v[196:199], v123 offset:20480\n"
    "v_mfma_f32_16x16x32_bf16 v[168:171], v[200:203], a[132:135], v[168:171]\n"
    "s_add_u32 m0, s50, 12288\n"
    "s_add_u32 s64, s48, 12288\n"
    "s_addc_u32 s65, s49, 0\n"
    "v_cvt_pk_bf16_f32 v232, v136, v137\n"
    "v_cvt_pk_bf16_f32 v233, v138, v139\n"
    "v_mfma_f32_16x16x32_bf16 v[172:175], v[200:203], a[196:199], v[172:175]\n"
    "s_add_u32 s48, s48, 0x10000\n"
    "s_addc_u32 s49, s49, 0\n"
    "v_cvt_pk_bf16_f32 v234, v152, v153\n"
    "v_cvt_pk_bf16_f32 v235, v154, v155\n"
    "v_mfma_f32_16x16x32_bf16 v[176:179], v[204:207], a[4:7], v[176:179]\n"
    "global_load_lds_dwordx4 v121, s[64:65] offset:0 sc1\n"
    "v_mfma_f32_16x16x32_bf16 v[180:183], v[204:207], a[68:71], v[180:183]\n"
    "v_cvt_pk_bf16_f32 v236, v140, v141\n"
    "v_cvt_pk_bf16_f32 v237, v142, v143\n"
    "v_mfma_f32_16x16x32_bf16 v[184:187], v[204:207], a[132:135], v[184:187]\n"
    "global_store_dwordx4 v121, v[224:227], s[44:45] offset:0 sc1\n"
    "v_mfma_f32_16x16x32_bf16 v[188:191], v[204:207], a[196:199], v[188:191]\n"
    "v_cvt_pk_bf16_f32 v238, v156, v157\n"
    "v_cvt_pk_bf16_f32 v239, v158, v159\n"
    "s_waitcnt lgkmcnt(4)\n"
    "v_mfma_f32_16x16x32_bf16 v[160:163], v[208:211], a[8:11], v[160:163]\n"
    "ds_read_b128 v[200:203], v123 offset:5120\n"
    "v_mfma_f32_16x16x32_bf16 v[164:167], v[208:211], a[72:75], v[164:167]\n"
    "ds_read_b128 v[204:207], v123 offset:21504\n"
    "v_mfma_f32_16x16x32_bf16 v[168:171], v[208:211], a[136:139], v[168:171]\n"
    "v_mfma_f32_16x16x32_bf16 v[172:175], v[208:211], a[200:203], v[172:175]\n"
    "v_mfma_f32_16x16x32_bf16 v[176:179], v[212:215], a[8:11], v[176:179]\n"
    "global_load_lds_dwordx4 v121, s[64:65] offset:1024 sc1\n"
    "v_mfma_f32_16x16x32_bf16 v[180:183], v[212:215], a[72:75], v[180:183]\n"
    "v_mfma_f32_16x16x32_bf16 v[184:187], v[212:215], a[136:139], v[184:187]\n"
    "global_store_dwordx4 v121, v[228:231], s[44:45] offset:1024 sc1\n"
    "v_mfma_f32_16x16x32_bf16 v[188:191], v[212:215], a[200:203], v[188:191]\n"
    "s_waitcnt lgkmcnt(4)\n"
    "v_mfma_f32_16x16x32_bf16 v[160:163], v[216:219], a[12:15], v[160:163]\n"
    "ds_read_b128 v[208:211], v123 offset:6144\n"
    "v_mfma_f32_16x16x32_bf16 v[164:167], v[216:219], a[76:79], v[164:167]\n"
    "ds_read_b128 v[212:215], v123 offset:22528\n"
    "v_mfma_f32_16x16x32_bf16 v[168:171], v[216:219], a[140:143], v[168:171]\n"
    "v_mfma_f32_16x16x32_bf16 v[172:175], v[216:219], a[204:207], v[172:175]\n"
    "v_mfma_f32_16x16x32_bf16 v[176:179], v[220:223], a[12:15], v[176:179]\n"
    "global_load_lds_dwordx4 v121, s[64:65] offset:2048 sc1\n"
    "v_mfma_f32_16x16x32_bf16 v[180:183], v[220:223], a[76:79], v[180:183]\n"
    "v_mfma_f32_16x16x32_bf16 v[184:187], v[220:223], a[140:143], v[184:187]\n"
    "global_store_dwordx4 v121, v[232:235], s[44:45] offset:2048 sc1\n"
    "v_mfma_f32_16x16x32_bf16 v[188:191], v[220:223], a[204:207], v[188:191]\n"
    "s_waitcnt lgkmcnt(4)\n"
    "v_mfma_f32_16x16x32_bf16 v[160:163], v[192:195], a[16:19], v[160:163]\n"
    "ds_read_b128 v[216:219], v123 offset:7168\n"
    "v_mfma_f32_16x16x32_bf16 v[164:167], v[192:195], a[80:83], v[164:167]\n"
    "ds_read_b128 v[220:223], v123 offset:23552\n"
    "v_mfma_f32_16x16x32_bf16 v[168:171], v[192:195], a[144:147], v[168:171]\n"
    "v_mfma_f32_16x16x32_bf16 v[172:175], v[192:195], a[208:211], v[172:175]\n"
    "v_mfma_f32_16x16x32_bf16 v[176:179], v[196:199], a[16:19], v[176:179]\n"
    "global_load_lds_dwordx4 v121, s[64:65] offset:3072 sc1\n"
    "v_mfma_f32_16x16x32_bf16 v[180:183], v[196:199], a[80:83], v[180:183]\n"
    "v_mfma_f32_16x16x32_bf16 v[184:187], v[196:199], a[144:147], v[184:187]\n"
    "global_store_dwordx4 v121, v[236:239], s[44:45] offset:3072 sc1\n"
    "v_mfma_f32_16x16x32_bf16 v[188:191], v[196:199], a[208:211], v[188:191]\n"
    "s_waitcnt lgkmcnt(4)\n"
    "v_mfma_f32_16x16x32_bf16 v[160:163], v[200:203], a[20:23], v[160:163]\n"
    "s_waitcnt vmcnt(12) lgkmcnt(0)\n"
    "s_barrier\n"
    "v_mfma_f32_16x16x32_bf16 v[164:167], v[200:203], a[84:87], v[164:167]\n"
    "ds_read_b128 v[192:195], v123 offset:8192\n"
    "v_mfma_f32_16x16x32_bf16 v[168:171], v[200:203], a[148:151], v[168:171]\n"
    "ds_read_b128 v[196:199], v123 offset:24576\n"
    "s_add_u32 m0, s50, 65536\n"
    "s_add_u32 s64, s48, 0\n"
    "s_addc_u32 s65, s49, 0\n"
    "v_mfma_f32_16x16x32_bf16 v[172:175], v[200:203], a[212:215], v[172:175]\n"
    "v_mfma_f32_16x16x32_bf16 v[176:179], v[204:207], a[20:23], v[176:179]\n"
    "global_load_lds_dwordx4 v121, s[64:65] offset:0 sc1\n"
    "v_mfma_f32_16x16x32_bf16 v[180:183], v[204:207], a[84:87], v[180:183]\n"
    "v_mfma_f32_16x16x32_bf16 v[184:187], v[204:207], a[148:151], v[184:187]\n"
    "v_mfma_f32_16x16x32_bf16 v[188:191], v[204:207], a[212:215], v[188:191]\n"
    "s_waitcnt lgkmcnt(4)\n"
    "v_mfma_f32_16x16x32_bf16 v[160:163], v[208:211], a[24:27], v[160:163]\n"
    "ds_read_b128 v[200:203], v123 offset:9216\n"
    "v_mfma_f32_16x16x32_bf16 v[164:167], v[208:211], a[88:91], v[164:167]\n"
    "ds_read_b128 v[204:207], v123 offset:25600\n"
    "v_mfma_f32_16x16x32_bf16 v[168:171], v[208:211], a[152:155], v[168:171]\n"
    "v_mfma_f32_16x16x32_bf16 v[172:175], v[208:211], a[216:219], v[172:175]\n"
    "v_mfma_f32_16x16x32_bf16 v[176:179], v[212:215], a[24:27], v[176:179]\n"
    "global_load_lds_dwordx4 v121, s[64:65] offset:1024 sc1\n"
    "v_mfma_f32_16x16x32_bf16 v[180:183], v[212:215], a[88:91], v[180:183]\n"
    "v_mfma_f32_16x16x32_bf16 v[184:187], v[212:215], a[152:155], v[184:187]\n"
    "v_mfma_f32_16x16x32_bf16 v[188:191], v[212:215], a[216:219], v[188:191]\n"
    "s_waitcnt lgkmcnt(4)\n"
    "v_mfma_f32_16x16x32_bf16 v[160:163], v[216:219], a[28:31], v[160:163]\n"
    "ds_read_b128 v[208:211], v123 offset:10240\n"
    "v_mfma_f32_16x16x32_bf16 v[164:167], v[216:219], a[92:95], v[164:167]\n"
    "ds_read_b128 v[212:215], v123 offset:26624\n"
    "v_mfma_f32_16x16x32_bf16 v[168:171], v[216:219], a[156:159], v[168:171]\n"
    "v_mfma_f32_16x16x32_bf16 v[172:175], v[216:219], a[220:223], v[172:175]\n"
    "v_mfma_f32_16x16x32_bf16 v[176:179], v[220:223], a[28:31], v[176:179]\n"
    "global_load_lds_dwordx4 v121, s[64:65] offset:2048 sc1\n"
    "v_mfma_f32_16x16x32_bf16 v[180:183], v[220:223], a[92:95], v[180:183]\n"
    "s_mov_b64 s[44:45], s[66:67]\n"
    "s_add_u32 s66, s66, 0x100000\n"
    "s_addc_u32 s67, s67, 0\n"
    "v_mfma_f32_16x16x32_bf16 v[184:187], v[220:223], a[156:159], v[184:187]\n"
    "s_xor_b32 s66, s66, 0x2000\n"
    "v_mfma_f32_16x16x32_bf16 v[188:191], v[220:223], a[220:223], v[188:191]\n"
    "s_waitcnt lgkmcnt(4)\n"
    "v_mfma_f32_16x16x32_bf16 v[160:163], v[192:195], a[32:35], v[160:163]\n"
    "ds_read_b128 v[216:219], v123 offset:11264\n"
    "v_mfma_f32_16x16x32_bf16 v[164:167], v[192:195], a[96:99], v[164:167]\n"
    "ds_read_b128 v[220:223], v123 offset:27648\n"
    "v_mfma_f32_16x16x32_bf16 v[168:171], v[192:195], a[160:163], v[168:171]\n"
    "v_mfma_f32_16x16x32_bf16 v[172:175], v[192:195], a[224:227], v[172:175]\n"
    "v_mfma_f32_16x16x32_bf16 v[176:179], v[196:199], a[32:35], v[176:179]\n"
    "global_load_lds_dwordx4 v121, s[64:65] offset:3072 sc1\n"
    "v_mfma_f32_16x16x32_bf16 v[180:183], v[196:199], a[96:99], v[180:183]\n"
    "v_mfma_f32_16x16x32_bf16 v[184:187], v[196:199], a[160:163], v[184:187]\n"
    "v_mfma_f32_16x16x32_bf16 v[188:191], v[196:199], a[224:227], v[188:191]\n"
    "s_waitcnt lgkmcnt(4)\n"
    "v_mfma_f32_16x16x32_bf16 v[160:163], v[200:203], a[36:39], v[160:163]\n"
    "ds_read_b128 v[192:195], v123 offset:12288\n"
    "v_mfma_f32_16x16x32_bf16 v[164:167], v[200:203], a[100:103], v[164:167]\n"
    "ds_read_b128 v[196:199], v123 offset:28672\n"
    "v_mfma_f32_16x16x32_bf16 v[168:171], v[200:203], a[164:167], v[168:171]\n"
    "s_add_u32 m0, s50, 69632\n"
    "s_add_u32 s64, s48, 4096\n"
    "s_addc_u32 s65, s49, 0\n"
    "v_mfma_f32_16x16x32_bf16 v[172:175], v[200:203], a[228:231], v[172:175]\n"
    "v_mfma_f32_16x16x32_bf16 v[176:179], v[204:207], a[36:39], v[176:179]\n"
    "global_load_lds_dwordx4 v121, s[64:65] offset:0 sc1\n"
    "v_mfma_f32_16x16x32_bf16 v[180:183], v[204:207], a[100:103], v[180:183]\n"
    "v_mfma_f32_16x16x32_bf16 v[184:187], v[204:207], a[164:167], v[184:187]\n"
    "v_mfma_f32_16x16x32_bf16 v[188:191], v[204:207], a[228:231], v[188:191]\n"
    "s_waitcnt lgkmcnt(4)\n"
    "v_mfma_f32_16x16x32_bf16 v[160:163], v[208:211], a[40:43], v[160:163]\n"
    "ds_read_b128 v[200:203], v123 offset:13312\n"
    "v_mfma_f32_16x16x32_bf16 v[164:167], v[208:211], a[104:107], v[164:167]\n"
    "ds_read_b128 v[204:207], v123 offset:29696\n"
    "v_mfma_f32_16x16x32_bf16 v[168:171], v[208:211], a[168:171], v[168:171]\n"
    "v_mfma_f32_16x16x32_bf16 v[172:175], v[208:211], a[232:235], v[172:175]\n"
    "v_mfma_f32_16x16x32_bf16 v[176:179], v[212:215], a[40:43], v[176:179]\n"
    "global_load_lds_dwordx4 v121, s[64:65] offset:1024 sc1\n"
    "v_mfma_f32_16x16x32_bf16 v[180:183], v[212:215], a[104:107], v[180:183]\n"
    "v_mfma_f32_16x16x32_bf16 v[184:187], v[212:215], a[168:171], v[184:187]\n"
    "v_mfma_f32_16x16x32_bf16 v[188:191], v[212:215], a[232:235], v[188:191]\n"
    "s_waitcnt lgkmcnt(4)\n"
    "v_mfma_f32_16x16x32_bf16 v[160:163], v[216:219], a[44:47], v[160:163]\n"
    "ds_read_b128 v[208:211], v123 offset:14336\n"
    "v_mfma_f32_16x16x32_bf16 v[164:167], v[216:219], a[108:111], v[164:167]\n"
    "ds_read_b128 v[212:215], v123 offset:30720\n"
    "v_mfma_f32_16x16x32_bf16 v[168:171], v[216:219], a[172:175], v[168:171]\n"
    "v_mfma_f32_16x16x32_bf16 v[172:175], v[216:219], a[236:239], v[172:175]\n"
    "v_mfma_f32_16x16x32_bf16 v[176:179], v[220:223], a[44:47], v[176:179]\n"
    "global_load_lds_dwordx4 v121, s[64:65] offset:2048 sc1\n"
    "v_mfma_f32_16x16x32_bf16 v[180:183], v[220:223], a[108:111], v[180:183]\n"
    "v_mfma_f32_16x16x32_bf16 v[184:187], v[220:223], a[172:175], v[184:187]\n"
    "v_mfma_f32_16x16x32_bf16 v[188:191], v[220:223], a[236:239], v[188:191]\n"
    "s_waitcnt lgkmcnt(4)\n"
    "v_mfma_f32_16x16x32_bf16 v[160:163], v[192:195], a[48:51], v[160:163]\n"
    "ds_read_b128 v[216:219], v123 offset:15360\n"
    "v_mfma_f32_16x16x32_bf16 v[164:167], v[192:195], a[112:115], v[164:167]\n"
    "ds_read_b128 v[220:223], v123 offset:31744\n"
    "v_mfma_f32_16x16x32_bf16 v[168:171], v[192:195], a[176:179], v[168:171]\n"
    "v_mfma_f32_16x16x32_bf16 v[172:175], v[192:195], a[240:243], v[172:175]\n"
    "v_mfma_f32_16x16x32_bf16 v[176:179], v[196:199], a[48:51], v[176:179]\n"
    "global_load_lds_dwordx4 v121, s[64:65] offset:3072 sc1\n"
    "v_mfma_f32_16x16x32_bf16 v[180:183], v[196:199], a[112:115], v[180:183]\n"
    "v_mfma_f32_16x16x32_bf16 v[184:187], v[196:199], a[176:179], v[184:187]\n"
    "v_mfma_f32_16x16x32_bf16 v[188:191], v[196:199], a[240:243], v[188:191]\n"
    "s_waitcnt lgkmcnt(4)\n"
    "v_mfma_f32_16x16x32_bf16 v[160:163], v[200:203], a[52:55], v[160:163]\n"
    "s_waitcnt vmcnt(8) lgkmcnt(0)\n"
    "s_barrier\n"
    "v_mfma_f32_16x16x32_bf16 v[164:167], v[200:203], a[116:119], v[164:167]\n"
    "ds_read_b128 v[192:195], v122 offset:0\n"
    "v_mfma_f32_16x16x32_bf16 v[168:171], v[200:203], a[180:183], v[168:171]\n"
    "ds_read_b128 v[196:199], v122 offset:16384\n"
    "s_add_u32 m0, s50, 73728\n"
    "s_add_u32 s64, s48, 8192\n"
    "s_addc_u32 s65, s49, 0\n"
    "v_mfma_f32_16x16x32_bf16 v[172:175], v[200:203], a[244:247], v[172:175]\n"
    "v_mfma_f32_16x16x32_bf16 v[176:179], v[204:207], a[52:55], v[176:179]\n"
    "global_load_lds_dwordx4 v121, s[64:65] offset:0 sc1\n"
    "v_mfma_f32_16x16x32_bf16 v[180:183], v[204:207], a[116:119], v[180:183]\n"
    "s_cmp_lg_u32 s56, 0\n"
    "s_cbranch_scc1 G_f1%=\n"
    "s_or_b32 s58, s42, s51\n"
    "s_mov_b64 exec, 1\n"
    "v_mov_b32 v116, s58\n"
    "global_store_dwordx2 v118, v[116:117], s[54:55] sc1\n"
    "s_mov_b64 exec, -1\n"
    "G_f1%=:\n"
    "v_mfma_f32_16x16x32_bf16 v[184:187], v[204:207], a[180:183], v[184:187]\n"
    "v_mfma_f32_16x16x32_bf16 v[188:191], v[204:207], a[244:247], v[188:191]\n"
    "s_waitcnt lgkmcnt(4)\n"
    "v_mfma_f32_16x16x32_bf16 v[160:163], v[208:211], a[56:59], v[160:163]\n"
    "ds_read_b128 v[200:203], v122 offset:1024\n"
    "v_mfma_f32_16x16x32_bf16 v[164:167], v[208:211], a[120:123], v[164:167]\n"
    "ds_read_b128 v[204:207], v122 offset:17408\n"
    "v_mfma_f32_16x16x32_bf16 v[168:171], v[208:211], a[184:187], v[168:171]\n"
    "v_mfma_f32_16x16x32_bf16 v[172:175], v[208:211], a[248:251], v[172:175]\n"
    "v_mfma_f32_16x16x32_bf16 v[176:179], v[212:215], a[56:59], v[176:179]\n"
    "global_load_lds_dwordx4 v121, s[64:65] offset:1024 sc1\n"
    "v_mfma_f32_16x16x32_bf16 v[180:183], v[212:215], a[120:123], v[180:183]\n"
    "v_mfma_f32_16x16x32_bf16 v[184:187], v[212:215], a[184:187], v[184:187]\n"
    "v_mfma_f32_16x16x32_bf16 v[188:191], v[212:215], a[248:251], v[188:191]\n"
    "s_add_u32 s51, s51, 1\n"
    "s_waitcnt lgkmcnt(4)\n"
    "v_mfma_f32_16x16x32_bf16 v[160:163], v[216:219], a[60:63], v[160:163]\n"
    "ds_read_b128 v[208:211], v122 offset:2048\n"
    "v_mfma_f32_16x16x32_bf16 v[164:167], v[216:219], a[124:127], v[164:167]\n"
    "ds_read_b128 v[212:215], v122 offset:18432\n"
    "v_mfma_f32_16x16x32_bf16 v[168:171], v[216:219], a[188:191], v[168:171]\n"
    "v_mfma_f32_16x16x32_bf16 v[172:175], v[216:219], a[252:255], v[172:175]\n"
    "v_mfma_f32_16x16x32_bf16 v[176:179], v[220:223], a[60:63], v[176:179]\n"
    "global_load_lds_dwordx4 v121, s[64:65] offset:2048 sc1\n"
    "v_mfma_f32_16x16x32_bf16 v[180:183], v[220:223], a[124:127], v[180:183]\n"
    "v_mfma_f32_16x16x32_bf16 v[184:187], v[220:223], a[188:191], v[184:187]\n"
    "v_mfma_f32_16x16x32_bf16 v[188:191], v[220:223], a[252:255], v[188:191]\n"
    "s_add_u32 s58, s51, -3\n"
    "s_cmp_gt_u32 s58, 28\n"
    "s_waitcnt lgkmcnt(4)\n"
    "s_cbranch_scc1 G_t1%=\n"
    "G_even%=:\n"
    "v_mfma_f32_16x16x32_bf16 v[128:131], v[192:195], a[0:3], 0\n"
    "ds_read_b128 v[216:219], v122 offset:3072\n"
    "v_mfma_f32_16x16x32_bf16 v[132:135], v[192:195], a[64:67], 0\n"
    "ds_read_b128 v[220:223], v122 offset:19456\n"
    "v_mfma_f32_16x16x32_bf16 v[136:139], v[192:195], a[128:131], 0\n"
    "v_mfma_f32_16x16x32_bf16 v[140:143], v[192:195], a[192:195], 0\n"
    "v_cvt_pk_bf16_f32 v224, v160, v161\n"
    "v_cvt_pk_bf16_f32 v225, v162, v163\n"
    "v_mfma_f32_16x16x32_bf16 v[144:147], v[196:199], a[0:3], 0\n"
    "global_load_lds_dwordx4 v121, s[64:65] offset:3072 sc1\n"
    "v_mfma_f32_16x16x32_bf16 v[148:151], v[196:199], a[64:67], 0\n"
    "v_cvt_pk_bf16_f32 v226, v176, v177\n"
    "v_cvt_pk_bf16_f32 v227, v178, v179\n"
    "v_mfma_f32_16x16x32_bf16 v[152:155], v[196:199], a[128:131], 0\n"
    "v_cvt_pk_bf16_f32 v228, v164, v165\n"
    "v_cvt_pk_bf16_f32 v229, v166, v167\n"
    "v_mfma_f32_16x16x32_bf16 v[156:159], v[196:199], a[192:195], 0\n"
    "v_cvt_pk_bf16_f32 v230, v180, v181\n"
    "v_cvt_pk_bf16_f32 v231, v182, v183\n"
    "s_waitcnt lgkmcnt(4)\n"
    "v_mfma_f32_16x16x32_bf16 v[128:131], v[200:203], a[4:7], v[128:131]\n"
    "ds_read_b128 v[192:195], v122 offset:4096\n"
    "v_mfma_f32_16x16x32_bf16 v[132:135], v[200:203], a[68:71], v[132:135]\n"
    "ds_read_b128 v[196:199], v122 offset:20480\n"
    "v_mfma_f32_16x16x32_bf16 v[136:139], v[200:203], a[132:135], v[136:139]\n"
    "s_add_u32 m0, s50, 77824\n"
    "s_add_u32 s64, s48, 12288\n"
    "s_addc_u32 s65, s49, 0\n"
    "v_cvt_pk_bf16_f32 v232, v168, v169\n"
    "v_cvt_pk_bf16_f32 v233, v170, v171\n"
    "v_mfma_f32_16x16x32_bf16 v[140:143], v[200:203], a[196:199], v[140:143]\n"
    "s_add_u32 s48, s48, 0x10000\n"
    "s_addc_u32 s49, s49, 0\n"
    "v_cvt_pk_bf16_f32 v234, v184, v185\n"
    "v_cvt_pk_bf16_f32 v235, v186, v187\n"
    "v_mfma_f32_16x16x32_bf16 v[144:147], v[204:207], a[4:7], v[144:147]\n"
    "global_load_lds_dwordx4 v121, s[64:65] offset:0 sc1\n"
    "v_mfma_f32_16x16x32_bf16 v[148:151], v[204:207], a[68:71], v[148:151]\n"
    "v_cvt_pk_bf16_f32 v236, v172, v173\n"
    "v_cvt_pk_bf16_f32 v237, v174, v175\n"
    "v_mfma_f32_16x16x32_bf16 v[152:155], v[204:207], a[132:135], v[152:155]\n"
    "global_store_dwordx4 v121, v[224:227], s[44:45] offset:0 sc1\n"
    "v_mfma_f32_16x16x32_bf16 v[156:159], v[204:207], a[196:199], v[156:159]\n"
    "v_cvt_pk_bf16_f32 v238, v188, v189\n"
    "v_cvt_pk_bf16_f32 v239, v190, v191\n"
    "s_waitcnt lgkmcnt(4)\n"
    "v_mfma_f32_16x16x32_bf16 v[128:131], v[208:211], a[8:11], v[128:131]\n"
    "ds_read_b128 v[200:203], v122 offset:5120\n"
    "v_mfma_f32_16x16x32_bf16 v[132:135], v[208:211], a[72:75], v[132:135]\n"
    "ds_read_b128 v[204:207], v122 offset:21504\n"
    "v_mfma_f32_16x16x32_bf16 v[136:139], v[208:211], a[136:139], v[136:139]\n"
    "v_mfma_f32_16x16x32_bf16 v[140:143], v[208:211], a[200:203], v[140:143]\n"
    "v_mfma_f32_16x16x32_bf16 v[144:147], v[212:215], a[8:11], v[144:147]\n"
    "global_load_lds_dwordx4 v121, s[64:65] offset:1024 sc1\n"
    "v_mfma_f32_16x16x32_bf16 v[148:151], v[212:215], a[72:75], v[148:151]\n"
    "v_mfma_f32_16x16x32_bf16 v[152:155], v[212:215], a[136:139], v[152:155]\n"
    "global_store_dwordx4 v121, v[228:231], s[44:45] offset:1024 sc1\n"
    "v_mfma_f32_16x16x32_bf16 v[156:159], v[212:215], a[200:203], v[156:159]\n"
    "s_waitcnt lgkmcnt(4)\n"
    "v_mfma_f32_16x16x32_bf16 v[128:131], v[216:219], a[12:15], v[128:131]\n"
    "ds_read_b128 v[208:211], v122 offset:6144\n"
    "v_mfma_f32_16x16x32_bf16 v[132:135], v[216:219], a[76:79], v[132:135]\n"
    "ds_read_b128 v[212:215], v122 offset:22528\n"
    "v_mfma_f32_16x16x32_bf16 v[136:139], v[216:219], a[140:143], v[136:139]\n"
    "v_mfma_f32_16x16x32_bf16 v[140:143], v[216:219], a[204:207], v[140:143]\n"
    "v_mfma_f32_16x16x32_bf16 v[144:147], v[220:223], a[12:15], v[144:147]\n"
    "global_load_lds_dwordx4 v121, s[64:65] offset:2048 sc1\n"
    "v_mfma_f32_16x16x32_bf16 v[148:151], v[220:223], a[76:79], v[148:151]\n"
    "v_mfma_f32_16x16x32_bf16 v[152:155], v[220:223], a[140:143], v[152:155]\n"
    "global_store_dwordx4 v121, v[232:235], s[44:45] offset:2048 sc1\n"
    "v_mfma_f32_16x16x32_bf16 v[156:159], v[220:223], a[204:207], v[156:159]\n"
    "s_waitcnt lgkmcnt(4)\n"
    "v_mfma_f32_16x16x32_bf16 v[128:131], v[192:195], a[16:19], v[128:131]\n"
    "ds_read_b128 v[216:219], v122 offset:7168\n"
    "v_mfma_f32_16x16x32_bf16 v[132:135], v[192:195], a[80:83], v[132:135]\n"
    "ds_read_b128 v[220:223], v122 offset:23552\n"
    "v_mfma_f32_16x16x32_bf16 v[136:139], v[192:195], a[144:147], v[136:139]\n"
    "v_mfma_f32_16x16x32_bf16 v[140:143], v[192:195], a[208:211], v[140:143]\n"
    "v_mfma_f32_16x16x32_bf16 v[144:147], v[196:199], a[16:19], v[144:147]\n"
    "global_load_lds_dwordx4 v121, s[64:65] offset:3072 sc1\n"
    "v_mfma_f32_16x16x32_bf16 v[148:151], v[196:199], a[80:83], v[148:151]\n"
    "v_mfma_f32_16x16x32_bf16 v[152:155], v[196:199], a[144:147], v[152:155]\n"
    "global_store_dwordx4 v121, v[236:239], s[44:45] offset:3072 sc1\n"
    "v_mfma_f32_16x16x32_bf16 v[156:159], v[196:199], a[208:211], v[156:159]\n"
    "s_waitcnt lgkmcnt(4)\n"
    "v_mfma_f32_16x16x32_bf16 v[128:131], v[200:203], a[20:23], v[128:131]\n"
    "s_waitcnt vmcnt(12) lgkmcnt(0)\n"
    "s_barrier\n"
    "v_mfma_f32_16x16x32_bf16 v[132:135], v[200:203], a[84:87], v[132:135]\n"
    "ds_read_b128 v[192:195], v122 offset:8192\n"
    "v_mfma_f32_16x16x32_bf16 v[136:139], v[200:203], a[148:151], v[136:139]\n"
    "ds_read_b128 v[196:199], v122 offset:24576\n"
    "s_add_u32 m0, s50, 0\n"
    "s_add_u32 s64, s48, 0\n"
    "s_addc_u32 s65, s49, 0\n"
    "v_mfma_f32_16x16x32_bf16 v[140:143], v[200:203], a[212:215], v[140:143]\n"
    "v_mfma_f32_16x16x32_bf16 v[144:147], v[204:207], a[20:23], v[144:147]\n"
    "global_load_lds_dwordx4 v121, s[64:65] offset:0 sc1\n"
    "v_mfma_f32_16x16x32_bf16 v[148:151], v[204:207], a[84:87], v[148:151]\n"
    "v_mfma_f32_16x16x32_bf16 v[152:155], v[204:207], a[148:151], v[152:155]\n"
    "v_mfma_f32_16x16x32_bf16 v[156:159], v[204:207], a[212:215], v[156:159]\n"
    "s_waitcnt lgkmcnt(4)\n"
    "v_mfma_f32_16x16x32_bf16 v[128:131], v[208:211], a[24:27], v[128:131]\n"
    "ds_read_b128 v[200:203], v122 offset:9216\n"
    "v_mfma_f32_16x16x32_bf16 v[132:135], v[208:211], a[88:91], v[132:135]\n"
    "ds_read_b128 v[204:207], v122 offset:25600\n"
    "v_mfma_f32_16x16x32_bf16 v[136:139], v[208:211], a[152:155], v[136:139]\n"
    "v_mfma_f32_16x16x32_bf16 v[140:143], v[208:211], a[216:219], v[140:143]\n"
    "v_mfma_f32_16x16x32_bf16 v[144:147], v[212:215], a[24:27], v[144:147]\n"
    "global_load_lds_dwordx4 v121, s[64:65] offset:1024 sc1\n"
    "v_mfma_f32_16x16x32_bf16 v[148:151], v[212:215], a[88:91], v[148:151]\n"
    "v_mfma_f32_16x16x32_bf16 v[152:155], v[212:215], a[152:155], v[152:155]\n"
    "v_mfma_f32_16x16x32_bf16 v[156:159], v[212:215], a[216:219], v[156:159]\n"
    "s_waitcnt lgkmcnt(4)\n"
    "v_mfma_f32_16x16x32_bf16 v[128:131], v[216:219], a[28:31], v[128:131]\n"
    "ds_read_b128 v[208:211], v122 offset:10240\n"
    "v_mfma_f32_16x16x32_bf16 v[132:135], v[216:219], a[92:95], v[132:135]\n"
    "ds_read_b128 v[212:215], v122 offset:26624\n"
    "v_mfma_f32_16x16x32_bf16 v[136:139], v[216:219], a[156:159], v[136:139]\n"
    "v_mfma_f32_16x16x32_bf16 v[140:143], v[216:219], a[220:223], v[140:143]\n"
    "v_mfma_f32_16x16x32_bf16 v[144:147], v[220:223], a[28:31], v[144:147]\n"
    "global_load_lds_dwordx4 v121, s[64:65] offset:2048 sc1\n"
    "v_mfma_f32_16x16x32_bf16 v[148:151], v[220:223], a[92:95], v[148:151]\n"
    "s_mov_b64 s[44:45], s[66:67]\n"
    "s_add_u32 s66, s66, 0x100000\n"
    "s_addc_u32 s67, s67, 0\n"
    "v_mfma_f32_16x16x32_bf16 v[152:155], v[220:223], a[156:159], v[152:155]\n"
    "v_mfma_f32_16x16x32_bf16 v[156:159], v[220:223], a[220:223], v[156:159]\n"
    "s_waitcnt lgkmcnt(4)\n"
    "v_mfma_f32_16x16x32_bf16 v[128:131], v[192:195], a[32:35], v[128:131]\n"
    "ds_read_b128 v[216:219], v122 offset:11264\n"
    "v_mfma_f32_16x16x32_bf16 v[132:135], v[192:195], a[96:99], v[132:135]\n"
    "ds_read_b128 v[220:223], v122 offset:27648\n"
    "v_mfma_f32_16x16x32_bf16 v[136:139], v[192:195], a[160:163], v[136:139]\n"
    "v_mfma_f32_16x16x32_bf16 v[140:143], v[192:195], a[224:227], v[140:143]\n"
    "v_mfma_f32_16x16x32_bf16 v[144:147], v[196:199], a[32:35], v[144:147]\n"
    "global_load_lds_dwordx4 v121, s[64:65] offset:3072 sc1\n"
    "v_mfma_f32_16x16x32_bf16 v[148:151], v[196:199], a[96:99], v[148:151]\n"
    "v_mfma_f32_16x16x32_bf16 v[152:155], v[196:199], a[160:163], v[152:155]\n"
    "v_mfma_f32_16x16x32_bf16 v[156:159], v[196:199], a[224:227], v[156:159]\n"
    "s_waitcnt lgkmcnt(4)\n"
    "v_mfma_f32_16x16x32_bf16 v[128:131], v[200:203], a[36:39], v[128:131]\n"
    "ds_read_b128 v[192:195], v122 offset:12288\n"
    "v_mfma_f32_16x16x32_bf16 v[132:135], v[200:203], a[100:103], v[132:135]\n"
    "ds_read_b128 v[196:199], v122 offset:28672\n"
    "v_mfma_f32_16x16x32_bf16 v[136:139], v[200:203], a[164:167], v[136:139]\n"
    "s_add_u32 m0, s50, 4096\n"
    "s_add_u32 s64, s48, 4096\n"
    "s_addc_u32 s65, s49, 0\n"
    "v_mfma_f32_16x16x32_bf16 v[140:143], v[200:203], a[228:231], v[140:143]\n"
    "v_mfma_f32_16x16x32_bf16 v[144:147], v[204:207], a[36:39], v[144:147]\n"
    "global_load_lds_dwordx4 v121, s[64:65] offset:0 sc1\n"
    "v_mfma_f32_16x16x32_bf16 v[148:151], v[204:207], a[100:103], v[148:151]\n"
    "v_mfma_f32_16x16x32_bf16 v[152:155], v[204:207], a[164:167], v[152:155]\n"
    "v_mfma_f32_16x16x32_bf16 v[156:159], v[204:207], a[228:231], v[156:159]\n"
    "s_waitcnt lgkmcnt(4)\n"
    "v_mfma_f32_16x16x32_bf16 v[128:131], v[208:211], a[40:43], v[128:131]\n"
    "ds_read_b128 v[200:203], v122 offset:13312\n"
    "v_mfma_f32_16x16x32_bf16 v[132:135], v[208:211], a[104:107], v[132:135]\n"
    "ds_read_b128 v[204:207], v122 offset:29696\n"
    "v_mfma_f32_16x16x32_bf16 v[136:139], v[208:211], a[168:171], v[136:139]\n"
    "v_mfma_f32_16x16x32_bf16 v[140:143], v[208:211], a[232:235], v[140:143]\n"
    "v_mfma_f32_16x16x32_bf16 v[144:147], v[212:215], a[40:43], v[144:147]\n"
    "global_load_lds_dwordx4 v121, s[64:65] offset:1024 sc1\n"
    "v_mfma_f32_16x16x32_bf16 v[148:151], v[212:215], a[104:107], v[148:151]\n"
    "v_mfma_f32_16x16x32_bf16 v[152:155], v[212:215], a[168:171], v[152:155]\n"
    "v_mfma_f32_16x16x32_bf16 v[156:159], v[212:215], a[232:235], v[156:159]\n"
    "s_waitcnt lgkmcnt(4)\n"
    "v_mfma_f32_16x16x32_bf16 v[128:131], v[216:219], a[44:47], v[128:131]\n"
    "ds_read_b128 v[208:211], v122 offset:14336\n"
    "v_mfma_f32_16x16x32_bf16 v[132:135], v[216:219], a[108:111], v[132:135]\n"
    "ds_read_b128 v[212:215], v122 offset:30720\n"
    "v_mfma_f32_16x16x32_bf16 v[136:139], v[216:219], a[172:175], v[136:139]\n"
    "v_mfma_f32_16x16x32_bf16 v[140:143], v[216:219], a[236:239], v[140:143]\n"
    "v_mfma_f32_16x16x32_bf16 v[144:147], v[220:223], a[44:47], v[144:147]\n"
    "global_load_lds_dwordx4 v121, s[64:65] offset:2048 sc1\n"
    "v_mfma_f32_16x16x32_bf16 v[148:151], v[220:223], a[108:111], v[148:151]\n"
    "v_mfma_f32_16x16x32_bf16 v[152:155], v[220:223], a[172:175], v[152:155]\n"
    "v_mfma_f32_16x16x32_bf16 v[156:159], v[220:223], a[236:239], v[156:159]\n"
    "s_waitcnt lgkmcnt(4)\n"
    "v_mfma_f32_16x16x32_bf16 v[128:131], v[192:195], a[48:51], v[128:131]\n"
    "ds_read_b128 v[216:219], v122 offset:15360\n"
    "v_mfma_f32_16x16x32_bf16 v[132:135], v[192:195], a[112:115], v[132:135]\n"
    "ds_read_b128 v[220:223], v122 offset:31744\n"
    "v_mfma_f32_16x16x32_bf16 v[136:139], v[192:195], a[176:179], v[136:139]\n"
    "v_mfma_f32_16x16x32_bf16 v[140:143], v[192:195], a[240:243], v[140:143]\n"
    "v_mfma_f32_16x16x32_bf16 v[144:147], v[196:199], a[48:51], v[144:147]\n"
    "global_load_lds_dwordx4 v121, s[64:65] offset:3072 sc1\n"
    "v_mfma_f32_16x16x32_bf16 v[148:151], v[196:199], a[112:115], v[148:151]\n"
    "v_mfma_f32_16x16x32_bf16 v[152:155], v[196:199], a[176:179], v[152:155]\n"
    "v_mfma_f32_16x16x32_bf16 v[156:159], v[196:199], a[240:243], v[156:159]\n"
    "s_waitcnt lgkmcnt(4)\n"
    "v_mfma_f32_16x16x32_bf16 v[128:131], v[200:203], a[52:55], v[128:131]\n"
    "s_waitcnt vmcnt(8) lgkmcnt(0)\n"
    "s_barrier\n"
    "v_mfma_f32_16x16x32_bf16 v[132:135], v[200:203], a[116:119], v[132:135]\n"
    "ds_read_b128 v[192:195], v123 offset:0\n"
    "v_mfma_f32_16x16x32_bf16 v[136:139], v[200:203], a[180:183], v[136:139]\n"
    "ds_read_b128 v[196:199], v123 offset:16384\n"
    "s_add_u32 m0, s50, 8192\n"
    "s_add_u32 s64, s48, 8192\n"
    "s_addc_u32 s65, s49, 0\n"
    "v_mfma_f32_16x16x32_bf16 v[140:143], v[200:203], a[244:247], v[140:143]\n"
    "v_mfma_f32_16x16x32_bf16 v[144:147], v[204:207], a[52:55], v[144:147]\n"
    "global_load_lds_dwordx4 v121, s[64:65] offset:0 sc1\n"
    "v_mfma_f32_16x16x32_bf16 v[148:151], v[204:207], a[116:119], v[148:151]\n"
    "s_cmp_lg_u32 s56, 0\n"
    "s_cbranch_scc1 G_f2%=\n"
    "s_or_b32 s58, s42, s51\n"
    "s_mov_b64 exec, 1\n"
    "v_mov_b32 v116, s58\n"
    "global_store_dwordx2 v118, v[116:117], s[54:55] sc1\n"
    "s_mov_b64 exec, -1\n"
    "G_f2%=:\n"
    "v_mfma_f32_16x16x32_bf16 v[152:155], v[204:207], a[180:183], v[152:155]\n"
    "v_mfma_f32_16x16x32_bf16 v[156:159], v[204:207], a[244:247], v[156:159]\n"
    "s_waitcnt lgkmcnt(4)\n"
    "v_mfma_f32_16x16x32_bf16 v[128:131], v[208:211], a[56:59], v[128:131]\n"
    "ds_read_b128 v[200:203], v123 offset:1024\n"
    "v_mfma_f32_16x16x32_bf16 v[132:135], v[208:211], a[120:123], v[132:135]\n"
    "ds_read_b128 v[204:207], v123 offset:17408\n"
    "v_mfma_f32_16x16x32_bf16 v[136:139], v[208:211], a[184:187], v[136:139]\n"
    "v_mfma_f32_16x16x32_bf16 v[140:143], v[208:211], a[248:251], v[140:143]\n"
    "v_mfma_f32_16x16x32_bf16 v[144:147], v[212:215], a[56:59], v[144:147]\n"
    "global_load_lds_dwordx4 v121, s[64:65] offset:1024 sc1\n"
    "v_mfma_f32_16x16x32_bf16 v[148:151], v[212:215], a[120:123], v[148:151]\n"
    "v_mfma_f32_16x16x32_bf16 v[152:155], v[212:215], a[184:187], v[152:155]\n"
    "v_mfma_f32_16x16x32_bf16 v[156:159], v[212:215], a[248:251], v[156:159]\n"
    "s_add_u32 s51, s51, 1\n"
    "s_waitcnt lgkmcnt(4)\n"
    "v_mfma_f32_16x16x32_bf16 v[128:131], v[216:219], a[60:63], v[128:131]\n"
    "ds_read_b128 v[208:211], v123 offset:2048\n"
    "v_mfma_f32_16x16x32_bf16 v[132:135], v[216:219], a[124:127], v[132:135]\n"
    "ds_read_b128 v[212:215], v123 offset:18432\n"
    "v_mfma_f32_16x16x32_bf16 v[136:139], v[216:219], a[188:191], v[136:139]\n"
    "v_mfma_f32_16x16x32_bf16 v[140:143], v[216:219], a[252:255], v[140:143]\n"
    "v_mfma_f32_16x16x32_bf16 v[144:147], v[220:223], a[60:63], v[144:147]\n"
    "global_load_lds_dwordx4 v121, s[64:65] offset:2048 sc1\n"
    "v_mfma_f32_16x16x32_bf16 v[148:151], v[220:223], a[124:127], v[148:151]\n"
    "v_mfma_f32_16x16x32_bf16 v[152:155], v[220:223], a[188:191], v[152:155]\n"
    "v_mfma_f32_16x16x32_bf16 v[156:159], v[220:223], a[252:255], v[156:159]\n"
    "s_add_u32 s58, s51, -3\n"
    "s_cmp_gt_u32 s58, 28\n"
    "s_waitcnt lgkmcnt(4)\n"
    "s_cbranch_scc0 G_loop%=\n"
    "s_branch G_pc%=\n"
    "G_pw%=:\n"
    "s_add_u32 s58, s40, 0x312000\n"
    "s_addc_u32 s59, s41, 0\n"
    "global_load_dwordx2 v[114:115], v112, s[58:59] sc1\n"
    "s_waitcnt vmcnt(0)\n"
    "G_pc%=:\n"
    "v_xor_b32 v114, s42, v114\n"
    "v_xor_b32 v115, s43, v115\n"
    "v_add_u32 v114, -1, v114\n"
    "v_cmp_le_u64 s[58:59], 8, v[114:115]\n"
    "v_and_b32 v114, 2, v114\n"
    "v_cmp_ne_u32 s[52:53], 0, v114\n"
    "s_cmp_eq_u64 s[58:59], 0\n"
    "s_cbranch_scc1 G_pok%=\n"
    "s_sleep 4\n"
    "s_add_u32 s57, s57, 1\n"
    "s_cmp_lt_u32 s57, 0x40000\n"
    "s_cbranch_scc1 G_pw%=\n"
    "G_pok%=:\n"
    "s_cmp_lg_u32 s52, 0\n"
    "s_cselect_b32 s52, 0, 0x2000\n"
    "s_xor_b32 s44, s44, s52\n"
    "s_xor_b32 s66, s66, s52\n"
    "s_nop 7\n"
    "v_cvt_pk_bf16_f32 v224, v128, v129\n"
    "v_cvt_pk_bf16_f32 v225, v130, v131\n"
    "v_cvt_pk_bf16_f32 v226, v144, v145\n"
    "v_cvt_pk_bf16_f32 v227, v146, v147\n"
    "v_cvt_pk_bf16_f32 v228, v132, v133\n"
    "v_cvt_pk_bf16_f32 v229, v134, v135\n"
    "v_cvt_pk_bf16_f32 v230, v148, v149\n"
    "v_cvt_pk_bf16_f32 v231, v150, v151\n"
    "v_cvt_pk_bf16_f32 v232, v136, v137\n"
    "v_cvt_pk_bf16_f32 v233, v138, v139\n"
    "v_cvt_pk_bf16_f32 v234, v152, v153\n"
    "v_cvt_pk_bf16_f32 v235, v154, v155\n"
    "v_cvt_pk_bf16_f32 v236, v140, v141\n"
    "v_cvt_pk_bf16_f32 v237, v142, v143\n"
    "v_cvt_pk_bf16_f32 v238, v156, v157\n"
    "v_cvt_pk_bf16_f32 v239, v158, v159\n"
    "global_store_dwordx4 v121, v[224:227], s[44:45] offset:0 sc1\n"
    "global_store_dwordx4 v121, v[228:231], s[44:45] offset:1024 sc1\n"
    "global_store_dwordx4 v121, v[232:235], s[44:45] offset:2048 sc1\n"
    "global_store_dwordx4 v121, v[236:239], s[44:45] offset:3072 sc1\n"
    "s_sub_u32 s44, s44, 0x2200000\n"
    "s_subb_u32 s45, s45, 0\n"
    "s_waitcnt vmcnt(0)\n"
    "s_barrier\n"
    "s_cmp_lg_u32 s56, 0\n"
    "s_cbranch_scc1 G_f3%=\n"
    "s_or_b32 s58, s42, s51\n"
    "s_mov_b64 exec, 1\n"
    "v_mov_b32 v116, s58\n"
    "global_store_dwordx2 v118, v[116:117], s[54:55] sc1\n"
    "s_mov_b64 exec, -1\n"
    "G_f3%=:\n"
    "s_branch G_loop%=\n"
    "G_t1%=:\n"
    "s_nop 7\n"
    "v_cvt_pk_bf16_f32 v224, v160, v161\n"
    "v_cvt_pk_bf16_f32 v225, v162, v163\n"
    "v_cvt_pk_bf16_f32 v226, v176, v177\n"
    "v_cvt_pk_bf16_f32 v227, v178, v179\n"
    "v_cvt_pk_bf16_f32 v228, v164, v165\n"
    "v_cvt_pk_bf16_f32 v229, v166, v167\n"
    "v_cvt_pk_bf16_f32 v230, v180, v181\n"
    "v_cvt_pk_bf16_f32 v231, v182, v183\n"
    "v_cvt_pk_bf16_f32 v232, v168, v169\n"
    "v_cvt_pk_bf16_f32 v233, v170, v171\n"
    "v_cvt_pk_bf16_f32 v234, v184, v185\n"
    "v_cvt_pk_bf16_f32 v235, v186, v187\n"
    "v_cvt_pk_bf16_f32 v236, v172, v173\n"
    "v_cvt_pk_bf16_f32 v237, v174, v175\n"
    "v_cvt_pk_bf16_f32 v238, v188, v189\n"
    "v_cvt_pk_bf16_f32 v239, v190, v191\n"
    "global_store_dwordx4 v121, v[224:227], s[44:45] offset:0 sc1\n"
    "global_store_dwordx4 v121, v[228:231], s[44:45] offset:1024 sc1\n"
    "global_store_dwordx4 v121, v[232:235], s[44:45] offset:2048 sc1\n"
    "global_store_dwordx4 v121, v[236:239], s[44:45] offset:3072 sc1\n"
    "s_sub_u32 s44, s44, 0x2200000\n"
    "s_subb_u32 s45, s45, 0\n"
    "s_waitcnt vmcnt(0)\n"
    "s_barrier\n"
    "s_cmp_lg_u32 s56, 0\n"
    "s_cbranch_scc1 G_f4%=\n"
    "s_or_b32 s58, s42, s51\n"
    "s_mov_b64 exec, 1\n"
    "v_mov_b32 v116, s58\n"
    "global_store_dwordx2 v118, v[116:117], s[54:55] sc1\n"
    "s_mov_b64 exec, -1\n"
    "G_f4%=:\n"
    "s_cmp_eq_u32 s51, 32\n"
    "s_cbranch_scc1 G_end%=\n"
    "s_add_u32 s58, s40, 0x20e000\n"
    "s_addc_u32 s59, s41, 0\n"
    "s_add_u32 s60, s40, 0x200200\n"
    "s_addc_u32 s61, s41, 0\n"
    "s_or_b32 s62, s42, 1\n"
    "s_mov_b32 s63, s43\n"
    "v_lshlrev_b32 v224, 3, v121\n"
    "G_xw1%=:\n"
    "global_load_dwordx2 v[226:227], v224, s[58:59] sc1\n"
    "G_xc1%=:\n"
    "s_waitcnt vmcnt(0)\n"
    "v_cmp_ne_u64 vcc, s[62:63], v[226:227]\n"
    "s_nop 0\n"
    "s_cbranch_vccz G_xok1%=\n"
    "s_sleep 4\n"
    "s_add_u32 s57, s57, 1\n"
    "s_and_b32 s68, s57, 0xff\n"
    "s_cbranch_scc1 G_xw1%=\n"
    "global_load_dwordx2 v[226:227], v118, s[60:61] sc1\n"
    "s_waitcnt vmcnt(0)\n"
    "v_cmp_eq_u64 vcc, s[42:43], v[226:227]\n"
    "s_nop 0\n"
    "s_cbranch_vccnz G_xok1%=\n"
    "s_cmp_lt_u32 s57, 0x40000\n"
    "s_cbranch_scc1 G_xw1%=\n"
    "v_mov_b32 v116, s42\n"
    "global_store_dwordx2 v118, v[116:117], s[60:61] sc1\n"
    "G_xok1%=:\n"
    "s_mov_b32 vcc_lo, 0xcccccccc\n"
    "s_mov_b32 vcc_hi, 0xcccccccc\n"
    "s_branch G_even%=\n"
    "G_end%=:\n"
    "s_waitcnt lgkmcnt(0)\n"
    :: GIN : GCLOB);
  return T;
}

__device__ __forceinline__ void xa_phase(char* ws, int grp, int h, u64 T, unsigned far, int lane) {
  const int l15 = lane & 15, g = lane >> 4;
  const float scale = 0.08838834764831845f;
  const __amdgpu_buffer_rsrc_t rs = __builtin_amdgcn_make_buffer_rsrc(ws, 0, (int)(250 * MBY), 0x00020000);
  auto ld16 = [&](int off) { return __builtin_bit_cast(bf16x8, __builtin_amdgcn_raw_buffer_load_b128(rs, off, 0, 16)); };
  PTD;
#pragma unroll 1
  for (int s = grp; s < NSEG; s += 4) {
    if (s < 48) wait_flag<8>(ws, (unsigned)OFF_RDY + ((lane < 3 ? lane : 0) * 32 + h) * 128, T, s < 44 ? (unsigned)(s >> 1) + 1u : 32u);
    const int bo = blk(s, h, far);
    const int qp = (int)OFF_QF + bo + lane * 16;
    const int kp = (int)OFF_KF + bo + ((l15 >> 1) & 1) * 1024 + (g * 16 + (l15 & 13)) * 16;
    const int vp = (int)OFF_VF + bo + lane * 16;
    const int km = (int)OFF_KM + bo;
    const int vm = km + 4096;
    bf16x8 qf[4][2], vf[8];
    f32x4 sc[2][3];
#pragma unroll
    for (int nt = 0; nt < 2; ++nt)
#pragma unroll
      for (int t = 0; t < 3; ++t) sc[nt][t] = (f32x4){0.f, 0.f, 0.f, 0.f};
#pragma unroll
    for (int ks = 0; ks < 4; ++ks) {
      bf16x8 kf[2];
#pragma unroll
      for (int nt = 0; nt < 2; ++nt) qf[ks][nt] = ld16(qp + ks * 2048 + nt * 1024);
#pragma unroll
      for (int t = 0; t < 2; ++t) kf[t] = ld16(kp + (ks >> 1) * 4096 + (ks & 1) * 32 + t * 2048);
#pragma unroll
      for (int nt = 0; nt < 2; ++nt)
#pragma unroll
        for (int t = 0; t < 2; ++t) sc[nt][t] = __builtin_amdgcn_mfma_f32_16x16x32_bf16(kf[t], qf[ks][nt], sc[nt][t], 0, 0, 0);
    }
#pragma unroll
    for (int e = 0; e < 8; ++e) vf[e] = ld16(vp + e * 1024);
    float mt[2], lt[2];
    f32x4 acc[2][8];
#pragma unroll
    for (int nt = 0; nt < 2; ++nt) {
      const int qi = nt * 16 + l15;
      float v[2][4], m = -3.0e38f;
#pragma unroll
      for (int t = 0; t < 2; ++t)
#pragma unroll
        for (int r = 0; r < 4; ++r) {
          v[t][r] = (t * 16 + g * 4 + r <= qi) ? rbf(rbf(sc[nt][t][r]) * scale) : -3.0e38f;
          m = fmaxf(m, v[t][r]);
        }
      m = max_g(m);
#pragma unroll
      for (int t = 0; t < 2; ++t)
#pragma unroll
        for (int r = 0; r < 4; r += 2) {
          v[t][r] = (t * 16 + g * 4 + r <= qi) ? __expf(v[t][r] - m) : 0.f;
          v[t][r + 1] = (t * 16 + g * 4 + r + 1 <= qi) ? __expf(v[t][r + 1] - m) : 0.f;
          rbf2(v[t][r], v[t][r + 1]);
        }
      mt[nt] = m;
      lt[nt] = sum_g(((v[0][0] + v[0][1]) + (v[0][2] + v[0][3])) + ((v[1][0] + v[1][1]) + (v[1][2] + v[1][3])));
      const bf16x8 pf = mk8(pk2(v[0][0], v[0][1]), pk2(v[0][2], v[0][3]), pk2(v[1][0], v[1][1]), pk2(v[1][2], v[1][3]));
#pragma unroll
      for (int e = 0; e < 8; ++e) acc[nt][e] = __builtin_amdgcn_mfma_f32_16x16x32_bf16(vf[e], pf, (f32x4){0.f, 0.f, 0.f, 0.f}, 0, 0, 0);
    }
#pragma unroll
    for (int e = 0; e < 8; ++e) asm volatile("" : "+v"(acc[0][e]), "+v"(acc[1][e]));
    asm volatile("" : "+v"(mt[0]), "+v"(mt[1]), "+v"(lt[0]), "+v"(lt[1]));
    if (h == 5) TLX(s * 8);
    wait_flag<PSL>(ws, (unsigned)OFF_FLAGS + (s * 64 + h + (lane & 1) * 32) * 128, T, 1u);
    if (s == 63) TL(4);
    PT(1, s == 63);
    if (h == 5) TLX(s * 8 + 1);
    bf16x8 k2[4];
    unsigned mv[4][2];
    for (unsigned spins = 0;;) {
#pragma unroll
    for (int ks = 0; ks < 4; ++ks) k2[ks] = ld16(km + (ks >> 1) * 1024 + (g * 16 + ((l15 >> 3) | ((l15 >> 1) & 2)) * 4 + (ks & 1) * 2 + (l15 & 1)) * 16);
#pragma unroll
    for (int w = 0; w < 4; ++w) { const auto t = __builtin_amdgcn_raw_buffer_load_b64(rs, vm + w * 512 + lane * 8, 0, 16); mv[w][0] = t[0]; mv[w][1] = t[1]; }
      bool mk = false;
#pragma unroll
      for (int ks = 0; ks < 4; ++ks) mk |= marked(__builtin_bit_cast(u32x4, k2[ks])) || mv[ks][0] == SENT || mv[ks][1] == SENT;
      if (!__any(mk) || spin_bail(spins, ws, T)) break;
      asm volatile("" ::: "memory");
    }
#pragma unroll
    for (int ks = 0; ks < 4; ++ks)
#pragma unroll
      for (int nt = 0; nt < 2; ++nt) sc[nt][2] = __builtin_amdgcn_mfma_f32_16x16x32_bf16(k2[ks], qf[ks][nt], sc[nt][2], 0, 0, 0);
#pragma unroll
    for (int nt = 0; nt < 2; ++nt) {
      float a = sc[nt][2][0], b = sc[nt][2][1];
      rbf2(a, b); a *= scale; b *= scale; rbf2(a, b);
      const float m = fmaxf(mt[nt], max_g(fmaxf(a, b)));
      const float al = __expf(mt[nt] - m);
      a = __expf(a - m); b = __expf(b - m);
      const float inv = __builtin_amdgcn_rcpf(al * lt[nt] + sum_g(a + b));
      const float sa = al * inv;
      if (nt == OFNT && s >= SLATE && lane == 0) set_flag(ws, OFF_OFL + (s * 32 + h) * 128, T, 1);
      const bf16x8 pf1 = mk8(pk2(a * inv, b * inv), 0u, 0u, 0u);
#pragma unroll
      for (int e = 0; e < 8; ++e) acc[nt][e] = __builtin_amdgcn_mfma_f32_16x16x32_bf16(mk8(mv[e >> 1][e & 1], 0u, 0u, 0u), pf1, acc[nt][e] * sa, 0, 0, 0);
#pragma unroll
      for (int w = 0; w < 4; ++w) {
        const f32x4 a0 = acc[nt][2 * w], a1 = acc[nt][2 * w + 1];
        u32x4 o; o.x = pk2(a0[0], a0[1]); o.y = pk2(a1[0], a1[1]); o.z = pk2(a0[2], a0[3]); o.w = pk2(a1[2], a1[3]);
        if (OFNT == 3 && nt == 1 && w == 0 && s >= SLATE && lane == 0) set_flag(ws, OFF_OFL + (s * 32 + h) * 128, T, 1);
        store16_sc1(ws + OFF_O, (unsigned)(bo + (nt * 4 + w) * 1024 + lane * 16), o);
      }
    }
    if ((s >> 2) == 9) {
#pragma unroll
  for (int i = 0; i < (NSEG - SLATE) * 2; ++i) store16_sc1(ws + OFF_O, (unsigned)(blk(SLATE + (i >> 3) * 4 + grp, h, far) + (i & 7) * 1024 + lane * 16), (u32x4){SENT, SENT, SENT, SENT});
    }
    if (s < SLATE) {
      asm volatile("s_waitcnt vmcnt(0)" ::: "memory");
      if (lane == 0) set_flag(ws, OFF_OFL + (s * 32 + h) * 128, T, 1);
    } else if (OFNT == 2 && lane == 0) set_flag(ws, OFF_OFL + (s * 32 + h) * 128, T, 1);
    if (h == 5) TLX(s * 8 + 2);
    PT(2, s >= 60);
  }
  PTE;
}

static_assert(SLATE % 4 == 0 && SLATE >= NSIX, "late segments belong to the four last groups");
__device__ __forceinline__ void op_phase(char* ws, const u16* __restrict__ x, const u16* __restrict__ w_r, u16* out, const u16* __restrict__ Wo, int grp, int ks4, int cs, u64 T, unsigned far, char* lds, int tid) {
  const int lane = tid & 63, wv = __builtin_amdgcn_readfirstlane(tid >> 6), l15 = lane & 15, g = lane >> 4;
  const __amdgpu_buffer_rsrc_t rs = __builtin_amdgcn_make_buffer_rsrc(ws, 0, (int)(250 * MBY), 0x00020000);
  const int h0 = ks4 * 8 + wv * 2;
  PTD;
  PT(5, grp < 4);
  bf16x8 wo[2][32];
#pragma unroll
  for (int hh = 0; hh < 2; ++hh) {
#pragma unroll
    for (int i = 0; i < 32; ++i) {
      const int r = i * 4 + g, f = (r & 1) | ((r >> 1) & 2) | ((r >> 2) & 4);
      __builtin_amdgcn_global_load_lds((const GAS void*)(Wo + (size_t)((h0 + hh) * DH + r) * DM + cs * 128 + ((l15 ^ (f << 1)) * 8)),
                                       (LAS void*)(lds + wv * 32768 + i * 1024), 16, 0, 0);
    }
    asm volatile("s_waitcnt vmcnt(0)" ::: "memory");
    const int qq = l15 >> 2, f = (qq & 1) | ((g & 1) << 1) | ((qq >> 1) << 2);
    const char* rp = lds + wv * 32768 + (g * 4 + (qq & 1) + (qq >> 1) * 16) * 256 + (l15 & 3) * 8;
#pragma unroll
    for (int j = 0; j < 8; ++j)
#pragma unroll
      for (int ks = 0; ks < 4; ++ks) {
        const char* p = rp + ks * 8192 + ((j ^ f) * 32);
        wo[hh][j * 4 + ks] = tr_frag(p, p + 512);
      }
#pragma unroll
    for (int f2 = 0; f2 < 32; ++f2) asm volatile("" : "+a"(wo[hh][f2]));
    asm volatile("s_waitcnt lgkmcnt(0)" ::: "memory");
  }
  __syncthreads();
  constexpr int RS = 528, RW = 32 * RS;
  auto ln2 = [&](int s, bool fresh) {
      const int row = s * 32 + (ks4 * 4 + cs) * 2 + (wv & 1);
      const u32x4 xv = *(const u32x4*)(x + (size_t)row * DM + lane * 8);
      float wr[8];
      unpack8(*(const u32x4*)(w_r + lane * 8), wr);
      if (fresh) __builtin_amdgcn_s_sleep(HSL);
      wait_flag<PSL>(ws, (unsigned)OFF_HFL + s * 2048 + l15 * 128, T, 1u);
      if (s == 63) TL(7);
      float f[8], y[8];
      for (unsigned spins = 0; load_h(rs, xv, row, far, lane, f) && !spin_bail(spins, ws, T);) asm volatile("" ::: "memory");
      ln_row(f, y);
      float r = 0.f;
#pragma unroll
      for (int i = 0; i < 8; ++i) r += y[i] * wr[i];
      r = wave_sum(r);
      u32x4 z;
      z.x = pk2(f[0] + y[0], f[1] + y[1]); z.y = pk2(f[2] + y[2], f[3] + y[3]); z.z = pk2(f[4] + y[4], f[5] + y[5]); z.w = pk2(f[6] + y[6], f[7] + y[7]);
      const unsigned rw = (pk2(r, r) & 0xffff0000u) | 1u;
      if (lane < 8 && row >= NSPEC) __hip_atomic_store((gu32*)(ws + OFF_RC2 + (lane * 64 + ((row - NSPEC) >> 2)) * 128 + (row & 3) * 4), rw, RLX_AGENT);
      store16_sc1(out, (unsigned)(row * 1024 + lane * 16), z);
      asm volatile("s_waitcnt vmcnt(0)" ::: "memory");
      if (lane == 0) __hip_atomic_store((gu32*)(ws + OFF_RC + (row >> 2) * 128 + (row & 3) * 4), rw, RLX_AGENT);
      if ((row & 31) == 0) TLX(s * 8 + 6);
  };
  if (grp < 4) {
#pragma unroll
    for (int i = 0; i < NSEG - SLATE; ++i)
      store16_sc1(ws + OFF_HP, (unsigned)(blk(SLATE + (i & ~3) + ((NSIX + grp) & 3), (ks4 * 4 + cs) * 2 + ((i >> 1) & 1), far) + (i & 1) * 4096 + tid * 16), (u32x4){SENT, SENT, SENT, SENT});
  }
  int pend = -1, turn = 0;
  unsigned spins = 0;
  bool have = false;
  bf16x8 of[16];
  auto load_o = [&](int s) {
#pragma unroll
    for (int f = 0; f < 16; ++f) of[f] = __builtin_bit_cast(bf16x8, __builtin_amdgcn_raw_buffer_load_b128(rs, (int)OFF_O + blk(s, h0 + (f >> 3), far) + lane * 16 + (f & 7) * 1024, 0, 16));
  };
#pragma unroll 1
  for (int s = grp, sn; s < NSEG; s = sn) {
    sn = s + 6 < NSIX ? s + 6 : (s < NSIX ? (grp < 4 ? NSIX + grp : NSEG) : s + 4);
    f32x4 acc[2][8];
#pragma unroll
    for (int i = 0; i < 2; ++i)
#pragma unroll
      for (int j = 0; j < 8; ++j) { acc[i][j] = (f32x4){0.f, 0.f, 0.f, 0.f}; asm volatile("" : "+v"(acc[i][j])); }
    if (!have) {
      if (pend >= 0) { ln2(pend, pend < SLATE); pend = -1; }
      wait_flag<PSL>(ws, (unsigned)OFF_OFL + (s * 32 + h0 + (lane & 1)) * 128, T, 1u);
      if (s == 63) TL(1);
      load_o(s);
    }
    if (ks4 == 0 && cs == 0 && tid == 0) TLX(s * 8 + 3);
    const u64 nf = __hip_atomic_load((gu64*)(ws + OFF_OFL + (min(sn, NSEG - 1) * 32 + h0 + (lane & 1)) * 128), RLX_AGENT);
    __builtin_amdgcn_sched_barrier(0);
#pragma unroll
    for (int f = 0; f < 16; ++f)
#pragma unroll
      for (int j = 0; j < 8; ++j)
        asm volatile("v_mfma_f32_16x16x32_bf16 %0, %1, %2, %0" : "+v"(acc[(f >> 2) & 1][j]) : "a"(wo[f >> 3][j * 4 + (f & 3)]), "v"(of[f]));
    __builtin_amdgcn_sched_barrier(0);
    asm volatile("s_nop 7\n\ts_nop 3" : "+v"(acc[1][4]), "+v"(acc[1][5]), "+v"(acc[1][6]), "+v"(acc[1][7]), "+v"(acc[1][0]), "+v"(acc[1][1]), "+v"(acc[1][2]), "+v"(acc[1][3]));
    __builtin_amdgcn_sched_barrier(0);
    if (s >= SLATE && __any(__builtin_isunordered(acc[0][0][0], acc[1][0][0]))) {
      bool mk = false;
#pragma unroll
      for (int f = 0; f < 16; ++f) mk |= marked(__builtin_bit_cast(u32x4, of[f]));
      if (__any(mk) && !spin_bail(spins, ws, T)) { have = false; sn = s; continue; }
    }
    have = sn < NSEG && __all(nf == T + 1);
    if (have) load_o(sn);
    if (s == 63) TL(3);
    if (ks4 == 0 && cs == 0 && tid == 0) TLX(s * 8 + 4);
#pragma unroll
    for (int tt = 0; tt < 2; ++tt)
#pragma unroll
      for (int j = 0; j < 8; ++j) *(f32x4*)(lds + wv * RW + (tt * 16 + l15) * RS + j * 64 + g * 16) = acc[tt][j];
    asm volatile("s_waitcnt lgkmcnt(0)" ::: "memory");
    __builtin_amdgcn_s_barrier();
    asm volatile("" ::: "memory");
    if (HFP == 0 && tid == 0 && s >= SLATE) set_flag(ws, OFF_HFL + (s * 16 + ks4 * 4 + cs) * 128, T, 1);
#pragma unroll
    for (int i = 0; i < 4; ++i) {
      const int row = i * 8 + (tid >> 5);
      const char* p = lds + row * RS + (tid & 31) * 16;
      const f32x4 a = (*(const f32x4*)p + *(const f32x4*)(p + RW)) + (*(const f32x4*)(p + 2 * RW) + *(const f32x4*)(p + 3 * RW));
      store16_sc1(ws + OFF_HP, (unsigned)(blk(s, (ks4 * 4 + cs) * 2 + (i >> 1), far) + (i & 1) * 4096 + tid * 16), __builtin_bit_cast(u32x4, a));
    }
    if (s < SLATE) {
      asm volatile("s_waitcnt vmcnt(0)" ::: "memory");
      __syncthreads();
      if (tid == 0) set_flag(ws, OFF_HFL + (s * 16 + ks4 * 4 + cs) * 128, T, 1);
    } else {
      if (HFP == 1 && tid == 0) set_flag(ws, OFF_HFL + (s * 16 + ks4 * 4 + cs) * 128, T, 1);
      __builtin_amdgcn_s_barrier();
    }
    if (s == 63) TL(6);
    PT(3, s >= 60); PT(6, s == 48);
    if (ks4 == 0 && cs == 0 && tid == 0) TLX(s * 8 + 5);
    if (turn == 0 && pend >= 0) { ln2(pend, false); pend = -1; }
    if ((wv >> 1) == turn) pend = s;
    turn ^= 1;
  }
  if (pend >= 0) ln2(pend, pend < SLATE);
  PT(4, grp < 4);
  PTE;
}

__device__ __forceinline__ unsigned rkey(unsigned w, int idx) { return ((w ^ (((int)w >> 31) | 0x80000000u)) & 0xffff0000u) | (unsigned)(0xffff - idx); }
__device__ __forceinline__ int key_row(unsigned k) { return (0xffff - (int)(k & 0xffffu)) & 2047; }
__device__ __forceinline__ float key_val(unsigned k) { k &= 0xffff0000u; return __builtin_bit_cast(float, (k & 0x80000000u) ? k ^ 0x80000000u : (~k) & 0xffff0000u); }
__device__ __forceinline__ unsigned wave_umax(unsigned v) {
  v = max(v, (unsigned)__builtin_amdgcn_update_dpp(0, (int)v, 0x121, 0xf, 0xf, false)); v = max(v, (unsigned)__builtin_amdgcn_update_dpp(0, (int)v, 0x122, 0xf, 0xf, false));
  v = max(v, (unsigned)__builtin_amdgcn_update_dpp(0, (int)v, 0x124, 0xf, 0xf, false)); v = max(v, (unsigned)__builtin_amdgcn_update_dpp(0, (int)v, 0x128, 0xf, 0xf, false));
  return max(max((unsigned)__builtin_amdgcn_readlane((int)v, 0), (unsigned)__builtin_amdgcn_readlane((int)v, 16)),
             max((unsigned)__builtin_amdgcn_readlane((int)v, 32), (unsigned)__builtin_amdgcn_readlane((int)v, 48)));
}
__device__ __forceinline__ void push2(unsigned& a, unsigned& b, unsigned k) { b = max(b, min(a, k)); a = max(a, k); }
__device__ __forceinline__ void wave_top2(unsigned& a, unsigned& b) {
  const unsigned m = wave_umax(a);
  b = wave_umax(a == m ? b : a); a = m;
}
__device__ __forceinline__ void top2(char* ws, u64 T, int nrows, int tid, unsigned& k0, unsigned& k1, unsigned* sk) {
  const __amdgpu_buffer_rsrc_t rs = __builtin_amdgcn_make_buffer_rsrc(ws, 0, (int)(250 * MBY), 0x00020000);
  const int lane = tid & 63, wv = tid >> 6;
  const bool use0 = tid * 4 < nrows, use1 = (256 + tid) * 4 < nrows;
  u32x4 c[2];
  unsigned spins = 0;
  if (wv == 0) { wait_flag<12>(ws, (unsigned)OFF_HFL + ((nrows >> 5) - 1) * 2048, T, 1u); __builtin_amdgcn_s_sleep(48); }
  __syncthreads();
  for (;;) {
    c[0] = __builtin_bit_cast(u32x4, __builtin_amdgcn_raw_buffer_load_b128(rs, (int)OFF_RC + (use0 ? tid : 0) * 128, 0, 16));
    c[1] = __builtin_bit_cast(u32x4, __builtin_amdgcn_raw_buffer_load_b128(rs, (int)OFF_RC + (use1 ? 256 + tid : 0) * 128, 0, 16));
    if (__all(c[0].x & c[0].y & c[0].z & c[0].w & c[1].x & c[1].y & c[1].z & c[1].w & 1u)) break;
    __builtin_amdgcn_s_sleep(8);
    if (spin_bail(spins, ws, T)) break;
    asm volatile("" ::: "memory");
  }
  unsigned a = 0, b = 0;
#pragma unroll
  for (int k = 0; k < 2; ++k)
#pragma unroll
    for (int e = 0; e < 4; ++e) { const unsigned w = c[k][e]; push2(a, b, (k == 0 ? use0 : use1) ? rkey(w, (k * 256 + tid) * 4 + e) : 0u); }
  wave_top2(a, b);
  if (lane == 0) { sk[wv * 2] = a; sk[wv * 2 + 1] = b; }
  __syncthreads();
  a = 0; b = 0;
#pragma unroll
  for (int i = 0; i < 8; ++i) push2(a, b, sk[i]);
  k0 = a; k1 = b;
}
__device__ __forceinline__ bool spec_holds(char* ws, u64 T, unsigned xcc, unsigned& k0, unsigned& k1, unsigned* sk, int tid) {
  static_assert(S_ - NSPEC <= 256, "one line per lane");
  if (tid < 64) {
    const __amdgpu_buffer_rsrc_t rs = __builtin_amdgcn_make_buffer_rsrc(ws, 0, (int)(250 * MBY), 0x00020000);
    const bool use = tid < (S_ - NSPEC) / 4;
    u32x4 c;
    unsigned spins = 0;
    for (;;) {
      c = __builtin_bit_cast(u32x4, __builtin_amdgcn_raw_buffer_load_b128(rs, (int)OFF_RC2 + (xcc * 64 + (use ? tid : 0)) * 128, 0, 16));
      if (__all(c.x & c.y & c.z & c.w & 1u)) break;
      __builtin_amdgcn_s_sleep(2);
      if (spin_bail(spins, ws, T)) break;
      asm volatile("" ::: "memory");
    }
    unsigned a = 0, b = 0;
#pragma unroll
    for (int e = 0; e < 4; ++e) { const unsigned w = c[e]; push2(a, b, use ? rkey(w, NSPEC + tid * 4 + e) : 0u); }
    wave_top2(a, b);
    push2(a, b, k0); push2(a, b, k1);
    if (tid == 0) { sk[8] = a; sk[9] = b; }
  }
  __syncthreads();
  const bool same = sk[8] == k0 && sk[9] == k1;
  k0 = sk[8]; k1 = sk[9];
  return same;
}
__device__ __forceinline__ void sel_rows(char* ws, const u16* __restrict__ x, u64 T, unsigned far, int tid, int i0, int i1, float* hs, float* ys, char* yb) {
  const __amdgpu_buffer_rsrc_t rs = __builtin_amdgcn_make_buffer_rsrc(ws, 0, (int)(250 * MBY), 0x00020000);
  const int lane = tid & 63, wv = tid >> 6;
  if (wv < 2) {
    const int row = wv ? i1 : i0;
    for (unsigned spins = 0; __hip_atomic_load((gu32*)(ws + OFF_RC + (row >> 2) * 128 + (row & 3) * 4), RLX_AGENT) == 0u;)
      if (spin_bail(spins, ws, T)) break;
    float f[8], y[8];
    load_h(rs, *(const u32x4*)(x + (size_t)row * DM + lane * 8), row, far, lane, f);
    ln_row(f, y);
#pragma unroll
    for (int i = 0; i < 8; ++i) { hs[wv * DM + lane * 8 + i] = f[i]; ys[wv * DM + lane * 8 + i] = y[i]; }
    u32x4 o; o.x = pk2(y[0], y[1]); o.y = pk2(y[2], y[3]); o.z = pk2(y[4], y[5]); o.w = pk2(y[6], y[7]);
    *(u32x4*)(yb + wv * 1040 + lane * 16) = o;
  }
  __syncthreads();
}
template <int NF>
__device__ __forceinline__ void load_frags(bf16x8 (&w)[NF], const u16* __restrict__ src, int pitch, char* st, int lane) {
  const int l15 = lane & 15, g = lane >> 4, qq = l15 >> 2;
#pragma unroll
  for (int b = 0; b < NF / 2; b += 16) {
    constexpr int NK = 16;
#pragma unroll
    for (int i = 0; i < NK * 2; ++i) {
      if (b * 2 + i < NF) {
        const int r = i * 16 + (lane >> 2);
        __builtin_amdgcn_global_load_lds((const GAS void*)(src + (size_t)(b * 32 + r) * pitch + (((lane & 3) ^ ((r >> 2) & 2)) * 8)), (LAS void*)(st + i * 1024), 16, 0, 0);
      }
    }
    asm volatile("s_waitcnt vmcnt(0)" ::: "memory");
#pragma unroll
    for (int ks = 0; ks < NK; ++ks)
#pragma unroll
      for (int j = 0; j < 2; ++j)
        if ((b + ks) * 2 < NF) {
          const char* p = st + (ks * 32 + g * 8 + qq) * 64 + ((j ^ (g & 1)) * 32) + (l15 & 3) * 8;
          w[(b + ks) * 2 + j] = tr_frag(p, p + 256);
        }
#pragma unroll
    for (int f = 0; f < NK * 2; ++f) if (b * 2 + f < NF) asm volatile("" : "+a"(w[b * 2 + f]));
    asm volatile("s_waitcnt lgkmcnt(0)" ::: "memory");
  }
}

__device__ __forceinline__ void mlp1_phase(char* ws, const u16* __restrict__ x, const u16* __restrict__ wg, const u16* __restrict__ wu, int m, u64 T, unsigned far, char* lds, int tid) {
  unsigned* sk = (unsigned*)lds; float* hs = (float*)(lds + 128); float* xs = hs + 2 * DM; char* yb = (char*)(xs + 2 * DM);
  const int lane = tid & 63, wv = __builtin_amdgcn_readfirstlane(tid >> 6), l15 = lane & 15, g = lane >> 4;
  bf16x8 a[32], b[32];
  load_frags<32>(a, wg + m * 128 + wv * 32, FF, lds + 16384 + wv * 32768, lane);
  load_frags<32>(b, wu + m * 128 + wv * 32, FF, lds + 16384 + wv * 32768, lane);
  unsigned xcc, k0, k1;
  asm volatile("s_getreg_b32 %0, hwreg(HW_REG_XCC_ID)" : "=s"(xcc));
  xcc &= 7u;
#pragma unroll 1
  for (int round = 1; round < 3; ++round) {
    if (round < 2) top2(ws, T, NSPEC, tid, k0, k1, sk);
    const int i0 = key_row(k0), i1 = key_row(k1);
    TL(1);
    sel_rows(ws, x, T, far, tid, i0, i1, hs, xs, yb);
    TL(3);
    bf16x8 xf[16];
#pragma unroll
    for (int ks = 0; ks < 16; ++ks) xf[ks] = *(const bf16x8*)(yb + (l15 & 1) * 1040 + ks * 64 + g * 16);
    f32x4 acc[4];
#pragma unroll
    for (int j = 0; j < 4; ++j) { acc[j] = (f32x4){0.f, 0.f, 0.f, 0.f}; asm volatile("" : "+v"(acc[j])); }
    asm volatile("s_nop 1" ::: "memory");
#pragma unroll
    for (int ks = 0; ks < 16; ++ks)
#pragma unroll
      for (int j = 0; j < 2; ++j) {
        asm volatile("v_mfma_f32_16x16x32_bf16 %0, %1, %2, %0" : "+v"(acc[j]) : "v"(xf[ks]), "a"(a[ks * 2 + j]));
        asm volatile("v_mfma_f32_16x16x32_bf16 %0, %1, %2, %0" : "+v"(acc[2 + j]) : "v"(xf[ks]), "a"(b[ks * 2 + j]));
      }
    asm volatile("s_nop 7\n\ts_nop 3" : "+v"(acc[0]), "+v"(acc[1]), "+v"(acc[2]), "+v"(acc[3]));
    {
      const int t = g >> 1, j = g & 1;
      const float g0 = j ? acc[1][0] : acc[0][0], g1 = j ? acc[1][1] : acc[0][1], u0 = j ? acc[3][0] : acc[2][0], u1 = j ? acc[3][1] : acc[2][1];
      const float gt = rbf(t ? g1 : g0), u = rbf(t ? u1 : u0);
      const float sg = rbf(gt / (1.0f + __expf(-gt)));
      const float av = sg * u;
      u16* ab = (u16*)(lds + 12288) + wv * 64;
      ab[lane] = (u16)pk2(av, av);
      __builtin_amdgcn_fence(__ATOMIC_RELEASE, "wavefront"); __builtin_amdgcn_wave_barrier(); __builtin_amdgcn_fence(__ATOMIC_ACQUIRE, "wavefront");
      store16_sc1(ws + OFF_ACT, (unsigned)(round * 65536 + (lane >> 3) * 8192 + (m * 4 + wv) * 128 + (lane & 7) * 16), *(const u32x4*)(ab + (lane & 7) * 8));
    }
    TL(6);
    if (round == 1 && spec_holds(ws, T, xcc, k0, k1, sk, tid)) break;
  }
}

__device__ __forceinline__ void mlp2_phase(char* ws, const u16* __restrict__ x, const u16* __restrict__ wd, u16* out, int m, u64 T, unsigned far, char* lds, int tid) {
  unsigned* sk = (unsigned*)lds; float* hs = (float*)(lds + 128); float* xs = hs + 2 * DM; char* yb = (char*)(xs + 2 * DM);
  float* part = (float*)(lds + 12288);
  const __amdgpu_buffer_rsrc_t rs = __builtin_amdgcn_make_buffer_rsrc(ws, 0, (int)(250 * MBY), 0x00020000);
  const int lane = tid & 63, wv = __builtin_amdgcn_readfirstlane(tid >> 6), l15 = lane & 15, g = lane >> 4;
  bf16x8 a[28];
  load_frags<28>(a, wd + (size_t)wv * 448 * DM + m * 32, DM, lds + 16384 + wv * 32768, lane);
  unsigned xcc, k0, k1;
  asm volatile("s_getreg_b32 %0, hwreg(HW_REG_XCC_ID)" : "=s"(xcc));
  xcc &= 7u;
#pragma unroll 1
  for (int round = 1; round < 3; ++round) {
    if (round < 2) top2(ws, T, NSPEC, tid, k0, k1, sk);
    const int i0 = key_row(k0), i1 = key_row(k1);
    TL(1);
    sel_rows(ws, x, T, far, tid, i0, i1, hs, xs, yb);
    TL(3);
    u32x4 xf[14];
    unsigned spins = 0;
    __builtin_amdgcn_s_sleep(ASL);
    for (;;) {
      bool ok = true;
#pragma unroll
      for (int ks = 0; ks < 14; ++ks) {
        xf[ks] = __builtin_bit_cast(u32x4, __builtin_amdgcn_raw_buffer_load_b128(rs, (int)OFF_ACT + round * 65536 + xcc * 8192 + (wv * 14 + ks) * 128 + (l15 & 1) * 64 + g * 16, 0, 16));
        ok = ok && xf[ks].x != SENT && xf[ks].y != SENT && xf[ks].z != SENT && xf[ks].w != SENT;
      }
      if (__all(ok)) break;
      __builtin_amdgcn_s_sleep(2);
      if (spin_bail(spins, ws, T)) break;
      asm volatile("" ::: "memory");
    }
    TL(7);
    f32x4 acc[2];
#pragma unroll
    for (int j = 0; j < 2; ++j) { acc[j] = (f32x4){0.f, 0.f, 0.f, 0.f}; asm volatile("" : "+v"(acc[j])); }
    asm volatile("s_nop 1" ::: "memory");
#pragma unroll
    for (int ks = 0; ks < 14; ++ks)
#pragma unroll
      for (int j = 0; j < 2; ++j) asm volatile("v_mfma_f32_16x16x32_bf16 %0, %1, %2, %0" : "+v"(acc[j]) : "v"(xf[ks]), "a"(a[ks * 2 + j]));
    asm volatile("s_nop 7\n\ts_nop 3" : "+v"(acc[0]), "+v"(acc[1]));
    {
      const float e0 = (g & 1) ? acc[1][0] : acc[0][0], e1 = (g & 1) ? acc[1][1] : acc[0][1];
      part[tid] = (g >> 1) ? e1 : e0;
    }
    __syncthreads();
    const int t = (tid >> 4) & 1, cc = (tid & 15) * 2, idx = t ? i1 : i0, col = m * 32 + cc;
    const float r = key_val(t ? k1 : k0);
    const float sg = rbf(1.0f / (1.0f + __expf(-r)));
    float o[2];
#pragma unroll
    for (int e = 0; e < 2; ++e) {
      const int q = t * 32 + cc + e;
      const float y = (part[q] + part[64 + q]) + (part[128 + q] + part[192 + q]);
      const float upd = rbf(rbf(y) * sg);
      o[e] = hs[t * DM + col + e] + rbf(xs[t * DM + col + e] + upd);
    }
    TL(4);
    gu32* op = (gu32*)(out + (size_t)idx * DM + col);
    if (tid < 32) __hip_atomic_store(op, pk2(o[0], o[1]), RLX_AGENT);
    if (round == 2 || spec_holds(ws, T, xcc, k0, k1, sk, tid)) break;
    if (tid < 32) __hip_atomic_store(op, pk2(hs[t * DM + col] + xs[t * DM + col], hs[t * DM + col + 1] + xs[t * DM + col + 1]), RLX_AGENT);
    asm volatile("s_waitcnt vmcnt(0)" ::: "memory");
    __syncthreads();
  }
}

__device__ __forceinline__ void red_phase(char* ws, int b, u64 T, unsigned psi, int lane) {
  unsigned xcc; asm volatile("s_getreg_b32 %0, hwreg(HW_REG_XCC_ID)" : "=s"(xcc));
  const int g = lane >> 4;
  const unsigned own = (psi ^ pgc(xcc & 3u) ^ 5u) * 4096u;
  const unsigned so = (psi ^ pgc(((lane >> 3) & 1) + (lane >> 5) * 2) ^ 1u) * 4096 + (b >> 5) * 32768 + (b & 31) * 128 + ((g & 1) + (lane & 1) * 2 + (lane & 4)) * 16 + (lane & 2) * 4;
  const unsigned io = (unsigned)OFF_G2 + (b >> 2) * 65536 + (psi ^ pgc(b & 3) ^ ((b >> 5) << 2)) * 4096 + lane * 16;
  const unsigned one = (lane & 4) ? 0x3f800000u : 0x3f80u;
  asm volatile(
    "s_mov_b64 s[36:37], %0\n"
    "s_mov_b64 s[38:39], %1\n"
    "v_mov_b32 v31, %2\n"
    "v_mov_b32 v32, %3\n"
    "v_mov_b32 v33, %4\n"
    "v_mov_b32 v34, %5\n"
    "v_mov_b32 v35, %6\n"
    "v_mov_b32 v36, %7\n"
    "v_mov_b32 v37, %8\n"
    "v_mov_b32 v39, %9\n"
    "v_add_u32 v38, 0x10000, v37\n"
    "v_mov_b32 v40, 0xffbfffbf\n"
    "v_mov_b32 v41, 0xffbfffbf\n"
    "v_mov_b32 v42, 0xffbfffbf\n"
    "v_mov_b32 v43, 0xffbfffbf\n"
    "s_or_b32 s38, s38, 1\n"
    "s_mov_b32 s41, 0\n"
    "s_mov_b32 s47, 0\n"
    "s_add_u32 s64, s36, 0x200200\n"
    "s_addc_u32 s65, s37, 0\n"
    "s_getpc_b64 s[60:61]\n"
    "R_pc%=:\n"
    "s_add_u32 s60, s60, R_go%=-R_pc%=\n"
    "s_addc_u32 s61, s61, 0\n"
    "R_step%=:\n"
    "s_and_b32 s70, s41, 3\n"
    "s_lshl_b32 s71, s70, 16\n"
    "s_add_u32 s71, s71, 0x180000\n"
    "s_add_u32 s48, s36, s71\n"
    "s_addc_u32 s49, s37, 0\n"
    "s_add_u32 s71, s70, 1\n"
    "s_and_b32 s71, s71, 3\n"
    "s_lshl_b32 s71, s71, 17\n"
    "s_add_u32 s71, s71, 0x100000\n"
    "s_add_u32 s52, s36, s71\n"
    "s_addc_u32 s53, s37, 0\n"
    "s_xor_b32 s71, s71, 0x40000\n"
    "s_add_u32 s54, s36, s71\n"
    "s_addc_u32 s55, s37, 0\n"
    "s_and_b32 s70, s41, 1\n"
    "s_lshl_b32 s70, s70, 15\n"
    "s_add_u32 s50, s36, s70\n"
    "s_addc_u32 s51, s37, 0\n"
    "s_mov_b32 s45, 0\n"
    "s_mov_b32 s46, 0\n"
    "s_add_u32 s42, s38, s41\n"
    "s_cmp_eq_u32 s41, 0\n"
    "s_cbranch_scc1 R_look%=\n"
    "s_add_u32 s44, s44, 2950\n"
    "R_lookat%=:\n"
    "R_w1%=:\n"
    "s_memtime s[56:57]\n"
    "s_waitcnt lgkmcnt(0)\n"
    "s_sub_u32 s70, s44, s56\n"
    "s_sub_u32 s71, s70, 800\n"
    "s_cmp_lt_u32 s71, 0x2000\n"
    "s_cbranch_scc1 R_w1%=\n"
    "R_look%=:\n"
    "global_load_dwordx4 v[64:67], v39, s[48:49] sc1\n"
    "s_sleep 3\n"
    "s_memtime s[56:57]\n"
    "s_memrealtime s[70:71]\n"
    "s_nop 3\n"
    "s_memrealtime s[72:73]\n"
    "s_nop 3\n"
    "s_memrealtime s[74:75]\n"
    "s_nop 3\n"
    "s_memrealtime s[76:77]\n"
    "s_nop 3\n"
    "s_memrealtime s[78:79]\n"
    "s_nop 3\n"
    "s_memrealtime s[80:81]\n"
    "s_mov_b32 s59, 0\n"
    "s_waitcnt lgkmcnt(0)\n"
    "s_mul_i32 s58, s70, 24\n"
    "s_cmp_eq_u32 s70, s72\n"
    "s_addc_u32 s59, s59, 0\n"
    "s_cmp_eq_u32 s70, s74\n"
    "s_addc_u32 s59, s59, 0\n"
    "s_cmp_eq_u32 s70, s76\n"
    "s_addc_u32 s59, s59, 0\n"
    "s_cmp_eq_u32 s70, s78\n"
    "s_addc_u32 s59, s59, 0\n"
    "s_cmp_eq_u32 s70, s80\n"
    "s_addc_u32 s59, s59, 0\n"
    "s_mul_i32 s59, s59, 20\n"
    "s_sub_u32 s58, s58, s59\n"
    "s_sub_u32 s58, s58, 190\n"
    "s_waitcnt vmcnt(0)\n"
    "v_subrev_u32 v67, s58, v67\n"
    "v_cmp_ne_u64 s[74:75], s[38:39], v[64:65]\n"
    "v_cmp_ne_u32 vcc, s42, v66\n"
    "v_max_i32_dpp v67, v67, v67 row_ror:1 row_mask:0xf bank_mask:0xf\n"
    "s_nop 1\n"
    "v_max_i32_dpp v67, v67, v67 row_ror:2 row_mask:0xf bank_mask:0xf\n"
    "s_nop 1\n"
    "v_max_i32_dpp v67, v67, v67 row_ror:4 row_mask:0xf bank_mask:0xf\n"
    "s_nop 1\n"
    "v_max_i32_dpp v67, v67, v67 row_ror:8 row_mask:0xf bank_mask:0xf\n"
    "s_nop 0\n"
    "v_readlane_b32 s70, v67, 0\n"
    "v_readlane_b32 s71, v67, 16\n"
    "v_readlane_b32 s72, v67, 32\n"
    "v_readlane_b32 s73, v67, 48\n"
    "s_or_b64 vcc, vcc, s[74:75]\n"
    "s_cbranch_scc1 R_notyet%=\n"
    "s_max_i32 s70, s70, s71\n"
    "s_max_i32 s72, s72, s73\n"
    "s_max_i32 s70, s70, s72\n"
    "s_min_i32 s70, s70, 8000\n"
    "s_add_u32 s44, s56, s70\n"
    "s_cmp_gt_i32 s70, 1210\n"
    "s_cbranch_scc1 R_far%=\n"
    "R_timed%=:\n"
    "R_w2%=:\n"
    "s_memtime s[56:57]\n"
    "s_waitcnt lgkmcnt(0)\n"
    "s_sub_u32 s70, s44, s56\n"
    "s_sub_u32 s71, s70, 200\n"
    "s_cmp_lt_u32 s71, 0x2000\n"
    "s_cbranch_scc1 R_w2%=\n"
    "s_sub_i32 s70, s70, 76\n"
    "s_max_i32 s70, s70, 0\n"
    "s_min_i32 s70, s70, 124\n"
    "s_andn2_b32 s70, s70, 3\n"
    "s_sub_u32 s72, s60, s70\n"
    "s_subb_u32 s73, s61, 0\n"
    "s_setpc_b64 s[72:73]\n"
    ".p2align 6\n"
    "s_nop 0\n"
    "s_nop 0\n"
    "s_nop 0\n"
    "s_nop 0\n"
    "s_nop 0\n"
    "s_nop 0\n"
    "s_nop 0\n"
    "s_nop 0\n"
    "s_nop 0\n"
    "s_nop 0\n"
    "s_nop 0\n"
    "s_nop 0\n"
    "s_nop 0\n"
    "s_nop 0\n"
    "s_nop 0\n"
    "s_nop 0\n"
    "s_nop 0\n"
    "s_nop 0\n"
    "s_nop 0\n"
    "s_nop 0\n"
    "s_nop 0\n"
    "s_nop 0\n"
    "s_nop 0\n"
    "s_nop 0\n"
    "s_nop 0\n"
    "s_nop 0\n"
    "s_nop 0\n"
    "s_nop 0\n"
    "s_nop 0\n"
    "s_nop 0\n"
    "s_nop 0\n"
    "s_nop 0\n"
    "R_go%=:\n"
    "global_load_dwordx4 v[48:51], v36, s[50:51] offset:0 sc1\n"
    "global_load_dwordx4 v[52:55], v36, s[50:51] offset:1024 sc1\n"
    "global_load_dwordx4 v[56:59], v36, s[50:51] offset:2048 sc1\n"
    "global_load_dwordx4 v[60:63], v36, s[50:51] offset:3072 sc1\n"
    "s_waitcnt vmcnt(3)\n"
    "v_mfma_f32_16x16x32_bf16 v[44:47], v[32:35], v[48:51], 0\n"
    "s_waitcnt vmcnt(2)\n"
    "v_mfma_f32_16x16x32_bf16 v[44:47], v[32:35], v[52:55], v[44:47]\n"
    "s_waitcnt vmcnt(1)\n"
    "v_mfma_f32_16x16x32_bf16 v[44:47], v[32:35], v[56:59], v[44:47]\n"
    "s_waitcnt vmcnt(0)\n"
    "v_mfma_f32_16x16x32_bf16 v[44:47], v[32:35], v[60:63], v[44:47]\n"
    "s_nop 7\n"
    "v_add_f32_dpp v44, v44, v44 row_ror:8 row_mask:0xf bank_mask:0xf\n"
    "v_add_f32_dpp v45, v45, v45 row_ror:8 row_mask:0xf bank_mask:0xf\n"
    "v_add_f32_dpp v46, v46, v46 row_ror:8 row_mask:0xf bank_mask:0xf\n"
    "v_add_f32_dpp v47, v47, v47 row_ror:8 row_mask:0xf bank_mask:0xf\n"
    "v_cmpx_o_f32 vcc, v44, v45\n"
    "R_pub%=:\n"
    "v_cvt_pk_bf16_f32 v64, v44, v45\n"
    "v_cvt_pk_bf16_f32 v65, v46, v47\n"
    "global_store_dwordx2 v37, v[64:65], s[52:53] sc1\n"
    "global_store_dwordx2 v38, v[64:65], s[52:53] sc1\n"
    "s_memtime s[58:59]\n"
    "s_cmp_eq_u64 exec, -1\n"
    "s_mov_b64 exec, -1\n"
    "s_cbranch_scc0 R_miss%=\n"
    "s_waitcnt lgkmcnt(0)\n"
    "s_cmp_eq_u32 s45, 0\n"
    "s_cbranch_scc1 R_nomiss%=\n"
    "s_add_u32 s48, s48, 0x10000\n"
    "s_andn2_b32 s48, s48, 0x40000\n"
    "s_add_u32 s70, s41, 1\n"
    "s_add_u32 s70, s70, s38\n"
    "v_mov_b32 v66, s70\n"
    "s_mov_b64 exec, 15\n"
    "global_store_dword v31, v66, s[48:49] offset:16 sc1\n"
    "s_mov_b64 exec, -1\n"
    "R_nomiss%=:\n"
    "s_add_u32 s58, s58, 600\n"
    "R_w3%=:\n"
    "s_memtime s[56:57]\n"
    "s_waitcnt lgkmcnt(0)\n"
    "s_sub_u32 s70, s58, s56\n"
    "s_sub_u32 s71, s70, 0\n"
    "s_cmp_lt_u32 s71, 0x2000\n"
    "s_cbranch_scc1 R_w3%=\n"
    "global_store_dwordx2 v37, v[40:41], s[54:55] sc1\n"
    "global_store_dwordx2 v38, v[40:41], s[54:55] sc1\n"
    "global_store_dwordx4 v36, v[40:43], s[50:51] offset:0 sc1\n"
    "global_store_dwordx4 v36, v[40:43], s[50:51] offset:1024 sc1\n"
    "global_store_dwordx4 v36, v[40:43], s[50:51] offset:2048 sc1\n"
    "global_store_dwordx4 v36, v[40:43], s[50:51] offset:3072 sc1\n"
    "s_add_u32 s41, s41, 1\n"
    "s_cmp_lt_u32 s41, 63\n"
    "s_cbranch_scc1 R_step%=\n"
    "s_endpgm\n"
    "R_far%=:\n"
    "s_add_u32 s46, s46, 1\n"
    "s_cmp_lt_u32 s46, 16\n"
    "s_cbranch_scc1 R_lookat%=\n"
    "s_branch R_timed%=\n"
    "R_notyet%=:\n"
    "s_add_u32 s47, s47, 1\n"
    "s_and_b32 s70, s47, 0xff\n"
    "s_cbranch_scc1 R_look%=\n"
    "v_mov_b32 v64, 0\n"
    "global_load_dwordx2 v[66:67], v64, s[64:65] sc1\n"
    "s_sub_u32 s70, s38, 1\n"
    "s_mov_b32 s71, s39\n"
    "s_waitcnt vmcnt(0)\n"
    "v_cmp_eq_u64 vcc, s[70:71], v[66:67]\n"
    "s_nop 1\n"
    "s_cbranch_vccnz R_go%=\n"
    "s_cmp_lt_u32 s47, 0x40000\n"
    "s_cbranch_scc1 R_look%=\n"
    "v_mov_b32 v66, s70\n"
    "v_mov_b32 v67, s71\n"
    "global_store_dwordx2 v64, v[66:67], s[64:65] sc1\n"
    "s_branch R_go%=\n"
    "R_miss%=:\n"
    "s_add_u32 s45, s45, 1\n"
    "s_cmp_eq_u32 s45, 1\n"
    "s_cbranch_scc1 R_go%=\n"
    "s_mov_b64 s[72:73], 0\n"
    "v_cmp_eq_u32 vcc, v40, v48\n"
    "s_nop 0\n"
    "s_or_b64 s[72:73], s[72:73], vcc\n"
    "v_cmp_eq_u32 vcc, v40, v49\n"
    "s_nop 0\n"
    "s_or_b64 s[72:73], s[72:73], vcc\n"
    "v_cmp_eq_u32 vcc, v40, v50\n"
    "s_nop 0\n"
    "s_or_b64 s[72:73], s[72:73], vcc\n"
    "v_cmp_eq_u32 vcc, v40, v51\n"
    "s_nop 0\n"
    "s_or_b64 s[72:73], s[72:73], vcc\n"
    "v_cmp_eq_u32 vcc, v40, v52\n"
    "s_nop 0\n"
    "s_or_b64 s[72:73], s[72:73], vcc\n"
    "v_cmp_eq_u32 vcc, v40, v53\n"
    "s_nop 0\n"
    "s_or_b64 s[72:73], s[72:73], vcc\n"
    "v_cmp_eq_u32 vcc, v40, v54\n"
    "s_nop 0\n"
    "s_or_b64 s[72:73], s[72:73], vcc\n"
    "v_cmp_eq_u32 vcc, v40, v55\n"
    "s_nop 0\n"
    "s_or_b64 s[72:73], s[72:73], vcc\n"
    "v_cmp_eq_u32 vcc, v40, v56\n"
    "s_nop 0\n"
    "s_or_b64 s[72:73], s[72:73], vcc\n"
    "v_cmp_eq_u32 vcc, v40, v57\n"
    "s_nop 0\n"
    "s_or_b64 s[72:73], s[72:73], vcc\n"
    "v_cmp_eq_u32 vcc, v40, v58\n"
    "s_nop 0\n"
    "s_or_b64 s[72:73], s[72:73], vcc\n"
    "v_cmp_eq_u32 vcc, v40, v59\n"
    "s_nop 0\n"
    "s_or_b64 s[72:73], s[72:73], vcc\n"
    "v_cmp_eq_u32 vcc, v40, v60\n"
    "s_nop 0\n"
    "s_or_b64 s[72:73], s[72:73], vcc\n"
    "v_cmp_eq_u32 vcc, v40, v61\n"
    "s_nop 0\n"
    "s_or_b64 s[72:73], s[72:73], vcc\n"
    "v_cmp_eq_u32 vcc, v40, v62\n"
    "s_nop 0\n"
    "s_or_b64 s[72:73], s[72:73], vcc\n"
    "v_cmp_eq_u32 vcc, v40, v63\n"
    "s_nop 0\n"
    "s_or_b64 s[72:73], s[72:73], vcc\n"
    "s_cbranch_scc0 R_pub%=\n"
    "s_add_u32 s47, s47, 1\n"
    "s_and_b32 s70, s47, 0xff\n"
    "s_cbranch_scc1 R_look%=\n"
    "v_mov_b32 v64, 0\n"
    "global_load_dwordx2 v[66:67], v64, s[64:65] sc1\n"
    "s_sub_u32 s70, s38, 1\n"
    "s_mov_b32 s71, s39\n"
    "s_waitcnt vmcnt(0)\n"
    "v_cmp_eq_u64 vcc, s[70:71], v[66:67]\n"
    "s_nop 1\n"
    "s_cbranch_vccnz R_pub%=\n"
    "s_cmp_lt_u32 s47, 0x40000\n"
    "s_cbranch_scc1 R_look%=\n"
    "v_mov_b32 v66, s70\n"
    "v_mov_b32 v67, s71\n"
    "global_store_dwordx2 v64, v[66:67], s[64:65] sc1\n"
    "s_branch R_pub%=\n"
    "mk_code_end:\n"
    :: "s"(ws), "s"(uni(T)), "v"((psi ^ pgc(lane & 3) ^ 5u) * 4096 + b * 64), "v"((lane & 3) == 0 ? one : 0u), "v"((lane & 3) == 1 ? one : 0u), "v"((lane & 3) == 2 ? one : 0u), "v"((lane & 3) == 3 ? one : 0u),
       "v"(io), "v"(so), "v"(own + lane * 64), "s"(ws + 224 * MBY + b * 2048)
    : "memory", "vcc", "scc", "s36", "s37", "s38", "s39", "s40", "s62", "s63", "s99", "v31", "v32", "v33", "v34", "v35", "v36", "v37", "v38", "v39", "v67");
  __builtin_unreachable();
}

__global__ void __launch_bounds__(256, 1) rec_kernel(char* ws, const u16* __restrict__ x, const u16* __restrict__ mem0, const u16* __restrict__ Wq,
                                                     const u16* __restrict__ Wk, const u16* __restrict__ Wv, const u16* __restrict__ Wo,
                                                     const u16* __restrict__ w_r, const u16* __restrict__ wg, const u16* __restrict__ wu,
                                                     const u16* __restrict__ wd, u16* out) {
  __shared__ __attribute__((aligned(1024))) char lds[158720];
  const int tid = threadIdx.x, lane = tid & 63, wv = tid >> 6;
#define PADOP
  TL(0);
  u64 ep0;
  int role;
  {
    unsigned xcc; asm volatile("s_getreg_b32 %0, hwreg(HW_REG_XCC_ID)" : "=s"(xcc));
    role = (int)((blockIdx.x & ~7u) | ((xcc + XROT) & 7u));
  }
  if ((role >> 5) == ((role & 4) ? 0 : 6)) {
    unsigned tmp;
    asm volatile("s_getpc_b64 s[40:41]\n"
                 "mk_pc:\n\t"
                 "s_mov_b32 m0, 0x24000\n\t"
                 "v_min_u32 %0, mk_code_end-mk_pc-4, %1\n\t"
                 "global_load_lds_dword %0, s[40:41]" : "=&v"(tmp) : "v"((((blockIdx.x >> 3) & 3) * 256 + tid) * 128) : "s40", "s41", "memory");
  }
  const int wave = __builtin_amdgcn_readfirstlane(wv);
#pragma unroll 1
  for (int pass = 0;; ++pass) {
    role = __builtin_amdgcn_readfirstlane(role);
    const int bx = role & 7, by = role >> 3;
    const bool red = bx < 4 && by >= 16, att = bx >= 4 && by < 8;
    const char* src = red ? (const char*)(x + (size_t)((by - 16) * 4 + bx) * 32 * DM) : att ? (const char*)(Wo + (size_t)((bx & 3) * 8 + by) * DH * DM)
                    : (const char*)((bx < 4 ? (by < 8 ? Wk : Wq) : (by < 16 ? Wk : (by < 24 ? Wv : Wq))) + ((bx & 3) * 8 + (by & 7)) * DH);
    const unsigned sstep = red || att ? 16384u : 64u * HD * 2u, lstep = red ? 1040u : 1024u;
    unsigned vo[4];
#pragma unroll
    for (int i = 0; i < 4; ++i) {
      const int r = wave * 16 + i * 4 + (lane >> 4), f = (r & 3) | ((bx < 4 ? r : r >> 1) & 4);
      vo[i] = red || att ? (wave * 4 + i) * 1024 + lane * 16 : r * (HD * 2) + (((lane & 15) ^ (f << 1)) * 16);
    }
    if (att) __builtin_amdgcn_s_sleep(ADL);
    if (bx >= 4 && by >= 24) __builtin_amdgcn_s_sleep(QDL);
    if (red && by >= 20 && wave == 3) probe(ws, (by - 16) * 4 + bx, lane);
    unsigned olo, ohi;
#define FD4 "global_load_lds_dwordx4 %3, s[42:43]\n\ts_add_u32 m0, m0, %10\n\ts_nop 0\n\tglobal_load_lds_dwordx4 %4, s[42:43]\n\ts_add_u32 m0, m0, %10\n\ts_nop 0\n\t" \
            "global_load_lds_dwordx4 %5, s[42:43]\n\ts_add_u32 m0, m0, %10\n\ts_nop 0\n\tglobal_load_lds_dwordx4 %6, s[42:43]\n\t" \
            "s_add_u32 m0, m0, s44\n\ts_add_u32 s42, s42, %9\n\ts_addc_u32 s43, s43, 0\n\t"
    asm volatile("s_load_dwordx2 %0, %11, 0x0\n\ts_mov_b64 s[42:43], %7\n\ts_mov_b32 m0, %8\n\ts_mul_i32 s44, %10, 13\n\ts_mov_b32 s45, %13\n\t"
                 FD4 FD4
                 "s_waitcnt lgkmcnt(0)\n\ts_mov_b64 s[46:47], %0\n\ts_add_u32 s46, s46, 1\n\ts_addc_u32 s47, s47, 0\n\ts_lshl_b64 s[46:47], s[46:47], 6\n\t"
                 "v_mov_b32 v40, s46\n\tv_mov_b32 v41, s47\n\tv_mov_b32 v42, 0\n\t"
                 "s_cmp_eq_u32 %14, 0\n\ts_cbranch_scc0 2f\n\t"
                 "s_mov_b64 exec, 1\n\tglobal_atomic_swap_x2 v[40:41], v42, v[40:41], %12 sc0\n\ts_mov_b64 exec, -1\n"
                 "2:\n\ts_cmp_eq_u32 s45, 0\n\ts_cbranch_scc0 1f\n\ts_waitcnt vmcnt(0)\n\ts_branch 3f\n"
                 "1:\n\t" FD4 "s_sub_u32 s45, s45, 1\n\ts_cmp_lg_u32 s45, 0\n\ts_cbranch_scc1 1b\n\ts_waitcnt vmcnt(24)\n"
                 "3:\n\tv_mov_b32 %1, v40\n\tv_mov_b32 %2, v41"
                 : "=&s"(ep0), "=v"(olo), "=v"(ohi)
                 : "v"(vo[0]), "v"(vo[1]), "v"(vo[2]), "v"(vo[3]), "s"(uni((u64)src)), "s"(wave * 4 * lstep), "s"(sstep), "s"(lstep), "s"(ws + OFF_EP), "s"(ws + OFF_CLM + role * 128),
                   "s"(red ? 0 : (att ? ATN : 6)), "s"(wave + pass)
                 : "memory", "scc", "s42", "s43", "s44", "s45", "s46", "s47", "v40", "v41", "v42");
    ep0 = uni(ep0);
    u64 old = ((u64)ohi << 32) | olo;
    int got = role;
    if (tid == 0 && pass == 0) {
      const u64 T0 = tag_of(ep0);
      while (old == T0) { got = (got + 1) & 255; old = __hip_atomic_exchange((gu64*)(ws + OFF_CLM + got * 128), T0, RLX_AGENT); }
      *(int*)(lds + 158704) = got;
    }
    asm volatile("s_waitcnt lgkmcnt(0)\n\ts_barrier" ::: "memory");
    got = __builtin_amdgcn_readfirstlane(*(int*)(lds + 158704));
    if (got == role) break;
    asm volatile("s_waitcnt vmcnt(0)\n\ts_barrier" ::: "memory");
    role = got;
  }
  const int bx = role & 7, by = role >> 3;
  if (bx >= 4 || by >= 16) {
    const int xc = bx & 3;
    if (bx < 4) {
      const int i = (by - 16) * 4 + bx;
      const u64 T = tag_of(ep0);
      const int p = lane >> 4, q = lane & 3;
      unsigned lo = SENT, hi = SENT;
      if (wv == 0) {
        u16 el[4];
#pragma unroll
        for (int k = 0; k < 4; ++k) {
          u16 t = mem0[((i & 1) * 4 + p) * DM + (i >> 1) * 16 + k * 4 + q];
          if ((t & 0x7f80) == 0x7f80 && (t & 0x7f)) t |= 0x40;
          el[k] = t;
        }
        lo = el[0] | ((unsigned)el[1] << 16); hi = el[2] | ((unsigned)el[3] << 16);
      }
      ln1_phase(ws, i, T, lds, tid);
      TL(1);
      const unsigned par = wait_par(ws, T, bx);
      TL(2);
      gu64* gp = (gu64*)(ws + OFF_G3 + wv * 131072 + (par ^ pgc((lane >> 2) & 3) ^ 1u) * 4096 + (i >> 5) * 32768 + (i & 31) * 128 + ((p & 1) + (q & 1) * 2 + (p & 2) * 2) * 16 + (q & 2) * 4);
      __hip_atomic_store(gp, ((u64)hi << 32) | lo, RLX_AGENT);
      __hip_atomic_store(gp + 8192, ((u64)hi << 32) | lo, RLX_AGENT);
#pragma unroll
      for (int k = 0; k < 2; ++k)
        store16_sc1(ws + OFF_G2, (unsigned)(k * 32768 + (i >> 2) * 65536 + (par ^ pgc(i & 3) ^ ((i >> 5) << 2)) * 4096 + tid * 16), (u32x4){SENT, SENT, SENT, SENT});
      if (wv == 1) store16_sc1(ws + OFF_PIL, (unsigned)((i >> 4) * 65536 + (par ^ pgc((i >> 2) & 3) ^ 5u) * 4096 + (i & 3) * 1024 + lane * 16), (u32x4){0u, 0u, 0u, 0u});
      asm volatile("s_waitcnt vmcnt(0)" ::: "memory");
      __syncthreads();
      if (tid == 0) set_flag(ws, OFF_TAB + i * 128, T, 1u + par);
      TL(3);
      if (wv == 0) red_phase(ws, i, T, par, lane);
    } else if (by < 8) {
      if (role == 63) {
        if (wave == 0) decide(ws, tag_of(ep0), (unsigned*)(lds + 0x25000), lane);
        TL(1);
        __syncthreads();
      }
      {
        const unsigned o = (((xc * 8 + by) * 4 + wv) * 64 + lane) * 128;
        __builtin_amdgcn_s_sleep(WDL);
        asm volatile("s_mov_b32 m0, 0x24000\n\ts_nop 0\n\tglobal_load_lds_dword %0, %2\n\t"
                     "global_load_lds_dword %0, %4\n\tglobal_load_lds_dword %7, %4\n\tglobal_load_lds_dword %0, %5\n\tglobal_load_lds_dword %7, %5\n\t"
                     "global_load_lds_dword %0, %6\n\tglobal_load_lds_dword %7, %6\n\t"
                     :: "v"(o), "v"(o + 1048576), "s"(ws + OFF_FLAGS), "s"(out), "s"(wg), "s"(wu), "s"(wd), "v"(min(o + 1048576, (unsigned)(DM * FF * 2 - 128))) : "memory");
      }
      xa_phase(ws, by & 3, (xc * 2 + (by >> 2)) * 4 + wv, tag_of(ep0), (~wait_par(ws, tag_of(ep0), bx) & 2u) << 12, lane);
    } else {
      const int jj = by - 8, h = xc * 8 + (jj & 7), m = (jj & 7) * 4 + xc;
      u64 T;
      if (jj < 8) T = qkv_phase<1>(ws, h, ep0, tid);
      else if (jj < 16) T = qkv_phase<2>(ws, h, ep0, tid);
      else T = qkv_phase<0>(ws, h, ep0, tid);
      TL(2);
      const unsigned far = (~wait_par(ws, T, bx) & 2u) << 12;
      if (jj < 16) {
#pragma unroll
        for (int k = 0; k < 6; ++k)
          store16_sc1(ws + OFF_KM, (unsigned)(blk(52 + k * 2 + (tid >> 7), h, far) + (jj >> 3) * 4096 + (tid & 127) * 16), (u32x4){SENT, SENT, SENT, SENT});
        asm volatile("s_waitcnt vmcnt(0)" ::: "memory");
        __syncthreads();
        if (tid == 0) set_flag(ws, OFF_RDY + (32 + (jj >> 3) * 32 + h) * 128, T, 33);
      }
      op_phase(ws, x, w_r, out, Wo, jj >> 2, xc, jj & 3, T, far, lds, tid);
      if (jj >= 16) __syncthreads();
      if (jj < 16) {}
      else if (m < 14) mlp1_phase(ws, x, wg, wu, m, T, far, lds, tid);
      else if (m >= 16) {
        mlp2_phase(ws, x, wd, out, m - 16, T, far, lds, tid);
        if (m == 31 && tid == 0) __hip_atomic_store((gu64*)(ws + OFF_EP), T >> 6, RLX_AGENT);
      }
    }
    TL(5);
    return;
  }
  const unsigned ldsb = (unsigned)(size_t)(LAS char*)lds;
  asm volatile(
    "s_mov_b64 s[36:37], %0\n"
    "s_mov_b32 s38, %1\n"
    "s_mov_b32 s39, %2\n"
    "s_mov_b32 s40, %3\n"
    "s_load_dwordx2 s[96:97], %4, 0x0\n"
    "s_mov_b64 s[22:23], %5\n"
    "s_mov_b64 s[24:25], %6\n"
    "s_mov_b64 s[26:27], %7\n"
    "s_mov_b64 s[28:29], %8\n"
    "s_mov_b64 exec, -1\n"
    "s_mov_b32 s90, 0\n"
    "s_getreg_b32 s10, hwreg(HW_REG_XCC_ID)\n"
    "s_and_b32 s41, s38, 3\n"
    "s_lshl_b32 s41, s41, 3\n"
    "s_bfe_u32 s42, s38, 0x30002\n"
    "s_add_u32 s41, s41, s42\n"
    "s_lshr_b32 s42, s38, 5\n"
    "v_mbcnt_lo_u32_b32 v0, -1, 0\n"
    "v_mbcnt_hi_u32_b32 v0, -1, v0\n"
    "v_and_b32 v1, 15, v0\n"
    "v_lshrrev_b32 v2, 4, v0\n"
    "v_lshlrev_b32 v71, 4, v0\n"
    "s_and_b32 s11, s10, 7\n"
    "s_add_u32 s68, s36, 0x200200\n"
    "s_addc_u32 s69, s37, 0\n"
    "s_lshl_b32 s78, s39, 4\n"
    "v_add_u32 v8, s78, v2\n"
    "v_lshlrev_b32 v12, 1, v2\n"
    "v_xor_b32 v12, v12, v1\n"
    "v_and_b32 v9, 1, v2\n"
    "v_lshlrev_b32 v9, 1, v9\n"
    "v_xor_b32 v9, v9, v1\n"
    "s_and_b32 s79, s39, 1\n"
    "s_lshl_b32 s79, s79, 3\n"
    "v_add_u32 v10, 0, v8\n"
    "v_xor_b32 v11, 0, v12\n"
    "v_lshlrev_b32 v11, 4, v11\n"
    "v_lshl_add_u32 v84, v10, 13, v11\n"
    "v_xor_b32 v11, 0, v9\n"
    "v_xor_b32 v11, s79, v11\n"
    "v_lshlrev_b32 v11, 4, v11\n"
    "v_lshl_add_u32 v88, v10, 10, v11\n"
    "v_add_u32 v10, 4, v8\n"
    "v_xor_b32 v11, 8, v12\n"
    "v_lshlrev_b32 v11, 4, v11\n"
    "v_lshl_add_u32 v85, v10, 13, v11\n"
    "v_xor_b32 v11, 4, v9\n"
    "v_xor_b32 v11, s79, v11\n"
    "v_lshlrev_b32 v11, 4, v11\n"
    "v_lshl_add_u32 v89, v10, 10, v11\n"
    "v_add_u32 v10, 8, v8\n"
    "v_xor_b32 v11, 0, v12\n"
    "v_lshlrev_b32 v11, 4, v11\n"
    "v_lshl_add_u32 v86, v10, 13, v11\n"
    "v_xor_b32 v11, 0, v9\n"
    "v_xor_b32 v11, s79, v11\n"
    "v_lshlrev_b32 v11, 4, v11\n"
    "v_lshl_add_u32 v90, v10, 10, v11\n"
    "v_add_u32 v10, 12, v8\n"
    "v_xor_b32 v11, 8, v12\n"
    "v_lshlrev_b32 v11, 4, v11\n"
    "v_lshl_add_u32 v87, v10, 13, v11\n"
    "v_xor_b32 v11, 4, v9\n"
    "v_xor_b32 v11, s79, v11\n"
    "v_lshlrev_b32 v11, 4, v11\n"
    "v_lshl_add_u32 v91, v10, 10, v11\n"
    "s_lshl_b32 s78, s41, 8\n"
    "s_add_u32 s22, s22, s78\n"
    "s_addc_u32 s23, s23, 0\n"
    "s_add_u32 s24, s24, s78\n"
    "s_addc_u32 s25, s25, 0\n"
    "s_add_u32 s26, s26, s78\n"
    "s_addc_u32 s27, s27, 0\n"
    "s_lshl_b32 s78, s41, 17\n"
    "s_lshl_b32 s79, s42, 9\n"
    "s_add_u32 s78, s78, s79\n"
    "s_add_u32 s28, s28, s78\n"
    "s_addc_u32 s29, s29, 0\n"
    "s_lshl_b32 s30, s39, 12\n"
    "s_add_u32 s30, s30, s40\n"
    "v_lshrrev_b32 v8, 2, v1\n"
    "v_and_b32 v9, 1, v8\n"
    "v_lshrrev_b32 v10, 1, v8\n"
    "v_and_b32 v11, 1, v2\n"
    "v_and_b32 v12, 3, v1\n"
    "v_lshl_add_u32 v14, v11, 1, v10\n"
    "v_lshl_add_u32 v14, v9, 2, v14\n"
    "v_lshrrev_b32 v13, 1, v2\n"
    "v_lshl_add_u32 v13, v13, 4, v14\n"
    "v_lshlrev_b32 v13, 8, v13\n"
    "v_add_u32 v13, s40, v13\n"
    "v_lshlrev_b32 v14, 5, v14\n"
    "s_lshl_b32 s78, s39, 6\n"
    "v_lshl_add_u32 v15, v12, 3, s78\n"
    "v_xor_b32 v15, v15, v14\n"
    "v_add_u32 v96, v15, v13\n"
    "v_xor_b32 v15, 32, v15\n"
    "v_add_u32 v97, v15, v13\n"
    "s_lshr_b32 s78, s39, 1\n"
    "s_lshl_b32 s78, s78, 7\n"
    "s_and_b32 s79, s39, 1\n"
    "s_lshl_b32 s79, s79, 4\n"
    "s_add_u32 s78, s78, s79\n"
    "v_and_b32 v15, 1, v12\n"
    "v_lshl_add_u32 v15, v15, 5, s78\n"
    "v_lshrrev_b32 v16, 1, v12\n"
    "v_lshl_add_u32 v15, v16, 3, v15\n"
    "v_xor_b32 v15, v15, v14\n"
    "v_add_u32 v92, v15, v13\n"
    "v_xor_b32 v15, 64, v15\n"
    "v_add_u32 v93, v15, v13\n"
    "v_add_u32 v98, 0x10000, v96\n"
    "v_add_u32 v99, 0x10000, v97\n"
    "v_add_u32 v94, 0x10000, v92\n"
    "v_add_u32 v95, 0x10000, v93\n"
    "v_lshl_add_u32 v13, v2, 2, v9\n"
    "v_lshl_add_u32 v13, v10, 4, v13\n"
    "v_lshl_add_u32 v14, v11, 1, v9\n"
    "v_lshl_add_u32 v14, v10, 2, v14\n"
    "v_xor_b32 v14, v14, v12\n"
    "v_lshlrev_b32 v14, 1, v14\n"
    "s_and_b32 s78, s39, 1\n"
    "s_lshl_b32 s78, s78, 3\n"
    "v_xor_b32 v14, s78, v14\n"
    "v_lshlrev_b32 v14, 4, v14\n"
    "v_lshl_add_u32 v14, v13, 8, v14\n"
    "s_lshr_b32 s78, s39, 1\n"
    "s_lshl_b32 s78, s78, 14\n"
    "s_add_u32 s78, s78, 0x10000\n"
    "v_add_u32 v104, s78, v14\n"
    "s_mul_i32 s78, s39, 34816\n"
    "s_add_u32 s78, s78, 18688\n"
    "v_add_u32 v116, s78, v71\n"
    "s_cmp_lg_u32 s42, 0\n"
    "s_cbranch_scc1 L_rb0%=\n"
    "ds_read_b64_tr_b16 a[0:1], v92 offset:0\n"
    "ds_read_b64_tr_b16 a[2:3], v92 offset:2048\n"
    "ds_read_b64_tr_b16 a[4:5], v92 offset:8192\n"
    "ds_read_b64_tr_b16 a[6:7], v92 offset:10240\n"
    "ds_read_b64_tr_b16 a[64:65], v93 offset:0\n"
    "ds_read_b64_tr_b16 a[66:67], v93 offset:2048\n"
    "ds_read_b64_tr_b16 a[68:69], v93 offset:8192\n"
    "ds_read_b64_tr_b16 a[70:71], v93 offset:10240\n"
    "s_waitcnt vmcnt(24) lgkmcnt(0)\n"
    "s_barrier\n"
    "s_add_u32 s32, s26, 0x0\n"
    "s_addc_u32 s33, s27, 0\n"
    "s_add_u32 m0, s30, 0\n"
    "s_nop 0\n"
    "global_load_lds_dwordx4 v84, s[32:33]\n"
    "s_add_u32 m0, s30, 1024\n"
    "s_nop 0\n"
    "global_load_lds_dwordx4 v85, s[32:33]\n"
    "s_add_u32 m0, s30, 2048\n"
    "s_nop 0\n"
    "global_load_lds_dwordx4 v86, s[32:33]\n"
    "s_add_u32 m0, s30, 3072\n"
    "s_nop 0\n"
    "global_load_lds_dwordx4 v87, s[32:33]\n"
    "ds_read_b64_tr_b16 a[8:9], v92 offset:16384\n"
    "ds_read_b64_tr_b16 a[10:11], v92 offset:18432\n"
    "ds_read_b64_tr_b16 a[12:13], v92 offset:24576\n"
    "ds_read_b64_tr_b16 a[14:15], v92 offset:26624\n"
    "ds_read_b64_tr_b16 a[72:73], v93 offset:16384\n"
    "ds_read_b64_tr_b16 a[74:75], v93 offset:18432\n"
    "ds_read_b64_tr_b16 a[76:77], v93 offset:24576\n"
    "ds_read_b64_tr_b16 a[78:79], v93 offset:26624\n"
    "s_waitcnt vmcnt(24) lgkmcnt(0)\n"
    "s_barrier\n"
    "s_add_u32 s32, s26, 0x80000\n"
    "s_addc_u32 s33, s27, 0\n"
    "s_add_u32 m0, s30, 16384\n"
    "s_nop 0\n"
    "global_load_lds_dwordx4 v84, s[32:33]\n"
    "s_add_u32 m0, s30, 17408\n"
    "s_nop 0\n"
    "global_load_lds_dwordx4 v85, s[32:33]\n"
    "s_add_u32 m0, s30, 18432\n"
    "s_nop 0\n"
    "global_load_lds_dwordx4 v86, s[32:33]\n"
    "s_add_u32 m0, s30, 19456\n"
    "s_nop 0\n"
    "global_load_lds_dwordx4 v87, s[32:33]\n"
    "ds_read_b64_tr_b16 a[16:17], v92 offset:32768\n"
    "ds_read_b64_tr_b16 a[18:19], v92 offset:34816\n"
    "ds_read_b64_tr_b16 a[20:21], v92 offset:40960\n"
    "ds_read_b64_tr_b16 a[22:23], v92 offset:43008\n"
    "ds_read_b64_tr_b16 a[80:81], v93 offset:32768\n"
    "ds_read_b64_tr_b16 a[82:83], v93 offset:34816\n"
    "ds_read_b64_tr_b16 a[84:85], v93 offset:40960\n"
    "ds_read_b64_tr_b16 a[86:87], v93 offset:43008\n"
    "s_waitcnt vmcnt(24) lgkmcnt(0)\n"
    "s_barrier\n"
    "s_add_u32 s32, s26, 0x100000\n"
    "s_addc_u32 s33, s27, 0\n"
    "s_add_u32 m0, s30, 32768\n"
    "s_nop 0\n"
    "global_load_lds_dwordx4 v84, s[32:33]\n"
    "s_add_u32 m0, s30, 33792\n"
    "s_nop 0\n"
    "global_load_lds_dwordx4 v85, s[32:33]\n"
    "s_add_u32 m0, s30, 34816\n"
    "s_nop 0\n"
    "global_load_lds_dwordx4 v86, s[32:33]\n"
    "s_add_u32 m0, s30, 35840\n"
    "s_nop 0\n"
    "global_load_lds_dwordx4 v87, s[32:33]\n"
    "ds_read_b64_tr_b16 a[24:25], v92 offset:49152\n"
    "ds_read_b64_tr_b16 a[26:27], v92 offset:51200\n"
    "ds_read_b64_tr_b16 a[28:29], v92 offset:57344\n"
    "ds_read_b64_tr_b16 a[30:31], v92 offset:59392\n"
    "ds_read_b64_tr_b16 a[88:89], v93 offset:49152\n"
    "ds_read_b64_tr_b16 a[90:91], v93 offset:51200\n"
    "ds_read_b64_tr_b16 a[92:93], v93 offset:57344\n"
    "ds_read_b64_tr_b16 a[94:95], v93 offset:59392\n"
    "s_waitcnt vmcnt(24) lgkmcnt(0)\n"
    "s_barrier\n"
    "s_add_u32 s32, s26, 0x180000\n"
    "s_addc_u32 s33, s27, 0\n"
    "s_add_u32 m0, s30, 49152\n"
    "s_nop 0\n"
    "global_load_lds_dwordx4 v84, s[32:33]\n"
    "s_add_u32 m0, s30, 50176\n"
    "s_nop 0\n"
    "global_load_lds_dwordx4 v85, s[32:33]\n"
    "s_add_u32 m0, s30, 51200\n"
    "s_nop 0\n"
    "global_load_lds_dwordx4 v86, s[32:33]\n"
    "s_add_u32 m0, s30, 52224\n"
    "s_nop 0\n"
    "global_load_lds_dwordx4 v87, s[32:33]\n"
    "ds_read_b64_tr_b16 a[32:33], v94 offset:0\n"
    "ds_read_b64_tr_b16 a[34:35], v94 offset:2048\n"
    "ds_read_b64_tr_b16 a[36:37], v94 offset:8192\n"
    "ds_read_b64_tr_b16 a[38:39], v94 offset:10240\n"
    "ds_read_b64_tr_b16 a[96:97], v95 offset:0\n"
    "ds_read_b64_tr_b16 a[98:99], v95 offset:2048\n"
    "ds_read_b64_tr_b16 a[100:101], v95 offset:8192\n"
    "ds_read_b64_tr_b16 a[102:103], v95 offset:10240\n"
    "s_waitcnt vmcnt(24) lgkmcnt(0)\n"
    "s_barrier\n"
    "s_add_u32 s32, s28, 0x0\n"
    "s_addc_u32 s33, s29, 0\n"
    "s_add_u32 m0, s30, 65536\n"
    "s_nop 0\n"
    "global_load_lds_dwordx4 v88, s[32:33]\n"
    "s_add_u32 m0, s30, 66560\n"
    "s_nop 0\n"
    "global_load_lds_dwordx4 v89, s[32:33]\n"
    "s_add_u32 m0, s30, 67584\n"
    "s_nop 0\n"
    "global_load_lds_dwordx4 v90, s[32:33]\n"
    "s_add_u32 m0, s30, 68608\n"
    "s_nop 0\n"
    "global_load_lds_dwordx4 v91, s[32:33]\n"
    "ds_read_b64_tr_b16 a[40:41], v94 offset:16384\n"
    "ds_read_b64_tr_b16 a[42:43], v94 offset:18432\n"
    "ds_read_b64_tr_b16 a[44:45], v94 offset:24576\n"
    "ds_read_b64_tr_b16 a[46:47], v94 offset:26624\n"
    "ds_read_b64_tr_b16 a[104:105], v95 offset:16384\n"
    "ds_read_b64_tr_b16 a[106:107], v95 offset:18432\n"
    "ds_read_b64_tr_b16 a[108:109], v95 offset:24576\n"
    "ds_read_b64_tr_b16 a[110:111], v95 offset:26624\n"
    "s_waitcnt vmcnt(24) lgkmcnt(0)\n"
    "s_barrier\n"
    "s_add_u32 s32, s28, 0x100\n"
    "s_addc_u32 s33, s29, 0\n"
    "s_add_u32 m0, s30, 81920\n"
    "s_nop 0\n"
    "global_load_lds_dwordx4 v88, s[32:33]\n"
    "s_add_u32 m0, s30, 82944\n"
    "s_nop 0\n"
    "global_load_lds_dwordx4 v89, s[32:33]\n"
    "s_add_u32 m0, s30, 83968\n"
    "s_nop 0\n"
    "global_load_lds_dwordx4 v90, s[32:33]\n"
    "s_add_u32 m0, s30, 84992\n"
    "s_nop 0\n"
    "global_load_lds_dwordx4 v91, s[32:33]\n"
    "ds_read_b64_tr_b16 a[48:49], v94 offset:32768\n"
    "ds_read_b64_tr_b16 a[50:51], v94 offset:34816\n"
    "ds_read_b64_tr_b16 a[52:53], v94 offset:40960\n"
    "ds_read_b64_tr_b16 a[54:55], v94 offset:43008\n"
    "ds_read_b64_tr_b16 a[112:113], v95 offset:32768\n"
    "ds_read_b64_tr_b16 a[114:115], v95 offset:34816\n"
    "ds_read_b64_tr_b16 a[116:117], v95 offset:40960\n"
    "ds_read_b64_tr_b16 a[118:119], v95 offset:43008\n"
    "s_waitcnt vmcnt(24) lgkmcnt(0)\n"
    "s_barrier\n"
    "s_add_u32 s32, s28, 0x10000\n"
    "s_addc_u32 s33, s29, 0\n"
    "s_add_u32 m0, s30, 98304\n"
    "s_nop 0\n"
    "global_load_lds_dwordx4 v88, s[32:33]\n"
    "s_add_u32 m0, s30, 99328\n"
    "s_nop 0\n"
    "global_load_lds_dwordx4 v89, s[32:33]\n"
    "s_add_u32 m0, s30, 100352\n"
    "s_nop 0\n"
    "global_load_lds_dwordx4 v90, s[32:33]\n"
    "s_add_u32 m0, s30, 101376\n"
    "s_nop 0\n"
    "global_load_lds_dwordx4 v91, s[32:33]\n"
    "ds_read_b64_tr_b16 a[56:57], v94 offset:49152\n"
    "ds_read_b64_tr_b16 a[58:59], v94 offset:51200\n"
    "ds_read_b64_tr_b16 a[60:61], v94 offset:57344\n"
    "ds_read_b64_tr_b16 a[62:63], v94 offset:59392\n"
    "ds_read_b64_tr_b16 a[120:121], v95 offset:49152\n"
    "ds_read_b64_tr_b16 a[122:123], v95 offset:51200\n"
    "ds_read_b64_tr_b16 a[124:125], v95 offset:57344\n"
    "ds_read_b64_tr_b16 a[126:127], v95 offset:59392\n"
    "s_branch L_rc0%=\n"
    "L_rb0%=:\n"
    "ds_read_b64_tr_b16 v[128:129], v92 offset:0\n"
    "ds_read_b64_tr_b16 v[130:131], v92 offset:2048\n"
    "ds_read_b64_tr_b16 v[132:133], v92 offset:8192\n"
    "ds_read_b64_tr_b16 v[134:135], v92 offset:10240\n"
    "ds_read_b64_tr_b16 v[136:137], v93 offset:0\n"
    "ds_read_b64_tr_b16 v[138:139], v93 offset:2048\n"
    "ds_read_b64_tr_b16 v[140:141], v93 offset:8192\n"
    "ds_read_b64_tr_b16 v[142:143], v93 offset:10240\n"
    "s_waitcnt vmcnt(24) lgkmcnt(0)\n"
    "s_barrier\n"
    "s_add_u32 s32, s26, 0x200000\n"
    "s_addc_u32 s33, s27, 0\n"
    "s_add_u32 m0, s30, 0\n"
    "s_nop 0\n"
    "global_load_lds_dwordx4 v84, s[32:33]\n"
    "s_add_u32 m0, s30, 1024\n"
    "s_nop 0\n"
    "global_load_lds_dwordx4 v85, s[32:33]\n"
    "s_add_u32 m0, s30, 2048\n"
    "s_nop 0\n"
    "global_load_lds_dwordx4 v86, s[32:33]\n"
    "s_add_u32 m0, s30, 3072\n"
    "s_nop 0\n"
    "global_load_lds_dwordx4 v87, s[32:33]\n"
    "ds_read_b64_tr_b16 v[144:145], v92 offset:16384\n"
    "ds_read_b64_tr_b16 v[146:147], v92 offset:18432\n"
    "ds_read_b64_tr_b16 v[148:149], v92 offset:24576\n"
    "ds_read_b64_tr_b16 v[150:151], v92 offset:26624\n"
    "ds_read_b64_tr_b16 v[152:153], v93 offset:16384\n"
    "ds_read_b64_tr_b16 v[154:155], v93 offset:18432\n"
    "ds_read_b64_tr_b16 v[156:157], v93 offset:24576\n"
    "ds_read_b64_tr_b16 v[158:159], v93 offset:26624\n"
    "s_waitcnt vmcnt(24) lgkmcnt(0)\n"
    "s_barrier\n"
    "s_add_u32 s32, s26, 0x280000\n"
    "s_addc_u32 s33, s27, 0\n"
    "s_add_u32 m0, s30, 16384\n"
    "s_nop 0\n"
    "global_load_lds_dwordx4 v84, s[32:33]\n"
    "s_add_u32 m0, s30, 17408\n"
    "s_nop 0\n"
    "global_load_lds_dwordx4 v85, s[32:33]\n"
    "s_add_u32 m0, s30, 18432\n"
    "s_nop 0\n"
    "global_load_lds_dwordx4 v86, s[32:33]\n"
    "s_add_u32 m0, s30, 19456\n"
    "s_nop 0\n"
    "global_load_lds_dwordx4 v87, s[32:33]\n"
    "ds_read_b64_tr_b16 v[160:161], v92 offset:32768\n"
    "ds_read_b64_tr_b16 v[162:163], v92 offset:34816\n"
    "ds_read_b64_tr_b16 v[164:165], v92 offset:40960\n"
    "ds_read_b64_tr_b16 v[166:167], v92 offset:43008\n"
    "ds_read_b64_tr_b16 v[168:169], v93 offset:32768\n"
    "ds_read_b64_tr_b16 v[170:171], v93 offset:34816\n"
    "ds_read_b64_tr_b16 v[172:173], v93 offset:40960\n"
    "ds_read_b64_tr_b16 v[174:175], v93 offset:43008\n"
    "s_waitcnt vmcnt(24) lgkmcnt(0)\n"
    "s_barrier\n"
    "s_add_u32 s32, s26, 0x300000\n"
    "s_addc_u32 s33, s27, 0\n"
    "s_add_u32 m0, s30, 32768\n"
    "s_nop 0\n"
    "global_load_lds_dwordx4 v84, s[32:33]\n"
    "s_add_u32 m0, s30, 33792\n"
    "s_nop 0\n"
    "global_load_lds_dwordx4 v85, s[32:33]\n"
    "s_add_u32 m0, s30, 34816\n"
    "s_nop 0\n"
    "global_load_lds_dwordx4 v86, s[32:33]\n"
    "s_add_u32 m0, s30, 35840\n"
    "s_nop 0\n"
    "global_load_lds_dwordx4 v87, s[32:33]\n"
    "ds_read_b64_tr_b16 v[176:177], v92 offset:49152\n"
    "ds_read_b64_tr_b16 v[178:179], v92 offset:51200\n"
    "ds_read_b64_tr_b16 v[180:181], v92 offset:57344\n"
    "ds_read_b64_tr_b16 v[182:183], v92 offset:59392\n"
    "ds_read_b64_tr_b16 v[184:185], v93 offset:49152\n"
    "ds_read_b64_tr_b16 v[186:187], v93 offset:51200\n"
    "ds_read_b64_tr_b16 v[188:189], v93 offset:57344\n"
    "ds_read_b64_tr_b16 v[190:191], v93 offset:59392\n"
    "s_waitcnt vmcnt(24) lgkmcnt(0)\n"
    "s_barrier\n"
    "s_add_u32 s32, s26, 0x380000\n"
    "s_addc_u32 s33, s27, 0\n"
    "s_add_u32 m0, s30, 49152\n"
    "s_nop 0\n"
    "global_load_lds_dwordx4 v84, s[32:33]\n"
    "s_add_u32 m0, s30, 50176\n"
    "s_nop 0\n"
    "global_load_lds_dwordx4 v85, s[32:33]\n"
    "s_add_u32 m0, s30, 51200\n"
    "s_nop 0\n"
    "global_load_lds_dwordx4 v86, s[32:33]\n"
    "s_add_u32 m0, s30, 52224\n"
    "s_nop 0\n"
    "global_load_lds_dwordx4 v87, s[32:33]\n"
    "ds_read_b64_tr_b16 v[192:193], v94 offset:0\n"
    "ds_read_b64_tr_b16 v[194:195], v94 offset:2048\n"
    "ds_read_b64_tr_b16 v[196:197], v94 offset:8192\n"
    "ds_read_b64_tr_b16 v[198:199], v94 offset:10240\n"
    "ds_read_b64_tr_b16 v[200:201], v95 offset:0\n"
    "ds_read_b64_tr_b16 v[202:203], v95 offset:2048\n"
    "ds_read_b64_tr_b16 v[204:205], v95 offset:8192\n"
    "ds_read_b64_tr_b16 v[206:207], v95 offset:10240\n"
    "s_waitcnt vmcnt(24) lgkmcnt(0)\n"
    "s_barrier\n"
    "s_add_u32 s32, s28, 0x0\n"
    "s_addc_u32 s33, s29, 0\n"
    "s_add_u32 m0, s30, 65536\n"
    "s_nop 0\n"
    "global_load_lds_dwordx4 v88, s[32:33]\n"
    "s_add_u32 m0, s30, 66560\n"
    "s_nop 0\n"
    "global_load_lds_dwordx4 v89, s[32:33]\n"
    "s_add_u32 m0, s30, 67584\n"
    "s_nop 0\n"
    "global_load_lds_dwordx4 v90, s[32:33]\n"
    "s_add_u32 m0, s30, 68608\n"
    "s_nop 0\n"
    "global_load_lds_dwordx4 v91, s[32:33]\n"
    "ds_read_b64_tr_b16 v[208:209], v94 offset:16384\n"
    "ds_read_b64_tr_b16 v[210:211], v94 offset:18432\n"
    "ds_read_b64_tr_b16 v[212:213], v94 offset:24576\n"
    "ds_read_b64_tr_b16 v[214:215], v94 offset:26624\n"
    "ds_read_b64_tr_b16 v[216:217], v95 offset:16384\n"
    "ds_read_b64_tr_b16 v[218:219], v95 offset:18432\n"
    "ds_read_b64_tr_b16 v[220:221], v95 offset:24576\n"
    "ds_read_b64_tr_b16 v[222:223], v95 offset:26624\n"
    "s_waitcnt vmcnt(24) lgkmcnt(0)\n"
    "s_barrier\n"
    "s_add_u32 s32, s28, 0x100\n"
    "s_addc_u32 s33, s29, 0\n"
    "s_add_u32 m0, s30, 81920\n"
    "s_nop 0\n"
    "global_load_lds_dwordx4 v88, s[32:33]\n"
    "s_add_u32 m0, s30, 82944\n"
    "s_nop 0\n"
    "global_load_lds_dwordx4 v89, s[32:33]\n"
    "s_add_u32 m0, s30, 83968\n"
    "s_nop 0\n"
    "global_load_lds_dwordx4 v90, s[32:33]\n"
    "s_add_u32 m0, s30, 84992\n"
    "s_nop 0\n"
    "global_load_lds_dwordx4 v91, s[32:33]\n"
    "ds_read_b64_tr_b16 v[224:225], v94 offset:32768\n"
    "ds_read_b64_tr_b16 v[226:227], v94 offset:34816\n"
    "ds_read_b64_tr_b16 v[228:229], v94 offset:40960\n"
    "ds_read_b64_tr_b16 v[230:231], v94 offset:43008\n"
    "ds_read_b64_tr_b16 v[232:233], v95 offset:32768\n"
    "ds_read_b64_tr_b16 v[234:235], v95 offset:34816\n"
    "ds_read_b64_tr_b16 v[236:237], v95 offset:40960\n"
    "ds_read_b64_tr_b16 v[238:239], v95 offset:43008\n"
    "s_waitcnt vmcnt(24) lgkmcnt(0)\n"
    "s_barrier\n"
    "s_add_u32 s32, s28, 0x10000\n"
    "s_addc_u32 s33, s29, 0\n"
    "s_add_u32 m0, s30, 98304\n"
    "s_nop 0\n"
    "global_load_lds_dwordx4 v88, s[32:33]\n"
    "s_add_u32 m0, s30, 99328\n"
    "s_nop 0\n"
    "global_load_lds_dwordx4 v89, s[32:33]\n"
    "s_add_u32 m0, s30, 100352\n"
    "s_nop 0\n"
    "global_load_lds_dwordx4 v90, s[32:33]\n"
    "s_add_u32 m0, s30, 101376\n"
    "s_nop 0\n"
    "global_load_lds_dwordx4 v91, s[32:33]\n"
    "ds_read_b64_tr_b16 v[240:241], v94 offset:49152\n"
    "ds_read_b64_tr_b16 v[242:243], v94 offset:51200\n"
    "ds_read_b64_tr_b16 v[244:245], v94 offset:57344\n"
    "ds_read_b64_tr_b16 v[246:247], v94 offset:59392\n"
    "ds_read_b64_tr_b16 v[248:249], v95 offset:49152\n"
    "ds_read_b64_tr_b16 v[250:251], v95 offset:51200\n"
    "ds_read_b64_tr_b16 v[252:253], v95 offset:57344\n"
    "ds_read_b64_tr_b16 v[254:255], v95 offset:59392\n"
    "L_rc0%=:\n"
    "s_mov_b32 s70, 0xcccccccc\n"
    "s_mov_b32 s71, 0xcccccccc\n"
    "s_mov_b32 s50, 0xffbfffbf\n"
    "s_mov_b32 s72, 0x33333333\n"
    "s_mov_b32 s73, 0x0\n"
    "s_mov_b32 s74, 0x0\n"
    "s_mov_b32 s75, 0x33333333\n"
    "v_mov_b32 v9, 0x4444\n"
    "v_mov_b32 v10, 0xeeee\n"
    "v_cndmask_b32 v67, v9, v10, s[70:71]\n"
    "s_mov_b32 s76, 0x3e0293ee\n"
    "s_mov_b32 s77, 0x3e0293ee\n"
    "v_mov_b32 v80, 0x3f803f80\n"
    "v_mov_b32 v81, 0x3f803f80\n"
    "v_mov_b32 v82, 0x3f803f80\n"
    "v_mov_b32 v83, 0x3f803f80\n"
    "v_lshrrev_b32 v8, 2, v1\n"
    "v_lshlrev_b32 v8, 3, v8\n"
    "v_lshl_add_u32 v8, v2, 8, v8\n"
    "v_and_b32 v9, 1, v2\n"
    "v_lshlrev_b32 v10, 1, v9\n"
    "v_and_b32 v11, 3, v1\n"
    "v_xor_b32 v10, v10, v11\n"
    "v_lshl_add_u32 v13, v10, 5, v8\n"
    "v_lshl_add_u32 v112, v9, 7, v13\n"
    "v_xor_b32 v10, 1, v10\n"
    "v_xor_b32 v9, 1, v9\n"
    "v_lshl_add_u32 v13, v10, 5, v8\n"
    "v_lshl_add_u32 v113, v9, 7, v13\n"
    "v_and_b32 v8, 13, v1\n"
    "v_lshlrev_b32 v8, 4, v8\n"
    "v_lshrrev_b32 v9, 1, v2\n"
    "v_lshl_add_u32 v8, v9, 8, v8\n"
    "v_and_b32 v9, 1, v2\n"
    "v_lshl_add_u32 v8, v9, 3, v8\n"
    "v_add_u32 v9, 1056, v8\n"
    "v_cndmask_b32 v75, v8, v9, s[70:71]\n"
    "v_lshrrev_b32 v8, 2, v1\n"
    "v_lshrrev_b32 v9, 3, v1\n"
    "v_xor_b32 v9, v9, v8\n"
    "v_lshlrev_b32 v9, 7, v9\n"
    "v_and_b32 v10, 3, v1\n"
    "v_lshl_add_u32 v9, v10, 3, v9\n"
    "v_xor_b32 v10, v8, v2\n"
    "v_lshl_add_u32 v114, v10, 5, v9\n"
    "v_and_b32 v10, 1, v2\n"
    "v_lshrrev_b32 v11, 1, v2\n"
    "v_lshl_add_u32 v10, v10, 1, v11\n"
    "v_xor_b32 v10, v8, v10\n"
    "v_lshl_add_u32 v115, v10, 5, v9\n"
    "v_lshrrev_b32 v79, 1, v71\n"
    "s_add_u32 s62, s36, 0x6b82000\n"
    "s_addc_u32 s63, s37, 0\n"
    "s_lshl_b32 s78, s42, 12\n"
    "s_add_u32 s62, s62, s78\n"
    "s_addc_u32 s63, s63, 0\n"
    "s_lshl_b32 s78, s41, 14\n"
    "s_add_u32 s62, s62, s78\n"
    "s_addc_u32 s63, s63, 0\n"
    "s_lshl_b32 s79, s39, 10\n"
    "s_add_u32 s78, s78, s79\n"
    "s_add_u32 s58, s36, 0xc00800\n"
    "s_addc_u32 s59, s37, 0\n"
    "s_add_u32 s58, s58, s78\n"
    "s_addc_u32 s59, s59, 0\n"
    "s_add_u32 s78, s78, s79\n"
    "s_add_u32 s60, s36, 0x2c00000\n"
    "s_addc_u32 s61, s37, 0\n"
    "s_add_u32 s60, s60, s78\n"
    "s_addc_u32 s61, s61, 0\n"
    "s_add_u32 s12, s36, 0x180000\n"
    "s_addc_u32 s13, s37, 0\n"
    "s_add_u32 s66, s36, 0x202000\n"
    "s_addc_u32 s67, s37, 0\n"
    "s_lshl_b32 s78, s41, 7\n"
    "s_add_u32 s66, s66, s78\n"
    "s_addc_u32 s67, s67, 0\n"
    "s_add_u32 s44, s36, 0x0\n"
    "s_addc_u32 s45, s37, 0\n"
    "s_waitcnt lgkmcnt(0)\n"
    "s_add_u32 s96, s96, 1\n"
    "s_addc_u32 s97, s97, 0\n"
    "s_lshl_b64 s[96:97], s[96:97], 6\n"
    "s_cmp_lg_u32 s42, 0\n"
    "s_cbranch_scc1 L_rb1%=\n"
    "s_waitcnt vmcnt(24) lgkmcnt(0)\n"
    "s_barrier\n"
    "s_add_u32 s32, s28, 0x10100\n"
    "s_addc_u32 s33, s29, 0\n"
    "s_add_u32 m0, s30, 114688\n"
    "s_nop 0\n"
    "global_load_lds_dwordx4 v88, s[32:33]\n"
    "s_add_u32 m0, s30, 115712\n"
    "s_nop 0\n"
    "global_load_lds_dwordx4 v89, s[32:33]\n"
    "s_add_u32 m0, s30, 116736\n"
    "s_nop 0\n"
    "global_load_lds_dwordx4 v90, s[32:33]\n"
    "s_add_u32 m0, s30, 117760\n"
    "s_nop 0\n"
    "global_load_lds_dwordx4 v91, s[32:33]\n"
    "ds_read_b64_tr_b16 a[128:129], v96 offset:0\n"
    "ds_read_b64_tr_b16 a[130:131], v96 offset:2048\n"
    "ds_read_b64_tr_b16 a[132:133], v96 offset:8192\n"
    "ds_read_b64_tr_b16 a[134:135], v96 offset:10240\n"
    "ds_read_b64_tr_b16 a[192:193], v97 offset:0\n"
    "ds_read_b64_tr_b16 a[194:195], v97 offset:2048\n"
    "ds_read_b64_tr_b16 a[196:197], v97 offset:8192\n"
    "ds_read_b64_tr_b16 a[198:199], v97 offset:10240\n"
    "s_waitcnt vmcnt(24) lgkmcnt(0)\n"
    "s_barrier\n"
    "s_add_u32 s32, s26, 0x200000\n"
    "s_addc_u32 s33, s27, 0\n"
    "s_add_u32 m0, s30, 0\n"
    "s_nop 0\n"
    "global_load_lds_dwordx4 v84, s[32:33]\n"
    "s_add_u32 m0, s30, 1024\n"
    "s_nop 0\n"
    "global_load_lds_dwordx4 v85, s[32:33]\n"
    "s_add_u32 m0, s30, 2048\n"
    "s_nop 0\n"
    "global_load_lds_dwordx4 v86, s[32:33]\n"
    "s_add_u32 m0, s30, 3072\n"
    "s_nop 0\n"
    "global_load_lds_dwordx4 v87, s[32:33]\n"
    "ds_read_b64_tr_b16 a[136:137], v96 offset:16384\n"
    "ds_read_b64_tr_b16 a[138:139], v96 offset:18432\n"
    "ds_read_b64_tr_b16 a[140:141], v96 offset:24576\n"
    "ds_read_b64_tr_b16 a[142:143], v96 offset:26624\n"
    "ds_read_b64_tr_b16 a[200:201], v97 offset:16384\n"
    "ds_read_b64_tr_b16 a[202:203], v97 offset:18432\n"
    "ds_read_b64_tr_b16 a[204:205], v97 offset:24576\n"
    "ds_read_b64_tr_b16 a[206:207], v97 offset:26624\n"
    "s_waitcnt vmcnt(24) lgkmcnt(0)\n"
    "s_barrier\n"
    "s_add_u32 s32, s26, 0x280000\n"
    "s_addc_u32 s33, s27, 0\n"
    "s_add_u32 m0, s30, 16384\n"
    "s_nop 0\n"
    "global_load_lds_dwordx4 v84, s[32:33]\n"
    "s_add_u32 m0, s30, 17408\n"
    "s_nop 0\n"
    "global_load_lds_dwordx4 v85, s[32:33]\n"
    "s_add_u32 m0, s30, 18432\n"
    "s_nop 0\n"
    "global_load_lds_dwordx4 v86, s[32:33]\n"
    "s_add_u32 m0, s30, 19456\n"
    "s_nop 0\n"
    "global_load_lds_dwordx4 v87, s[32:33]\n"
    "ds_read_b64_tr_b16 a[144:145], v96 offset:32768\n"
    "ds_read_b64_tr_b16 a[146:147], v96 offset:34816\n"
    "ds_read_b64_tr_b16 a[148:149], v96 offset:40960\n"
    "ds_read_b64_tr_b16 a[150:151], v96 offset:43008\n"
    "ds_read_b64_tr_b16 a[208:209], v97 offset:32768\n"
    "ds_read_b64_tr_b16 a[210:211], v97 offset:34816\n"
    "ds_read_b64_tr_b16 a[212:213], v97 offset:40960\n"
    "ds_read_b64_tr_b16 a[214:215], v97 offset:43008\n"
    "s_waitcnt vmcnt(24) lgkmcnt(0)\n"
    "s_barrier\n"
    "s_add_u32 s32, s26, 0x300000\n"
    "s_addc_u32 s33, s27, 0\n"
    "s_add_u32 m0, s30, 32768\n"
    "s_nop 0\n"
    "global_load_lds_dwordx4 v84, s[32:33]\n"
    "s_add_u32 m0, s30, 33792\n"
    "s_nop 0\n"
    "global_load_lds_dwordx4 v85, s[32:33]\n"
    "s_add_u32 m0, s30, 34816\n"
    "s_nop 0\n"
    "global_load_lds_dwordx4 v86, s[32:33]\n"
    "s_add_u32 m0, s30, 35840\n"
    "s_nop 0\n"
    "global_load_lds_dwordx4 v87, s[32:33]\n"
    "ds_read_b64_tr_b16 a[152:153], v96 offset:49152\n"
    "ds_read_b64_tr_b16 a[154:155], v96 offset:51200\n"
    "ds_read_b64_tr_b16 a[156:157], v96 offset:57344\n"
    "ds_read_b64_tr_b16 a[158:159], v96 offset:59392\n"
    "ds_read_b64_tr_b16 a[216:217], v97 offset:49152\n"
    "ds_read_b64_tr_b16 a[218:219], v97 offset:51200\n"
    "ds_read_b64_tr_b16 a[220:221], v97 offset:57344\n"
    "ds_read_b64_tr_b16 a[222:223], v97 offset:59392\n"
    "s_waitcnt vmcnt(24) lgkmcnt(0)\n"
    "s_barrier\n"
    "s_add_u32 s32, s26, 0x380000\n"
    "s_addc_u32 s33, s27, 0\n"
    "s_add_u32 m0, s30, 49152\n"
    "s_nop 0\n"
    "global_load_lds_dwordx4 v84, s[32:33]\n"
    "s_add_u32 m0, s30, 50176\n"
    "s_nop 0\n"
    "global_load_lds_dwordx4 v85, s[32:33]\n"
    "s_add_u32 m0, s30, 51200\n"
    "s_nop 0\n"
    "global_load_lds_dwordx4 v86, s[32:33]\n"
    "s_add_u32 m0, s30, 52224\n"
    "s_nop 0\n"
    "global_load_lds_dwordx4 v87, s[32:33]\n"
    "s_waitcnt vmcnt(24) lgkmcnt(0)\n"
    "s_barrier\n"
    "ds_read_b64_tr_b16 v[0:1], v104 offset:0\n"
    "ds_read_b64_tr_b16 v[2:3], v104 offset:512\n"
    "ds_read_b64_tr_b16 v[4:5], v104 offset:8192\n"
    "ds_read_b64_tr_b16 v[6:7], v104 offset:8704\n"
    "ds_read_b64_tr_b16 v[16:17], v104 offset:8\n"
    "ds_read_b64_tr_b16 v[18:19], v104 offset:520\n"
    "ds_read_b64_tr_b16 v[20:21], v104 offset:8200\n"
    "ds_read_b64_tr_b16 v[22:23], v104 offset:8712\n"
    "s_waitcnt lgkmcnt(4)\n"
    "ds_read_b64_tr_b16 v[32:33], v104 offset:16\n"
    "ds_read_b64_tr_b16 v[34:35], v104 offset:528\n"
    "ds_read_b64_tr_b16 v[36:37], v104 offset:8208\n"
    "ds_read_b64_tr_b16 v[38:39], v104 offset:8720\n"
    "ds_read_b64_tr_b16 v[48:49], v104 offset:24\n"
    "ds_read_b64_tr_b16 v[50:51], v104 offset:536\n"
    "ds_read_b64_tr_b16 v[52:53], v104 offset:8216\n"
    "ds_read_b64_tr_b16 v[54:55], v104 offset:8728\n"
    "s_waitcnt vmcnt(20) lgkmcnt(0)\n"
    "s_barrier\n"
    "s_add_u32 s32, s22, 0x0\n"
    "s_addc_u32 s33, s23, 0\n"
    "s_add_u32 m0, s30, 65536\n"
    "s_nop 0\n"
    "global_load_lds_dwordx4 v84, s[32:33]\n"
    "s_add_u32 m0, s30, 66560\n"
    "s_nop 0\n"
    "global_load_lds_dwordx4 v85, s[32:33]\n"
    "s_add_u32 m0, s30, 67584\n"
    "s_nop 0\n"
    "global_load_lds_dwordx4 v86, s[32:33]\n"
    "s_add_u32 m0, s30, 68608\n"
    "s_nop 0\n"
    "global_load_lds_dwordx4 v87, s[32:33]\n"
    "s_add_u32 s32, s22, 0x80000\n"
    "s_addc_u32 s33, s23, 0\n"
    "s_add_u32 m0, s30, 81920\n"
    "s_nop 0\n"
    "global_load_lds_dwordx4 v84, s[32:33]\n"
    "s_add_u32 m0, s30, 82944\n"
    "s_nop 0\n"
    "global_load_lds_dwordx4 v85, s[32:33]\n"
    "s_add_u32 m0, s30, 83968\n"
    "s_nop 0\n"
    "global_load_lds_dwordx4 v86, s[32:33]\n"
    "s_add_u32 m0, s30, 84992\n"
    "s_nop 0\n"
    "global_load_lds_dwordx4 v87, s[32:33]\n"
    "s_waitcnt vmcnt(24) lgkmcnt(0)\n"
    "s_barrier\n"
    "ds_read_b64_tr_b16 v[8:9], v104 offset:32768\n"
    "ds_read_b64_tr_b16 v[10:11], v104 offset:33280\n"
    "ds_read_b64_tr_b16 v[12:13], v104 offset:40960\n"
    "ds_read_b64_tr_b16 v[14:15], v104 offset:41472\n"
    "ds_read_b64_tr_b16 v[24:25], v104 offset:32776\n"
    "ds_read_b64_tr_b16 v[26:27], v104 offset:33288\n"
    "ds_read_b64_tr_b16 v[28:29], v104 offset:40968\n"
    "ds_read_b64_tr_b16 v[30:31], v104 offset:41480\n"
    "s_waitcnt lgkmcnt(4)\n"
    "ds_read_b64_tr_b16 v[40:41], v104 offset:32784\n"
    "ds_read_b64_tr_b16 v[42:43], v104 offset:33296\n"
    "ds_read_b64_tr_b16 v[44:45], v104 offset:40976\n"
    "ds_read_b64_tr_b16 v[46:47], v104 offset:41488\n"
    "ds_read_b64_tr_b16 v[56:57], v104 offset:32792\n"
    "ds_read_b64_tr_b16 v[58:59], v104 offset:33304\n"
    "ds_read_b64_tr_b16 v[60:61], v104 offset:40984\n"
    "ds_read_b64_tr_b16 v[62:63], v104 offset:41496\n"
    "s_waitcnt vmcnt(20) lgkmcnt(0)\n"
    "s_barrier\n"
    "s_add_u32 s32, s22, 0x100000\n"
    "s_addc_u32 s33, s23, 0\n"
    "s_add_u32 m0, s30, 98304\n"
    "s_nop 0\n"
    "global_load_lds_dwordx4 v84, s[32:33]\n"
    "s_add_u32 m0, s30, 99328\n"
    "s_nop 0\n"
    "global_load_lds_dwordx4 v85, s[32:33]\n"
    "s_add_u32 m0, s30, 100352\n"
    "s_nop 0\n"
    "global_load_lds_dwordx4 v86, s[32:33]\n"
    "s_add_u32 m0, s30, 101376\n"
    "s_nop 0\n"
    "global_load_lds_dwordx4 v87, s[32:33]\n"
    "s_add_u32 s32, s22, 0x180000\n"
    "s_addc_u32 s33, s23, 0\n"
    "s_add_u32 m0, s30, 114688\n"
    "s_nop 0\n"
    "global_load_lds_dwordx4 v84, s[32:33]\n"
    "s_add_u32 m0, s30, 115712\n"
    "s_nop 0\n"
    "global_load_lds_dwordx4 v85, s[32:33]\n"
    "s_add_u32 m0, s30, 116736\n"
    "s_nop 0\n"
    "global_load_lds_dwordx4 v86, s[32:33]\n"
    "s_add_u32 m0, s30, 117760\n"
    "s_nop 0\n"
    "global_load_lds_dwordx4 v87, s[32:33]\n"
    "ds_read_b64_tr_b16 a[160:161], v96 offset:0\n"
    "ds_read_b64_tr_b16 a[162:163], v96 offset:2048\n"
    "ds_read_b64_tr_b16 a[164:165], v96 offset:8192\n"
    "ds_read_b64_tr_b16 a[166:167], v96 offset:10240\n"
    "ds_read_b64_tr_b16 a[224:225], v97 offset:0\n"
    "ds_read_b64_tr_b16 a[226:227], v97 offset:2048\n"
    "ds_read_b64_tr_b16 a[228:229], v97 offset:8192\n"
    "ds_read_b64_tr_b16 a[230:231], v97 offset:10240\n"
    "s_waitcnt vmcnt(24) lgkmcnt(0)\n"
    "s_barrier\n"
    "s_add_u32 s32, s22, 0x200000\n"
    "s_addc_u32 s33, s23, 0\n"
    "s_add_u32 m0, s30, 0\n"
    "s_nop 0\n"
    "global_load_lds_dwordx4 v84, s[32:33]\n"
    "s_add_u32 m0, s30, 1024\n"
    "s_nop 0\n"
    "global_load_lds_dwordx4 v85, s[32:33]\n"
    "s_add_u32 m0, s30, 2048\n"
    "s_nop 0\n"
    "global_load_lds_dwordx4 v86, s[32:33]\n"
    "s_add_u32 m0, s30, 3072\n"
    "s_nop 0\n"
    "global_load_lds_dwordx4 v87, s[32:33]\n"
    "ds_read_b64_tr_b16 a[168:169], v96 offset:16384\n"
    "ds_read_b64_tr_b16 a[170:171], v96 offset:18432\n"
    "ds_read_b64_tr_b16 a[172:173], v96 offset:24576\n"
    "ds_read_b64_tr_b16 a[174:175], v96 offset:26624\n"
    "ds_read_b64_tr_b16 a[232:233], v97 offset:16384\n"
    "ds_read_b64_tr_b16 a[234:235], v97 offset:18432\n"
    "ds_read_b64_tr_b16 a[236:237], v97 offset:24576\n"
    "ds_read_b64_tr_b16 a[238:239], v97 offset:26624\n"
    "s_waitcnt vmcnt(24) lgkmcnt(0)\n"
    "s_barrier\n"
    "s_add_u32 s32, s22, 0x280000\n"
    "s_addc_u32 s33, s23, 0\n"
    "s_add_u32 m0, s30, 16384\n"
    "s_nop 0\n"
    "global_load_lds_dwordx4 v84, s[32:33]\n"
    "s_add_u32 m0, s30, 17408\n"
    "s_nop 0\n"
    "global_load_lds_dwordx4 v85, s[32:33]\n"
    "s_add_u32 m0, s30, 18432\n"
    "s_nop 0\n"
    "global_load_lds_dwordx4 v86, s[32:33]\n"
    "s_add_u32 m0, s30, 19456\n"
    "s_nop 0\n"
    "global_load_lds_dwordx4 v87, s[32:33]\n"
    "ds_read_b64_tr_b16 a[176:177], v96 offset:32768\n"
    "ds_read_b64_tr_b16 a[178:179], v96 offset:34816\n"
    "ds_read_b64_tr_b16 a[180:181], v96 offset:40960\n"
    "ds_read_b64_tr_b16 a[182:183], v96 offset:43008\n"
    "ds_read_b64_tr_b16 a[240:241], v97 offset:32768\n"
    "ds_read_b64_tr_b16 a[242:243], v97 offset:34816\n"
    "ds_read_b64_tr_b16 a[244:245], v97 offset:40960\n"
    "ds_read_b64_tr_b16 a[246:247], v97 offset:43008\n"
    "s_waitcnt vmcnt(24) lgkmcnt(0)\n"
    "s_barrier\n"
    "s_add_u32 s32, s22, 0x300000\n"
    "s_addc_u32 s33, s23, 0\n"
    "s_add_u32 m0, s30, 32768\n"
    "s_nop 0\n"
    "global_load_lds_dwordx4 v84, s[32:33]\n"
    "s_add_u32 m0, s30, 33792\n"
    "s_nop 0\n"
    "global_load_lds_dwordx4 v85, s[32:33]\n"
    "s_add_u32 m0, s30, 34816\n"
    "s_nop 0\n"
    "global_load_lds_dwordx4 v86, s[32:33]\n"
    "s_add_u32 m0, s30, 35840\n"
    "s_nop 0\n"
    "global_load_lds_dwordx4 v87, s[32:33]\n"
    "ds_read_b64_tr_b16 a[184:185], v96 offset:49152\n"
    "ds_read_b64_tr_b16 a[186:187], v96 offset:51200\n"
    "ds_read_b64_tr_b16 a[188:189], v96 offset:57344\n"
    "ds_read_b64_tr_b16 a[190:191], v96 offset:59392\n"
    "ds_read_b64_tr_b16 a[248:249], v97 offset:49152\n"
    "ds_read_b64_tr_b16 a[250:251], v97 offset:51200\n"
    "ds_read_b64_tr_b16 a[252:253], v97 offset:57344\n"
    "ds_read_b64_tr_b16 a[254:255], v97 offset:59392\n"
    "s_waitcnt vmcnt(24) lgkmcnt(0)\n"
    "s_barrier\n"
    "s_add_u32 s32, s22, 0x380000\n"
    "s_addc_u32 s33, s23, 0\n"
    "s_add_u32 m0, s30, 49152\n"
    "s_nop 0\n"
    "global_load_lds_dwordx4 v84, s[32:33]\n"
    "s_add_u32 m0, s30, 50176\n"
    "s_nop 0\n"
    "global_load_lds_dwordx4 v85, s[32:33]\n"
    "s_add_u32 m0, s30, 51200\n"
    "s_nop 0\n"
    "global_load_lds_dwordx4 v86, s[32:33]\n"
    "s_add_u32 m0, s30, 52224\n"
    "s_nop 0\n"
    "global_load_lds_dwordx4 v87, s[32:33]\n"
    "ds_read_b64_tr_b16 v[128:129], v94 offset:0\n"
    "ds_read_b64_tr_b16 v[130:131], v94 offset:2048\n"
    "ds_read_b64_tr_b16 v[132:133], v94 offset:8192\n"
    "ds_read_b64_tr_b16 v[134:135], v94 offset:10240\n"
    "ds_read_b64_tr_b16 v[136:137], v95 offset:0\n"
    "ds_read_b64_tr_b16 v[138:139], v95 offset:2048\n"
    "ds_read_b64_tr_b16 v[140:141], v95 offset:8192\n"
    "ds_read_b64_tr_b16 v[142:143], v95 offset:10240\n"
    "s_waitcnt vmcnt(24) lgkmcnt(0)\n"
    "s_barrier\n"
    "ds_read_b64_tr_b16 v[144:145], v94 offset:16384\n"
    "ds_read_b64_tr_b16 v[146:147], v94 offset:18432\n"
    "ds_read_b64_tr_b16 v[148:149], v94 offset:24576\n"
    "ds_read_b64_tr_b16 v[150:151], v94 offset:26624\n"
    "ds_read_b64_tr_b16 v[152:153], v95 offset:16384\n"
    "ds_read_b64_tr_b16 v[154:155], v95 offset:18432\n"
    "ds_read_b64_tr_b16 v[156:157], v95 offset:24576\n"
    "ds_read_b64_tr_b16 v[158:159], v95 offset:26624\n"
    "s_waitcnt vmcnt(20) lgkmcnt(0)\n"
    "s_barrier\n"
    "ds_read_b64_tr_b16 v[160:161], v94 offset:32768\n"
    "ds_read_b64_tr_b16 v[162:163], v94 offset:34816\n"
    "ds_read_b64_tr_b16 v[164:165], v94 offset:40960\n"
    "ds_read_b64_tr_b16 v[166:167], v94 offset:43008\n"
    "ds_read_b64_tr_b16 v[168:169], v95 offset:32768\n"
    "ds_read_b64_tr_b16 v[170:171], v95 offset:34816\n"
    "ds_read_b64_tr_b16 v[172:173], v95 offset:40960\n"
    "ds_read_b64_tr_b16 v[174:175], v95 offset:43008\n"
    "s_waitcnt vmcnt(16) lgkmcnt(0)\n"
    "s_barrier\n"
    "ds_read_b64_tr_b16 v[176:177], v94 offset:49152\n"
    "ds_read_b64_tr_b16 v[178:179], v94 offset:51200\n"
    "ds_read_b64_tr_b16 v[180:181], v94 offset:57344\n"
    "ds_read_b64_tr_b16 v[182:183], v94 offset:59392\n"
    "ds_read_b64_tr_b16 v[184:185], v95 offset:49152\n"
    "ds_read_b64_tr_b16 v[186:187], v95 offset:51200\n"
    "ds_read_b64_tr_b16 v[188:189], v95 offset:57344\n"
    "ds_read_b64_tr_b16 v[190:191], v95 offset:59392\n"
    "s_waitcnt vmcnt(12) lgkmcnt(0)\n"
    "s_barrier\n"
    "ds_read_b64_tr_b16 v[192:193], v92 offset:0\n"
    "ds_read_b64_tr_b16 v[194:195], v92 offset:2048\n"
    "ds_read_b64_tr_b16 v[196:197], v92 offset:8192\n"
    "ds_read_b64_tr_b16 v[198:199], v92 offset:10240\n"
    "ds_read_b64_tr_b16 v[200:201], v93 offset:0\n"
    "ds_read_b64_tr_b16 v[202:203], v93 offset:2048\n"
    "ds_read_b64_tr_b16 v[204:205], v93 offset:8192\n"
    "ds_read_b64_tr_b16 v[206:207], v93 offset:10240\n"
    "s_waitcnt vmcnt(8) lgkmcnt(0)\n"
    "s_barrier\n"
    "v_lshlrev_b32 v100, 3, v71\n"
    "s_add_u32 s82, s36, 0x20c000\n"
    "s_addc_u32 s83, s37, 0\n"
    "v_and_b32 v126, 16, v71\n"
    "v_lshlrev_b32 v126, 8, v126\n"
    "global_load_dwordx2 v[84:85], v100, s[82:83] sc1\n"
    "global_load_dwordx2 v[124:125], v126, s[66:67] sc1\n"
    "ds_read_b64_tr_b16 v[208:209], v92 offset:16384\n"
    "ds_read_b64_tr_b16 v[210:211], v92 offset:18432\n"
    "ds_read_b64_tr_b16 v[212:213], v92 offset:24576\n"
    "ds_read_b64_tr_b16 v[214:215], v92 offset:26624\n"
    "ds_read_b64_tr_b16 v[216:217], v93 offset:16384\n"
    "ds_read_b64_tr_b16 v[218:219], v93 offset:18432\n"
    "ds_read_b64_tr_b16 v[220:221], v93 offset:24576\n"
    "ds_read_b64_tr_b16 v[222:223], v93 offset:26624\n"
    "s_waitcnt vmcnt(6) lgkmcnt(0)\n"
    "s_barrier\n"
    "ds_read_b64_tr_b16 v[224:225], v92 offset:32768\n"
    "ds_read_b64_tr_b16 v[226:227], v92 offset:34816\n"
    "ds_read_b64_tr_b16 v[228:229], v92 offset:40960\n"
    "ds_read_b64_tr_b16 v[230:231], v92 offset:43008\n"
    "ds_read_b64_tr_b16 v[232:233], v93 offset:32768\n"
    "ds_read_b64_tr_b16 v[234:235], v93 offset:34816\n"
    "ds_read_b64_tr_b16 v[236:237], v93 offset:40960\n"
    "ds_read_b64_tr_b16 v[238:239], v93 offset:43008\n"
    "s_waitcnt vmcnt(2) lgkmcnt(0)\n"
    "s_barrier\n"
    "ds_read_b64_tr_b16 v[240:241], v92 offset:49152\n"
    "ds_read_b64_tr_b16 v[242:243], v92 offset:51200\n"
    "ds_read_b64_tr_b16 v[244:245], v92 offset:57344\n"
    "ds_read_b64_tr_b16 v[246:247], v92 offset:59392\n"
    "ds_read_b64_tr_b16 v[248:249], v93 offset:49152\n"
    "ds_read_b64_tr_b16 v[250:251], v93 offset:51200\n"
    "ds_read_b64_tr_b16 v[252:253], v93 offset:57344\n"
    "ds_read_b64_tr_b16 v[254:255], v93 offset:59392\n"
    "s_branch L_rc1%=\n"
    "L_rb1%=:\n"
    "s_waitcnt vmcnt(24) lgkmcnt(0)\n"
    "s_barrier\n"
    "s_add_u32 s32, s28, 0x10100\n"
    "s_addc_u32 s33, s29, 0\n"
    "s_add_u32 m0, s30, 114688\n"
    "s_nop 0\n"
    "global_load_lds_dwordx4 v88, s[32:33]\n"
    "s_add_u32 m0, s30, 115712\n"
    "s_nop 0\n"
    "global_load_lds_dwordx4 v89, s[32:33]\n"
    "s_add_u32 m0, s30, 116736\n"
    "s_nop 0\n"
    "global_load_lds_dwordx4 v90, s[32:33]\n"
    "s_add_u32 m0, s30, 117760\n"
    "s_nop 0\n"
    "global_load_lds_dwordx4 v91, s[32:33]\n"
    "ds_read_b64_tr_b16 a[160:161], v96 offset:0\n"
    "ds_read_b64_tr_b16 a[162:163], v96 offset:2048\n"
    "ds_read_b64_tr_b16 a[164:165], v96 offset:8192\n"
    "ds_read_b64_tr_b16 a[166:167], v96 offset:10240\n"
    "ds_read_b64_tr_b16 a[224:225], v97 offset:0\n"
    "ds_read_b64_tr_b16 a[226:227], v97 offset:2048\n"
    "ds_read_b64_tr_b16 a[228:229], v97 offset:8192\n"
    "ds_read_b64_tr_b16 a[230:231], v97 offset:10240\n"
    "s_waitcnt vmcnt(24) lgkmcnt(0)\n"
    "s_barrier\n"
    "s_add_u32 s32, s26, 0x0\n"
    "s_addc_u32 s33, s27, 0\n"
    "s_add_u32 m0, s30, 0\n"
    "s_nop 0\n"
    "global_load_lds_dwordx4 v84, s[32:33]\n"
    "s_add_u32 m0, s30, 1024\n"
    "s_nop 0\n"
    "global_load_lds_dwordx4 v85, s[32:33]\n"
    "s_add_u32 m0, s30, 2048\n"
    "s_nop 0\n"
    "global_load_lds_dwordx4 v86, s[32:33]\n"
    "s_add_u32 m0, s30, 3072\n"
    "s_nop 0\n"
    "global_load_lds_dwordx4 v87, s[32:33]\n"
    "ds_read_b64_tr_b16 a[168:169], v96 offset:16384\n"
    "ds_read_b64_tr_b16 a[170:171], v96 offset:18432\n"
    "ds_read_b64_tr_b16 a[172:173], v96 offset:24576\n"
    "ds_read_b64_tr_b16 a[174:175], v96 offset:26624\n"
    "ds_read_b64_tr_b16 a[232:233], v97 offset:16384\n"
    "ds_read_b64_tr_b16 a[234:235], v97 offset:18432\n"
    "ds_read_b64_tr_b16 a[236:237], v97 offset:24576\n"
    "ds_read_b64_tr_b16 a[238:239], v97 offset:26624\n"
    "s_waitcnt vmcnt(24) lgkmcnt(0)\n"
    "s_barrier\n"
    "s_add_u32 s32, s26, 0x80000\n"
    "s_addc_u32 s33, s27, 0\n"
    "s_add_u32 m0, s30, 16384\n"
    "s_nop 0\n"
    "global_load_lds_dwordx4 v84, s[32:33]\n"
    "s_add_u32 m0, s30, 17408\n"
    "s_nop 0\n"
    "global_load_lds_dwordx4 v85, s[32:33]\n"
    "s_add_u32 m0, s30, 18432\n"
    "s_nop 0\n"
    "global_load_lds_dwordx4 v86, s[32:33]\n"
    "s_add_u32 m0, s30, 19456\n"
    "s_nop 0\n"
    "global_load_lds_dwordx4 v87, s[32:33]\n"
    "ds_read_b64_tr_b16 a[176:177], v96 offset:32768\n"
    "ds_read_b64_tr_b16 a[178:179], v96 offset:34816\n"
    "ds_read_b64_tr_b16 a[180:181], v96 offset:40960\n"
    "ds_read_b64_tr_b16 a[182:183], v96 offset:43008\n"
    "ds_read_b64_tr_b16 a[240:241], v97 offset:32768\n"
    "ds_read_b64_tr_b16 a[242:243], v97 offset:34816\n"
    "ds_read_b64_tr_b16 a[244:245], v97 offset:40960\n"
    "ds_read_b64_tr_b16 a[246:247], v97 offset:43008\n"
    "s_waitcnt vmcnt(24) lgkmcnt(0)\n"
    "s_barrier\n"
    "s_add_u32 s32, s26, 0x100000\n"
    "s_addc_u32 s33, s27, 0\n"
    "s_add_u32 m0, s30, 32768\n"
    "s_nop 0\n"
    "global_load_lds_dwordx4 v84, s[32:33]\n"
    "s_add_u32 m0, s30, 33792\n"
    "s_nop 0\n"
    "global_load_lds_dwordx4 v85, s[32:33]\n"
    "s_add_u32 m0, s30, 34816\n"
    "s_nop 0\n"
    "global_load_lds_dwordx4 v86, s[32:33]\n"
    "s_add_u32 m0, s30, 35840\n"
    "s_nop 0\n"
    "global_load_lds_dwordx4 v87, s[32:33]\n"
    "ds_read_b64_tr_b16 a[184:185], v96 offset:49152\n"
    "ds_read_b64_tr_b16 a[186:187], v96 offset:51200\n"
    "ds_read_b64_tr_b16 a[188:189], v96 offset:57344\n"
    "ds_read_b64_tr_b16 a[190:191], v96 offset:59392\n"
    "ds_read_b64_tr_b16 a[248:249], v97 offset:49152\n"
    "ds_read_b64_tr_b16 a[250:251], v97 offset:51200\n"
    "ds_read_b64_tr_b16 a[252:253], v97 offset:57344\n"
    "ds_read_b64_tr_b16 a[254:255], v97 offset:59392\n"
    "s_waitcnt vmcnt(24) lgkmcnt(0)\n"
    "s_barrier\n"
    "s_add_u32 s32, s26, 0x180000\n"
    "s_addc_u32 s33, s27, 0\n"
    "s_add_u32 m0, s30, 49152\n"
    "s_nop 0\n"
    "global_load_lds_dwordx4 v84, s[32:33]\n"
    "s_add_u32 m0, s30, 50176\n"
    "s_nop 0\n"
    "global_load_lds_dwordx4 v85, s[32:33]\n"
    "s_add_u32 m0, s30, 51200\n"
    "s_nop 0\n"
    "global_load_lds_dwordx4 v86, s[32:33]\n"
    "s_add_u32 m0, s30, 52224\n"
    "s_nop 0\n"
    "global_load_lds_dwordx4 v87, s[32:33]\n"
    "s_waitcnt vmcnt(24) lgkmcnt(0)\n"
    "s_barrier\n"
    "ds_read_b64_tr_b16 v[0:1], v104 offset:0\n"
    "ds_read_b64_tr_b16 v[2:3], v104 offset:512\n"
    "ds_read_b64_tr_b16 v[4:5], v104 offset:8192\n"
    "ds_read_b64_tr_b16 v[6:7], v104 offset:8704\n"
    "ds_read_b64_tr_b16 v[16:17], v104 offset:8\n"
    "ds_read_b64_tr_b16 v[18:19], v104 offset:520\n"
    "ds_read_b64_tr_b16 v[20:21], v104 offset:8200\n"
    "ds_read_b64_tr_b16 v[22:23], v104 offset:8712\n"
    "s_waitcnt lgkmcnt(4)\n"
    "ds_read_b64_tr_b16 v[32:33], v104 offset:16\n"
    "ds_read_b64_tr_b16 v[34:35], v104 offset:528\n"
    "ds_read_b64_tr_b16 v[36:37], v104 offset:8208\n"
    "ds_read_b64_tr_b16 v[38:39], v104 offset:8720\n"
    "ds_read_b64_tr_b16 v[48:49], v104 offset:24\n"
    "ds_read_b64_tr_b16 v[50:51], v104 offset:536\n"
    "ds_read_b64_tr_b16 v[52:53], v104 offset:8216\n"
    "ds_read_b64_tr_b16 v[54:55], v104 offset:8728\n"
    "s_waitcnt vmcnt(20) lgkmcnt(0)\n"
    "s_barrier\n"
    "s_add_u32 s32, s24, 0x0\n"
    "s_addc_u32 s33, s25, 0\n"
    "s_add_u32 m0, s30, 65536\n"
    "s_nop 0\n"
    "global_load_lds_dwordx4 v84, s[32:33]\n"
    "s_add_u32 m0, s30, 66560\n"
    "s_nop 0\n"
    "global_load_lds_dwordx4 v85, s[32:33]\n"
    "s_add_u32 m0, s30, 67584\n"
    "s_nop 0\n"
    "global_load_lds_dwordx4 v86, s[32:33]\n"
    "s_add_u32 m0, s30, 68608\n"
    "s_nop 0\n"
    "global_load_lds_dwordx4 v87, s[32:33]\n"
    "s_add_u32 s32, s24, 0x80000\n"
    "s_addc_u32 s33, s25, 0\n"
    "s_add_u32 m0, s30, 81920\n"
    "s_nop 0\n"
    "global_load_lds_dwordx4 v84, s[32:33]\n"
    "s_add_u32 m0, s30, 82944\n"
    "s_nop 0\n"
    "global_load_lds_dwordx4 v85, s[32:33]\n"
    "s_add_u32 m0, s30, 83968\n"
    "s_nop 0\n"
    "global_load_lds_dwordx4 v86, s[32:33]\n"
    "s_add_u32 m0, s30, 84992\n"
    "s_nop 0\n"
    "global_load_lds_dwordx4 v87, s[32:33]\n"
    "s_waitcnt vmcnt(24) lgkmcnt(0)\n"
    "s_barrier\n"
    "ds_read_b64_tr_b16 v[8:9], v104 offset:32768\n"
    "ds_read_b64_tr_b16 v[10:11], v104 offset:33280\n"
    "ds_read_b64_tr_b16 v[12:13], v104 offset:40960\n"
    "ds_read_b64_tr_b16 v[14:15], v104 offset:41472\n"
    "ds_read_b64_tr_b16 v[24:25], v104 offset:32776\n"
    "ds_read_b64_tr_b16 v[26:27], v104 offset:33288\n"
    "ds_read_b64_tr_b16 v[28:29], v104 offset:40968\n"
    "ds_read_b64_tr_b16 v[30:31], v104 offset:41480\n"
    "s_waitcnt lgkmcnt(4)\n"
    "ds_read_b64_tr_b16 v[40:41], v104 offset:32784\n"
    "ds_read_b64_tr_b16 v[42:43], v104 offset:33296\n"
    "ds_read_b64_tr_b16 v[44:45], v104 offset:40976\n"
    "ds_read_b64_tr_b16 v[46:47], v104 offset:41488\n"
    "ds_read_b64_tr_b16 v[56:57], v104 offset:32792\n"
    "ds_read_b64_tr_b16 v[58:59], v104 offset:33304\n"
    "ds_read_b64_tr_b16 v[60:61], v104 offset:40984\n"
    "ds_read_b64_tr_b16 v[62:63], v104 offset:41496\n"
    "s_waitcnt vmcnt(20) lgkmcnt(0)\n"
    "s_barrier\n"
    "s_add_u32 s32, s24, 0x100000\n"
    "s_addc_u32 s33, s25, 0\n"
    "s_add_u32 m0, s30, 98304\n"
    "s_nop 0\n"
    "global_load_lds_dwordx4 v84, s[32:33]\n"
    "s_add_u32 m0, s30, 99328\n"
    "s_nop 0\n"
    "global_load_lds_dwordx4 v85, s[32:33]\n"
    "s_add_u32 m0, s30, 100352\n"
    "s_nop 0\n"
    "global_load_lds_dwordx4 v86, s[32:33]\n"
    "s_add_u32 m0, s30, 101376\n"
    "s_nop 0\n"
    "global_load_lds_dwordx4 v87, s[32:33]\n"
    "s_add_u32 s32, s24, 0x180000\n"
    "s_addc_u32 s33, s25, 0\n"
    "s_add_u32 m0, s30, 114688\n"
    "s_nop 0\n"
    "global_load_lds_dwordx4 v84, s[32:33]\n"
    "s_add_u32 m0, s30, 115712\n"
    "s_nop 0\n"
    "global_load_lds_dwordx4 v85, s[32:33]\n"
    "s_add_u32 m0, s30, 116736\n"
    "s_nop 0\n"
    "global_load_lds_dwordx4 v86, s[32:33]\n"
    "s_add_u32 m0, s30, 117760\n"
    "s_nop 0\n"
    "global_load_lds_dwordx4 v87, s[32:33]\n"
    "ds_read_b64_tr_b16 a[128:129], v96 offset:0\n"
    "ds_read_b64_tr_b16 a[130:131], v96 offset:2048\n"
    "ds_read_b64_tr_b16 a[132:133], v96 offset:8192\n"
    "ds_read_b64_tr_b16 a[134:135], v96 offset:10240\n"
    "ds_read_b64_tr_b16 a[192:193], v97 offset:0\n"
    "ds_read_b64_tr_b16 a[194:195], v97 offset:2048\n"
    "ds_read_b64_tr_b16 a[196:197], v97 offset:8192\n"
    "ds_read_b64_tr_b16 a[198:199], v97 offset:10240\n"
    "s_waitcnt vmcnt(24) lgkmcnt(0)\n"
    "s_barrier\n"
    "s_add_u32 s32, s24, 0x200000\n"
    "s_addc_u32 s33, s25, 0\n"
    "s_add_u32 m0, s30, 0\n"
    "s_nop 0\n"
    "global_load_lds_dwordx4 v84, s[32:33]\n"
    "s_add_u32 m0, s30, 1024\n"
    "s_nop 0\n"
    "global_load_lds_dwordx4 v85, s[32:33]\n"
    "s_add_u32 m0, s30, 2048\n"
    "s_nop 0\n"
    "global_load_lds_dwordx4 v86, s[32:33]\n"
    "s_add_u32 m0, s30, 3072\n"
    "s_nop 0\n"
    "global_load_lds_dwordx4 v87, s[32:33]\n"
    "ds_read_b64_tr_b16 a[136:137], v96 offset:16384\n"
    "ds_read_b64_tr_b16 a[138:139], v96 offset:18432\n"
    "ds_read_b64_tr_b16 a[140:141], v96 offset:24576\n"
    "ds_read_b64_tr_b16 a[142:143], v96 offset:26624\n"
    "ds_read_b64_tr_b16 a[200:201], v97 offset:16384\n"
    "ds_read_b64_tr_b16 a[202:203], v97 offset:18432\n"
    "ds_read_b64_tr_b16 a[204:205], v97 offset:24576\n"
    "ds_read_b64_tr_b16 a[206:207], v97 offset:26624\n"
    "s_waitcnt vmcnt(24) lgkmcnt(0)\n"
    "s_barrier\n"
    "s_add_u32 s32, s24, 0x280000\n"
    "s_addc_u32 s33, s25, 0\n"
    "s_add_u32 m0, s30, 16384\n"
    "s_nop 0\n"
    "global_load_lds_dwordx4 v84, s[32:33]\n"
    "s_add_u32 m0, s30, 17408\n"
    "s_nop 0\n"
    "global_load_lds_dwordx4 v85, s[32:33]\n"
    "s_add_u32 m0, s30, 18432\n"
    "s_nop 0\n"
    "global_load_lds_dwordx4 v86, s[32:33]\n"
    "s_add_u32 m0, s30, 19456\n"
    "s_nop 0\n"
    "global_load_lds_dwordx4 v87, s[32:33]\n"
    "ds_read_b64_tr_b16 a[144:145], v96 offset:32768\n"
    "ds_read_b64_tr_b16 a[146:147], v96 offset:34816\n"
    "ds_read_b64_tr_b16 a[148:149], v96 offset:40960\n"
    "ds_read_b64_tr_b16 a[150:151], v96 offset:43008\n"
    "ds_read_b64_tr_b16 a[208:209], v97 offset:32768\n"
    "ds_read_b64_tr_b16 a[210:211], v97 offset:34816\n"
    "ds_read_b64_tr_b16 a[212:213], v97 offset:40960\n"
    "ds_read_b64_tr_b16 a[214:215], v97 offset:43008\n"
    "s_waitcnt vmcnt(24) lgkmcnt(0)\n"
    "s_barrier\n"
    "s_add_u32 s32, s24, 0x300000\n"
    "s_addc_u32 s33, s25, 0\n"
    "s_add_u32 m0, s30, 32768\n"
    "s_nop 0\n"
    "global_load_lds_dwordx4 v84, s[32:33]\n"
    "s_add_u32 m0, s30, 33792\n"
    "s_nop 0\n"
    "global_load_lds_dwordx4 v85, s[32:33]\n"
    "s_add_u32 m0, s30, 34816\n"
    "s_nop 0\n"
    "global_load_lds_dwordx4 v86, s[32:33]\n"
    "s_add_u32 m0, s30, 35840\n"
    "s_nop 0\n"
    "global_load_lds_dwordx4 v87, s[32:33]\n"
    "ds_read_b64_tr_b16 a[152:153], v96 offset:49152\n"
    "ds_read_b64_tr_b16 a[154:155], v96 offset:51200\n"
    "ds_read_b64_tr_b16 a[156:157], v96 offset:57344\n"
    "ds_read_b64_tr_b16 a[158:159], v96 offset:59392\n"
    "ds_read_b64_tr_b16 a[216:217], v97 offset:49152\n"
    "ds_read_b64_tr_b16 a[218:219], v97 offset:51200\n"
    "ds_read_b64_tr_b16 a[220:221], v97 offset:57344\n"
    "ds_read_b64_tr_b16 a[222:223], v97 offset:59392\n"
    "s_waitcnt vmcnt(24) lgkmcnt(0)\n"
    "s_barrier\n"
    "s_add_u32 s32, s24, 0x380000\n"
    "s_addc_u32 s33, s25, 0\n"
    "s_add_u32 m0, s30, 49152\n"
    "s_nop 0\n"
    "global_load_lds_dwordx4 v84, s[32:33]\n"
    "s_add_u32 m0, s30, 50176\n"
    "s_nop 0\n"
    "global_load_lds_dwordx4 v85, s[32:33]\n"
    "s_add_u32 m0, s30, 51200\n"
    "s_nop 0\n"
    "global_load_lds_dwordx4 v86, s[32:33]\n"
    "s_add_u32 m0, s30, 52224\n"
    "s_nop 0\n"
    "global_load_lds_dwordx4 v87, s[32:33]\n"
    "ds_read_b64_tr_b16 a[0:1], v94 offset:0\n"
    "ds_read_b64_tr_b16 a[2:3], v94 offset:2048\n"
    "ds_read_b64_tr_b16 a[4:5], v94 offset:8192\n"
    "ds_read_b64_tr_b16 a[6:7], v94 offset:10240\n"
    "ds_read_b64_tr_b16 a[64:65], v95 offset:0\n"
    "ds_read_b64_tr_b16 a[66:67], v95 offset:2048\n"
    "ds_read_b64_tr_b16 a[68:69], v95 offset:8192\n"
    "ds_read_b64_tr_b16 a[70:71], v95 offset:10240\n"
    "s_waitcnt vmcnt(24) lgkmcnt(0)\n"
    "s_barrier\n"
    "ds_read_b64_tr_b16 a[8:9], v94 offset:16384\n"
    "ds_read_b64_tr_b16 a[10:11], v94 offset:18432\n"
    "ds_read_b64_tr_b16 a[12:13], v94 offset:24576\n"
    "ds_read_b64_tr_b16 a[14:15], v94 offset:26624\n"
    "ds_read_b64_tr_b16 a[72:73], v95 offset:16384\n"
    "ds_read_b64_tr_b16 a[74:75], v95 offset:18432\n"
    "ds_read_b64_tr_b16 a[76:77], v95 offset:24576\n"
    "ds_read_b64_tr_b16 a[78:79], v95 offset:26624\n"
    "s_waitcnt vmcnt(20) lgkmcnt(0)\n"
    "s_barrier\n"
    "ds_read_b64_tr_b16 a[16:17], v94 offset:32768\n"
    "ds_read_b64_tr_b16 a[18:19], v94 offset:34816\n"
    "ds_read_b64_tr_b16 a[20:21], v94 offset:40960\n"
    "ds_read_b64_tr_b16 a[22:23], v94 offset:43008\n"
    "ds_read_b64_tr_b16 a[80:81], v95 offset:32768\n"
    "ds_read_b64_tr_b16 a[82:83], v95 offset:34816\n"
    "ds_read_b64_tr_b16 a[84:85], v95 offset:40960\n"
    "ds_read_b64_tr_b16 a[86:87], v95 offset:43008\n"
    "s_waitcnt vmcnt(16) lgkmcnt(0)\n"
    "s_barrier\n"
    "ds_read_b64_tr_b16 a[24:25], v94 offset:49152\n"
    "ds_read_b64_tr_b16 a[26:27], v94 offset:51200\n"
    "ds_read_b64_tr_b16 a[28:29], v94 offset:57344\n"
    "ds_read_b64_tr_b16 a[30:31], v94 offset:59392\n"
    "ds_read_b64_tr_b16 a[88:89], v95 offset:49152\n"
    "ds_read_b64_tr_b16 a[90:91], v95 offset:51200\n"
    "ds_read_b64_tr_b16 a[92:93], v95 offset:57344\n"
    "ds_read_b64_tr_b16 a[94:95], v95 offset:59392\n"
    "s_waitcnt vmcnt(12) lgkmcnt(0)\n"
    "s_barrier\n"
    "ds_read_b64_tr_b16 a[32:33], v92 offset:0\n"
    "ds_read_b64_tr_b16 a[34:35], v92 offset:2048\n"
    "ds_read_b64_tr_b16 a[36:37], v92 offset:8192\n"
    "ds_read_b64_tr_b16 a[38:39], v92 offset:10240\n"
    "ds_read_b64_tr_b16 a[96:97], v93 offset:0\n"
    "ds_read_b64_tr_b16 a[98:99], v93 offset:2048\n"
    "ds_read_b64_tr_b16 a[100:101], v93 offset:8192\n"
    "ds_read_b64_tr_b16 a[102:103], v93 offset:10240\n"
    "s_waitcnt vmcnt(8) lgkmcnt(0)\n"
    "s_barrier\n"
    "v_lshlrev_b32 v100, 3, v71\n"
    "s_add_u32 s82, s36, 0x20c000\n"
    "s_addc_u32 s83, s37, 0\n"
    "v_and_b32 v126, 16, v71\n"
    "v_lshlrev_b32 v126, 8, v126\n"
    "global_load_dwordx2 v[84:85], v100, s[82:83] sc1\n"
    "global_load_dwordx2 v[124:125], v126, s[66:67] sc1\n"
    "ds_read_b64_tr_b16 a[40:41], v92 offset:16384\n"
    "ds_read_b64_tr_b16 a[42:43], v92 offset:18432\n"
    "ds_read_b64_tr_b16 a[44:45], v92 offset:24576\n"
    "ds_read_b64_tr_b16 a[46:47], v92 offset:26624\n"
    "ds_read_b64_tr_b16 a[104:105], v93 offset:16384\n"
    "ds_read_b64_tr_b16 a[106:107], v93 offset:18432\n"
    "ds_read_b64_tr_b16 a[108:109], v93 offset:24576\n"
    "ds_read_b64_tr_b16 a[110:111], v93 offset:26624\n"
    "s_waitcnt vmcnt(6) lgkmcnt(0)\n"
    "s_barrier\n"
    "ds_read_b64_tr_b16 a[48:49], v92 offset:32768\n"
    "ds_read_b64_tr_b16 a[50:51], v92 offset:34816\n"
    "ds_read_b64_tr_b16 a[52:53], v92 offset:40960\n"
    "ds_read_b64_tr_b16 a[54:55], v92 offset:43008\n"
    "ds_read_b64_tr_b16 a[112:113], v93 offset:32768\n"
    "ds_read_b64_tr_b16 a[114:115], v93 offset:34816\n"
    "ds_read_b64_tr_b16 a[116:117], v93 offset:40960\n"
    "ds_read_b64_tr_b16 a[118:119], v93 offset:43008\n"
    "s_waitcnt vmcnt(2) lgkmcnt(0)\n"
    "s_barrier\n"
    "ds_read_b64_tr_b16 a[56:57], v92 offset:49152\n"
    "ds_read_b64_tr_b16 a[58:59], v92 offset:51200\n"
    "ds_read_b64_tr_b16 a[60:61], v92 offset:57344\n"
    "ds_read_b64_tr_b16 a[62:63], v92 offset:59392\n"
    "ds_read_b64_tr_b16 a[120:121], v93 offset:49152\n"
    "ds_read_b64_tr_b16 a[122:123], v93 offset:51200\n"
    "ds_read_b64_tr_b16 a[124:125], v93 offset:57344\n"
    "ds_read_b64_tr_b16 a[126:127], v93 offset:59392\n"
    "L_rc1%=:\n"
    "s_waitcnt vmcnt(0) lgkmcnt(0)\n"
    "s_barrier\n"
    "v_xor_b32 v124, s96, v124\n"
    "v_xor_b32 v125, s97, v125\n"
    "v_cmp_gt_u64 vcc, 64, v[124:125]\n"
    "s_nop 1\n"
    "v_cndmask_b32 v124, 0, v124, vcc\n"
    "s_nop 0\n"
    "v_readlane_b32 s80, v124, 0\n"
    "v_readlane_b32 s81, v124, 1\n"
    "ds_write_b128 v116, v[60:63] offset:15360\n"
    "ds_write_b128 v116, v[56:59] offset:14336\n"
    "ds_write_b128 v116, v[52:55] offset:13312\n"
    "ds_write_b128 v116, v[48:51] offset:12288\n"
    "ds_write_b128 v116, v[44:47] offset:11264\n"
    "ds_write_b128 v116, v[40:43] offset:10240\n"
    "ds_write_b128 v116, v[36:39] offset:9216\n"
    "ds_write_b128 v116, v[32:35] offset:8192\n"
    "s_waitcnt lgkmcnt(4)\n"
    "ds_write_b128 v116, v[28:31] offset:7168\n"
    "ds_write_b128 v116, v[24:27] offset:6144\n"
    "ds_write_b128 v116, v[20:23] offset:5120\n"
    "ds_write_b128 v116, v[16:19] offset:4096\n"
    "ds_write_b128 v116, v[12:15] offset:3072\n"
    "ds_write_b128 v116, v[8:11] offset:2048\n"
    "ds_write_b128 v116, v[4:7] offset:1024\n"
    "ds_write_b128 v116, v[0:3] offset:0\n"
    "s_waitcnt lgkmcnt(4)\n"
    "s_min_u32 s92, s80, s81\n"
    "s_branch L_tabc%=\n"
    "L_tab%=:\n"
    "global_load_dwordx2 v[84:85], v100, s[82:83] sc1\n"
    "s_waitcnt vmcnt(0)\n"
    "L_tabc%=:\n"
    "v_xor_b32 v84, s96, v84\n"
    "v_xor_b32 v85, s97, v85\n"
    "v_cmp_gt_u64 vcc, 64, v[84:85]\n"
    "s_nop 1\n"
    "v_cndmask_b32 v84, 0, v84, vcc\n"
    "v_cmp_eq_u32 vcc, 0, v84\n"
    "s_nop 0\n"
    "s_cbranch_vccz L_tabok%=\n"
    "s_sleep 2\n"
    "s_add_u32 s90, s90, 1\n"
    "s_and_b32 s78, s90, 0xff\n"
    "s_cmp_lg_u32 s78, 0\n"
    "s_cbranch_scc1 L_tab%=\n"
    "v_mov_b32 v126, 0\n"
    "global_load_dwordx2 v[126:127], v126, s[68:69] sc1\n"
    "s_waitcnt vmcnt(0)\n"
    "v_cmp_eq_u64 vcc, s[96:97], v[126:127]\n"
    "s_nop 1\n"
    "s_cbranch_vccnz L_bail_tab%=\n"
    "s_cmp_lt_u32 s90, 0x40000\n"
    "s_cbranch_scc1 L_tab%=\n"
    "v_mov_b32 v126, s96\n"
    "v_mov_b32 v127, s97\n"
    "s_mov_b64 exec, 1\n"
    "global_store_dwordx2 v71, v[126:127], s[68:69] sc1\n"
    "s_mov_b64 exec, -1\n"
    "L_bail_tab%=:\n"
    "L_tabok%=:\n"
    "v_readfirstlane_b32 s93, v84\n"
    "s_nop 0\n"
    "s_sub_u32 s93, s93, 1\n"
    "s_and_b32 s93, s93, 7\n"
    "s_andn2_b32 s78, 2, s93\n"
    "s_lshl_b32 s78, s78, 12\n"
    "s_xor_b32 s58, s58, s78\n"
    "s_xor_b32 s60, s60, s78\n"
    "s_xor_b32 s62, s62, s78\n"
    "s_mov_b32 s43, 0\n"
    "s_mov_b32 s19, 0\n"
    "s_cmp_eq_u32 s92, 0\n"
    "s_cbranch_scc1 L_nokv%=\n"
    "global_load_dwordx4 v[56:59], v71, s[58:59] offset:-2048 sc1\n"
    "global_load_dwordx4 v[60:63], v71, s[58:59] offset:2048 sc1\n"
    "global_load_dwordx2 v[64:65], v71, s[60:61] offset:0 sc1\n"
    "global_load_dwordx2 v[68:69], v71, s[60:61] offset:8 sc1\n"
    "global_load_dwordx2 v[72:73], v71, s[60:61] offset:1024 sc1\n"
    "global_load_dwordx2 v[76:77], v71, s[60:61] offset:1032 sc1\n"
    "s_mov_b32 s19, 1\n"
    "L_nokv%=:\n"
    "s_cmp_lg_u32 s39, 0\n"
    "s_cbranch_scc1 L_sv1%=\n"
    "ds_write_b128 v116, a[0:3] offset:16384\n"
    "ds_write_b128 v116, a[4:7] offset:17408\n"
    "ds_write_b128 v116, a[64:67] offset:18432\n"
    "ds_write_b128 v116, a[68:71] offset:19456\n"
    "ds_write_b128 v116, a[8:11] offset:20480\n"
    "ds_write_b128 v116, a[12:15] offset:21504\n"
    "ds_write_b128 v116, a[72:75] offset:22528\n"
    "ds_write_b128 v116, a[76:79] offset:23552\n"
    "s_waitcnt lgkmcnt(4)\n"
    "ds_write_b128 v116, a[16:19] offset:24576\n"
    "ds_write_b128 v116, a[20:23] offset:25600\n"
    "ds_write_b128 v116, a[80:83] offset:26624\n"
    "ds_write_b128 v116, a[84:87] offset:27648\n"
    "ds_write_b128 v116, a[32:35] offset:28672\n"
    "ds_write_b128 v116, a[36:39] offset:29696\n"
    "ds_write_b128 v116, a[96:99] offset:30720\n"
    "ds_write_b128 v116, a[100:103] offset:31744\n"
    "s_waitcnt lgkmcnt(4)\n"
    "s_branch L_sv4%=\n"
    "L_sv1%=:\n"
    "s_cmp_lg_u32 s39, 1\n"
    "s_cbranch_scc1 L_sv2%=\n"
    "ds_write_b128 v116, a[16:19] offset:16384\n"
    "ds_write_b128 v116, a[20:23] offset:17408\n"
    "ds_write_b128 v116, a[80:83] offset:18432\n"
    "ds_write_b128 v116, a[84:87] offset:19456\n"
    "ds_write_b128 v116, a[24:27] offset:20480\n"
    "ds_write_b128 v116, a[28:31] offset:21504\n"
    "ds_write_b128 v116, a[88:91] offset:22528\n"
    "ds_write_b128 v116, a[92:95] offset:23552\n"
    "s_waitcnt lgkmcnt(4)\n"
    "ds_write_b128 v116, a[0:3] offset:24576\n"
    "ds_write_b128 v116, a[4:7] offset:25600\n"
    "ds_write_b128 v116, a[64:67] offset:26624\n"
    "ds_write_b128 v116, a[68:71] offset:27648\n"
    "ds_write_b128 v116, a[48:51] offset:28672\n"
    "ds_write_b128 v116, a[52:55] offset:29696\n"
    "ds_write_b128 v116, a[112:115] offset:30720\n"
    "ds_write_b128 v116, a[116:119] offset:31744\n"
    "s_waitcnt lgkmcnt(4)\n"
    "s_branch L_sv4%=\n"
    "L_sv2%=:\n"
    "s_cmp_lg_u32 s39, 2\n"
    "s_cbranch_scc1 L_sv3%=\n"
    "ds_write_b128 v116, a[32:35] offset:16384\n"
    "ds_write_b128 v116, a[36:39] offset:17408\n"
    "ds_write_b128 v116, a[96:99] offset:18432\n"
    "ds_write_b128 v116, a[100:103] offset:19456\n"
    "ds_write_b128 v116, a[40:43] offset:20480\n"
    "ds_write_b128 v116, a[44:47] offset:21504\n"
    "ds_write_b128 v116, a[104:107] offset:22528\n"
    "ds_write_b128 v116, a[108:111] offset:23552\n"
    "s_waitcnt lgkmcnt(4)\n"
    "ds_write_b128 v116, a[48:51] offset:24576\n"
    "ds_write_b128 v116, a[52:55] offset:25600\n"
    "ds_write_b128 v116, a[112:115] offset:26624\n"
    "ds_write_b128 v116, a[116:119] offset:27648\n"
    "ds_write_b128 v116, a[0:3] offset:28672\n"
    "ds_write_b128 v116, a[4:7] offset:29696\n"
    "ds_write_b128 v116, a[64:67] offset:30720\n"
    "ds_write_b128 v116, a[68:71] offset:31744\n"
    "s_waitcnt lgkmcnt(4)\n"
    "s_branch L_sv4%=\n"
    "L_sv3%=:\n"
    "ds_write_b128 v116, a[48:51] offset:16384\n"
    "ds_write_b128 v116, a[52:55] offset:17408\n"
    "ds_write_b128 v116, a[112:115] offset:18432\n"
    "ds_write_b128 v116, a[116:119] offset:19456\n"
    "ds_write_b128 v116, a[56:59] offset:20480\n"
    "ds_write_b128 v116, a[60:63] offset:21504\n"
    "ds_write_b128 v116, a[120:123] offset:22528\n"
    "ds_write_b128 v116, a[124:127] offset:23552\n"
    "s_waitcnt lgkmcnt(4)\n"
    "ds_write_b128 v116, a[32:35] offset:24576\n"
    "ds_write_b128 v116, a[36:39] offset:25600\n"
    "ds_write_b128 v116, a[96:99] offset:26624\n"
    "ds_write_b128 v116, a[100:103] offset:27648\n"
    "ds_write_b128 v116, a[16:19] offset:28672\n"
    "ds_write_b128 v116, a[20:23] offset:29696\n"
    "ds_write_b128 v116, a[80:83] offset:30720\n"
    "ds_write_b128 v116, a[84:87] offset:31744\n"
    "s_waitcnt lgkmcnt(4)\n"
    "L_sv4%=:\n"
    "v_lshrrev_b32 v0, 4, v71\n"
    "v_and_b32 v1, 15, v0\n"
    "v_lshrrev_b32 v2, 4, v0\n"
    "s_lshl_b32 s78, s42, 4\n"
    "s_lshl_b32 s79, s39, 2\n"
    "s_add_u32 s78, s78, s79\n"
    "v_lshrrev_b32 v8, 2, v1\n"
    "v_add_u32 v8, s78, v8\n"
    "v_lshrrev_b32 v10, 1, v8\n"
    "v_lshlrev_b32 v10, 16, v10\n"
    "v_and_b32 v9, 1, v8\n"
    "s_lshr_b32 s78, s39, 1\n"
    "s_lshl_b32 s78, s78, 4\n"
    "s_xor_b32 s78, s78, s41\n"
    "v_xor_b32 v8, s78, v8\n"
    "v_lshl_add_u32 v10, v8, 7, v10\n"
    "v_lshrrev_b32 v8, 1, v2\n"
    "v_lshl_add_u32 v9, v8, 2, v9\n"
    "s_lshl_b32 s78, s42, 2\n"
    "s_xor_b32 s78, s78, s93\n"
    "v_xor_b32 v9, s78, v9\n"
    "v_lshl_add_u32 v10, v9, 12, v10\n"
    "v_and_b32 v8, 1, v2\n"
    "v_lshl_add_u32 v10, v8, 6, v10\n"
    "v_and_b32 v8, 3, v1\n"
    "v_lshl_add_u32 v117, v8, 4, v10\n"
    "s_lshl_b32 s78, s42, 5\n"
    "s_add_u32 s78, s78, s41\n"
    "s_lshl_b32 s78, s78, 7\n"
    "s_add_u32 s64, s36, 0x220000\n"
    "s_addc_u32 s65, s37, 0\n"
    "s_add_u32 s64, s64, s78\n"
    "s_addc_u32 s65, s65, 0\n"
    "s_and_b32 s78, s11, 1\n"
    "s_lshl_b32 s78, s78, 2\n"
    "s_bfe_u32 s79, s11, 0x10001\n"
    "s_or_b32 s78, s78, s79\n"
    "s_xor_b32 s78, s78, s93\n"
    "s_xor_b32 s78, s78, 1\n"
    "s_lshl_b32 s78, s78, 12\n"
    "s_lshl_b32 s79, s42, 16\n"
    "s_add_u32 s79, s79, s78\n"
    "s_lshr_b32 s80, s39, 1\n"
    "s_lshl_b32 s80, s80, 15\n"
    "s_add_u32 s79, s79, s80\n"
    "s_and_b32 s80, s39, 1\n"
    "s_lshl_b32 s80, s80, 11\n"
    "s_add_u32 s79, s79, s80\n"
    "s_add_u32 s54, s36, 0x100000\n"
    "s_addc_u32 s55, s37, 0\n"
    "s_add_u32 s54, s54, s79\n"
    "s_addc_u32 s55, s55, 0\n"
    "s_mov_b64 s[48:49], s[54:55]\n"
    "s_lshl_b32 s79, s38, 6\n"
    "s_add_u32 s56, s12, s79\n"
    "s_addc_u32 s57, s13, 0\n"
    "s_xor_b32 s78, s78, 0x4000\n"
    "s_add_u32 s12, s12, s78\n"
    "s_addc_u32 s13, s13, 0\n"
    "v_and_b32 v8, 1, v0\n"
    "v_bfe_u32 v9, v0, 1, 1\n"
    "v_lshl_add_u32 v8, v8, 2, v9\n"
    "s_xor_b32 s78, s93, 5\n"
    "v_xor_b32 v8, s78, v8\n"
    "v_lshlrev_b32 v118, 12, v8\n"
    "v_lshlrev_b32 v119, 2, v71\n"
    "s_mov_b32 s43, 0\n"
    "v_mov_b64 v[104:105], 0\n"
    "v_mov_b64 v[106:107], 0\n"
    "v_mov_b64 v[108:109], 0\n"
    "v_mov_b64 v[110:111], 0\n"
    "v_mov_b64 v[32:33], 0\n"
    "v_mov_b64 v[34:35], 0\n"
    "v_mov_b64 v[36:37], 0\n"
    "v_mov_b64 v[38:39], 0\n"
    "v_mov_b64 v[40:41], 0\n"
    "v_mov_b64 v[42:43], 0\n"
    "v_mov_b64 v[44:45], 0\n"
    "v_mov_b64 v[46:47], 0\n"
    "v_mov_b64 v[48:49], 0\n"
    "v_mov_b64 v[50:51], 0\n"
    "v_mov_b64 v[52:53], 0\n"
    "v_mov_b64 v[54:55], 0\n"
    "s_cmp_lg_u32 s19, 0\n"
    "s_cbranch_scc1 L_kvdone%=\n"
    "s_add_u32 s79, s43, 1\n"
    "s_lshr_b32 s79, s79, 1\n"
    "s_cmp_ge_u32 s79, 24\n"
    "s_cselect_b32 s79, 32, s79\n"
    "s_cmp_lt_u32 s79, s92\n"
    "s_cbranch_scc1 L_cntokp%=\n"
    "L_cntp%=:\n"
    "v_and_b32 v126, 16, v71\n"
    "v_lshlrev_b32 v126, 8, v126\n"
    "global_load_dwordx2 v[126:127], v126, s[66:67] sc1\n"
    "s_waitcnt vmcnt(0)\n"
    "v_xor_b32 v126, s96, v126\n"
    "v_xor_b32 v127, s97, v127\n"
    "v_cmp_gt_u64 vcc, 64, v[126:127]\n"
    "s_nop 1\n"
    "v_cndmask_b32 v126, 0, v126, vcc\n"
    "s_nop 0\n"
    "v_readlane_b32 s80, v126, 0\n"
    "v_readlane_b32 s81, v126, 1\n"
    "s_nop 1\n"
    "s_min_u32 s92, s80, s81\n"
    "s_cmp_lt_u32 s79, s92\n"
    "s_cbranch_scc1 L_cntokp%=\n"
    "s_sleep 2\n"
    "s_add_u32 s90, s90, 1\n"
    "s_and_b32 s78, s90, 0xff\n"
    "s_cmp_lg_u32 s78, 0\n"
    "s_cbranch_scc1 L_cntp%=\n"
    "v_mov_b32 v126, 0\n"
    "global_load_dwordx2 v[126:127], v126, s[68:69] sc1\n"
    "s_waitcnt vmcnt(0)\n"
    "v_cmp_eq_u64 vcc, s[96:97], v[126:127]\n"
    "s_nop 1\n"
    "s_cbranch_vccnz L_bail_cntp%=\n"
    "s_cmp_lt_u32 s90, 0x40000\n"
    "s_cbranch_scc1 L_cntp%=\n"
    "v_mov_b32 v126, s96\n"
    "v_mov_b32 v127, s97\n"
    "s_mov_b64 exec, 1\n"
    "global_store_dwordx2 v71, v[126:127], s[68:69] sc1\n"
    "s_mov_b64 exec, -1\n"
    "L_bail_cntp%=:\n"
    "L_cntokp%=:\n"
    "global_load_dwordx4 v[56:59], v71, s[58:59] offset:-2048 sc1\n"
    "global_load_dwordx4 v[60:63], v71, s[58:59] offset:2048 sc1\n"
    "global_load_dwordx2 v[64:65], v71, s[60:61] offset:0 sc1\n"
    "global_load_dwordx2 v[68:69], v71, s[60:61] offset:8 sc1\n"
    "global_load_dwordx2 v[72:73], v71, s[60:61] offset:1024 sc1\n"
    "global_load_dwordx2 v[76:77], v71, s[60:61] offset:1032 sc1\n"
    "L_kvdone%=:\n"
    "s_add_u32 s11, s43, 1\n"
    "s_and_b32 s11, s11, 3\n"
    "s_cselect_b32 s11, 0, 0x2000\n"
    "s_add_u32 s58, s58, 0x80000\n"
    "s_addc_u32 s59, s59, 0\n"
    "s_xor_b32 s58, s58, s11\n"
    "s_add_u32 s60, s60, 0x80000\n"
    "s_addc_u32 s61, s61, 0\n"
    "s_xor_b32 s60, s60, s11\n"
    "s_waitcnt lgkmcnt(0)\n"
    "s_memtime s[6:7]\n"
    "s_memrealtime s[78:79]\n"
    "s_nop 3\n"
    "s_memrealtime s[80:81]\n"
    "s_nop 3\n"
    "s_memrealtime s[82:83]\n"
    "s_nop 3\n"
    "s_memrealtime s[84:85]\n"
    "s_nop 3\n"
    "s_memrealtime s[86:87]\n"
    "s_nop 3\n"
    "s_memrealtime s[88:89]\n"
    "s_mov_b32 s10, 0\n"
    "s_waitcnt lgkmcnt(0)\n"
    "s_mul_i32 s91, s78, 24\n"
    "s_cmp_eq_u32 s78, s80\n"
    "s_addc_u32 s10, s10, 0\n"
    "s_cmp_eq_u32 s78, s82\n"
    "s_addc_u32 s10, s10, 0\n"
    "s_cmp_eq_u32 s78, s84\n"
    "s_addc_u32 s10, s10, 0\n"
    "s_cmp_eq_u32 s78, s86\n"
    "s_addc_u32 s10, s10, 0\n"
    "s_cmp_eq_u32 s78, s88\n"
    "s_addc_u32 s10, s10, 0\n"
    "s_mul_i32 s10, s10, 20\n"
    "s_sub_u32 s91, s91, s6\n"
    "s_sub_u32 s91, s91, s10\n"
    "s_mov_b32 s94, 100\n"
    "s_mov_b64 s[0:1], 0\n"
    "s_mov_b64 s[40:41], 0\n"
    "s_mov_b32 s51, 2120\n"
    "s_mov_b32 s15, 740\n"
    "s_mov_b32 s3, 1140\n"
    "s_add_u32 s46, s91, 2400\n"
    "s_getpc_b64 s[4:5]\n"
    "L_pc%=:\n"
    "s_add_u32 s4, s4, L_sled%=-L_pc%=\n"
    "s_addc_u32 s5, s5, 0\n"
    "v_or_b32 v84, 1, s96\n"
    "v_mov_b32 v85, s97\n"
    "v_add_u32 v86, s43, v84\n"
    "s_cmp_eq_u32 s39, 1\n"
    "s_cbranch_scc1 L_poll1%=\n"
    "s_cmp_eq_u32 s39, 2\n"
    "s_cbranch_scc1 L_poll2%=\n"
    "s_cmp_eq_u32 s39, 3\n"
    "s_cbranch_scc1 L_poll3%=\n"
    "s_branch L_poll0%=\n"
    ".p2align 6\n"
    "L_poll0%=:\n"
    "global_load_dwordx4 v[0:3], v71, s[54:55] offset:0 sc1\n"
    "global_load_dwordx4 v[4:7], v71, s[54:55] offset:1024 sc1\n"
    "s_mov_b32 m0, 51456\n"
    "s_waitcnt vmcnt(0)\n"
    "s_branch L_top0%=\n"
    ".p2align 6\n"
    "L_top0%=:\n"
    "s_waitcnt vmcnt(3)\n"
    "v_smfmac_f32_16x16x64_bf16 v[40:43], v[0:3], a[0:7], v67\n"
    "ds_write_b128 v71, v[0:3] offset:0\n"
    "v_smfmac_f32_16x16x64_bf16 v[44:47], v[0:3], a[64:71], v67\n"
    "v_smfmac_f32_16x16x64_bf16 v[32:35], v[0:3], v[128:135], v67\n"
    "v_smfmac_f32_16x16x64_bf16 v[36:39], v[0:3], v[136:143], v67\n"
    "s_waitcnt vmcnt(2)\n"
    "v_smfmac_f32_16x16x64_bf16 v[40:43], v[4:7], a[8:15], v67\n"
    "ds_write_b128 v71, v[4:7] offset:1024\n"
    "s_waitcnt lgkmcnt(1)\n"
    "s_barrier\n"
    "ds_read_b128 v[8:11], v71 offset:2048\n"
    "v_smfmac_f32_16x16x64_bf16 v[44:47], v[4:7], a[72:79], v67\n"
    "ds_read_b128 v[16:19], v71 offset:4096\n"
    "v_smfmac_f32_16x16x64_bf16 v[32:35], v[4:7], v[144:151], v67\n"
    "ds_read_b128 v[24:27], v71 offset:6144\n"
    "v_smfmac_f32_16x16x64_bf16 v[36:39], v[4:7], v[152:159], v67\n"
    "s_waitcnt lgkmcnt(3)\n"
    "s_barrier\n"
    "v_smfmac_f32_16x16x64_bf16 v[48:51], v[0:3], a[128:135], v67\n"
    "s_waitcnt lgkmcnt(2)\n"
    "ds_read_b128 v[12:15], v71 offset:3072\n"
    "v_smfmac_f32_16x16x64_bf16 v[32:35], v[8:11], v[160:167], v67\n"
    "ds_read_b128 v[20:23], v71 offset:5120\n"
    "v_smfmac_f32_16x16x64_bf16 v[36:39], v[8:11], v[168:175], v67\n"
    "s_waitcnt lgkmcnt(2)\n"
    "ds_read_b128 v[28:31], v71 offset:7168\n"
    "v_smfmac_f32_16x16x64_bf16 v[32:35], v[16:19], v[192:199], v67\n"
    "s_mov_b32 s78, 0x12345\n"
    "ds_read_b128 a[0:3], v116 offset:0\n"
    "v_smfmac_f32_16x16x64_bf16 v[36:39], v[16:19], v[200:207], v67\n"
    "s_mov_b32 s78, 0x12345\n"
    "ds_read_b128 a[4:7], v116 offset:1024\n"
    "v_smfmac_f32_16x16x64_bf16 v[32:35], v[24:27], v[224:231], v67\n"
    "s_mov_b32 s78, 0x12345\n"
    "ds_read_b128 a[64:67], v116 offset:4096\n"
    "v_smfmac_f32_16x16x64_bf16 v[36:39], v[24:27], v[232:239], v67\n"
    "s_waitcnt lgkmcnt(5)\n"
    "ds_read_b128 a[68:71], v116 offset:5120\n"
    "v_smfmac_f32_16x16x64_bf16 v[32:35], v[12:15], v[176:183], v67\n"
    "s_sub_i32 s2, s6, s16\n"
    "ds_read_b128 a[8:11], v116 offset:2048\n"
    "v_smfmac_f32_16x16x64_bf16 v[36:39], v[12:15], v[184:191], v67\n"
    "s_waitcnt lgkmcnt(5)\n"
    "ds_read_b128 a[12:15], v116 offset:3072\n"
    "v_smfmac_f32_16x16x64_bf16 v[32:35], v[20:23], v[208:215], v67\n"
    "s_cmp_gt_i32 s2, s3\n"
    "ds_read_b128 a[72:75], v116 offset:6144\n"
    "v_smfmac_f32_16x16x64_bf16 v[36:39], v[20:23], v[216:223], v67\n"
    "ds_read_b128 a[76:79], v116 offset:7168\n"
    "v_smfmac_f32_16x16x64_bf16 v[32:35], v[28:31], v[240:247], v67\n"
    "s_nop 0\n"
    "v_smfmac_f32_16x16x64_bf16 v[36:39], v[28:31], v[248:255], v67\n"
    "v_smfmac_f32_16x16x64_bf16 v[40:43], v[8:11], a[16:23], v67\n"
    "v_add_f32 v32, v32, v34\n"
    "v_add_f32_e64 v33, v33, v35\n"
    "v_smfmac_f32_16x16x64_bf16 v[44:47], v[8:11], a[80:87], v67\n"
    "ds_read_b128 a[16:19], v116 offset:8192\n"
    "v_cvt_pk_bf16_f32 v126, v32, v33\n"
    "v_add_f32 v36, v36, v38\n"
    "v_add_f32 v37, v37, v39\n"
    "v_cvt_pk_bf16_f32 v127, v36, v37\n"
    "v_smfmac_f32_16x16x64_bf16 v[40:43], v[16:19], a[32:39], v67\n"
    "ds_write_b64 v114, v[126:127] offset:8192\n"
    "v_smfmac_f32_16x16x64_bf16 v[44:47], v[16:19], a[96:103], v67\n"
    "ds_read_b128 a[20:23], v116 offset:9216\n"
    "v_cmp_u_f32_e64 vcc, v32, v33\n"
    "v_smfmac_f32_16x16x64_bf16 v[40:43], v[24:27], a[48:55], v67\n"
    "ds_read_b128 a[80:83], v116 offset:12288\n"
    "v_mov_b32_e64 v87, s76\n"
    "v_smfmac_f32_16x16x64_bf16 v[44:47], v[24:27], a[112:119], v67\n"
    "s_cbranch_vccnz L_slow0%=\n"
    "L_go0%=:\n"
    "v_smfmac_f32_16x16x64_bf16 v[40:43], v[12:15], a[24:31], v67\n"
    "s_waitcnt lgkmcnt(2)\n"
    "s_barrier\n"
    "ds_read_b64_tr_b16 v[32:33], v112 offset:8192\n"
    "v_smfmac_f32_16x16x64_bf16 v[44:47], v[12:15], a[88:95], v67\n"
    "ds_read_b64_tr_b16 v[34:35], v113 offset:8192\n"
    "ds_read_b64_tr_b16 v[36:37], v112 offset:9216\n"
    "v_smfmac_f32_16x16x64_bf16 v[40:43], v[20:23], a[40:47], v67\n"
    "ds_read_b64_tr_b16 v[38:39], v113 offset:9216\n"
    "v_smfmac_f32_16x16x64_bf16 v[44:47], v[20:23], a[104:111], v67\n"
    "global_load_dwordx4 v[96:99], v71, s[58:59] offset:-2048 sc1\n"
    "v_smfmac_f32_16x16x64_bf16 v[40:43], v[28:31], a[56:63], v67\n"
    "ds_read_b128 a[84:87], v116 offset:13312\n"
    "v_smfmac_f32_16x16x64_bf16 v[44:47], v[28:31], a[120:127], v67\n"
    "s_waitcnt lgkmcnt(1)\n"
    "ds_read_b128 a[32:35], v116 offset:10240\n"
    "v_mfma_f32_16x16x32_bf16 v[88:91], v[56:59], v[32:35], 0\n"
    "v_add_f32 v40, v40, v42\n"
    "v_add_f32 v41, v41, v43\n"
    "v_mfma_f32_16x16x32_bf16 v[88:91], v[60:63], v[36:39], v[88:91]\n"
    "v_cvt_pk_bf16_f32 v126, v40, v41\n"
    "v_add_f32 v44, v44, v46\n"
    "v_add_f32 v45, v45, v47\n"
    "v_cvt_pk_bf16_f32 v127, v44, v45\n"
    "v_smfmac_f32_16x16x64_bf16 v[52:55], v[0:3], a[192:199], v67\n"
    "ds_write_b64 v115, v[126:127] offset:10240\n"
    "v_add_f32_dpp v88, v90, v88 quad_perm:[2,3,2,3] row_mask:0xf bank_mask:0xf\n"
    "v_add_f32_dpp v89, v91, v89 quad_perm:[2,3,2,3] row_mask:0xf bank_mask:0xf\n"
    "v_smfmac_f32_16x16x64_bf16 v[48:51], v[4:7], a[136:143], v67\n"
    "ds_write_b64 v75, v[88:89] offset:14336\n"
    "v_smfmac_f32_16x16x64_bf16 v[52:55], v[4:7], a[200:207], v67\n"
    "global_load_dwordx4 v[100:103], v71, s[58:59] offset:2048 sc1\n"
    "v_smfmac_f32_16x16x64_bf16 v[48:51], v[8:11], a[144:151], v67\n"
    "global_load_lds_dwordx4 v71, s[60:61] offset:0 sc1\n"
    "v_smfmac_f32_16x16x64_bf16 v[52:55], v[8:11], a[208:215], v67\n"
    "global_load_lds_dwordx4 v71, s[60:61] offset:1024 sc1\n"
    "v_smfmac_f32_16x16x64_bf16 v[48:51], v[16:19], a[160:167], v67\n"
    "s_waitcnt lgkmcnt(0)\n"
    "s_barrier\n"
    "ds_read_b64_tr_b16 v[40:41], v112 offset:10240\n"
    "v_smfmac_f32_16x16x64_bf16 v[52:55], v[16:19], a[224:231], v67\n"
    "ds_read_b64_tr_b16 v[42:43], v113 offset:10240\n"
    "ds_read_b64_tr_b16 v[44:45], v112 offset:11264\n"
    "v_smfmac_f32_16x16x64_bf16 v[48:51], v[24:27], a[176:183], v67\n"
    "ds_read_b64_tr_b16 v[46:47], v113 offset:11264\n"
    "ds_read_b128 v[88:91], v71 offset:14336\n"
    "v_smfmac_f32_16x16x64_bf16 v[52:55], v[24:27], a[240:247], v67\n"
    "s_cmp_eq_u32 s43, 63\n"
    "ds_read_b128 a[36:39], v116 offset:11264\n"
    "v_smfmac_f32_16x16x64_bf16 v[48:51], v[12:15], a[152:159], v67\n"
    "s_waitcnt lgkmcnt(4)\n"
    "v_mfma_f32_16x16x32_bf16 v[92:95], v[40:43], v[32:35], 0\n"
    "s_waitcnt lgkmcnt(2)\n"
    "ds_read_b128 a[96:99], v116 offset:14336\n"
    "v_mfma_f32_16x16x32_bf16 v[92:95], v[44:47], v[36:39], v[92:95]\n"
    "s_cbranch_scc1 L_last0%=\n"
    "s_waitcnt lgkmcnt(2)\n"
    "v_max3_f32 v122, v88, v89, v90\n"
    "v_max_f32 v122, v122, v91\n"
    "v_smfmac_f32_16x16x64_bf16 v[52:55], v[12:15], a[216:223], v67\n"
    "ds_read_b128 a[100:103], v116 offset:15360\n"
    "v_max_f32_dpp v123, v122, v122 quad_perm:[2,3,0,1] row_mask:0xf bank_mask:0xf\n"
    "v_add_f32_dpp v92, v94, v92 quad_perm:[2,3,2,3] row_mask:0xf bank_mask:0xf\n"
    "v_add_f32_dpp v93, v95, v93 quad_perm:[2,3,2,3] row_mask:0xf bank_mask:0xf\n"
    "v_max3_f32 v122, v123, v92, v93\n"
    "v_smfmac_f32_16x16x64_bf16 v[48:51], v[20:23], a[168:175], v67\n"
    "v_mul_f32_dpp v121, v122, v87 quad_perm:[0,1,0,1] row_mask:0xf bank_mask:0xf\n"
    "v_mul_f32_dpp v123, v122, v87 quad_perm:[0,1,0,1] row_mask:0xf bank_mask:0xf\n"
    "v_smfmac_f32_16x16x64_bf16 v[52:55], v[20:23], a[232:239], v67\n"
    "v_permlane16_swap_b32 v123, v121\n"
    "v_max_f32 v122, v123, v121\n"
    "v_max_f32_e64 v123, v123, v121\n"
    "v_smfmac_f32_16x16x64_bf16 v[48:51], v[28:31], a[184:191], v67\n"
    "v_smfmac_f32_16x16x64_bf16 v[52:55], v[28:31], a[248:255], v67\n"
    "v_permlane32_swap_b32 v122, v123\n"
    "v_max_f32 v122, v122, v123\n"
    "v_pk_fma_f32 v[92:93], v[92:93], s[76:77], v[122:123] op_sel_hi:[1,1,0] neg_lo:[0,0,1] neg_hi:[0,0,1]\n"
    "v_pk_fma_f32 v[88:89], v[88:89], s[76:77], v[122:123] op_sel_hi:[1,1,0] neg_lo:[0,0,1] neg_hi:[0,0,1]\n"
    "v_pk_fma_f32 v[90:91], v[90:91], s[76:77], v[122:123] op_sel_hi:[1,1,0] neg_lo:[0,0,1] neg_hi:[0,0,1]\n"
    "v_exp_f32 v92, v92\n"
    "v_exp_f32 v93, v93\n"
    "v_exp_f32 v88, v88\n"
    "v_exp_f32 v89, v89\n"
    "v_exp_f32 v90, v90\n"
    "v_exp_f32 v91, v91\n"
    "v_pk_add_f32 v[48:49], v[48:49], v[50:51]\n"
    "v_pk_add_f32 v[52:53], v[52:53], v[54:55]\n"
    "v_cvt_pk_bf16_f32 v126, v92, v93\n"
    "v_cvt_pk_bf16_f32 v88, v88, v89\n"
    "v_cvt_pk_bf16_f32 v90, v90, v91\n"
    "v_cndmask_b32 v89, 0, v126, s[72:73]\n"
    "v_cndmask_b32 v91, 0, v126, s[74:75]\n"
    "v_cvt_pk_bf16_f32 v66, v48, v49\n"
    "v_cvt_pk_bf16_f32 v70, v48, v49\n"
    "v_cvt_pk_bf16_f32 v74, v52, v53\n"
    "v_cvt_pk_bf16_f32 v78, v52, v53\n"
    "v_smfmac_f32_16x16x64_bf16 v[104:107], v[88:91], v[64:71], v67\n"
    "v_mov_b64_e64 v[0:1], 0\n"
    "v_smfmac_f32_16x16x64_bf16 v[108:111], v[88:91], v[72:79], v67\n"
    "v_mov_b64_e64 v[2:3], 0\n"
    "v_mfma_f32_16x16x32_bf16 v[84:87], v[88:91], v[80:83], 0\n"
    "v_add_f32 v104, v104, v106\n"
    "v_add_f32 v105, v105, v107\n"
    "v_cvt_pk_bf16_f32 v104, v104, v105\n"
    "v_add_f32 v108, v108, v110\n"
    "v_add_f32 v109, v109, v111\n"
    "v_cvt_pk_bf16_f32 v105, v108, v109\n"
    "ds_write_b64 v114, v[104:105] offset:12288\n"
    "v_pk_add_f32 v[124:125], v[84:85], v[86:87]\n"
    "v_rcp_f32 v124, v124\n"
    "v_rcp_f32 v125, v125\n"
    "v_mov_b64 v[4:5], 0\n"
    "v_mov_b64 v[6:7], 0\n"
    "v_mov_b64 v[8:9], 0\n"
    "v_mov_b64 v[10:11], 0\n"
    "v_mov_b64 v[12:13], 0\n"
    "v_mov_b64 v[14:15], 0\n"
    "v_mfma_f32_16x16x32_bf16 v[92:95], v[80:83], v[80:83], 0\n"
    "s_waitcnt lgkmcnt(0)\n"
    "s_barrier\n"
    "ds_read_b64_tr_b16 v[32:33], v112 offset:12288\n"
    "v_mfma_f32_16x16x32_bf16 v[92:95], v[80:83], v[80:83], 0\n"
    "ds_read_b64_tr_b16 v[34:35], v113 offset:12288\n"
    "ds_read_b64_tr_b16 v[36:37], v112 offset:13312\n"
    "v_mfma_f32_16x16x32_bf16 v[92:95], v[80:83], v[80:83], 0\n"
    "ds_read_b64_tr_b16 v[38:39], v113 offset:13312\n"
    "v_mfma_f32_16x16x32_bf16 v[92:95], v[80:83], v[80:83], 0\n"
    "s_waitcnt lgkmcnt(2)\n"
    "v_smfmac_f32_16x16x64_bf16 v[0:3], v[32:35], a[0:7], v67\n"
    "v_smfmac_f32_16x16x64_bf16 v[4:7], v[32:35], a[64:71], v67\n"
    "s_waitcnt lgkmcnt(0)\n"
    "v_smfmac_f32_16x16x64_bf16 v[0:3], v[36:39], a[8:15], v67\n"
    "v_smfmac_f32_16x16x64_bf16 v[4:7], v[36:39], a[72:79], v67\n"
    "v_smfmac_f32_16x16x64_bf16 v[8:11], v[32:35], a[16:23], v67\n"
    "v_add_f32_e64 v0, v0, v2\n"
    "v_smfmac_f32_16x16x64_bf16 v[12:15], v[32:35], a[80:87], v67\n"
    "v_add_f32_e64 v1, v1, v3\n"
    "v_smfmac_f32_16x16x64_bf16 v[8:11], v[36:39], a[32:39], v67\n"
    "v_add_f32_e64 v4, v4, v6\n"
    "v_smfmac_f32_16x16x64_bf16 v[12:15], v[36:39], a[96:103], v67\n"
    "v_add_f32 v5, v5, v7\n"
    "v_mul_f32 v0, v0, v124\n"
    "v_mul_f32_e64 v1, v1, v125\n"
    "v_cvt_pk_bf16_f32 v88, v0, v1\n"
    "v_pk_mul_f32 v[4:5], v[4:5], v[124:125]\n"
    "v_pk_add_f32 v[8:9], v[8:9], v[10:11]\n"
    "v_pk_add_f32 v[12:13], v[12:13], v[14:15]\n"
    "v_cvt_pk_bf16_f32 v89, v4, v5\n"
    "v_pk_mul_f32 v[8:9], v[8:9], v[124:125]\n"
    "v_pk_mul_f32 v[12:13], v[12:13], v[124:125]\n"
    "v_cvt_pk_bf16_f32 v90, v8, v9\n"
    "v_cvt_pk_bf16_f32 v91, v12, v13\n"
    "global_store_dwordx4 v117, v[88:91], s[44:45] sc1\n"
    "s_add_u32 s43, s43, 1\n"
    "s_sleep 3\n"
    "ds_read_b128 a[0:3], v116 offset:16384\n"
    "ds_read_b128 a[4:7], v116 offset:17408\n"
    "ds_read_b128 a[64:67], v116 offset:18432\n"
    "ds_read_b128 a[68:71], v116 offset:19456\n"
    "ds_read_b128 a[8:11], v116 offset:20480\n"
    "ds_read_b128 a[12:15], v116 offset:21504\n"
    "ds_read_b128 a[72:75], v116 offset:22528\n"
    "ds_read_b128 a[76:79], v116 offset:23552\n"
    "ds_read_b128 a[16:19], v116 offset:24576\n"
    "ds_read_b128 a[20:23], v116 offset:25600\n"
    "ds_read_b128 a[80:83], v116 offset:26624\n"
    "ds_read_b128 a[84:87], v116 offset:27648\n"
    "ds_read_b128 a[32:35], v116 offset:28672\n"
    "s_waitcnt lgkmcnt(8)\n"
    "ds_read_b128 a[36:39], v116 offset:29696\n"
    "ds_read_b128 a[96:99], v116 offset:30720\n"
    "ds_read_b128 a[100:103], v116 offset:31744\n"
    "s_and_b32 s11, s43, 3\n"
    "s_lshl_b32 s11, s11, 17\n"
    "s_add_u32 s54, s48, s11\n"
    "s_addc_u32 s55, s49, 0\n"
    "s_add_u32 s11, s43, -1\n"
    "s_and_b32 s11, s11, 3\n"
    "s_cselect_b32 s11, 0, 0x2000\n"
    "s_add_u32 s62, s62, 0x80000\n"
    "s_addc_u32 s63, s63, 0\n"
    "s_xor_b32 s62, s62, s11\n"
    "s_add_u32 s11, s43, 1\n"
    "s_and_b32 s11, s11, 3\n"
    "s_cselect_b32 s11, 0, 0x2000\n"
    "s_add_u32 s58, s58, 0x80000\n"
    "s_addc_u32 s59, s59, 0\n"
    "s_xor_b32 s58, s58, s11\n"
    "s_add_u32 s60, s60, 0x80000\n"
    "s_addc_u32 s61, s61, 0\n"
    "s_xor_b32 s60, s60, s11\n"
    "s_xor_b32 s44, s44, 0x8000\n"
    "v_mov_b64 v[32:33], 0\n"
    "v_mov_b64 v[34:35], 0\n"
    "v_mov_b64 v[36:37], 0\n"
    "v_mov_b64 v[38:39], 0\n"
    "v_mov_b64 v[48:49], 0\n"
    "v_mov_b64 v[50:51], 0\n"
    "v_mov_b64 v[52:53], 0\n"
    "v_mov_b64 v[54:55], 0\n"
    "v_mov_b64 v[104:105], 0\n"
    "v_mov_b64 v[106:107], 0\n"
    "v_mov_b64 v[108:109], 0\n"
    "v_mov_b64 v[110:111], 0\n"
    "v_or_b32 v84, 1, s96\n"
    "v_mov_b32 v85, s97\n"
    "v_add_u32 v86, s43, v84\n"
    "s_cmp_lg_u32 s43, 54\n"
    "s_cbranch_scc1 L_fl%=\n"
    "s_mov_b64 exec, 1\n"
    "global_store_dwordx2 v71, v[84:85], s[64:65] sc1\n"
    "s_mov_b64 exec, -1\n"
    "s_add_u32 s64, s64, 8192\n"
    "s_addc_u32 s65, s65, 0\n"
    "L_fl%=:\n"
    "s_waitcnt vmcnt(1)\n"
    "ds_read_b64 v[64:65], v116 offset:32768\n"
    "ds_read_b64 v[68:69], v116 offset:32776\n"
    "ds_read_b64 v[72:73], v116 offset:33792\n"
    "ds_read_b64 v[76:77], v116 offset:33800\n"
    "v_mov_b64 v[56:57], v[96:97]\n"
    "v_mov_b64 v[58:59], v[98:99]\n"
    "v_mov_b64 v[60:61], v[100:101]\n"
    "v_mov_b64 v[62:63], v[102:103]\n"
    "s_waitcnt lgkmcnt(0)\n"
    "s_barrier\n"
    "global_load_dwordx4 v[0:3], v71, s[54:55] offset:0 sc1\n"
    "s_nop 3\n"
    "global_load_dwordx4 v[4:7], v71, s[54:55] offset:1024 sc1\n"
    "s_nop 15\n"
    "s_cmp_lg_u32 s42, 0\n"
    "s_cbranch_scc1 L_vmb0%=\n"
    "s_mov_b32 exec_hi, 0\n"
    "global_store_dwordx4 v71, v[40:43], s[62:63] offset:0 sc1\n"
    "s_mov_b64 exec, -1\n"
    "s_branch L_kvb0%=\n"
    "L_vmb0%=:\n"
    "v_mov_b32 v126, v66\n"
    "v_mov_b32 v127, v74\n"
    "global_store_dwordx2 v79, v[126:127], s[62:63] offset:0 sc1\n"
    "L_kvb0%=:\n"
    "s_mov_b64 exec, 1\n"
    "global_store_dwordx2 v71, v[84:85], s[64:65] sc1\n"
    "s_add_u32 s64, s64, 8192\n"
    "s_addc_u32 s65, s65, 0\n"
    "s_mov_b64 exec, -1\n"
    "s_nop 1\n"
    "v_mov_b64 v[40:41], 0\n"
    "v_mov_b64 v[42:43], 0\n"
    "v_mov_b64 v[44:45], 0\n"
    "v_mov_b64 v[46:47], 0\n"
    "s_branch L_top0%=\n"
    "L_slow0%=:\n"
    "v_cmp_eq_u32 s[78:79], s50, v0\n"
    "v_cmp_eq_u32 s[80:81], s50, v1\n"
    "v_cmp_eq_u32 s[82:83], s50, v2\n"
    "v_cmp_eq_u32 s[84:85], s50, v3\n"
    "v_cmp_eq_u32 s[86:87], s50, v4\n"
    "v_cmp_eq_u32 s[88:89], s50, v5\n"
    "v_cmp_eq_u32 s[52:53], s50, v6\n"
    "v_cmp_eq_u32 vcc, s50, v7\n"
    "global_load_dwordx4 v[0:3], v71, s[54:55] offset:0 sc1\n"
    "global_load_dwordx4 v[4:7], v71, s[54:55] offset:1024 sc1\n"
    "s_waitcnt lgkmcnt(0)\n"
    "s_barrier\n"
    "s_or_b64 vcc, vcc, s[78:79]\n"
    "s_or_b64 vcc, vcc, s[80:81]\n"
    "s_or_b64 vcc, vcc, s[82:83]\n"
    "s_or_b64 vcc, vcc, s[84:85]\n"
    "s_or_b64 vcc, vcc, s[86:87]\n"
    "s_or_b64 vcc, vcc, s[88:89]\n"
    "s_or_b64 vcc, vcc, s[52:53]\n"
    "s_cbranch_scc1 L_redo0%=\n"
    "v_cmp_eq_u32 s[78:79], s50, v8\n"
    "v_cmp_eq_u32 s[80:81], s50, v9\n"
    "v_cmp_eq_u32 s[82:83], s50, v10\n"
    "v_cmp_eq_u32 s[84:85], s50, v11\n"
    "v_cmp_eq_u32 s[86:87], s50, v16\n"
    "v_cmp_eq_u32 s[88:89], s50, v17\n"
    "v_cmp_eq_u32 s[52:53], s50, v18\n"
    "v_cmp_eq_u32 vcc, s50, v19\n"
    "s_nop 0\n"
    "s_or_b64 vcc, vcc, s[78:79]\n"
    "s_or_b64 vcc, vcc, s[80:81]\n"
    "s_or_b64 vcc, vcc, s[82:83]\n"
    "s_or_b64 vcc, vcc, s[84:85]\n"
    "s_or_b64 vcc, vcc, s[86:87]\n"
    "s_or_b64 vcc, vcc, s[88:89]\n"
    "s_or_b64 vcc, vcc, s[52:53]\n"
    "s_cbranch_scc1 L_redo0%=\n"
    "v_cmp_eq_u32 s[78:79], s50, v24\n"
    "v_cmp_eq_u32 s[80:81], s50, v25\n"
    "v_cmp_eq_u32 s[82:83], s50, v26\n"
    "v_cmp_eq_u32 s[84:85], s50, v27\n"
    "v_cmp_eq_u32 s[86:87], s50, v12\n"
    "v_cmp_eq_u32 s[88:89], s50, v13\n"
    "v_cmp_eq_u32 s[52:53], s50, v14\n"
    "v_cmp_eq_u32 vcc, s50, v15\n"
    "s_nop 0\n"
    "s_or_b64 vcc, vcc, s[78:79]\n"
    "s_or_b64 vcc, vcc, s[80:81]\n"
    "s_or_b64 vcc, vcc, s[82:83]\n"
    "s_or_b64 vcc, vcc, s[84:85]\n"
    "s_or_b64 vcc, vcc, s[86:87]\n"
    "s_or_b64 vcc, vcc, s[88:89]\n"
    "s_or_b64 vcc, vcc, s[52:53]\n"
    "s_cbranch_scc1 L_redo0%=\n"
    "v_cmp_eq_u32 s[78:79], s50, v20\n"
    "v_cmp_eq_u32 s[80:81], s50, v21\n"
    "v_cmp_eq_u32 s[82:83], s50, v22\n"
    "v_cmp_eq_u32 s[84:85], s50, v23\n"
    "v_cmp_eq_u32 s[86:87], s50, v28\n"
    "v_cmp_eq_u32 s[88:89], s50, v29\n"
    "v_cmp_eq_u32 s[52:53], s50, v30\n"
    "v_cmp_eq_u32 vcc, s50, v31\n"
    "s_nop 0\n"
    "s_or_b64 vcc, vcc, s[78:79]\n"
    "s_or_b64 vcc, vcc, s[80:81]\n"
    "s_or_b64 vcc, vcc, s[82:83]\n"
    "s_or_b64 vcc, vcc, s[84:85]\n"
    "s_or_b64 vcc, vcc, s[86:87]\n"
    "s_or_b64 vcc, vcc, s[88:89]\n"
    "s_or_b64 vcc, vcc, s[52:53]\n"
    "s_cbranch_scc1 L_redo0%=\n"
    "s_waitcnt vmcnt(0)\n"
    "s_branch L_go0%=\n"
    "L_redo0%=:\n"
    "ds_read_b128 a[0:3], v116 offset:16384\n"
    "ds_read_b128 a[4:7], v116 offset:17408\n"
    "ds_read_b128 a[64:67], v116 offset:18432\n"
    "ds_read_b128 a[68:71], v116 offset:19456\n"
    "ds_read_b128 a[8:11], v116 offset:20480\n"
    "ds_read_b128 a[12:15], v116 offset:21504\n"
    "ds_read_b128 a[72:75], v116 offset:22528\n"
    "ds_read_b128 a[76:79], v116 offset:23552\n"
    "ds_read_b128 a[16:19], v116 offset:24576\n"
    "ds_read_b128 a[20:23], v116 offset:25600\n"
    "ds_read_b128 a[80:83], v116 offset:26624\n"
    "ds_read_b128 a[84:87], v116 offset:27648\n"
    "ds_read_b128 a[32:35], v116 offset:28672\n"
    "ds_read_b128 a[36:39], v116 offset:29696\n"
    "s_waitcnt lgkmcnt(8)\n"
    "ds_read_b128 a[96:99], v116 offset:30720\n"
    "ds_read_b128 a[100:103], v116 offset:31744\n"
    "v_mov_b64 v[32:33], 0\n"
    "v_mov_b64 v[34:35], 0\n"
    "v_mov_b64 v[36:37], 0\n"
    "v_mov_b64 v[38:39], 0\n"
    "v_mov_b64 v[40:41], 0\n"
    "v_mov_b64 v[42:43], 0\n"
    "v_mov_b64 v[44:45], 0\n"
    "v_mov_b64 v[46:47], 0\n"
    "v_mov_b64 v[48:49], 0\n"
    "v_mov_b64 v[50:51], 0\n"
    "v_mov_b64 v[52:53], 0\n"
    "v_mov_b64 v[54:55], 0\n"
    "s_add_u32 s90, s90, 1\n"
    "s_and_b32 s78, s90, 0xff\n"
    "s_cmp_lg_u32 s78, 0\n"
    "s_cbranch_scc1 L_re0%=\n"
    "v_mov_b32 v126, 0\n"
    "global_load_dwordx2 v[126:127], v126, s[68:69] sc1\n"
    "s_waitcnt vmcnt(0)\n"
    "v_cmp_eq_u64 vcc, s[96:97], v[126:127]\n"
    "s_nop 1\n"
    "s_cbranch_vccnz L_bail_g0%=\n"
    "s_cmp_lt_u32 s90, 0x40000\n"
    "s_cbranch_scc1 L_re0%=\n"
    "v_mov_b32 v126, s96\n"
    "v_mov_b32 v127, s97\n"
    "s_mov_b64 exec, 1\n"
    "global_store_dwordx2 v71, v[126:127], s[68:69] sc1\n"
    "s_mov_b64 exec, -1\n"
    "L_bail_g0%=:\n"
    "s_branch L_go0%=\n"
    "L_re0%=:\n"
    "s_waitcnt vmcnt(0) lgkmcnt(0)\n"
    "s_branch L_top0%=\n"
    "L_last0%=:\n"
    "s_mov_b64 exec, 1\n"
    "global_store_dwordx2 v71, v[84:85], s[64:65] sc1\n"
    "s_mov_b64 exec, -1\n"
    "v_smfmac_f32_16x16x64_bf16 v[52:55], v[12:15], a[216:223], v67\n"
    "v_smfmac_f32_16x16x64_bf16 v[48:51], v[20:23], a[168:175], v67\n"
    "v_smfmac_f32_16x16x64_bf16 v[52:55], v[20:23], a[232:239], v67\n"
    "v_smfmac_f32_16x16x64_bf16 v[48:51], v[28:31], a[184:191], v67\n"
    "v_smfmac_f32_16x16x64_bf16 v[52:55], v[28:31], a[248:255], v67\n"
    "s_add_u32 s11, s43, 0\n"
    "s_and_b32 s11, s11, 3\n"
    "s_cselect_b32 s11, 0, 0x2000\n"
    "s_add_u32 s62, s62, 0x80000\n"
    "s_addc_u32 s63, s63, 0\n"
    "s_xor_b32 s62, s62, s11\n"
    "v_pk_add_f32 v[48:49], v[48:49], v[50:51]\n"
    "s_nop 0\n"
    "v_pk_add_f32 v[52:53], v[52:53], v[54:55]\n"
    "v_cvt_pk_bf16_f32 v66, v48, v49\n"
    "v_cvt_pk_bf16_f32 v74, v52, v53\n"
    "s_cmp_lg_u32 s42, 0\n"
    "s_cbranch_scc1 L_vmc0%=\n"
    "s_mov_b32 exec_hi, 0\n"
    "global_store_dwordx4 v71, v[40:43], s[62:63] offset:0 sc1\n"
    "s_mov_b64 exec, -1\n"
    "s_branch L_kvc0%=\n"
    "L_vmc0%=:\n"
    "v_mov_b32 v126, v66\n"
    "v_mov_b32 v127, v74\n"
    "global_store_dwordx2 v79, v[126:127], s[62:63] offset:0 sc1\n"
    "L_kvc0%=:\n"
    "s_waitcnt vmcnt(0) lgkmcnt(0)\n"
    "s_endpgm\n"
    ".p2align 6\n"
    "L_poll1%=:\n"
    "global_load_dwordx4 v[8:11], v71, s[54:55] offset:0 sc1\n"
    "global_load_dwordx4 v[12:15], v71, s[54:55] offset:1024 sc1\n"
    "s_memtime s[16:17]\n"
    "s_mov_b32 s38, 0\n"
    "s_waitcnt lgkmcnt(0)\n"
    "s_add_u32 s14, s16, s46\n"
    "s_andn2_b32 s14, s14, 1\n"
    "s_or_b32 s14, s14, s38\n"
    "s_mov_b64 exec, 15\n"
    "v_mov_b32 v87, s14\n"
    "global_store_dwordx4 v118, v[84:87], s[56:57] sc1\n"
    "s_mov_b64 exec, -1\n"
    "s_mov_b32 m0, 86272\n"
    "s_waitcnt vmcnt(0)\n"
    "s_branch L_top1%=\n"
    ".p2align 6\n"
    "L_top1%=:\n"
    "s_waitcnt vmcnt(3)\n"
    "v_smfmac_f32_16x16x64_bf16 v[40:43], v[8:11], a[16:23], v67\n"
    "ds_write_b128 v71, v[8:11] offset:2048\n"
    "v_smfmac_f32_16x16x64_bf16 v[44:47], v[8:11], a[80:87], v67\n"
    "v_smfmac_f32_16x16x64_bf16 v[32:35], v[8:11], v[160:167], v67\n"
    "v_smfmac_f32_16x16x64_bf16 v[36:39], v[8:11], v[168:175], v67\n"
    "s_waitcnt vmcnt(2)\n"
    "v_smfmac_f32_16x16x64_bf16 v[40:43], v[12:15], a[24:31], v67\n"
    "ds_write_b128 v71, v[12:15] offset:3072\n"
    "s_waitcnt lgkmcnt(1)\n"
    "s_barrier\n"
    "ds_read_b128 v[0:3], v71 offset:0\n"
    "v_smfmac_f32_16x16x64_bf16 v[44:47], v[12:15], a[88:95], v67\n"
    "ds_read_b128 v[24:27], v71 offset:6144\n"
    "v_smfmac_f32_16x16x64_bf16 v[32:35], v[12:15], v[176:183], v67\n"
    "ds_read_b128 v[16:19], v71 offset:4096\n"
    "v_smfmac_f32_16x16x64_bf16 v[36:39], v[12:15], v[184:191], v67\n"
    "s_waitcnt lgkmcnt(3)\n"
    "s_barrier\n"
    "v_smfmac_f32_16x16x64_bf16 v[48:51], v[8:11], a[144:151], v67\n"
    "s_waitcnt lgkmcnt(2)\n"
    "ds_read_b128 v[4:7], v71 offset:1024\n"
    "v_smfmac_f32_16x16x64_bf16 v[32:35], v[0:3], v[128:135], v67\n"
    "ds_read_b128 v[28:31], v71 offset:7168\n"
    "v_smfmac_f32_16x16x64_bf16 v[36:39], v[0:3], v[136:143], v67\n"
    "s_waitcnt lgkmcnt(2)\n"
    "ds_read_b128 v[20:23], v71 offset:5120\n"
    "v_smfmac_f32_16x16x64_bf16 v[32:35], v[24:27], v[224:231], v67\n"
    "s_memtime s[6:7]\n"
    "ds_read_b128 a[16:19], v116 offset:0\n"
    "v_smfmac_f32_16x16x64_bf16 v[36:39], v[24:27], v[232:239], v67\n"
    "s_mov_b32 s78, 0x12345\n"
    "ds_read_b128 a[20:23], v116 offset:1024\n"
    "v_smfmac_f32_16x16x64_bf16 v[32:35], v[16:19], v[192:199], v67\n"
    "s_mov_b32 s78, 0x12345\n"
    "ds_read_b128 a[80:83], v116 offset:4096\n"
    "v_smfmac_f32_16x16x64_bf16 v[36:39], v[16:19], v[200:207], v67\n"
    "s_waitcnt lgkmcnt(5)\n"
    "ds_read_b128 a[84:87], v116 offset:5120\n"
    "v_smfmac_f32_16x16x64_bf16 v[32:35], v[4:7], v[144:151], v67\n"
    "s_sub_i32 s2, s6, s16\n"
    "ds_read_b128 a[24:27], v116 offset:2048\n"
    "v_smfmac_f32_16x16x64_bf16 v[36:39], v[4:7], v[152:159], v67\n"
    "s_waitcnt lgkmcnt(5)\n"
    "ds_read_b128 a[28:31], v116 offset:3072\n"
    "v_smfmac_f32_16x16x64_bf16 v[32:35], v[28:31], v[240:247], v67\n"
    "s_cmp_gt_i32 s2, s3\n"
    "ds_read_b128 a[88:91], v116 offset:6144\n"
    "v_smfmac_f32_16x16x64_bf16 v[36:39], v[28:31], v[248:255], v67\n"
    "ds_read_b128 a[92:95], v116 offset:7168\n"
    "v_smfmac_f32_16x16x64_bf16 v[32:35], v[20:23], v[208:215], v67\n"
    "s_cbranch_scc1 L_fix%=\n"
    "L_fixed%=:\n"
    "v_smfmac_f32_16x16x64_bf16 v[36:39], v[20:23], v[216:223], v67\n"
    "v_smfmac_f32_16x16x64_bf16 v[40:43], v[0:3], a[0:7], v67\n"
    "v_add_f32 v32, v32, v34\n"
    "v_add_f32_e64 v33, v33, v35\n"
    "v_smfmac_f32_16x16x64_bf16 v[44:47], v[0:3], a[64:71], v67\n"
    "ds_read_b128 a[0:3], v116 offset:8192\n"
    "v_cvt_pk_bf16_f32 v126, v32, v33\n"
    "v_add_f32 v36, v36, v38\n"
    "v_add_f32 v37, v37, v39\n"
    "v_cvt_pk_bf16_f32 v127, v36, v37\n"
    "v_smfmac_f32_16x16x64_bf16 v[40:43], v[24:27], a[48:55], v67\n"
    "ds_write_b64 v114, v[126:127] offset:8704\n"
    "v_smfmac_f32_16x16x64_bf16 v[44:47], v[24:27], a[112:119], v67\n"
    "ds_read_b128 a[4:7], v116 offset:9216\n"
    "v_cmp_u_f32_e64 vcc, v32, v33\n"
    "v_smfmac_f32_16x16x64_bf16 v[40:43], v[16:19], a[32:39], v67\n"
    "ds_read_b128 a[64:67], v116 offset:12288\n"
    "v_mov_b32_e64 v87, s76\n"
    "v_smfmac_f32_16x16x64_bf16 v[44:47], v[16:19], a[96:103], v67\n"
    "s_cbranch_vccnz L_slow1%=\n"
    "L_go1%=:\n"
    "v_smfmac_f32_16x16x64_bf16 v[40:43], v[4:7], a[8:15], v67\n"
    "s_waitcnt lgkmcnt(2)\n"
    "s_barrier\n"
    "ds_read_b64_tr_b16 v[32:33], v112 offset:8192\n"
    "v_smfmac_f32_16x16x64_bf16 v[44:47], v[4:7], a[72:79], v67\n"
    "ds_read_b64_tr_b16 v[34:35], v113 offset:8192\n"
    "ds_read_b64_tr_b16 v[36:37], v112 offset:9216\n"
    "v_smfmac_f32_16x16x64_bf16 v[40:43], v[28:31], a[56:63], v67\n"
    "ds_read_b64_tr_b16 v[38:39], v113 offset:9216\n"
    "v_smfmac_f32_16x16x64_bf16 v[44:47], v[28:31], a[120:127], v67\n"
    "global_load_dwordx4 v[96:99], v71, s[58:59] offset:-2048 sc1\n"
    "v_smfmac_f32_16x16x64_bf16 v[40:43], v[20:23], a[40:47], v67\n"
    "ds_read_b128 a[68:71], v116 offset:13312\n"
    "v_smfmac_f32_16x16x64_bf16 v[44:47], v[20:23], a[104:111], v67\n"
    "s_waitcnt lgkmcnt(1)\n"
    "ds_read_b128 a[48:51], v116 offset:10240\n"
    "v_mfma_f32_16x16x32_bf16 v[88:91], v[56:59], v[32:35], 0\n"
    "v_add_f32 v40, v40, v42\n"
    "v_add_f32 v41, v41, v43\n"
    "v_mfma_f32_16x16x32_bf16 v[88:91], v[60:63], v[36:39], v[88:91]\n"
    "v_cvt_pk_bf16_f32 v126, v40, v41\n"
    "v_add_f32 v44, v44, v46\n"
    "v_add_f32 v45, v45, v47\n"
    "v_cvt_pk_bf16_f32 v127, v44, v45\n"
    "v_smfmac_f32_16x16x64_bf16 v[52:55], v[8:11], a[208:215], v67\n"
    "ds_write_b64 v115, v[126:127] offset:10752\n"
    "v_add_f32_dpp v88, v90, v88 quad_perm:[2,3,2,3] row_mask:0xf bank_mask:0xf\n"
    "v_add_f32_dpp v89, v91, v89 quad_perm:[2,3,2,3] row_mask:0xf bank_mask:0xf\n"
    "v_smfmac_f32_16x16x64_bf16 v[48:51], v[12:15], a[152:159], v67\n"
    "ds_write_b64 v75, v[88:89] offset:14368\n"
    "v_smfmac_f32_16x16x64_bf16 v[52:55], v[12:15], a[216:223], v67\n"
    "global_load_dwordx4 v[100:103], v71, s[58:59] offset:2048 sc1\n"
    "v_smfmac_f32_16x16x64_bf16 v[48:51], v[0:3], a[128:135], v67\n"
    "global_load_lds_dwordx4 v71, s[60:61] offset:0 sc1\n"
    "v_smfmac_f32_16x16x64_bf16 v[52:55], v[0:3], a[192:199], v67\n"
    "global_load_lds_dwordx4 v71, s[60:61] offset:1024 sc1\n"
    "v_smfmac_f32_16x16x64_bf16 v[48:51], v[24:27], a[176:183], v67\n"
    "s_waitcnt lgkmcnt(0)\n"
    "s_barrier\n"
    "ds_read_b64_tr_b16 v[40:41], v112 offset:10240\n"
    "v_smfmac_f32_16x16x64_bf16 v[52:55], v[24:27], a[240:247], v67\n"
    "ds_read_b64_tr_b16 v[42:43], v113 offset:10240\n"
    "ds_read_b64_tr_b16 v[44:45], v112 offset:11264\n"
    "v_smfmac_f32_16x16x64_bf16 v[48:51], v[16:19], a[160:167], v67\n"
    "ds_read_b64_tr_b16 v[46:47], v113 offset:11264\n"
    "ds_read_b128 v[88:91], v71 offset:14336\n"
    "v_smfmac_f32_16x16x64_bf16 v[52:55], v[16:19], a[224:231], v67\n"
    "s_cmp_eq_u32 s43, 63\n"
    "ds_read_b128 a[52:55], v116 offset:11264\n"
    "v_smfmac_f32_16x16x64_bf16 v[48:51], v[4:7], a[136:143], v67\n"
    "s_waitcnt lgkmcnt(4)\n"
    "v_mfma_f32_16x16x32_bf16 v[92:95], v[40:43], v[32:35], 0\n"
    "s_waitcnt lgkmcnt(2)\n"
    "ds_read_b128 a[112:115], v116 offset:14336\n"
    "v_mfma_f32_16x16x32_bf16 v[92:95], v[44:47], v[36:39], v[92:95]\n"
    "s_cbranch_scc1 L_last1%=\n"
    "s_waitcnt lgkmcnt(2)\n"
    "v_max3_f32 v122, v88, v89, v90\n"
    "v_max_f32 v122, v122, v91\n"
    "v_smfmac_f32_16x16x64_bf16 v[52:55], v[4:7], a[200:207], v67\n"
    "ds_read_b128 a[116:119], v116 offset:15360\n"
    "v_max_f32_dpp v123, v122, v122 quad_perm:[2,3,0,1] row_mask:0xf bank_mask:0xf\n"
    "v_add_f32_dpp v92, v94, v92 quad_perm:[2,3,2,3] row_mask:0xf bank_mask:0xf\n"
    "v_add_f32_dpp v93, v95, v93 quad_perm:[2,3,2,3] row_mask:0xf bank_mask:0xf\n"
    "v_max3_f32 v122, v123, v92, v93\n"
    "v_smfmac_f32_16x16x64_bf16 v[48:51], v[28:31], a[184:191], v67\n"
    "v_mul_f32_dpp v121, v122, v87 quad_perm:[0,1,0,1] row_mask:0xf bank_mask:0xf\n"
    "v_mul_f32_dpp v123, v122, v87 quad_perm:[0,1,0,1] row_mask:0xf bank_mask:0xf\n"
    "v_smfmac_f32_16x16x64_bf16 v[52:55], v[28:31], a[248:255], v67\n"
    "v_permlane16_swap_b32 v123, v121\n"
    "v_max_f32 v122, v123, v121\n"
    "v_max_f32_e64 v123, v123, v121\n"
    "v_smfmac_f32_16x16x64_bf16 v[48:51], v[20:23], a[168:175], v67\n"
    "v_smfmac_f32_16x16x64_bf16 v[52:55], v[20:23], a[232:239], v67\n"
    "v_permlane32_swap_b32 v122, v123\n"
    "v_max_f32 v122, v122, v123\n"
    "v_pk_fma_f32 v[92:93], v[92:93], s[76:77], v[122:123] op_sel_hi:[1,1,0] neg_lo:[0,0,1] neg_hi:[0,0,1]\n"
    "v_pk_fma_f32 v[88:89], v[88:89], s[76:77], v[122:123] op_sel_hi:[1,1,0] neg_lo:[0,0,1] neg_hi:[0,0,1]\n"
    "v_pk_fma_f32 v[90:91], v[90:91], s[76:77], v[122:123] op_sel_hi:[1,1,0] neg_lo:[0,0,1] neg_hi:[0,0,1]\n"
    "v_exp_f32 v92, v92\n"
    "v_exp_f32 v93, v93\n"
    "v_exp_f32 v88, v88\n"
    "v_exp_f32 v89, v89\n"
    "v_exp_f32 v90, v90\n"
    "v_exp_f32 v91, v91\n"
    "v_pk_add_f32 v[48:49], v[48:49], v[50:51]\n"
    "v_pk_add_f32 v[52:53], v[52:53], v[54:55]\n"
    "v_cvt_pk_bf16_f32 v126, v92, v93\n"
    "v_cvt_pk_bf16_f32 v88, v88, v89\n"
    "v_cvt_pk_bf16_f32 v90, v90, v91\n"
    "v_cndmask_b32 v89, 0, v126, s[72:73]\n"
    "v_cndmask_b32 v91, 0, v126, s[74:75]\n"
    "v_cvt_pk_bf16_f32 v66, v48, v49\n"
    "v_cvt_pk_bf16_f32 v70, v48, v49\n"
    "v_cvt_pk_bf16_f32 v74, v52, v53\n"
    "v_cvt_pk_bf16_f32 v78, v52, v53\n"
    "v_smfmac_f32_16x16x64_bf16 v[104:107], v[88:91], v[64:71], v67\n"
    "v_mov_b64_e64 v[0:1], 0\n"
    "v_smfmac_f32_16x16x64_bf16 v[108:111], v[88:91], v[72:79], v67\n"
    "v_mov_b64_e64 v[2:3], 0\n"
    "v_mfma_f32_16x16x32_bf16 v[84:87], v[88:91], v[80:83], 0\n"
    "v_add_f32 v104, v104, v106\n"
    "v_add_f32 v105, v105, v107\n"
    "v_cvt_pk_bf16_f32 v104, v104, v105\n"
    "v_add_f32 v108, v108, v110\n"
    "v_add_f32 v109, v109, v111\n"
    "v_cvt_pk_bf16_f32 v105, v108, v109\n"
    "ds_write_b64 v114, v[104:105] offset:12800\n"
    "v_pk_add_f32 v[124:125], v[84:85], v[86:87]\n"
    "v_rcp_f32 v124, v124\n"
    "v_rcp_f32 v125, v125\n"
    "v_mov_b64 v[4:5], 0\n"
    "v_mov_b64 v[6:7], 0\n"
    "v_mov_b64 v[8:9], 0\n"
    "v_mov_b64 v[10:11], 0\n"
    "v_mov_b64 v[12:13], 0\n"
    "v_mov_b64 v[14:15], 0\n"
    "v_mfma_f32_16x16x32_bf16 v[92:95], v[80:83], v[80:83], 0\n"
    "s_waitcnt lgkmcnt(0)\n"
    "s_barrier\n"
    "ds_read_b64_tr_b16 v[32:33], v112 offset:12288\n"
    "v_mfma_f32_16x16x32_bf16 v[92:95], v[80:83], v[80:83], 0\n"
    "ds_read_b64_tr_b16 v[34:35], v113 offset:12288\n"
    "ds_read_b64_tr_b16 v[36:37], v112 offset:13312\n"
    "v_mfma_f32_16x16x32_bf16 v[92:95], v[80:83], v[80:83], 0\n"
    "ds_read_b64_tr_b16 v[38:39], v113 offset:13312\n"
    "v_mfma_f32_16x16x32_bf16 v[92:95], v[80:83], v[80:83], 0\n"
    "global_load_dwordx3 v[120:122], v119, s[12:13] offset:8 sc1\n"
    "s_waitcnt lgkmcnt(2)\n"
    "v_smfmac_f32_16x16x64_bf16 v[0:3], v[32:35], a[16:23], v67\n"
    "v_smfmac_f32_16x16x64_bf16 v[4:7], v[32:35], a[80:87], v67\n"
    "s_waitcnt lgkmcnt(0)\n"
    "v_smfmac_f32_16x16x64_bf16 v[0:3], v[36:39], a[24:31], v67\n"
    "v_smfmac_f32_16x16x64_bf16 v[4:7], v[36:39], a[88:95], v67\n"
    "v_smfmac_f32_16x16x64_bf16 v[8:11], v[32:35], a[0:7], v67\n"
    "v_add_f32_e64 v0, v0, v2\n"
    "v_smfmac_f32_16x16x64_bf16 v[12:15], v[32:35], a[64:71], v67\n"
    "v_add_f32_e64 v1, v1, v3\n"
    "v_smfmac_f32_16x16x64_bf16 v[8:11], v[36:39], a[48:55], v67\n"
    "v_add_f32_e64 v4, v4, v6\n"
    "v_smfmac_f32_16x16x64_bf16 v[12:15], v[36:39], a[112:119], v67\n"
    "v_add_f32 v5, v5, v7\n"
    "v_mul_f32 v0, v0, v124\n"
    "v_mul_f32_e64 v1, v1, v125\n"
    "v_cvt_pk_bf16_f32 v88, v0, v1\n"
    "v_pk_mul_f32 v[4:5], v[4:5], v[124:125]\n"
    "v_pk_add_f32 v[8:9], v[8:9], v[10:11]\n"
    "v_pk_add_f32 v[12:13], v[12:13], v[14:15]\n"
    "v_cvt_pk_bf16_f32 v89, v4, v5\n"
    "v_pk_mul_f32 v[8:9], v[8:9], v[124:125]\n"
    "v_pk_mul_f32 v[12:13], v[12:13], v[124:125]\n"
    "v_cvt_pk_bf16_f32 v90, v8, v9\n"
    "v_cvt_pk_bf16_f32 v91, v12, v13\n"
    "global_store_dwordx4 v117, v[88:91], s[44:45] sc1\n"
    "s_memtime s[8:9]\n"
    "s_add_u32 s43, s43, 1\n"
    "ds_read_b128 a[16:19], v116 offset:16384\n"
    "ds_read_b128 a[20:23], v116 offset:17408\n"
    "ds_read_b128 a[80:83], v116 offset:18432\n"
    "ds_read_b128 a[84:87], v116 offset:19456\n"
    "ds_read_b128 a[24:27], v116 offset:20480\n"
    "ds_read_b128 a[28:31], v116 offset:21504\n"
    "ds_read_b128 a[88:91], v116 offset:22528\n"
    "ds_read_b128 a[92:95], v116 offset:23552\n"
    "ds_read_b128 a[0:3], v116 offset:24576\n"
    "ds_read_b128 a[4:7], v116 offset:25600\n"
    "ds_read_b128 a[64:67], v116 offset:26624\n"
    "ds_read_b128 a[68:71], v116 offset:27648\n"
    "ds_read_b128 a[48:51], v116 offset:28672\n"
    "s_waitcnt lgkmcnt(8)\n"
    "ds_read_b128 a[52:55], v116 offset:29696\n"
    "ds_read_b128 a[112:115], v116 offset:30720\n"
    "ds_read_b128 a[116:119], v116 offset:31744\n"
    "s_and_b32 s11, s43, 3\n"
    "s_lshl_b32 s11, s11, 17\n"
    "s_add_u32 s54, s48, s11\n"
    "s_addc_u32 s55, s49, 0\n"
    "s_sub_u32 s19, s8, s16\n"
    "s_sub_i32 s2, s2, s15\n"
    "s_max_i32 s2, s2, -64\n"
    "s_min_i32 s2, s2, 64\n"
    "s_ashr_i32 s2, s2, 1\n"
    "s_add_i32 s15, s15, s2\n"
    "s_cmp_lt_u32 s43, 4\n"
    "s_cselect_b32 s46, 120, 0\n"
    "s_mov_b32 s47, s91\n"
    "s_waitcnt vmcnt(1)\n"
    "ds_read_b64 v[64:65], v116 offset:32768\n"
    "ds_read_b64 v[68:69], v116 offset:32776\n"
    "ds_read_b64 v[72:73], v116 offset:33792\n"
    "ds_read_b64 v[76:77], v116 offset:33800\n"
    "v_mov_b64 v[56:57], v[96:97]\n"
    "v_mov_b64 v[58:59], v[98:99]\n"
    "v_mov_b64 v[60:61], v[100:101]\n"
    "v_mov_b64 v[62:63], v[102:103]\n"
    "s_add_u32 s39, s96, s43\n"
    "v_cmp_eq_u32 s[52:53], s39, v122\n"
    "s_mov_b32 s11, 0\n"
    "L_lk%=:\n"
    "v_cmp_eq_u32 vcc, s39, v120\n"
    "v_and_b32 v122, 1, v121\n"
    "v_subrev_u32 v120, s14, v121\n"
    "v_cmp_eq_u32 s[100:101], 1, v122\n"
    "v_max_i32 v120, 0, v120\n"
    "v_cndmask_b32 v120, 0, v120, vcc\n"
    "s_and_b64 s[100:101], s[100:101], vcc\n"
    "s_not_b64 s[82:83], vcc\n"
    "v_max_u32_dpp v120, v120, v120 row_ror:1 row_mask:0xf bank_mask:0xf\n"
    "s_nop 1\n"
    "v_max_u32_dpp v120, v120, v120 row_ror:2 row_mask:0xf bank_mask:0xf\n"
    "s_nop 1\n"
    "v_max_u32_dpp v120, v120, v120 row_ror:4 row_mask:0xf bank_mask:0xf\n"
    "s_nop 1\n"
    "v_max_u32_dpp v120, v120, v120 row_ror:8 row_mask:0xf bank_mask:0xf\n"
    "s_memtime s[6:7]\n"
    "v_readlane_b32 s78, v120, 0\n"
    "v_readlane_b32 s79, v120, 16\n"
    "v_readlane_b32 s80, v120, 32\n"
    "v_readlane_b32 s81, v120, 48\n"
    "s_add_u32 s9, s14, 1030\n"
    "s_max_u32 s78, s78, s79\n"
    "s_max_u32 s80, s80, s81\n"
    "s_max_u32 s78, s78, s80\n"
    "s_min_u32 s78, s78, 0x4000\n"
    "s_add_u32 s9, s9, s78\n"
    "s_min_u32 s3, s78, 100\n"
    "s_add_u32 s3, s3, 30\n"
    "s_add_u32 s3, s3, s15\n"
    "s_sub_u32 s8, s9, s91\n"
    "s_sub_u32 s8, s8, s94\n"
    "s_waitcnt lgkmcnt(0)\n"
    "s_sub_i32 s78, s8, s6\n"
    "s_cmp_gt_i32 s78, 1300\n"
    "s_cbranch_scc1 L_far%=\n"
    "s_cmp_lg_u64 s[82:83], 0\n"
    "s_cbranch_scc1 L_miss%=\n"
    "L_near%=:\n"
    "s_cmp_lt_i32 s78, 640\n"
    "s_cbranch_scc1 L_sy%=\n"
    "L_wa%=:\n"
    "s_memtime s[6:7]\n"
    "s_waitcnt lgkmcnt(0)\n"
    "s_sub_i32 s78, s8, s6\n"
    "s_sub_u32 s79, s78, 540\n"
    "s_cmp_lt_u32 s79, 0x5000\n"
    "s_cbranch_scc1 L_wa%=\n"
    "L_sy%=:\n"
    "s_memtime s[6:7]\n"
    "s_memrealtime s[78:79]\n"
    "s_cmp_lg_u64 s[52:53], 0\n"
    "s_cselect_b64 s[100:101], 0, s[100:101]\n"
    "s_lshl_b32 s11, s0, 1\n"
    "s_sub_u32 s11, s11, s40\n"
    "s_memrealtime s[80:81]\n"
    "s_add_u32 s11, s11, 24\n"
    "s_min_u32 s11, s11, 700\n"
    "s_add_u32 s10, s0, 8\n"
    "s_cmp_eq_u32 s0, 0\n"
    "s_memrealtime s[82:83]\n"
    "s_cselect_b32 s10, 0, s10\n"
    "s_sub_i32 s0, s0, 4\n"
    "s_max_i32 s0, s0, s40\n"
    "s_cmp_lg_u64 s[52:53], 0\n"
    "s_memrealtime s[84:85]\n"
    "s_cselect_b32 s0, s11, s0\n"
    "s_cselect_b32 s40, s10, s40\n"
    "s_lshl_b32 s11, s1, 1\n"
    "s_sub_u32 s11, s11, s41\n"
    "s_memrealtime s[86:87]\n"
    "s_add_u32 s11, s11, 24\n"
    "s_min_u32 s11, s11, 700\n"
    "s_add_u32 s10, s1, 8\n"
    "s_cmp_eq_u32 s1, 0\n"
    "s_memrealtime s[88:89]\n"
    "s_cselect_b32 s10, 0, s10\n"
    "s_sub_i32 s1, s1, 4\n"
    "s_max_i32 s1, s1, s41\n"
    "s_cmp_lg_u64 s[100:101], 0\n"
    "s_cselect_b32 s1, s11, s1\n"
    "s_cselect_b32 s41, s10, s41\n"
    "s_mov_b32 s38, 0\n"
    "s_add_u32 s9, s9, s1\n"
    "s_add_u32 s46, s46, s0\n"
    "s_mov_b32 s10, 0\n"
    "s_waitcnt lgkmcnt(0)\n"
    "s_mul_i32 s91, s78, 24\n"
    "s_cmp_eq_u32 s78, s80\n"
    "s_addc_u32 s10, s10, 0\n"
    "s_cmp_eq_u32 s78, s82\n"
    "s_addc_u32 s10, s10, 0\n"
    "s_cmp_eq_u32 s78, s84\n"
    "s_addc_u32 s10, s10, 0\n"
    "s_cmp_eq_u32 s78, s86\n"
    "s_addc_u32 s10, s10, 0\n"
    "s_cmp_eq_u32 s78, s88\n"
    "s_addc_u32 s10, s10, 0\n"
    "s_mul_i32 s10, s10, 20\n"
    "s_sub_u32 s91, s91, s6\n"
    "s_sub_u32 s91, s91, s10\n"
    "s_sub_u32 s8, s9, s91\n"
    "s_sub_i32 s94, s91, s47\n"
    "s_add_i32 s19, s19, s94\n"
    "s_sub_i32 s19, s19, s51\n"
    "s_max_i32 s19, s19, -24\n"
    "s_min_i32 s19, s19, 100\n"
    "s_ashr_i32 s19, s19, 1\n"
    "s_add_i32 s51, s51, s19\n"
    "s_add_u32 s46, s46, s51\n"
    "s_add_u32 s46, s46, s91\n"
    "L_wb%=:\n"
    "s_memtime s[6:7]\n"
    "s_waitcnt lgkmcnt(0)\n"
    "s_sub_i32 s78, s8, s6\n"
    "s_sub_u32 s79, s78, 200\n"
    "s_cmp_lt_u32 s79, 0x5000\n"
    "s_cbranch_scc1 L_wb%=\n"
    "s_sub_i32 s78, s78, 84\n"
    "s_max_i32 s78, s78, 0\n"
    "s_min_i32 s78, s78, 124\n"
    "s_andn2_b32 s78, s78, 3\n"
    "s_sub_u32 s80, s4, s78\n"
    "s_subb_u32 s81, s5, 0\n"
    "s_setpc_b64 s[80:81]\n"
    "L_far%=:\n"
    "L_wc%=:\n"
    "s_memtime s[6:7]\n"
    "s_waitcnt lgkmcnt(0)\n"
    "s_sub_i32 s78, s8, s6\n"
    "s_sub_u32 s79, s78, 1300\n"
    "s_cmp_lt_u32 s79, 0x5000\n"
    "s_cbranch_scc1 L_wc%=\n"
    "L_miss%=:\n"
    "global_load_dwordx2 v[120:121], v119, s[12:13] offset:8 sc1\n"
    "s_add_u32 s11, s11, 1\n"
    "s_cmp_gt_u32 s11, 40\n"
    "s_waitcnt vmcnt(0)\n"
    "s_cbranch_scc0 L_lk%=\n"
    "s_branch L_near%=\n"
    ".p2align 6\n"
    "s_nop 0\n"
    "s_nop 0\n"
    "s_nop 0\n"
    "s_nop 0\n"
    "s_nop 0\n"
    "s_nop 0\n"
    "s_nop 0\n"
    "s_nop 0\n"
    "s_nop 0\n"
    "s_nop 0\n"
    "s_nop 0\n"
    "s_nop 0\n"
    "s_nop 0\n"
    "s_nop 0\n"
    "s_nop 0\n"
    "s_nop 0\n"
    "s_nop 0\n"
    "s_nop 0\n"
    "s_nop 0\n"
    "s_nop 0\n"
    "s_nop 0\n"
    "s_nop 0\n"
    "s_nop 0\n"
    "s_nop 0\n"
    "s_nop 0\n"
    "s_nop 0\n"
    "s_nop 0\n"
    "s_nop 0\n"
    "s_nop 0\n"
    "s_nop 0\n"
    "s_nop 0\n"
    "s_nop 0\n"
    "L_sled%=:\n"
    "s_barrier\n"
    "global_load_dwordx4 v[8:11], v71, s[54:55] offset:0 sc1\n"
    "s_nop 3\n"
    "global_load_dwordx4 v[12:15], v71, s[54:55] offset:1024 sc1\n"
    "s_memtime s[16:17]\n"
    "s_add_u32 s11, s43, -1\n"
    "s_and_b32 s11, s11, 3\n"
    "s_cselect_b32 s11, 0, 0x2000\n"
    "s_add_u32 s62, s62, 0x80000\n"
    "s_addc_u32 s63, s63, 0\n"
    "s_xor_b32 s62, s62, s11\n"
    "s_add_u32 s11, s43, 1\n"
    "s_and_b32 s11, s11, 3\n"
    "s_cselect_b32 s11, 0, 0x2000\n"
    "s_add_u32 s58, s58, 0x80000\n"
    "s_addc_u32 s59, s59, 0\n"
    "s_xor_b32 s58, s58, s11\n"
    "s_add_u32 s60, s60, 0x80000\n"
    "s_addc_u32 s61, s61, 0\n"
    "s_xor_b32 s60, s60, s11\n"
    "s_xor_b32 s44, s44, 0x8000\n"
    "s_cmp_lg_u32 s42, 0\n"
    "s_cbranch_scc1 L_vmb1%=\n"
    "s_mov_b32 exec_lo, 0\n"
    "global_store_dwordx4 v71, v[40:43], s[62:63] offset:0 sc1\n"
    "s_mov_b64 exec, -1\n"
    "s_branch L_kvb1%=\n"
    "L_vmb1%=:\n"
    "v_mov_b32 v126, v66\n"
    "v_mov_b32 v127, v74\n"
    "global_store_dwordx2 v79, v[126:127], s[62:63] offset:512 sc1\n"
    "L_kvb1%=:\n"
    "s_nop 1\n"
    "v_mov_b64 v[40:41], 0\n"
    "v_mov_b64 v[42:43], 0\n"
    "v_mov_b64 v[44:45], 0\n"
    "v_mov_b64 v[46:47], 0\n"
    "s_add_u32 s12, s12, 0x10000\n"
    "s_andn2_b32 s12, s12, 0x40000\n"
    "s_add_u32 s56, s56, 0x10000\n"
    "s_andn2_b32 s56, s56, 0x40000\n"
    "s_waitcnt lgkmcnt(0)\n"
    "v_mov_b64 v[32:33], 0\n"
    "v_mov_b64 v[34:35], 0\n"
    "v_mov_b64 v[36:37], 0\n"
    "v_mov_b64 v[38:39], 0\n"
    "v_mov_b64 v[48:49], 0\n"
    "v_mov_b64 v[50:51], 0\n"
    "v_mov_b64 v[52:53], 0\n"
    "v_mov_b64 v[54:55], 0\n"
    "v_mov_b64 v[104:105], 0\n"
    "v_mov_b64 v[106:107], 0\n"
    "v_mov_b64 v[108:109], 0\n"
    "v_mov_b64 v[110:111], 0\n"
    "v_or_b32 v84, 1, s96\n"
    "v_mov_b32 v85, s97\n"
    "v_add_u32 v86, s43, v84\n"
    "s_add_u32 s14, s16, s46\n"
    "s_andn2_b32 s14, s14, 1\n"
    "s_or_b32 s14, s14, s38\n"
    "s_mov_b64 exec, 15\n"
    "v_mov_b32 v87, s14\n"
    "global_store_dwordx4 v118, v[84:87], s[56:57] sc1\n"
    "s_mov_b64 exec, -1\n"
    "s_branch L_top1%=\n"
    "L_fix%=:\n"
    "s_sub_u32 s16, s6, s15\n"
    "s_add_u32 s16, s16, 60\n"
    "s_add_u32 s14, s16, s46\n"
    "s_andn2_b32 s14, s14, 1\n"
    "s_or_b32 s14, s14, s38\n"
    "s_mov_b64 exec, 15\n"
    "v_mov_b32 v87, s14\n"
    "global_store_dwordx4 v118, v[84:87], s[56:57] sc1\n"
    "s_mov_b64 exec, -1\n"
    "s_branch L_fixed%=\n"
    "L_slow1%=:\n"
    "v_cmp_eq_u32 s[78:79], s50, v8\n"
    "v_cmp_eq_u32 s[80:81], s50, v9\n"
    "v_cmp_eq_u32 s[82:83], s50, v10\n"
    "v_cmp_eq_u32 s[84:85], s50, v11\n"
    "v_cmp_eq_u32 s[86:87], s50, v12\n"
    "v_cmp_eq_u32 s[88:89], s50, v13\n"
    "v_cmp_eq_u32 s[52:53], s50, v14\n"
    "v_cmp_eq_u32 vcc, s50, v15\n"
    "global_load_dwordx4 v[8:11], v71, s[54:55] offset:0 sc1\n"
    "global_load_dwordx4 v[12:15], v71, s[54:55] offset:1024 sc1\n"
    "s_memtime s[16:17]\n"
    "s_waitcnt lgkmcnt(0)\n"
    "s_barrier\n"
    "s_add_u32 s16, s16, 400\n"
    "s_mov_b32 s38, 1\n"
    "s_add_u32 s14, s16, s46\n"
    "s_andn2_b32 s14, s14, 1\n"
    "s_or_b32 s14, s14, s38\n"
    "s_mov_b64 exec, 15\n"
    "v_mov_b32 v87, s14\n"
    "global_store_dwordx4 v118, v[84:87], s[56:57] sc1\n"
    "s_mov_b64 exec, -1\n"
    "s_or_b64 vcc, vcc, s[78:79]\n"
    "s_or_b64 vcc, vcc, s[80:81]\n"
    "s_or_b64 vcc, vcc, s[82:83]\n"
    "s_or_b64 vcc, vcc, s[84:85]\n"
    "s_or_b64 vcc, vcc, s[86:87]\n"
    "s_or_b64 vcc, vcc, s[88:89]\n"
    "s_or_b64 vcc, vcc, s[52:53]\n"
    "s_cbranch_scc1 L_redo1%=\n"
    "v_cmp_eq_u32 s[78:79], s50, v0\n"
    "v_cmp_eq_u32 s[80:81], s50, v1\n"
    "v_cmp_eq_u32 s[82:83], s50, v2\n"
    "v_cmp_eq_u32 s[84:85], s50, v3\n"
    "v_cmp_eq_u32 s[86:87], s50, v24\n"
    "v_cmp_eq_u32 s[88:89], s50, v25\n"
    "v_cmp_eq_u32 s[52:53], s50, v26\n"
    "v_cmp_eq_u32 vcc, s50, v27\n"
    "s_nop 0\n"
    "s_or_b64 vcc, vcc, s[78:79]\n"
    "s_or_b64 vcc, vcc, s[80:81]\n"
    "s_or_b64 vcc, vcc, s[82:83]\n"
    "s_or_b64 vcc, vcc, s[84:85]\n"
    "s_or_b64 vcc, vcc, s[86:87]\n"
    "s_or_b64 vcc, vcc, s[88:89]\n"
    "s_or_b64 vcc, vcc, s[52:53]\n"
    "s_cbranch_scc1 L_redo1%=\n"
    "v_cmp_eq_u32 s[78:79], s50, v16\n"
    "v_cmp_eq_u32 s[80:81], s50, v17\n"
    "v_cmp_eq_u32 s[82:83], s50, v18\n"
    "v_cmp_eq_u32 s[84:85], s50, v19\n"
    "v_cmp_eq_u32 s[86:87], s50, v4\n"
    "v_cmp_eq_u32 s[88:89], s50, v5\n"
    "v_cmp_eq_u32 s[52:53], s50, v6\n"
    "v_cmp_eq_u32 vcc, s50, v7\n"
    "s_nop 0\n"
    "s_or_b64 vcc, vcc, s[78:79]\n"
    "s_or_b64 vcc, vcc, s[80:81]\n"
    "s_or_b64 vcc, vcc, s[82:83]\n"
    "s_or_b64 vcc, vcc, s[84:85]\n"
    "s_or_b64 vcc, vcc, s[86:87]\n"
    "s_or_b64 vcc, vcc, s[88:89]\n"
    "s_or_b64 vcc, vcc, s[52:53]\n"
    "s_cbranch_scc1 L_redo1%=\n"
    "v_cmp_eq_u32 s[78:79], s50, v28\n"
    "v_cmp_eq_u32 s[80:81], s50, v29\n"
    "v_cmp_eq_u32 s[82:83], s50, v30\n"
    "v_cmp_eq_u32 s[84:85], s50, v31\n"
    "v_cmp_eq_u32 s[86:87], s50, v20\n"
    "v_cmp_eq_u32 s[88:89], s50, v21\n"
    "v_cmp_eq_u32 s[52:53], s50, v22\n"
    "v_cmp_eq_u32 vcc, s50, v23\n"
    "s_nop 0\n"
    "s_or_b64 vcc, vcc, s[78:79]\n"
    "s_or_b64 vcc, vcc, s[80:81]\n"
    "s_or_b64 vcc, vcc, s[82:83]\n"
    "s_or_b64 vcc, vcc, s[84:85]\n"
    "s_or_b64 vcc, vcc, s[86:87]\n"
    "s_or_b64 vcc, vcc, s[88:89]\n"
    "s_or_b64 vcc, vcc, s[52:53]\n"
    "s_cbranch_scc1 L_redo1%=\n"
    "s_waitcnt vmcnt(0)\n"
    "s_branch L_go1%=\n"
    "L_redo1%=:\n"
    "ds_read_b128 a[16:19], v116 offset:16384\n"
    "ds_read_b128 a[20:23], v116 offset:17408\n"
    "ds_read_b128 a[80:83], v116 offset:18432\n"
    "ds_read_b128 a[84:87], v116 offset:19456\n"
    "ds_read_b128 a[24:27], v116 offset:20480\n"
    "ds_read_b128 a[28:31], v116 offset:21504\n"
    "ds_read_b128 a[88:91], v116 offset:22528\n"
    "ds_read_b128 a[92:95], v116 offset:23552\n"
    "ds_read_b128 a[0:3], v116 offset:24576\n"
    "ds_read_b128 a[4:7], v116 offset:25600\n"
    "ds_read_b128 a[64:67], v116 offset:26624\n"
    "ds_read_b128 a[68:71], v116 offset:27648\n"
    "ds_read_b128 a[48:51], v116 offset:28672\n"
    "ds_read_b128 a[52:55], v116 offset:29696\n"
    "s_waitcnt lgkmcnt(8)\n"
    "ds_read_b128 a[112:115], v116 offset:30720\n"
    "ds_read_b128 a[116:119], v116 offset:31744\n"
    "v_mov_b64 v[32:33], 0\n"
    "v_mov_b64 v[34:35], 0\n"
    "v_mov_b64 v[36:37], 0\n"
    "v_mov_b64 v[38:39], 0\n"
    "v_mov_b64 v[40:41], 0\n"
    "v_mov_b64 v[42:43], 0\n"
    "v_mov_b64 v[44:45], 0\n"
    "v_mov_b64 v[46:47], 0\n"
    "v_mov_b64 v[48:49], 0\n"
    "v_mov_b64 v[50:51], 0\n"
    "v_mov_b64 v[52:53], 0\n"
    "v_mov_b64 v[54:55], 0\n"
    "s_add_u32 s90, s90, 1\n"
    "s_and_b32 s78, s90, 0xff\n"
    "s_cmp_lg_u32 s78, 0\n"
    "s_cbranch_scc1 L_re1%=\n"
    "v_mov_b32 v126, 0\n"
    "global_load_dwordx2 v[126:127], v126, s[68:69] sc1\n"
    "s_waitcnt vmcnt(0)\n"
    "v_cmp_eq_u64 vcc, s[96:97], v[126:127]\n"
    "s_nop 1\n"
    "s_cbranch_vccnz L_bail_g1%=\n"
    "s_cmp_lt_u32 s90, 0x40000\n"
    "s_cbranch_scc1 L_re1%=\n"
    "v_mov_b32 v126, s96\n"
    "v_mov_b32 v127, s97\n"
    "s_mov_b64 exec, 1\n"
    "global_store_dwordx2 v71, v[126:127], s[68:69] sc1\n"
    "s_mov_b64 exec, -1\n"
    "L_bail_g1%=:\n"
    "s_branch L_go1%=\n"
    "L_re1%=:\n"
    "s_waitcnt vmcnt(1) lgkmcnt(0)\n"
    "s_branch L_top1%=\n"
    "L_last1%=:\n"
    "v_smfmac_f32_16x16x64_bf16 v[52:55], v[4:7], a[200:207], v67\n"
    "v_smfmac_f32_16x16x64_bf16 v[48:51], v[28:31], a[184:191], v67\n"
    "v_smfmac_f32_16x16x64_bf16 v[52:55], v[28:31], a[248:255], v67\n"
    "v_smfmac_f32_16x16x64_bf16 v[48:51], v[20:23], a[168:175], v67\n"
    "v_smfmac_f32_16x16x64_bf16 v[52:55], v[20:23], a[232:239], v67\n"
    "s_add_u32 s11, s43, 0\n"
    "s_and_b32 s11, s11, 3\n"
    "s_cselect_b32 s11, 0, 0x2000\n"
    "s_add_u32 s62, s62, 0x80000\n"
    "s_addc_u32 s63, s63, 0\n"
    "s_xor_b32 s62, s62, s11\n"
    "v_pk_add_f32 v[48:49], v[48:49], v[50:51]\n"
    "s_nop 0\n"
    "v_pk_add_f32 v[52:53], v[52:53], v[54:55]\n"
    "v_cvt_pk_bf16_f32 v66, v48, v49\n"
    "v_cvt_pk_bf16_f32 v74, v52, v53\n"
    "s_cmp_lg_u32 s42, 0\n"
    "s_cbranch_scc1 L_vmc1%=\n"
    "s_mov_b32 exec_lo, 0\n"
    "global_store_dwordx4 v71, v[40:43], s[62:63] offset:0 sc1\n"
    "s_mov_b64 exec, -1\n"
    "s_branch L_kvc1%=\n"
    "L_vmc1%=:\n"
    "v_mov_b32 v126, v66\n"
    "v_mov_b32 v127, v74\n"
    "global_store_dwordx2 v79, v[126:127], s[62:63] offset:512 sc1\n"
    "L_kvc1%=:\n"
    "s_waitcnt vmcnt(0) lgkmcnt(0)\n"
    "s_endpgm\n"
    ".p2align 6\n"
    "L_poll2%=:\n"
    "global_load_dwordx4 v[16:19], v71, s[54:55] offset:0 sc1\n"
    "global_load_dwordx4 v[20:23], v71, s[54:55] offset:1024 sc1\n"
    "s_mov_b32 m0, 121088\n"
    "s_waitcnt vmcnt(0)\n"
    "s_branch L_top2%=\n"
    ".p2align 6\n"
    "L_top2%=:\n"
    "s_waitcnt vmcnt(2)\n"
    "v_smfmac_f32_16x16x64_bf16 v[40:43], v[16:19], a[32:39], v67\n"
    "ds_write_b128 v71, v[16:19] offset:4096\n"
    "v_smfmac_f32_16x16x64_bf16 v[44:47], v[16:19], a[96:103], v67\n"
    "v_smfmac_f32_16x16x64_bf16 v[32:35], v[16:19], v[192:199], v67\n"
    "v_smfmac_f32_16x16x64_bf16 v[36:39], v[16:19], v[200:207], v67\n"
    "s_waitcnt vmcnt(1)\n"
    "v_smfmac_f32_16x16x64_bf16 v[40:43], v[20:23], a[40:47], v67\n"
    "ds_write_b128 v71, v[20:23] offset:5120\n"
    "s_waitcnt lgkmcnt(1)\n"
    "s_barrier\n"
    "ds_read_b128 v[24:27], v71 offset:6144\n"
    "v_smfmac_f32_16x16x64_bf16 v[44:47], v[20:23], a[104:111], v67\n"
    "ds_read_b128 v[0:3], v71 offset:0\n"
    "v_smfmac_f32_16x16x64_bf16 v[32:35], v[20:23], v[208:215], v67\n"
    "ds_read_b128 v[8:11], v71 offset:2048\n"
    "v_smfmac_f32_16x16x64_bf16 v[36:39], v[20:23], v[216:223], v67\n"
    "s_waitcnt lgkmcnt(3)\n"
    "s_barrier\n"
    "v_smfmac_f32_16x16x64_bf16 v[48:51], v[16:19], a[160:167], v67\n"
    "s_waitcnt lgkmcnt(2)\n"
    "ds_read_b128 v[28:31], v71 offset:7168\n"
    "v_smfmac_f32_16x16x64_bf16 v[32:35], v[24:27], v[224:231], v67\n"
    "ds_read_b128 v[4:7], v71 offset:1024\n"
    "v_smfmac_f32_16x16x64_bf16 v[36:39], v[24:27], v[232:239], v67\n"
    "s_waitcnt lgkmcnt(2)\n"
    "ds_read_b128 v[12:15], v71 offset:3072\n"
    "v_smfmac_f32_16x16x64_bf16 v[32:35], v[0:3], v[128:135], v67\n"
    "s_mov_b32 s78, 0x12345\n"
    "ds_read_b128 a[32:35], v116 offset:0\n"
    "v_smfmac_f32_16x16x64_bf16 v[36:39], v[0:3], v[136:143], v67\n"
    "s_mov_b32 s78, 0x12345\n"
    "ds_read_b128 a[36:39], v116 offset:1024\n"
    "v_smfmac_f32_16x16x64_bf16 v[32:35], v[8:11], v[160:167], v67\n"
    "s_mov_b32 s78, 0x12345\n"
    "ds_read_b128 a[96:99], v116 offset:4096\n"
    "v_smfmac_f32_16x16x64_bf16 v[36:39], v[8:11], v[168:175], v67\n"
    "s_waitcnt lgkmcnt(5)\n"
    "ds_read_b128 a[100:103], v116 offset:5120\n"
    "v_smfmac_f32_16x16x64_bf16 v[32:35], v[28:31], v[240:247], v67\n"
    "s_sub_i32 s2, s6, s16\n"
    "ds_read_b128 a[40:43], v116 offset:2048\n"
    "v_smfmac_f32_16x16x64_bf16 v[36:39], v[28:31], v[248:255], v67\n"
    "s_waitcnt lgkmcnt(5)\n"
    "ds_read_b128 a[44:47], v116 offset:3072\n"
    "v_smfmac_f32_16x16x64_bf16 v[32:35], v[4:7], v[144:151], v67\n"
    "s_cmp_gt_i32 s2, s3\n"
    "ds_read_b128 a[104:107], v116 offset:6144\n"
    "v_smfmac_f32_16x16x64_bf16 v[36:39], v[4:7], v[152:159], v67\n"
    "ds_read_b128 a[108:111], v116 offset:7168\n"
    "v_smfmac_f32_16x16x64_bf16 v[32:35], v[12:15], v[176:183], v67\n"
    "s_nop 0\n"
    "v_smfmac_f32_16x16x64_bf16 v[36:39], v[12:15], v[184:191], v67\n"
    "v_smfmac_f32_16x16x64_bf16 v[40:43], v[24:27], a[48:55], v67\n"
    "v_add_f32 v32, v32, v34\n"
    "v_add_f32_e64 v33, v33, v35\n"
    "v_smfmac_f32_16x16x64_bf16 v[44:47], v[24:27], a[112:119], v67\n"
    "ds_read_b128 a[48:51], v116 offset:8192\n"
    "v_cvt_pk_bf16_f32 v126, v32, v33\n"
    "v_add_f32 v36, v36, v38\n"
    "v_add_f32 v37, v37, v39\n"
    "v_cvt_pk_bf16_f32 v127, v36, v37\n"
    "v_smfmac_f32_16x16x64_bf16 v[40:43], v[0:3], a[0:7], v67\n"
    "ds_write_b64 v114, v[126:127] offset:9216\n"
    "v_smfmac_f32_16x16x64_bf16 v[44:47], v[0:3], a[64:71], v67\n"
    "ds_read_b128 a[52:55], v116 offset:9216\n"
    "v_cmp_u_f32_e64 vcc, v32, v33\n"
    "v_smfmac_f32_16x16x64_bf16 v[40:43], v[8:11], a[16:23], v67\n"
    "ds_read_b128 a[112:115], v116 offset:12288\n"
    "v_mov_b32_e64 v87, s76\n"
    "v_smfmac_f32_16x16x64_bf16 v[44:47], v[8:11], a[80:87], v67\n"
    "s_cbranch_vccnz L_slow2%=\n"
    "L_go2%=:\n"
    "v_smfmac_f32_16x16x64_bf16 v[40:43], v[28:31], a[56:63], v67\n"
    "s_waitcnt lgkmcnt(2)\n"
    "s_barrier\n"
    "ds_read_b64_tr_b16 v[32:33], v112 offset:8192\n"
    "v_smfmac_f32_16x16x64_bf16 v[44:47], v[28:31], a[120:127], v67\n"
    "ds_read_b64_tr_b16 v[34:35], v113 offset:8192\n"
    "ds_read_b64_tr_b16 v[36:37], v112 offset:9216\n"
    "v_smfmac_f32_16x16x64_bf16 v[40:43], v[4:7], a[8:15], v67\n"
    "ds_read_b64_tr_b16 v[38:39], v113 offset:9216\n"
    "v_smfmac_f32_16x16x64_bf16 v[44:47], v[4:7], a[72:79], v67\n"
    "global_load_dwordx4 v[96:99], v71, s[58:59] offset:-2048 sc1\n"
    "v_smfmac_f32_16x16x64_bf16 v[40:43], v[12:15], a[24:31], v67\n"
    "ds_read_b128 a[116:119], v116 offset:13312\n"
    "v_smfmac_f32_16x16x64_bf16 v[44:47], v[12:15], a[88:95], v67\n"
    "s_waitcnt lgkmcnt(1)\n"
    "ds_read_b128 a[0:3], v116 offset:10240\n"
    "v_mfma_f32_16x16x32_bf16 v[88:91], v[56:59], v[32:35], 0\n"
    "v_add_f32 v40, v40, v42\n"
    "v_add_f32 v41, v41, v43\n"
    "v_mfma_f32_16x16x32_bf16 v[88:91], v[60:63], v[36:39], v[88:91]\n"
    "v_cvt_pk_bf16_f32 v126, v40, v41\n"
    "v_add_f32 v44, v44, v46\n"
    "v_add_f32 v45, v45, v47\n"
    "v_cvt_pk_bf16_f32 v127, v44, v45\n"
    "v_smfmac_f32_16x16x64_bf16 v[52:55], v[16:19], a[224:231], v67\n"
    "ds_write_b64 v115, v[126:127] offset:11264\n"
    "v_add_f32_dpp v88, v90, v88 quad_perm:[2,3,2,3] row_mask:0xf bank_mask:0xf\n"
    "v_add_f32_dpp v89, v91, v89 quad_perm:[2,3,2,3] row_mask:0xf bank_mask:0xf\n"
    "v_smfmac_f32_16x16x64_bf16 v[48:51], v[20:23], a[168:175], v67\n"
    "ds_write_b64 v75, v[88:89] offset:14848\n"
    "v_smfmac_f32_16x16x64_bf16 v[52:55], v[20:23], a[232:239], v67\n"
    "global_load_dwordx4 v[100:103], v71, s[58:59] offset:2048 sc1\n"
    "v_smfmac_f32_16x16x64_bf16 v[48:51], v[24:27], a[176:183], v67\n"
    "global_load_lds_dwordx4 v71, s[60:61] offset:0 sc1\n"
    "v_smfmac_f32_16x16x64_bf16 v[52:55], v[24:27], a[240:247], v67\n"
    "global_load_lds_dwordx4 v71, s[60:61] offset:1024 sc1\n"
    "v_smfmac_f32_16x16x64_bf16 v[48:51], v[0:3], a[128:135], v67\n"
    "s_waitcnt lgkmcnt(0)\n"
    "s_barrier\n"
    "ds_read_b64_tr_b16 v[40:41], v112 offset:10240\n"
    "v_smfmac_f32_16x16x64_bf16 v[52:55], v[0:3], a[192:199], v67\n"
    "ds_read_b64_tr_b16 v[42:43], v113 offset:10240\n"
    "ds_read_b64_tr_b16 v[44:45], v112 offset:11264\n"
    "v_smfmac_f32_16x16x64_bf16 v[48:51], v[8:11], a[144:151], v67\n"
    "ds_read_b64_tr_b16 v[46:47], v113 offset:11264\n"
    "ds_read_b128 v[88:91], v71 offset:14336\n"
    "v_smfmac_f32_16x16x64_bf16 v[52:55], v[8:11], a[208:215], v67\n"
    "s_cmp_eq_u32 s43, 63\n"
    "ds_read_b128 a[4:7], v116 offset:11264\n"
    "v_smfmac_f32_16x16x64_bf16 v[48:51], v[28:31], a[184:191], v67\n"
    "s_waitcnt lgkmcnt(4)\n"
    "v_mfma_f32_16x16x32_bf16 v[92:95], v[40:43], v[32:35], 0\n"
    "s_waitcnt lgkmcnt(2)\n"
    "ds_read_b128 a[64:67], v116 offset:14336\n"
    "v_mfma_f32_16x16x32_bf16 v[92:95], v[44:47], v[36:39], v[92:95]\n"
    "s_cbranch_scc1 L_last2%=\n"
    "s_waitcnt lgkmcnt(2)\n"
    "v_max3_f32 v122, v88, v89, v90\n"
    "v_max_f32 v122, v122, v91\n"
    "v_smfmac_f32_16x16x64_bf16 v[52:55], v[28:31], a[248:255], v67\n"
    "ds_read_b128 a[68:71], v116 offset:15360\n"
    "v_max_f32_dpp v123, v122, v122 quad_perm:[2,3,0,1] row_mask:0xf bank_mask:0xf\n"
    "v_add_f32_dpp v92, v94, v92 quad_perm:[2,3,2,3] row_mask:0xf bank_mask:0xf\n"
    "v_add_f32_dpp v93, v95, v93 quad_perm:[2,3,2,3] row_mask:0xf bank_mask:0xf\n"
    "v_max3_f32 v122, v123, v92, v93\n"
    "v_smfmac_f32_16x16x64_bf16 v[48:51], v[4:7], a[136:143], v67\n"
    "v_mul_f32_dpp v121, v122, v87 quad_perm:[0,1,0,1] row_mask:0xf bank_mask:0xf\n"
    "v_mul_f32_dpp v123, v122, v87 quad_perm:[0,1,0,1] row_mask:0xf bank_mask:0xf\n"
    "v_smfmac_f32_16x16x64_bf16 v[52:55], v[4:7], a[200:207], v67\n"
    "v_permlane16_swap_b32 v123, v121\n"
    "v_max_f32 v122, v123, v121\n"
    "v_max_f32_e64 v123, v123, v121\n"
    "v_smfmac_f32_16x16x64_bf16 v[48:51], v[12:15], a[152:159], v67\n"
    "v_smfmac_f32_16x16x64_bf16 v[52:55], v[12:15], a[216:223], v67\n"
    "v_permlane32_swap_b32 v122, v123\n"
    "v_max_f32 v122, v122, v123\n"
    "v_pk_fma_f32 v[92:93], v[92:93], s[76:77], v[122:123] op_sel_hi:[1,1,0] neg_lo:[0,0,1] neg_hi:[0,0,1]\n"
    "v_pk_fma_f32 v[88:89], v[88:89], s[76:77], v[122:123] op_sel_hi:[1,1,0] neg_lo:[0,0,1] neg_hi:[0,0,1]\n"
    "v_pk_fma_f32 v[90:91], v[90:91], s[76:77], v[122:123] op_sel_hi:[1,1,0] neg_lo:[0,0,1] neg_hi:[0,0,1]\n"
    "v_exp_f32 v92, v92\n"
    "v_exp_f32 v93, v93\n"
    "v_exp_f32 v88, v88\n"
    "v_exp_f32 v89, v89\n"
    "v_exp_f32 v90, v90\n"
    "v_exp_f32 v91, v91\n"
    "v_pk_add_f32 v[48:49], v[48:49], v[50:51]\n"
    "v_pk_add_f32 v[52:53], v[52:53], v[54:55]\n"
    "v_cvt_pk_bf16_f32 v126, v92, v93\n"
    "v_cvt_pk_bf16_f32 v88, v88, v89\n"
    "v_cvt_pk_bf16_f32 v90, v90, v91\n"
    "v_cndmask_b32 v89, 0, v126, s[72:73]\n"
    "v_cndmask_b32 v91, 0, v126, s[74:75]\n"
    "v_cvt_pk_bf16_f32 v66, v48, v49\n"
    "v_cvt_pk_bf16_f32 v70, v48, v49\n"
    "v_cvt_pk_bf16_f32 v74, v52, v53\n"
    "v_cvt_pk_bf16_f32 v78, v52, v53\n"
    "v_smfmac_f32_16x16x64_bf16 v[104:107], v[88:91], v[64:71], v67\n"
    "v_mov_b64_e64 v[0:1], 0\n"
    "v_smfmac_f32_16x16x64_bf16 v[108:111], v[88:91], v[72:79], v67\n"
    "v_mov_b64_e64 v[2:3], 0\n"
    "v_mfma_f32_16x16x32_bf16 v[84:87], v[88:91], v[80:83], 0\n"
    "v_add_f32 v104, v104, v106\n"
    "v_add_f32 v105, v105, v107\n"
    "v_cvt_pk_bf16_f32 v104, v104, v105\n"
    "v_add_f32 v108, v108, v110\n"
    "v_add_f32 v109, v109, v111\n"
    "v_cvt_pk_bf16_f32 v105, v108, v109\n"
    "ds_write_b64 v114, v[104:105] offset:13312\n"
    "v_pk_add_f32 v[124:125], v[84:85], v[86:87]\n"
    "v_rcp_f32 v124, v124\n"
    "v_rcp_f32 v125, v125\n"
    "v_mov_b64 v[4:5], 0\n"
    "v_mov_b64 v[6:7], 0\n"
    "v_mov_b64 v[8:9], 0\n"
    "v_mov_b64 v[10:11], 0\n"
    "v_mov_b64 v[12:13], 0\n"
    "v_mov_b64 v[14:15], 0\n"
    "v_mfma_f32_16x16x32_bf16 v[92:95], v[80:83], v[80:83], 0\n"
    "s_waitcnt lgkmcnt(0)\n"
    "s_barrier\n"
    "ds_read_b64_tr_b16 v[32:33], v112 offset:12288\n"
    "v_mfma_f32_16x16x32_bf16 v[92:95], v[80:83], v[80:83], 0\n"
    "ds_read_b64_tr_b16 v[34:35], v113 offset:12288\n"
    "ds_read_b64_tr_b16 v[36:37], v112 offset:13312\n"
    "v_mfma_f32_16x16x32_bf16 v[92:95], v[80:83], v[80:83], 0\n"
    "ds_read_b64_tr_b16 v[38:39], v113 offset:13312\n"
    "v_mfma_f32_16x16x32_bf16 v[92:95], v[80:83], v[80:83], 0\n"
    "s_waitcnt lgkmcnt(2)\n"
    "v_smfmac_f32_16x16x64_bf16 v[0:3], v[32:35], a[32:39], v67\n"
    "v_smfmac_f32_16x16x64_bf16 v[4:7], v[32:35], a[96:103], v67\n"
    "s_waitcnt lgkmcnt(0)\n"
    "v_smfmac_f32_16x16x64_bf16 v[0:3], v[36:39], a[40:47], v67\n"
    "v_smfmac_f32_16x16x64_bf16 v[4:7], v[36:39], a[104:111], v67\n"
    "v_smfmac_f32_16x16x64_bf16 v[8:11], v[32:35], a[48:55], v67\n"
    "v_add_f32_e64 v0, v0, v2\n"
    "v_smfmac_f32_16x16x64_bf16 v[12:15], v[32:35], a[112:119], v67\n"
    "v_add_f32_e64 v1, v1, v3\n"
    "v_smfmac_f32_16x16x64_bf16 v[8:11], v[36:39], a[0:7], v67\n"
    "v_add_f32_e64 v4, v4, v6\n"
    "v_smfmac_f32_16x16x64_bf16 v[12:15], v[36:39], a[64:71], v67\n"
    "v_add_f32 v5, v5, v7\n"
    "v_mul_f32 v0, v0, v124\n"
    "v_mul_f32_e64 v1, v1, v125\n"
    "v_cvt_pk_bf16_f32 v88, v0, v1\n"
    "v_pk_mul_f32 v[4:5], v[4:5], v[124:125]\n"
    "v_pk_add_f32 v[8:9], v[8:9], v[10:11]\n"
    "v_pk_add_f32 v[12:13], v[12:13], v[14:15]\n"
    "v_cvt_pk_bf16_f32 v89, v4, v5\n"
    "v_pk_mul_f32 v[8:9], v[8:9], v[124:125]\n"
    "v_pk_mul_f32 v[12:13], v[12:13], v[124:125]\n"
    "v_cvt_pk_bf16_f32 v90, v8, v9\n"
    "v_cvt_pk_bf16_f32 v91, v12, v13\n"
    "global_store_dwordx4 v117, v[88:91], s[44:45] sc1\n"
    "s_add_u32 s43, s43, 1\n"
    "s_sleep 3\n"
    "s_add_u32 s79, s43, 1\n"
    "s_lshr_b32 s79, s79, 1\n"
    "s_cmp_ge_u32 s79, 24\n"
    "s_cselect_b32 s79, 32, s79\n"
    "s_cmp_lt_u32 s79, s92\n"
    "s_cbranch_scc1 L_cntokl%=\n"
    "L_cntl%=:\n"
    "v_and_b32 v126, 16, v71\n"
    "v_lshlrev_b32 v126, 8, v126\n"
    "global_load_dwordx2 v[126:127], v126, s[66:67] sc1\n"
    "s_waitcnt vmcnt(0)\n"
    "v_xor_b32 v126, s96, v126\n"
    "v_xor_b32 v127, s97, v127\n"
    "v_cmp_gt_u64 vcc, 64, v[126:127]\n"
    "s_nop 1\n"
    "v_cndmask_b32 v126, 0, v126, vcc\n"
    "s_nop 0\n"
    "v_readlane_b32 s80, v126, 0\n"
    "v_readlane_b32 s81, v126, 1\n"
    "s_nop 1\n"
    "s_min_u32 s92, s80, s81\n"
    "s_cmp_lt_u32 s79, s92\n"
    "s_cbranch_scc1 L_cntokl%=\n"
    "s_sleep 2\n"
    "s_add_u32 s90, s90, 1\n"
    "s_and_b32 s78, s90, 0xff\n"
    "s_cmp_lg_u32 s78, 0\n"
    "s_cbranch_scc1 L_cntl%=\n"
    "v_mov_b32 v126, 0\n"
    "global_load_dwordx2 v[126:127], v126, s[68:69] sc1\n"
    "s_waitcnt vmcnt(0)\n"
    "v_cmp_eq_u64 vcc, s[96:97], v[126:127]\n"
    "s_nop 1\n"
    "s_cbranch_vccnz L_bail_cntl%=\n"
    "s_cmp_lt_u32 s90, 0x40000\n"
    "s_cbranch_scc1 L_cntl%=\n"
    "v_mov_b32 v126, s96\n"
    "v_mov_b32 v127, s97\n"
    "s_mov_b64 exec, 1\n"
    "global_store_dwordx2 v71, v[126:127], s[68:69] sc1\n"
    "s_mov_b64 exec, -1\n"
    "L_bail_cntl%=:\n"
    "L_cntokl%=:\n"
    "ds_read_b128 a[32:35], v116 offset:16384\n"
    "ds_read_b128 a[36:39], v116 offset:17408\n"
    "ds_read_b128 a[96:99], v116 offset:18432\n"
    "ds_read_b128 a[100:103], v116 offset:19456\n"
    "ds_read_b128 a[40:43], v116 offset:20480\n"
    "ds_read_b128 a[44:47], v116 offset:21504\n"
    "ds_read_b128 a[104:107], v116 offset:22528\n"
    "ds_read_b128 a[108:111], v116 offset:23552\n"
    "ds_read_b128 a[48:51], v116 offset:24576\n"
    "ds_read_b128 a[52:55], v116 offset:25600\n"
    "ds_read_b128 a[112:115], v116 offset:26624\n"
    "ds_read_b128 a[116:119], v116 offset:27648\n"
    "ds_read_b128 a[0:3], v116 offset:28672\n"
    "s_waitcnt lgkmcnt(8)\n"
    "ds_read_b128 a[4:7], v116 offset:29696\n"
    "ds_read_b128 a[64:67], v116 offset:30720\n"
    "ds_read_b128 a[68:71], v116 offset:31744\n"
    "s_and_b32 s11, s43, 3\n"
    "s_lshl_b32 s11, s11, 17\n"
    "s_add_u32 s54, s48, s11\n"
    "s_addc_u32 s55, s49, 0\n"
    "s_add_u32 s11, s43, -1\n"
    "s_and_b32 s11, s11, 3\n"
    "s_cselect_b32 s11, 0, 0x2000\n"
    "s_add_u32 s62, s62, 0x80000\n"
    "s_addc_u32 s63, s63, 0\n"
    "s_xor_b32 s62, s62, s11\n"
    "s_add_u32 s11, s43, 1\n"
    "s_and_b32 s11, s11, 3\n"
    "s_cselect_b32 s11, 0, 0x2000\n"
    "s_add_u32 s58, s58, 0x80000\n"
    "s_addc_u32 s59, s59, 0\n"
    "s_xor_b32 s58, s58, s11\n"
    "s_add_u32 s60, s60, 0x80000\n"
    "s_addc_u32 s61, s61, 0\n"
    "s_xor_b32 s60, s60, s11\n"
    "s_xor_b32 s44, s44, 0x8000\n"
    "v_mov_b64 v[32:33], 0\n"
    "v_mov_b64 v[34:35], 0\n"
    "v_mov_b64 v[36:37], 0\n"
    "v_mov_b64 v[38:39], 0\n"
    "v_mov_b64 v[48:49], 0\n"
    "v_mov_b64 v[50:51], 0\n"
    "v_mov_b64 v[52:53], 0\n"
    "v_mov_b64 v[54:55], 0\n"
    "v_mov_b64 v[104:105], 0\n"
    "v_mov_b64 v[106:107], 0\n"
    "v_mov_b64 v[108:109], 0\n"
    "v_mov_b64 v[110:111], 0\n"
    "s_waitcnt vmcnt(1)\n"
    "ds_read_b64 v[64:65], v116 offset:32768\n"
    "ds_read_b64 v[68:69], v116 offset:32776\n"
    "ds_read_b64 v[72:73], v116 offset:33792\n"
    "ds_read_b64 v[76:77], v116 offset:33800\n"
    "v_mov_b64 v[56:57], v[96:97]\n"
    "v_mov_b64 v[58:59], v[98:99]\n"
    "v_mov_b64 v[60:61], v[100:101]\n"
    "v_mov_b64 v[62:63], v[102:103]\n"
    "s_waitcnt lgkmcnt(0)\n"
    "s_barrier\n"
    "global_load_dwordx4 v[16:19], v71, s[54:55] offset:0 sc1\n"
    "s_nop 3\n"
    "global_load_dwordx4 v[20:23], v71, s[54:55] offset:1024 sc1\n"
    "s_nop 15\n"
    "s_cmp_lg_u32 s42, 0\n"
    "s_cbranch_scc1 L_vmb2%=\n"
    "s_mov_b32 exec_hi, 0\n"
    "global_store_dwordx4 v71, v[44:47], s[62:63] offset:1024 sc1\n"
    "s_mov_b64 exec, -1\n"
    "s_branch L_kvb2%=\n"
    "L_vmb2%=:\n"
    "v_mov_b32 v126, v66\n"
    "v_mov_b32 v127, v74\n"
    "global_store_dwordx2 v79, v[126:127], s[62:63] offset:1024 sc1\n"
    "L_kvb2%=:\n"
    "s_nop 1\n"
    "v_mov_b64 v[40:41], 0\n"
    "v_mov_b64 v[42:43], 0\n"
    "v_mov_b64 v[44:45], 0\n"
    "v_mov_b64 v[46:47], 0\n"
    "s_branch L_top2%=\n"
    "L_slow2%=:\n"
    "v_cmp_eq_u32 s[78:79], s50, v16\n"
    "v_cmp_eq_u32 s[80:81], s50, v17\n"
    "v_cmp_eq_u32 s[82:83], s50, v18\n"
    "v_cmp_eq_u32 s[84:85], s50, v19\n"
    "v_cmp_eq_u32 s[86:87], s50, v20\n"
    "v_cmp_eq_u32 s[88:89], s50, v21\n"
    "v_cmp_eq_u32 s[52:53], s50, v22\n"
    "v_cmp_eq_u32 vcc, s50, v23\n"
    "global_load_dwordx4 v[16:19], v71, s[54:55] offset:0 sc1\n"
    "global_load_dwordx4 v[20:23], v71, s[54:55] offset:1024 sc1\n"
    "s_waitcnt lgkmcnt(0)\n"
    "s_barrier\n"
    "s_or_b64 vcc, vcc, s[78:79]\n"
    "s_or_b64 vcc, vcc, s[80:81]\n"
    "s_or_b64 vcc, vcc, s[82:83]\n"
    "s_or_b64 vcc, vcc, s[84:85]\n"
    "s_or_b64 vcc, vcc, s[86:87]\n"
    "s_or_b64 vcc, vcc, s[88:89]\n"
    "s_or_b64 vcc, vcc, s[52:53]\n"
    "s_cbranch_scc1 L_redo2%=\n"
    "v_cmp_eq_u32 s[78:79], s50, v24\n"
    "v_cmp_eq_u32 s[80:81], s50, v25\n"
    "v_cmp_eq_u32 s[82:83], s50, v26\n"
    "v_cmp_eq_u32 s[84:85], s50, v27\n"
    "v_cmp_eq_u32 s[86:87], s50, v0\n"
    "v_cmp_eq_u32 s[88:89], s50, v1\n"
    "v_cmp_eq_u32 s[52:53], s50, v2\n"
    "v_cmp_eq_u32 vcc, s50, v3\n"
    "s_nop 0\n"
    "s_or_b64 vcc, vcc, s[78:79]\n"
    "s_or_b64 vcc, vcc, s[80:81]\n"
    "s_or_b64 vcc, vcc, s[82:83]\n"
    "s_or_b64 vcc, vcc, s[84:85]\n"
    "s_or_b64 vcc, vcc, s[86:87]\n"
    "s_or_b64 vcc, vcc, s[88:89]\n"
    "s_or_b64 vcc, vcc, s[52:53]\n"
    "s_cbranch_scc1 L_redo2%=\n"
    "v_cmp_eq_u32 s[78:79], s50, v8\n"
    "v_cmp_eq_u32 s[80:81], s50, v9\n"
    "v_cmp_eq_u32 s[82:83], s50, v10\n"
    "v_cmp_eq_u32 s[84:85], s50, v11\n"
    "v_cmp_eq_u32 s[86:87], s50, v28\n"
    "v_cmp_eq_u32 s[88:89], s50, v29\n"
    "v_cmp_eq_u32 s[52:53], s50, v30\n"
    "v_cmp_eq_u32 vcc, s50, v31\n"
    "s_nop 0\n"
    "s_or_b64 vcc, vcc, s[78:79]\n"
    "s_or_b64 vcc, vcc, s[80:81]\n"
    "s_or_b64 vcc, vcc, s[82:83]\n"
    "s_or_b64 vcc, vcc, s[84:85]\n"
    "s_or_b64 vcc, vcc, s[86:87]\n"
    "s_or_b64 vcc, vcc, s[88:89]\n"
    "s_or_b64 vcc, vcc, s[52:53]\n"
    "s_cbranch_scc1 L_redo2%=\n"
    "v_cmp_eq_u32 s[78:79], s50, v4\n"
    "v_cmp_eq_u32 s[80:81], s50, v5\n"
    "v_cmp_eq_u32 s[82:83], s50, v6\n"
    "v_cmp_eq_u32 s[84:85], s50, v7\n"
    "v_cmp_eq_u32 s[86:87], s50, v12\n"
    "v_cmp_eq_u32 s[88:89], s50, v13\n"
    "v_cmp_eq_u32 s[52:53], s50, v14\n"
    "v_cmp_eq_u32 vcc, s50, v15\n"
    "s_nop 0\n"
    "s_or_b64 vcc, vcc, s[78:79]\n"
    "s_or_b64 vcc, vcc, s[80:81]\n"
    "s_or_b64 vcc, vcc, s[82:83]\n"
    "s_or_b64 vcc, vcc, s[84:85]\n"
    "s_or_b64 vcc, vcc, s[86:87]\n"
    "s_or_b64 vcc, vcc, s[88:89]\n"
    "s_or_b64 vcc, vcc, s[52:53]\n"
    "s_cbranch_scc1 L_redo2%=\n"
    "s_waitcnt vmcnt(0)\n"
    "s_branch L_go2%=\n"
    "L_redo2%=:\n"
    "ds_read_b128 a[32:35], v116 offset:16384\n"
    "ds_read_b128 a[36:39], v116 offset:17408\n"
    "ds_read_b128 a[96:99], v116 offset:18432\n"
    "ds_read_b128 a[100:103], v116 offset:19456\n"
    "ds_read_b128 a[40:43], v116 offset:20480\n"
    "ds_read_b128 a[44:47], v116 offset:21504\n"
    "ds_read_b128 a[104:107], v116 offset:22528\n"
    "ds_read_b128 a[108:111], v116 offset:23552\n"
    "ds_read_b128 a[48:51], v116 offset:24576\n"
    "ds_read_b128 a[52:55], v116 offset:25600\n"
    "ds_read_b128 a[112:115], v116 offset:26624\n"
    "ds_read_b128 a[116:119], v116 offset:27648\n"
    "ds_read_b128 a[0:3], v116 offset:28672\n"
    "ds_read_b128 a[4:7], v116 offset:29696\n"
    "s_waitcnt lgkmcnt(8)\n"
    "ds_read_b128 a[64:67], v116 offset:30720\n"
    "ds_read_b128 a[68:71], v116 offset:31744\n"
    "v_mov_b64 v[32:33], 0\n"
    "v_mov_b64 v[34:35], 0\n"
    "v_mov_b64 v[36:37], 0\n"
    "v_mov_b64 v[38:39], 0\n"
    "v_mov_b64 v[40:41], 0\n"
    "v_mov_b64 v[42:43], 0\n"
    "v_mov_b64 v[44:45], 0\n"
    "v_mov_b64 v[46:47], 0\n"
    "v_mov_b64 v[48:49], 0\n"
    "v_mov_b64 v[50:51], 0\n"
    "v_mov_b64 v[52:53], 0\n"
    "v_mov_b64 v[54:55], 0\n"
    "s_add_u32 s90, s90, 1\n"
    "s_and_b32 s78, s90, 0xff\n"
    "s_cmp_lg_u32 s78, 0\n"
    "s_cbranch_scc1 L_re2%=\n"
    "v_mov_b32 v126, 0\n"
    "global_load_dwordx2 v[126:127], v126, s[68:69] sc1\n"
    "s_waitcnt vmcnt(0)\n"
    "v_cmp_eq_u64 vcc, s[96:97], v[126:127]\n"
    "s_nop 1\n"
    "s_cbranch_vccnz L_bail_g2%=\n"
    "s_cmp_lt_u32 s90, 0x40000\n"
    "s_cbranch_scc1 L_re2%=\n"
    "v_mov_b32 v126, s96\n"
    "v_mov_b32 v127, s97\n"
    "s_mov_b64 exec, 1\n"
    "global_store_dwordx2 v71, v[126:127], s[68:69] sc1\n"
    "s_mov_b64 exec, -1\n"
    "L_bail_g2%=:\n"
    "s_branch L_go2%=\n"
    "L_re2%=:\n"
    "s_waitcnt vmcnt(0) lgkmcnt(0)\n"
    "s_branch L_top2%=\n"
    "L_last2%=:\n"
    "v_smfmac_f32_16x16x64_bf16 v[52:55], v[28:31], a[248:255], v67\n"
    "v_smfmac_f32_16x16x64_bf16 v[48:51], v[4:7], a[136:143], v67\n"
    "v_smfmac_f32_16x16x64_bf16 v[52:55], v[4:7], a[200:207], v67\n"
    "v_smfmac_f32_16x16x64_bf16 v[48:51], v[12:15], a[152:159], v67\n"
    "v_smfmac_f32_16x16x64_bf16 v[52:55], v[12:15], a[216:223], v67\n"
    "s_add_u32 s11, s43, 0\n"
    "s_and_b32 s11, s11, 3\n"
    "s_cselect_b32 s11, 0, 0x2000\n"
    "s_add_u32 s62, s62, 0x80000\n"
    "s_addc_u32 s63, s63, 0\n"
    "s_xor_b32 s62, s62, s11\n"
    "v_pk_add_f32 v[48:49], v[48:49], v[50:51]\n"
    "s_nop 0\n"
    "v_pk_add_f32 v[52:53], v[52:53], v[54:55]\n"
    "v_cvt_pk_bf16_f32 v66, v48, v49\n"
    "v_cvt_pk_bf16_f32 v74, v52, v53\n"
    "s_cmp_lg_u32 s42, 0\n"
    "s_cbranch_scc1 L_vmc2%=\n"
    "s_mov_b32 exec_hi, 0\n"
    "global_store_dwordx4 v71, v[44:47], s[62:63] offset:1024 sc1\n"
    "s_mov_b64 exec, -1\n"
    "s_branch L_kvc2%=\n"
    "L_vmc2%=:\n"
    "v_mov_b32 v126, v66\n"
    "v_mov_b32 v127, v74\n"
    "global_store_dwordx2 v79, v[126:127], s[62:63] offset:1024 sc1\n"
    "L_kvc2%=:\n"
    "s_waitcnt vmcnt(0) lgkmcnt(0)\n"
    "s_endpgm\n"
    ".p2align 6\n"
    "L_poll3%=:\n"
    "global_load_dwordx4 v[24:27], v71, s[54:55] offset:0 sc1\n"
    "global_load_dwordx4 v[28:31], v71, s[54:55] offset:1024 sc1\n"
    "s_mov_b32 m0, 155904\n"
    "s_waitcnt vmcnt(0)\n"
    "s_branch L_top3%=\n"
    ".p2align 6\n"
    "L_top3%=:\n"
    "s_waitcnt vmcnt(2)\n"
    "v_smfmac_f32_16x16x64_bf16 v[40:43], v[24:27], a[48:55], v67\n"
    "ds_write_b128 v71, v[24:27] offset:6144\n"
    "v_smfmac_f32_16x16x64_bf16 v[44:47], v[24:27], a[112:119], v67\n"
    "v_smfmac_f32_16x16x64_bf16 v[32:35], v[24:27], v[224:231], v67\n"
    "v_smfmac_f32_16x16x64_bf16 v[36:39], v[24:27], v[232:239], v67\n"
    "s_waitcnt vmcnt(1)\n"
    "v_smfmac_f32_16x16x64_bf16 v[40:43], v[28:31], a[56:63], v67\n"
    "ds_write_b128 v71, v[28:31] offset:7168\n"
    "s_waitcnt lgkmcnt(1)\n"
    "s_barrier\n"
    "ds_read_b128 v[16:19], v71 offset:4096\n"
    "v_smfmac_f32_16x16x64_bf16 v[44:47], v[28:31], a[120:127], v67\n"
    "ds_read_b128 v[8:11], v71 offset:2048\n"
    "v_smfmac_f32_16x16x64_bf16 v[32:35], v[28:31], v[240:247], v67\n"
    "ds_read_b128 v[0:3], v71 offset:0\n"
    "v_smfmac_f32_16x16x64_bf16 v[36:39], v[28:31], v[248:255], v67\n"
    "s_waitcnt lgkmcnt(3)\n"
    "s_barrier\n"
    "v_smfmac_f32_16x16x64_bf16 v[48:51], v[24:27], a[176:183], v67\n"
    "s_waitcnt lgkmcnt(2)\n"
    "ds_read_b128 v[20:23], v71 offset:5120\n"
    "v_smfmac_f32_16x16x64_bf16 v[32:35], v[16:19], v[192:199], v67\n"
    "ds_read_b128 v[12:15], v71 offset:3072\n"
    "v_smfmac_f32_16x16x64_bf16 v[36:39], v[16:19], v[200:207], v67\n"
    "s_waitcnt lgkmcnt(2)\n"
    "ds_read_b128 v[4:7], v71 offset:1024\n"
    "v_smfmac_f32_16x16x64_bf16 v[32:35], v[8:11], v[160:167], v67\n"
    "s_mov_b32 s78, 0x12345\n"
    "ds_read_b128 a[48:51], v116 offset:0\n"
    "v_smfmac_f32_16x16x64_bf16 v[36:39], v[8:11], v[168:175], v67\n"
    "s_mov_b32 s78, 0x12345\n"
    "ds_read_b128 a[52:55], v116 offset:1024\n"
    "v_smfmac_f32_16x16x64_bf16 v[32:35], v[0:3], v[128:135], v67\n"
    "s_mov_b32 s78, 0x12345\n"
    "ds_read_b128 a[112:115], v116 offset:4096\n"
    "v_smfmac_f32_16x16x64_bf16 v[36:39], v[0:3], v[136:143], v67\n"
    "s_waitcnt lgkmcnt(5)\n"
    "ds_read_b128 a[116:119], v116 offset:5120\n"
    "v_smfmac_f32_16x16x64_bf16 v[32:35], v[20:23], v[208:215], v67\n"
    "s_sub_i32 s2, s6, s16\n"
    "ds_read_b128 a[56:59], v116 offset:2048\n"
    "v_smfmac_f32_16x16x64_bf16 v[36:39], v[20:23], v[216:223], v67\n"
    "s_waitcnt lgkmcnt(5)\n"
    "ds_read_b128 a[60:63], v116 offset:3072\n"
    "v_smfmac_f32_16x16x64_bf16 v[32:35], v[12:15], v[176:183], v67\n"
    "s_cmp_gt_i32 s2, s3\n"
    "ds_read_b128 a[120:123], v116 offset:6144\n"
    "v_smfmac_f32_16x16x64_bf16 v[36:39], v[12:15], v[184:191], v67\n"
    "ds_read_b128 a[124:127], v116 offset:7168\n"
    "v_smfmac_f32_16x16x64_bf16 v[32:35], v[4:7], v[144:151], v67\n"
    "s_nop 0\n"
    "v_smfmac_f32_16x16x64_bf16 v[36:39], v[4:7], v[152:159], v67\n"
    "v_smfmac_f32_16x16x64_bf16 v[40:43], v[16:19], a[32:39], v67\n"
    "v_add_f32 v32, v32, v34\n"
    "v_add_f32_e64 v33, v33, v35\n"
    "v_smfmac_f32_16x16x64_bf16 v[44:47], v[16:19], a[96:103], v67\n"
    "ds_read_b128 a[32:35], v116 offset:8192\n"
    "v_cvt_pk_bf16_f32 v126, v32, v33\n"
    "v_add_f32 v36, v36, v38\n"
    "v_add_f32 v37, v37, v39\n"
    "v_cvt_pk_bf16_f32 v127, v36, v37\n"
    "v_smfmac_f32_16x16x64_bf16 v[40:43], v[8:11], a[16:23], v67\n"
    "ds_write_b64 v114, v[126:127] offset:9728\n"
    "v_smfmac_f32_16x16x64_bf16 v[44:47], v[8:11], a[80:87], v67\n"
    "ds_read_b128 a[36:39], v116 offset:9216\n"
    "v_cmp_u_f32_e64 vcc, v32, v33\n"
    "v_smfmac_f32_16x16x64_bf16 v[40:43], v[0:3], a[0:7], v67\n"
    "ds_read_b128 a[96:99], v116 offset:12288\n"
    "v_mov_b32_e64 v87, s76\n"
    "v_smfmac_f32_16x16x64_bf16 v[44:47], v[0:3], a[64:71], v67\n"
    "s_cbranch_vccnz L_slow3%=\n"
    "L_go3%=:\n"
    "v_smfmac_f32_16x16x64_bf16 v[40:43], v[20:23], a[40:47], v67\n"
    "s_waitcnt lgkmcnt(2)\n"
    "s_barrier\n"
    "ds_read_b64_tr_b16 v[32:33], v112 offset:8192\n"
    "v_smfmac_f32_16x16x64_bf16 v[44:47], v[20:23], a[104:111], v67\n"
    "ds_read_b64_tr_b16 v[34:35], v113 offset:8192\n"
    "ds_read_b64_tr_b16 v[36:37], v112 offset:9216\n"
    "v_smfmac_f32_16x16x64_bf16 v[40:43], v[12:15], a[24:31], v67\n"
    "ds_read_b64_tr_b16 v[38:39], v113 offset:9216\n"
    "v_smfmac_f32_16x16x64_bf16 v[44:47], v[12:15], a[88:95], v67\n"
    "global_load_dwordx4 v[96:99], v71, s[58:59] offset:-2048 sc1\n"
    "v_smfmac_f32_16x16x64_bf16 v[40:43], v[4:7], a[8:15], v67\n"
    "ds_read_b128 a[100:103], v116 offset:13312\n"
    "v_smfmac_f32_16x16x64_bf16 v[44:47], v[4:7], a[72:79], v67\n"
    "s_waitcnt lgkmcnt(1)\n"
    "ds_read_b128 a[16:19], v116 offset:10240\n"
    "v_mfma_f32_16x16x32_bf16 v[88:91], v[56:59], v[32:35], 0\n"
    "v_add_f32 v40, v40, v42\n"
    "v_add_f32 v41, v41, v43\n"
    "v_mfma_f32_16x16x32_bf16 v[88:91], v[60:63], v[36:39], v[88:91]\n"
    "v_cvt_pk_bf16_f32 v126, v40, v41\n"
    "v_add_f32 v44, v44, v46\n"
    "v_add_f32 v45, v45, v47\n"
    "v_cvt_pk_bf16_f32 v127, v44, v45\n"
    "v_smfmac_f32_16x16x64_bf16 v[52:55], v[24:27], a[240:247], v67\n"
    "ds_write_b64 v115, v[126:127] offset:11776\n"
    "v_add_f32_dpp v88, v90, v88 quad_perm:[2,3,2,3] row_mask:0xf bank_mask:0xf\n"
    "v_add_f32_dpp v89, v91, v89 quad_perm:[2,3,2,3] row_mask:0xf bank_mask:0xf\n"
    "v_smfmac_f32_16x16x64_bf16 v[48:51], v[28:31], a[184:191], v67\n"
    "ds_write_b64 v75, v[88:89] offset:14880\n"
    "v_smfmac_f32_16x16x64_bf16 v[52:55], v[28:31], a[248:255], v67\n"
    "global_load_dwordx4 v[100:103], v71, s[58:59] offset:2048 sc1\n"
    "v_smfmac_f32_16x16x64_bf16 v[48:51], v[16:19], a[160:167], v67\n"
    "global_load_lds_dwordx4 v71, s[60:61] offset:0 sc1\n"
    "v_smfmac_f32_16x16x64_bf16 v[52:55], v[16:19], a[224:231], v67\n"
    "global_load_lds_dwordx4 v71, s[60:61] offset:1024 sc1\n"
    "v_smfmac_f32_16x16x64_bf16 v[48:51], v[8:11], a[144:151], v67\n"
    "s_waitcnt lgkmcnt(0)\n"
    "s_barrier\n"
    "ds_read_b64_tr_b16 v[40:41], v112 offset:10240\n"
    "v_smfmac_f32_16x16x64_bf16 v[52:55], v[8:11], a[208:215], v67\n"
    "ds_read_b64_tr_b16 v[42:43], v113 offset:10240\n"
    "ds_read_b64_tr_b16 v[44:45], v112 offset:11264\n"
    "v_smfmac_f32_16x16x64_bf16 v[48:51], v[0:3], a[128:135], v67\n"
    "ds_read_b64_tr_b16 v[46:47], v113 offset:11264\n"
    "ds_read_b128 v[88:91], v71 offset:14336\n"
    "v_smfmac_f32_16x16x64_bf16 v[52:55], v[0:3], a[192:199], v67\n"
    "s_cmp_eq_u32 s43, 63\n"
    "ds_read_b128 a[20:23], v116 offset:11264\n"
    "v_smfmac_f32_16x16x64_bf16 v[48:51], v[20:23], a[168:175], v67\n"
    "s_waitcnt lgkmcnt(4)\n"
    "v_mfma_f32_16x16x32_bf16 v[92:95], v[40:43], v[32:35], 0\n"
    "s_waitcnt lgkmcnt(2)\n"
    "ds_read_b128 a[80:83], v116 offset:14336\n"
    "v_mfma_f32_16x16x32_bf16 v[92:95], v[44:47], v[36:39], v[92:95]\n"
    "s_cbranch_scc1 L_last3%=\n"
    "s_waitcnt lgkmcnt(2)\n"
    "v_max3_f32 v122, v88, v89, v90\n"
    "v_max_f32 v122, v122, v91\n"
    "v_smfmac_f32_16x16x64_bf16 v[52:55], v[20:23], a[232:239], v67\n"
    "ds_read_b128 a[84:87], v116 offset:15360\n"
    "v_max_f32_dpp v123, v122, v122 quad_perm:[2,3,0,1] row_mask:0xf bank_mask:0xf\n"
    "v_add_f32_dpp v92, v94, v92 quad_perm:[2,3,2,3] row_mask:0xf bank_mask:0xf\n"
    "v_add_f32_dpp v93, v95, v93 quad_perm:[2,3,2,3] row_mask:0xf bank_mask:0xf\n"
    "v_max3_f32 v122, v123, v92, v93\n"
    "v_smfmac_f32_16x16x64_bf16 v[48:51], v[12:15], a[152:159], v67\n"
    "v_mul_f32_dpp v121, v122, v87 quad_perm:[0,1,0,1] row_mask:0xf bank_mask:0xf\n"
    "v_mul_f32_dpp v123, v122, v87 quad_perm:[0,1,0,1] row_mask:0xf bank_mask:0xf\n"
    "v_smfmac_f32_16x16x64_bf16 v[52:55], v[12:15], a[216:223], v67\n"
    "v_permlane16_swap_b32 v123, v121\n"
    "v_max_f32 v122, v123, v121\n"
    "v_max_f32_e64 v123, v123, v121\n"
    "v_smfmac_f32_16x16x64_bf16 v[48:51], v[4:7], a[136:143], v67\n"
    "v_smfmac_f32_16x16x64_bf16 v[52:55], v[4:7], a[200:207], v67\n"
    "v_permlane32_swap_b32 v122, v123\n"
    "v_max_f32 v122, v122, v123\n"
    "v_pk_fma_f32 v[92:93], v[92:93], s[76:77], v[122:123] op_sel_hi:[1,1,0] neg_lo:[0,0,1] neg_hi:[0,0,1]\n"
    "v_pk_fma_f32 v[88:89], v[88:89], s[76:77], v[122:123] op_sel_hi:[1,1,0] neg_lo:[0,0,1] neg_hi:[0,0,1]\n"
    "v_pk_fma_f32 v[90:91], v[90:91], s[76:77], v[122:123] op_sel_hi:[1,1,0] neg_lo:[0,0,1] neg_hi:[0,0,1]\n"
    "v_exp_f32 v92, v92\n"
    "v_exp_f32 v93, v93\n"
    "v_exp_f32 v88, v88\n"
    "v_exp_f32 v89, v89\n"
    "v_exp_f32 v90, v90\n"
    "v_exp_f32 v91, v91\n"
    "v_pk_add_f32 v[48:49], v[48:49], v[50:51]\n"
    "v_pk_add_f32 v[52:53], v[52:53], v[54:55]\n"
    "v_cvt_pk_bf16_f32 v126, v92, v93\n"
    "v_cvt_pk_bf16_f32 v88, v88, v89\n"
    "v_cvt_pk_bf16_f32 v90, v90, v91\n"
    "v_cndmask_b32 v89, 0, v126, s[72:73]\n"
    "v_cndmask_b32 v91, 0, v126, s[74:75]\n"
    "v_cvt_pk_bf16_f32 v66, v48, v49\n"
    "v_cvt_pk_bf16_f32 v70, v48, v49\n"
    "v_cvt_pk_bf16_f32 v74, v52, v53\n"
    "v_cvt_pk_bf16_f32 v78, v52, v53\n"
    "v_smfmac_f32_16x16x64_bf16 v[104:107], v[88:91], v[64:71], v67\n"
    "v_mov_b64_e64 v[0:1], 0\n"
    "v_smfmac_f32_16x16x64_bf16 v[108:111], v[88:91], v[72:79], v67\n"
    "v_mov_b64_e64 v[2:3], 0\n"
    "v_mfma_f32_16x16x32_bf16 v[84:87], v[88:91], v[80:83], 0\n"
    "v_add_f32 v104, v104, v106\n"
    "v_add_f32 v105, v105, v107\n"
    "v_cvt_pk_bf16_f32 v104, v104, v105\n"
    "v_add_f32 v108, v108, v110\n"
    "v_add_f32 v109, v109, v111\n"
    "v_cvt_pk_bf16_f32 v105, v108, v109\n"
    "ds_write_b64 v114, v[104:105] offset:13824\n"
    "v_pk_add_f32 v[124:125], v[84:85], v[86:87]\n"
    "v_rcp_f32 v124, v124\n"
    "v_rcp_f32 v125, v125\n"
    "v_mov_b64 v[4:5], 0\n"
    "v_mov_b64 v[6:7], 0\n"
    "v_mov_b64 v[8:9], 0\n"
    "v_mov_b64 v[10:11], 0\n"
    "v_mov_b64 v[12:13], 0\n"
    "v_mov_b64 v[14:15], 0\n"
    "v_mfma_f32_16x16x32_bf16 v[92:95], v[80:83], v[80:83], 0\n"
    "s_waitcnt lgkmcnt(0)\n"
    "s_barrier\n"
    "ds_read_b64_tr_b16 v[32:33], v112 offset:12288\n"
    "v_mfma_f32_16x16x32_bf16 v[92:95], v[80:83], v[80:83], 0\n"
    "ds_read_b64_tr_b16 v[34:35], v113 offset:12288\n"
    "ds_read_b64_tr_b16 v[36:37], v112 offset:13312\n"
    "v_mfma_f32_16x16x32_bf16 v[92:95], v[80:83], v[80:83], 0\n"
    "ds_read_b64_tr_b16 v[38:39], v113 offset:13312\n"
    "v_mfma_f32_16x16x32_bf16 v[92:95], v[80:83], v[80:83], 0\n"
    "s_waitcnt lgkmcnt(2)\n"
    "v_smfmac_f32_16x16x64_bf16 v[0:3], v[32:35], a[48:55], v67\n"
    "v_smfmac_f32_16x16x64_bf16 v[4:7], v[32:35], a[112:119], v67\n"
    "s_waitcnt lgkmcnt(0)\n"
    "v_smfmac_f32_16x16x64_bf16 v[0:3], v[36:39], a[56:63], v67\n"
    "v_smfmac_f32_16x16x64_bf16 v[4:7], v[36:39], a[120:127], v67\n"
    "v_smfmac_f32_16x16x64_bf16 v[8:11], v[32:35], a[32:39], v67\n"
    "v_add_f32_e64 v0, v0, v2\n"
    "v_smfmac_f32_16x16x64_bf16 v[12:15], v[32:35], a[96:103], v67\n"
    "v_add_f32_e64 v1, v1, v3\n"
    "v_smfmac_f32_16x16x64_bf16 v[8:11], v[36:39], a[16:23], v67\n"
    "v_add_f32_e64 v4, v4, v6\n"
    "v_smfmac_f32_16x16x64_bf16 v[12:15], v[36:39], a[80:87], v67\n"
    "v_add_f32 v5, v5, v7\n"
    "v_mul_f32 v0, v0, v124\n"
    "v_mul_f32_e64 v1, v1, v125\n"
    "v_cvt_pk_bf16_f32 v88, v0, v1\n"
    "v_pk_mul_f32 v[4:5], v[4:5], v[124:125]\n"
    "v_pk_add_f32 v[8:9], v[8:9], v[10:11]\n"
    "v_pk_add_f32 v[12:13], v[12:13], v[14:15]\n"
    "v_cvt_pk_bf16_f32 v89, v4, v5\n"
    "v_pk_mul_f32 v[8:9], v[8:9], v[124:125]\n"
    "v_pk_mul_f32 v[12:13], v[12:13], v[124:125]\n"
    "v_cvt_pk_bf16_f32 v90, v8, v9\n"
    "v_cvt_pk_bf16_f32 v91, v12, v13\n"
    "global_store_dwordx4 v117, v[88:91], s[44:45] sc1\n"
    "s_add_u32 s43, s43, 1\n"
    "s_sleep 3\n"
    "ds_read_b128 a[48:51], v116 offset:16384\n"
    "ds_read_b128 a[52:55], v116 offset:17408\n"
    "ds_read_b128 a[112:115], v116 offset:18432\n"
    "ds_read_b128 a[116:119], v116 offset:19456\n"
    "ds_read_b128 a[56:59], v116 offset:20480\n"
    "ds_read_b128 a[60:63], v116 offset:21504\n"
    "ds_read_b128 a[120:123], v116 offset:22528\n"
    "ds_read_b128 a[124:127], v116 offset:23552\n"
    "ds_read_b128 a[32:35], v116 offset:24576\n"
    "ds_read_b128 a[36:39], v116 offset:25600\n"
    "ds_read_b128 a[96:99], v116 offset:26624\n"
    "ds_read_b128 a[100:103], v116 offset:27648\n"
    "ds_read_b128 a[16:19], v116 offset:28672\n"
    "s_waitcnt lgkmcnt(8)\n"
    "ds_read_b128 a[20:23], v116 offset:29696\n"
    "ds_read_b128 a[80:83], v116 offset:30720\n"
    "ds_read_b128 a[84:87], v116 offset:31744\n"
    "s_and_b32 s11, s43, 3\n"
    "s_lshl_b32 s11, s11, 17\n"
    "s_add_u32 s54, s48, s11\n"
    "s_addc_u32 s55, s49, 0\n"
    "s_add_u32 s11, s43, -1\n"
    "s_and_b32 s11, s11, 3\n"
    "s_cselect_b32 s11, 0, 0x2000\n"
    "s_add_u32 s62, s62, 0x80000\n"
    "s_addc_u32 s63, s63, 0\n"
    "s_xor_b32 s62, s62, s11\n"
    "s_add_u32 s11, s43, 1\n"
    "s_and_b32 s11, s11, 3\n"
    "s_cselect_b32 s11, 0, 0x2000\n"
    "s_add_u32 s58, s58, 0x80000\n"
    "s_addc_u32 s59, s59, 0\n"
    "s_xor_b32 s58, s58, s11\n"
    "s_add_u32 s60, s60, 0x80000\n"
    "s_addc_u32 s61, s61, 0\n"
    "s_xor_b32 s60, s60, s11\n"
    "s_xor_b32 s44, s44, 0x8000\n"
    "v_mov_b64 v[32:33], 0\n"
    "v_mov_b64 v[34:35], 0\n"
    "v_mov_b64 v[36:37], 0\n"
    "v_mov_b64 v[38:39], 0\n"
    "v_mov_b64 v[48:49], 0\n"
    "v_mov_b64 v[50:51], 0\n"
    "v_mov_b64 v[52:53], 0\n"
    "v_mov_b64 v[54:55], 0\n"
    "v_mov_b64 v[104:105], 0\n"
    "v_mov_b64 v[106:107], 0\n"
    "v_mov_b64 v[108:109], 0\n"
    "v_mov_b64 v[110:111], 0\n"
    "s_waitcnt vmcnt(1)\n"
    "ds_read_b64 v[64:65], v116 offset:32768\n"
    "ds_read_b64 v[68:69], v116 offset:32776\n"
    "ds_read_b64 v[72:73], v116 offset:33792\n"
    "ds_read_b64 v[76:77], v116 offset:33800\n"
    "v_mov_b64 v[56:57], v[96:97]\n"
    "v_mov_b64 v[58:59], v[98:99]\n"
    "v_mov_b64 v[60:61], v[100:101]\n"
    "v_mov_b64 v[62:63], v[102:103]\n"
    "s_waitcnt lgkmcnt(0)\n"
    "s_barrier\n"
    "global_load_dwordx4 v[24:27], v71, s[54:55] offset:0 sc1\n"
    "s_nop 3\n"
    "global_load_dwordx4 v[28:31], v71, s[54:55] offset:1024 sc1\n"
    "s_nop 15\n"
    "s_cmp_lg_u32 s42, 0\n"
    "s_cbranch_scc1 L_vmb3%=\n"
    "s_mov_b32 exec_lo, 0\n"
    "global_store_dwordx4 v71, v[44:47], s[62:63] offset:1024 sc1\n"
    "s_mov_b64 exec, -1\n"
    "s_branch L_kvb3%=\n"
    "L_vmb3%=:\n"
    "v_mov_b32 v126, v66\n"
    "v_mov_b32 v127, v74\n"
    "global_store_dwordx2 v79, v[126:127], s[62:63] offset:1536 sc1\n"
    "L_kvb3%=:\n"
    "s_nop 1\n"
    "v_mov_b64 v[40:41], 0\n"
    "v_mov_b64 v[42:43], 0\n"
    "v_mov_b64 v[44:45], 0\n"
    "v_mov_b64 v[46:47], 0\n"
    "s_branch L_top3%=\n"
    "L_slow3%=:\n"
    "v_cmp_eq_u32 s[78:79], s50, v24\n"
    "v_cmp_eq_u32 s[80:81], s50, v25\n"
    "v_cmp_eq_u32 s[82:83], s50, v26\n"
    "v_cmp_eq_u32 s[84:85], s50, v27\n"
    "v_cmp_eq_u32 s[86:87], s50, v28\n"
    "v_cmp_eq_u32 s[88:89], s50, v29\n"
    "v_cmp_eq_u32 s[52:53], s50, v30\n"
    "v_cmp_eq_u32 vcc, s50, v31\n"
    "global_load_dwordx4 v[24:27], v71, s[54:55] offset:0 sc1\n"
    "global_load_dwordx4 v[28:31], v71, s[54:55] offset:1024 sc1\n"
    "s_waitcnt lgkmcnt(0)\n"
    "s_barrier\n"
    "s_or_b64 vcc, vcc, s[78:79]\n"
    "s_or_b64 vcc, vcc, s[80:81]\n"
    "s_or_b64 vcc, vcc, s[82:83]\n"
    "s_or_b64 vcc, vcc, s[84:85]\n"
    "s_or_b64 vcc, vcc, s[86:87]\n"
    "s_or_b64 vcc, vcc, s[88:89]\n"
    "s_or_b64 vcc, vcc, s[52:53]\n"
    "s_cbranch_scc1 L_redo3%=\n"
    "v_cmp_eq_u32 s[78:79], s50, v16\n"
    "v_cmp_eq_u32 s[80:81], s50, v17\n"
    "v_cmp_eq_u32 s[82:83], s50, v18\n"
    "v_cmp_eq_u32 s[84:85], s50, v19\n"
    "v_cmp_eq_u32 s[86:87], s50, v8\n"
    "v_cmp_eq_u32 s[88:89], s50, v9\n"
    "v_cmp_eq_u32 s[52:53], s50, v10\n"
    "v_cmp_eq_u32 vcc, s50, v11\n"
    "s_nop 0\n"
    "s_or_b64 vcc, vcc, s[78:79]\n"
    "s_or_b64 vcc, vcc, s[80:81]\n"
    "s_or_b64 vcc, vcc, s[82:83]\n"
    "s_or_b64 vcc, vcc, s[84:85]\n"
    "s_or_b64 vcc, vcc, s[86:87]\n"
    "s_or_b64 vcc, vcc, s[88:89]\n"
    "s_or_b64 vcc, vcc, s[52:53]\n"
    "s_cbranch_scc1 L_redo3%=\n"
    "v_cmp_eq_u32 s[78:79], s50, v0\n"
    "v_cmp_eq_u32 s[80:81], s50, v1\n"
    "v_cmp_eq_u32 s[82:83], s50, v2\n"
    "v_cmp_eq_u32 s[84:85], s50, v3\n"
    "v_cmp_eq_u32 s[86:87], s50, v20\n"
    "v_cmp_eq_u32 s[88:89], s50, v21\n"
    "v_cmp_eq_u32 s[52:53], s50, v22\n"
    "v_cmp_eq_u32 vcc, s50, v23\n"
    "s_nop 0\n"
    "s_or_b64 vcc, vcc, s[78:79]\n"
    "s_or_b64 vcc, vcc, s[80:81]\n"
    "s_or_b64 vcc, vcc, s[82:83]\n"
    "s_or_b64 vcc, vcc, s[84:85]\n"
    "s_or_b64 vcc, vcc, s[86:87]\n"
    "s_or_b64 vcc, vcc, s[88:89]\n"
    "s_or_b64 vcc, vcc, s[52:53]\n"
    "s_cbranch_scc1 L_redo3%=\n"
    "v_cmp_eq_u32 s[78:79], s50, v12\n"
    "v_cmp_eq_u32 s[80:81], s50, v13\n"
    "v_cmp_eq_u32 s[82:83], s50, v14\n"
    "v_cmp_eq_u32 s[84:85], s50, v15\n"
    "v_cmp_eq_u32 s[86:87], s50, v4\n"
    "v_cmp_eq_u32 s[88:89], s50, v5\n"
    "v_cmp_eq_u32 s[52:53], s50, v6\n"
    "v_cmp_eq_u32 vcc, s50, v7\n"
    "s_nop 0\n"
    "s_or_b64 vcc, vcc, s[78:79]\n"
    "s_or_b64 vcc, vcc, s[80:81]\n"
    "s_or_b64 vcc, vcc, s[82:83]\n"
    "s_or_b64 vcc, vcc, s[84:85]\n"
    "s_or_b64 vcc, vcc, s[86:87]\n"
    "s_or_b64 vcc, vcc, s[88:89]\n"
    "s_or_b64 vcc, vcc, s[52:53]\n"
    "s_cbranch_scc1 L_redo3%=\n"
    "s_waitcnt vmcnt(0)\n"
    "s_branch L_go3%=\n"
    "L_redo3%=:\n"
    "ds_read_b128 a[48:51], v116 offset:16384\n"
    "ds_read_b128 a[52:55], v116 offset:17408\n"
    "ds_read_b128 a[112:115], v116 offset:18432\n"
    "ds_read_b128 a[116:119], v116 offset:19456\n"
    "ds_read_b128 a[56:59], v116 offset:20480\n"
    "ds_read_b128 a[60:63], v116 offset:21504\n"
    "ds_read_b128 a[120:123], v116 offset:22528\n"
    "ds_read_b128 a[124:127], v116 offset:23552\n"
    "ds_read_b128 a[32:35], v116 offset:24576\n"
    "ds_read_b128 a[36:39], v116 offset:25600\n"
    "ds_read_b128 a[96:99], v116 offset:26624\n"
    "ds_read_b128 a[100:103], v116 offset:27648\n"
    "ds_read_b128 a[16:19], v116 offset:28672\n"
    "ds_read_b128 a[20:23], v116 offset:29696\n"
    "s_waitcnt lgkmcnt(8)\n"
    "ds_read_b128 a[80:83], v116 offset:30720\n"
    "ds_read_b128 a[84:87], v116 offset:31744\n"
    "v_mov_b64 v[32:33], 0\n"
    "v_mov_b64 v[34:35], 0\n"
    "v_mov_b64 v[36:37], 0\n"
    "v_mov_b64 v[38:39], 0\n"
    "v_mov_b64 v[40:41], 0\n"
    "v_mov_b64 v[42:43], 0\n"
    "v_mov_b64 v[44:45], 0\n"
    "v_mov_b64 v[46:47], 0\n"
    "v_mov_b64 v[48:49], 0\n"
    "v_mov_b64 v[50:51], 0\n"
    "v_mov_b64 v[52:53], 0\n"
    "v_mov_b64 v[54:55], 0\n"
    "s_add_u32 s90, s90, 1\n"
    "s_and_b32 s78, s90, 0xff\n"
    "s_cmp_lg_u32 s78, 0\n"
    "s_cbranch_scc1 L_re3%=\n"
    "v_mov_b32 v126, 0\n"
    "global_load_dwordx2 v[126:127], v126, s[68:69] sc1\n"
    "s_waitcnt vmcnt(0)\n"
    "v_cmp_eq_u64 vcc, s[96:97], v[126:127]\n"
    "s_nop 1\n"
    "s_cbranch_vccnz L_bail_g3%=\n"
    "s_cmp_lt_u32 s90, 0x40000\n"
    "s_cbranch_scc1 L_re3%=\n"
    "v_mov_b32 v126, s96\n"
    "v_mov_b32 v127, s97\n"
    "s_mov_b64 exec, 1\n"
    "global_store_dwordx2 v71, v[126:127], s[68:69] sc1\n"
    "s_mov_b64 exec, -1\n"
    "L_bail_g3%=:\n"
    "s_branch L_go3%=\n"
    "L_re3%=:\n"
    "s_waitcnt vmcnt(0) lgkmcnt(0)\n"
    "s_branch L_top3%=\n"
    "L_last3%=:\n"
    "v_smfmac_f32_16x16x64_bf16 v[52:55], v[20:23], a[232:239], v67\n"
    "v_smfmac_f32_16x16x64_bf16 v[48:51], v[12:15], a[152:159], v67\n"
    "v_smfmac_f32_16x16x64_bf16 v[52:55], v[12:15], a[216:223], v67\n"
    "v_smfmac_f32_16x16x64_bf16 v[48:51], v[4:7], a[136:143], v67\n"
    "v_smfmac_f32_16x16x64_bf16 v[52:55], v[4:7], a[200:207], v67\n"
    "s_add_u32 s11, s43, 0\n"
    "s_and_b32 s11, s11, 3\n"
    "s_cselect_b32 s11, 0, 0x2000\n"
    "s_add_u32 s62, s62, 0x80000\n"
    "s_addc_u32 s63, s63, 0\n"
    "s_xor_b32 s62, s62, s11\n"
    "v_pk_add_f32 v[48:49], v[48:49], v[50:51]\n"
    "s_nop 0\n"
    "v_pk_add_f32 v[52:53], v[52:53], v[54:55]\n"
    "v_cvt_pk_bf16_f32 v66, v48, v49\n"
    "v_cvt_pk_bf16_f32 v74, v52, v53\n"
    "s_cmp_lg_u32 s42, 0\n"
    "s_cbranch_scc1 L_vmc3%=\n"
    "s_mov_b32 exec_lo, 0\n"
    "global_store_dwordx4 v71, v[44:47], s[62:63] offset:1024 sc1\n"
    "s_mov_b64 exec, -1\n"
    "s_branch L_kvc3%=\n"
    "L_vmc3%=:\n"
    "v_mov_b32 v126, v66\n"
    "v_mov_b32 v127, v74\n"
    "global_store_dwordx2 v79, v[126:127], s[62:63] offset:1536 sc1\n"
    "L_kvc3%=:\n"
    "s_waitcnt vmcnt(0) lgkmcnt(0)\n"
    "s_endpgm\n"
    :: "s"(ws), "s"(__builtin_amdgcn_readfirstlane(by * 4 + bx)), "s"(wave), "s"(ldsb), "s"(ws + OFF_EP), "s"(Wq), "s"(Wk), "s"(Wv), "s"(Wo) PADOP
    : "memory", "vcc", "v255", "a255", "s34", "s35", "s36", "s37", "s38", "s39", "s40", "s96", "s97", "s101", "s22", "s23", "s24", "s25", "s26", "s27", "s28", "s29", "scc");
  __builtin_unreachable();
}

extern "C" void kernel_launch(void* const* d_in, const int* in_sizes, int n_in, void* d_out, int out_size, void* d_ws, size_t ws_size,
                              hipStream_t stream) {
  const u16* x = (const u16*)d_in[0];
  const u16* mem0 = (const u16*)d_in[1];
  const u16* Wq = (const u16*)d_in[2];
  const u16* Wk = (const u16*)d_in[3];
  const u16* Wv = (const u16*)d_in[4];
  const u16* Wo = (const u16*)d_in[5];
  const u16* w_r = (const u16*)d_in[8];
  const u16* w_gate = (const u16*)d_in[9];
  const u16* w_up = (const u16*)d_in[10];
  const u16* w_down = (const u16*)d_in[11];
  u16* out = (u16*)d_out;
  char* ws = (char*)(((size_t)d_ws + 2 * MBY - 1) & ~(size_t)(2 * MBY - 1));
  rec_kernel<<<256, 256, 0, stream>>>(ws, x, mem0, Wq, Wk, Wv, Wo, w_r, w_gate, w_up, w_down, out);
}
```
